```python
import torch

def _softmax(x, axis=-1):
    return torch.softmax(x, dim=axis)
def _gelu(x):
    return torch.nn.functional.gelu(x, approximate="tanh")
def _silu(x):
    return torch.nn.functional.silu(x)
def _sigmoid(x):
    return torch.sigmoid(x)
def _layernorm(x, axis=-1, eps=1e-5):
    m = x.mean(dim=axis, keepdim=True)
    v = x.var(dim=axis, keepdim=True, unbiased=False)
    return (x - m) / torch.sqrt(v + eps)
def _scatter_add(tgt, idx, src, axis=0):
    return tgt.clone().index_add_(axis, idx, src)
def _segment_sum(src, seg, n):
    out = torch.zeros((n,) + src.shape[1:], dtype=src.dtype)
    return out.index_add_(0, seg, src)
def _pad(x, pw):
    flat = tuple(v for pair in reversed(pw) for v in pair)
    return torch.nn.functional.pad(x, flat)

kernel_name = 'gated_mix__compressed_kv_cla__sliding_local_dilated__prompt'

def setup_inputs():

    _w = (lambda a: (lambda t: t.to(torch.bfloat16) if t.is_floating_point() else t)(torch.as_tensor(a)))
    def _A_setup():
        B, H, Sq, Sk, D, M = 1, 32, 2048, 2048, 128, 1024
        Dc = 4 * D
        return {
            "x": _w((lambda *s: torch.randn(*s, dtype=torch.bfloat16) * 0.05)(B, Sq, M)),
            "c_kv": _w((lambda *s: torch.randn(*s, dtype=torch.bfloat16) * 1.6)(B, Sk, Dc)),
            "w_q": _w((lambda *s: torch.randn(*s, dtype=torch.bfloat16) * 6.4)(M, H, D) * (M ** -0.5)),
            "w_uk": _w((lambda *s: torch.randn(*s, dtype=torch.bfloat16) * 6.4)(Dc, H, D) * (Dc ** -0.5)),
            "w_uv": _w((lambda *s: torch.randn(*s, dtype=torch.bfloat16) * 1.6)(Dc, H, D) * (Dc ** -0.5)),
            "w_o": _w((lambda *s: torch.randn(*s, dtype=torch.bfloat16) * 1.6)(H, D, M) * ((H * D) ** -0.5)),
        }
    _A_in = _A_setup()

    def _B_setup():
        B, S, D = 1, 2048, 1024
        return {
            "x": _w((lambda *s: torch.randn(*s, dtype=torch.bfloat16) * 0.05)(B, S, D)),
            "wqkv": _w((lambda *s: torch.randn(*s, dtype=torch.bfloat16) * 0.8)(D, 3 * D)),
            "wo": _w((lambda *s: torch.randn(*s, dtype=torch.bfloat16) * 0.05)(D, D)),
        }
    _B_in = _B_setup()

    return {
        "A_x": _A_in["x"],
        "A_c_kv": _A_in["c_kv"],
        "A_w_q": _A_in["w_q"],
        "A_w_uk": _A_in["w_uk"],
        "A_w_uv": _A_in["w_uv"],
        "A_w_o": _A_in["w_o"],
        "B_x": _B_in["x"],
        "B_wqkv": _B_in["wqkv"],
        "B_wo": _B_in["wo"],
        "gate_w": _w((lambda *s: torch.randn(*s, dtype=torch.bfloat16) * 0.05)(1024) * 16.0),
    }

def reference(A_x, A_c_kv, A_w_q, A_w_uk, A_w_uv, A_w_o, B_x, B_wqkv, B_wo, gate_w):
    def _A_ref(x, c_kv, w_q, w_uk, w_uv, w_o):
        B, Sq, M = x.shape[0], x.shape[1], x.shape[2]
        Sk = c_kv.shape[1]
        H, D = w_q.shape[1], w_q.shape[2]
        q = torch.einsum("bqm,mhd->bhqd", x, w_q)
        k = torch.einsum("bkc,chd->bhkd", c_kv, w_uk)
        v = torch.einsum("bkc,chd->bhkd", c_kv, w_uv)
        scores = torch.einsum("bhqd,bhkd->bhqk", q, k) * (D ** -0.5)
        off = Sk - Sq
        qi = torch.arange(Sq)[:, None]
        kj = torch.arange(Sk)[None, :]
        cmask = kj <= (qi + off)
        scores = torch.where(cmask[None, None], scores, -1e9)
        p = _softmax(scores, axis=-1)
        ctx = torch.einsum("bhqk,bhkd->bhqd", p, v)
        return torch.einsum("bhqd,hdm->bqm", ctx, w_o)

    def _B_ref(x, wqkv, wo):
        B, S, D = x.shape
        H = 32; Dh = D // H
        W, dil = 8, 2
        qkv = (lambda x, *d: x.permute(*d))((x @ wqkv).reshape(B, S, 3, H, Dh), 2, 0, 3, 1, 4)
        q, k, v = qkv[0], qkv[1], qkv[2]
        scale = Dh ** -0.5
        pos = torch.arange(S)
        off = torch.arange(W) * dil
        idx = pos[:, None] - off[None, :]
        mask = idx >= 0
        idx_c = torch.where(mask, idx, 0).reshape(-1)
        kg = k[:, :, idx_c, :].reshape(B, H, S, W, Dh)
        vg = v[:, :, idx_c, :].reshape(B, H, S, W, Dh)
        logits = torch.einsum("bhsd,bhswd->bhsw", q, kg) * scale
        logits = torch.where(mask[None, None, :, :], logits, -1e9)
        attn = _softmax(logits, axis=-1)
        o = torch.einsum("bhsw,bhswd->bhsd", attn, vg)
        return (lambda x, *d: x.permute(*d))(o, 0, 2, 1, 3).reshape(B, S, D) @ wo

    x = A_x
    g = _sigmoid((x * gate_w).sum(-1, keepdims=True))
    a = _A_ref(x, A_c_kv, A_w_q, A_w_uk, A_w_uv, A_w_o)
    b = _B_ref(x, B_wqkv, B_wo)
    return g * a + (1 - g) * b
```

```cpp
#include <hip/hip_runtime.h>
#include <hip/hip_bf16.h>
#include <stdint.h>
#include <type_traits>

#define LAS __attribute__((address_space(3)))
struct Args { const void* p[11]; };
__global__ __launch_bounds__(256, 1)
void mega(Args args) {
    __shared__ __attribute__((aligned(1024))) char smem[163840];
    const unsigned tid = threadIdx.x + (unsigned)(uintptr_t)(LAS char*)smem;
    asm volatile(
R"(.macro cv a,b,c
v_cvt_pk_bf16_f32 v\a,v\b,v\c
.endm
.macro cvaa a,b,c,d
v_mfma_f32_16x16x32_bf16 v[\a:\a+3],a[\b:\b+3],a[\c:\c+3],v[\d:\d+3]
.endm
.macro cvvv a,b,c,d
v_mfma_f32_16x16x32_bf16 v[\a:\a+3],v[\b:\b+3],v[\c:\c+3],v[\d:\d+3]
.endm
.macro dl a,b,c,d
buffer_load_dwordx4 v\a,s[\b:\b+3],s\c offen offset:\d lds
.endm
.macro ds a,b,c,d
buffer_load_dwordx4 v\a,s[\b:\b+3],s\c offen offset:\d sc1 lds
.endm
.macro ex a
v_exp_f32_e32 v\a,v\a
.endm
.macro la a,b,c,d
buffer_load_dwordx4 a[\a:\a+3],v\b,s[8:11],s\c offen offset:\d sc1
.endm
.macro lv a,b,c,d
buffer_load_dwordx4 v[\a:\a+3],v\b,s[8:11],s\c offen offset:\d sc1
.endm
.macro maaa a,b,c
v_mfma_f32_16x16x32_bf16 a[\a:\a+3],a[\b:\b+3],a[\c:\c+3],a[\a:\a+3]
.endm
.macro maav a,b,c
v_mfma_f32_16x16x32_bf16 a[\a:\a+3],a[\b:\b+3],v[\c:\c+3],a[\a:\a+3]
.endm
.macro mavv a,b,c
v_mfma_f32_16x16x32_bf16 a[\a:\a+3],v[\b:\b+3],v[\c:\c+3],a[\a:\a+3]
.endm
.macro mvaa a,b,c
v_mfma_f32_16x16x32_bf16 v[\a:\a+3],a[\b:\b+3],a[\c:\c+3],v[\a:\a+3]
.endm
.macro mvav a,b,c
v_mfma_f32_16x16x32_bf16 v[\a:\a+3],a[\b:\b+3],v[\c:\c+3],v[\a:\a+3]
.endm
.macro mvva a,b,c
v_mfma_f32_16x16x32_bf16 v[\a:\a+3],v[\b:\b+3],a[\c:\c+3],v[\a:\a+3]
.endm
.macro mvvv a,b,c
v_mfma_f32_16x16x32_bf16 v[\a:\a+3],v[\b:\b+3],v[\c:\c+3],v[\a:\a+3]
.endm
.macro ra a,b,c
ds_read_b128 a[\a:\a+3],v\b offset:\c
.endm
.macro rv a,b,c
ds_read_b128 v[\a:\a+3],v\b offset:\c
.endm
.macro ta a,b,c
ds_read_b64_tr_b16 a[\a:\a+1],v\b offset:\c
.endm
.macro tv a,b,c
ds_read_b64_tr_b16 v[\a:\a+1],v\b offset:\c
.endm
.macro zaaa a,b,c
v_mfma_f32_16x16x32_bf16 a[\a:\a+3],a[\b:\b+3],a[\c:\c+3],0
.endm
.macro zaav a,b,c
v_mfma_f32_16x16x32_bf16 a[\a:\a+3],a[\b:\b+3],v[\c:\c+3],0
.endm
.macro zvaa a,b,c
v_mfma_f32_16x16x32_bf16 v[\a:\a+3],a[\b:\b+3],a[\c:\c+3],0
.endm
.macro zvav a,b,c
v_mfma_f32_16x16x32_bf16 v[\a:\a+3],a[\b:\b+3],v[\c:\c+3],0
.endm
.macro zvva a,b,c
v_mfma_f32_16x16x32_bf16 v[\a:\a+3],v[\b:\b+3],a[\c:\c+3],0
.endm
.macro zvvv a,b,c
v_mfma_f32_16x16x32_bf16 v[\a:\a+3],v[\b:\b+3],v[\c:\c+3],0
.endm
L_begin:
s_load_dwordx2 s[28:29],%0,72
s_load_dwordx16 s[68:83],%0,0
s_load_dwordx2 s[84:85],%0,64
s_load_dwordx2 s[8:9],%0,80
v_lshrrev_b32_e32 v246,6,%2
v_and_b32_e32 v247,63,%2
v_lshlrev_b32_e32 v244,4,v247
v_readfirstlane_b32 s12,v246
s_and_b32 s20,%1,7
s_lshr_b32 s21,%1,3
s_getpc_b64 s[96:97]
L_att_pc:
s_lshl_b32 s86,s21,2
s_add_u32 s86,s86,s12
s_mul_i32 s86,s86,256
v_lshl_add_u32 v246,v247,7,s86
v_min_u32_e32 v246,L_att_end-L_att_pc-4,v246
s_mov_b64 exec,3
global_load_dword a188,v246,s[96:97]
s_mov_b64 exec,-1
s_and_b32 s19,s21,7
s_lshr_b32 s86,s21,3
s_lshl_b32 s18,s20,2
s_add_u32 s18,s18,s86
v_lshrrev_b32_e32 v248,3,v247
v_and_b32_e32 v250,112,v244
v_and_b32_e32 v249,48,v247
v_xor_b32_e32 v249,v249,v250
v_and_b32_e32 v251,16,v247
v_lshlrev_b32_e32 v251,1,v251
v_xor_b32_e32 v250,v250,v251
v_lshl_add_u32 v224,v248,10,v249
v_xor_b32_e32 v225,64,v224
v_add_u32_e32 v227,21504,v225
v_add_u32_e32 v226,14336,v224
v_add_u32_e32 v225,7168,v225
v_lshl_add_u32 v228,v248,13,v250
v_xor_b32_e32 v229,64,v228
v_add_u32_e32 v231,193536,v229
v_add_u32_e32 v230,129024,v228
v_add_u32_e32 v229,64512,v229
s_lshl_b32 s86,s19,2
s_add_u32 s86,s86,s12
s_lshl_b32 s52,s86,16
s_bfe_u32 s87,s21,65539
s_lshl_b32 s88,s20,1
s_add_u32 s87,s87,s88
s_lshl_b32 s87,s87,2
s_add_u32 s87,s87,s12
s_lshl_b32 s53,s87,7
s_mov_b32 s54,s53
s_mov_b32 s39,131072
s_mov_b32 s43,131072
s_mov_b32 s47,131072
s_mov_b32 s51,131072
s_mov_b32 s38,2097152
s_mov_b32 s42,4194304
s_mov_b32 s46,4194304
s_mov_b32 s56,32768
s_mov_b32 s60,-32640
s_mov_b32 s57,262144
s_mov_b32 s58,262144
s_bfe_u32 s91,s21,65536
s_lshl_b32 s91,s91,18
s_add_u32 s53,s53,s91
s_mov_b32 s54,s53
s_lshl_b32 s91,s91,1
s_sub_u32 s57,s57,s91
s_add_u32 s58,s58,s91
s_lshl_b32 s98,s12,13
s_lshl_b32 s62,s12,12
s_add_u32 s62,s62,98304
s_lshl_b32 s35,%1,2
s_add_u32 s35,s35,83896320
v_lshrrev_b32_e32 v48,2,v247
v_and_b32_e32 v49,3,v247
v_lshlrev_b32_e32 v49,7,v49
v_lshl_add_u32 v48,v48,13,v49
s_lshl_b32 s89,s86,14
s_and_b32 s90,s53,7680
s_add_u32 s90,s90,s89
s_lshr_b32 s89,s21,3
s_lshl_b32 s89,s89,2
s_add_u32 s89,s89,s12
s_lshl_b32 s89,s89,14
s_lshl_b32 s91,s19,18
s_add_u32 s89,s89,s91
s_cmp_ge_u32 s21,16
s_waitcnt lgkmcnt(0)
s_mov_b64 s[36:37],s[70:71]
s_cselect_b64 s[40:41],s[76:77],s[74:75]
s_mov_b64 s[44:45],s[40:41]
s_bfm_b64 exec,8,0
buffer_load_dword a189,v48,s[40:43],s90 offen
s_mov_b64 exec,-1
s_and_b32 s9,s9,65535
s_mov_b32 s10,83951616
s_mov_b32 s11,131072
buffer_load_dword v44,off,s[8:11],s35 sc1
buffer_load_dword v45,off,s[8:11],s35 offset:1024 sc1
s_add_u32 m0,s62,0
s_nop 0
dl 228,40,53,0
dl 229,40,53,1024
s_add_u32 s53,s53,s57
dl 230,44,54,2048
dl 231,44,54,3072
s_add_u32 s54,s54,s57
s_add_u32 m0,s98,0
s_nop 0
dl 224,36,52,0
dl 225,36,52,1024
dl 226,36,52,2048
dl 227,36,52,3072
s_add_u32 s52,s52,s56
s_add_u32 m0,s98,4096
s_nop 0
dl 224,36,52,0
dl 225,36,52,1024
dl 226,36,52,2048
dl 227,36,52,3072
s_add_u32 s52,s52,s60
s_add_u32 s61,s98,65536
s_mov_b32 s63,0
s_lshl_b32 s88,%1,2
s_add_u32 s88,s88,s12
s_lshl_b32 s88,s88,12
s_add_u32 s30,s68,s88
s_addc_u32 s31,s69,0
v_mov_b32_e32 v40,v247
v_and_b32_e32 v246,15,v40
v_lshlrev_b32_e32 v248,3,v40
v_xor_b32_e32 v248,v248,v40
v_and_b32_e32 v248,112,v248
v_lshl_add_u32 v232,v246,7,v248
s_and_b32 s86,s98,16384
v_add_u32_e32 v232,s86,v232
s_bfe_u32 s86,s21,65536
s_lshl_b32 s86,s86,6
v_xor_b32_e32 v232,s86,v232
v_xor_b32_e32 v233,64,v232
v_and_b32_e32 v246,12,v40
v_lshlrev_b32_e32 v246,5,v246
v_and_b32_e32 v248,3,v40
v_lshl_add_u32 v246,v248,3,v246
v_and_b32_e32 v248,24,v40
v_lshl_add_u32 v246,v248,2,v246
s_and_b32 s87,s98,8192
s_add_u32 s87,s87,98304
v_add_u32_e32 v246,s87,v246
v_and_b32_e32 v248,48,v40
v_lshl_add_u32 v234,v248,6,v246
v_xor_b32_e32 v235,32,v234
v_xor_b32_e32 v236,64,v234
v_xor_b32_e32 v237,96,v234
s_lshl_b32 s86,s19,1
s_add_u32 s86,s86,2
s_lshl_b32 s15,1,s86
s_xor_b32 s87,s19,15
s_lshl_b32 s22,s87,7
s_lshl_b32 s23,s19,7
s_lshl_b32 s87,s87,2
s_lshl_b32 s88,s19,2
s_add_u32 s87,s87,s12
s_add_u32 s88,s88,s12
s_lshl_b32 s90,s18,13
s_add_u32 s90,s90,62914560
s_mul_i32 s14,s87,327680
s_add_u32 s14,s14,s90
s_mul_i32 s13,s88,327680
s_add_u32 s13,s13,s90
s_mul_i32 s90,s18,524288
s_lshl_b32 s91,s12,12
s_add_u32 s90,s90,s91
s_add_u32 s90,s90,16777216
v_add_u32_e32 v249,s90,v244
v_add_u32_e32 v255,16777216,v249
v_accvgpr_write_b32 a240,v249
v_accvgpr_write_b32 a241,v255
s_or_b32 s86,%1,s12
s_cselect_b32 s65,0,4
v_lshrrev_b32_e32 v241,3,v40
v_lshlrev_b32_e32 v241,27,v241
v_lshl_add_u32 v241,v40,7,v241
s_cmp_lt_u32 s12,2
s_cselect_b64 s[48:49],s[72:73],s[80:81]
s_cselect_b32 s50,8,6
s_lshl_b32 s50,s50,20
s_and_b32 s86,s12,1
s_lshl_b32 s87,%1,1
s_add_u32 s86,s86,s87
s_lshl_b32 s55,s86,10
s_mov_b32 s59,524288
s_cmp_lt_u32 s12,2
s_cselect_b32 s87,12,9
v_cmp_gt_u32_e32 vcc,s87,v40
v_lshlrev_b32_e32 v241,7,v40
v_mov_b32_e32 v41,134217728
s_lshl_b32 s87,s87,7
s_mul_i32 s55,s86,s87
s_lshl_b32 s59,s87,9
v_cndmask_b32_e32 v241,v41,v241,vcc
s_cmp_lt_u32 s12,2
s_cbranch_scc1 L_lx
s_cmp_eq_u32 s12,2
s_cselect_b32 s87,18,12
v_cmp_gt_u32_e32 vcc,s87,v40
v_lshlrev_b32_e32 v241,7,v40
s_mul_i32 s55,%1,2304
v_cndmask_b32_e32 v241,v41,v241,vcc
s_cmp_eq_u32 s12,2
s_cbranch_scc1 L_lx
s_lshl_b32 s55,%1,14
s_add_u32 s86,s55,16384
s_mov_b64 s[48:49],s[68:69]
s_mov_b32 s50,s86
s_mov_b32 s55,s55
s_mov_b32 s59,1536
L_lx:
s_mov_b32 s33,s50
s_mov_b32 s34,s59
s_mov_b32 s50,0
s_mov_b32 s59,0
v_and_b32_e32 v246,15,v40
v_and_b32_e32 v248,48,v40
v_lshlrev_b32_e32 v249,2,v40
v_and_b32_e32 v249,32,v249
v_xor_b32_e32 v248,v248,v249
v_lshl_add_u32 v240,v246,6,v248
s_lshr_b32 s86,s12,1
s_lshl_b32 s86,s86,10
s_add_u32 s86,s86,112640
v_add_u32_e32 v240,s86,v240
v_and_b32_e32 v246,12,v40
v_lshlrev_b32_e32 v246,5,v246
v_and_b32_e32 v248,3,v40
v_lshl_add_u32 v246,v248,3,v246
v_and_b32_e32 v248,24,v40
v_lshl_add_u32 v246,v248,2,v246
v_add_u32_e32 v246,98304,v246
v_and_b32_e32 v248,32,v40
v_lshl_add_u32 v238,v248,5,v246
v_xor_b32_e32 v239,32,v238
v_lshrrev_b32_e32 v248,3,v40
v_and_b32_e32 v250,112,v244
v_and_b32_e32 v249,48,v40
v_xor_b32_e32 v249,v249,v250
v_and_b32_e32 v251,16,v40
v_lshlrev_b32_e32 v251,1,v251
v_xor_b32_e32 v250,v250,v251
v_lshl_add_u32 v48,v248,11,v249
v_xor_b32_e32 v49,64,v48
v_add_u32_e32 v51,46080,v49
v_add_u32_e32 v50,30720,v48
v_add_u32_e32 v49,15360,v49
v_mov_b32_e32 v52,v228
v_mov_b32_e32 v53,v229
v_mov_b32_e32 v54,v230
v_mov_b32_e32 v55,v231
s_cmp_lt_u32 s12,2
s_cbranch_scc1 L_pqx
v_mul_u32_u24_e32 v246,6144,v248
v_and_b32_e32 v251,48,v250
v_add_u32_e32 v246,v246,v251
v_and_b32_e32 v251,64,v250
v_lshl_add_u32 v52,v251,5,v246
v_xor_b32_e32 v252,64,v251
v_lshl_add_u32 v53,v252,5,v246
v_add_u32_e32 v55,144384,v53
v_add_u32_e32 v54,96256,v52
v_add_u32_e32 v53,48128,v53
s_cmp_eq_u32 s12,2
s_cbranch_scc1 L_pqx
v_mul_u32_u24_e32 v251,768,v251
v_add_u32_e32 v246,v246,v251
v_add_u32_e32 v52,4096,v246
v_add_u32_e32 v53,101376,v246
v_lshrrev_b32_e32 v252,2,v40
v_and_b32_e32 v253,32,v40
v_and_b32_e32 v250,48,v244
v_xor_b32_e32 v253,v253,v250
v_lshl_add_u32 v246,v252,11,v253
s_lshl_b32 s86,s22,11
s_sub_u32 s86,s86,34816
v_add_u32_e32 v54,s86,v246
s_lshl_b32 s86,s23,11
s_sub_u32 s86,s86,35840
v_add_u32_e32 v55,s86,v246
v_max_i32_e32 v55,0,v55
L_pqx:
v_accvgpr_write_b32 a242,v48
v_accvgpr_write_b32 a243,v49
v_accvgpr_write_b32 a244,v50
v_accvgpr_write_b32 a245,v51
v_accvgpr_write_b32 a246,v52
v_accvgpr_write_b32 a247,v53
v_accvgpr_write_b32 a248,v54
v_accvgpr_write_b32 a249,v55
s_add_u32 m0,s62,16384
s_nop 0
dl 228,40,53,0
dl 229,40,53,1024
s_add_u32 s53,s53,s58
dl 230,44,54,2048
dl 231,44,54,3072
s_add_u32 s54,s54,s58
s_add_u32 m0,s98,32768
s_nop 0
dl 224,36,52,0
dl 225,36,52,1024
dl 226,36,52,2048
dl 227,36,52,3072
s_add_u32 s52,s52,s56
s_waitcnt vmcnt(20)
s_cmp_lg_u32 s12,0
s_cbranch_scc1 L_rqx
v_readfirstlane_b32 s32,v44
v_readfirstlane_b32 s86,v45
s_add_u32 s32,s32,1
s_cmp_eq_u32 s32,s86
s_addc_u32 s32,s32,0
v_mov_b32_e32 v44,s32
v_mov_b32_e32 v45,0
v_lshlrev_b32_e32 v46,3,v244
v_mov_b32_e32 v47,0
s_lshl_b32 s86,%1,7
s_add_u32 s87,s86,83914752
s_and_b32 s86,s86,16383
s_add_u32 s86,s86,83898368
s_mov_b64 exec,1
buffer_store_dword v44,off,s[8:11],s35 sc0 sc1
v_mov_b32_e32 v48,0
v_mov_b32_e32 v49,0
buffer_store_dwordx4 v[46:49],off,s[8:11],s86 sc0 sc1
buffer_store_dwordx4 v[46:49],off,s[8:11],s87 sc0 sc1
s_cmp_lg_u32 %1,0
s_cbranch_scc1 L_rqy
s_mov_b64 exec,65535
s_mov_b32 s86,83894272
buffer_store_dword v45,v46,s[8:11],s86 offen sc0 sc1
L_rqy:
s_mov_b64 exec,-1
L_rqx:
L_vr0:
s_cmp_ge_u32 s21,16
s_cbranch_scc1 L_vtile
s_waitcnt vmcnt(8)
s_barrier
ta 32,234,0
ta 34,234,512
ra 0,232,0
ta 36,235,0
ta 38,235,512
ra 4,232,2048
ra 8,232,4096
ra 12,232,6144
ra 16,232,8192
ra 20,232,10240
ra 24,232,12288
ra 28,232,14336
s_add_u32 m0,s62,32768
ta 40,236,0
dl 228,40,53,0
ta 42,236,512
ta 44,237,0
ta 46,237,512
ta 48,234,4096
ta 50,234,4608
ta 52,235,4096
ta 54,235,4608
ta 56,236,4096
ta 58,236,4608
ta 60,237,4096
ta 62,237,4608
s_cmp_ge_u32 s63,65536
s_cselect_b32 s99,-2,1
s_lshl_b32 s99,s99,15
s_add_u32 s63,s63,s99
v_add_u32_e32 v232,s99,v232
s_mov_b32 s64,1
s_waitcnt lgkmcnt(15)
zaaa 128,32,0
zaaa 132,36,0
zaaa 140,36,4
zaaa 136,32,4
dl 229,40,53,1024
zvaa 32,32,8
s_add_u32 s53,s53,s57
zvaa 36,36,8
zvaa 44,36,12
zvaa 40,32,12
dl 230,44,54,2048
zvaa 96,32,16
zvaa 100,36,16
s_waitcnt lgkmcnt(0)
zvaa 108,36,20
zvaa 104,32,20
dl 231,44,54,3072
zvaa 160,32,24
s_add_u32 m0,s98,36864
s_add_u32 s54,s54,s57
zvaa 164,36,24
zvaa 172,36,28
zvaa 168,32,28
dl 224,36,52,0
zvaa 184,40,28
zvaa 188,44,28
zvaa 180,44,24
zvaa 176,40,24
dl 225,36,52,1024
zvaa 120,40,20
zvaa 124,44,20
zvaa 116,44,16
zvaa 112,40,16
dl 226,36,52,2048
zvaa 56,40,12
s_waitcnt vmcnt(11)
s_barrier
zvaa 60,44,12
ta 96,234,16384
zvaa 52,44,8
ta 98,234,16896
zvaa 48,40,8
dl 227,36,52,3072
zaaa 152,40,4
s_add_u32 m0,s62,49152
ra 64,233,0
s_add_u32 s52,s52,s60
zaaa 156,44,4
ta 100,235,16384
zaaa 148,44,0
ta 102,235,16896
zaaa 144,40,0
dl 228,40,53,0
zvaa 0,48,0
ra 68,233,2048
zvaa 4,52,0
ta 104,236,16384
zvaa 12,52,4
ta 106,236,16896
zvaa 8,48,4
dl 229,40,53,1024
zvaa 64,48,8
s_add_u32 s53,s53,s58
ra 72,233,4096
zvaa 68,52,8
ta 108,237,16384
zvaa 76,52,12
ta 110,237,16896
zvaa 72,48,12
dl 230,44,54,2048
zvaa 128,48,16
ra 76,233,6144
zvaa 132,52,16
ta 112,234,20480
zvaa 140,52,20
ta 114,234,20992
zvaa 136,48,20
dl 231,44,54,3072
zvaa 192,48,24
s_add_u32 m0,s61,0
ra 80,233,8192
s_add_u32 s54,s54,s58
zvaa 196,52,24
ta 116,235,20480
zvaa 204,52,28
ta 118,235,20992
zvaa 200,48,28
dl 224,36,52,0
zvaa 216,56,28
ra 84,233,10240
zvaa 220,60,28
ta 120,236,20480
zvaa 212,60,24
ta 122,236,20992
zvaa 208,56,24
dl 225,36,52,1024
ra 88,233,12288
zvaa 152,56,20
ta 124,237,20480
zvaa 156,60,20
ta 126,237,20992
zvaa 148,60,16
ra 92,233,14336
zvaa 144,56,16
dl 226,36,52,2048
zvaa 88,56,12
zvaa 92,60,12
zvaa 84,60,8
zvaa 80,56,8
dl 227,36,52,3072
zvaa 24,56,4
s_add_u32 s52,s52,s56
zvaa 28,60,4
zvaa 20,60,0
zvaa 16,56,0
buffer_load_dword a184,v241,s[48:51],s55 offen
s_add_u32 s55,s55,s59
s_waitcnt vmcnt(9)
s_waitcnt lgkmcnt(0)
s_barrier
L_k_head:
maaa 128,96,64
ta 32,234,32768
maaa 132,100,64
s_add_u32 m0,s62,0
ta 34,234,33280
maaa 140,100,68
dl 228,40,53,0
maaa 136,96,68
mvaa 32,96,72
ra 0,232,0
mvaa 36,100,72
mvaa 44,100,76
ta 36,235,32768
mvaa 40,96,76
dl 229,40,53,1024
mvaa 96,96,80
s_add_u32 s53,s53,s57
mvaa 100,100,80
ta 38,235,33280
mvaa 108,100,84
mvaa 104,96,84
ra 4,232,2048
mvaa 160,96,88
dl 230,44,54,2048
mvaa 164,100,88
mvaa 172,100,92
ta 40,236,32768
mvaa 168,96,92
mvaa 184,104,92
ta 42,236,33280
mvaa 188,108,92
dl 231,44,54,3072
mvaa 180,108,88
s_add_u32 s54,s54,s57
mvaa 176,104,88
ra 8,232,4096
mvaa 120,104,84
mvaa 124,108,84
ta 44,237,32768
mvaa 116,108,80
s_add_u32 m0,s61,4096
mvaa 112,104,80
dl 224,36,52,0
mvaa 56,104,76
ta 46,237,33280
mvaa 60,108,76
mvaa 52,108,72
ra 12,232,6144
mvaa 48,104,72
maaa 152,104,68
ta 48,234,36864
maaa 156,108,68
dl 225,36,52,1024
maaa 148,108,64
ta 50,234,37376
maaa 144,104,64
mvaa 0,112,64
ra 16,232,8192
mvaa 4,116,64
mvaa 12,116,68
ta 52,235,36864
mvaa 8,112,68
dl 226,36,52,2048
mvaa 64,112,72
mvaa 68,116,72
ta 54,235,37376
mvaa 76,116,76
mvaa 72,112,76
ra 20,232,10240
mvaa 128,112,80
mvaa 132,116,80
dl 227,36,52,3072
mvaa 140,116,84
s_add_u32 s52,s52,s60
ta 56,236,36864
mvaa 136,112,84
v_add_u32_e32 v233,s99,v233
mvaa 192,112,88
ta 58,236,37376
s_sub_u32 s61,s63,s99
mvaa 196,116,88
s_add_u32 s61,s61,s98
mvaa 204,116,92
ra 24,232,12288
mvaa 200,112,92
buffer_load_dword a185,v241,s[48:51],s55 offen
s_add_u32 s55,s55,s59
mvaa 216,120,92
mvaa 220,124,92
ta 60,237,36864
mvaa 212,124,88
mvaa 208,120,88
ta 62,237,37376
mvaa 152,120,84
mvaa 156,124,84
ra 28,232,14336
mvaa 148,124,80
mvaa 144,120,80
mvaa 88,120,76
mvaa 92,124,76
mvaa 84,124,72
mvaa 80,120,72
mvaa 24,120,68
mvaa 28,124,68
mvaa 20,124,64
s_waitcnt vmcnt(10) lgkmcnt(0)
s_barrier
mvaa 16,120,64
maaa 128,32,0
ta 96,234,49152
maaa 132,36,0
s_add_u32 m0,s62,16384
ta 98,234,49664
maaa 140,36,4
dl 228,40,53,0
maaa 136,32,4
mvaa 32,32,8
ra 64,233,0
mvaa 36,36,8
mvaa 44,36,12
ta 100,235,49152
mvaa 40,32,12
dl 229,40,53,1024
mvaa 96,32,16
s_add_u32 s53,s53,s58
mvaa 100,36,16
ta 102,235,49664
mvaa 108,36,20
mvaa 104,32,20
ra 68,233,2048
mvaa 160,32,24
dl 230,44,54,2048
mvaa 164,36,24
mvaa 172,36,28
ta 104,236,49152
mvaa 168,32,28
mvaa 184,40,28
ta 106,236,49664
mvaa 188,44,28
dl 231,44,54,3072
mvaa 180,44,24
s_add_u32 s54,s54,s58
mvaa 176,40,24
ra 72,233,4096
mvaa 120,40,20
mvaa 124,44,20
ta 108,237,49152
mvaa 116,44,16
s_add_u32 m0,s61,0
mvaa 112,40,16
dl 224,36,52,0
mvaa 56,40,12
ta 110,237,49664
mvaa 60,44,12
mvaa 52,44,8
ra 76,233,6144
mvaa 48,40,8
maaa 152,40,4
ta 112,234,53248
maaa 156,44,4
dl 225,36,52,1024
maaa 148,44,0
ta 114,234,53760
maaa 144,40,0
mvaa 0,48,0
ra 80,233,8192
mvaa 4,52,0
mvaa 12,52,4
ta 116,235,53248
mvaa 8,48,4
dl 226,36,52,2048
mvaa 64,48,8
mvaa 68,52,8
ta 118,235,53760
mvaa 76,52,12
mvaa 72,48,12
ra 84,233,10240
mvaa 128,48,16
mvaa 132,52,16
dl 227,36,52,3072
mvaa 140,52,20
s_add_u32 s52,s52,s56
ta 120,236,53248
mvaa 136,48,20
s_cmp_ge_u32 s63,65536
s_cselect_b32 s99,-2,1
s_lshl_b32 s99,s99,15
mvaa 192,48,24
ta 122,236,53760
s_add_u32 s63,s63,s99
mvaa 196,52,24
v_add_u32_e32 v232,s99,v232
mvaa 204,52,28
ra 88,233,12288
mvaa 200,48,28
buffer_load_dword a186,v241,s[48:51],s55 offen
s_add_u32 s55,s55,s59
mvaa 216,56,28
mvaa 220,60,28
ta 124,237,53248
mvaa 212,60,24
mvaa 208,56,24
ta 126,237,53760
mvaa 152,56,20
mvaa 156,60,20
ra 92,233,14336
mvaa 148,60,16
mvaa 144,56,16
mvaa 88,56,12
mvaa 92,60,12
mvaa 84,60,8
mvaa 80,56,8
mvaa 24,56,4
mvaa 28,60,4
mvaa 20,60,0
s_waitcnt vmcnt(10) lgkmcnt(0)
s_barrier
mvaa 16,56,0
maaa 128,96,64
ta 32,234,0
maaa 132,100,64
s_add_u32 m0,s62,32768
ta 34,234,512
maaa 140,100,68
dl 228,40,53,0
maaa 136,96,68
mvaa 32,96,72
ra 0,232,0
mvaa 36,100,72
mvaa 44,100,76
ta 36,235,0
mvaa 40,96,76
dl 229,40,53,1024
mvaa 96,96,80
s_add_u32 s53,s53,s57
mvaa 100,100,80
ta 38,235,512
mvaa 108,100,84
mvaa 104,96,84
ra 4,232,2048
mvaa 160,96,88
dl 230,44,54,2048
mvaa 164,100,88
mvaa 172,100,92
ta 40,236,0
mvaa 168,96,92
mvaa 184,104,92
ta 42,236,512
mvaa 188,108,92
dl 231,44,54,3072
mvaa 180,108,88
s_add_u32 s54,s54,s57
mvaa 176,104,88
ra 8,232,4096
mvaa 120,104,84
mvaa 124,108,84
ta 44,237,0
mvaa 116,108,80
s_add_u32 m0,s61,4096
mvaa 112,104,80
dl 224,36,52,0
mvaa 56,104,76
ta 46,237,512
mvaa 60,108,76
mvaa 52,108,72
ra 12,232,6144
mvaa 48,104,72
maaa 152,104,68
ta 48,234,4096
maaa 156,108,68
dl 225,36,52,1024
maaa 148,108,64
ta 50,234,4608
maaa 144,104,64
mvaa 0,112,64
ra 16,232,8192
mvaa 4,116,64
mvaa 12,116,68
ta 52,235,4096
mvaa 8,112,68
dl 226,36,52,2048
mvaa 64,112,72
mvaa 68,116,72
ta 54,235,4608
mvaa 76,116,76
mvaa 72,112,76
ra 20,232,10240
mvaa 128,112,80
mvaa 132,116,80
dl 227,36,52,3072
mvaa 140,116,84
s_add_u32 s52,s52,s60
ta 56,236,4096
mvaa 136,112,84
v_add_u32_e32 v233,s99,v233
mvaa 192,112,88
ta 58,236,4608
s_sub_u32 s61,s63,s99
mvaa 196,116,88
s_add_u32 s61,s61,s98
mvaa 204,116,92
ra 24,232,12288
mvaa 200,112,92
buffer_load_dword a187,v241,s[48:51],s55 offen
s_add_u32 s55,s55,s59
mvaa 216,120,92
mvaa 220,124,92
ta 60,237,4096
mvaa 212,124,88
mvaa 208,120,88
ta 62,237,4608
mvaa 152,120,84
mvaa 156,124,84
ra 28,232,14336
mvaa 148,124,80
mvaa 144,120,80
mvaa 88,120,76
mvaa 92,124,76
mvaa 84,124,72
mvaa 80,120,72
mvaa 24,120,68
mvaa 28,124,68
s_cmp_eq_u32 s64,3
mvaa 20,124,64
s_waitcnt vmcnt(10) lgkmcnt(0)
s_barrier
mvaa 16,120,64
s_cbranch_scc1 L_k_exit
maaa 128,32,0
ta 96,234,16384
maaa 132,36,0
s_add_u32 m0,s62,49152
ta 98,234,16896
maaa 140,36,4
dl 228,40,53,0
maaa 136,32,4
mvaa 32,32,8
ra 64,233,0
mvaa 36,36,8
mvaa 44,36,12
ta 100,235,16384
mvaa 40,32,12
dl 229,40,53,1024
mvaa 96,32,16
s_add_u32 s53,s53,s58
mvaa 100,36,16
ta 102,235,16896
mvaa 108,36,20
mvaa 104,32,20
ra 68,233,2048
mvaa 160,32,24
dl 230,44,54,2048
mvaa 164,36,24
mvaa 172,36,28
ta 104,236,16384
mvaa 168,32,28
mvaa 184,40,28
ta 106,236,16896
mvaa 188,44,28
dl 231,44,54,3072
mvaa 180,44,24
s_add_u32 s54,s54,s58
mvaa 176,40,24
ra 72,233,4096
s_bitcmp1_b32 s65,s64
s_cbranch_scc1 L_k_ev
L_k_evr:
mvaa 120,40,20
mvaa 124,44,20
ta 108,237,16384
mvaa 116,44,16
s_add_u32 m0,s61,0
mvaa 112,40,16
dl 224,36,52,0
mvaa 56,40,12
ta 110,237,16896
mvaa 60,44,12
mvaa 52,44,8
ra 76,233,6144
mvaa 48,40,8
maaa 152,40,4
ta 112,234,20480
maaa 156,44,4
dl 225,36,52,1024
maaa 148,44,0
ta 114,234,20992
maaa 144,40,0
mvaa 0,48,0
ra 80,233,8192
mvaa 4,52,0
mvaa 12,52,4
ta 116,235,20480
mvaa 8,48,4
dl 226,36,52,2048
mvaa 64,48,8
mvaa 68,52,8
ta 118,235,20992
mvaa 76,52,12
mvaa 72,48,12
ra 84,233,10240
mvaa 128,48,16
mvaa 132,52,16
dl 227,36,52,3072
mvaa 140,52,20
s_add_u32 s52,s52,s56
ta 120,236,20480
mvaa 136,48,20
s_cmp_ge_u32 s63,65536
s_cselect_b32 s99,-2,1
s_lshl_b32 s99,s99,15
mvaa 192,48,24
ta 122,236,20992
s_add_u32 s63,s63,s99
mvaa 196,52,24
v_add_u32_e32 v232,s99,v232
mvaa 204,52,28
ra 88,233,12288
mvaa 200,48,28
buffer_load_dword a184,v241,s[48:51],s55 offen
s_add_u32 s55,s55,s59
mvaa 216,56,28
mvaa 220,60,28
)"
R"(ta 124,237,20480
mvaa 212,60,24
mvaa 208,56,24
ta 126,237,20992
mvaa 152,56,20
mvaa 156,60,20
ra 92,233,14336
mvaa 148,60,16
mvaa 144,56,16
mvaa 88,56,12
mvaa 92,60,12
mvaa 84,60,8
mvaa 80,56,8
mvaa 24,56,4
mvaa 28,60,4
s_mov_b32 s50,s33
s_mov_b32 s59,s34
s_lshl_b32 s86,s21,2
s_add_u32 s86,s86,s12
s_mul_i32 s86,s86,384
s_add_u32 s86,s86,4096
v_lshl_add_u32 v253,v244,3,s86
s_mov_b64 exec,7
global_load_dword a188,v253,s[96:97]
s_mov_b64 exec,-1
s_add_u32 s64,s64,1
mvaa 20,60,0
s_waitcnt vmcnt(10) lgkmcnt(0)
s_barrier
mvaa 16,56,0
s_branch L_k_head
L_k_exit:
global_load_dwordx4 a[160:163],v244,s[30:31] offset:0
s_add_u32 m0,s62,49152
maaa 128,32,0
s_cmp_lt_u32 s12,2
s_cselect_b32 s86,s22,s23
ta 96,234,16384
v_accvgpr_read_b32 v224,a242
maaa 132,36,0
s_and_b32 s87,s12,1
ta 98,234,16896
s_lshl_b32 s86,s86,11
maaa 140,36,4
s_lshl_b32 s87,s87,17
ra 64,233,0
s_add_u32 s86,s86,s87
maaa 136,32,4
s_mov_b64 s[36:37],s[68:69]
s_mov_b32 s38,4194304
s_mov_b32 s52,s86
mvaa 32,32,8
dl 228,40,53,0
mvaa 36,36,8
s_mov_b32 s56,65536
ta 100,235,16384
v_accvgpr_read_b32 v225,a243
mvaa 44,36,12
s_lshl_b32 s88,s18,1
ta 102,235,16896
v_accvgpr_read_b32 v226,a244
mvaa 40,32,12
s_add_u32 s88,s88,s12
ra 68,233,2048
v_accvgpr_read_b32 v227,a245
mvaa 96,32,16
s_mov_b32 s91,196608
ra 72,233,4096
s_lshl_b32 s88,s88,7
mvaa 100,36,16
dl 229,40,53,1024
mvaa 108,36,20
s_lshl_b32 s89,s18,6
ra 76,233,6144
s_mov_b32 s90,6291456
mvaa 104,32,20
s_cmp_lt_u32 s12,2
s_cselect_b32 s57,262144,s91
ra 80,233,8192
s_add_u32 s53,s53,s58
mvaa 160,32,24
s_cmp_lt_u32 s12,2
s_cselect_b32 s42,8388608,s90
ra 84,233,10240
v_accvgpr_read_b32 v228,a246
mvaa 164,36,24
s_cmp_lt_u32 s12,2
s_cselect_b32 s53,s88,s89
ra 88,233,12288
v_accvgpr_read_b32 v229,a247
mvaa 172,36,28
dl 230,44,54,2048
mvaa 168,32,28
s_bitcmp1_b32 s21,0
s_cselect_b32 s90,s57,0
ra 92,233,14336
s_add_u32 s53,s53,s90
mvaa 184,40,28
s_cmp_lt_u32 s12,2
s_cselect_b64 s[40:41],s[72:73],s[80:81]
ta 104,236,16384
v_accvgpr_read_b32 v230,a248
mvaa 188,44,28
s_lshl_b32 s90,s90,1
ta 106,236,16896
s_mov_b32 s60,-65408
mvaa 180,44,24
s_and_b32 s89,s98,8192
ta 108,237,16384
s_add_u32 s91,s89,65536
mvaa 176,40,24
dl 231,44,54,3072
mvaa 120,40,20
s_add_u32 m0,s98,65536
ta 110,237,16896
s_add_u32 s54,s54,s58
mvaa 124,44,20
s_cmp_eq_u32 s12,3
s_cselect_b64 s[44:45],s[68:69],s[40:41]
ta 112,234,20480
v_accvgpr_read_b32 v231,a249
mvaa 116,44,16
s_cmp_eq_u32 s12,3
s_cselect_b32 s46,0,s42
ta 114,234,20992
s_add_u32 s58,s57,s90
mvaa 112,40,16
s_cmp_eq_u32 s12,3
s_cselect_b32 s54,0,s53
ta 116,235,20480
s_sub_u32 s57,s57,s90
mvaa 56,40,12
global_load_dwordx4 a[168:171],v244,s[30:31] offset:2048
mvaa 60,44,12
s_add_u32 s90,s89,65536
ta 118,235,20992
s_bfe_u32 s88,s21,65539
mvaa 52,44,8
s_lshl_b32 s88,s88,1
ta 120,236,20480
s_lshl_b32 s87,s20,2
mvaa 48,40,8
s_add_u32 s88,s88,s87
ta 122,236,20992
s_and_b32 s87,s12,1
maaa 152,40,4
s_add_u32 s88,s88,s87
ta 124,237,20480
s_mul_i32 s88,s88,524288
maaa 156,44,4
global_load_dwordx4 a[176:179],v244,s[84:85] offset:0
maaa 148,44,0
s_and_b32 s87,s21,7
ta 126,237,20992
s_add_u32 s86,s88,16777216
maaa 144,40,0
s_lshl_b32 s87,s87,16
s_mov_b32 s26,32768
s_add_u32 s86,s86,s87
mvaa 0,48,0
s_lshr_b32 s87,s12,1
s_mov_b32 s27,131072
s_lshl_b32 s87,s87,15
mvaa 4,52,0
s_add_u32 s86,s86,s87
s_lshl_b32 s87,s12,3
s_add_u32 s61,s98,32768
mvaa 12,52,4
global_load_dwordx4 a[164:167],v244,s[30:31] offset:1024
mvaa 8,48,4
s_add_u32 s24,s8,s86
s_addc_u32 s25,s9,0
s_lshl_b32 s86,%1,5
s_add_u32 s86,s86,s87
mvaa 64,48,8
s_add_u32 s86,s86,83886080
s_mov_b32 s63,65536
s_mov_b32 s67,2048
mvaa 68,52,8
s_cmp_ge_u32 s63,65536
s_cselect_b32 s99,-2,1
s_lshl_b32 s99,s99,15
s_mov_b32 s66,8
mvaa 76,52,12
s_cmp_lg_u32 s65,0
s_cbranch_scc1 L_k_ec1
L_k_ecr1:
s_cmp_eq_u32 s12,0
s_cselect_b32 s65,96,0
mvaa 72,48,12
global_load_dwordx4 a[172:175],v244,s[30:31] offset:3072
mvaa 128,48,16
s_mov_b32 s30,1048577
s_mov_b32 s31,268435712
s_mov_b32 s88,4096
mvaa 132,52,16
s_mov_b32 s64,1
s_or_b32 s65,s65,128
mvaa 140,52,20
s_bitcmp1_b32 s12,0
s_cselect_b64 vcc,-1,0
mvaa 136,48,20
mvaa 192,48,24
global_load_dwordx4 a[180:183],v244,s[84:85] offset:1024
mvaa 196,52,24
mvaa 204,52,28
mvaa 200,48,28
mvaa 216,56,28
mvaa 220,60,28
dl 224,36,52,0
mvaa 212,60,24
mvaa 208,56,24
mvaa 152,56,20
mvaa 156,60,20
mvaa 148,60,16
dl 225,36,52,1024
mvaa 144,56,16
mvaa 88,56,12
s_waitcnt vmcnt(13) lgkmcnt(0)
s_barrier
mvaa 92,60,12
ta 32,234,32768
mvaa 84,60,8
ta 34,234,33280
mvaa 80,56,8
dl 226,36,52,2048
mvaa 24,56,4
ta 36,235,32768
mvaa 28,60,4
ta 38,235,33280
mvaa 20,60,0
ta 40,236,32768
mvaa 16,56,0
ta 42,236,33280
maaa 128,96,64
dl 227,36,52,3072
maaa 132,100,64
s_add_u32 s52,s52,s56
s_add_u32 m0,s98,69632
ta 44,237,32768
maaa 140,100,68
ta 46,237,33280
maaa 136,96,68
ta 48,234,36864
mvaa 32,96,72
ta 50,234,37376
mvaa 36,100,72
dl 224,36,52,0
mvaa 44,100,76
ta 52,235,36864
mvaa 40,96,76
ta 54,235,37376
mvaa 96,96,80
ra 20,232,43008
mvaa 100,100,80
ra 24,232,45056
mvaa 108,100,84
dl 225,36,52,1024
mvaa 104,96,84
ra 28,232,47104
mvaa 160,96,88
ra 16,232,40960
mvaa 164,100,88
ra 0,232,32768
mvaa 172,100,92
ra 4,232,34816
mvaa 168,96,92
dl 226,36,52,2048
mvaa 184,104,92
ra 12,232,38912
mvaa 188,108,92
ra 8,232,36864
v_add_u32_e32 v232,s90,v232
mvaa 180,108,88
ta 60,237,36864
mvaa 176,104,88
ta 62,237,37376
mvaa 120,104,84
dl 227,36,52,3072
mvaa 124,108,84
s_add_u32 m0,s62,0
ta 56,236,36864
s_add_u32 s52,s52,s60
mvaa 116,108,80
ta 58,236,37376
mvaa 112,104,80
mvaa 56,104,76
mvaa 60,108,76
dl 228,40,53,0
mvaa 52,108,72
mvaa 48,104,72
maaa 152,104,68
maaa 156,108,68
maaa 148,108,64
dl 229,40,53,1024
maaa 144,104,64
s_add_u32 s53,s53,s57
mvaa 0,112,64
mvaa 4,116,64
mvaa 12,116,68
mvaa 8,112,68
dl 230,44,54,2048
mvaa 64,112,72
mvaa 68,116,72
mvaa 76,116,76
mvaa 72,112,76
mvaa 128,112,80
dl 231,44,54,3072
mvaa 132,116,80
s_add_u32 m0,s62,16384
s_add_u32 s54,s54,s57
mvaa 140,116,84
mvaa 136,112,84
mvaa 192,112,88
mvaa 196,116,88
dl 228,40,53,0
mvaa 204,116,92
mvaa 200,112,92
mvaa 216,120,92
mvaa 220,124,92
mvaa 212,124,88
dl 229,40,53,1024
mvaa 208,120,88
s_waitcnt vmcnt(19) lgkmcnt(0)
s_barrier
mvaa 152,120,84
s_add_u32 s53,s53,s58
ta 96,234,49152
mvaa 156,124,84
ra 92,233,47104
mvaa 148,124,80
ta 98,234,49664
mvaa 144,120,80
dl 230,44,54,2048
mvaa 88,120,76
ra 88,233,45056
mvaa 92,124,76
ta 100,235,49152
mvaa 84,124,72
ta 112,234,53248
mvaa 80,120,72
ta 116,235,53248
mvaa 24,120,68
dl 231,44,54,3072
mvaa 28,124,68
s_add_u32 m0,s98,0
ta 118,235,53760
s_add_u32 s54,s54,s58
mvaa 20,124,64
ta 114,234,53760
v_subrev_u32_e32 v234,s89,v234
mvaa 16,120,64
ta 102,235,49664
v_subrev_u32_e32 v235,s89,v235
maaa 128,32,0
ta 124,237,53248
maaa 132,36,0
dl 224,36,52,0
maaa 140,36,4
ta 126,237,53760
maaa 136,32,4
ta 120,236,53248
mvaa 32,32,8
ta 122,236,53760
mvaa 36,36,8
ta 104,236,49152
mvaa 44,36,12
dl 225,36,52,1024
mvaa 40,32,12
ta 106,236,49664
v_subrev_u32_e32 v236,s89,v236
mvaa 56,40,12
ta 108,237,49152
mvaa 60,44,12
ta 110,237,49664
v_subrev_u32_e32 v237,s89,v237
mvaa 52,44,8
ra 84,233,43008
mvaa 48,40,8
dl 226,36,52,2048
maaa 152,40,4
ra 76,233,38912
maaa 156,44,4
ra 80,233,40960
maaa 148,44,0
ra 68,233,34816
maaa 144,40,0
ra 72,233,36864
mvaa 0,48,0
dl 227,36,52,3072
mvaa 4,52,0
s_add_u32 s52,s52,s56
s_add_u32 m0,s98,4096
ra 64,233,32768
mvaa 12,52,4
v_add_u32_e32 v233,s91,v233
mvaa 8,48,4
mvaa 24,56,4
mvaa 28,60,4
dl 224,36,52,0
mvaa 20,60,0
mvaa 16,56,0
mvaa 220,60,28
s_waitcnt lgkmcnt(2)
mvaa 220,124,92
mvaa 216,120,92
dl 225,36,52,1024
mvaa 216,56,28
mvaa 212,60,24
mvaa 212,124,88
mvaa 208,120,88
mvaa 208,56,24
dl 226,36,52,2048
s_waitcnt vmcnt(25)
zvaa 246,160,176
mvaa 204,52,28
cv 223,222,223
cv 222,220,221
mvaa 204,116,92
cv 221,218,219
zvaa 250,168,176
mvaa 200,112,92
cv 220,216,217
cv 219,214,215
mvaa 200,48,28
cv 218,212,213
cv 217,210,211
mvaa 196,52,24
dl 227,36,52,3072
mvaa 196,116,88
s_add_u32 m0,s62,32768
cv 216,208,209
cv 215,206,207
mvaa 192,112,88
s_add_u32 s52,s52,s60
cv 214,204,205
cv 213,202,203
mvaa 192,48,24
cv 212,200,201
s_waitcnt vmcnt(24)
mvaa 246,164,180
mvaa 250,172,180
mvaa 188,44,28
cv 211,198,199
cv 210,196,197
mvaa 188,108,92
dl 228,40,53,0
mvaa 184,104,92
cv 209,194,195
v_cndmask_b32_e64 v246,0,v246,s[30:31]
v_cndmask_b32_e64 v250,0,v250,s[30:31]
s_lshl_b64 s[30:31],s[30:31],1
v_cndmask_b32_e64 v246,v246,v247,s[30:31]
v_cndmask_b32_e64 v250,v250,v251,s[30:31]
s_lshl_b64 s[30:31],s[30:31],1
mvaa 184,40,28
v_cndmask_b32_e64 v246,v246,v248,s[30:31]
v_cndmask_b32_e64 v250,v250,v252,s[30:31]
s_lshl_b64 s[30:31],s[30:31],1
v_cndmask_b32_e64 v246,v246,v249,s[30:31]
v_cndmask_b32_e64 v250,v250,v253,s[30:31]
mvaa 180,44,24
v_add_f32_dpp v246,v246,v246 quad_perm:[1,0,3,2] row_mask:0xf bank_mask:0xf
v_add_f32_dpp v250,v250,v250 quad_perm:[1,0,3,2] row_mask:0xf bank_mask:0xf
mvaa 180,108,88
v_add_f32_dpp v246,v246,v246 quad_perm:[2,3,0,1] row_mask:0xf bank_mask:0xf
v_add_f32_dpp v250,v250,v250 quad_perm:[2,3,0,1] row_mask:0xf bank_mask:0xf
mvaa 176,104,88
dl 229,40,53,1024
mvaa 176,40,24
s_add_u32 s53,s53,s57
v_add_f32_dpp v246,v246,v246 row_half_mirror row_mask:0xf bank_mask:0xf
v_add_f32_dpp v250,v250,v250 row_half_mirror row_mask:0xf bank_mask:0xf
mvaa 172,36,28
v_add_f32_dpp v246,v246,v246 row_mirror row_mask:0xf bank_mask:0xf
v_add_f32_dpp v250,v250,v250 row_mirror row_mask:0xf bank_mask:0xf
mvaa 172,100,92
cv 208,192,193
cv 207,190,191
mvaa 168,96,92
v_permlane16_swap_b32_e32 v246,v250
mvaa 168,32,28
dl 230,44,54,2048
v_add_f32_e32 v246,v246,v250
v_mov_b32_e32 v250,v246
mvaa 164,36,24
cv 206,188,189
cv 205,186,187
mvaa 164,100,88
v_permlane32_swap_b32_e32 v246,v250
mvaa 160,96,88
v_add_f32_e32 v246,v246,v250
v_lshrrev_b32_e32 v250,6,v244
mvaa 160,32,24
v_mul_f32_e32 v246,0xbfb8aa3b,v246
cv 204,184,185
mvaa 156,60,20
dl 231,44,54,3072
ex 246
mvaa 156,124,84
s_add_u32 s54,s54,s57
v_add_f32_e32 v246,1.0,v246
cv 203,182,183
mvaa 152,120,84
v_rcp_f32_e32 v246,v246
mvaa 152,56,20
cv 202,180,181
cv 201,178,179
mvaa 148,60,16
cv 200,176,177
cv 199,174,175
mvaa 148,124,80
buffer_load_dword a242,v241,s[48:51],s55 offen
mvaa 144,120,80
s_add_u32 s55,s55,s59
cv 198,172,173
cv 197,170,171
mvaa 144,56,16
s_waitcnt vmcnt(17) lgkmcnt(0)
s_barrier
s_add_u32 m0,s62,49152
mvaa 140,52,20
cv 196,168,169
cv 195,166,167
mvaa 140,116,84
cv 194,164,165
cv 193,162,163
mvaa 136,112,84
dl 228,40,53,0
mvaa 136,48,20
cv 192,160,161
cv 191,158,159
mvaa 132,52,16
ra 4,232,0
cv 190,156,157
mvaa 132,116,80
ta 88,238,12288
cv 189,154,155
mvaa 128,112,80
ta 90,238,12800
cv 188,152,153
mvaa 128,48,16
dl 229,40,53,1024
mvaa 124,44,20
s_add_u32 s53,s53,s58
ta 92,239,12288
cv 187,150,151
mvaa 124,108,84
ta 94,239,12800
cv 186,148,149
mvaa 120,104,84
cv 185,146,147
cv 184,144,145
mvaa 120,40,20
cv 183,142,143
cv 182,140,141
mvaa 116,44,16
dl 230,44,54,2048
mvaa 116,108,80
cv 181,138,139
cv 180,136,137
mvaa 112,104,80
cv 179,134,135
cv 178,132,133
mvaa 112,40,16
cv 177,130,131
cv 176,128,129
mvaa 108,36,20
cv 175,126,127
cv 174,124,125
mvaa 108,100,84
dl 231,44,54,3072
mvaa 104,96,84
s_add_u32 m0,s98,32768
cv 173,122,123
cv 172,120,121
mvaa 104,32,20
s_add_u32 s54,s54,s58
ta 44,235,0
cv 171,118,119
mvaa 100,36,16
ta 46,235,512
cv 170,116,117
mvaa 100,100,80
ta 40,234,0
cv 169,114,115
mvaa 96,96,80
dl 224,36,52,0
mvaa 96,32,16
ta 42,234,512
cv 168,112,113
mvaa 92,60,12
cv 167,110,111
cv 166,108,109
mvaa 92,124,76
ta 84,237,8192
cv 165,106,107
mvaa 88,120,76
ta 86,237,8704
cv 164,104,105
mvaa 88,56,12
dl 225,36,52,1024
mvaa 84,60,8
cv 163,102,103
cv 162,100,101
mvaa 84,124,72
ta 80,236,8192
cv 161,98,99
mvaa 80,120,72
ra 16,232,6144
cv 160,96,97
mvaa 80,56,8
ta 82,236,8704
cv 159,94,95
mvaa 76,52,12
dl 226,36,52,2048
mvaa 76,116,76
ta 60,235,4096
cv 158,92,93
mvaa 72,112,76
ta 62,235,4608
cv 157,90,91
mvaa 72,48,12
ta 56,234,4096
cv 156,88,89
mvaa 68,52,8
ta 58,234,4608
cv 151,78,79
mvaa 68,116,72
dl 227,36,52,3072
mvaa 64,112,72
s_add_u32 m0,s62,0
ra 12,232,4096
s_add_u32 s52,s52,s56
mvaa 64,48,8
ta 52,237,0
cv 150,76,77
mvaa 60,108,76
ta 54,237,512
cv 147,70,71
mvaa 56,104,76
ra 8,232,2048
cv 146,68,69
mvaa 52,108,72
buffer_load_dword a243,v241,s[48:51],s55 offen
mvaa 48,104,72
s_add_u32 s55,s55,s59
ta 48,236,0
cv 145,66,67
mvaa 44,100,76
ta 50,236,512
cv 144,64,65
mvaa 40,96,76
cv 143,62,63
cv 142,60,61
mvaa 36,100,72
cv 141,58,59
cv 140,56,57
mvaa 32,96,72
buffer_load_dword a244,v241,s[48:51],s55 offen
mvaa 28,124,68
s_add_u32 s55,s55,s59
cv 139,54,55
cv 138,52,53
mvaa 24,120,68
cv 137,50,51
cv 136,48,49
mvaa 20,124,64
cv 135,46,47
cv 134,44,45
mvaa 16,120,64
ta 76,235,8192
cv 133,42,43
mvaa 12,116,68
buffer_load_dword a245,v241,s[48:51],s55 offen
mvaa 8,112,68
s_add_u32 s55,s55,s59
ta 78,235,8704
cv 132,40,41
mvaa 4,116,64
cv 131,38,39
cv 130,36,37
mvaa 0,112,64
ta 72,234,8192
cv 129,34,35
maaa 156,108,68
ta 74,234,8704
cv 128,32,33
maaa 152,104,68
s_mov_b64 exec,65537
buffer_store_dword v246,v250,s[8:11],s86 offen sc0 sc1
s_mov_b64 exec,-1
maaa 148,108,64
cv 127,30,31
v_accvgpr_read_b32 v63,a159
maaa 144,104,64
v_accvgpr_read_b32 v62,a158
v_accvgpr_read_b32 v61,a157
maaa 140,100,68
v_accvgpr_read_b32 v60,a156
v_accvgpr_read_b32 v59,a155
maaa 136,96,68
v_accvgpr_read_b32 v58,a154
v_accvgpr_read_b32 v57,a153
maaa 132,100,64
v_accvgpr_read_b32 v56,a152
cv 56,56,57
maaa 128,96,64
ta 68,237,4096
ta 70,237,4608
s_waitcnt lgkmcnt(4)
zaaa 236,40,4
cv 57,58,59
cv 58,60,61
zaaa 220,44,4
ta 64,236,4096
ta 66,236,4608
zaaa 216,44,8
cv 59,62,63
v_accvgpr_read_b32 v55,a151
zaaa 232,40,8
buffer_store_dwordx4 v[56:59],v244,s[24:27],0 offen offset:3072 sc0 sc1
zaaa 228,40,12
v_accvgpr_read_b32 v54,a150
v_accvgpr_read_b32 v53,a149
zaaa 212,44,12
v_accvgpr_read_b32 v52,a148
v_accvgpr_read_b32 v51,a147
zaaa 208,44,16
v_accvgpr_read_b32 v50,a146
v_accvgpr_read_b32 v49,a145
zaaa 224,40,16
v_accvgpr_read_b32 v48,a144
cv 48,48,49
zaaa 192,48,16
cv 49,50,51
cv 50,52,53
zaaa 176,52,16
cv 51,54,55
s_waitcnt vmcnt(26) lgkmcnt(0)
s_barrier
zaaa 180,52,12
dl 228,40,53,0
zaaa 196,48,12
ra 24,233,0
v_accvgpr_read_b32 v47,a143
zaaa 200,48,8
ra 28,233,2048
v_accvgpr_read_b32 v46,a142
v_accvgpr_read_b32 v45,a141
zaaa 184,52,8
ra 32,233,4096
ra 36,233,6144
zaaa 188,52,4
v_accvgpr_read_b32 v43,a139
v_accvgpr_read_b32 v42,a138
zaaa 204,48,4
dl 229,40,53,1024
ta 96,236,24576
zaaa 172,56,4
s_add_u32 s53,s53,s57
ta 98,236,25088
v_accvgpr_read_b32 v41,a137
zaaa 156,60,4
ta 100,237,24576
v_accvgpr_read_b32 v39,a135
zaaa 152,60,8
ta 102,237,25088
ta 104,238,28672
zaaa 168,56,8
v_accvgpr_read_b32 v38,a134
v_accvgpr_read_b32 v35,a131
zaaa 164,56,12
buffer_store_dwordx4 v[48:51],v244,s[24:27],0 offen offset:2048 sc0 sc1
ta 106,238,29184
zaaa 148,60,12
ta 108,239,28672
v_accvgpr_read_b32 v34,a130
zaaa 144,60,16
ta 110,239,29184
v_accvgpr_read_b32 v33,a129
zaaa 160,56,16
ta 44,235,16384
ta 46,235,16896
ta 40,234,16384
ta 42,234,16896
ta 52,237,16384
ta 54,237,16896
ta 48,236,16384
ta 50,236,16896
ta 60,235,20480
ta 62,235,20992
ta 56,234,20480
ta 58,234,20992
v_accvgpr_read_b32 v32,a128
zaaa 128,64,16
v_accvgpr_read_b32 v37,a133
v_accvgpr_read_b32 v36,a132
zaaa 112,68,16
dl 230,44,54,2048
zaaa 116,68,12
v_accvgpr_read_b32 v40,a136
v_accvgpr_read_b32 v44,a140
zaaa 132,64,12
cv 251,42,43
cv 250,40,41
zaaa 136,64,8
cv 253,46,47
cv 252,44,45
zaaa 120,68,8
cv 126,28,29
cv 125,26,27
zaaa 124,68,4
dl 231,44,54,3072
zaaa 140,64,4
s_add_u32 m0,s61,4096
cv 249,38,39
cv 247,34,35
zvaa 40,72,4
s_add_u32 s54,s54,s57
ta 68,237,20480
cv 246,32,33
zvaa 44,76,4
s_add_u32 s61,s63,s98
ta 70,237,20992
cv 248,36,37
zvaa 52,76,8
s_add_u32 s63,s63,s99
ta 64,236,20480
cv 124,24,25
zvaa 48,72,8
dl 224,36,52,0
zvaa 56,72,12
ta 66,236,20992
cv 123,22,23
zvaa 60,76,12
cv 121,18,19
cv 120,16,17
zvaa 68,76,16
cv 122,20,21
cv 119,14,15
zvaa 64,72,16
cv 118,12,13
cv 117,10,11
zvaa 32,80,16
dl 225,36,52,1024
ta 76,235,24576
ta 78,235,25088
zvaa 36,84,16
ta 72,234,24576
ta 74,234,25088
zvaa 28,84,12
cv 116,8,9
cv 153,82,83
zvaa 24,80,12
cv 152,80,81
cv 155,86,87
zvaa 16,80,8
cv 154,84,85
cv 149,74,75
zvaa 20,84,8
dl 226,36,52,2048
zvaa 12,84,4
cv 148,72,73
v_add_u32_e32 v232,s99,v232
zvaa 8,80,4
cv 115,6,7
cv 114,4,5
zvaa 76,4,88
cv 113,2,3
cv 112,0,1
zvaa 96,4,92
zvaa 100,8,92
dl 227,36,52,3072
zvaa 80,8,88
s_add_u32 s52,s52,s60
zvaa 84,12,88
zvaa 104,12,92
zvaa 108,16,92
s_waitcnt vmcnt(23) lgkmcnt(0)
s_barrier
zvaa 88,16,88
buffer_store_dwordx4 v[250:253],v244,s[24:27],0 offen offset:1024 sc0 sc1
buffer_store_dwordx4 v[246:249],v244,s[24:27],0 offen offset:0 sc0 sc1
s_branch L_kvx
L_vtile:
s_waitcnt vmcnt(8)
s_barrier
ta 32,234,0
ta 34,234,512
ra 0,232,0
ta 36,235,0
ta 38,235,512
ra 4,232,2048
ra 8,232,4096
ra 12,232,6144
ra 16,232,8192
ra 20,232,10240
ra 24,232,12288
ra 28,232,14336
s_add_u32 m0,s62,32768
ta 40,236,0
dl 228,40,53,0
ta 42,236,512
ta 44,237,0
ta 46,237,512
ta 48,234,4096
ta 50,234,4608
ta 52,235,4096
ta 54,235,4608
ta 56,236,4096
ta 58,236,4608
ta 60,237,4096
ta 62,237,4608
s_cmp_ge_u32 s63,65536
s_cselect_b32 s99,-2,1
s_lshl_b32 s99,s99,15
s_add_u32 s63,s63,s99
v_add_u32_e32 v232,s99,v232
s_mov_b32 s64,1
s_waitcnt lgkmcnt(15)
zaaa 128,0,32
zaaa 136,0,36
zaaa 140,4,36
zaaa 132,4,32
dl 229,40,53,1024
zvaa 32,8,32
s_add_u32 s53,s53,s57
zvaa 40,8,36
zvaa 44,12,36
zvaa 36,12,32
dl 230,44,54,2048
zvaa 96,16,32
zvaa 104,16,36
s_waitcnt lgkmcnt(0)
zvaa 108,20,36
zvaa 100,20,32
dl 231,44,54,3072
zvaa 160,24,32
s_add_u32 m0,s98,36864
s_add_u32 s54,s54,s57
zvaa 168,24,36
zvaa 172,28,36
zvaa 164,28,32
dl 224,36,52,0
zvaa 180,28,40
zvaa 188,28,44
zvaa 184,24,44
zvaa 176,24,40
dl 225,36,52,1024
zvaa 116,20,40
zvaa 124,20,44
zvaa 120,16,44
zvaa 112,16,40
dl 226,36,52,2048
zvaa 52,12,40
s_waitcnt vmcnt(11)
s_barrier
zvaa 60,12,44
ta 96,234,16384
zvaa 56,8,44
ta 98,234,16896
zvaa 48,8,40
dl 227,36,52,3072
zaaa 148,4,40
s_add_u32 m0,s62,49152
ra 64,233,0
s_add_u32 s52,s52,s60
zaaa 156,4,44
ta 100,235,16384
zaaa 152,0,44
ta 102,235,16896
zaaa 144,0,40
dl 228,40,53,0
zvaa 0,0,48
ra 68,233,2048
zvaa 8,0,52
ta 104,236,16384
zvaa 12,4,52
ta 106,236,16896
zvaa 4,4,48
dl 229,40,53,1024
zvaa 64,8,48
s_add_u32 s53,s53,s58
ra 72,233,4096
zvaa 72,8,52
ta 108,237,16384
zvaa 76,12,52
ta 110,237,16896
zvaa 68,12,48
dl 230,44,54,2048
zvaa 128,16,48
ra 76,233,6144
zvaa 136,16,52
ta 112,234,20480
zvaa 140,20,52
ta 114,234,20992
zvaa 132,20,48
dl 231,44,54,3072
zvaa 192,24,48
)"
R"(s_add_u32 m0,s61,0
ra 80,233,8192
s_add_u32 s54,s54,s58
zvaa 200,24,52
ta 116,235,20480
zvaa 204,28,52
ta 118,235,20992
zvaa 196,28,48
dl 224,36,52,0
zvaa 212,28,56
ra 84,233,10240
zvaa 220,28,60
ta 120,236,20480
zvaa 216,24,60
ta 122,236,20992
zvaa 208,24,56
dl 225,36,52,1024
ra 88,233,12288
zvaa 148,20,56
ta 124,237,20480
zvaa 156,20,60
ta 126,237,20992
zvaa 152,16,60
ra 92,233,14336
zvaa 144,16,56
dl 226,36,52,2048
zvaa 84,12,56
zvaa 92,12,60
zvaa 88,8,60
zvaa 80,8,56
dl 227,36,52,3072
zvaa 20,4,56
s_add_u32 s52,s52,s56
zvaa 28,4,60
zvaa 24,0,60
zvaa 16,0,56
buffer_load_dword a184,v241,s[48:51],s55 offen
s_add_u32 s55,s55,s59
s_waitcnt vmcnt(9)
s_waitcnt lgkmcnt(0)
s_barrier
L_v_head:
maaa 128,64,96
ta 32,234,32768
maaa 136,64,100
s_add_u32 m0,s62,0
ta 34,234,33280
maaa 140,68,100
dl 228,40,53,0
maaa 132,68,96
mvaa 32,72,96
ra 0,232,0
mvaa 40,72,100
mvaa 44,76,100
ta 36,235,32768
mvaa 36,76,96
dl 229,40,53,1024
mvaa 96,80,96
s_add_u32 s53,s53,s57
mvaa 104,80,100
ta 38,235,33280
mvaa 108,84,100
mvaa 100,84,96
ra 4,232,2048
mvaa 160,88,96
dl 230,44,54,2048
mvaa 168,88,100
mvaa 172,92,100
ta 40,236,32768
mvaa 164,92,96
mvaa 180,92,104
ta 42,236,33280
mvaa 188,92,108
dl 231,44,54,3072
mvaa 184,88,108
s_add_u32 s54,s54,s57
mvaa 176,88,104
ra 8,232,4096
mvaa 116,84,104
mvaa 124,84,108
ta 44,237,32768
mvaa 120,80,108
s_add_u32 m0,s61,4096
mvaa 112,80,104
dl 224,36,52,0
mvaa 52,76,104
ta 46,237,33280
mvaa 60,76,108
mvaa 56,72,108
ra 12,232,6144
mvaa 48,72,104
maaa 148,68,104
ta 48,234,36864
maaa 156,68,108
dl 225,36,52,1024
maaa 152,64,108
ta 50,234,37376
maaa 144,64,104
mvaa 0,64,112
ra 16,232,8192
mvaa 8,64,116
mvaa 12,68,116
ta 52,235,36864
mvaa 4,68,112
dl 226,36,52,2048
mvaa 64,72,112
mvaa 72,72,116
ta 54,235,37376
mvaa 76,76,116
mvaa 68,76,112
ra 20,232,10240
mvaa 128,80,112
mvaa 136,80,116
dl 227,36,52,3072
mvaa 140,84,116
s_add_u32 s52,s52,s60
ta 56,236,36864
mvaa 132,84,112
v_add_u32_e32 v233,s99,v233
mvaa 192,88,112
ta 58,236,37376
s_sub_u32 s61,s63,s99
mvaa 200,88,116
s_add_u32 s61,s61,s98
mvaa 204,92,116
ra 24,232,12288
mvaa 196,92,112
buffer_load_dword a185,v241,s[48:51],s55 offen
s_add_u32 s55,s55,s59
mvaa 212,92,120
mvaa 220,92,124
ta 60,237,36864
mvaa 216,88,124
mvaa 208,88,120
ta 62,237,37376
mvaa 148,84,120
mvaa 156,84,124
ra 28,232,14336
mvaa 152,80,124
mvaa 144,80,120
mvaa 84,76,120
mvaa 92,76,124
mvaa 88,72,124
mvaa 80,72,120
mvaa 20,68,120
mvaa 28,68,124
mvaa 24,64,124
s_waitcnt vmcnt(10) lgkmcnt(0)
s_barrier
mvaa 16,64,120
maaa 128,0,32
ta 96,234,49152
maaa 136,0,36
s_add_u32 m0,s62,16384
ta 98,234,49664
maaa 140,4,36
dl 228,40,53,0
maaa 132,4,32
mvaa 32,8,32
ra 64,233,0
mvaa 40,8,36
mvaa 44,12,36
ta 100,235,49152
mvaa 36,12,32
dl 229,40,53,1024
mvaa 96,16,32
s_add_u32 s53,s53,s58
mvaa 104,16,36
ta 102,235,49664
mvaa 108,20,36
mvaa 100,20,32
ra 68,233,2048
mvaa 160,24,32
dl 230,44,54,2048
mvaa 168,24,36
mvaa 172,28,36
ta 104,236,49152
mvaa 164,28,32
mvaa 180,28,40
ta 106,236,49664
mvaa 188,28,44
dl 231,44,54,3072
mvaa 184,24,44
s_add_u32 s54,s54,s58
mvaa 176,24,40
ra 72,233,4096
mvaa 116,20,40
mvaa 124,20,44
ta 108,237,49152
mvaa 120,16,44
s_add_u32 m0,s61,0
mvaa 112,16,40
dl 224,36,52,0
mvaa 52,12,40
ta 110,237,49664
mvaa 60,12,44
mvaa 56,8,44
ra 76,233,6144
mvaa 48,8,40
maaa 148,4,40
ta 112,234,53248
maaa 156,4,44
dl 225,36,52,1024
maaa 152,0,44
ta 114,234,53760
maaa 144,0,40
mvaa 0,0,48
ra 80,233,8192
mvaa 8,0,52
mvaa 12,4,52
ta 116,235,53248
mvaa 4,4,48
dl 226,36,52,2048
mvaa 64,8,48
mvaa 72,8,52
ta 118,235,53760
mvaa 76,12,52
mvaa 68,12,48
ra 84,233,10240
mvaa 128,16,48
mvaa 136,16,52
dl 227,36,52,3072
mvaa 140,20,52
s_add_u32 s52,s52,s56
ta 120,236,53248
mvaa 132,20,48
s_cmp_ge_u32 s63,65536
s_cselect_b32 s99,-2,1
s_lshl_b32 s99,s99,15
mvaa 192,24,48
ta 122,236,53760
s_add_u32 s63,s63,s99
mvaa 200,24,52
v_add_u32_e32 v232,s99,v232
mvaa 204,28,52
ra 88,233,12288
mvaa 196,28,48
buffer_load_dword a186,v241,s[48:51],s55 offen
s_add_u32 s55,s55,s59
mvaa 212,28,56
mvaa 220,28,60
ta 124,237,53248
mvaa 216,24,60
mvaa 208,24,56
ta 126,237,53760
mvaa 148,20,56
mvaa 156,20,60
ra 92,233,14336
mvaa 152,16,60
mvaa 144,16,56
mvaa 84,12,56
mvaa 92,12,60
mvaa 88,8,60
mvaa 80,8,56
mvaa 20,4,56
mvaa 28,4,60
mvaa 24,0,60
s_waitcnt vmcnt(10) lgkmcnt(0)
s_barrier
mvaa 16,0,56
maaa 128,64,96
ta 32,234,0
maaa 136,64,100
s_add_u32 m0,s62,32768
ta 34,234,512
maaa 140,68,100
dl 228,40,53,0
maaa 132,68,96
mvaa 32,72,96
ra 0,232,0
mvaa 40,72,100
mvaa 44,76,100
ta 36,235,0
mvaa 36,76,96
dl 229,40,53,1024
mvaa 96,80,96
s_add_u32 s53,s53,s57
mvaa 104,80,100
ta 38,235,512
mvaa 108,84,100
mvaa 100,84,96
ra 4,232,2048
mvaa 160,88,96
dl 230,44,54,2048
mvaa 168,88,100
mvaa 172,92,100
ta 40,236,0
mvaa 164,92,96
mvaa 180,92,104
ta 42,236,512
mvaa 188,92,108
dl 231,44,54,3072
mvaa 184,88,108
s_add_u32 s54,s54,s57
mvaa 176,88,104
ra 8,232,4096
mvaa 116,84,104
mvaa 124,84,108
ta 44,237,0
mvaa 120,80,108
s_add_u32 m0,s61,4096
mvaa 112,80,104
dl 224,36,52,0
mvaa 52,76,104
ta 46,237,512
mvaa 60,76,108
mvaa 56,72,108
ra 12,232,6144
mvaa 48,72,104
maaa 148,68,104
ta 48,234,4096
maaa 156,68,108
dl 225,36,52,1024
maaa 152,64,108
ta 50,234,4608
maaa 144,64,104
mvaa 0,64,112
ra 16,232,8192
mvaa 8,64,116
mvaa 12,68,116
ta 52,235,4096
mvaa 4,68,112
dl 226,36,52,2048
mvaa 64,72,112
mvaa 72,72,116
ta 54,235,4608
mvaa 76,76,116
mvaa 68,76,112
ra 20,232,10240
mvaa 128,80,112
mvaa 136,80,116
dl 227,36,52,3072
mvaa 140,84,116
s_add_u32 s52,s52,s60
ta 56,236,4096
mvaa 132,84,112
v_add_u32_e32 v233,s99,v233
mvaa 192,88,112
ta 58,236,4608
s_sub_u32 s61,s63,s99
mvaa 200,88,116
s_add_u32 s61,s61,s98
mvaa 204,92,116
ra 24,232,12288
mvaa 196,92,112
buffer_load_dword a187,v241,s[48:51],s55 offen
s_add_u32 s55,s55,s59
mvaa 212,92,120
mvaa 220,92,124
ta 60,237,4096
mvaa 216,88,124
mvaa 208,88,120
ta 62,237,4608
mvaa 148,84,120
mvaa 156,84,124
ra 28,232,14336
mvaa 152,80,124
mvaa 144,80,120
mvaa 84,76,120
mvaa 92,76,124
mvaa 88,72,124
mvaa 80,72,120
mvaa 20,68,120
mvaa 28,68,124
s_cmp_eq_u32 s64,3
mvaa 24,64,124
s_waitcnt vmcnt(10) lgkmcnt(0)
s_barrier
mvaa 16,64,120
s_cbranch_scc1 L_v_exit
maaa 128,0,32
ta 96,234,16384
maaa 136,0,36
s_add_u32 m0,s62,49152
ta 98,234,16896
maaa 140,4,36
dl 228,40,53,0
maaa 132,4,32
mvaa 32,8,32
ra 64,233,0
mvaa 40,8,36
mvaa 44,12,36
ta 100,235,16384
mvaa 36,12,32
dl 229,40,53,1024
mvaa 96,16,32
s_add_u32 s53,s53,s58
mvaa 104,16,36
ta 102,235,16896
mvaa 108,20,36
mvaa 100,20,32
ra 68,233,2048
mvaa 160,24,32
dl 230,44,54,2048
mvaa 168,24,36
mvaa 172,28,36
ta 104,236,16384
mvaa 164,28,32
mvaa 180,28,40
ta 106,236,16896
mvaa 188,28,44
dl 231,44,54,3072
mvaa 184,24,44
s_add_u32 s54,s54,s58
mvaa 176,24,40
ra 72,233,4096
s_bitcmp1_b32 s65,s64
s_cbranch_scc1 L_v_ev
L_v_evr:
mvaa 116,20,40
mvaa 124,20,44
ta 108,237,16384
mvaa 120,16,44
s_add_u32 m0,s61,0
mvaa 112,16,40
dl 224,36,52,0
mvaa 52,12,40
ta 110,237,16896
mvaa 60,12,44
mvaa 56,8,44
ra 76,233,6144
mvaa 48,8,40
maaa 148,4,40
ta 112,234,20480
maaa 156,4,44
dl 225,36,52,1024
maaa 152,0,44
ta 114,234,20992
maaa 144,0,40
mvaa 0,0,48
ra 80,233,8192
mvaa 8,0,52
mvaa 12,4,52
ta 116,235,20480
mvaa 4,4,48
dl 226,36,52,2048
mvaa 64,8,48
mvaa 72,8,52
ta 118,235,20992
mvaa 76,12,52
mvaa 68,12,48
ra 84,233,10240
mvaa 128,16,48
mvaa 136,16,52
dl 227,36,52,3072
mvaa 140,20,52
s_add_u32 s52,s52,s56
ta 120,236,20480
mvaa 132,20,48
s_cmp_ge_u32 s63,65536
s_cselect_b32 s99,-2,1
s_lshl_b32 s99,s99,15
mvaa 192,24,48
ta 122,236,20992
s_add_u32 s63,s63,s99
mvaa 200,24,52
v_add_u32_e32 v232,s99,v232
mvaa 204,28,52
ra 88,233,12288
mvaa 196,28,48
buffer_load_dword a184,v241,s[48:51],s55 offen
s_add_u32 s55,s55,s59
mvaa 212,28,56
mvaa 220,28,60
ta 124,237,20480
mvaa 216,24,60
mvaa 208,24,56
ta 126,237,20992
mvaa 148,20,56
mvaa 156,20,60
ra 92,233,14336
mvaa 152,16,60
mvaa 144,16,56
mvaa 84,12,56
mvaa 92,12,60
mvaa 88,8,60
mvaa 80,8,56
mvaa 20,4,56
mvaa 28,4,60
s_mov_b32 s50,s33
s_mov_b32 s59,s34
s_lshl_b32 s86,s21,2
s_add_u32 s86,s86,s12
s_mul_i32 s86,s86,384
s_add_u32 s86,s86,4096
v_lshl_add_u32 v253,v244,3,s86
s_mov_b64 exec,7
global_load_dword a188,v253,s[96:97]
s_mov_b64 exec,-1
s_add_u32 s64,s64,1
mvaa 24,0,60
s_waitcnt vmcnt(10) lgkmcnt(0)
s_barrier
mvaa 16,0,56
s_branch L_v_head
L_v_exit:
global_load_dwordx4 a[160:163],v244,s[30:31] offset:0
s_add_u32 m0,s62,49152
maaa 128,0,32
s_cmp_lt_u32 s12,2
s_cselect_b32 s86,s22,s23
ta 96,234,16384
v_accvgpr_read_b32 v224,a242
maaa 136,0,36
s_and_b32 s87,s12,1
ta 98,234,16896
s_lshl_b32 s86,s86,11
maaa 140,4,36
s_lshl_b32 s87,s87,17
ra 64,233,0
s_add_u32 s86,s86,s87
maaa 132,4,32
s_mov_b64 s[36:37],s[68:69]
s_mov_b32 s38,4194304
s_mov_b32 s52,s86
mvaa 32,8,32
dl 228,40,53,0
mvaa 40,8,36
s_mov_b32 s56,65536
ta 100,235,16384
v_accvgpr_read_b32 v225,a243
mvaa 44,12,36
s_lshl_b32 s88,s18,1
ta 102,235,16896
v_accvgpr_read_b32 v226,a244
mvaa 36,12,32
s_add_u32 s88,s88,s12
ra 68,233,2048
v_accvgpr_read_b32 v227,a245
mvaa 96,16,32
s_mov_b32 s91,196608
ra 72,233,4096
s_lshl_b32 s88,s88,7
mvaa 104,16,36
dl 229,40,53,1024
mvaa 108,20,36
s_lshl_b32 s89,s18,6
ra 76,233,6144
s_mov_b32 s90,6291456
mvaa 100,20,32
s_cmp_lt_u32 s12,2
s_cselect_b32 s57,262144,s91
ra 80,233,8192
s_add_u32 s53,s53,s58
mvaa 160,24,32
s_cmp_lt_u32 s12,2
s_cselect_b32 s42,8388608,s90
ra 84,233,10240
v_accvgpr_read_b32 v228,a246
mvaa 168,24,36
s_cmp_lt_u32 s12,2
s_cselect_b32 s53,s88,s89
ra 88,233,12288
v_accvgpr_read_b32 v229,a247
mvaa 172,28,36
dl 230,44,54,2048
mvaa 164,28,32
s_bitcmp1_b32 s21,0
s_cselect_b32 s90,s57,0
ra 92,233,14336
s_add_u32 s53,s53,s90
mvaa 180,28,40
s_cmp_lt_u32 s12,2
s_cselect_b64 s[40:41],s[72:73],s[80:81]
ta 104,236,16384
v_accvgpr_read_b32 v230,a248
mvaa 188,28,44
s_lshl_b32 s90,s90,1
ta 106,236,16896
s_mov_b32 s60,-65408
mvaa 184,24,44
s_and_b32 s89,s98,8192
ta 108,237,16384
s_add_u32 s91,s89,65536
mvaa 176,24,40
dl 231,44,54,3072
mvaa 116,20,40
s_add_u32 m0,s98,65536
ta 110,237,16896
s_add_u32 s54,s54,s58
mvaa 124,20,44
s_cmp_eq_u32 s12,3
s_cselect_b64 s[44:45],s[68:69],s[40:41]
ta 112,234,20480
v_accvgpr_read_b32 v231,a249
mvaa 120,16,44
s_cmp_eq_u32 s12,3
s_cselect_b32 s46,0,s42
ta 114,234,20992
s_add_u32 s58,s57,s90
mvaa 112,16,40
s_cmp_eq_u32 s12,3
s_cselect_b32 s54,0,s53
ta 116,235,20480
s_sub_u32 s57,s57,s90
mvaa 52,12,40
global_load_dwordx4 a[168:171],v244,s[30:31] offset:2048
mvaa 60,12,44
s_add_u32 s90,s89,65536
ta 118,235,20992
s_bfe_u32 s88,s21,65539
mvaa 56,8,44
s_lshl_b32 s88,s88,1
ta 120,236,20480
s_lshl_b32 s87,s20,2
mvaa 48,8,40
s_add_u32 s88,s88,s87
ta 122,236,20992
s_and_b32 s87,s12,1
maaa 148,4,40
s_add_u32 s88,s88,s87
ta 124,237,20480
s_mul_i32 s88,s88,524288
maaa 156,4,44
global_load_dwordx4 a[176:179],v244,s[84:85] offset:0
maaa 152,0,44
s_and_b32 s87,s21,7
ta 126,237,20992
s_add_u32 s86,s88,33554432
maaa 144,0,40
s_lshl_b32 s87,s87,16
s_mov_b32 s26,32768
s_add_u32 s86,s86,s87
mvaa 0,0,48
s_lshr_b32 s87,s12,1
s_mov_b32 s27,131072
s_lshl_b32 s87,s87,15
mvaa 8,0,52
s_add_u32 s86,s86,s87
s_lshl_b32 s87,s12,3
s_add_u32 s61,s98,32768
mvaa 12,4,52
global_load_dwordx4 a[164:167],v244,s[30:31] offset:1024
mvaa 4,4,48
s_add_u32 s24,s8,s86
s_addc_u32 s25,s9,0
s_lshl_b32 s86,%1,5
s_add_u32 s86,s86,s87
mvaa 64,8,48
s_add_u32 s86,s86,83886080
s_mov_b32 s63,65536
s_mov_b32 s67,2048
mvaa 72,8,52
s_cmp_ge_u32 s63,65536
s_cselect_b32 s99,-2,1
s_lshl_b32 s99,s99,15
s_mov_b32 s66,8
mvaa 76,12,52
s_cmp_eq_u32 s12,0
s_cselect_b32 s65,96,0
s_mov_b32 s88,4096
s_mov_b32 s64,1
mvaa 68,12,48
global_load_dwordx4 a[172:175],v244,s[30:31] offset:3072
mvaa 128,16,48
s_mov_b32 s30,1048577
s_mov_b32 s31,268435712
s_or_b32 s65,s65,128
mvaa 136,16,52
s_bitcmp1_b32 s12,0
s_cselect_b64 vcc,-1,0
mvaa 140,20,52
mvaa 132,20,48
mvaa 192,24,48
global_load_dwordx4 a[180:183],v244,s[84:85] offset:1024
mvaa 200,24,52
mvaa 204,28,52
mvaa 196,28,48
mvaa 212,28,56
mvaa 220,28,60
dl 224,36,52,0
mvaa 216,24,60
mvaa 208,24,56
mvaa 148,20,56
mvaa 156,20,60
mvaa 152,16,60
dl 225,36,52,1024
mvaa 144,16,56
mvaa 84,12,56
s_waitcnt vmcnt(13) lgkmcnt(0)
s_barrier
mvaa 92,12,60
ta 32,234,32768
mvaa 88,8,60
ta 34,234,33280
mvaa 80,8,56
dl 226,36,52,2048
mvaa 20,4,56
ta 36,235,32768
mvaa 28,4,60
ta 38,235,33280
mvaa 24,0,60
ta 40,236,32768
mvaa 16,0,56
ta 42,236,33280
maaa 128,64,96
dl 227,36,52,3072
maaa 136,64,100
s_add_u32 s52,s52,s56
s_add_u32 m0,s98,69632
ta 44,237,32768
maaa 140,68,100
ta 46,237,33280
maaa 132,68,96
ta 48,234,36864
mvaa 32,72,96
ta 50,234,37376
mvaa 40,72,100
dl 224,36,52,0
mvaa 44,76,100
ta 52,235,36864
mvaa 36,76,96
ta 54,235,37376
mvaa 96,80,96
ra 20,232,43008
mvaa 104,80,100
ra 24,232,45056
mvaa 108,84,100
dl 225,36,52,1024
mvaa 100,84,96
ra 28,232,47104
mvaa 160,88,96
ra 16,232,40960
mvaa 168,88,100
ra 0,232,32768
mvaa 172,92,100
ra 4,232,34816
mvaa 164,92,96
dl 226,36,52,2048
mvaa 180,92,104
ra 12,232,38912
mvaa 188,92,108
ra 8,232,36864
v_add_u32_e32 v232,s90,v232
mvaa 184,88,108
ta 60,237,36864
mvaa 176,88,104
ta 62,237,37376
mvaa 116,84,104
dl 227,36,52,3072
mvaa 124,84,108
s_add_u32 m0,s62,0
ta 56,236,36864
s_add_u32 s52,s52,s60
mvaa 120,80,108
ta 58,236,37376
mvaa 112,80,104
mvaa 52,76,104
mvaa 60,76,108
dl 228,40,53,0
mvaa 56,72,108
mvaa 48,72,104
maaa 148,68,104
maaa 156,68,108
maaa 152,64,108
dl 229,40,53,1024
maaa 144,64,104
s_add_u32 s53,s53,s57
mvaa 0,64,112
mvaa 8,64,116
mvaa 12,68,116
mvaa 4,68,112
dl 230,44,54,2048
mvaa 64,72,112
mvaa 72,72,116
mvaa 76,76,116
mvaa 68,76,112
mvaa 128,80,112
dl 231,44,54,3072
mvaa 136,80,116
s_add_u32 m0,s62,16384
s_add_u32 s54,s54,s57
mvaa 140,84,116
mvaa 132,84,112
mvaa 192,88,112
mvaa 200,88,116
dl 228,40,53,0
mvaa 204,92,116
mvaa 196,92,112
mvaa 212,92,120
mvaa 220,92,124
mvaa 216,88,124
dl 229,40,53,1024
mvaa 208,88,120
s_waitcnt vmcnt(19) lgkmcnt(0)
s_barrier
mvaa 148,84,120
s_add_u32 s53,s53,s58
ta 96,234,49152
mvaa 156,84,124
ra 92,233,47104
mvaa 152,80,124
ta 98,234,49664
mvaa 144,80,120
dl 230,44,54,2048
mvaa 84,76,120
ra 88,233,45056
mvaa 92,76,124
ta 100,235,49152
mvaa 88,72,124
ta 102,235,49664
mvaa 80,72,120
ta 116,235,53248
mvaa 20,68,120
dl 231,44,54,3072
mvaa 28,68,124
s_add_u32 m0,s98,0
ta 118,235,53760
v_subrev_u32_e32 v235,s89,v235
mvaa 24,64,124
s_add_u32 s54,s54,s58
ta 104,236,49152
mvaa 16,64,120
ta 106,236,49664
maaa 128,0,32
ta 124,237,53248
maaa 136,0,36
dl 224,36,52,0
maaa 140,4,36
ta 126,237,53760
maaa 132,4,32
ta 120,236,53248
mvaa 32,8,32
ta 122,236,53760
v_subrev_u32_e32 v236,s89,v236
mvaa 40,8,36
ta 112,234,53248
mvaa 44,12,36
dl 225,36,52,1024
mvaa 36,12,32
ta 114,234,53760
v_subrev_u32_e32 v234,s89,v234
mvaa 52,12,40
ta 108,237,49152
mvaa 60,12,44
ta 110,237,49664
v_subrev_u32_e32 v237,s89,v237
mvaa 56,8,44
ra 84,233,43008
mvaa 48,8,40
dl 226,36,52,2048
maaa 148,4,40
ra 76,233,38912
maaa 156,4,44
ra 80,233,40960
maaa 152,0,44
ra 68,233,34816
maaa 144,0,40
ra 72,233,36864
mvaa 0,0,48
dl 227,36,52,3072
mvaa 8,0,52
s_add_u32 s52,s52,s56
s_add_u32 m0,s98,4096
ra 64,233,32768
mvaa 12,4,52
v_add_u32_e32 v233,s91,v233
mvaa 4,4,48
mvaa 20,4,56
mvaa 28,4,60
dl 224,36,52,0
mvaa 24,0,60
mvaa 16,0,56
mvaa 220,28,60
s_waitcnt lgkmcnt(2)
mvaa 220,92,124
mvaa 216,88,124
dl 225,36,52,1024
mvaa 216,24,60
mvaa 212,28,56
mvaa 212,92,120
mvaa 208,88,120
mvaa 208,24,56
dl 226,36,52,2048
s_waitcnt vmcnt(25)
zvaa 246,160,176
mvaa 204,28,52
cv 223,222,223
cv 222,220,221
mvaa 204,92,116
cv 221,218,219
zvaa 250,168,176
mvaa 200,88,116
cv 220,216,217
cv 219,214,215
mvaa 200,24,52
cv 218,212,213
cv 217,210,211
mvaa 196,28,48
dl 227,36,52,3072
mvaa 196,92,112
s_add_u32 m0,s62,32768
cv 216,208,209
cv 215,206,207
mvaa 192,88,112
s_add_u32 s52,s52,s60
cv 214,204,205
cv 213,202,203
mvaa 192,24,48
cv 212,200,201
s_waitcnt vmcnt(24)
mvaa 246,164,180
mvaa 250,172,180
mvaa 188,28,44
cv 211,198,199
cv 210,196,197
mvaa 188,92,108
dl 228,40,53,0
mvaa 184,88,108
cv 209,194,195
v_cndmask_b32_e64 v246,0,v246,s[30:31]
v_cndmask_b32_e64 v250,0,v250,s[30:31]
s_lshl_b64 s[30:31],s[30:31],1
v_cndmask_b32_e64 v246,v246,v247,s[30:31]
v_cndmask_b32_e64 v250,v250,v251,s[30:31]
s_lshl_b64 s[30:31],s[30:31],1
mvaa 184,24,44
v_cndmask_b32_e64 v246,v246,v248,s[30:31]
v_cndmask_b32_e64 v250,v250,v252,s[30:31]
s_lshl_b64 s[30:31],s[30:31],1
v_cndmask_b32_e64 v246,v246,v249,s[30:31]
v_cndmask_b32_e64 v250,v250,v253,s[30:31]
mvaa 180,28,40
v_add_f32_dpp v246,v246,v246 quad_perm:[1,0,3,2] row_mask:0xf bank_mask:0xf
v_add_f32_dpp v250,v250,v250 quad_perm:[1,0,3,2] row_mask:0xf bank_mask:0xf
mvaa 180,92,104
v_add_f32_dpp v246,v246,v246 quad_perm:[2,3,0,1] row_mask:0xf bank_mask:0xf
v_add_f32_dpp v250,v250,v250 quad_perm:[2,3,0,1] row_mask:0xf bank_mask:0xf
mvaa 176,88,104
dl 229,40,53,1024
mvaa 176,24,40
s_add_u32 s53,s53,s57
v_add_f32_dpp v246,v246,v246 row_half_mirror row_mask:0xf bank_mask:0xf
v_add_f32_dpp v250,v250,v250 row_half_mirror row_mask:0xf bank_mask:0xf
mvaa 172,28,36
v_add_f32_dpp v246,v246,v246 row_mirror row_mask:0xf bank_mask:0xf
v_add_f32_dpp v250,v250,v250 row_mirror row_mask:0xf bank_mask:0xf
mvaa 172,92,100
cv 208,192,193
cv 207,190,191
mvaa 168,88,100
v_permlane16_swap_b32_e32 v246,v250
mvaa 168,24,36
dl 230,44,54,2048
v_add_f32_e32 v246,v246,v250
v_mov_b32_e32 v250,v246
mvaa 164,28,32
cv 206,188,189
cv 205,186,187
mvaa 164,92,96
v_permlane32_swap_b32_e32 v246,v250
mvaa 160,88,96
v_add_f32_e32 v246,v246,v250
v_lshrrev_b32_e32 v250,6,v244
mvaa 160,24,32
v_mul_f32_e32 v246,0xbfb8aa3b,v246
cv 204,184,185
mvaa 156,20,60
dl 231,44,54,3072
ex 246
mvaa 156,84,124
s_add_u32 s54,s54,s57
v_add_f32_e32 v246,1.0,v246
cv 203,182,183
mvaa 152,80,124
v_rcp_f32_e32 v246,v246
mvaa 152,16,60
cv 202,180,181
cv 201,178,179
mvaa 148,20,56
cv 200,176,177
cv 199,174,175
mvaa 148,84,120
buffer_load_dword a242,v241,s[48:51],s55 offen
mvaa 144,80,120
s_add_u32 s55,s55,s59
cv 198,172,173
cv 197,170,171
mvaa 144,16,56
s_waitcnt vmcnt(17) lgkmcnt(0)
s_barrier
s_add_u32 m0,s62,49152
mvaa 140,20,52
cv 196,168,169
cv 195,166,167
mvaa 140,84,116
cv 194,164,165
cv 193,162,163
mvaa 136,80,116
dl 228,40,53,0
mvaa 136,16,52
cv 192,160,161
cv 191,158,159
mvaa 132,20,48
ra 4,232,0
cv 190,156,157
mvaa 132,84,112
ta 88,238,12288
cv 189,154,155
mvaa 128,80,112
)"
R"(ta 90,238,12800
cv 188,152,153
mvaa 128,16,48
dl 229,40,53,1024
mvaa 124,20,44
s_add_u32 s53,s53,s58
ta 92,239,12288
cv 187,150,151
mvaa 124,84,108
ta 94,239,12800
cv 186,148,149
mvaa 120,80,108
cv 185,146,147
cv 184,144,145
mvaa 120,16,44
cv 183,142,143
cv 182,140,141
mvaa 116,20,40
dl 230,44,54,2048
mvaa 116,84,104
cv 181,138,139
cv 180,136,137
mvaa 112,80,104
cv 179,134,135
cv 178,132,133
mvaa 112,16,40
cv 177,130,131
cv 176,128,129
mvaa 108,20,36
cv 175,126,127
cv 174,124,125
mvaa 108,84,100
dl 231,44,54,3072
mvaa 104,80,100
s_add_u32 m0,s98,32768
ta 44,235,0
cv 173,122,123
mvaa 104,16,36
s_add_u32 s54,s54,s58
ta 46,235,512
cv 172,120,121
mvaa 100,20,32
cv 171,118,119
cv 170,116,117
mvaa 100,84,96
ta 40,234,0
cv 169,114,115
mvaa 96,80,96
dl 224,36,52,0
mvaa 96,16,32
ta 42,234,512
cv 168,112,113
mvaa 92,12,60
cv 167,110,111
cv 166,108,109
mvaa 92,76,124
cv 165,106,107
cv 164,104,105
mvaa 88,72,124
ta 84,237,8192
cv 163,102,103
mvaa 88,8,60
dl 225,36,52,1024
mvaa 84,12,56
ta 86,237,8704
cv 162,100,101
mvaa 84,76,120
ta 80,236,8192
cv 161,98,99
mvaa 80,72,120
ra 16,232,6144
cv 160,96,97
mvaa 80,8,56
ta 82,236,8704
cv 159,94,95
mvaa 76,12,52
dl 226,36,52,2048
mvaa 76,76,116
ta 60,235,4096
cv 158,92,93
mvaa 72,72,116
ta 62,235,4608
cv 157,90,91
mvaa 72,8,52
ta 56,234,4096
cv 156,88,89
mvaa 68,12,48
ta 58,234,4608
cv 151,78,79
mvaa 68,76,112
dl 227,36,52,3072
mvaa 64,72,112
s_add_u32 m0,s62,0
ta 52,237,0
s_add_u32 s52,s52,s56
mvaa 64,8,48
ra 12,232,4096
cv 150,76,77
mvaa 60,76,108
ta 54,237,512
cv 147,70,71
mvaa 56,72,108
ra 8,232,2048
cv 146,68,69
mvaa 52,76,104
buffer_load_dword a243,v241,s[48:51],s55 offen
mvaa 48,72,104
s_add_u32 s55,s55,s59
ta 48,236,0
cv 145,66,67
mvaa 44,76,100
ta 50,236,512
cv 144,64,65
mvaa 40,72,100
cv 143,62,63
cv 142,60,61
mvaa 36,76,96
cv 141,58,59
cv 140,56,57
mvaa 32,72,96
buffer_load_dword a244,v241,s[48:51],s55 offen
mvaa 28,68,124
s_add_u32 s55,s55,s59
cv 139,54,55
cv 138,52,53
mvaa 24,64,124
cv 137,50,51
cv 136,48,49
mvaa 20,68,120
cv 135,46,47
cv 134,44,45
mvaa 16,64,120
cv 133,42,43
cv 132,40,41
mvaa 12,68,116
buffer_load_dword a245,v241,s[48:51],s55 offen
mvaa 8,64,116
s_add_u32 s55,s55,s59
ta 76,235,8192
cv 131,38,39
mvaa 4,68,112
ta 78,235,8704
cv 130,36,37
mvaa 0,64,112
ta 72,234,8192
cv 129,34,35
maaa 156,68,108
ta 74,234,8704
cv 128,32,33
maaa 152,64,108
s_mov_b64 exec,65537
buffer_store_dword v246,v250,s[8:11],s86 offen sc0 sc1
s_mov_b64 exec,-1
maaa 148,68,104
cv 127,30,31
v_accvgpr_read_b32 v63,a159
maaa 144,64,104
v_accvgpr_read_b32 v62,a158
v_accvgpr_read_b32 v61,a157
maaa 140,68,100
v_accvgpr_read_b32 v60,a156
v_accvgpr_read_b32 v59,a155
maaa 136,64,100
v_accvgpr_read_b32 v58,a154
v_accvgpr_read_b32 v57,a153
maaa 132,68,96
v_accvgpr_read_b32 v56,a152
cv 56,56,57
maaa 128,64,96
cv 57,58,59
cv 58,60,61
s_waitcnt lgkmcnt(2)
zaaa 236,40,4
ta 68,237,4096
ta 70,237,4608
zaaa 220,44,4
ta 64,236,4096
ta 66,236,4608
zaaa 216,44,8
cv 59,62,63
v_accvgpr_read_b32 v55,a151
zaaa 232,40,8
buffer_store_dwordx4 v[56:59],v244,s[24:27],0 offen offset:3072 sc0 sc1
zaaa 228,40,12
v_accvgpr_read_b32 v54,a150
v_accvgpr_read_b32 v53,a149
zaaa 212,44,12
v_accvgpr_read_b32 v52,a148
v_accvgpr_read_b32 v51,a147
zaaa 208,44,16
v_accvgpr_read_b32 v50,a146
v_accvgpr_read_b32 v49,a145
zaaa 224,40,16
v_accvgpr_read_b32 v48,a144
cv 48,48,49
zaaa 192,48,16
cv 49,50,51
cv 50,52,53
zaaa 176,52,16
cv 51,54,55
s_waitcnt vmcnt(26) lgkmcnt(0)
s_barrier
zaaa 180,52,12
dl 228,40,53,0
zaaa 196,48,12
ra 24,233,0
v_accvgpr_read_b32 v47,a143
zaaa 200,48,8
ra 28,233,2048
v_accvgpr_read_b32 v46,a142
v_accvgpr_read_b32 v45,a141
zaaa 184,52,8
ra 32,233,4096
ra 36,233,6144
zaaa 188,52,4
v_accvgpr_read_b32 v43,a139
v_accvgpr_read_b32 v42,a138
zaaa 204,48,4
dl 229,40,53,1024
ta 96,236,24576
zaaa 172,56,4
s_add_u32 s53,s53,s57
ta 98,236,25088
v_accvgpr_read_b32 v41,a137
zaaa 156,60,4
ta 100,237,24576
v_accvgpr_read_b32 v39,a135
zaaa 152,60,8
ta 102,237,25088
ta 104,238,28672
zaaa 168,56,8
v_accvgpr_read_b32 v38,a134
v_accvgpr_read_b32 v35,a131
zaaa 164,56,12
buffer_store_dwordx4 v[48:51],v244,s[24:27],0 offen offset:2048 sc0 sc1
ta 106,238,29184
zaaa 148,60,12
ta 108,239,28672
v_accvgpr_read_b32 v34,a130
zaaa 144,60,16
ta 110,239,29184
v_accvgpr_read_b32 v33,a129
zaaa 160,56,16
ta 44,235,16384
ta 46,235,16896
ta 40,234,16384
ta 42,234,16896
ta 52,237,16384
ta 54,237,16896
ta 48,236,16384
ta 50,236,16896
ta 60,235,20480
ta 62,235,20992
ta 56,234,20480
ta 58,234,20992
v_accvgpr_read_b32 v32,a128
zaaa 128,64,16
v_accvgpr_read_b32 v37,a133
v_accvgpr_read_b32 v36,a132
zaaa 112,68,16
dl 230,44,54,2048
zaaa 116,68,12
v_accvgpr_read_b32 v40,a136
v_accvgpr_read_b32 v44,a140
zaaa 132,64,12
cv 251,42,43
cv 250,40,41
zaaa 136,64,8
cv 253,46,47
cv 252,44,45
zaaa 120,68,8
cv 126,28,29
cv 125,26,27
zaaa 124,68,4
dl 231,44,54,3072
zaaa 140,64,4
s_add_u32 m0,s61,4096
cv 249,38,39
cv 247,34,35
zvaa 40,72,4
s_add_u32 s54,s54,s57
ta 68,237,20480
cv 246,32,33
zvaa 44,76,4
s_add_u32 s61,s63,s98
ta 70,237,20992
cv 248,36,37
zvaa 52,76,8
s_add_u32 s63,s63,s99
ta 64,236,20480
cv 124,24,25
zvaa 48,72,8
dl 224,36,52,0
zvaa 56,72,12
ta 66,236,20992
cv 123,22,23
zvaa 60,76,12
cv 121,18,19
cv 120,16,17
zvaa 68,76,16
cv 122,20,21
cv 119,14,15
zvaa 64,72,16
cv 118,12,13
cv 117,10,11
zvaa 32,80,16
dl 225,36,52,1024
ta 76,235,24576
ta 78,235,25088
zvaa 36,84,16
ta 72,234,24576
ta 74,234,25088
zvaa 28,84,12
cv 116,8,9
cv 153,82,83
zvaa 24,80,12
cv 152,80,81
cv 155,86,87
zvaa 16,80,8
cv 154,84,85
cv 149,74,75
zvaa 20,84,8
dl 226,36,52,2048
zvaa 12,84,4
cv 148,72,73
v_add_u32_e32 v232,s99,v232
zvaa 8,80,4
cv 115,6,7
cv 114,4,5
zvaa 76,4,88
cv 113,2,3
cv 112,0,1
zvaa 96,4,92
zvaa 100,8,92
dl 227,36,52,3072
zvaa 80,8,88
s_add_u32 s52,s52,s60
zvaa 84,12,88
zvaa 104,12,92
zvaa 108,16,92
s_waitcnt vmcnt(23) lgkmcnt(0)
s_barrier
zvaa 88,16,88
buffer_store_dwordx4 v[250:253],v244,s[24:27],0 offen offset:1024 sc0 sc1
buffer_store_dwordx4 v[246:249],v244,s[24:27],0 offen offset:0 sc0 sc1
L_kvx:
L_p_exit:
s_add_u32 m0,s62,16384
ra 4,232,0
s_lshl_b32 s86,s21,2
maaa 236,40,24
dl 228,40,53,0
maaa 220,44,24
s_add_u32 s86,s86,s12
ra 8,232,2048
s_lshl_b32 s86,s86,8
maaa 216,44,28
s_add_u32 s86,s86,L_p_exit-L_att_pc+2048
ra 12,232,4096
v_lshl_add_u32 v253,v244,3,s86
maaa 232,40,28
s_mov_b32 s67,4096
ra 16,232,6144
s_and_b32 s87,s12,1
maaa 228,40,32
s_cmp_lt_u32 s12,2
s_cselect_b32 s94,s22,s23
ta 80,236,40960
s_lshr_b32 s94,s94,6
maaa 212,44,32
s_add_u32 s94,s94,s87
ta 82,236,41472
v_mov_b32_e32 v248,-1
maaa 208,44,36
dl 229,40,53,1024
maaa 224,40,36
s_add_u32 s53,s53,s58
ta 84,237,40960
v_mov_b32_e32 v249,-1
maaa 192,48,36
s_lshl_b32 s87,s18,5
ta 86,237,41472
s_add_u32 s94,s94,s87
maaa 176,52,36
s_lshl_b32 s94,s94,11
ta 88,238,45056
s_add_u32 s94,s94,12582912
maaa 180,52,32
s_bfe_u32 s88,s21,65539
ta 90,238,45568
v_mov_b32_e32 v250,-1
maaa 196,48,32
s_lshl_b32 s87,s20,1
ta 92,239,45056
v_mov_b32_e32 v251,-1
maaa 200,48,28
dl 230,44,54,2048
maaa 184,52,28
s_add_u32 s88,s88,s87
ta 94,239,45568
v_add_u32_e32 v233,s99,v233
maaa 188,52,24
s_cmp_ge_u32 s63,65536
s_cselect_b32 s99,-2,1
ta 44,235,32768
s_lshl_b32 s99,s99,15
maaa 204,48,24
s_lshl_b32 s88,s88,7
ta 46,235,33280
v_add_u32_e32 v232,s99,v232
maaa 172,56,24
s_add_u32 s85,s88,83894272
ta 40,234,32768
v_mov_b32_e32 v247,1
maaa 156,60,24
s_lshl_b32 s91,s12,4
ta 42,234,33280
v_bfe_u32 v252,v244,4,4
maaa 152,60,28
dl 231,44,54,3072
maaa 168,56,28
s_add_u32 m0,s61,0
ta 52,237,32768
s_add_u32 s54,s54,s58
maaa 164,56,32
s_mov_b32 s64,0x55505555
ta 54,237,33280
v_bfe_u32 v241,v244,6,4
maaa 148,60,32
s_mov_b32 s65,0x50005500
ta 48,236,32768
v_and_b32_e32 v241,12,v241
maaa 144,60,36
s_mov_b32 s66,0x50000
ta 50,236,33280
v_sub_u32_e32 v252,v252,v241
maaa 160,56,36
s_cmp_lt_u32 s12,2
s_cselect_b32 s90,s22,s23
v_add_u32_e32 v252,s91,v252
v_mov_b32_e32 v241,0xf149f2ca
maaa 128,64,36
s_mov_b64 exec,3
global_load_dword a250,v253,s[96:97]
s_mov_b64 exec,-1
maaa 112,68,36
s_mul_i32 s33,s90,10240
ta 60,235,36864
v_add_u32_e32 v253,10240,v253
maaa 116,68,32
s_cmp_lt_u32 s12,2
s_cselect_b32 s91,32768,0
ta 62,235,37376
s_add_u32 s91,s91,98304
maaa 132,64,32
s_lshl_b32 s95,s12,12
ta 56,234,36864
v_accvgpr_read_b32 v255,a241
maaa 136,64,28
s_add_u32 s70,s95,32768
ta 58,234,37376
s_add_u32 s72,s95,65536
maaa 120,68,28
s_mov_b64 s[30:31],s[28:29]
s_mov_b64 s[28:29],s[78:79]
v_cvt_f32_i32_e32 v252,v252
maaa 124,68,24
buffer_store_dwordx4 v[112:115],v244,s[24:27],s67 offen offset:0 sc0 sc1
maaa 140,64,24
s_add_u32 s68,s95,0
v_cndmask_b32_e64 v112,v241,0,s[64:65]
s_mov_b32 s64,0xaaa0aaaa
mvaa 40,72,24
s_mov_b32 s65,0xa000aa00
ta 68,237,36864
v_cndmask_b32_e64 v113,v241,0,s[64:65]
mvaa 44,76,24
s_mov_b32 s64,0x55405554
ta 70,237,37376
s_mov_b32 s65,0x40005400
mvaa 52,76,28
s_add_u32 s69,s95,16384
ta 64,236,36864
v_cndmask_b32_e64 v114,v241,0,s[64:65]
mvaa 48,72,28
s_mov_b32 s64,0xaa80aaa8
ta 66,236,37376
s_mov_b32 s65,0x8000a800
mvaa 56,72,32
dl 224,36,52,0
mvaa 60,76,32
s_add_u32 s74,s95,98304
v_cndmask_b32_e64 v115,v241,0,s[64:65]
v_mul_f32_e32 v252,0x7149f2ca,v252
mvaa 68,76,36
s_add_u32 s71,s95,49152
s_add_u32 s73,s95,81920
v_and_b32_e32 v245,48,v244
mvaa 64,72,36
s_lshl_b32 s59,%1,2
v_lshlrev_b32_e32 v245,3,v245
s_lshl_b32 s84,%1,2
mvaa 32,96,36
s_add_u32 s59,s59,s12
ta 76,235,40960
s_add_u32 s75,s95,114688
mvaa 36,100,36
s_add_u32 s84,s84,s12
ta 78,235,41472
s_mul_i32 s84,s84,12288
mvaa 28,100,32
dl 225,36,52,1024
mvaa 24,96,32
s_sub_u32 s55,s14,s13
ta 72,234,40960
s_mov_b32 s48,0x38000000
mvaa 16,96,28
s_mov_b32 s81,1
ta 74,234,41472
s_mov_b32 s84,0
mvaa 20,100,28
mvaa 12,100,24
mvaa 8,96,24
mvaa 76,24,104
dl 226,36,52,2048
mvaa 96,24,108
mvaa 100,28,108
mvaa 80,28,104
mvaa 84,32,104
mvaa 104,32,108
mvaa 108,36,108
dl 227,36,52,3072
mvaa 88,36,104
s_waitcnt vmcnt(20) lgkmcnt(0)
s_barrier
maaa 236,40,4
s_add_u32 m0,s62,32768
ra 24,233,0
s_add_u32 s52,s52,s56
maaa 220,44,4
ra 28,233,2048
maaa 216,44,8
ta 96,236,57344
maaa 232,40,8
ta 98,236,57856
maaa 228,40,12
dl 228,40,53,0
maaa 212,44,12
ta 100,237,57344
maaa 208,44,16
ta 102,237,57856
maaa 224,40,16
ra 32,233,4096
maaa 192,48,16
ta 108,239,61440
maaa 176,52,16
ta 110,239,61952
maaa 180,52,12
buffer_store_dwordx4 v[248:251],v244,s[8:11],s94 offen offset:0 sc0 sc1
maaa 196,48,12
ra 36,233,6144
v_add_u32_e32 v233,s99,v233
maaa 200,48,8
ta 104,238,61440
maaa 184,52,8
ta 106,238,61952
maaa 188,52,4
ta 44,235,49152
maaa 204,48,4
ta 46,235,49664
maaa 172,56,4
buffer_store_dwordx4 v[116:119],v244,s[24:27],s67 offen offset:1024 sc0 sc1
maaa 156,60,4
ta 40,234,49152
maaa 152,60,8
ta 42,234,49664
maaa 168,56,8
ta 52,237,49152
maaa 164,56,12
ta 54,237,49664
maaa 148,60,12
ta 48,236,49152
maaa 144,60,16
dl 229,40,53,1024
maaa 160,56,16
s_add_u32 s53,s53,s57
ta 50,236,49664
maaa 128,64,16
ta 60,235,53248
maaa 112,68,16
ta 62,235,53760
maaa 116,68,12
ta 56,234,53248
maaa 132,64,12
ta 58,234,53760
maaa 136,64,8
dl 230,44,54,2048
maaa 120,68,8
maaa 124,68,4
maaa 140,64,4
mvaa 40,72,4
ta 68,237,53248
mvaa 44,76,4
ta 70,237,53760
mvaa 52,76,8
dl 231,44,54,3072
mvaa 48,72,8
s_add_u32 m0,s61,4096
ta 64,236,53248
s_add_u32 s54,s54,s57
mvaa 56,72,12
s_add_u32 s61,s63,s98
ta 66,236,53760
s_add_u32 s63,s63,s99
mvaa 60,76,12
s_cmp_ge_u32 s63,65536
s_cselect_b32 s99,-2,1
s_lshl_b32 s99,s99,15
mvaa 68,76,16
mvaa 64,72,16
mvaa 32,80,16
dl 224,36,52,0
mvaa 36,84,16
ta 76,235,57344
mvaa 28,84,12
ta 78,235,57856
mvaa 24,80,12
ta 72,234,57344
mvaa 16,80,8
ta 74,234,57856
mvaa 20,84,8
mvaa 12,84,4
dl 225,36,52,1024
mvaa 8,80,4
mvaa 76,4,88
mvaa 96,4,92
mvaa 100,8,92
mvaa 80,8,88
mvaa 84,12,88
dl 226,36,52,2048
mvaa 104,12,92
mvaa 108,16,92
s_waitcnt vmcnt(21) lgkmcnt(0)
s_barrier
mvaa 88,16,88
ra 4,232,0
maaa 236,40,24
ra 8,232,2048
maaa 220,44,24
ta 80,236,8192
maaa 216,44,28
buffer_store_dwordx4 v[120:123],v244,s[24:27],s67 offen offset:2048 sc0 sc1
maaa 232,40,28
ta 82,236,8704
maaa 228,40,32
ta 84,237,8192
maaa 212,44,32
ta 86,237,8704
maaa 208,44,36
ra 12,232,4096
maaa 224,40,36
ta 92,239,12288
maaa 192,48,36
buffer_store_dwordx4 v[248:251],v244,s[8:11],s94 offen offset:1024 sc0 sc1
maaa 176,52,36
ta 94,239,12800
v_and_b32_e32 v250,240,v244
maaa 180,52,32
ra 16,232,6144
v_add_u32_e32 v232,s99,v232
maaa 196,48,32
ta 88,238,12288
v_accvgpr_read_b32 v249,a240
maaa 200,48,28
ta 90,238,12800
maaa 184,52,28
ta 44,235,0
maaa 188,52,24
dl 227,36,52,3072
maaa 204,48,24
s_add_u32 m0,s62,49152
ta 46,235,512
s_add_u32 s52,s52,s60
maaa 172,56,24
ta 40,234,0
maaa 156,60,24
ta 42,234,512
maaa 152,60,28
ta 52,237,0
maaa 168,56,28
ta 54,237,512
maaa 164,56,32
dl 228,40,53,0
maaa 148,60,32
ta 48,236,0
maaa 144,60,36
ta 50,236,512
maaa 160,56,36
maaa 128,64,36
ta 60,235,4096
maaa 112,68,36
ta 62,235,4608
maaa 116,68,32
dl 229,40,53,1024
maaa 132,64,32
s_add_u32 s53,s53,s58
ta 56,234,4096
maaa 136,64,28
ta 58,234,4608
maaa 120,68,28
maaa 124,68,24
maaa 140,64,24
mvaa 40,72,24
dl 230,44,54,2048
mvaa 44,76,24
ta 68,237,4096
mvaa 52,76,28
ta 70,237,4608
mvaa 48,72,28
ta 64,236,4096
mvaa 56,72,32
ta 66,236,4608
mvaa 60,76,32
mvaa 68,76,36
buffer_store_dwordx4 v[124:127],v244,s[24:27],s67 offen offset:3072 sc0 sc1
mvaa 64,72,36
s_mov_b32 s67,8192
v_mov_b32_e32 v124,-1
v_mov_b32_e32 v125,-1
mvaa 32,96,36
ta 76,235,8192
v_mov_b32_e32 v126,-1
mvaa 36,100,36
ta 78,235,8704
v_mov_b32_e32 v127,-1
mvaa 28,100,32
ta 72,234,8192
mvaa 24,96,32
ta 74,234,8704
mvaa 16,96,28
dl 231,44,54,3072
mvaa 20,100,28
s_add_u32 m0,s61,0
s_add_u32 s54,s54,s58
mvaa 12,100,24
mvaa 8,96,24
mvaa 76,24,104
mvaa 96,24,108
mvaa 100,28,108
dl 224,36,52,0
mvaa 80,28,104
mvaa 84,32,104
mvaa 104,32,108
mvaa 108,36,108
s_waitcnt vmcnt(18) lgkmcnt(0)
s_barrier
mvaa 88,36,104
ra 24,233,0
maaa 236,40,4
dl 225,36,52,1024
maaa 220,44,4
ra 28,233,2048
maaa 216,44,8
ta 96,236,24576
maaa 232,40,8
ta 98,236,25088
maaa 228,40,12
ta 100,237,24576
maaa 212,44,12
ta 102,237,25088
maaa 208,44,16
dl 226,36,52,2048
maaa 224,40,16
ra 32,233,4096
maaa 192,48,16
ta 108,239,28672
maaa 176,52,16
ta 110,239,29184
maaa 180,52,12
ra 36,233,6144
v_add_u32_e32 v233,s99,v233
maaa 196,48,12
ta 104,238,28672
maaa 200,48,8
dl 227,36,52,3072
maaa 184,52,8
s_add_u32 m0,s62,0
ta 106,238,29184
s_add_u32 s52,s52,s56
maaa 188,52,4
ta 44,235,16384
maaa 204,48,4
ta 46,235,16896
maaa 172,56,4
ta 40,234,16384
maaa 156,60,4
ta 42,234,16896
maaa 152,60,8
dl 228,40,53,0
maaa 168,56,8
ta 52,237,16384
maaa 164,56,12
ta 54,237,16896
maaa 148,60,12
ta 48,236,16384
maaa 144,60,16
ta 50,236,16896
maaa 160,56,16
maaa 128,64,16
buffer_store_dwordx4 v[128:131],v244,s[24:27],s67 offen offset:0 sc0 sc1
maaa 112,68,16
ta 60,235,20480
v_add_f32_e32 v128,0x70c9f2ca,v252
maaa 116,68,12
ta 62,235,20992
v_add_f32_e32 v129,0xf0c9f2ca,v252
maaa 132,64,12
ta 56,234,20480
v_add_f32_e32 v130,0xf1977617,v252
maaa 136,64,8
ta 58,234,20992
v_add_f32_e32 v131,0xf1fc6f7c,v252
maaa 120,68,8
v_min_f32_e32 v128,0xc1a00000,v128
v_min_f32_e32 v129,0xc1a00000,v129
maaa 124,68,4
dl 229,40,53,1024
maaa 140,64,4
s_add_u32 s53,s53,s57
v_min_f32_e32 v130,0xc1a00000,v130
v_min_f32_e32 v131,0xc1a00000,v131
mvaa 40,72,4
ta 68,237,20480
mvaa 44,76,4
ta 70,237,20992
mvaa 52,76,8
ta 64,236,20480
mvaa 48,72,8
ta 66,236,20992
mvaa 56,72,12
dl 230,44,54,2048
mvaa 60,76,12
mvaa 68,76,16
mvaa 64,72,16
mvaa 32,80,16
ta 76,235,24576
mvaa 36,84,16
ta 78,235,25088
mvaa 28,84,12
dl 231,44,54,3072
mvaa 24,80,12
s_add_u32 m0,s61,4096
ta 72,234,24576
s_add_u32 s54,s54,s57
mvaa 16,80,8
s_add_u32 s61,s63,s98
ta 74,234,25088
s_add_u32 s63,s63,s99
mvaa 20,84,8
s_cmp_ge_u32 s63,65536
s_cselect_b32 s99,-2,1
s_lshl_b32 s99,s99,15
mvaa 12,84,4
mvaa 8,80,4
mvaa 76,4,88
dl 224,36,52,0
mvaa 96,4,92
mvaa 100,8,92
mvaa 80,8,88
mvaa 84,12,88
mvaa 104,12,92
mvaa 108,16,92
dl 225,36,52,1024
mvaa 88,16,88
s_waitcnt vmcnt(16) lgkmcnt(0)
s_barrier
maaa 236,40,24
ra 4,232,0
maaa 220,44,24
ra 8,232,2048
maaa 216,44,28
ta 80,236,40960
maaa 232,40,28
ta 82,236,41472
maaa 228,40,32
buffer_load_dword v246,off,s[8:11],s35 offset:1024 sc1
maaa 212,44,32
ta 84,237,40960
maaa 208,44,36
ta 86,237,41472
maaa 224,40,36
ra 12,232,4096
maaa 192,48,36
ta 92,239,45056
maaa 176,52,36
ta 94,239,45568
maaa 180,52,32
buffer_store_dwordx4 v[132:135],v244,s[24:27],s67 offen offset:1024 sc0 sc1
maaa 196,48,32
ra 16,232,6144
v_add_u32_e32 v232,s99,v232
maaa 200,48,28
ta 88,238,45056
v_add_f32_e32 v132,0xf343a333,v252
maaa 184,52,28
ta 90,238,45568
v_add_f32_e32 v133,0xf3504260,v252
maaa 188,52,24
ta 44,235,32768
v_add_f32_e32 v134,0xf35ce18d,v252
maaa 204,48,24
ta 46,235,33280
v_min_f32_e32 v132,0xc1a00000,v132
maaa 172,56,24
dl 226,36,52,2048
maaa 156,60,24
ta 40,234,32768
v_add_f32_e32 v135,0xf36980b9,v252
maaa 152,60,28
ta 42,234,33280
v_min_f32_e32 v133,0xc1a00000,v133
maaa 168,56,28
ta 52,237,32768
v_min_f32_e32 v134,0xc1a00000,v134
maaa 164,56,32
ta 54,237,33280
v_min_f32_e32 v135,0xc1a00000,v135
maaa 148,60,32
ta 48,236,32768
maaa 144,60,36
dl 227,36,52,3072
maaa 160,56,36
s_add_u32 m0,s62,16384
ta 50,236,33280
s_add_u32 s52,s52,s60
maaa 128,64,36
ta 60,235,36864
maaa 112,68,36
ta 62,235,37376
maaa 116,68,32
ta 56,234,36864
maaa 132,64,32
ta 58,234,37376
maaa 136,64,28
dl 228,40,53,0
maaa 120,68,28
maaa 124,68,24
maaa 140,64,24
mvaa 40,72,24
ta 68,237,36864
mvaa 44,76,24
ta 70,237,37376
mvaa 52,76,28
dl 229,40,53,1024
mvaa 48,72,28
s_add_u32 s53,s53,s58
ta 64,236,36864
mvaa 56,72,32
ta 66,236,37376
mvaa 60,76,32
mvaa 68,76,36
mvaa 64,72,36
mvaa 32,96,36
dl 230,44,54,2048
mvaa 36,100,36
ta 76,235,40960
mvaa 28,100,32
ta 78,235,41472
mvaa 24,96,32
ta 72,234,40960
mvaa 16,96,28
ta 74,234,41472
mvaa 20,100,28
mvaa 12,100,24
buffer_store_dwordx4 v[136:139],v244,s[24:27],s67 offen offset:2048 sc0 sc1
mvaa 8,96,24
mvaa 76,24,104
v_add_f32_e32 v136,0xf3c6caff,v252
v_add_f32_e32 v137,0xf3cd1a95,v252
mvaa 96,24,108
v_add_f32_e32 v138,0xf3d36a2b,v252
v_add_f32_e32 v139,0xf3d9b9c2,v252
mvaa 100,28,108
v_min_f32_e32 v136,0xc1a00000,v136
v_min_f32_e32 v137,0xc1a00000,v137
mvaa 80,28,104
v_min_f32_e32 v138,0xc1a00000,v138
v_min_f32_e32 v139,0xc1a00000,v139
mvaa 84,32,104
dl 231,44,54,3072
mvaa 104,32,108
s_add_u32 m0,s61,0
s_add_u32 s54,s54,s58
mvaa 108,36,108
s_waitcnt vmcnt(16) lgkmcnt(0)
s_barrier
mvaa 88,36,104
ra 24,233,0
maaa 236,40,4
ra 28,233,2048
)"
R"(maaa 220,44,4
ta 96,236,57344
maaa 216,44,8
dl 224,36,52,0
maaa 232,40,8
ta 98,236,57856
maaa 228,40,12
ta 100,237,57344
maaa 212,44,12
ta 102,237,57856
maaa 208,44,16
ra 32,233,4096
maaa 224,40,16
ta 108,239,61440
maaa 192,48,16
dl 225,36,52,1024
maaa 176,52,16
ta 110,239,61952
maaa 180,52,12
ra 36,233,6144
v_add_u32_e32 v233,s99,v233
maaa 196,48,12
ta 104,238,61440
maaa 200,48,8
ta 106,238,61952
maaa 184,52,8
ta 44,235,49152
maaa 188,52,4
dl 226,36,52,2048
maaa 204,48,4
ta 46,235,49664
maaa 172,56,4
ta 40,234,49152
maaa 156,60,4
ta 42,234,49664
maaa 152,60,8
ta 52,237,49152
maaa 168,56,8
ta 54,237,49664
maaa 164,56,12
dl 227,36,52,3072
maaa 148,60,12
s_add_u32 m0,s62,32768
ta 48,236,49152
s_add_u32 s52,s52,s56
maaa 144,60,16
ta 50,236,49664
maaa 160,56,16
s_waitcnt vmcnt(12)
v_readfirstlane_b32 s87,v246
maaa 128,64,16
s_cmp_eq_u32 s12,0
s_cselect_b32 s89,s32,s87
ta 60,235,53248
maaa 112,68,16
s_cmp_lg_u32 s87,s89
s_cbranch_scc1 L_q_ack
L_q_acked:
ta 62,235,53760
s_and_b32 s87,s12,1
maaa 116,68,12
dl 228,40,53,0
maaa 132,64,12
s_lshl_b32 s88,s87,6
ta 56,234,53248
s_add_u32 s90,s90,s88
maaa 136,64,8
s_cmp_eq_u32 s90,0
s_cselect_b64 s[64:65],-1,0
ta 58,234,53760
s_lshl_b32 s88,s87,10
maaa 120,68,8
s_lshl_b32 s87,s87,14
s_add_u32 s33,s33,s88
s_add_u32 s91,s91,s87
maaa 124,68,4
s_lshr_b32 s87,s18,1
v_add_u32_e32 v251,s91,v244
s_lshl_b32 s87,s87,4
maaa 140,64,4
s_lshl_b32 s88,s18,11
s_lshr_b32 s91,s21,3
s_add_u32 s88,s33,s88
mvaa 40,72,4
buffer_store_dwordx4 v[140:143],v244,s[24:27],s67 offen offset:3072 sc0 sc1
mvaa 44,76,4
s_mov_b32 s67,12288
ta 68,237,53248
s_add_u32 s33,s88,63176704
mvaa 52,76,8
s_add_u32 s34,s88,63504384
ta 70,237,53760
s_add_u32 s50,s88,63832064
mvaa 48,72,8
s_add_u32 s51,s88,64159744
ta 64,236,53248
s_and_b32 s88,s20,4
mvaa 56,72,12
s_and_b32 s89,s20,3
ta 66,236,53760
s_add_u32 s91,s91,s88
mvaa 60,76,12
s_lshl_b32 s88,s89,6
v_add_f32_e32 v140,0xf415e232,v252
v_add_f32_e32 v141,0xf41909fd,v252
mvaa 68,76,16
dl 229,40,53,1024
mvaa 64,72,16
s_add_u32 s53,s53,s57
v_add_f32_e32 v142,0xf41c31c8,v252
v_min_f32_e32 v140,0xc1a00000,v140
mvaa 32,80,16
ta 76,235,57344
v_add_f32_e32 v143,0xf41f5993,v252
mvaa 36,84,16
ta 78,235,57856
v_bfe_i32 v252,v244,4,1
mvaa 28,84,12
ta 72,234,57344
v_min_f32_e32 v141,0xc1a00000,v141
mvaa 24,80,12
ta 74,234,57856
v_min_f32_e32 v142,0xc1a00000,v142
mvaa 16,80,8
dl 230,44,54,2048
mvaa 20,84,8
v_min_f32_e32 v143,0xc1a00000,v143
v_xor_b32_e32 v252,65535,v252
mvaa 12,84,4
v_lshrrev_b32_e32 v246,6,v244
v_lshl_add_u32 v245,v246,22,v245
mvaa 8,80,4
v_lshlrev_b32_e32 v246,16,v244
mvaa 76,4,88
mvaa 96,4,92
mvaa 100,8,92
dl 231,44,54,3072
mvaa 80,8,88
s_add_u32 m0,s61,4096
s_add_u32 s54,s54,s57
s_add_u32 s61,s63,s98
mvaa 84,12,88
s_add_u32 s63,s63,s99
mvaa 104,12,92
s_cmp_ge_u32 s63,65536
s_cselect_b32 s99,-2,1
s_lshl_b32 s99,s99,15
mvaa 108,16,92
mvaa 88,16,88
s_waitcnt lgkmcnt(0)
maaa 236,40,24
dl 224,36,52,0
maaa 220,44,24
maaa 216,44,28
maaa 232,40,28
s_waitcnt vmcnt(15)
s_barrier
maaa 228,40,32
ra 4,232,0
maaa 212,44,32
ra 8,232,2048
maaa 208,44,36
dl 225,36,52,1024
maaa 224,40,36
ta 80,236,8192
maaa 192,48,36
ta 82,236,8704
maaa 176,52,36
ta 84,237,8192
maaa 180,52,32
ta 86,237,8704
maaa 196,48,32
ra 12,232,4096
maaa 200,48,28
dl 226,36,52,2048
maaa 184,52,28
ta 92,239,12288
maaa 188,52,24
ra 16,232,6144
v_add_u32_e32 v232,s99,v232
maaa 204,48,24
ta 94,239,12800
maaa 172,56,24
ta 88,238,12288
maaa 156,60,24
ta 90,238,12800
maaa 152,60,28
buffer_store_dwordx4 v[144:147],v244,s[24:27],s67 offen offset:0 sc0 sc1
maaa 168,56,28
ta 44,235,0
maaa 164,56,32
ta 46,235,512
maaa 148,60,32
ta 40,234,0
maaa 144,60,36
ta 42,234,512
maaa 160,56,36
ta 52,237,0
maaa 128,64,36
dl 227,36,52,3072
maaa 112,68,36
s_add_u32 m0,s62,49152
ta 54,237,512
s_add_u32 s52,s52,s60
maaa 116,68,32
ta 48,236,0
maaa 132,64,32
ta 50,236,512
maaa 136,64,28
ta 60,235,4096
maaa 120,68,28
ta 62,235,4608
maaa 124,68,24
dl 228,40,53,0
maaa 140,64,24
ta 56,234,4096
mvaa 40,72,24
ta 58,234,4608
mvaa 44,76,24
ta 68,237,4096
mvaa 52,76,28
ta 70,237,4608
mvaa 48,72,28
ta 64,236,4096
mvaa 56,72,32
dl 229,40,53,1024
mvaa 60,76,32
s_add_u32 s53,s53,s58
ta 66,236,4608
mvaa 68,76,36
mvaa 64,72,36
mvaa 32,96,36
ta 76,235,8192
mvaa 36,100,36
ta 78,235,8704
mvaa 28,100,32
dl 230,44,54,2048
mvaa 24,96,32
ta 72,234,8192
mvaa 16,96,28
ta 74,234,8704
mvaa 20,100,28
mvaa 12,100,24
mvaa 8,96,24
mvaa 76,24,104
dl 231,44,54,3072
mvaa 96,24,108
s_add_u32 m0,s61,0
s_add_u32 s54,s54,s58
mvaa 100,28,108
mvaa 80,28,104
mvaa 84,32,104
mvaa 104,32,108
mvaa 108,36,108
buffer_store_dwordx4 v[148:151],v244,s[24:27],s67 offen offset:1024 sc0 sc1
mvaa 88,36,104
s_waitcnt lgkmcnt(0)
maaa 236,40,4
maaa 220,44,4
s_waitcnt vmcnt(15)
s_barrier
maaa 216,44,8
ra 24,233,0
maaa 232,40,8
ra 28,233,2048
maaa 228,40,12
s_cmp_eq_u32 s12,0
s_cselect_b64 exec,1,0
buffer_atomic_add v247,off,s[8:11],s85 sc1
s_mov_b64 exec,-1
maaa 212,44,12
ta 96,236,24576
v_mov_b32_e32 v247,32
maaa 208,44,16
ta 98,236,25088
maaa 224,40,16
ta 100,237,24576
maaa 192,48,16
ta 102,237,25088
maaa 176,52,16
ra 32,233,4096
maaa 180,52,12
dl 224,36,52,0
maaa 196,48,12
ta 108,239,28672
maaa 200,48,8
ta 110,239,29184
maaa 184,52,8
ra 36,233,6144
v_add_u32_e32 v233,s99,v233
maaa 188,52,4
ta 104,238,28672
maaa 204,48,4
ta 106,238,29184
maaa 172,56,4
dl 225,36,52,1024
maaa 156,60,4
ta 44,235,16384
maaa 152,60,8
ta 46,235,16896
maaa 168,56,8
ta 40,234,16384
maaa 164,56,12
ta 42,234,16896
maaa 148,60,12
ta 52,237,16384
maaa 144,60,16
dl 226,36,52,2048
maaa 160,56,16
ta 54,237,16896
maaa 128,64,16
ta 48,236,16384
maaa 112,68,16
ta 50,236,16896
maaa 116,68,12
ta 60,235,20480
maaa 132,64,12
ta 62,235,20992
maaa 136,64,8
dl 227,36,52,3072
maaa 120,68,8
s_add_u32 m0,s62,0
ta 56,234,20480
s_add_u32 s52,s52,s56
maaa 124,68,4
ta 58,234,20992
maaa 140,64,4
mvaa 40,72,4
ta 68,237,20480
mvaa 44,76,4
ta 70,237,20992
mvaa 52,76,8
dl 228,40,53,0
mvaa 48,72,8
ta 64,236,20480
mvaa 56,72,12
ta 66,236,20992
mvaa 60,76,12
mvaa 68,76,16
mvaa 64,72,16
mvaa 32,80,16
buffer_store_dwordx4 v[152:155],v244,s[24:27],s67 offen offset:2048 sc0 sc1
mvaa 36,84,16
ta 76,235,24576
mvaa 28,84,12
ta 78,235,25088
mvaa 24,80,12
ta 72,234,24576
mvaa 16,80,8
ta 74,234,25088
mvaa 20,84,8
mvaa 12,84,4
dl 229,40,53,1024
mvaa 8,80,4
s_add_u32 s53,s53,s57
mvaa 76,4,88
mvaa 96,4,92
mvaa 100,8,92
mvaa 80,8,88
mvaa 84,12,88
dl 230,44,54,2048
mvaa 104,12,92
mvaa 108,16,92
mvaa 88,16,88
s_waitcnt lgkmcnt(0)
maaa 236,40,24
maaa 220,44,24
maaa 216,44,28
dl 231,44,54,3072
maaa 232,40,28
s_add_u32 m0,s61,4096
s_add_u32 s54,s54,s57
s_add_u32 s61,s63,s98
maaa 228,40,32
s_add_u32 s63,s63,s99
maaa 212,44,32
s_waitcnt vmcnt(14)
s_barrier
maaa 208,44,36
s_cmp_ge_u32 s63,65536
s_cselect_b32 s99,-2,1
ra 4,232,0
s_lshl_b32 s99,s99,15
maaa 224,40,36
ra 8,232,2048
maaa 192,48,36
dl 224,36,52,0
maaa 176,52,36
ta 80,236,40960
maaa 180,52,32
ta 82,236,41472
maaa 196,48,32
ta 84,237,40960
maaa 200,48,28
ta 86,237,41472
maaa 184,52,28
ra 12,232,4096
maaa 188,52,24
dl 225,36,52,1024
maaa 204,48,24
ta 92,239,45056
maaa 172,56,24
ta 94,239,45568
maaa 156,60,24
ra 16,232,6144
v_add_u32_e32 v232,s99,v232
maaa 152,60,28
ta 88,238,45056
maaa 168,56,28
ta 90,238,45568
maaa 164,56,32
dl 226,36,52,2048
maaa 148,60,32
ta 44,235,32768
maaa 144,60,36
ta 46,235,33280
maaa 160,56,36
ta 40,234,32768
maaa 128,64,36
ta 42,234,33280
maaa 112,68,36
ta 52,237,32768
maaa 116,68,32
buffer_store_dwordx4 v[156:159],v244,s[24:27],s67 offen offset:3072 sc0 sc1
maaa 132,64,32
s_mov_b32 s67,16384
ta 54,237,33280
maaa 136,64,28
ta 48,236,32768
maaa 120,68,28
ta 50,236,33280
maaa 124,68,24
ta 60,235,36864
maaa 140,64,24
ta 62,235,37376
mvaa 40,72,24
dl 227,36,52,3072
mvaa 44,76,24
s_add_u32 m0,s62,16384
ta 56,234,36864
s_add_u32 s52,s52,s60
mvaa 52,76,28
ta 58,234,37376
mvaa 48,72,28
ta 68,237,36864
mvaa 56,72,32
ta 70,237,37376
mvaa 60,76,32
ta 64,236,36864
mvaa 68,76,36
dl 228,40,53,0
mvaa 64,72,36
ta 66,236,37376
mvaa 32,96,36
ta 76,235,40960
mvaa 36,100,36
ta 78,235,41472
mvaa 28,100,32
ta 72,234,40960
mvaa 24,96,32
ta 74,234,41472
mvaa 16,96,28
dl 229,40,53,1024
mvaa 20,100,28
s_add_u32 s53,s53,s58
mvaa 12,100,24
mvaa 8,96,24
mvaa 76,24,104
mvaa 96,24,108
mvaa 100,28,108
dl 230,44,54,2048
mvaa 80,28,104
mvaa 84,32,104
mvaa 104,32,108
mvaa 108,36,108
mvaa 88,36,104
s_waitcnt lgkmcnt(0)
maaa 236,40,4
dl 231,44,54,3072
maaa 220,44,4
s_add_u32 m0,s61,0
s_add_u32 s54,s54,s58
maaa 216,44,8
maaa 232,40,8
maaa 228,40,12
maaa 212,44,12
maaa 208,44,16
buffer_store_dwordx4 v[160:163],v244,s[24:27],s67 offen offset:0 sc0 sc1
maaa 224,40,16
maaa 192,48,16
maaa 176,52,16
maaa 180,52,12
s_waitcnt vmcnt(15)
s_barrier
maaa 196,48,12
dl 224,36,52,0
maaa 200,48,8
ra 24,233,0
maaa 184,52,8
ra 28,233,2048
maaa 188,52,4
ra 32,233,4096
maaa 204,48,4
ta 96,236,57344
maaa 172,56,4
dl 225,36,52,1024
maaa 156,60,4
ta 98,236,57856
maaa 152,60,8
ta 100,237,57344
maaa 168,56,8
ta 102,237,57856
maaa 164,56,12
ta 108,239,61440
maaa 148,60,12
ta 110,239,61952
maaa 144,60,16
dl 226,36,52,2048
maaa 160,56,16
ra 36,233,6144
v_add_u32_e32 v233,s99,v233
maaa 128,64,16
ta 104,238,61440
maaa 112,68,16
ta 106,238,61952
maaa 116,68,12
ta 44,235,49152
maaa 132,64,12
dl 227,36,52,3072
maaa 136,64,8
s_add_u32 m0,s62,32768
ta 46,235,49664
s_add_u32 s52,s52,s56
maaa 120,68,8
ta 40,234,49152
maaa 124,68,4
ta 42,234,49664
maaa 140,64,4
ta 52,237,49152
mvaa 40,72,4
dl 228,40,53,0
mvaa 44,76,4
ta 54,237,49664
mvaa 52,76,8
ta 48,236,49152
mvaa 48,72,8
ta 50,236,49664
mvaa 56,72,12
ta 60,235,53248
ta 62,235,53760
mvaa 60,76,12
dl 229,40,53,1024
mvaa 68,76,16
s_add_u32 s53,s53,s57
ta 56,234,53248
mvaa 64,72,16
ta 58,234,53760
mvaa 32,80,16
ta 68,237,53248
mvaa 36,84,16
ta 70,237,53760
ta 64,236,53248
mvaa 28,84,12
mvaa 24,80,12
dl 230,44,54,2048
ta 66,236,53760
mvaa 16,80,8
ta 76,235,57344
mvaa 20,84,8
ta 78,235,57856
mvaa 12,84,4
ta 72,234,57344
ta 74,234,57856
mvaa 8,80,4
mvaa 76,4,88
buffer_store_dwordx4 v[164:167],v244,s[24:27],s67 offen offset:1024 sc0 sc1
mvaa 96,4,92
mvaa 100,8,92
mvaa 80,8,88
mvaa 84,12,88
mvaa 104,12,92
dl 231,44,54,3072
mvaa 108,16,92
s_add_u32 m0,s61,4096
s_add_u32 s54,s54,s57
s_add_u32 s61,s63,s98
mvaa 88,16,88
s_add_u32 s63,s63,s99
s_waitcnt lgkmcnt(2)
maaa 236,40,24
s_cmp_ge_u32 s63,65536
s_cselect_b32 s99,-2,1
s_lshl_b32 s99,s99,15
maaa 220,44,24
maaa 216,44,28
dl 224,36,52,0
maaa 232,40,28
maaa 228,40,32
maaa 212,44,32
maaa 208,44,36
maaa 224,40,36
maaa 192,48,36
dl 225,36,52,1024
maaa 176,52,36
maaa 180,52,32
maaa 196,48,32
maaa 200,48,28
maaa 184,52,28
dl 226,36,52,2048
maaa 188,52,24
s_waitcnt vmcnt(17) lgkmcnt(0)
s_barrier
maaa 204,48,24
ra 4,232,0
maaa 172,56,24
ra 8,232,2048
maaa 156,60,24
ra 12,232,4096
maaa 152,60,28
dl 227,36,52,3072
maaa 168,56,28
s_add_u32 m0,s62,49152
ra 16,232,6144
s_add_u32 s52,s52,s60
maaa 164,56,32
ta 80,236,8192
v_add_u32_e32 v232,s99,v232
maaa 148,60,32
ta 82,236,8704
maaa 144,60,36
ta 84,237,8192
ta 86,237,8704
maaa 160,56,36
dl 228,40,53,0
maaa 128,64,36
ta 88,238,12288
maaa 112,68,36
ta 90,238,12800
maaa 116,68,32
ta 92,239,12288
maaa 132,64,32
ta 94,239,12800
ta 44,235,0
maaa 136,64,28
maaa 120,68,28
dl 229,40,53,1024
ta 46,235,512
maaa 124,68,24
s_add_u32 s53,s53,s58
ta 40,234,0
maaa 140,64,24
ta 42,234,512
mvaa 40,72,24
ta 52,237,0
mvaa 44,76,24
ta 54,237,512
ta 48,236,0
mvaa 52,76,28
buffer_store_dwordx4 v[168:171],v244,s[24:27],s67 offen offset:2048 sc0 sc1
mvaa 48,72,28
ta 50,236,512
mvaa 56,72,32
ta 60,235,4096
mvaa 60,76,32
ta 62,235,4608
mvaa 68,76,36
ta 56,234,4096
ta 58,234,4608
mvaa 64,72,36
dl 230,44,54,2048
mvaa 32,96,36
ta 68,237,4096
mvaa 36,100,36
ta 70,237,4608
mvaa 28,100,32
ta 64,236,4096
mvaa 24,96,32
ta 66,236,4608
ta 76,235,8192
mvaa 16,96,28
dl 231,44,54,3072
mvaa 20,100,28
s_add_u32 m0,s61,0
ta 78,235,8704
s_add_u32 s54,s54,s58
mvaa 12,100,24
ta 72,234,8192
ta 74,234,8704
mvaa 8,96,24
mvaa 76,24,104
mvaa 96,24,108
dl 224,36,52,0
mvaa 100,28,108
mvaa 80,28,104
mvaa 84,32,104
mvaa 104,32,108
mvaa 108,36,108
mvaa 88,36,104
dl 225,36,52,1024
s_waitcnt lgkmcnt(2)
maaa 236,40,4
maaa 220,44,4
maaa 216,44,8
maaa 232,40,8
maaa 228,40,12
dl 226,36,52,2048
maaa 212,44,12
maaa 208,44,16
maaa 224,40,16
maaa 192,48,16
s_waitcnt vmcnt(17) lgkmcnt(0)
s_barrier
maaa 176,52,16
dl 227,36,52,3072
maaa 180,52,12
s_add_u32 m0,s62,0
ra 24,233,0
s_add_u32 s52,s52,s56
maaa 196,48,12
ra 28,233,2048
maaa 200,48,8
ta 96,236,24576
maaa 184,52,8
ta 98,236,25088
maaa 188,52,4
buffer_store_dwordx4 v[172:175],v244,s[24:27],s67 offen offset:3072 sc0 sc1
maaa 204,48,4
s_mov_b32 s67,20480
ta 100,237,24576
maaa 172,56,4
ta 102,237,25088
maaa 156,60,4
ra 32,233,4096
maaa 152,60,8
ta 108,239,28672
maaa 168,56,8
ta 110,239,29184
maaa 164,56,12
dl 228,40,53,0
maaa 148,60,12
ra 36,233,6144
v_add_u32_e32 v233,s99,v233
maaa 144,60,16
ta 104,238,28672
maaa 160,56,16
ta 106,238,29184
maaa 128,64,16
ta 44,235,16384
maaa 112,68,16
s_mov_b64 exec,3
global_load_dword a250,v253,s[96:97]
s_mov_b64 exec,-1
maaa 116,68,12
ta 46,235,16896
maaa 132,64,12
ta 40,234,16384
maaa 136,64,8
ta 42,234,16896
maaa 120,68,8
ta 52,237,16384
maaa 124,68,4
dl 229,40,53,1024
maaa 140,64,4
s_add_u32 s53,s53,s57
ta 54,237,16896
mvaa 40,72,4
ta 48,236,16384
mvaa 44,76,4
ta 50,236,16896
mvaa 52,76,8
ta 60,235,20480
mvaa 48,72,8
dl 230,44,54,2048
mvaa 56,72,12
ta 62,235,20992
mvaa 60,76,12
ta 56,234,20480
mvaa 68,76,16
ta 58,234,20992
mvaa 64,72,16
ta 68,237,20480
ta 70,237,20992
mvaa 32,80,16
mvaa 36,84,16
dl 231,44,54,3072
ta 64,236,20480
mvaa 28,84,12
s_add_u32 m0,s61,4096
ta 66,236,20992
s_add_u32 s54,s54,s57
mvaa 24,80,12
s_add_u32 s61,s63,s98
ta 76,235,24576
s_add_u32 s63,s63,s99
mvaa 16,80,8
s_cmp_ge_u32 s63,65536
s_cselect_b32 s99,-2,1
ta 78,235,25088
s_lshl_b32 s99,s99,15
mvaa 20,84,8
ta 72,234,24576
ta 74,234,25088
mvaa 12,84,4
dl 224,36,52,0
mvaa 8,80,4
mvaa 76,4,88
mvaa 96,4,92
mvaa 100,8,92
mvaa 80,8,88
dl 225,36,52,1024
mvaa 84,12,88
mvaa 104,12,92
mvaa 108,16,92
mvaa 88,16,88
s_waitcnt vmcnt(17) lgkmcnt(0)
s_barrier
maaa 236,40,24
buffer_store_dwordx4 v[176:179],v244,s[24:27],s67 offen offset:0 sc0 sc1
maaa 220,44,24
ra 4,232,0
maaa 216,44,28
ra 8,232,2048
maaa 232,40,28
ta 80,236,40960
maaa 228,40,32
ta 82,236,41472
maaa 212,44,32
ta 84,237,40960
maaa 208,44,36
dl 226,36,52,2048
maaa 224,40,36
ta 86,237,41472
maaa 192,48,36
ra 12,232,4096
maaa 176,52,36
ta 92,239,45056
maaa 180,52,32
ta 94,239,45568
maaa 196,48,32
dl 227,36,52,3072
maaa 200,48,28
s_add_u32 m0,s62,16384
ra 16,232,6144
s_add_u32 s52,s52,s60
maaa 184,52,28
ta 88,238,45056
v_add_u32_e32 v232,s99,v232
maaa 188,52,24
ta 90,238,45568
maaa 204,48,24
ta 44,235,32768
maaa 172,56,24
dl 228,40,53,0
maaa 156,60,24
ta 46,235,33280
maaa 152,60,28
ta 40,234,32768
maaa 168,56,28
ta 42,234,33280
maaa 164,56,32
ta 52,237,32768
maaa 148,60,32
dl 229,40,53,1024
maaa 144,60,36
s_add_u32 s53,s53,s58
ta 54,237,33280
maaa 160,56,36
ta 48,236,32768
maaa 128,64,36
ta 50,236,33280
maaa 112,68,36
ta 60,235,36864
maaa 116,68,32
dl 230,44,54,2048
maaa 132,64,32
ta 62,235,37376
maaa 136,64,28
ta 56,234,36864
maaa 120,68,28
ta 58,234,37376
maaa 124,68,24
maaa 140,64,24
mvaa 40,72,24
dl 231,44,54,3072
mvaa 44,76,24
s_add_u32 m0,s61,0
ta 68,237,36864
s_add_u32 s54,s54,s58
mvaa 52,76,28
ta 70,237,37376
mvaa 48,72,28
ta 64,236,36864
mvaa 56,72,32
ta 66,236,37376
mvaa 60,76,32
dl 224,36,52,0
mvaa 68,76,36
mvaa 64,72,36
mvaa 32,96,36
ta 76,235,40960
mvaa 36,100,36
ta 78,235,41472
mvaa 28,100,32
buffer_store_dwordx4 v[180:183],v244,s[24:27],s67 offen offset:1024 sc0 sc1
mvaa 24,96,32
ta 72,234,40960
mvaa 16,96,28
ta 74,234,41472
mvaa 20,100,28
mvaa 12,100,24
mvaa 8,96,24
dl 225,36,52,1024
mvaa 76,24,104
mvaa 96,24,108
mvaa 100,28,108
mvaa 80,28,104
mvaa 84,32,104
mvaa 104,32,108
dl 226,36,52,2048
mvaa 108,36,108
s_waitcnt vmcnt(19) lgkmcnt(0)
s_barrier
mvaa 88,36,104
ra 24,233,0
maaa 236,40,4
ra 28,233,2048
maaa 220,44,4
ta 96,236,57344
maaa 216,44,8
dl 227,36,52,3072
maaa 232,40,8
s_add_u32 m0,s62,32768
ta 98,236,57856
s_add_u32 s52,s52,s56
maaa 228,40,12
ta 100,237,57344
maaa 212,44,12
ra 32,233,4096
maaa 208,44,16
ta 102,237,57856
maaa 224,40,16
dl 228,40,53,0
maaa 192,48,16
ta 108,239,61440
maaa 176,52,16
ta 110,239,61952
maaa 180,52,12
ra 36,233,6144
v_add_u32_e32 v233,s99,v233
maaa 196,48,12
ta 104,238,61440
maaa 200,48,8
dl 229,40,53,1024
maaa 184,52,8
s_add_u32 s53,s53,s57
ta 106,238,61952
maaa 188,52,4
ta 44,235,49152
maaa 204,48,4
ta 46,235,49664
maaa 172,56,4
ta 40,234,49152
maaa 156,60,4
ta 42,234,49664
maaa 152,60,8
dl 230,44,54,2048
maaa 168,56,8
ta 52,237,49152
maaa 164,56,12
ta 54,237,49664
maaa 148,60,12
ta 48,236,49152
maaa 144,60,16
ta 50,236,49664
maaa 160,56,16
buffer_store_dwordx4 v[184:187],v244,s[24:27],s67 offen offset:2048 sc0 sc1
maaa 128,64,16
ta 60,235,53248
maaa 112,68,16
ta 62,235,53760
maaa 116,68,12
ta 56,234,53248
maaa 132,64,12
ta 58,234,53760
maaa 136,64,8
dl 231,44,54,3072
maaa 120,68,8
s_add_u32 m0,s61,4096
s_add_u32 s54,s54,s57
s_add_u32 s61,s63,s98
maaa 124,68,4
s_add_u32 s63,s63,s99
maaa 140,64,4
s_cmp_ge_u32 s63,65536
s_cselect_b32 s99,-2,1
s_lshl_b32 s99,s99,15
ta 68,237,53248
mvaa 40,72,4
ta 70,237,53760
mvaa 44,76,4
)"
R"(dl 224,36,52,0
mvaa 52,76,8
ta 64,236,53248
mvaa 48,72,8
ta 66,236,53760
mvaa 56,72,12
mvaa 60,76,12
mvaa 68,76,16
mvaa 64,72,16
dl 225,36,52,1024
mvaa 32,80,16
ta 76,235,57344
ta 78,235,57856
mvaa 36,84,16
ta 72,234,57344
ta 74,234,57856
mvaa 28,84,12
mvaa 24,80,12
mvaa 16,80,8
dl 226,36,52,2048
mvaa 20,84,8
mvaa 12,84,4
mvaa 8,80,4
mvaa 76,4,88
mvaa 96,4,92
dl 227,36,52,3072
mvaa 100,8,92
s_add_u32 m0,s62,49152
s_add_u32 s52,s52,s60
mvaa 80,8,88
mvaa 84,12,88
mvaa 104,12,92
mvaa 108,16,92
s_waitcnt vmcnt(18) lgkmcnt(0)
s_barrier
mvaa 88,16,88
dl 228,40,53,0
maaa 236,40,24
ra 4,232,0
maaa 220,44,24
ra 8,232,2048
maaa 216,44,28
ta 80,236,8192
maaa 232,40,28
ta 82,236,8704
maaa 228,40,32
dl 229,40,53,1024
maaa 212,44,32
s_add_u32 s53,s53,s58
ta 84,237,8192
maaa 208,44,36
ta 86,237,8704
maaa 224,40,36
ra 12,232,4096
maaa 192,48,36
ta 92,239,12288
maaa 176,52,36
buffer_store_dwordx4 v[188:191],v244,s[24:27],s67 offen offset:3072 sc0 sc1
maaa 180,52,32
s_mov_b32 s67,24576
ta 94,239,12800
maaa 196,48,32
ra 16,232,6144
v_add_u32_e32 v232,s99,v232
maaa 200,48,28
ta 88,238,12288
maaa 184,52,28
ta 90,238,12800
maaa 188,52,24
dl 230,44,54,2048
maaa 204,48,24
ta 44,235,0
maaa 172,56,24
ta 46,235,512
maaa 156,60,24
ta 40,234,0
maaa 152,60,28
ta 42,234,512
maaa 168,56,28
dl 231,44,54,3072
maaa 164,56,32
s_add_u32 m0,s61,0
ta 52,237,0
s_add_u32 s54,s54,s58
maaa 148,60,32
ta 54,237,512
maaa 144,60,36
ta 48,236,0
maaa 160,56,36
ta 50,236,512
maaa 128,64,36
dl 224,36,52,0
maaa 112,68,36
ta 60,235,4096
maaa 116,68,32
ta 62,235,4608
maaa 132,64,32
ta 56,234,4096
maaa 136,64,28
ta 58,234,4608
maaa 120,68,28
maaa 124,68,24
dl 225,36,52,1024
maaa 140,64,24
mvaa 40,72,24
ta 68,237,4096
mvaa 44,76,24
ta 70,237,4608
mvaa 52,76,28
ta 64,236,4096
mvaa 48,72,28
dl 226,36,52,2048
mvaa 56,72,32
ta 66,236,4608
mvaa 60,76,32
mvaa 68,76,36
mvaa 64,72,36
mvaa 32,96,36
dl 227,36,52,3072
ta 76,235,8192
ta 78,235,8704
mvaa 36,100,36
s_add_u32 m0,s62,0
ta 72,234,8192
ta 74,234,8704
mvaa 28,100,32
s_add_u32 s52,s52,s56
mvaa 24,96,32
mvaa 16,96,28
mvaa 20,100,28
mvaa 12,100,24
buffer_store_dwordx4 v[192:195],v244,s[24:27],s67 offen offset:0 sc0 sc1
mvaa 8,96,24
mvaa 76,24,104
mvaa 96,24,108
mvaa 100,28,108
mvaa 80,28,104
mvaa 84,32,104
mvaa 104,32,108
mvaa 108,36,108
s_waitcnt vmcnt(19) lgkmcnt(0)
s_barrier
mvaa 88,36,104
dl 228,40,53,0
maaa 236,40,4
ra 24,233,0
maaa 220,44,4
ra 28,233,2048
maaa 216,44,8
ta 96,236,24576
maaa 232,40,8
ta 98,236,25088
maaa 228,40,12
ta 100,237,24576
v_accvgpr_read_b32 v0,a236
maaa 212,44,12
dl 229,40,53,1024
maaa 208,44,16
s_add_u32 s53,s53,s57
ta 102,237,25088
v_accvgpr_read_b32 v1,a237
maaa 224,40,16
ra 32,233,4096
v_accvgpr_read_b32 v2,a238
maaa 192,48,16
ta 108,239,28672
v_accvgpr_read_b32 v3,a239
maaa 176,52,16
ta 110,239,29184
v_accvgpr_read_b32 v4,a220
maaa 180,52,12
dl 230,44,54,2048
maaa 196,48,12
ra 36,233,6144
v_accvgpr_read_b32 v5,a221
maaa 200,48,8
ta 104,238,28672
v_accvgpr_read_b32 v6,a222
maaa 184,52,8
ta 106,238,29184
v_accvgpr_read_b32 v7,a223
maaa 188,52,4
ta 44,235,16384
v_add_u32_e32 v233,s99,v233
maaa 204,48,4
ta 46,235,16896
maaa 172,56,4
dl 231,44,54,3072
maaa 156,60,4
s_add_u32 m0,s61,4096
ta 40,234,16384
s_add_u32 s54,s54,s57
maaa 152,60,8
s_add_u32 s61,s63,s98
ta 42,234,16896
s_add_u32 s63,s63,s99
maaa 168,56,8
s_cmp_ge_u32 s63,65536
s_cselect_b32 s99,-2,1
ta 52,237,16384
s_lshl_b32 s99,s99,15
maaa 164,56,12
ta 54,237,16896
maaa 148,60,12
dl 224,36,52,0
maaa 144,60,16
ta 48,236,16384
maaa 160,56,16
ta 50,236,16896
maaa 128,64,16
ta 60,235,20480
maaa 112,68,16
ta 62,235,20992
maaa 116,68,12
dl 225,36,52,1024
maaa 132,64,12
ta 56,234,20480
maaa 136,64,8
ta 58,234,20992
maaa 120,68,8
maaa 124,68,4
maaa 140,64,4
buffer_store_dwordx4 v[196:199],v244,s[24:27],s67 offen offset:1024 sc0 sc1
mvaa 40,72,4
ta 68,237,20480
mvaa 44,76,4
ta 70,237,20992
mvaa 52,76,8
ta 64,236,20480
mvaa 48,72,8
ta 66,236,20992
mvaa 56,72,12
dl 226,36,52,2048
mvaa 60,76,12
mvaa 68,76,16
mvaa 64,72,16
mvaa 32,80,16
ta 76,235,24576
ta 78,235,25088
mvaa 36,84,16
ta 72,234,24576
ta 74,234,25088
mvaa 28,84,12
dl 227,36,52,3072
mvaa 24,80,12
s_add_u32 m0,s62,16384
s_add_u32 s52,s52,s60
mvaa 16,80,8
mvaa 20,84,8
mvaa 12,84,4
mvaa 8,80,4
mvaa 76,4,88
mvaa 96,4,92
mvaa 100,8,92
mvaa 80,8,88
mvaa 84,12,88
mvaa 104,12,92
mvaa 108,16,92
s_waitcnt vmcnt(19) lgkmcnt(0)
s_barrier
mvaa 88,16,88
dl 228,40,53,0
mvaa 0,40,24
ra 4,232,0
mvaa 4,44,24
ra 8,232,2048
maaa 216,44,28
ta 80,236,40960
maaa 232,40,28
ta 82,236,41472
maaa 228,40,32
dl 229,40,53,1024
maaa 212,44,32
s_add_u32 s53,s53,s58
ta 84,237,40960
v_accvgpr_read_b32 v92,a216
maaa 208,44,36
ra 12,232,4096
v_accvgpr_read_b32 v93,a217
maaa 224,40,36
ta 86,237,41472
v_accvgpr_read_b32 v94,a218
maaa 192,48,36
ta 92,239,45056
v_accvgpr_read_b32 v95,a219
maaa 176,52,36
dl 230,44,54,2048
maaa 180,52,32
ta 94,239,45568
v_accvgpr_read_b32 v72,a232
maaa 196,48,32
ra 16,232,6144
v_accvgpr_read_b32 v73,a233
maaa 200,48,28
ta 88,238,45056
v_accvgpr_read_b32 v74,a234
maaa 184,52,28
ta 90,238,45568
v_accvgpr_read_b32 v75,a235
maaa 188,52,24
ta 44,235,32768
v_add_u32_e32 v232,s99,v232
maaa 204,48,24
buffer_store_dwordx4 v[200:203],v244,s[24:27],s67 offen offset:2048 sc0 sc1
maaa 172,56,24
ta 46,235,33280
maaa 156,60,24
ta 40,234,32768
maaa 152,60,28
ta 42,234,33280
maaa 168,56,28
ta 52,237,32768
maaa 164,56,32
dl 231,44,54,3072
maaa 148,60,32
s_add_u32 m0,s61,0
ta 54,237,33280
s_add_u32 s54,s54,s58
maaa 144,60,36
ta 48,236,32768
maaa 160,56,36
ta 50,236,33280
maaa 128,64,36
ta 60,235,36864
maaa 112,68,36
dl 224,36,52,0
maaa 116,68,32
ta 62,235,37376
maaa 132,64,32
ta 56,234,36864
maaa 136,64,28
ta 58,234,37376
maaa 120,68,28
maaa 124,68,24
dl 225,36,52,1024
maaa 140,64,24
mvaa 40,72,24
ta 68,237,36864
mvaa 44,76,24
ta 70,237,37376
mvaa 52,76,28
ta 64,236,36864
mvaa 48,72,28
dl 226,36,52,2048
mvaa 56,72,32
ta 66,236,37376
mvaa 60,76,32
mvaa 68,76,36
mvaa 64,72,36
mvaa 32,96,36
ta 76,235,40960
mvaa 36,100,36
dl 227,36,52,3072
mvaa 28,100,32
s_add_u32 m0,s62,32768
ta 78,235,41472
s_add_u32 s52,s52,s56
mvaa 24,96,32
ta 72,234,40960
mvaa 16,96,28
ta 74,234,41472
mvaa 20,100,28
mvaa 12,100,24
mvaa 8,96,24
mvaa 76,24,104
mvaa 96,24,108
mvaa 100,28,108
mvaa 80,28,104
mvaa 84,32,104
mvaa 104,32,108
mvaa 108,36,108
buffer_store_dwordx4 v[204:207],v244,s[24:27],s67 offen offset:3072 sc0 sc1
mvaa 88,36,104
s_waitcnt vmcnt(20) lgkmcnt(0)
s_barrier
mvaa 0,40,4
s_mov_b32 s67,28672
ra 24,233,0
mvaa 4,44,4
ra 28,233,2048
mvaa 92,44,8
ta 96,236,57344
mvaa 72,40,8
dl 228,40,53,0
maaa 228,40,12
ta 98,236,57856
maaa 212,44,12
ta 100,237,57344
maaa 208,44,16
ta 102,237,57856
maaa 224,40,16
ra 32,233,4096
maaa 192,48,16
dl 229,40,53,1024
maaa 176,52,16
s_add_u32 s53,s53,s57
ta 108,239,61440
v_accvgpr_read_b32 v144,a228
maaa 180,52,12
ta 110,239,61952
v_accvgpr_read_b32 v145,a229
maaa 196,48,12
ra 36,233,6144
v_accvgpr_read_b32 v146,a230
maaa 200,48,8
ta 104,238,61440
v_accvgpr_read_b32 v147,a231
maaa 184,52,8
ta 106,238,61952
v_accvgpr_read_b32 v148,a212
maaa 188,52,4
dl 230,44,54,2048
maaa 204,48,4
ta 44,235,49152
v_accvgpr_read_b32 v149,a213
maaa 172,56,4
ta 46,235,49664
v_accvgpr_read_b32 v150,a214
maaa 156,60,4
ta 40,234,49152
v_accvgpr_read_b32 v151,a215
maaa 152,60,8
ta 42,234,49664
v_add_u32_e32 v233,s99,v233
maaa 168,56,8
dl 231,44,54,3072
maaa 164,56,12
s_add_u32 m0,s61,4096
ta 52,237,49152
s_add_u32 s54,s54,s57
maaa 148,60,12
s_add_u32 s61,s63,s98
ta 54,237,49664
s_add_u32 s63,s63,s99
maaa 144,60,16
s_cmp_ge_u32 s63,65536
s_cselect_b32 s99,-2,1
ta 48,236,49152
s_lshl_b32 s99,s99,15
maaa 160,56,16
ta 50,236,49664
maaa 128,64,16
dl 224,36,52,0
maaa 112,68,16
ta 60,235,53248
maaa 116,68,12
ta 62,235,53760
maaa 132,64,12
ta 56,234,53248
maaa 136,64,8
ta 58,234,53760
maaa 120,68,8
dl 225,36,52,1024
maaa 124,68,4
maaa 140,64,4
mvaa 40,72,4
ta 68,237,53248
mvaa 44,76,4
ta 70,237,53760
mvaa 52,76,8
dl 226,36,52,2048
mvaa 48,72,8
ta 64,236,53248
mvaa 56,72,12
ta 66,236,53760
mvaa 60,76,12
mvaa 68,76,16
mvaa 64,72,16
mvaa 32,80,16
buffer_store_dwordx4 v[208:211],v244,s[24:27],s67 offen offset:0 sc0 sc1
ta 76,235,57344
ta 78,235,57856
mvaa 36,84,16
ta 72,234,57344
ta 74,234,57856
mvaa 28,84,12
mvaa 24,80,12
mvaa 16,80,8
mvaa 20,84,8
dl 227,36,52,3072
mvaa 12,84,4
s_add_u32 m0,s62,49152
s_add_u32 s52,s52,s60
mvaa 8,80,4
mvaa 76,4,88
mvaa 96,4,92
mvaa 100,8,92
mvaa 80,8,88
mvaa 84,12,88
mvaa 104,12,92
mvaa 108,16,92
s_waitcnt vmcnt(19) lgkmcnt(0)
s_barrier
mvaa 88,16,88
dl 228,40,53,0
mvaa 0,40,24
ra 4,232,0
mvaa 4,44,24
ra 8,232,2048
mvaa 92,44,28
ta 80,236,8192
mvaa 72,40,28
ta 82,236,8704
mvaa 144,40,32
ta 84,237,8192
mvaa 148,44,32
dl 229,40,53,1024
maaa 208,44,36
s_add_u32 s53,s53,s58
ta 86,237,8704
maaa 224,40,36
ra 12,232,4096
maaa 192,48,36
ta 92,239,12288
maaa 176,52,36
ra 16,232,6144
v_accvgpr_read_b32 v156,a208
maaa 180,52,32
dl 230,44,54,2048
maaa 196,48,32
ta 94,239,12800
v_accvgpr_read_b32 v157,a209
maaa 200,48,28
ta 88,238,12288
v_accvgpr_read_b32 v158,a210
maaa 184,52,28
ta 90,238,12800
v_accvgpr_read_b32 v159,a211
maaa 188,52,24
ta 44,235,0
v_accvgpr_read_b32 v152,a224
maaa 204,48,24
dl 231,44,54,3072
maaa 172,56,24
s_add_u32 m0,s61,0
ta 46,235,512
v_accvgpr_read_b32 v153,a225
maaa 156,60,24
s_add_u32 s54,s54,s58
ta 40,234,0
v_accvgpr_read_b32 v154,a226
maaa 152,60,28
ta 42,234,512
v_accvgpr_read_b32 v155,a227
maaa 168,56,28
ta 52,237,0
v_add_u32_e32 v232,s99,v232
maaa 164,56,32
dl 224,36,52,0
maaa 148,60,32
ta 54,237,512
maaa 144,60,36
ta 48,236,0
maaa 160,56,36
ta 50,236,512
maaa 128,64,36
ta 60,235,4096
maaa 112,68,36
ta 62,235,4608
maaa 116,68,32
buffer_store_dwordx4 v[212:215],v244,s[24:27],s67 offen offset:1024 sc0 sc1
maaa 132,64,32
ta 56,234,4096
maaa 136,64,28
ta 58,234,4608
maaa 120,68,28
maaa 124,68,24
maaa 140,64,24
dl 225,36,52,1024
mvaa 40,72,24
ta 68,237,4096
mvaa 44,76,24
ta 70,237,4608
mvaa 52,76,28
ta 64,236,4096
mvaa 48,72,28
ta 66,236,4608
mvaa 56,72,32
dl 226,36,52,2048
mvaa 60,76,32
mvaa 68,76,36
mvaa 64,72,36
mvaa 32,96,36
ta 76,235,8192
ta 78,235,8704
mvaa 36,100,36
dl 227,36,52,3072
ta 72,234,8192
ta 74,234,8704
mvaa 28,100,32
s_add_u32 m0,s62,0
s_add_u32 s52,s52,s56
mvaa 24,96,32
mvaa 16,96,28
mvaa 20,100,28
mvaa 12,100,24
mvaa 8,96,24
mvaa 76,24,104
mvaa 96,24,108
mvaa 100,28,108
mvaa 80,28,104
mvaa 84,32,104
mvaa 104,32,108
mvaa 108,36,108
s_waitcnt vmcnt(19) lgkmcnt(0)
s_barrier
mvaa 88,36,104
dl 228,40,53,0
mvaa 0,40,4
ra 24,233,0
mvaa 4,44,4
ra 28,233,2048
mvaa 92,44,8
ta 96,236,24576
mvaa 72,40,8
ta 98,236,25088
mvaa 144,40,12
dl 229,40,53,1024
mvaa 148,44,12
s_add_u32 s53,s53,s57
ta 100,237,24576
mvaa 156,44,16
ta 102,237,25088
mvaa 152,40,16
ra 32,233,4096
maaa 192,48,16
ta 108,239,28672
maaa 176,52,16
buffer_store_dwordx4 v[216:219],v244,s[24:27],s67 offen offset:2048 sc0 sc1
maaa 180,52,12
ta 110,239,29184
maaa 196,48,12
ra 36,233,6144
v_add_u32_e32 v233,s99,v233
maaa 200,48,8
ta 104,238,28672
v_accvgpr_read_b32 v160,a192
maaa 184,52,8
ta 106,238,29184
v_accvgpr_read_b32 v161,a193
maaa 188,52,4
dl 230,44,54,2048
maaa 204,48,4
ta 44,235,16384
v_accvgpr_read_b32 v162,a194
maaa 172,56,4
ta 46,235,16896
v_accvgpr_read_b32 v163,a195
maaa 156,60,4
ta 40,234,16384
v_accvgpr_read_b32 v164,a176
maaa 152,60,8
ta 42,234,16896
v_accvgpr_read_b32 v165,a177
maaa 168,56,8
dl 231,44,54,3072
maaa 164,56,12
s_add_u32 m0,s61,4096
ta 52,237,16384
v_accvgpr_read_b32 v166,a178
maaa 148,60,12
s_add_u32 s54,s54,s57
ta 54,237,16896
v_accvgpr_read_b32 v167,a179
maaa 144,60,16
s_add_u32 s61,s63,s98
ta 48,236,16384
s_add_u32 s63,s63,s99
maaa 160,56,16
s_cmp_ge_u32 s63,65536
s_cselect_b32 s99,-2,1
ta 50,236,16896
s_lshl_b32 s99,s99,15
maaa 128,64,16
ta 60,235,20480
maaa 112,68,16
dl 224,36,52,0
maaa 116,68,12
ta 62,235,20992
maaa 132,64,12
ta 56,234,20480
maaa 136,64,8
ta 58,234,20992
maaa 120,68,8
maaa 124,68,4
dl 225,36,52,1024
maaa 140,64,4
mvaa 40,72,4
ta 68,237,20480
mvaa 44,76,4
ta 70,237,20992
mvaa 52,76,8
ta 64,236,20480
mvaa 48,72,8
dl 226,36,52,2048
mvaa 56,72,12
ta 66,236,20992
mvaa 60,76,12
mvaa 68,76,16
mvaa 64,72,16
mvaa 32,80,16
dl 227,36,52,3072
ta 76,235,24576
ta 78,235,25088
mvaa 36,84,16
s_add_u32 m0,s62,16384
ta 72,234,24576
ta 74,234,25088
mvaa 28,84,12
s_add_u32 s52,s52,s60
mvaa 24,80,12
mvaa 16,80,8
mvaa 20,84,8
mvaa 12,84,4
buffer_store_dwordx4 v[220:223],v244,s[24:27],s67 offen offset:3072 sc0 sc1
mvaa 8,80,4
s_mov_b32 s67,0x5550055
s_mov_b32 s26,2097152
v_cndmask_b32_e64 v116,v241,0,s[66:67]
mvaa 76,4,88
s_mov_b32 s66,0xa0000
s_mov_b32 s67,0xaaa00aa
v_cndmask_b32_e64 v117,v241,0,s[66:67]
mvaa 96,4,92
s_mov_b32 s66,0x150001
v_cndmask_b32_e64 v120,v116,v241,s[64:65]
s_mov_b32 s67,0x15550155
mvaa 100,8,92
s_mov_b32 s27,131072
v_cndmask_b32_e64 v118,v241,0,s[66:67]
v_cndmask_b32_e64 v121,v117,v241,s[64:65]
mvaa 80,8,88
s_mov_b32 s66,0x2a0002
s_mov_b32 s67,0x2aaa02aa
v_cndmask_b32_e64 v119,v241,0,s[66:67]
mvaa 84,12,88
s_lshr_b32 s66,s18,2
v_cndmask_b32_e64 v122,v118,v241,s[64:65]
v_cndmask_b32_e64 v123,v119,v241,s[64:65]
mvaa 104,12,92
s_lshl_b32 s66,s66,13
v_cmp_eq_u32_e64 s[64:65],s87,v250
v_lshlrev_b32_e32 v250,3,v250
mvaa 108,16,92
s_waitcnt vmcnt(19) lgkmcnt(0)
s_barrier
mvaa 88,16,88
dl 228,40,53,0
mvaa 0,40,24
s_lshl_b32 s67,s18,13
ra 4,232,0
v_add_u32_e32 v250,83894272,v250
mvaa 4,44,24
s_mov_b64 s[24:25],s[82:83]
ra 8,232,2048
v_cndmask_b32_e64 v242,31,-1,s[64:65]
mvaa 92,44,28
s_cmp_eq_u32 s12,0
s_cselect_b32 s82,0,0
ta 80,236,40960
v_and_b32_e32 v243,528,v242
mvaa 72,40,28
s_or_b32 s82,s82,s15
ta 82,236,41472
s_mov_b32 s83,0
mvaa 144,40,32
s_or_b32 s82,s82,16384
ta 84,237,40960
mvaa 148,44,32
dl 229,40,53,1024
mvaa 156,44,36
s_add_u32 s53,s53,s58
ta 86,237,41472
mvaa 152,40,36
ra 12,232,4096
mvaa 160,48,36
ta 92,239,45056
mvaa 164,52,36
ta 94,239,45568
maaa 180,52,32
dl 230,44,54,2048
maaa 196,48,32
ra 16,232,6144
v_add_u32_e32 v232,s99,v232
maaa 200,48,28
ta 88,238,45056
maaa 184,52,28
ta 90,238,45568
v_accvgpr_read_b32 v172,a180
maaa 188,52,24
ta 44,235,32768
v_accvgpr_read_b32 v173,a181
maaa 204,48,24
ta 46,235,33280
v_accvgpr_read_b32 v174,a182
maaa 172,56,24
dl 231,44,54,3072
maaa 156,60,24
s_add_u32 m0,s61,0
ta 40,234,32768
v_accvgpr_read_b32 v175,a183
maaa 152,60,28
s_add_u32 s54,s54,s58
ta 42,234,33280
v_accvgpr_read_b32 v168,a196
maaa 168,56,28
ta 52,237,32768
v_accvgpr_read_b32 v169,a197
maaa 164,56,32
ta 54,237,33280
v_accvgpr_read_b32 v170,a198
maaa 148,60,32
dl 224,36,52,0
maaa 144,60,36
ta 48,236,32768
v_accvgpr_read_b32 v171,a199
maaa 160,56,36
ta 50,236,33280
maaa 128,64,36
ta 60,235,36864
maaa 112,68,36
ta 62,235,37376
maaa 116,68,32
dl 225,36,52,1024
maaa 132,64,32
ta 56,234,36864
maaa 136,64,28
ta 58,234,37376
maaa 120,68,28
maaa 124,68,24
maaa 140,64,24
dl 226,36,52,2048
mvaa 40,72,24
ta 68,237,36864
mvaa 44,76,24
ta 70,237,37376
mvaa 52,76,28
ta 64,236,36864
mvaa 48,72,28
ta 66,236,37376
mvaa 56,72,32
mvaa 60,76,32
dl 227,36,52,3072
mvaa 68,76,36
s_add_u32 m0,s62,32768
s_add_u32 s52,s52,s56
mvaa 64,72,36
mvaa 32,96,36
ta 76,235,40960
ta 78,235,41472
mvaa 36,100,36
ta 72,234,40960
ta 74,234,41472
mvaa 28,100,32
mvaa 24,96,32
mvaa 16,96,28
mvaa 20,100,28
mvaa 12,100,24
mvaa 8,96,24
mvaa 76,24,104
mvaa 96,24,108
mvaa 100,28,108
mvaa 80,28,104
mvaa 84,32,104
mvaa 104,32,108
mvaa 108,36,108
s_waitcnt vmcnt(18) lgkmcnt(0)
s_barrier
mvaa 88,36,104
dl 228,40,53,0
mvaa 0,40,4
ra 24,233,0
mvaa 4,44,4
ra 28,233,2048
mvaa 92,44,8
ta 96,236,57344
mvaa 72,40,8
ta 98,236,57856
mvaa 144,40,12
dl 229,40,53,1024
mvaa 148,44,12
s_add_u32 s53,s53,s57
ta 100,237,57344
mvaa 156,44,16
ta 102,237,57856
mvaa 152,40,16
ra 32,233,4096
mvaa 160,48,16
ta 108,239,61440
mvaa 164,52,16
dl 230,44,54,2048
mvaa 172,52,12
ra 36,233,6144
v_add_u32_e32 v233,s99,v233
mvaa 168,48,12
ta 110,239,61952
maaa 200,48,8
ta 104,238,61440
maaa 184,52,8
ta 106,238,61952
maaa 188,52,4
ta 44,235,49152
maaa 204,48,4
dl 231,44,54,3072
maaa 172,56,4
s_add_u32 m0,s61,4096
ta 46,235,49664
v_accvgpr_read_b32 v176,a200
maaa 156,60,4
s_add_u32 s54,s54,s57
ta 40,234,49152
v_accvgpr_read_b32 v177,a201
maaa 152,60,8
s_add_u32 s61,s63,s98
ta 42,234,49664
v_accvgpr_read_b32 v178,a202
maaa 168,56,8
s_add_u32 s63,s63,s99
ta 52,237,49152
v_accvgpr_read_b32 v179,a203
maaa 164,56,12
s_mov_b64 exec,3
global_load_dword a250,v253,s[96:97]
s_mov_b64 exec,-1
maaa 148,60,12
s_cmp_ge_u32 s63,65536
s_cselect_b32 s99,-2,1
ta 54,237,49664
v_accvgpr_read_b32 v180,a184
maaa 144,60,16
s_lshl_b32 s99,s99,15
ta 48,236,49152
v_accvgpr_read_b32 v181,a185
maaa 160,56,16
ta 50,236,49664
v_accvgpr_read_b32 v182,a186
maaa 128,64,16
ta 60,235,53248
v_accvgpr_read_b32 v183,a187
maaa 112,68,16
dl 224,36,52,0
maaa 116,68,12
ta 62,235,53760
v_add_u32_e32 v253,24576,v253
maaa 132,64,12
ta 56,234,53248
maaa 136,64,8
ta 58,234,53760
maaa 120,68,8
maaa 124,68,4
dl 225,36,52,1024
maaa 140,64,4
mvaa 40,72,4
ta 68,237,53248
mvaa 44,76,4
ta 70,237,53760
mvaa 52,76,8
ta 64,236,53248
mvaa 48,72,8
ta 66,236,53760
mvaa 56,72,12
dl 226,36,52,2048
mvaa 60,76,12
mvaa 68,76,16
mvaa 64,72,16
mvaa 32,80,16
ta 76,235,57344
ta 78,235,57856
mvaa 36,84,16
dl 227,36,52,3072
ta 72,234,57344
ta 74,234,57856
mvaa 28,84,12
s_add_u32 m0,s62,49152
s_add_u32 s52,s52,s60
mvaa 24,80,12
mvaa 16,80,8
mvaa 20,84,8
mvaa 12,84,4
mvaa 8,80,4
mvaa 76,4,88
mvaa 96,4,92
mvaa 100,8,92
mvaa 80,8,88
mvaa 84,12,88
mvaa 104,12,92
mvaa 108,16,92
s_waitcnt vmcnt(17) lgkmcnt(0)
s_barrier
mvaa 88,16,88
s_cmp_eq_u32 s12,0
s_cselect_b64 exec,1,0
buffer_atomic_add v247,off,s[8:11],s85 sc1
s_mov_b64 exec,-1
mvaa 0,40,24
ra 4,232,0
v_bfe_u32 v247,v244,2,6
mvaa 4,44,24
ra 8,232,2048
mvaa 92,44,28
)"
R"(ta 80,236,8192
mvaa 72,40,28
ta 82,236,8704
mvaa 144,40,32
dl 228,40,53,0
mvaa 148,44,32
ta 84,237,8192
mvaa 156,44,36
ta 86,237,8704
mvaa 152,40,36
ra 12,232,4096
mvaa 160,48,36
ta 92,239,12288
mvaa 164,52,36
dl 229,40,53,1024
mvaa 172,52,32
s_add_u32 s53,s53,s58
ra 16,232,6144
v_add_u32_e32 v232,s99,v232
mvaa 168,48,32
ta 94,239,12800
mvaa 176,48,28
ta 88,238,12288
mvaa 180,52,28
ta 90,238,12800
maaa 188,52,24
dl 230,44,54,2048
maaa 204,48,24
ta 44,235,0
maaa 172,56,24
ta 46,235,512
maaa 156,60,24
ta 40,234,0
maaa 152,60,28
ta 42,234,512
v_accvgpr_read_b32 v188,a188
maaa 168,56,28
dl 231,44,54,3072
maaa 164,56,32
s_add_u32 m0,s61,0
ta 52,237,0
v_accvgpr_read_b32 v189,a189
maaa 148,60,32
s_add_u32 s54,s54,s58
ta 54,237,512
v_accvgpr_read_b32 v190,a190
maaa 144,60,36
ta 48,236,0
v_accvgpr_read_b32 v191,a191
maaa 160,56,36
ta 50,236,512
v_accvgpr_read_b32 v184,a204
maaa 128,64,36
dl 224,36,52,0
maaa 112,68,36
ta 60,235,4096
v_accvgpr_read_b32 v185,a205
maaa 116,68,32
ta 62,235,4608
v_accvgpr_read_b32 v186,a206
maaa 132,64,32
ta 56,234,4096
v_accvgpr_read_b32 v187,a207
maaa 136,64,28
ta 58,234,4608
maaa 120,68,28
dl 225,36,52,1024
maaa 124,68,24
maaa 140,64,24
mvaa 40,72,24
ta 68,237,4096
mvaa 44,76,24
ta 70,237,4608
mvaa 52,76,28
dl 226,36,52,2048
mvaa 48,72,28
ta 64,236,4096
mvaa 56,72,32
ta 66,236,4608
mvaa 60,76,32
mvaa 68,76,36
mvaa 64,72,36
dl 227,36,52,3072
mvaa 32,96,36
s_add_u32 m0,s62,0
ta 76,235,8192
ta 78,235,8704
mvaa 36,100,36
s_add_u32 s52,s52,s56
ta 72,234,8192
ta 74,234,8704
mvaa 28,100,32
mvaa 24,96,32
mvaa 16,96,28
s_waitcnt vmcnt(12)
s_mov_b64 exec,3
global_load_dword a250,v253,s[96:97]
s_mov_b64 exec,-1
mvaa 20,100,28
v_lshrrev_b32_e32 v253,1,v244
mvaa 12,100,24
mvaa 8,96,24
mvaa 76,24,104
mvaa 96,24,108
mvaa 100,28,108
mvaa 80,28,104
mvaa 84,32,104
mvaa 104,32,108
mvaa 108,36,108
s_waitcnt lgkmcnt(0)
s_barrier
mvaa 88,36,104
dl 228,40,53,0
mvaa 0,40,4
ra 24,233,0
mvaa 4,44,4
ra 28,233,2048
mvaa 92,44,8
ta 96,236,24576
mvaa 72,40,8
ta 98,236,25088
mvaa 144,40,12
dl 229,40,53,1024
mvaa 148,44,12
s_add_u32 s53,s53,s57
ta 100,237,24576
mvaa 156,44,16
ta 102,237,25088
mvaa 152,40,16
ra 32,233,4096
mvaa 160,48,16
ta 108,239,28672
mvaa 164,52,16
dl 230,44,54,2048
mvaa 172,52,12
ta 110,239,29184
mvaa 168,48,12
ra 36,233,6144
v_add_u32_e32 v233,s99,v233
mvaa 176,48,8
ta 104,238,28672
mvaa 180,52,8
ta 106,238,29184
mvaa 188,52,4
dl 231,44,54,3072
mvaa 184,48,4
s_add_u32 m0,s61,4096
ta 44,235,16384
s_add_u32 s54,s54,s57
maaa 172,56,4
s_add_u32 s61,s63,s98
ta 46,235,16896
s_add_u32 s63,s63,s99
maaa 156,60,4
s_cmp_ge_u32 s63,65536
s_cselect_b32 s99,-2,1
ta 40,234,16384
s_lshl_b32 s99,s99,15
maaa 152,60,8
ta 42,234,16896
v_accvgpr_read_b32 v192,a172
maaa 168,56,8
dl 224,36,52,0
maaa 164,56,12
ta 52,237,16384
v_accvgpr_read_b32 v193,a173
maaa 148,60,12
ta 54,237,16896
v_accvgpr_read_b32 v194,a174
maaa 144,60,16
ta 48,236,16384
v_accvgpr_read_b32 v195,a175
maaa 160,56,16
ta 50,236,16896
v_accvgpr_read_b32 v196,a156
maaa 128,64,16
dl 225,36,52,1024
maaa 112,68,16
ta 60,235,20480
v_accvgpr_read_b32 v197,a157
maaa 116,68,12
ta 62,235,20992
v_accvgpr_read_b32 v198,a158
maaa 132,64,12
ta 56,234,20480
v_accvgpr_read_b32 v199,a159
maaa 136,64,8
ta 58,234,20992
maaa 120,68,8
dl 226,36,52,2048
maaa 124,68,4
maaa 140,64,4
mvaa 40,72,4
ta 68,237,20480
mvaa 44,76,4
ta 70,237,20992
mvaa 52,76,8
dl 227,36,52,3072
mvaa 48,72,8
s_add_u32 m0,s62,16384
ta 64,236,20480
s_add_u32 s52,s52,s60
mvaa 56,72,12
ta 66,236,20992
mvaa 60,76,12
mvaa 68,76,16
mvaa 64,72,16
mvaa 32,80,16
ta 76,235,24576
ta 78,235,25088
mvaa 36,84,16
ta 72,234,24576
ta 74,234,25088
mvaa 28,84,12
mvaa 24,80,12
mvaa 16,80,8
mvaa 20,84,8
mvaa 12,84,4
mvaa 8,80,4
mvaa 76,4,88
mvaa 96,4,92
mvaa 100,8,92
mvaa 80,8,88
mvaa 84,12,88
mvaa 104,12,92
mvaa 108,16,92
s_waitcnt vmcnt(17) lgkmcnt(0)
s_barrier
mvaa 88,16,88
dl 228,40,53,0
mvaa 0,40,24
ra 4,232,0
mvaa 4,44,24
ra 8,232,2048
mvaa 92,44,28
ta 80,236,40960
mvaa 72,40,28
ta 82,236,41472
mvaa 144,40,32
dl 229,40,53,1024
mvaa 148,44,32
s_add_u32 s53,s53,s58
ta 84,237,40960
mvaa 156,44,36
ta 86,237,41472
mvaa 152,40,36
ra 12,232,4096
mvaa 160,48,36
ta 92,239,45056
mvaa 164,52,36
dl 230,44,54,2048
mvaa 172,52,32
ta 94,239,45568
mvaa 168,48,32
ra 16,232,6144
v_add_u32_e32 v232,s99,v232
mvaa 176,48,28
ta 88,238,45056
mvaa 180,52,28
ta 90,238,45568
mvaa 188,52,24
dl 231,44,54,3072
mvaa 184,48,24
s_add_u32 m0,s61,0
ta 44,235,32768
s_add_u32 s54,s54,s58
mvaa 192,56,24
ta 46,235,33280
mvaa 196,60,24
ta 40,234,32768
maaa 152,60,28
ta 42,234,33280
maaa 168,56,28
dl 224,36,52,0
maaa 164,56,32
ta 52,237,32768
maaa 148,60,32
ta 54,237,33280
maaa 144,60,36
ta 48,236,32768
v_accvgpr_read_b32 v204,a152
maaa 160,56,36
ta 50,236,33280
v_accvgpr_read_b32 v205,a153
maaa 128,64,36
dl 225,36,52,1024
maaa 112,68,36
ta 60,235,36864
v_accvgpr_read_b32 v206,a154
maaa 116,68,32
ta 62,235,37376
v_accvgpr_read_b32 v207,a155
maaa 132,64,32
ta 56,234,36864
v_accvgpr_read_b32 v200,a168
maaa 136,64,28
ta 58,234,37376
v_accvgpr_read_b32 v201,a169
maaa 120,68,28
dl 226,36,52,2048
maaa 124,68,24
v_accvgpr_read_b32 v202,a170
v_accvgpr_read_b32 v203,a171
maaa 140,64,24
mvaa 40,72,24
ta 68,237,36864
mvaa 44,76,24
ta 70,237,37376
mvaa 52,76,28
dl 227,36,52,3072
mvaa 48,72,28
s_add_u32 m0,s62,32768
ta 64,236,36864
s_add_u32 s52,s52,s56
mvaa 56,72,32
s_mul_i32 s56,s59,12288
ta 66,236,37376
mvaa 60,76,32
mvaa 68,76,36
mvaa 64,72,36
mvaa 32,96,36
ta 76,235,40960
ta 78,235,41472
mvaa 36,100,36
ta 72,234,40960
ta 74,234,41472
mvaa 28,100,32
mvaa 24,96,32
mvaa 16,96,28
mvaa 20,100,28
mvaa 12,100,24
mvaa 8,96,24
mvaa 76,24,104
mvaa 96,24,108
mvaa 100,28,108
mvaa 80,28,104
mvaa 84,32,104
mvaa 104,32,108
mvaa 108,36,108
s_waitcnt vmcnt(16) lgkmcnt(0)
s_barrier
mvaa 88,36,104
dl 228,40,53,0
mvaa 0,40,4
ra 24,233,0
mvaa 4,44,4
ra 28,233,2048
mvaa 92,44,8
ta 96,236,57344
mvaa 72,40,8
ta 98,236,57856
mvaa 144,40,12
dl 229,40,53,1024
mvaa 148,44,12
s_add_u32 s53,s53,s57
ta 100,237,57344
mvaa 156,44,16
ta 102,237,57856
mvaa 152,40,16
ra 32,233,4096
mvaa 160,48,16
ta 108,239,61440
mvaa 164,52,16
dl 230,44,54,2048
mvaa 172,52,12
ta 110,239,61952
mvaa 168,48,12
ra 36,233,6144
v_add_u32_e32 v233,s99,v233
mvaa 176,48,8
ta 104,238,61440
mvaa 180,52,8
ta 106,238,61952
mvaa 188,52,4
dl 231,44,54,3072
mvaa 184,48,4
s_add_u32 m0,s61,4096
ta 44,235,49152
s_add_u32 s54,s54,s57
mvaa 192,56,4
s_add_u32 s61,s63,s98
ta 46,235,49664
s_add_u32 s63,s63,s99
mvaa 196,60,4
s_mov_b32 s98,s94
ta 40,234,49152
s_mov_b32 s99,s98
mvaa 204,60,8
s_and_b32 s94,s12,1
ta 42,234,49664
s_lshl_b32 s94,s94,14
mvaa 200,56,8
dl 224,36,52,0
maaa 164,56,12
s_add_u32 s57,s56,4096
ta 52,237,49152
v_mov_b32_e32 v224,v244
maaa 148,60,12
ta 54,237,49664
maaa 144,60,16
ta 48,236,49152
maaa 160,56,16
ta 50,236,49664
v_accvgpr_read_b32 v208,a164
maaa 128,64,16
dl 225,36,52,1024
maaa 112,68,16
ta 60,235,53248
v_accvgpr_read_b32 v209,a165
maaa 116,68,12
ta 62,235,53760
v_accvgpr_read_b32 v210,a166
maaa 132,64,12
ta 56,234,53248
v_accvgpr_read_b32 v211,a167
maaa 136,64,8
ta 58,234,53760
v_accvgpr_read_b32 v212,a148
maaa 120,68,8
dl 226,36,52,2048
maaa 124,68,4
v_accvgpr_read_b32 v213,a149
v_accvgpr_read_b32 v214,a150
maaa 140,64,4
v_accvgpr_read_b32 v215,a151
v_add_u32_e32 v225,65536,v224
mvaa 40,72,4
ta 68,237,53248
v_mov_b32_e32 v226,1065369472
mvaa 44,76,4
ta 70,237,53760
mvaa 52,76,8
dl 227,36,52,3072
mvaa 48,72,8
s_add_u32 m0,s62,49152
ta 64,236,53248
s_add_u32 s52,s52,s60
mvaa 56,72,12
s_lshl_b32 s60,%1,7
ta 66,236,53760
s_mov_b64 s[36:37],s[28:29]
mvaa 60,76,12
s_mov_b32 s38,8388608
s_mov_b32 s39,131072
s_add_u32 s61,s60,83914752
mvaa 68,76,16
s_lshl_b32 s52,s12,12
mvaa 64,72,16
mvaa 32,80,16
ta 76,235,57344
ta 78,235,57856
mvaa 36,84,16
ta 72,234,57344
ta 74,234,57856
mvaa 28,84,12
mvaa 24,80,12
mvaa 16,80,8
mvaa 20,84,8
mvaa 12,84,4
mvaa 8,80,4
mvaa 76,4,88
mvaa 96,4,92
mvaa 100,8,92
mvaa 80,8,88
mvaa 84,12,88
mvaa 104,12,92
mvaa 108,16,92
s_waitcnt vmcnt(16) lgkmcnt(0)
s_barrier
mvaa 88,16,88
dl 228,40,53,0
mvaa 0,40,24
ra 4,232,0
mvaa 4,44,24
ra 8,232,2048
mvaa 92,44,28
ta 80,236,8192
mvaa 72,40,28
ta 82,236,8704
mvaa 144,40,32
dl 229,40,53,1024
mvaa 148,44,32
s_lshl_b32 s43,s19,15
ta 84,237,8192
s_add_u32 s42,s43,16384
mvaa 156,44,36
s_add_u32 s53,s53,s58
ta 86,237,8704
s_mov_b32 s40,858993459
mvaa 152,40,36
s_mov_b32 s41,858993459
ra 12,232,4096
v_cndmask_b32_e64 v228,0,v226,s[40:41]
mvaa 160,48,36
ta 92,239,12288
v_cndmask_b32_e64 v229,0,v226,s[40:41]
mvaa 164,52,36
buffer_load_dword v248,v250,s[8:11],0 offen sc1
mvaa 172,52,32
ta 94,239,12800
mvaa 168,48,32
ra 16,232,6144
mvaa 176,48,28
ta 88,238,12288
mvaa 180,52,28
ta 90,238,12800
mvaa 188,52,24
dl 230,44,54,2048
mvaa 184,48,24
ta 44,235,0
v_cndmask_b32_e64 v230,0,v226,s[40:41]
mvaa 192,56,24
ta 46,235,512
mvaa 196,60,24
ta 40,234,0
mvaa 204,60,28
ta 42,234,512
mvaa 200,56,28
dl 231,44,54,3072
mvaa 208,56,32
s_add_u32 s54,s54,s58
ta 52,237,0
s_mov_b32 s46,16384
mvaa 212,60,32
s_sub_u32 s47,32768,s43
ta 54,237,512
v_cndmask_b32_e64 v231,0,v226,s[40:41]
maaa 144,60,36
s_mov_b32 s40,0xf0000
ta 48,236,0
s_mov_b32 s41,0xfff00ff
maaa 160,56,36
s_add_u32 s47,s47,32768
ta 50,236,512
v_cndmask_b32_e64 v226,0,v252,s[40:41]
maaa 128,64,36
s_mov_b32 s40,0x3f0003
ta 60,235,4096
v_accvgpr_read_b32 v220,a144
maaa 112,68,36
s_mov_b32 s41,0x3fff03ff
ta 62,235,4608
v_accvgpr_read_b32 v221,a145
maaa 116,68,32
s_lshr_b32 s45,s15,1
ta 56,234,4096
v_accvgpr_read_b32 v222,a146
maaa 132,64,32
s_add_u32 s58,s56,8192
ta 58,234,4608
v_accvgpr_read_b32 v223,a147
maaa 136,64,28
s_ff1_i32_b32 s80,s45
v_accvgpr_read_b32 v216,a160
v_accvgpr_read_b32 v217,a161
maaa 120,68,28
s_sub_u32 s44,s56,2048
v_accvgpr_read_b32 v218,a162
v_accvgpr_read_b32 v219,a163
maaa 124,68,24
v_cndmask_b32_e64 v227,0,v252,s[40:41]
s_mov_b32 s40,0xfff0ffff
maaa 140,64,24
s_mov_b32 s41,0xf000ff00
mvaa 40,72,24
ta 68,237,4096
mvaa 44,76,24
ta 70,237,4608
mvaa 52,76,28
ta 64,236,4096
mvaa 48,72,28
ta 66,236,4608
mvaa 56,72,32
mvaa 60,76,32
mvaa 68,76,36
mvaa 64,72,36
mvaa 32,96,36
ta 76,235,8192
ta 78,235,8704
mvaa 36,100,36
ta 72,234,8192
ta 74,234,8704
mvaa 28,100,32
mvaa 24,96,32
mvaa 16,96,28
mvaa 20,100,28
mvaa 12,100,24
mvaa 8,96,24
mvaa 76,24,104
mvaa 96,24,108
mvaa 100,28,108
mvaa 80,28,104
mvaa 84,32,104
mvaa 104,32,108
mvaa 108,36,108
s_waitcnt vmcnt(13) lgkmcnt(0)
s_barrier
mvaa 88,36,104
ra 0,233,0
ta 144,234,16384
ta 146,234,16896
ta 164,235,16384
mvaa 0,40,4
ta 166,235,16896
mvaa 4,44,4
ra 28,233,2048
mvaa 92,44,8
ra 96,233,4096
mvaa 72,40,8
ta 168,236,16384
ta 170,236,16896
mvaa 144,40,12
ta 188,237,16384
mvaa 148,44,12
ta 190,237,16896
mvaa 156,44,16
ta 192,234,20480
mvaa 152,40,16
ta 194,234,20992
ta 212,235,20480
mvaa 160,48,16
ta 214,235,20992
mvaa 164,52,16
ta 216,236,20480
mvaa 172,52,12
ta 218,236,20992
mvaa 168,48,12
ta 236,237,20480
ta 238,237,20992
mvaa 176,48,8
ta 244,235,24576
mvaa 180,52,8
ta 246,235,25088
mvaa 188,52,4
ra 108,233,6144
mvaa 184,48,4
mvaa 192,56,4
ta 52,239,28672
ta 54,239,29184
mvaa 196,60,4
mvaa 204,60,8
mvaa 200,56,8
mvaa 208,56,12
mvaa 212,60,12
mvaa 220,60,16
mvaa 216,56,16
maaa 128,64,16
maaa 112,68,16
s_waitcnt vmcnt(0)
s_barrier
ra 20,232,32768
ta 148,234,32768
ta 150,234,33280
ra 24,233,32768
ta 152,234,49152
maaa 116,68,12
ta 154,234,49664
ta 156,235,49152
ta 158,235,49664
ta 160,235,32768
ta 162,235,33280
ra 32,232,34816
ra 36,233,34816
maaa 132,64,12
ra 100,232,36864
ra 104,233,36864
ta 172,236,32768
ta 174,236,33280
ta 176,236,49152
maaa 136,64,8
ta 178,236,49664
ta 180,237,49152
v_and_b32_e32 v248,v242,v248
v_cmp_ne_u32_e64 s[64:65],v243,v248
maaa 120,68,8
ta 182,237,49664
ta 184,237,32768
maaa 124,68,4
s_cmp_lg_u64 s[64:65],0
s_cbranch_scc1 L_q_spin
L_q_ok:
ta 186,237,33280
ta 196,234,36864
s_mov_b32 m0,s95
maaa 140,64,4
ds 249,8,42,0
ta 198,234,37376
ta 200,234,53248
mvaa 40,72,4
s_and_b32 s87,s60,7552
ta 202,234,53760
ta 204,235,53248
mvaa 44,76,4
s_add_u32 s53,s87,83898368
ta 206,235,53760
ta 208,235,36864
mvaa 52,76,8
s_lshl_b32 s87,s91,8
ta 210,235,37376
ta 220,236,36864
mvaa 48,72,8
s_add_u32 s54,s87,s88
ta 222,236,37376
ta 224,236,53248
mvaa 56,72,12
ds 249,8,42,1024
ta 226,236,53760
ta 228,237,53248
mvaa 60,76,12
s_lshl_b32 s87,s12,16
ta 230,237,53760
ta 232,237,36864
mvaa 68,76,16
s_add_u32 s87,s87,s54
ta 234,237,37376
ta 240,235,40960
mvaa 64,72,16
s_andn2_b32 s87,s87,64
ta 242,235,41472
ra 44,232,38912
mvaa 32,80,16
s_lshl_b32 s88,s66,5
ra 40,233,38912
ta 48,239,45056
mvaa 36,84,16
ds 249,8,42,2048
ta 50,239,45568
ta 60,239,61440
mvaa 28,84,12
s_lshl_b32 s86,s21,2
ta 62,239,61952
ta 56,238,61440
mvaa 24,80,12
s_add_u32 s63,s87,s88
ta 58,238,61952
ta 68,238,45056
mvaa 16,80,8
s_lshl_b32 s88,s67,5
ta 70,238,45568
ta 64,238,28672
mvaa 20,84,8
s_add_u32 s86,s86,s12
ta 66,238,29184
ta 76,237,24576
mvaa 12,84,4
ds 249,8,42,3072
ta 78,237,25088
ta 72,237,40960
mvaa 8,80,4
s_add_u32 s65,s87,s88
ta 74,237,41472
s_and_b32 s87,s20,4
mvaa 76,4,88
s_lshr_b32 s85,s86,1
ta 84,237,57344
ta 86,237,57856
s_lshl_b32 s87,s87,8
s_lshl_b32 s85,s85,11
s_and_b32 s89,s86,1
s_and_b32 s88,s20,3
s_add_u32 s85,s85,s87
mvaa 96,4,92
s_lshl_b32 s89,s89,9
s_lshr_b32 s49,s86,3
s_add_u32 s85,s85,s89
s_lshl_b32 s89,s88,20
s_add_u32 s85,s85,s89
s_lshl_b32 s49,s49,11
s_and_b32 s89,s86,7
s_add_u32 s49,s49,s87
mvaa 100,8,92
s_lshl_b32 s89,s89,7
ta 80,236,57344
ta 82,236,57856
s_add_u32 s49,s49,s89
s_lshl_b32 s89,s88,18
s_add_u32 s49,s49,s89
s_lshl_b32 s89,s12,6
s_lshl_b32 s35,s90,2
mvaa 80,8,88
s_add_u32 s89,s89,83886080
s_lshl_b32 s90,s22,2
s_add_u32 s90,s90,s89
ta 4,236,40960
ta 6,236,41472
mvaa 84,12,88
buffer_load_dword v240,v247,s[8:11],s90 offen offset:0 sc1
mvaa 104,12,92
s_lshl_b32 s91,s23,2
ta 8,236,24576
ta 10,236,25088
mvaa 108,16,92
s_add_u32 s91,s91,s89
s_mov_b32 s86,268435456
v_mov_b32_e32 v232,0xc1a00000
mvaa 88,16,88
s_add_u32 s35,s35,83886080
ta 12,234,24576
ta 14,234,25088
s_waitcnt lgkmcnt(8)
mvaa 0,144,0
s_bfe_u32 s87,s98,327691
s_cselect_b32 s87,2048,0
ta 92,234,40960
ta 94,234,41472
mvaa 0,148,20
buffer_load_dword v241,v247,s[8:11],s90 offen offset:256 sc1
ta 16,234,57344
ta 18,234,57856
ta 88,235,57344
ta 90,235,57856
mvaa 0,152,24
s_sub_u32 s98,s98,s87
v_mov_b32_e32 v233,0xc1a00000
v_mov_b32_e32 v234,0xc1a00000
mvaa 4,156,24
s_xor_b32 s62,s63,128
v_mov_b32_e32 v235,0xc1a00000
v_cndmask_b32_e64 v250,0,v252,s[40:41]
mvaa 4,160,20
s_mov_b32 s40,0xffc0fffc
s_mov_b32 s41,0xc000fc00
v_mul_f32_e32 v0,0x3e0293ee,v0
mvaa 4,164,0
s_xor_b32 s64,s65,128
v_mul_f32_e32 v1,0x3e0293ee,v1
v_mul_f32_e32 v2,0x3e0293ee,v2
mvaa 92,164,28
buffer_load_dword v242,v247,s[8:11],s91 offen offset:0 sc1
mvaa 92,160,32
v_mul_f32_e32 v3,0x3e0293ee,v3
cv 0,0,1
mvaa 92,156,36
v_mul_f32_e32 v4,0x3e0293ee,v4
cv 1,2,3
mvaa 72,152,36
v_mul_f32_e32 v5,0x3e0293ee,v5
s_waitcnt lgkmcnt(0)
s_barrier
s_mov_b32 m0,s70
mvaa 72,148,32
v_mul_f32_e32 v6,0x3e0293ee,v6
v_mul_f32_e32 v7,0x3e0293ee,v7
mvaa 72,144,28
ds 249,8,43,0
mvaa 144,144,96
cv 2,4,5
cv 3,6,7
mvaa 144,148,100
v_mul_f32_e32 v92,0x3e0293ee,v92
v_mul_f32_e32 v93,0x3e0293ee,v93
mvaa 144,152,104
v_mul_f32_e32 v94,0x3e0293ee,v94
v_mul_f32_e32 v95,0x3e0293ee,v95
mvaa 148,156,104
v_mul_f32_e32 v72,0x3e0293ee,v72
v_mul_f32_e32 v73,0x3e0293ee,v73
mvaa 148,160,100
ds 249,8,43,1024
mvaa 148,164,96
v_mul_f32_e32 v74,0x3e0293ee,v74
v_mul_f32_e32 v75,0x3e0293ee,v75
mvaa 156,164,108
cv 72,72,73
cv 73,74,75
mvaa 156,160,44
cv 74,92,93
cv 75,94,95
mvaa 156,156,40
v_mul_f32_e32 v144,0x3e0293ee,v144
v_mul_f32_e32 v145,0x3e0293ee,v145
mvaa 152,152,40
ds 249,8,43,2048
mvaa 152,148,44
v_mul_f32_e32 v146,0x3e0293ee,v146
v_mul_f32_e32 v147,0x3e0293ee,v147
mvaa 152,144,108
cv 144,144,145
v_mul_f32_e32 v148,0x3e0293ee,v148
mvaa 160,168,108
cv 145,146,147
v_mul_f32_e32 v149,0x3e0293ee,v149
mvaa 160,172,44
v_mul_f32_e32 v150,0x3e0293ee,v150
v_mul_f32_e32 v151,0x3e0293ee,v151
mvaa 160,176,40
ds 249,8,43,3072
mvaa 164,180,40
s_mov_b32 m0,s72
cv 146,148,149
cv 147,150,151
mvaa 164,184,44
v_mul_f32_e32 v156,0x3e0293ee,v156
v_mul_f32_e32 v157,0x3e0293ee,v157
mvaa 164,188,108
v_mul_f32_e32 v158,0x3e0293ee,v158
v_mul_f32_e32 v152,0x3e0293ee,v152
mvaa 172,188,96
v_mul_f32_e32 v159,0x3e0293ee,v159
v_mul_f32_e32 v153,0x3e0293ee,v153
mvaa 172,184,100
ds 255,8,42,0
mvaa 172,180,104
v_mul_f32_e32 v154,0x3e0293ee,v154
v_mul_f32_e32 v155,0x3e0293ee,v155
mvaa 168,176,104
cv 152,152,153
cv 153,154,155
mvaa 168,172,100
ds_write_b128 v251,v[0:3] offset:0
mvaa 168,168,96
cv 154,156,157
cv 155,158,159
mvaa 176,168,28
ds 255,8,42,1024
mvaa 176,172,32
v_mul_f32_e32 v160,0x3e0293ee,v160
v_mul_f32_e32 v161,0x3e0293ee,v161
mvaa 176,176,36
v_mul_f32_e32 v162,0x3e0293ee,v162
v_mul_f32_e32 v163,0x3e0293ee,v163
mvaa 180,180,36
cv 160,160,161
cv 161,162,163
mvaa 180,184,32
v_mul_f32_e32 v164,0x3e0293ee,v164
v_mul_f32_e32 v165,0x3e0293ee,v165
mvaa 180,188,28
ds 255,8,42,2048
ds_write_b128 v251,v[72:75] offset:4096
mvaa 188,188,0
v_mul_f32_e32 v166,0x3e0293ee,v166
v_mul_f32_e32 v167,0x3e0293ee,v167
mvaa 188,184,20
cv 162,164,165
cv 163,166,167
mvaa 188,180,24
v_mul_f32_e32 v172,0x3e0293ee,v172
v_mul_f32_e32 v173,0x3e0293ee,v173
mvaa 184,176,24
v_mul_f32_e32 v174,0x3e0293ee,v174
v_mul_f32_e32 v175,0x3e0293ee,v175
mvaa 184,172,20
ds 255,8,42,3072
mvaa 184,168,0
v_mul_f32_e32 v168,0x3e0293ee,v168
v_mul_f32_e32 v169,0x3e0293ee,v169
mvaa 192,192,0
ds_write_b128 v251,v[144:147] offset:8192
mvaa 192,196,20
v_mul_f32_e32 v170,0x3e0293ee,v170
v_mul_f32_e32 v171,0x3e0293ee,v171
mvaa 192,200,24
cv 168,168,169
cv 169,170,171
mvaa 196,204,24
buffer_load_dword v243,v247,s[8:11],s91 offen offset:256 sc1
mvaa 196,208,20
cv 170,172,173
cv 171,174,175
mvaa 196,212,0
v_mul_f32_e32 v176,0x3e0293ee,v176
v_mul_f32_e32 v177,0x3e0293ee,v177
mvaa 204,212,28
v_mul_f32_e32 v178,0x3e0293ee,v178
v_mul_f32_e32 v179,0x3e0293ee,v179
mvaa 204,208,32
ds_write_b128 v251,v[152:155] offset:12288
mvaa 204,204,36
cv 176,176,177
cv 177,178,179
mvaa 200,200,36
v_mul_f32_e32 v180,0x3e0293ee,v180
v_mul_f32_e32 v181,0x3e0293ee,v181
mvaa 200,196,32
v_mul_f32_e32 v182,0x3e0293ee,v182
v_mul_f32_e32 v183,0x3e0293ee,v183
mvaa 200,192,28
cv 178,180,181
cv 179,182,183
mvaa 208,192,96
v_mul_f32_e32 v188,0x3e0293ee,v188
v_mul_f32_e32 v189,0x3e0293ee,v189
mvaa 208,196,100
v_mul_f32_e32 v190,0x3e0293ee,v190
v_mul_f32_e32 v191,0x3e0293ee,v191
mvaa 208,200,104
ds_write_b128 v251,v[160:163] offset:13312
mvaa 212,204,104
v_mul_f32_e32 v184,0x3e0293ee,v184
v_mul_f32_e32 v185,0x3e0293ee,v185
mvaa 212,208,100
v_mul_f32_e32 v186,0x3e0293ee,v186
v_mul_f32_e32 v187,0x3e0293ee,v187
mvaa 212,212,96
cv 184,184,185
cv 185,186,187
mvaa 220,212,108
cv 186,188,189
cv 187,190,191
)"
R"(mvaa 220,208,44
v_mul_f32_e32 v192,0x3e0293ee,v192
v_mul_f32_e32 v193,0x3e0293ee,v193
mvaa 220,204,40
v_mul_f32_e32 v194,0x3e0293ee,v194
v_mul_f32_e32 v195,0x3e0293ee,v195
mvaa 216,200,40
ds_write_b128 v251,v[168:171] offset:9216
mvaa 216,196,44
cv 192,192,193
cv 193,194,195
mvaa 216,192,108
v_mul_f32_e32 v196,0x3e0293ee,v196
v_mul_f32_e32 v197,0x3e0293ee,v197
maaa 128,216,108
s_waitcnt vmcnt(12)
s_barrier
maaa 128,220,44
v_mul_f32_e32 v198,0x3e0293ee,v198
v_mul_f32_e32 v199,0x3e0293ee,v199
maaa 128,224,40
cv 194,196,197
cv 195,198,199
maaa 112,228,40
v_mul_f32_e32 v204,0x3e0293ee,v204
v_mul_f32_e32 v205,0x3e0293ee,v205
maaa 112,232,44
ds_write_b128 v251,v[176:179] offset:5120
maaa 112,236,108
v_mul_f32_e32 v206,0x3e0293ee,v206
v_mul_f32_e32 v207,0x3e0293ee,v207
maaa 116,236,96
v_mul_f32_e32 v200,0x3e0293ee,v200
v_mul_f32_e32 v201,0x3e0293ee,v201
maaa 116,232,100
v_mul_f32_e32 v202,0x3e0293ee,v202
v_mul_f32_e32 v203,0x3e0293ee,v203
maaa 116,228,104
cv 200,200,201
cv 201,202,203
maaa 132,224,104
cv 202,204,205
cv 203,206,207
maaa 132,220,100
v_mul_f32_e32 v208,0x3e0293ee,v208
v_mul_f32_e32 v209,0x3e0293ee,v209
maaa 132,216,96
ds_write_b128 v251,v[184:187] offset:1024
maaa 136,216,28
v_mul_f32_e32 v210,0x3e0293ee,v210
v_mul_f32_e32 v211,0x3e0293ee,v211
maaa 136,220,32
cv 208,208,209
cv 209,210,211
maaa 136,224,36
v_mul_f32_e32 v212,0x3e0293ee,v212
v_mul_f32_e32 v213,0x3e0293ee,v213
maaa 120,228,36
v_mul_f32_e32 v214,0x3e0293ee,v214
v_mul_f32_e32 v215,0x3e0293ee,v215
maaa 120,232,32
cv 210,212,213
cv 211,214,215
maaa 120,236,28
v_mul_f32_e32 v220,0x3e0293ee,v220
v_mul_f32_e32 v221,0x3e0293ee,v221
maaa 124,236,0
ds_write_b128 v251,v[192:195] offset:2048
maaa 124,232,20
v_mul_f32_e32 v216,0x3e0293ee,v216
v_mul_f32_e32 v217,0x3e0293ee,v217
maaa 124,228,24
v_mul_f32_e32 v222,0x3e0293ee,v222
v_mul_f32_e32 v223,0x3e0293ee,v223
maaa 140,224,24
v_mul_f32_e32 v218,0x3e0293ee,v218
v_mul_f32_e32 v219,0x3e0293ee,v219
maaa 140,220,20
cv 216,216,217
cv 217,218,219
maaa 140,216,0
cv 218,220,221
cv 219,222,223
mvaa 88,108,64
v_accvgpr_read_b32 v0,a128
v_accvgpr_read_b32 v1,a129
mvaa 88,44,68
ds_write_b128 v251,v[200:203] offset:6144
mvaa 88,40,56
v_mul_f32_e32 v0,0x3e0293ee,v0
v_mul_f32_e32 v1,0x3e0293ee,v1
mvaa 108,40,60
v_accvgpr_read_b32 v2,a130
v_accvgpr_read_b32 v3,a131
mvaa 108,44,48
v_mul_f32_e32 v2,0x3e0293ee,v2
v_mul_f32_e32 v3,0x3e0293ee,v3
cv 88,88,89
cv 89,90,91
mvaa 108,108,52
buffer_store_dwordx2 v[88:89],v253,s[8:11],s99 offen offset:1024 sc0 sc1
mvaa 36,76,108
cv 0,0,1
v_accvgpr_read_b32 v4,a112
mvaa 36,72,44
v_accvgpr_read_b32 v5,a113
v_accvgpr_read_b32 v6,a114
mvaa 36,84,40
ds_write_b128 v251,v[208:211] offset:10240
mvaa 32,80,40
cv 108,108,109
cv 109,110,111
mvaa 32,4,44
buffer_store_dwordx2 v[108:109],v253,s[8:11],s99 offen offset:1536 sc0 sc1
mvaa 32,8,108
cv 1,2,3
v_mul_f32_e32 v4,0x3e0293ee,v4
mvaa 64,12,108
v_mul_f32_e32 v5,0x3e0293ee,v5
v_accvgpr_read_b32 v7,a115
mvaa 64,92,44
v_mul_f32_e32 v6,0x3e0293ee,v6
v_mul_f32_e32 v7,0x3e0293ee,v7
mvaa 64,16,40
cv 32,32,33
cv 33,34,35
cv 34,36,37
cv 35,38,39
mvaa 68,88,40
buffer_store_dwordx4 v[32:35],v244,s[8:11],s99 offen sc0 sc1
ds_write_b128 v251,v[216:219] offset:14336
mvaa 68,240,44
cv 2,4,5
cv 3,6,7
mvaa 68,244,108
v_accvgpr_read_b32 v92,a116
v_accvgpr_read_b32 v93,a117
mvaa 60,244,96
ra 192,224,0
v_accvgpr_read_b32 v94,a118
mvaa 60,240,100
ra 196,224,1024
v_mul_f32_e32 v92,0x3e0293ee,v92
mvaa 60,88,104
ra 200,224,2048
v_mul_f32_e32 v93,0x3e0293ee,v93
mvaa 56,16,104
v_mul_f32_e32 v94,0x3e0293ee,v94
v_accvgpr_read_b32 v95,a119
mvaa 56,92,100
ds_write_b128 v251,v[0:3] offset:15360
mvaa 56,12,96
v_accvgpr_read_b32 v72,a132
v_accvgpr_read_b32 v73,a133
mvaa 24,8,96
v_accvgpr_read_b32 v74,a134
v_mul_f32_e32 v95,0x3e0293ee,v95
mvaa 24,4,100
v_mul_f32_e32 v72,0x3e0293ee,v72
v_mul_f32_e32 v73,0x3e0293ee,v73
mvaa 24,80,104
v_mul_f32_e32 v74,0x3e0293ee,v74
v_accvgpr_read_b32 v75,a135
mvaa 28,84,104
v_mul_f32_e32 v75,0x3e0293ee,v75
cv 72,72,73
mvaa 28,72,100
ra 204,224,3072
cv 73,74,75
cv 74,92,93
mvaa 28,76,96
ra 208,224,4096
cv 75,94,95
mvaa 20,76,28
ds_write_b128 v251,v[72:75] offset:11264
mvaa 20,72,32
v_accvgpr_read_b32 v144,a136
v_accvgpr_read_b32 v145,a137
mvaa 20,84,36
v_accvgpr_read_b32 v146,a138
v_accvgpr_read_b32 v147,a139
mvaa 16,80,36
ra 212,224,5120
ra 248,224,14336
ra 252,224,15360
v_mul_f32_e32 v144,0x3e0293ee,v144
v_mul_f32_e32 v145,0x3e0293ee,v145
v_mul_f32_e32 v146,0x3e0293ee,v146
v_mul_f32_e32 v147,0x3e0293ee,v147
mvaa 16,4,32
ra 236,224,11264
cv 144,144,145
v_accvgpr_read_b32 v148,a120
v_accvgpr_read_b32 v149,a121
v_accvgpr_read_b32 v150,a122
cv 145,146,147
mvaa 16,8,28
v_mul_f32_e32 v148,0x3e0293ee,v148
v_mul_f32_e32 v149,0x3e0293ee,v149
v_mul_f32_e32 v150,0x3e0293ee,v150
v_accvgpr_read_b32 v151,a123
v_mul_f32_e32 v151,0x3e0293ee,v151
cv 146,148,149
mvaa 48,12,28
ra 228,224,9216
cv 147,150,151
mvaa 48,92,32
ds_write_b128 v251,v[144:147] offset:7168
mvaa 48,16,36
v_accvgpr_read_b32 v156,a124
v_accvgpr_read_b32 v157,a125
mvaa 52,88,36
ra 232,224,10240
ra 216,224,6144
ra 220,224,7168
ra 224,224,8192
v_accvgpr_read_b32 v158,a126
v_accvgpr_read_b32 v159,a127
mvaa 52,240,32
v_accvgpr_read_b32 v152,a140
v_mul_f32_e32 v156,0x3e0293ee,v156
v_mul_f32_e32 v157,0x3e0293ee,v157
v_mul_f32_e32 v158,0x3e0293ee,v158
v_mul_f32_e32 v159,0x3e0293ee,v159
mvaa 52,244,28
v_mul_f32_e32 v152,0x3e0293ee,v152
v_accvgpr_read_b32 v153,a141
v_mul_f32_e32 v153,0x3e0293ee,v153
v_accvgpr_read_b32 v154,a142
v_accvgpr_read_b32 v155,a143
mvaa 44,244,0
v_mul_f32_e32 v154,0x3e0293ee,v154
v_mul_f32_e32 v155,0x3e0293ee,v155
cv 152,152,153
cv 153,154,155
cv 154,156,157
mvaa 44,240,20
ra 244,224,13312
cv 155,158,159
mvaa 44,88,24
ds_write_b128 v251,v[152:155] offset:3072
mvaa 40,16,24
cv 64,64,65
cv 56,56,57
mvaa 40,92,20
cv 57,58,59
cv 58,60,61
mvaa 40,12,0
ra 240,224,12288
cv 59,62,63
mvaa 8,8,0
cv 16,16,17
cv 17,18,19
mvaa 8,4,20
cv 48,48,49
cv 194,44,45
mvaa 8,80,24
cv 195,46,47
cv 192,40,41
mvaa 12,84,24
cv 193,42,43
cv 49,50,51
mvaa 12,72,20
s_cmp_eq_u32 s19,0
s_cselect_b32 s76,32768,0
s_cselect_b32 s77,49152,s86
s_cselect_b32 s78,65536,s46
s_cselect_b32 s79,81920,s86
mvaa 12,76,0
s_add_u32 s86,s95,98304
cv 8,8,9
v_add_u32_e32 v236,s86,v244
mvaa 76,0,64
s_add_u32 s46,s46,16384
v_xor_b32_e32 v237,4096,v236
s_waitcnt lgkmcnt(0)
s_barrier
s_mov_b32 m0,s68
mvaa 76,20,68
ds 249,8,76,0
ra 128,237,32768
ra 132,237,33792
mvaa 76,24,56
s_add_u32 s95,s47,32768
ra 136,237,34816
cv 9,10,11
mvaa 96,24,60
ra 140,237,35840
ra 144,237,49152
mvaa 96,20,48
cv 10,12,13
cv 11,14,15
mvaa 96,0,52
ra 148,237,50176
cv 50,52,53
mvaa 100,28,52
ds 249,8,76,1024
ra 152,237,51200
ra 156,237,52224
mvaa 100,32,48
ra 176,236,16384
cv 51,54,55
mvaa 100,36,60
ra 180,236,17408
cv 18,20,21
mvaa 80,36,56
ra 184,236,18432
cv 19,22,23
mvaa 80,32,68
ra 188,236,19456
cv 24,24,25
mvaa 80,28,64
ds 249,8,76,2048
ra 160,236,0
mvaa 84,96,64
ra 164,236,1024
cv 25,26,27
mvaa 84,100,68
ra 168,236,2048
ra 172,236,3072
mvaa 84,104,56
cv 26,28,29
cv 27,30,31
mvaa 104,104,60
cv 65,66,67
cv 66,68,69
mvaa 104,100,48
ds 249,8,76,3072
mvaa 104,96,52
s_mov_b32 m0,s69
cv 67,70,71
v_cndmask_b32_e64 v252,0,v252,s[40:41]
cvvv 164,8,192,112
cv 70,76,77
cv 71,78,79
cvvv 168,8,48,116
ds 249,8,77,0
cvvv 172,16,48,112
cv 94,96,97
v_mov_b32_e32 v76,v88
cvvv 176,16,56,116
cv 95,98,99
s_waitcnt vmcnt(8) lgkmcnt(0)
s_barrier
cvvv 180,24,56,112
ds 249,8,77,1024
cvvv 184,24,64,116
ra 0,224,32768
cv 96,100,101
cvvv 188,32,64,112
ra 4,224,33792
cv 97,102,103
cvaa 0,192,128,232
buffer_load_dword v17,v247,s[8:11],s35 offen offset:64 sc1
mvaa 0,200,132
ra 8,224,34816
cv 72,80,81
mvaa 0,208,136
ra 12,224,35840
cv 73,82,83
mvaa 0,216,140
ds 249,8,77,2048
cvaa 4,196,128,232
ra 16,224,36864
cv 74,84,85
mvaa 4,204,132
ra 20,224,37888
v_max3_f32 v172,v168,v169,v172
mvaa 4,212,136
ds 249,8,77,3072
mvaa 4,220,140
s_mov_b32 m0,s74
ra 24,224,38912
v_max_f32_e32 v172,v172,v173
cvaa 32,224,128,232
ra 28,224,39936
v_max3_f32 v174,v170,v171,v174
mvaa 32,232,132
buffer_load_dword v19,v247,s[8:11],s35 offen offset:192 sc1
mvaa 32,240,136
ra 32,224,40960
v_max_f32_e32 v174,v174,v175
mvaa 32,248,140
ra 36,224,41984
ra 40,224,43008
cvaa 36,228,128,232
buffer_load_dword v18,v247,s[8:11],s35 offen offset:128 sc1
ra 44,224,44032
mvaa 36,236,132
ra 48,224,45056
v_max_f32_e32 v148,v172,v174
mvaa 36,244,136
ra 56,224,47104
ra 60,224,48128
mvaa 36,252,140
buffer_load_dword v16,v247,s[8:11],s35 offen offset:0 sc1
ra 52,224,46080
cvaa 8,192,144,232
v_mov_b32_e32 v149,v148
cv 75,86,87
mvaa 8,200,148
v_permlane16_swap_b32_e32 v148,v149
mvaa 8,208,152
ds 255,8,43,0
mvaa 8,216,156
v_max_f32_e32 v148,v148,v149
v_mov_b32_e32 v149,v148
cvaa 12,196,144,232
cv 98,104,105
v_max3_f32 v180,v176,v177,v180
mvaa 12,204,148
ds 255,8,43,1024
mvaa 12,212,152
v_permlane32_swap_b32_e32 v148,v149
mvaa 12,220,156
v_max_f32_e32 v148,v148,v149
s_waitcnt lgkmcnt(0)
s_barrier
cvaa 40,224,144,232
ds 255,8,43,2048
mvaa 40,232,148
ra 64,225,0
v_mul_f32_e32 v148,0xbe8293ee,v148
mvaa 40,240,152
ra 68,225,1024
v_fmamk_f32 v172,v172,0x3e8293ee,v148
mvaa 40,248,156
ds 255,8,43,3072
cvaa 44,228,144,232
s_mov_b32 m0,s70
ra 72,225,2048
v_fmamk_f32 v174,v174,0x3e8293ee,v148
mvaa 44,236,148
ra 76,225,3072
v_max3_f32 v182,v178,v179,v182
mvaa 44,244,152
ds 249,8,78,0
mvaa 44,252,156
ra 80,225,4096
v_max_f32_e32 v180,v180,v181
cvaa 24,192,176,128
ra 84,225,5120
v_max_f32_e32 v182,v182,v183
mvaa 24,200,180
ds 249,8,78,1024
mvaa 24,208,184
ra 88,225,6144
v_max_f32_e32 v152,v180,v182
mvaa 24,216,188
ra 92,225,7168
v_mov_b32_e32 v153,v152
cvaa 28,196,176,132
ds 249,8,78,2048
mvaa 28,204,180
ra 96,225,8192
v_max3_f32 v188,v184,v185,v188
mvaa 28,212,184
ra 100,225,9216
ra 104,225,10240
mvaa 28,220,188
ds 249,8,78,3072
ra 108,225,11264
cvaa 56,224,176,136
s_mov_b32 m0,s71
ra 112,225,12288
v_max3_f32 v190,v186,v187,v190
mvaa 56,232,180
ra 116,225,13312
ra 120,225,14336
mvaa 56,240,184
ds 249,8,79,0
ra 124,225,15360
mvaa 56,248,188
ex 172
cvaa 60,228,176,140
ex 174
mvaa 60,236,180
ds 249,8,79,1024
mvaa 60,244,184
v_permlane16_swap_b32_e32 v152,v153
mvaa 60,252,188
cv 172,172,172
cv 174,174,174
cvaa 80,0,160,128
ds 249,8,79,2048
mvaa 80,8,164
v_and_b32_e32 v168,v226,v172
v_max_f32_e32 v152,v152,v153
mvaa 80,16,168
v_and_b32_e32 v169,v227,v174
v_and_b32_e32 v170,v250,v172
v_and_b32_e32 v171,v252,v174
mvaa 80,24,172
ds 249,8,79,3072
v_mov_b32_e32 v153,v152
cvaa 84,4,160,132
v_max_f32_e32 v188,v188,v189
v_permlane32_swap_b32_e32 v152,v153
v_max_f32_e32 v152,v152,v153
mvaa 84,12,164
v_mul_f32_e32 v152,0xbe8293ee,v152
v_fmamk_f32 v180,v180,0x3e8293ee,v152
mvaa 84,20,168
v_fmamk_f32 v182,v182,0x3e8293ee,v152
v_max_f32_e32 v190,v190,v191
mvaa 84,28,172
ex 180
cvaa 112,32,160,136
ex 182
v_max_f32_e32 v156,v188,v190
mvaa 112,40,164
v_mov_b32_e32 v157,v156
cv 180,180,180
mvaa 112,48,168
v_permlane16_swap_b32_e32 v156,v157
mvaa 112,56,172
cv 182,182,182
v_and_b32_e32 v176,v226,v180
v_max_f32_e32 v156,v156,v157
s_waitcnt vmcnt(15) lgkmcnt(0)
s_barrier
s_mov_b32 m0,s72
cvaa 116,36,160,140
ds 255,8,76,0
v_and_b32_e32 v177,v227,v182
mvaa 116,44,164
v_and_b32_e32 v178,v250,v180
v_and_b32_e32 v179,v252,v182
v_mov_b32_e32 v157,v156
mvaa 116,52,168
cv 99,106,107
v_permlane32_swap_b32_e32 v156,v157
v_max_f32_e32 v156,v156,v157
mvaa 116,60,172
ds 255,8,76,1024
v_mul_f32_e32 v156,0xbe8293ee,v156
zvvv 148,228,168
v_fmamk_f32 v188,v188,0x3e8293ee,v156
ex 188
zvvv 200,70,168
v_fmamk_f32 v190,v190,0x3e8293ee,v156
ex 190
zvvv 204,94,168
ds 255,8,76,2048
zvvv 152,228,176
cv 188,188,188
cv 190,190,190
v_and_b32_e32 v184,v226,v188
zvvv 208,72,176
v_and_b32_e32 v185,v227,v190
v_and_b32_e32 v186,v250,v188
v_and_b32_e32 v187,v252,v190
zvvv 212,96,176
ds 255,8,76,3072
s_mov_b32 m0,s73
v_mov_b32_e32 v77,v89
zvvv 156,228,184
v_mov_b32_e32 v100,v108
v_mov_b32_e32 v101,v109
zvvv 216,74,184
ex 0
zvvv 220,98,184
ds 255,8,77,0
cvaa 64,0,128,232
ex 1
mvaa 64,8,132
v_mov_b32_e32 v170,v70
v_mov_b32_e32 v171,v71
mvaa 64,16,136
ds 255,8,77,1024
mvaa 64,24,140
ex 2
cvaa 68,4,128,232
ex 3
mvaa 68,12,132
ds 255,8,77,2048
mvaa 68,20,136
ex 4
mvaa 68,28,140
ex 5
cvaa 96,32,128,232
ds 255,8,77,3072
mvaa 96,40,132
ex 6
mvaa 96,48,136
ex 7
mvaa 96,56,140
ex 32
cvaa 100,36,128,232
ex 33
mvaa 100,44,132
ex 34
mvaa 100,52,136
v_mov_b32_e32 v160,v120
ex 35
mvaa 100,60,140
v_mov_b32_e32 v161,v121
ex 36
cvaa 72,0,144,232
v_mov_b32_e32 v162,v122
ex 37
mvaa 72,8,148
v_mov_b32_e32 v163,v123
ex 38
mvaa 72,16,152
v_mov_b32_e32 v175,v95
ex 39
mvaa 72,24,156
v_mov_b32_e32 v174,v94
ex 8
cvaa 76,4,144,232
cv 0,0,1
ex 9
mvaa 76,12,148
cv 1,2,3
ex 10
mvaa 76,20,152
cv 2,4,5
ex 11
mvaa 76,28,156
cv 3,6,7
ex 12
cvaa 104,32,144,232
cv 4,32,33
ex 13
mvaa 104,40,148
v_sub_f32_e32 v17,1.0,v17
ex 14
mvaa 104,48,152
v_rcp_f32_e32 v148,v148
s_waitcnt vmcnt(21)
v_sub_f32_e32 v19,1.0,v19
mvaa 104,56,156
v_mul_f32_e32 v148,v148,v17
v_mul_f32_e32 v200,v148,v200
v_sub_f32_e32 v18,1.0,v18
cvaa 108,36,144,232
v_mul_f32_e32 v201,v148,v201
v_mul_f32_e32 v202,v148,v202
v_mul_f32_e32 v203,v148,v203
mvaa 108,44,148
v_mul_f32_e32 v204,v148,v204
cv 200,200,201
cv 201,202,203
mvaa 108,52,152
buffer_load_dwordx4 v[176:179],v244,s[8:11],s98 offen sc1
v_mul_f32_e32 v205,v148,v205
mvaa 108,60,156
v_mul_f32_e32 v206,v148,v206
v_mul_f32_e32 v207,v148,v207
cv 202,204,205
cvaa 88,0,176,232
cv 203,206,207
v_rcp_f32_e32 v152,v152
mvaa 88,8,180
buffer_load_dwordx2 v[168:169],v253,s[8:11],s98 offen offset:1024 sc1
v_mul_f32_e32 v152,v152,v18
mvaa 88,16,184
v_mul_f32_e32 v208,v152,v208
v_mul_f32_e32 v209,v152,v209
v_mul_f32_e32 v210,v152,v210
mvaa 88,24,188
v_mul_f32_e32 v211,v152,v211
cv 208,208,209
cv 209,210,211
cvaa 92,4,176,232
buffer_load_dwordx2 v[172:173],v253,s[8:11],s98 offen offset:1536 sc1
v_mul_f32_e32 v212,v152,v212
mvaa 92,12,180
v_mul_f32_e32 v213,v152,v213
v_mul_f32_e32 v214,v152,v214
v_mul_f32_e32 v215,v152,v215
mvaa 92,20,184
cv 210,212,213
v_rcp_f32_e32 v156,v156
mvaa 92,28,188
buffer_store_dwordx4 v[200:203],v244,s[8:11],s34 offen sc0 sc1
cv 211,214,215
cvaa 120,32,176,232
v_mul_f32_e32 v156,v156,v19
v_mul_f32_e32 v216,v156,v216
v_mul_f32_e32 v217,v156,v217
mvaa 120,40,180
v_mul_f32_e32 v218,v156,v218
v_mul_f32_e32 v219,v156,v219
cv 216,216,217
mvaa 120,48,184
buffer_store_dwordx4 v[208:211],v244,s[8:11],s50 offen sc0 sc1
cv 217,218,219
mvaa 120,56,188
v_mul_f32_e32 v220,v156,v220
v_mul_f32_e32 v221,v156,v221
v_mul_f32_e32 v222,v156,v222
cvaa 124,36,176,232
v_mul_f32_e32 v223,v156,v223
cv 218,220,221
cv 219,222,223
mvaa 124,44,180
buffer_store_dwordx4 v[216:219],v244,s[8:11],s51 offen sc0 sc1
cv 5,34,35
mvaa 124,52,184
cv 6,36,37
ex 15
mvaa 124,60,188
s_cmp_lg_u32 s19,0
s_cbranch_scc1 L_j0
s_nop 1
s_lshl_b32 s86,s12,12
s_add_u32 s86,s86,131072
v_add_u32_e32 v236,s86,v244
ra 160,236,0
ra 164,236,1024
ra 168,236,2048
ra 172,236,3072
ra 176,236,16384
ra 180,236,17408
ra 184,236,18432
ra 188,236,19456
v_add_u32_e32 v224,s94,v224
s_waitcnt lgkmcnt(0)
L_j0:
cv 7,38,39
ex 40
zaav 0,64,0
buffer_load_dword v247,v246,s[24:27],s49 offen
maav 0,96,4
s_add_u32 s49,s49,32768
ex 41
zaav 4,68,0
s_lshl_b32 s86,s21,2
ex 42
v_bfe_i32 v248,v244,4,1
maav 4,100,4
buffer_load_dword v247,v245,s[36:39],s85 offen
v_and_b32_e32 v248,2863311530,v248
zaav 8,72,0
s_add_u32 s86,s86,s12
ex 43
v_or_b32_e32 v248,1145324612,v248
maav 8,104,4
s_lshl_b32 s86,s86,8
ex 44
v_mov_b64_e32 v[144:145],0
zaav 12,76,0
buffer_load_dword v247,v246,s[24:27],s49 offen
cv 8,8,9
maav 12,108,4
s_add_u32 s85,s85,131072
cv 9,10,11
ex 45
v_smfmac_f32_16x16x64_bf16 v[144:147],v[228:231],v[0:7],v248
cv 10,12,13
ex 46
zaav 16,80,0
s_add_u32 s49,s49,32768
cv 11,14,15
ex 47
maav 16,112,4
s_waitcnt vmcnt(8)
v_max3_u32 v180,v176,v177,v178
cv 12,40,41
zaav 20,84,0
s_waitcnt vmcnt(7)
v_max3_u32 v180,v180,v179,v168
s_waitcnt vmcnt(6)
v_max3_u32 v180,v180,v169,v172
v_max_u32_e32 v180,v180,v173
v_cmp_eq_u32_e64 s[40:41],-1,v180
maav 20,116,4
s_cmp_lg_u64 s[40:41],0
s_cbranch_scc1 L_bx
L_bxr:
s_bitcmp1_b32 170,s20
s_cselect_b32 s87,L_odd-L_last,0
s_add_u32 s86,s86,s87
s_add_u32 s86,s86,L_last-L_att_pc
mvvv 160,176,192
cv 13,42,43
zaav 24,88,0
cv 14,44,45
v_lshl_add_u32 v238,v244,3,s86
v_min_u32_e32 v238,L_att_end-L_att_pc-4,v238
cv 15,46,47
maav 24,120,4
s_mov_b64 exec,3
global_load_dword v239,v238,s[96:97]
s_mov_b64 exec,-1
zaav 28,92,0
v_max3_f32 v164,v160,v161,v164
v_max_f32_e32 v164,v164,v165
v_max3_f32 v166,v162,v163,v166
v_max_f32_e32 v166,v166,v167
v_max_f32_e32 v180,v164,v166
v_mov_b32_e32 v181,v180
maav 28,124,4
ex 24
v_permlane16_swap_b32_e32 v180,v181
v_max_f32_e32 v180,v180,v181
v_mov_b32_e32 v181,v180
zaav 32,64,8
ex 25
v_permlane32_swap_b32_e32 v180,v181
v_max_f32_e32 v180,v180,v181
v_mul_f32_e32 v180,0xbe8293ee,v180
v_fmamk_f32 v164,v164,0x3e8293ee,v180
maav 32,96,12
ex 164
v_fmamk_f32 v166,v166,0x3e8293ee,v180
ex 166
cv 164,164,164
cv 166,166,166
zaav 36,68,8
v_and_b32_e32 v160,v226,v164
v_and_b32_e32 v161,v227,v166
v_and_b32_e32 v162,v250,v164
v_and_b32_e32 v163,v252,v166
maav 36,100,12
v_sub_f32_e32 v16,1.0,v16
zvvv 180,228,160
zaav 40,72,8
ex 26
zvvv 184,168,160
maav 40,104,12
ex 27
zvvv 188,172,160
zaav 44,76,8
v_mov_b64_e32 v[148:149],0
v_rcp_f32_e32 v180,v180
maav 44,108,12
v_mul_f32_e32 v180,v180,v16
v_mul_f32_e32 v184,v180,v184
v_mul_f32_e32 v185,v180,v185
v_mul_f32_e32 v186,v180,v186
v_mul_f32_e32 v187,v180,v187
v_mul_f32_e32 v188,v180,v188
v_mul_f32_e32 v189,v180,v189
v_smfmac_f32_16x16x64_bf16 v[148:151],v[228:231],v[8:15],v248
v_mul_f32_e32 v190,v180,v190
v_mul_f32_e32 v191,v180,v191
cv 184,184,185
cv 185,186,187
cv 186,188,189
cv 187,190,191
zaav 48,80,8
buffer_store_dwordx4 v[184:187],v244,s[8:11],s33 offen sc0 sc1
cv 24,24,25
cv 25,26,27
ex 28
maav 48,112,12
ex 29
ex 30
ex 31
cv 26,28,29
zaav 52,84,8
cv 27,30,31
ex 56
ex 57
ex 58
maav 52,116,12
ex 59
ex 60
zaav 56,88,8
ra 192,224,0
ra 196,224,1024
ex 61
maav 56,120,12
ra 200,224,2048
cv 28,56,57
ex 62
zaav 60,92,8
ra 204,224,3072
ra 208,224,4096
ex 63
maav 60,124,12
ra 212,224,5120
cv 29,58,59
cv 30,60,61
cv 31,62,63
zvav 192,64,24
ra 216,224,6144
ra 220,224,7168
ex 64
mvav 192,96,28
ra 224,224,8192
v_mov_b32_e32 v251,-1
ex 65
zvav 196,68,24
ra 228,224,9216
ra 232,224,10240
ex 66
mvav 196,100,28
ra 236,224,11264
v_mov_b64_e32 v[152:153],0
ex 67
zvav 200,72,24
ra 240,224,12288
ra 244,224,13312
v_mov_b64_e32 v[156:157],0
cv 64,64,65
mvav 200,104,28
ra 248,224,14336
ra 252,224,15360
ex 68
zvav 204,76,24
ex 69
ex 70
mvav 204,108,28
cv 65,66,67
ex 71
cv 66,68,69
v_smfmac_f32_16x16x64_bf16 v[156:159],v[228:231],v[24:31],v248
ex 96
ex 97
zvav 208,80,24
ex 98
ex 99
mvav 208,112,28
cv 67,70,71
cv 68,96,97
ex 100
zvav 212,84,24
ex 101
ex 102
mvav 212,116,28
cv 69,98,99
ex 103
cv 70,100,101
zvav 216,88,24
ex 72
ex 73
mvav 216,120,28
ex 74
ex 75
zvav 220,92,24
ex 76
ex 77
mvav 220,124,28
s_cmp_ge_i32 s47,s46
s_cselect_b32 s76,s95,s46
s_cselect_b32 s77,s95,268435456
s_add_u32 s77,s77,16384
v_mov_b32_e32 v250,-1
v_mov_b32_e32 v252,-1
v_mov_b32_e32 v253,-1
cv 71,102,103
s_mov_b64 exec,0
s_bitcmp1_b32 170,s20
s_cbranch_scc1 L_vbO
s_cmp_eq_u32 s12,0
s_cbranch_scc1 L_vs1
s_cmp_eq_u32 s12,1
s_cbranch_scc1 L_vs2
s_cmp_eq_u32 s12,2
s_cbranch_scc1 L_vs3
s_branch L_vs4
L_vbO:
s_cmp_eq_u32 s12,0
s_cbranch_scc1 L_vs9
s_cmp_eq_u32 s12,1
s_cbranch_scc1 L_vs10
s_cmp_eq_u32 s12,2
s_cbranch_scc1 L_vs11
s_branch L_vs12
L_cw0:
s_mov_b64 exec,-1
s_waitcnt lgkmcnt(0)
s_barrier
cvaa 0,192,128,232
mvaa 0,200,132
s_add_u32 s81,s81,1
ex 78
mvaa 0,208,136
s_add_u32 s44,s44,1024
ex 79
mvaa 0,216,140
cv 72,72,73
cv 73,74,75
cvaa 4,196,128,232
s_mov_b32 m0,s68
ex 104
mvaa 4,204,132
ds 249,8,76,0
mvaa 4,212,136
ex 105
mvaa 4,220,140
ex 106
cvaa 32,224,128,232
ra 64,225,32768
cv 74,76,77
mvaa 32,232,132
ex 107
mvaa 32,240,136
ra 68,225,33792
cv 75,78,79
mvaa 32,248,140
ex 108
cvaa 36,228,128,232
ds 249,8,76,1024
mvaa 36,236,132
ex 109
)"
R"(mvaa 36,244,136
ra 72,225,34816
cv 76,104,105
mvaa 36,252,140
ex 110
cvaa 8,192,144,232
ra 76,225,35840
cv 77,106,107
mvaa 8,200,148
ex 111
mvaa 8,208,152
ex 80
mvaa 8,216,156
ds 249,8,76,2048
cvaa 12,196,144,232
ra 80,225,36864
cv 78,108,109
mvaa 12,204,148
ex 81
mvaa 12,212,152
ra 84,225,37888
cv 79,110,111
mvaa 12,220,156
ex 82
cvaa 40,224,144,232
ex 83
mvaa 40,232,148
ra 88,225,38912
cv 80,80,81
mvaa 40,240,152
ds 249,8,76,3072
mvaa 40,248,156
ex 84
cvaa 44,228,144,232
ra 92,225,39936
cv 81,82,83
mvaa 44,236,148
ex 85
mvaa 44,244,152
ex 86
mvaa 44,252,156
ra 96,225,40960
cv 82,84,85
cvaa 16,192,160,232
ex 87
mvaa 16,200,164
s_mov_b32 m0,s69
ex 112
mvaa 16,208,168
ds 249,8,77,0
mvaa 16,216,172
ra 100,225,41984
cv 83,86,87
cvaa 20,196,160,232
ex 113
mvaa 20,204,164
ra 104,225,43008
cv 84,112,113
mvaa 20,212,168
ex 114
mvaa 20,220,172
ex 115
cvaa 48,224,160,232
ra 108,225,44032
cv 85,114,115
mvaa 48,232,164
ds 249,8,77,1024
mvaa 48,240,168
ex 116
mvaa 48,248,172
ra 112,225,45056
cvaa 52,228,160,232
ex 117
mvaa 52,236,164
ex 118
mvaa 52,244,168
ra 116,225,46080
cv 86,116,117
mvaa 52,252,172
ex 119
cvaa 24,192,176,232
ds 249,8,77,2048
mvaa 24,200,180
ra 120,225,47104
cv 87,118,119
mvaa 24,208,184
ex 88
mvaa 24,216,188
ex 89
cvaa 28,196,176,232
ra 124,225,48128
cv 88,88,89
mvaa 28,204,180
ex 90
mvaa 28,212,184
ex 91
mvaa 28,220,188
ds 249,8,77,3072
cvaa 56,224,176,232
ex 92
mvaa 56,232,180
ex 93
mvaa 56,240,184
ex 94
mvaa 56,248,188
cvaa 60,228,176,232
ex 95
mvaa 60,236,180
ex 120
mvaa 60,244,184
ex 121
mvaa 60,252,188
s_waitcnt vmcnt(8) lgkmcnt(0)
s_barrier
maav 0,64,64
cv 89,90,91
cv 90,92,93
maav 0,96,68
s_mov_b32 m0,s74
ex 122
maav 4,68,64
ds 255,8,78,0
maav 4,100,68
ex 123
maav 8,72,64
ex 124
maav 8,104,68
ex 125
maav 12,76,64
cv 91,94,95
cv 92,120,121
maav 12,108,68
ex 126
v_smfmac_f32_16x16x64_bf16 v[144:147],v[228:231],v[64:71],v248
cv 93,122,123
maav 16,80,64
ex 127
maav 16,112,68
buffer_load_dword v247,v245,s[36:39],s85 offen
maav 20,84,64
cv 94,124,125
cv 95,126,127
maav 20,116,68
s_lshl_b32 s86,s19,3
s_lshr_b32 s87,s18,2
s_add_u32 s86,s86,s87
maav 24,88,64
s_lshl_b32 s86,s86,7
s_add_u32 s86,s86,83898368
v_mov_b32_e32 v238,1
maav 24,120,68
ra 192,224,32768
maav 28,92,64
ds 255,8,78,1024
maav 28,124,68
s_mov_b64 exec,1
buffer_atomic_add v238,off,s[8:11],s86 sc1
s_mov_b64 exec,-1
maav 32,64,72
ra 196,224,33792
maav 32,96,76
s_add_u32 s85,s85,131072
maav 36,68,72
ra 200,224,34816
maav 36,100,76
maav 40,72,72
ds 255,8,78,2048
maav 40,104,76
maav 44,76,72
ra 204,224,35840
maav 44,108,76
v_smfmac_f32_16x16x64_bf16 v[148:151],v[228:231],v[72:79],v248
ra 208,224,36864
maav 48,80,72
maav 48,112,76
maav 52,84,72
ds 255,8,78,3072
maav 52,116,76
ra 212,224,37888
maav 56,88,72
ex 0
maav 56,120,76
ra 216,224,38912
maav 60,92,72
ex 1
maav 60,124,76
s_mov_b32 m0,s75
ex 2
zvav 160,64,80
ra 220,224,39936
cv 0,0,1
mvav 160,96,84
ds 255,8,79,0
zvav 164,68,80
ex 3
mvav 164,100,84
ra 224,224,40960
cv 1,2,3
zvav 168,72,80
ex 4
mvav 168,104,84
ra 228,224,41984
zvav 172,76,80
ds 255,8,79,1024
mvav 172,108,84
s_add_u32 s46,s46,16384
ex 5
v_smfmac_f32_16x16x64_bf16 v[152:155],v[228:231],v[80:87],v248
ra 232,224,43008
zvav 176,80,80
s_add_u32 s47,s47,32768
ex 6
mvav 176,112,84
s_add_u32 s95,s47,32768
ex 7
zvav 180,84,80
ra 236,224,44032
cv 2,4,5
mvav 180,116,84
ex 32
zvav 184,88,80
ds 255,8,79,2048
mvav 184,120,84
ex 33
zvav 188,92,80
ra 240,224,45056
cv 3,6,7
mvav 188,124,84
ex 34
mvav 192,64,88
ra 244,224,46080
cv 4,32,33
mvav 192,96,92
ex 35
mvav 196,68,88
ex 36
mvav 196,100,92
ds 255,8,79,3072
mvav 200,72,88
ra 248,224,47104
cv 5,34,35
mvav 200,104,92
s_cmp_ge_i32 s47,s46
ex 37
mvav 204,76,88
s_cselect_b32 s78,s95,s46
ex 38
mvav 204,108,92
s_cselect_b32 s79,s95,268435456
ex 39
v_smfmac_f32_16x16x64_bf16 v[156:159],v[228:231],v[88:95],v248
ra 252,224,48128
mvav 208,80,88
s_add_u32 s79,s79,16384
ex 8
mvav 208,112,92
ex 9
mvav 212,84,88
ex 10
mvav 212,116,92
ex 11
mvav 216,88,88
cv 6,36,37
cv 7,38,39
mvav 216,120,92
ex 12
mvav 220,92,88
ex 13
mvav 220,124,92
s_waitcnt vmcnt(16) lgkmcnt(0)
s_barrier
cvaa 64,192,128,232
s_bitcmp0_b32 s82,s81
s_cbranch_scc1 L_loop0
s_bitcmp1_b32 s15,s81
s_cbranch_scc1 L_short0
.p2align 6
L_loop0:
mvaa 64,200,132
s_add_u32 s81,s81,1
ex 14
mvaa 64,208,136
s_add_u32 s44,s44,1024
ex 15
mvaa 64,216,140
cv 8,8,9
cv 9,10,11
cvaa 68,196,128,232
s_mov_b32 m0,s70
ex 40
mvaa 68,204,132
ds 249,8,78,0
mvaa 68,212,136
ex 41
mvaa 68,220,140
ex 42
cvaa 96,224,128,232
ra 64,225,0
cv 10,12,13
mvaa 96,232,132
ex 43
mvaa 96,240,136
ra 68,225,1024
cv 11,14,15
mvaa 96,248,140
ex 44
cvaa 100,228,128,232
ds 249,8,78,1024
mvaa 100,236,132
ex 45
mvaa 100,244,136
ra 72,225,2048
cv 12,40,41
mvaa 100,252,140
ex 46
cvaa 72,192,144,232
ra 76,225,3072
cv 13,42,43
mvaa 72,200,148
ex 47
mvaa 72,208,152
ex 16
mvaa 72,216,156
ds 249,8,78,2048
cvaa 76,196,144,232
ra 80,225,4096
cv 14,44,45
mvaa 76,204,148
ex 17
mvaa 76,212,152
ra 84,225,5120
cv 15,46,47
mvaa 76,220,156
ex 18
cvaa 104,224,144,232
ex 19
mvaa 104,232,148
ra 88,225,6144
cv 16,16,17
mvaa 104,240,152
ds 249,8,78,3072
mvaa 104,248,156
ex 20
cvaa 108,228,144,232
ra 92,225,7168
cv 17,18,19
mvaa 108,236,148
ex 21
mvaa 108,244,152
ex 22
mvaa 108,252,156
ra 96,225,8192
cv 18,20,21
cvaa 80,192,160,232
ex 23
mvaa 80,200,164
s_mov_b32 m0,s71
ex 48
mvaa 80,208,168
ds 249,8,79,0
mvaa 80,216,172
ra 100,225,9216
cv 19,22,23
cvaa 84,196,160,232
ex 49
mvaa 84,204,164
ra 104,225,10240
cv 20,48,49
mvaa 84,212,168
ex 50
mvaa 84,220,172
ex 51
cvaa 112,224,160,232
ra 108,225,11264
cv 21,50,51
mvaa 112,232,164
ds 249,8,79,1024
mvaa 112,240,168
ex 52
mvaa 112,248,172
ra 112,225,12288
cvaa 116,228,160,232
ex 53
mvaa 116,236,164
ex 54
mvaa 116,244,168
ra 116,225,13312
cv 22,52,53
mvaa 116,252,172
ex 55
cvaa 88,192,176,232
ds 249,8,79,2048
mvaa 88,200,180
ra 120,225,14336
cv 23,54,55
mvaa 88,208,184
ex 24
mvaa 88,216,188
ex 25
cvaa 92,196,176,232
ra 124,225,15360
cv 24,24,25
mvaa 92,204,180
ex 26
mvaa 92,212,184
ex 27
mvaa 92,220,188
ds 249,8,79,3072
cvaa 120,224,176,232
ex 28
mvaa 120,232,180
ex 29
mvaa 120,240,184
ex 30
mvaa 120,248,188
buffer_store_dwordx4 v[250:253],v244,s[8:11],s44 offen sc0 sc1
cvaa 124,228,176,232
ex 31
mvaa 124,236,180
ex 56
mvaa 124,244,184
ex 57
mvaa 124,252,188
s_waitcnt vmcnt(16) lgkmcnt(0)
s_barrier
maav 0,64,0
cv 25,26,27
cv 26,28,29
maav 0,96,4
s_mov_b32 m0,s72
ex 58
maav 4,68,0
ds 255,8,76,0
maav 4,100,4
ex 59
maav 8,72,0
ex 60
maav 8,104,4
ex 61
maav 12,76,0
s_cmp_lg_u32 s80,s81
cv 27,30,31
cv 28,56,57
maav 12,108,4
ex 62
v_smfmac_f32_16x16x64_bf16 v[144:147],v[228:231],v[0:7],v248
cv 29,58,59
maav 16,80,0
ex 63
maav 16,112,4
buffer_load_dword v247,v246,s[24:27],s49 offen
maav 20,84,0
cv 30,60,61
cv 31,62,63
maav 20,116,4
maav 24,88,0
s_cbranch_scc1 L_evb_r0
v_add_u32_e32 v224,s94,v224
s_lshl_b32 s86,s12,12
s_add_u32 s86,s86,131072
v_add_u32_e32 v236,s86,v244
ra 160,236,0
ra 164,236,1024
ra 168,236,2048
ra 172,236,3072
ra 176,236,16384
ra 180,236,17408
ra 184,236,18432
ra 188,236,19456
L_evb_r0:
maav 24,120,4
ra 192,224,0
maav 28,92,0
ds 255,8,76,1024
maav 28,124,4
maav 32,64,8
ra 196,224,1024
maav 32,96,12
s_add_u32 s49,s49,32768
maav 36,68,8
ra 200,224,2048
maav 36,100,12
maav 40,72,8
ds 255,8,76,2048
maav 40,104,12
maav 44,76,8
ra 204,224,3072
maav 44,108,12
v_smfmac_f32_16x16x64_bf16 v[148:151],v[228:231],v[8:15],v248
ra 208,224,4096
maav 48,80,8
maav 48,112,12
maav 52,84,8
ds 255,8,76,3072
maav 52,116,12
ra 212,224,5120
maav 56,88,8
ex 64
maav 56,120,12
ra 216,224,6144
maav 60,92,8
ex 65
maav 60,124,12
s_mov_b32 m0,s73
ex 66
mvav 160,64,16
ra 220,224,7168
cv 64,64,65
mvav 160,96,20
ds 255,8,77,0
mvav 164,68,16
ex 67
mvav 164,100,20
ra 224,224,8192
cv 65,66,67
mvav 168,72,16
ex 68
mvav 168,104,20
ra 228,224,9216
mvav 172,76,16
ds 255,8,77,1024
mvav 172,108,20
s_add_u32 s46,s46,16384
ex 69
v_smfmac_f32_16x16x64_bf16 v[152:155],v[228:231],v[16:23],v248
ra 232,224,10240
mvav 176,80,16
s_add_u32 s47,s47,32768
ex 70
mvav 176,112,20
s_add_u32 s95,s47,32768
ex 71
mvav 180,84,16
ra 236,224,11264
cv 66,68,69
mvav 180,116,20
ex 96
mvav 184,88,16
ds 255,8,77,2048
mvav 184,120,20
ex 97
mvav 188,92,16
ra 240,224,12288
cv 67,70,71
mvav 188,124,20
ex 98
mvav 192,64,24
ra 244,224,13312
cv 68,96,97
mvav 192,96,28
ex 99
mvav 196,68,24
ex 100
mvav 196,100,28
ds 255,8,77,3072
mvav 200,72,24
ra 248,224,14336
cv 69,98,99
mvav 200,104,28
s_cmp_ge_i32 s47,s46
ex 101
mvav 204,76,24
s_cselect_b32 s76,s95,s46
ex 102
mvav 204,108,28
s_cselect_b32 s77,s95,268435456
ex 103
v_smfmac_f32_16x16x64_bf16 v[156:159],v[228:231],v[24:31],v248
ra 252,224,15360
mvav 208,80,24
s_add_u32 s77,s77,16384
ex 72
mvav 208,112,28
ex 73
mvav 212,84,24
ex 74
mvav 212,116,28
ex 75
mvav 216,88,24
cv 70,100,101
cv 71,102,103
mvav 216,120,28
ex 76
mvav 220,92,24
ex 77
mvav 220,124,28
s_waitcnt vmcnt(16) lgkmcnt(0)
s_barrier
cvaa 0,192,128,232
mvaa 0,200,132
s_add_u32 s81,s81,1
ex 78
mvaa 0,208,136
s_add_u32 s44,s44,1024
ex 79
mvaa 0,216,140
cv 72,72,73
cv 73,74,75
cvaa 4,196,128,232
s_mov_b32 m0,s68
ex 104
mvaa 4,204,132
ds 249,8,76,0
mvaa 4,212,136
ex 105
mvaa 4,220,140
ex 106
cvaa 32,224,128,232
ra 64,225,32768
cv 74,76,77
mvaa 32,232,132
ex 107
mvaa 32,240,136
ra 68,225,33792
cv 75,78,79
mvaa 32,248,140
ex 108
cvaa 36,228,128,232
ds 249,8,76,1024
mvaa 36,236,132
ex 109
mvaa 36,244,136
ra 72,225,34816
cv 76,104,105
mvaa 36,252,140
ex 110
cvaa 8,192,144,232
ra 76,225,35840
cv 77,106,107
mvaa 8,200,148
ex 111
mvaa 8,208,152
ex 80
mvaa 8,216,156
ds 249,8,76,2048
cvaa 12,196,144,232
ra 80,225,36864
cv 78,108,109
mvaa 12,204,148
ex 81
mvaa 12,212,152
ra 84,225,37888
cv 79,110,111
mvaa 12,220,156
ex 82
cvaa 40,224,144,232
ex 83
mvaa 40,232,148
ra 88,225,38912
cv 80,80,81
mvaa 40,240,152
ds 249,8,76,3072
mvaa 40,248,156
ex 84
cvaa 44,228,144,232
ra 92,225,39936
cv 81,82,83
mvaa 44,236,148
ex 85
mvaa 44,244,152
ex 86
mvaa 44,252,156
ra 96,225,40960
cv 82,84,85
cvaa 16,192,160,232
ex 87
mvaa 16,200,164
s_mov_b32 m0,s69
ex 112
mvaa 16,208,168
ds 249,8,77,0
mvaa 16,216,172
ra 100,225,41984
cv 83,86,87
cvaa 20,196,160,232
ex 113
mvaa 20,204,164
ra 104,225,43008
cv 84,112,113
mvaa 20,212,168
ex 114
mvaa 20,220,172
ex 115
cvaa 48,224,160,232
ra 108,225,44032
cv 85,114,115
mvaa 48,232,164
ds 249,8,77,1024
mvaa 48,240,168
ex 116
mvaa 48,248,172
ra 112,225,45056
cvaa 52,228,160,232
ex 117
mvaa 52,236,164
ex 118
mvaa 52,244,168
ra 116,225,46080
cv 86,116,117
mvaa 52,252,172
ex 119
cvaa 24,192,176,232
ds 249,8,77,2048
mvaa 24,200,180
ra 120,225,47104
cv 87,118,119
mvaa 24,208,184
ex 88
mvaa 24,216,188
ex 89
cvaa 28,196,176,232
ra 124,225,48128
cv 88,88,89
mvaa 28,204,180
ex 90
mvaa 28,212,184
ex 91
mvaa 28,220,188
ds 249,8,77,3072
cvaa 56,224,176,232
ex 92
mvaa 56,232,180
ex 93
mvaa 56,240,184
ex 94
mvaa 56,248,188
buffer_store_dwordx4 v[250:253],v244,s[8:11],s44 offen sc0 sc1
cvaa 60,228,176,232
ex 95
mvaa 60,236,180
ex 120
mvaa 60,244,184
ex 121
mvaa 60,252,188
s_waitcnt vmcnt(16) lgkmcnt(0)
s_barrier
maav 0,64,64
cv 89,90,91
cv 90,92,93
maav 0,96,68
s_mov_b32 m0,s74
ex 122
maav 4,68,64
ds 255,8,78,0
maav 4,100,68
ex 123
maav 8,72,64
ex 124
maav 8,104,68
ex 125
maav 12,76,64
cv 91,94,95
cv 92,120,121
maav 12,108,68
ex 126
v_smfmac_f32_16x16x64_bf16 v[144:147],v[228:231],v[64:71],v248
cv 93,122,123
maav 16,80,64
ex 127
maav 16,112,68
buffer_load_dword v247,v245,s[36:39],s85 offen
maav 20,84,64
cv 94,124,125
cv 95,126,127
maav 20,116,68
maav 24,88,64
maav 24,120,68
ra 192,224,32768
maav 28,92,64
ds 255,8,78,1024
maav 28,124,68
maav 32,64,72
ra 196,224,33792
maav 32,96,76
s_add_u32 s85,s85,131072
maav 36,68,72
ra 200,224,34816
maav 36,100,76
maav 40,72,72
ds 255,8,78,2048
maav 40,104,76
maav 44,76,72
ra 204,224,35840
maav 44,108,76
v_smfmac_f32_16x16x64_bf16 v[148:151],v[228:231],v[72:79],v248
ra 208,224,36864
maav 48,80,72
maav 48,112,76
maav 52,84,72
ds 255,8,78,3072
maav 52,116,76
ra 212,224,37888
maav 56,88,72
ex 0
maav 56,120,76
ra 216,224,38912
maav 60,92,72
ex 1
maav 60,124,76
s_mov_b32 m0,s75
ex 2
mvav 160,64,80
ra 220,224,39936
cv 0,0,1
mvav 160,96,84
ds 255,8,79,0
mvav 164,68,80
ex 3
mvav 164,100,84
ra 224,224,40960
cv 1,2,3
mvav 168,72,80
ex 4
mvav 168,104,84
ra 228,224,41984
mvav 172,76,80
ds 255,8,79,1024
mvav 172,108,84
s_add_u32 s46,s46,16384
ex 5
v_smfmac_f32_16x16x64_bf16 v[152:155],v[228:231],v[80:87],v248
ra 232,224,43008
mvav 176,80,80
s_add_u32 s47,s47,32768
ex 6
mvav 176,112,84
s_add_u32 s95,s47,32768
ex 7
mvav 180,84,80
ra 236,224,44032
cv 2,4,5
mvav 180,116,84
ex 32
mvav 184,88,80
ds 255,8,79,2048
mvav 184,120,84
ex 33
mvav 188,92,80
ra 240,224,45056
cv 3,6,7
mvav 188,124,84
ex 34
mvav 192,64,88
ra 244,224,46080
cv 4,32,33
mvav 192,96,92
ex 35
mvav 196,68,88
ex 36
mvav 196,100,92
ds 255,8,79,3072
mvav 200,72,88
ra 248,224,47104
cv 5,34,35
mvav 200,104,92
s_cmp_ge_i32 s47,s46
ex 37
mvav 204,76,88
s_cselect_b32 s78,s95,s46
ex 38
mvav 204,108,92
s_cselect_b32 s79,s95,268435456
ex 39
v_smfmac_f32_16x16x64_bf16 v[156:159],v[228:231],v[88:95],v248
ra 252,224,48128
mvav 208,80,88
s_add_u32 s79,s79,16384
ex 8
mvav 208,112,92
ex 9
mvav 212,84,88
ex 10
mvav 212,116,92
ex 11
mvav 216,88,88
cv 6,36,37
cv 7,38,39
mvav 216,120,92
ex 12
mvav 220,92,88
ex 13
mvav 220,124,92
s_waitcnt vmcnt(16) lgkmcnt(0)
s_barrier
cvaa 64,192,128,232
s_bitcmp0_b32 s82,s81
s_cbranch_scc1 L_loop0
s_bitcmp0_b32 s15,s81
s_cbranch_scc1 L_eva_r0
L_short0:
v_add_f32_e32 v226,v152,v153
v_add_f32_e32 v227,v156,v157
v_subrev_u32_e32 v236,0x23800000,v226
v_subrev_u32_e32 v237,0x23800000,v227
v_max_u32_e32 v236,v236,v237
v_cmp_lt_u32_e64 s[40:41],s48,v236
v_rcp_f32_e32 v226,v226
v_rcp_f32_e32 v227,v227
s_add_u32 s86,s13,4096
v_mov_b32_e32 v236,0
v_mov_b32_e32 v237,0
v_mov_b32_e32 v238,0
v_mov_b32_e32 v239,0
v_mov_b64_e32 v[152:153],0
v_mov_b64_e32 v[156:157],0
v_mul_f32_e32 v226,v242,v226
v_mul_f32_e32 v227,v243,v227
v_add_u32_e32 v225,s94,v225
v_pk_mul_f32 v[160:161],v[160:161],v[226:227] op_sel:[0,0] op_sel_hi:[1,0]
v_pk_mul_f32 v[162:163],v[162:163],v[226:227] op_sel:[0,0] op_sel_hi:[1,0]
v_pk_mul_f32 v[164:165],v[164:165],v[226:227] op_sel:[0,0] op_sel_hi:[1,0]
v_pk_mul_f32 v[166:167],v[166:167],v[226:227] op_sel:[0,0] op_sel_hi:[1,0]
cv 160,160,161
cv 161,162,163
cv 162,164,165
cv 163,166,167
buffer_store_dwordx4 v[160:163],v244,s[8:11],s13 offen offset:0 sc0 sc1
v_pk_mul_f32 v[168:169],v[168:169],v[226:227] op_sel:[0,0] op_sel_hi:[1,0]
v_pk_mul_f32 v[170:171],v[170:171],v[226:227] op_sel:[0,0] op_sel_hi:[1,0]
v_pk_mul_f32 v[172:173],v[172:173],v[226:227] op_sel:[0,0] op_sel_hi:[1,0]
v_pk_mul_f32 v[174:175],v[174:175],v[226:227] op_sel:[0,0] op_sel_hi:[1,0]
cv 168,168,169
cv 169,170,171
cv 170,172,173
cv 171,174,175
buffer_store_dwordx4 v[168:171],v244,s[8:11],s13 offen offset:2048 sc0 sc1
v_pk_mul_f32 v[176:177],v[176:177],v[226:227] op_sel:[0,0] op_sel_hi:[1,0]
v_pk_mul_f32 v[178:179],v[178:179],v[226:227] op_sel:[0,0] op_sel_hi:[1,0]
v_pk_mul_f32 v[180:181],v[180:181],v[226:227] op_sel:[0,0] op_sel_hi:[1,0]
v_pk_mul_f32 v[182:183],v[182:183],v[226:227] op_sel:[0,0] op_sel_hi:[1,0]
cv 176,176,177
cv 177,178,179
cv 178,180,181
v_mfma_f32_32x32x16_bf16 v[160:175],v[236:239],v[236:239],0
cv 179,182,183
buffer_store_dwordx4 v[176:179],v244,s[8:11],s86 offen offset:0 sc0 sc1
v_pk_mul_f32 v[184:185],v[184:185],v[226:227] op_sel:[0,0] op_sel_hi:[1,0]
v_pk_mul_f32 v[186:187],v[186:187],v[226:227] op_sel:[0,0] op_sel_hi:[1,0]
v_pk_mul_f32 v[188:189],v[188:189],v[226:227] op_sel:[0,0] op_sel_hi:[1,0]
v_pk_mul_f32 v[190:191],v[190:191],v[226:227] op_sel:[0,0] op_sel_hi:[1,0]
cv 184,184,185
cv 185,186,187
cv 186,188,189
cv 187,190,191
buffer_store_dwordx4 v[184:187],v244,s[8:11],s86 offen offset:2048 sc0 sc1
v_pk_mul_f32 v[192:193],v[192:193],v[226:227] op_sel:[0,1] op_sel_hi:[1,1]
v_pk_mul_f32 v[194:195],v[194:195],v[226:227] op_sel:[0,1] op_sel_hi:[1,1]
v_pk_mul_f32 v[196:197],v[196:197],v[226:227] op_sel:[0,1] op_sel_hi:[1,1]
v_pk_mul_f32 v[198:199],v[198:199],v[226:227] op_sel:[0,1] op_sel_hi:[1,1]
cv 192,192,193
cv 193,194,195
cv 194,196,197
v_mfma_f32_32x32x16_bf16 v[176:191],v[236:239],v[236:239],0
cv 195,198,199
buffer_store_dwordx4 v[192:195],v244,s[8:11],s13 offen offset:1024 sc0 sc1
v_pk_mul_f32 v[200:201],v[200:201],v[226:227] op_sel:[0,1] op_sel_hi:[1,1]
v_pk_mul_f32 v[202:203],v[202:203],v[226:227] op_sel:[0,1] op_sel_hi:[1,1]
v_pk_mul_f32 v[204:205],v[204:205],v[226:227] op_sel:[0,1] op_sel_hi:[1,1]
v_pk_mul_f32 v[206:207],v[206:207],v[226:227] op_sel:[0,1] op_sel_hi:[1,1]
cv 200,200,201
cv 201,202,203
cv 202,204,205
cv 203,206,207
buffer_store_dwordx4 v[200:203],v244,s[8:11],s13 offen offset:3072 sc0 sc1
v_pk_mul_f32 v[208:209],v[208:209],v[226:227] op_sel:[0,1] op_sel_hi:[1,1]
v_pk_mul_f32 v[210:211],v[210:211],v[226:227] op_sel:[0,1] op_sel_hi:[1,1]
v_pk_mul_f32 v[212:213],v[212:213],v[226:227] op_sel:[0,1] op_sel_hi:[1,1]
v_pk_mul_f32 v[214:215],v[214:215],v[226:227] op_sel:[0,1] op_sel_hi:[1,1]
cv 208,208,209
cv 209,210,211
cv 210,212,213
v_mfma_f32_32x32x16_bf16 v[192:207],v[236:239],v[236:239],0
cv 211,214,215
buffer_store_dwordx4 v[208:211],v244,s[8:11],s86 offen offset:1024 sc0 sc1
v_pk_mul_f32 v[216:217],v[216:217],v[226:227] op_sel:[0,1] op_sel_hi:[1,1]
v_pk_mul_f32 v[218:219],v[218:219],v[226:227] op_sel:[0,1] op_sel_hi:[1,1]
v_pk_mul_f32 v[220:221],v[220:221],v[226:227] op_sel:[0,1] op_sel_hi:[1,1]
v_pk_mul_f32 v[222:223],v[222:223],v[226:227] op_sel:[0,1] op_sel_hi:[1,1]
cv 216,216,217
cv 217,218,219
cv 218,220,221
cv 219,222,223
buffer_store_dwordx4 v[216:219],v244,s[8:11],s86 offen offset:3072 sc0 sc1
s_nop 1
v_mfma_f32_32x32x16_bf16 v[208:223],v[236:239],v[236:239],0
L_eva_r0:
s_cmp_lg_u32 s81,14
s_cbranch_scc1 L_loop0
s_add_u32 s81,s81,1
s_bitcmp1_b32 170,s20
s_cbranch_scc1 L_odd
s_branch L_last
L_k_ec1:
buffer_store_dwordx4 a[252:255],v244,s[8:11],s35 offen offset:1024 sc0 sc1
s_branch L_k_ecr1
L_q_ack:
s_mov_b32 s86,65536
L_q_ack2:
s_sleep 8
buffer_load_dword v246,off,s[8:11],s35 offset:1024 sc1
)"
R"(s_waitcnt vmcnt(0)
v_readfirstlane_b32 s87,v246
s_nop 0
s_cmp_eq_u32 s87,s32
s_cbranch_scc1 L_q_acked
s_sub_u32 s86,s86,1
s_cbranch_scc0 L_q_ack2
s_branch L_q_acked
L_q_spin:
s_mov_b32 s86,65536
L_q_spin1:
s_sleep 8
buffer_load_dword v248,v250,s[8:11],0 offen sc1
s_or_b32 s87,%1,s12
s_cbranch_scc1 L_q_spin2
buffer_load_dwordx4 a[252:255],v244,s[8:11],s35 offen sc1
s_waitcnt vmcnt(0)
buffer_store_dwordx4 a[252:255],v244,s[8:11],s35 offen offset:1024 sc0 sc1
L_q_spin2:
s_waitcnt vmcnt(0)
v_and_b32_e32 v248,v242,v248
v_cmp_ne_u32_e64 s[64:65],v243,v248
s_nop 1
s_cmp_eq_u64 s[64:65],0
s_cbranch_scc1 L_q_ok
s_sub_u32 s86,s86,1
s_cbranch_scc0 L_q_spin1
s_branch L_q_ok
L_bx:
s_mov_b32 s87,65536
L_bx1:
s_sleep 2
buffer_load_dwordx4 v[176:179],v244,s[8:11],s98 offen sc1
buffer_load_dwordx2 v[168:169],v253,s[8:11],s98 offen offset:1024 sc1
buffer_load_dwordx2 v[172:173],v253,s[8:11],s98 offen offset:1536 sc1
s_waitcnt vmcnt(0)
v_max3_u32 v180,v176,v177,v178
v_max3_u32 v180,v180,v179,v168
v_max3_u32 v180,v180,v169,v172
v_max_u32_e32 v180,v180,v173
v_cmp_eq_u32_e64 s[40:41],-1,v180
s_nop 1
s_cmp_eq_u64 s[40:41],0
s_cbranch_scc1 L_bxr
s_sub_u32 s87,s87,1
s_cbranch_scc0 L_bx1
s_branch L_bxr
L_k_ev:
L_v_ev:
buffer_load_dwordx4 a[252:255],v244,s[8:11],s35 offen sc1
s_branch L_k_evr
.fill 194,4,0xbf800000
L_cw1:
s_mov_b64 exec,-1
s_waitcnt lgkmcnt(0)
s_barrier
cvaa 0,192,128,232
mvaa 0,200,132
s_add_u32 s81,s81,1
ex 78
mvaa 0,208,136
s_add_u32 s44,s44,1024
ex 79
mvaa 0,216,140
cv 72,72,73
cv 73,74,75
cvaa 4,196,128,232
ex 104
mvaa 4,204,132
s_mov_b32 m0,s68
ex 105
mvaa 4,212,136
ds 249,8,76,0
mvaa 4,220,140
ex 106
cvaa 32,224,128,232
ra 64,225,32768
cv 74,76,77
mvaa 32,232,132
ex 107
mvaa 32,240,136
ra 68,225,33792
cv 75,78,79
mvaa 32,248,140
ex 108
cvaa 36,228,128,232
ex 109
mvaa 36,236,132
ds 249,8,76,1024
mvaa 36,244,136
ra 72,225,34816
cv 76,104,105
mvaa 36,252,140
ex 110
cvaa 8,192,144,232
ra 76,225,35840
cv 77,106,107
mvaa 8,200,148
ex 111
mvaa 8,208,152
ex 80
mvaa 8,216,156
ra 80,225,36864
cv 78,108,109
cvaa 12,196,144,232
ds 249,8,76,2048
mvaa 12,204,148
ex 81
mvaa 12,212,152
ra 84,225,37888
cv 79,110,111
mvaa 12,220,156
ex 82
cvaa 40,224,144,232
ex 83
mvaa 40,232,148
ra 88,225,38912
cv 80,80,81
mvaa 40,240,152
ex 84
mvaa 40,248,156
ds 249,8,76,3072
cvaa 44,228,144,232
ra 92,225,39936
cv 81,82,83
mvaa 44,236,148
ex 85
mvaa 44,244,152
ex 86
mvaa 44,252,156
ra 96,225,40960
cv 82,84,85
cvaa 16,192,160,232
ex 87
mvaa 16,200,164
s_mov_b32 m0,s69
ex 112
mvaa 16,208,168
ra 100,225,41984
cv 83,86,87
mvaa 16,216,172
ds 249,8,77,0
cvaa 20,196,160,232
ex 113
mvaa 20,204,164
ra 104,225,43008
cv 84,112,113
mvaa 20,212,168
ex 114
mvaa 20,220,172
ex 115
cvaa 48,224,160,232
ra 108,225,44032
cv 85,114,115
mvaa 48,232,164
ex 116
mvaa 48,240,168
ds 249,8,77,1024
mvaa 48,248,172
ra 112,225,45056
cvaa 52,228,160,232
ex 117
mvaa 52,236,164
ex 118
mvaa 52,244,168
ra 116,225,46080
cv 86,116,117
mvaa 52,252,172
ex 119
cvaa 24,192,176,232
ra 120,225,47104
cv 87,118,119
mvaa 24,200,180
ds 249,8,77,2048
mvaa 24,208,184
ex 88
mvaa 24,216,188
ex 89
cvaa 28,196,176,232
ra 124,225,48128
cv 88,88,89
mvaa 28,204,180
ex 90
mvaa 28,212,184
ex 91
mvaa 28,220,188
ex 92
cvaa 56,224,176,232
ds 249,8,77,3072
mvaa 56,232,180
ex 93
mvaa 56,240,184
ex 94
mvaa 56,248,188
ex 95
cvaa 60,228,176,232
mvaa 60,236,180
ex 120
mvaa 60,244,184
ex 121
mvaa 60,252,188
s_waitcnt vmcnt(8) lgkmcnt(0)
s_barrier
maav 0,64,64
cv 89,90,91
cv 90,92,93
maav 0,96,68
ex 122
maav 4,68,64
s_mov_b32 m0,s74
ex 123
maav 4,100,68
ds 255,8,78,0
maav 8,72,64
ex 124
maav 8,104,68
ex 125
maav 12,76,64
cv 91,94,95
cv 92,120,121
maav 12,108,68
ex 126
v_smfmac_f32_16x16x64_bf16 v[144:147],v[228:231],v[64:71],v248
cv 93,122,123
maav 16,80,64
ex 127
maav 16,112,68
cv 94,124,125
cv 95,126,127
maav 20,84,64
buffer_load_dword v247,v245,s[36:39],s85 offen
maav 20,116,68
maav 24,88,64
maav 24,120,68
ra 192,224,32768
maav 28,92,64
maav 28,124,68
ds 255,8,78,1024
maav 32,64,72
ra 196,224,33792
maav 32,96,76
s_add_u32 s85,s85,131072
maav 36,68,72
ra 200,224,34816
maav 36,100,76
maav 40,72,72
maav 40,104,76
ds 255,8,78,2048
maav 44,76,72
ra 204,224,35840
maav 44,108,76
v_smfmac_f32_16x16x64_bf16 v[148:151],v[228:231],v[72:79],v248
ra 208,224,36864
maav 48,80,72
maav 48,112,76
maav 52,84,72
ra 212,224,37888
maav 52,116,76
ds 255,8,78,3072
maav 56,88,72
ex 0
maav 56,120,76
ra 216,224,38912
maav 60,92,72
ex 1
maav 60,124,76
ex 2
zvav 160,64,80
ra 220,224,39936
cv 0,0,1
mvav 160,96,84
s_mov_b32 m0,s75
ex 3
zvav 164,68,80
ds 255,8,79,0
mvav 164,100,84
ra 224,224,40960
cv 1,2,3
zvav 168,72,80
ex 4
mvav 168,104,84
ra 228,224,41984
zvav 172,76,80
s_add_u32 s46,s46,16384
ex 5
mvav 172,108,84
ds 255,8,79,1024
v_smfmac_f32_16x16x64_bf16 v[152:155],v[228:231],v[80:87],v248
ra 232,224,43008
zvav 176,80,80
s_add_u32 s47,s47,32768
ex 6
mvav 176,112,84
s_add_u32 s95,s47,32768
ex 7
zvav 180,84,80
ra 236,224,44032
cv 2,4,5
mvav 180,116,84
ex 32
zvav 184,88,80
ex 33
mvav 184,120,84
ds 255,8,79,2048
zvav 188,92,80
ra 240,224,45056
cv 3,6,7
mvav 188,124,84
ex 34
mvav 192,64,88
ra 244,224,46080
cv 4,32,33
mvav 192,96,92
ex 35
mvav 196,68,88
ex 36
mvav 196,100,92
ra 248,224,47104
cv 5,34,35
mvav 200,72,88
ds 255,8,79,3072
mvav 200,104,92
s_cmp_ge_i32 s47,s46
ex 37
mvav 204,76,88
s_cselect_b32 s78,s95,s46
ex 38
mvav 204,108,92
s_cselect_b32 s79,s95,268435456
ex 39
v_smfmac_f32_16x16x64_bf16 v[156:159],v[228:231],v[88:95],v248
ra 252,224,48128
mvav 208,80,88
s_add_u32 s79,s79,16384
ex 8
mvav 208,112,92
ex 9
mvav 212,84,88
ex 10
mvav 212,116,92
ex 11
mvav 216,88,88
cv 6,36,37
cv 7,38,39
mvav 216,120,92
ex 12
mvav 220,92,88
ex 13
mvav 220,124,92
s_waitcnt vmcnt(16) lgkmcnt(0)
s_barrier
cvaa 64,192,128,232
s_bitcmp0_b32 s82,s81
s_cbranch_scc1 L_loop1
s_bitcmp1_b32 s15,s81
s_cbranch_scc1 L_short1
.p2align 6
L_loop1:
mvaa 64,200,132
s_add_u32 s81,s81,1
ex 14
mvaa 64,208,136
s_add_u32 s44,s44,1024
ex 15
mvaa 64,216,140
cv 8,8,9
cv 9,10,11
cvaa 68,196,128,232
ex 40
mvaa 68,204,132
s_mov_b32 m0,s70
ex 41
mvaa 68,212,136
ds 249,8,78,0
mvaa 68,220,140
ex 42
cvaa 96,224,128,232
ra 64,225,0
cv 10,12,13
mvaa 96,232,132
ex 43
mvaa 96,240,136
ra 68,225,1024
cv 11,14,15
mvaa 96,248,140
ex 44
cvaa 100,228,128,232
ex 45
mvaa 100,236,132
ds 249,8,78,1024
mvaa 100,244,136
ra 72,225,2048
cv 12,40,41
mvaa 100,252,140
ex 46
cvaa 72,192,144,232
ra 76,225,3072
cv 13,42,43
mvaa 72,200,148
ex 47
mvaa 72,208,152
ex 16
mvaa 72,216,156
ra 80,225,4096
cv 14,44,45
cvaa 76,196,144,232
ds 249,8,78,2048
mvaa 76,204,148
ex 17
mvaa 76,212,152
ra 84,225,5120
cv 15,46,47
mvaa 76,220,156
ex 18
cvaa 104,224,144,232
ex 19
mvaa 104,232,148
ra 88,225,6144
cv 16,16,17
mvaa 104,240,152
ex 20
mvaa 104,248,156
ds 249,8,78,3072
cvaa 108,228,144,232
ra 92,225,7168
cv 17,18,19
mvaa 108,236,148
ex 21
mvaa 108,244,152
ex 22
mvaa 108,252,156
ra 96,225,8192
cv 18,20,21
cvaa 80,192,160,232
ex 23
mvaa 80,200,164
s_mov_b32 m0,s71
ex 48
mvaa 80,208,168
ra 100,225,9216
cv 19,22,23
mvaa 80,216,172
ds 249,8,79,0
cvaa 84,196,160,232
ex 49
mvaa 84,204,164
ra 104,225,10240
cv 20,48,49
mvaa 84,212,168
ex 50
mvaa 84,220,172
ex 51
cvaa 112,224,160,232
ra 108,225,11264
cv 21,50,51
mvaa 112,232,164
ex 52
mvaa 112,240,168
ds 249,8,79,1024
mvaa 112,248,172
ra 112,225,12288
cvaa 116,228,160,232
ex 53
mvaa 116,236,164
ex 54
mvaa 116,244,168
ra 116,225,13312
cv 22,52,53
mvaa 116,252,172
ex 55
cvaa 88,192,176,232
ra 120,225,14336
cv 23,54,55
mvaa 88,200,180
ds 249,8,79,2048
mvaa 88,208,184
ex 24
mvaa 88,216,188
ex 25
cvaa 92,196,176,232
ra 124,225,15360
cv 24,24,25
mvaa 92,204,180
ex 26
mvaa 92,212,184
ex 27
mvaa 92,220,188
ex 28
cvaa 120,224,176,232
ds 249,8,79,3072
mvaa 120,232,180
ex 29
mvaa 120,240,184
ex 30
mvaa 120,248,188
ex 31
cvaa 124,228,176,232
buffer_store_dwordx4 v[250:253],v244,s[8:11],s44 offen sc0 sc1
mvaa 124,236,180
ex 56
mvaa 124,244,184
ex 57
mvaa 124,252,188
s_waitcnt vmcnt(16) lgkmcnt(0)
s_barrier
maav 0,64,0
cv 25,26,27
cv 26,28,29
maav 0,96,4
ex 58
maav 4,68,0
s_mov_b32 m0,s72
ex 59
maav 4,100,4
ds 255,8,76,0
maav 8,72,0
ex 60
maav 8,104,4
ex 61
maav 12,76,0
s_cmp_lg_u32 s80,s81
cv 27,30,31
cv 28,56,57
maav 12,108,4
ex 62
v_smfmac_f32_16x16x64_bf16 v[144:147],v[228:231],v[0:7],v248
cv 29,58,59
maav 16,80,0
ex 63
maav 16,112,4
cv 30,60,61
cv 31,62,63
maav 20,84,0
buffer_load_dword v247,v246,s[24:27],s49 offen
maav 20,116,4
maav 24,88,0
s_cbranch_scc1 L_evb_r1
v_add_u32_e32 v224,s94,v224
s_lshl_b32 s86,s12,12
s_add_u32 s86,s86,131072
v_add_u32_e32 v236,s86,v244
ra 160,236,0
ra 164,236,1024
ra 168,236,2048
ra 172,236,3072
ra 176,236,16384
ra 180,236,17408
ra 184,236,18432
ra 188,236,19456
L_evb_r1:
maav 24,120,4
ra 192,224,0
maav 28,92,0
maav 28,124,4
ds 255,8,76,1024
maav 32,64,8
ra 196,224,1024
maav 32,96,12
s_add_u32 s49,s49,32768
maav 36,68,8
ra 200,224,2048
maav 36,100,12
maav 40,72,8
maav 40,104,12
ds 255,8,76,2048
maav 44,76,8
ra 204,224,3072
maav 44,108,12
v_smfmac_f32_16x16x64_bf16 v[148:151],v[228:231],v[8:15],v248
ra 208,224,4096
maav 48,80,8
maav 48,112,12
maav 52,84,8
ra 212,224,5120
maav 52,116,12
ds 255,8,76,3072
maav 56,88,8
ex 64
maav 56,120,12
ra 216,224,6144
maav 60,92,8
ex 65
maav 60,124,12
ex 66
mvav 160,64,16
ra 220,224,7168
cv 64,64,65
mvav 160,96,20
s_mov_b32 m0,s73
ex 67
mvav 164,68,16
ds 255,8,77,0
mvav 164,100,20
ra 224,224,8192
cv 65,66,67
mvav 168,72,16
ex 68
mvav 168,104,20
ra 228,224,9216
mvav 172,76,16
s_add_u32 s46,s46,16384
ex 69
mvav 172,108,20
ds 255,8,77,1024
v_smfmac_f32_16x16x64_bf16 v[152:155],v[228:231],v[16:23],v248
ra 232,224,10240
mvav 176,80,16
s_add_u32 s47,s47,32768
ex 70
mvav 176,112,20
s_add_u32 s95,s47,32768
ex 71
mvav 180,84,16
ra 236,224,11264
cv 66,68,69
mvav 180,116,20
ex 96
mvav 184,88,16
ex 97
mvav 184,120,20
ds 255,8,77,2048
mvav 188,92,16
ra 240,224,12288
cv 67,70,71
mvav 188,124,20
ex 98
mvav 192,64,24
ra 244,224,13312
cv 68,96,97
mvav 192,96,28
ex 99
mvav 196,68,24
ex 100
mvav 196,100,28
ra 248,224,14336
cv 69,98,99
mvav 200,72,24
ds 255,8,77,3072
mvav 200,104,28
s_cmp_ge_i32 s47,s46
ex 101
mvav 204,76,24
s_cselect_b32 s76,s95,s46
ex 102
mvav 204,108,28
s_cselect_b32 s77,s95,268435456
ex 103
v_smfmac_f32_16x16x64_bf16 v[156:159],v[228:231],v[24:31],v248
ra 252,224,15360
mvav 208,80,24
s_add_u32 s77,s77,16384
ex 72
mvav 208,112,28
ex 73
mvav 212,84,24
ex 74
mvav 212,116,28
ex 75
mvav 216,88,24
cv 70,100,101
cv 71,102,103
mvav 216,120,28
ex 76
mvav 220,92,24
ex 77
mvav 220,124,28
s_waitcnt vmcnt(16) lgkmcnt(0)
s_barrier
cvaa 0,192,128,232
mvaa 0,200,132
s_add_u32 s81,s81,1
ex 78
mvaa 0,208,136
s_add_u32 s44,s44,1024
ex 79
mvaa 0,216,140
cv 72,72,73
cv 73,74,75
cvaa 4,196,128,232
ex 104
mvaa 4,204,132
s_mov_b32 m0,s68
ex 105
mvaa 4,212,136
ds 249,8,76,0
mvaa 4,220,140
ex 106
cvaa 32,224,128,232
ra 64,225,32768
cv 74,76,77
mvaa 32,232,132
ex 107
mvaa 32,240,136
ra 68,225,33792
cv 75,78,79
mvaa 32,248,140
ex 108
cvaa 36,228,128,232
ex 109
mvaa 36,236,132
ds 249,8,76,1024
mvaa 36,244,136
ra 72,225,34816
cv 76,104,105
mvaa 36,252,140
ex 110
cvaa 8,192,144,232
ra 76,225,35840
cv 77,106,107
mvaa 8,200,148
ex 111
mvaa 8,208,152
ex 80
mvaa 8,216,156
ra 80,225,36864
cv 78,108,109
cvaa 12,196,144,232
ds 249,8,76,2048
mvaa 12,204,148
ex 81
mvaa 12,212,152
ra 84,225,37888
cv 79,110,111
mvaa 12,220,156
ex 82
cvaa 40,224,144,232
ex 83
mvaa 40,232,148
ra 88,225,38912
cv 80,80,81
mvaa 40,240,152
ex 84
mvaa 40,248,156
ds 249,8,76,3072
cvaa 44,228,144,232
ra 92,225,39936
cv 81,82,83
mvaa 44,236,148
ex 85
mvaa 44,244,152
ex 86
mvaa 44,252,156
ra 96,225,40960
cv 82,84,85
cvaa 16,192,160,232
ex 87
mvaa 16,200,164
s_mov_b32 m0,s69
ex 112
mvaa 16,208,168
ra 100,225,41984
cv 83,86,87
mvaa 16,216,172
ds 249,8,77,0
cvaa 20,196,160,232
ex 113
mvaa 20,204,164
ra 104,225,43008
cv 84,112,113
mvaa 20,212,168
ex 114
mvaa 20,220,172
ex 115
cvaa 48,224,160,232
ra 108,225,44032
cv 85,114,115
mvaa 48,232,164
ex 116
mvaa 48,240,168
ds 249,8,77,1024
mvaa 48,248,172
ra 112,225,45056
cvaa 52,228,160,232
ex 117
mvaa 52,236,164
ex 118
mvaa 52,244,168
ra 116,225,46080
cv 86,116,117
mvaa 52,252,172
ex 119
cvaa 24,192,176,232
ra 120,225,47104
cv 87,118,119
mvaa 24,200,180
ds 249,8,77,2048
mvaa 24,208,184
ex 88
mvaa 24,216,188
ex 89
cvaa 28,196,176,232
ra 124,225,48128
cv 88,88,89
mvaa 28,204,180
ex 90
mvaa 28,212,184
ex 91
mvaa 28,220,188
ex 92
cvaa 56,224,176,232
ds 249,8,77,3072
mvaa 56,232,180
ex 93
mvaa 56,240,184
ex 94
mvaa 56,248,188
ex 95
cvaa 60,228,176,232
buffer_store_dwordx4 v[250:253],v244,s[8:11],s44 offen sc0 sc1
mvaa 60,236,180
ex 120
mvaa 60,244,184
ex 121
mvaa 60,252,188
s_waitcnt vmcnt(16) lgkmcnt(0)
s_barrier
maav 0,64,64
cv 89,90,91
cv 90,92,93
maav 0,96,68
ex 122
maav 4,68,64
s_mov_b32 m0,s74
ex 123
maav 4,100,68
ds 255,8,78,0
maav 8,72,64
ex 124
maav 8,104,68
ex 125
maav 12,76,64
cv 91,94,95
cv 92,120,121
maav 12,108,68
ex 126
v_smfmac_f32_16x16x64_bf16 v[144:147],v[228:231],v[64:71],v248
cv 93,122,123
maav 16,80,64
ex 127
maav 16,112,68
cv 94,124,125
cv 95,126,127
maav 20,84,64
buffer_load_dword v247,v245,s[36:39],s85 offen
maav 20,116,68
maav 24,88,64
maav 24,120,68
ra 192,224,32768
maav 28,92,64
maav 28,124,68
ds 255,8,78,1024
maav 32,64,72
ra 196,224,33792
maav 32,96,76
s_add_u32 s85,s85,131072
maav 36,68,72
ra 200,224,34816
maav 36,100,76
maav 40,72,72
maav 40,104,76
ds 255,8,78,2048
maav 44,76,72
ra 204,224,35840
maav 44,108,76
v_smfmac_f32_16x16x64_bf16 v[148:151],v[228:231],v[72:79],v248
ra 208,224,36864
maav 48,80,72
maav 48,112,76
maav 52,84,72
ra 212,224,37888
maav 52,116,76
ds 255,8,78,3072
maav 56,88,72
ex 0
maav 56,120,76
ra 216,224,38912
maav 60,92,72
ex 1
maav 60,124,76
ex 2
mvav 160,64,80
ra 220,224,39936
cv 0,0,1
mvav 160,96,84
s_mov_b32 m0,s75
ex 3
mvav 164,68,80
ds 255,8,79,0
mvav 164,100,84
ra 224,224,40960
cv 1,2,3
mvav 168,72,80
ex 4
mvav 168,104,84
ra 228,224,41984
mvav 172,76,80
s_add_u32 s46,s46,16384
ex 5
mvav 172,108,84
ds 255,8,79,1024
v_smfmac_f32_16x16x64_bf16 v[152:155],v[228:231],v[80:87],v248
ra 232,224,43008
mvav 176,80,80
s_add_u32 s47,s47,32768
ex 6
mvav 176,112,84
s_add_u32 s95,s47,32768
ex 7
mvav 180,84,80
ra 236,224,44032
cv 2,4,5
mvav 180,116,84
ex 32
mvav 184,88,80
ex 33
mvav 184,120,84
ds 255,8,79,2048
mvav 188,92,80
ra 240,224,45056
cv 3,6,7
mvav 188,124,84
ex 34
mvav 192,64,88
ra 244,224,46080
cv 4,32,33
mvav 192,96,92
ex 35
mvav 196,68,88
ex 36
mvav 196,100,92
ra 248,224,47104
cv 5,34,35
mvav 200,72,88
ds 255,8,79,3072
mvav 200,104,92
s_cmp_ge_i32 s47,s46
ex 37
mvav 204,76,88
s_cselect_b32 s78,s95,s46
ex 38
mvav 204,108,92
s_cselect_b32 s79,s95,268435456
ex 39
v_smfmac_f32_16x16x64_bf16 v[156:159],v[228:231],v[88:95],v248
ra 252,224,48128
mvav 208,80,88
s_add_u32 s79,s79,16384
ex 8
mvav 208,112,92
ex 9
mvav 212,84,88
ex 10
mvav 212,116,92
ex 11
mvav 216,88,88
cv 6,36,37
cv 7,38,39
mvav 216,120,92
ex 12
mvav 220,92,88
ex 13
mvav 220,124,92
s_waitcnt vmcnt(16) lgkmcnt(0)
s_barrier
cvaa 64,192,128,232
s_bitcmp0_b32 s82,s81
s_cbranch_scc1 L_loop1
s_bitcmp0_b32 s15,s81
s_cbranch_scc1 L_eva_r1
L_short1:
v_add_f32_e32 v226,v152,v153
v_add_f32_e32 v227,v156,v157
v_subrev_u32_e32 v236,0x23800000,v226
v_subrev_u32_e32 v237,0x23800000,v227
v_max_u32_e32 v236,v236,v237
v_cmp_lt_u32_e64 s[40:41],s48,v236
v_rcp_f32_e32 v226,v226
v_rcp_f32_e32 v227,v227
s_add_u32 s86,s13,4096
v_mov_b32_e32 v236,0
v_mov_b32_e32 v237,0
v_mov_b32_e32 v238,0
v_mov_b32_e32 v239,0
v_mov_b64_e32 v[152:153],0
v_mov_b64_e32 v[156:157],0
v_mul_f32_e32 v226,v242,v226
v_mul_f32_e32 v227,v243,v227
v_add_u32_e32 v225,s94,v225
v_pk_mul_f32 v[160:161],v[160:161],v[226:227] op_sel:[0,0] op_sel_hi:[1,0]
v_pk_mul_f32 v[162:163],v[162:163],v[226:227] op_sel:[0,0] op_sel_hi:[1,0]
v_pk_mul_f32 v[164:165],v[164:165],v[226:227] op_sel:[0,0] op_sel_hi:[1,0]
v_pk_mul_f32 v[166:167],v[166:167],v[226:227] op_sel:[0,0] op_sel_hi:[1,0]
cv 160,160,161
cv 161,162,163
cv 162,164,165
cv 163,166,167
buffer_store_dwordx4 v[160:163],v244,s[8:11],s13 offen offset:0 sc0 sc1
v_pk_mul_f32 v[168:169],v[168:169],v[226:227] op_sel:[0,0] op_sel_hi:[1,0]
v_pk_mul_f32 v[170:171],v[170:171],v[226:227] op_sel:[0,0] op_sel_hi:[1,0]
v_pk_mul_f32 v[172:173],v[172:173],v[226:227] op_sel:[0,0] op_sel_hi:[1,0]
)"
R"(v_pk_mul_f32 v[174:175],v[174:175],v[226:227] op_sel:[0,0] op_sel_hi:[1,0]
cv 168,168,169
cv 169,170,171
cv 170,172,173
cv 171,174,175
buffer_store_dwordx4 v[168:171],v244,s[8:11],s13 offen offset:2048 sc0 sc1
v_pk_mul_f32 v[176:177],v[176:177],v[226:227] op_sel:[0,0] op_sel_hi:[1,0]
v_pk_mul_f32 v[178:179],v[178:179],v[226:227] op_sel:[0,0] op_sel_hi:[1,0]
v_pk_mul_f32 v[180:181],v[180:181],v[226:227] op_sel:[0,0] op_sel_hi:[1,0]
v_pk_mul_f32 v[182:183],v[182:183],v[226:227] op_sel:[0,0] op_sel_hi:[1,0]
cv 176,176,177
cv 177,178,179
cv 178,180,181
v_mfma_f32_32x32x16_bf16 v[160:175],v[236:239],v[236:239],0
cv 179,182,183
buffer_store_dwordx4 v[176:179],v244,s[8:11],s86 offen offset:0 sc0 sc1
v_pk_mul_f32 v[184:185],v[184:185],v[226:227] op_sel:[0,0] op_sel_hi:[1,0]
v_pk_mul_f32 v[186:187],v[186:187],v[226:227] op_sel:[0,0] op_sel_hi:[1,0]
v_pk_mul_f32 v[188:189],v[188:189],v[226:227] op_sel:[0,0] op_sel_hi:[1,0]
v_pk_mul_f32 v[190:191],v[190:191],v[226:227] op_sel:[0,0] op_sel_hi:[1,0]
cv 184,184,185
cv 185,186,187
cv 186,188,189
cv 187,190,191
buffer_store_dwordx4 v[184:187],v244,s[8:11],s86 offen offset:2048 sc0 sc1
v_pk_mul_f32 v[192:193],v[192:193],v[226:227] op_sel:[0,1] op_sel_hi:[1,1]
v_pk_mul_f32 v[194:195],v[194:195],v[226:227] op_sel:[0,1] op_sel_hi:[1,1]
v_pk_mul_f32 v[196:197],v[196:197],v[226:227] op_sel:[0,1] op_sel_hi:[1,1]
v_pk_mul_f32 v[198:199],v[198:199],v[226:227] op_sel:[0,1] op_sel_hi:[1,1]
cv 192,192,193
cv 193,194,195
cv 194,196,197
v_mfma_f32_32x32x16_bf16 v[176:191],v[236:239],v[236:239],0
cv 195,198,199
buffer_store_dwordx4 v[192:195],v244,s[8:11],s13 offen offset:1024 sc0 sc1
v_pk_mul_f32 v[200:201],v[200:201],v[226:227] op_sel:[0,1] op_sel_hi:[1,1]
v_pk_mul_f32 v[202:203],v[202:203],v[226:227] op_sel:[0,1] op_sel_hi:[1,1]
v_pk_mul_f32 v[204:205],v[204:205],v[226:227] op_sel:[0,1] op_sel_hi:[1,1]
v_pk_mul_f32 v[206:207],v[206:207],v[226:227] op_sel:[0,1] op_sel_hi:[1,1]
cv 200,200,201
cv 201,202,203
cv 202,204,205
cv 203,206,207
buffer_store_dwordx4 v[200:203],v244,s[8:11],s13 offen offset:3072 sc0 sc1
v_pk_mul_f32 v[208:209],v[208:209],v[226:227] op_sel:[0,1] op_sel_hi:[1,1]
v_pk_mul_f32 v[210:211],v[210:211],v[226:227] op_sel:[0,1] op_sel_hi:[1,1]
v_pk_mul_f32 v[212:213],v[212:213],v[226:227] op_sel:[0,1] op_sel_hi:[1,1]
v_pk_mul_f32 v[214:215],v[214:215],v[226:227] op_sel:[0,1] op_sel_hi:[1,1]
cv 208,208,209
cv 209,210,211
cv 210,212,213
v_mfma_f32_32x32x16_bf16 v[192:207],v[236:239],v[236:239],0
cv 211,214,215
buffer_store_dwordx4 v[208:211],v244,s[8:11],s86 offen offset:1024 sc0 sc1
v_pk_mul_f32 v[216:217],v[216:217],v[226:227] op_sel:[0,1] op_sel_hi:[1,1]
v_pk_mul_f32 v[218:219],v[218:219],v[226:227] op_sel:[0,1] op_sel_hi:[1,1]
v_pk_mul_f32 v[220:221],v[220:221],v[226:227] op_sel:[0,1] op_sel_hi:[1,1]
v_pk_mul_f32 v[222:223],v[222:223],v[226:227] op_sel:[0,1] op_sel_hi:[1,1]
cv 216,216,217
cv 217,218,219
cv 218,220,221
cv 219,222,223
buffer_store_dwordx4 v[216:219],v244,s[8:11],s86 offen offset:3072 sc0 sc1
s_nop 1
v_mfma_f32_32x32x16_bf16 v[208:223],v[236:239],v[236:239],0
L_eva_r1:
s_cmp_lg_u32 s81,14
s_cbranch_scc1 L_loop1
s_add_u32 s81,s81,1
s_bitcmp1_b32 170,s20
s_cbranch_scc1 L_odd
s_branch L_last
.fill 276,4,0xbf800000
L_cw2:
s_mov_b64 exec,-1
s_waitcnt lgkmcnt(0)
s_barrier
cvaa 0,192,128,232
mvaa 0,200,132
s_add_u32 s81,s81,1
ex 78
mvaa 0,208,136
s_add_u32 s44,s44,1024
ex 79
mvaa 0,216,140
cv 72,72,73
cv 73,74,75
cvaa 4,196,128,232
ex 104
mvaa 4,204,132
ex 105
mvaa 4,212,136
s_mov_b32 m0,s68
ex 106
mvaa 4,220,140
ds 249,8,76,0
cvaa 32,224,128,232
ra 64,225,32768
cv 74,76,77
mvaa 32,232,132
ex 107
mvaa 32,240,136
ra 68,225,33792
cv 75,78,79
mvaa 32,248,140
ex 108
cvaa 36,228,128,232
ex 109
mvaa 36,236,132
ra 72,225,34816
cv 76,104,105
mvaa 36,244,136
ds 249,8,76,1024
mvaa 36,252,140
ex 110
cvaa 8,192,144,232
ra 76,225,35840
cv 77,106,107
mvaa 8,200,148
ex 111
mvaa 8,208,152
ex 80
mvaa 8,216,156
ra 80,225,36864
cv 78,108,109
cvaa 12,196,144,232
ex 81
mvaa 12,204,148
ds 249,8,76,2048
mvaa 12,212,152
ra 84,225,37888
cv 79,110,111
mvaa 12,220,156
ex 82
cvaa 40,224,144,232
ex 83
mvaa 40,232,148
ra 88,225,38912
cv 80,80,81
mvaa 40,240,152
ex 84
mvaa 40,248,156
ra 92,225,39936
cv 81,82,83
cvaa 44,228,144,232
ds 249,8,76,3072
mvaa 44,236,148
ex 85
mvaa 44,244,152
ex 86
mvaa 44,252,156
ra 96,225,40960
cv 82,84,85
cvaa 16,192,160,232
ex 87
mvaa 16,200,164
ex 112
mvaa 16,208,168
ra 100,225,41984
cv 83,86,87
mvaa 16,216,172
s_mov_b32 m0,s69
ex 113
cvaa 20,196,160,232
ds 249,8,77,0
mvaa 20,204,164
ra 104,225,43008
cv 84,112,113
mvaa 20,212,168
ex 114
mvaa 20,220,172
ex 115
cvaa 48,224,160,232
ra 108,225,44032
cv 85,114,115
mvaa 48,232,164
ex 116
mvaa 48,240,168
ra 112,225,45056
mvaa 48,248,172
ds 249,8,77,1024
cvaa 52,228,160,232
ex 117
mvaa 52,236,164
ex 118
mvaa 52,244,168
ra 116,225,46080
cv 86,116,117
mvaa 52,252,172
ex 119
cvaa 24,192,176,232
ra 120,225,47104
cv 87,118,119
mvaa 24,200,180
ex 88
mvaa 24,208,184
ds 249,8,77,2048
mvaa 24,216,188
ex 89
cvaa 28,196,176,232
ra 124,225,48128
cv 88,88,89
mvaa 28,204,180
ex 90
mvaa 28,212,184
ex 91
mvaa 28,220,188
ex 92
cvaa 56,224,176,232
ex 93
mvaa 56,232,180
ds 249,8,77,3072
mvaa 56,240,184
ex 94
mvaa 56,248,188
ex 95
cvaa 60,228,176,232
ex 120
mvaa 60,236,180
mvaa 60,244,184
ex 121
mvaa 60,252,188
s_waitcnt vmcnt(8) lgkmcnt(0)
s_barrier
maav 0,64,64
cv 89,90,91
cv 90,92,93
maav 0,96,68
ex 122
maav 4,68,64
ex 123
maav 4,100,68
s_mov_b32 m0,s74
ex 124
maav 8,72,64
ds 255,8,78,0
maav 8,104,68
ex 125
maav 12,76,64
cv 91,94,95
cv 92,120,121
maav 12,108,68
ex 126
v_smfmac_f32_16x16x64_bf16 v[144:147],v[228:231],v[64:71],v248
cv 93,122,123
maav 16,80,64
ex 127
maav 16,112,68
cv 94,124,125
cv 95,126,127
maav 20,84,64
maav 20,116,68
buffer_load_dword v247,v245,s[36:39],s85 offen
maav 24,88,64
maav 24,120,68
ra 192,224,32768
maav 28,92,64
maav 28,124,68
ra 196,224,33792
maav 32,64,72
ds 255,8,78,1024
maav 32,96,76
s_add_u32 s85,s85,131072
maav 36,68,72
ra 200,224,34816
maav 36,100,76
maav 40,72,72
maav 40,104,76
ra 204,224,35840
maav 44,76,72
ds 255,8,78,2048
maav 44,108,76
v_smfmac_f32_16x16x64_bf16 v[148:151],v[228:231],v[72:79],v248
ra 208,224,36864
maav 48,80,72
maav 48,112,76
maav 52,84,72
ra 212,224,37888
maav 52,116,76
ex 0
maav 56,88,72
ds 255,8,78,3072
maav 56,120,76
ra 216,224,38912
maav 60,92,72
ex 1
maav 60,124,76
ex 2
zvav 160,64,80
ra 220,224,39936
cv 0,0,1
mvav 160,96,84
s_mov_b32 m0,s75
ex 3
zvav 164,68,80
ra 224,224,40960
cv 1,2,3
mvav 164,100,84
ds 255,8,79,0
zvav 168,72,80
ex 4
mvav 168,104,84
ra 228,224,41984
zvav 172,76,80
s_add_u32 s46,s46,16384
ex 5
mvav 172,108,84
s_add_u32 s47,s47,32768
ex 6
v_smfmac_f32_16x16x64_bf16 v[152:155],v[228:231],v[80:87],v248
ra 232,224,43008
zvav 176,80,80
ds 255,8,79,1024
mvav 176,112,84
s_add_u32 s95,s47,32768
ex 7
zvav 180,84,80
ra 236,224,44032
cv 2,4,5
mvav 180,116,84
ex 32
zvav 184,88,80
ex 33
mvav 184,120,84
ra 240,224,45056
cv 3,6,7
zvav 188,92,80
ds 255,8,79,2048
mvav 188,124,84
ex 34
mvav 192,64,88
ra 244,224,46080
cv 4,32,33
mvav 192,96,92
ex 35
mvav 196,68,88
ex 36
mvav 196,100,92
ra 248,224,47104
cv 5,34,35
mvav 200,72,88
ex 37
mvav 200,104,92
ds 255,8,79,3072
mvav 204,76,88
s_cmp_ge_i32 s47,s46
ex 38
mvav 204,108,92
s_cselect_b32 s78,s95,s46
ex 39
v_smfmac_f32_16x16x64_bf16 v[156:159],v[228:231],v[88:95],v248
ra 252,224,48128
mvav 208,80,88
s_cselect_b32 s79,s95,268435456
ex 8
mvav 208,112,92
s_add_u32 s79,s79,16384
ex 9
mvav 212,84,88
ex 10
mvav 212,116,92
ex 11
mvav 216,88,88
cv 6,36,37
cv 7,38,39
mvav 216,120,92
ex 12
mvav 220,92,88
ex 13
mvav 220,124,92
s_waitcnt vmcnt(16) lgkmcnt(0)
s_barrier
cvaa 64,192,128,232
s_bitcmp0_b32 s82,s81
s_cbranch_scc1 L_loop2
s_bitcmp1_b32 s15,s81
s_cbranch_scc1 L_short2
.p2align 6
L_loop2:
mvaa 64,200,132
s_add_u32 s81,s81,1
ex 14
mvaa 64,208,136
s_add_u32 s44,s44,1024
ex 15
mvaa 64,216,140
cv 8,8,9
cv 9,10,11
cvaa 68,196,128,232
ex 40
mvaa 68,204,132
ex 41
mvaa 68,212,136
s_mov_b32 m0,s70
ex 42
mvaa 68,220,140
ds 249,8,78,0
cvaa 96,224,128,232
ra 64,225,0
cv 10,12,13
mvaa 96,232,132
ex 43
mvaa 96,240,136
ra 68,225,1024
cv 11,14,15
mvaa 96,248,140
ex 44
cvaa 100,228,128,232
ex 45
mvaa 100,236,132
ra 72,225,2048
cv 12,40,41
mvaa 100,244,136
ds 249,8,78,1024
mvaa 100,252,140
ex 46
cvaa 72,192,144,232
ra 76,225,3072
cv 13,42,43
mvaa 72,200,148
ex 47
mvaa 72,208,152
ex 16
mvaa 72,216,156
ra 80,225,4096
cv 14,44,45
cvaa 76,196,144,232
ex 17
mvaa 76,204,148
ds 249,8,78,2048
mvaa 76,212,152
ra 84,225,5120
cv 15,46,47
mvaa 76,220,156
ex 18
cvaa 104,224,144,232
ex 19
mvaa 104,232,148
ra 88,225,6144
cv 16,16,17
mvaa 104,240,152
ex 20
mvaa 104,248,156
ra 92,225,7168
cv 17,18,19
cvaa 108,228,144,232
ds 249,8,78,3072
mvaa 108,236,148
ex 21
mvaa 108,244,152
ex 22
mvaa 108,252,156
ra 96,225,8192
cv 18,20,21
cvaa 80,192,160,232
ex 23
mvaa 80,200,164
ex 48
mvaa 80,208,168
ra 100,225,9216
cv 19,22,23
mvaa 80,216,172
s_mov_b32 m0,s71
ex 49
cvaa 84,196,160,232
ds 249,8,79,0
mvaa 84,204,164
ra 104,225,10240
cv 20,48,49
mvaa 84,212,168
ex 50
mvaa 84,220,172
ex 51
cvaa 112,224,160,232
ra 108,225,11264
cv 21,50,51
mvaa 112,232,164
ex 52
mvaa 112,240,168
ra 112,225,12288
mvaa 112,248,172
ds 249,8,79,1024
cvaa 116,228,160,232
ex 53
mvaa 116,236,164
ex 54
mvaa 116,244,168
ra 116,225,13312
cv 22,52,53
mvaa 116,252,172
ex 55
cvaa 88,192,176,232
ra 120,225,14336
cv 23,54,55
mvaa 88,200,180
ex 24
mvaa 88,208,184
ds 249,8,79,2048
mvaa 88,216,188
ex 25
cvaa 92,196,176,232
ra 124,225,15360
cv 24,24,25
mvaa 92,204,180
ex 26
mvaa 92,212,184
ex 27
mvaa 92,220,188
ex 28
cvaa 120,224,176,232
ex 29
mvaa 120,232,180
ds 249,8,79,3072
mvaa 120,240,184
ex 30
mvaa 120,248,188
ex 31
cvaa 124,228,176,232
ex 56
mvaa 124,236,180
buffer_store_dwordx4 v[250:253],v244,s[8:11],s44 offen sc0 sc1
mvaa 124,244,184
ex 57
mvaa 124,252,188
s_waitcnt vmcnt(16) lgkmcnt(0)
s_barrier
maav 0,64,0
cv 25,26,27
cv 26,28,29
maav 0,96,4
ex 58
maav 4,68,0
ex 59
maav 4,100,4
s_mov_b32 m0,s72
ex 60
maav 8,72,0
ds 255,8,76,0
maav 8,104,4
ex 61
maav 12,76,0
s_cmp_lg_u32 s80,s81
cv 27,30,31
cv 28,56,57
maav 12,108,4
ex 62
v_smfmac_f32_16x16x64_bf16 v[144:147],v[228:231],v[0:7],v248
cv 29,58,59
maav 16,80,0
ex 63
maav 16,112,4
cv 30,60,61
cv 31,62,63
maav 20,84,0
maav 20,116,4
buffer_load_dword v247,v246,s[24:27],s49 offen
maav 24,88,0
s_cbranch_scc1 L_evb_r2
v_add_u32_e32 v224,s94,v224
s_lshl_b32 s86,s12,12
s_add_u32 s86,s86,131072
v_add_u32_e32 v236,s86,v244
ra 160,236,0
ra 164,236,1024
ra 168,236,2048
ra 172,236,3072
ra 176,236,16384
ra 180,236,17408
ra 184,236,18432
ra 188,236,19456
L_evb_r2:
maav 24,120,4
ra 192,224,0
maav 28,92,0
maav 28,124,4
ra 196,224,1024
maav 32,64,8
ds 255,8,76,1024
maav 32,96,12
s_add_u32 s49,s49,32768
maav 36,68,8
ra 200,224,2048
maav 36,100,12
maav 40,72,8
maav 40,104,12
ra 204,224,3072
maav 44,76,8
ds 255,8,76,2048
maav 44,108,12
v_smfmac_f32_16x16x64_bf16 v[148:151],v[228:231],v[8:15],v248
ra 208,224,4096
maav 48,80,8
maav 48,112,12
maav 52,84,8
ra 212,224,5120
maav 52,116,12
ex 64
maav 56,88,8
ds 255,8,76,3072
maav 56,120,12
ra 216,224,6144
maav 60,92,8
ex 65
maav 60,124,12
ex 66
mvav 160,64,16
ra 220,224,7168
cv 64,64,65
mvav 160,96,20
s_mov_b32 m0,s73
ex 67
mvav 164,68,16
ra 224,224,8192
cv 65,66,67
mvav 164,100,20
ds 255,8,77,0
mvav 168,72,16
ex 68
mvav 168,104,20
ra 228,224,9216
mvav 172,76,16
s_add_u32 s46,s46,16384
ex 69
mvav 172,108,20
s_add_u32 s47,s47,32768
ex 70
v_smfmac_f32_16x16x64_bf16 v[152:155],v[228:231],v[16:23],v248
ra 232,224,10240
mvav 176,80,16
ds 255,8,77,1024
mvav 176,112,20
s_add_u32 s95,s47,32768
ex 71
mvav 180,84,16
ra 236,224,11264
cv 66,68,69
mvav 180,116,20
ex 96
mvav 184,88,16
ex 97
mvav 184,120,20
ra 240,224,12288
cv 67,70,71
mvav 188,92,16
ds 255,8,77,2048
mvav 188,124,20
ex 98
mvav 192,64,24
ra 244,224,13312
cv 68,96,97
mvav 192,96,28
ex 99
mvav 196,68,24
ex 100
mvav 196,100,28
ra 248,224,14336
cv 69,98,99
mvav 200,72,24
ex 101
mvav 200,104,28
ds 255,8,77,3072
mvav 204,76,24
s_cmp_ge_i32 s47,s46
ex 102
mvav 204,108,28
s_cselect_b32 s76,s95,s46
ex 103
v_smfmac_f32_16x16x64_bf16 v[156:159],v[228:231],v[24:31],v248
ra 252,224,15360
mvav 208,80,24
s_cselect_b32 s77,s95,268435456
ex 72
mvav 208,112,28
s_add_u32 s77,s77,16384
ex 73
mvav 212,84,24
ex 74
mvav 212,116,28
ex 75
mvav 216,88,24
cv 70,100,101
cv 71,102,103
mvav 216,120,28
ex 76
mvav 220,92,24
ex 77
mvav 220,124,28
s_waitcnt vmcnt(16) lgkmcnt(0)
s_barrier
cvaa 0,192,128,232
mvaa 0,200,132
s_add_u32 s81,s81,1
ex 78
mvaa 0,208,136
s_add_u32 s44,s44,1024
ex 79
mvaa 0,216,140
cv 72,72,73
cv 73,74,75
cvaa 4,196,128,232
ex 104
mvaa 4,204,132
ex 105
mvaa 4,212,136
s_mov_b32 m0,s68
ex 106
mvaa 4,220,140
ds 249,8,76,0
cvaa 32,224,128,232
ra 64,225,32768
cv 74,76,77
mvaa 32,232,132
ex 107
mvaa 32,240,136
ra 68,225,33792
cv 75,78,79
mvaa 32,248,140
ex 108
cvaa 36,228,128,232
ex 109
mvaa 36,236,132
ra 72,225,34816
cv 76,104,105
mvaa 36,244,136
ds 249,8,76,1024
mvaa 36,252,140
ex 110
cvaa 8,192,144,232
ra 76,225,35840
cv 77,106,107
mvaa 8,200,148
ex 111
mvaa 8,208,152
ex 80
mvaa 8,216,156
ra 80,225,36864
cv 78,108,109
cvaa 12,196,144,232
ex 81
mvaa 12,204,148
ds 249,8,76,2048
mvaa 12,212,152
ra 84,225,37888
cv 79,110,111
mvaa 12,220,156
ex 82
cvaa 40,224,144,232
ex 83
mvaa 40,232,148
ra 88,225,38912
cv 80,80,81
mvaa 40,240,152
ex 84
mvaa 40,248,156
ra 92,225,39936
cv 81,82,83
cvaa 44,228,144,232
ds 249,8,76,3072
mvaa 44,236,148
ex 85
mvaa 44,244,152
ex 86
mvaa 44,252,156
ra 96,225,40960
cv 82,84,85
cvaa 16,192,160,232
ex 87
mvaa 16,200,164
ex 112
mvaa 16,208,168
ra 100,225,41984
cv 83,86,87
mvaa 16,216,172
s_mov_b32 m0,s69
ex 113
cvaa 20,196,160,232
ds 249,8,77,0
mvaa 20,204,164
ra 104,225,43008
cv 84,112,113
mvaa 20,212,168
ex 114
mvaa 20,220,172
ex 115
cvaa 48,224,160,232
ra 108,225,44032
cv 85,114,115
mvaa 48,232,164
ex 116
mvaa 48,240,168
ra 112,225,45056
mvaa 48,248,172
ds 249,8,77,1024
cvaa 52,228,160,232
ex 117
mvaa 52,236,164
ex 118
mvaa 52,244,168
ra 116,225,46080
cv 86,116,117
mvaa 52,252,172
ex 119
cvaa 24,192,176,232
ra 120,225,47104
cv 87,118,119
mvaa 24,200,180
ex 88
mvaa 24,208,184
ds 249,8,77,2048
mvaa 24,216,188
ex 89
cvaa 28,196,176,232
ra 124,225,48128
cv 88,88,89
mvaa 28,204,180
ex 90
mvaa 28,212,184
ex 91
mvaa 28,220,188
ex 92
cvaa 56,224,176,232
ex 93
mvaa 56,232,180
ds 249,8,77,3072
mvaa 56,240,184
ex 94
mvaa 56,248,188
ex 95
cvaa 60,228,176,232
ex 120
mvaa 60,236,180
buffer_store_dwordx4 v[250:253],v244,s[8:11],s44 offen sc0 sc1
mvaa 60,244,184
ex 121
mvaa 60,252,188
s_waitcnt vmcnt(16) lgkmcnt(0)
s_barrier
maav 0,64,64
cv 89,90,91
cv 90,92,93
maav 0,96,68
ex 122
maav 4,68,64
ex 123
maav 4,100,68
s_mov_b32 m0,s74
ex 124
maav 8,72,64
ds 255,8,78,0
maav 8,104,68
ex 125
maav 12,76,64
cv 91,94,95
cv 92,120,121
maav 12,108,68
ex 126
v_smfmac_f32_16x16x64_bf16 v[144:147],v[228:231],v[64:71],v248
cv 93,122,123
maav 16,80,64
ex 127
maav 16,112,68
cv 94,124,125
cv 95,126,127
maav 20,84,64
maav 20,116,68
buffer_load_dword v247,v245,s[36:39],s85 offen
maav 24,88,64
maav 24,120,68
ra 192,224,32768
maav 28,92,64
maav 28,124,68
ra 196,224,33792
maav 32,64,72
ds 255,8,78,1024
maav 32,96,76
s_add_u32 s85,s85,131072
maav 36,68,72
ra 200,224,34816
maav 36,100,76
maav 40,72,72
maav 40,104,76
ra 204,224,35840
maav 44,76,72
ds 255,8,78,2048
maav 44,108,76
v_smfmac_f32_16x16x64_bf16 v[148:151],v[228:231],v[72:79],v248
ra 208,224,36864
maav 48,80,72
maav 48,112,76
maav 52,84,72
ra 212,224,37888
maav 52,116,76
ex 0
maav 56,88,72
ds 255,8,78,3072
maav 56,120,76
ra 216,224,38912
maav 60,92,72
ex 1
maav 60,124,76
ex 2
mvav 160,64,80
ra 220,224,39936
cv 0,0,1
mvav 160,96,84
s_mov_b32 m0,s75
ex 3
mvav 164,68,80
ra 224,224,40960
cv 1,2,3
mvav 164,100,84
ds 255,8,79,0
mvav 168,72,80
ex 4
mvav 168,104,84
ra 228,224,41984
mvav 172,76,80
s_add_u32 s46,s46,16384
ex 5
mvav 172,108,84
s_add_u32 s47,s47,32768
ex 6
v_smfmac_f32_16x16x64_bf16 v[152:155],v[228:231],v[80:87],v248
ra 232,224,43008
mvav 176,80,80
ds 255,8,79,1024
mvav 176,112,84
s_add_u32 s95,s47,32768
ex 7
mvav 180,84,80
ra 236,224,44032
cv 2,4,5
mvav 180,116,84
ex 32
mvav 184,88,80
ex 33
mvav 184,120,84
ra 240,224,45056
cv 3,6,7
mvav 188,92,80
ds 255,8,79,2048
mvav 188,124,84
ex 34
mvav 192,64,88
ra 244,224,46080
cv 4,32,33
mvav 192,96,92
ex 35
mvav 196,68,88
ex 36
mvav 196,100,92
ra 248,224,47104
cv 5,34,35
mvav 200,72,88
ex 37
mvav 200,104,92
ds 255,8,79,3072
mvav 204,76,88
s_cmp_ge_i32 s47,s46
ex 38
mvav 204,108,92
s_cselect_b32 s78,s95,s46
ex 39
v_smfmac_f32_16x16x64_bf16 v[156:159],v[228:231],v[88:95],v248
ra 252,224,48128
mvav 208,80,88
s_cselect_b32 s79,s95,268435456
ex 8
mvav 208,112,92
s_add_u32 s79,s79,16384
ex 9
mvav 212,84,88
ex 10
mvav 212,116,92
ex 11
mvav 216,88,88
cv 6,36,37
cv 7,38,39
mvav 216,120,92
ex 12
mvav 220,92,88
ex 13
mvav 220,124,92
s_waitcnt vmcnt(16) lgkmcnt(0)
s_barrier
cvaa 64,192,128,232
s_bitcmp0_b32 s82,s81
s_cbranch_scc1 L_loop2
s_bitcmp0_b32 s15,s81
s_cbranch_scc1 L_eva_r2
L_short2:
v_add_f32_e32 v226,v152,v153
v_add_f32_e32 v227,v156,v157
v_subrev_u32_e32 v236,0x23800000,v226
v_subrev_u32_e32 v237,0x23800000,v227
v_max_u32_e32 v236,v236,v237
v_cmp_lt_u32_e64 s[40:41],s48,v236
v_rcp_f32_e32 v226,v226
v_rcp_f32_e32 v227,v227
s_add_u32 s86,s13,4096
)"
R"(v_mov_b32_e32 v236,0
v_mov_b32_e32 v237,0
v_mov_b32_e32 v238,0
v_mov_b32_e32 v239,0
v_mov_b64_e32 v[152:153],0
v_mov_b64_e32 v[156:157],0
v_mul_f32_e32 v226,v242,v226
v_mul_f32_e32 v227,v243,v227
v_add_u32_e32 v225,s94,v225
v_pk_mul_f32 v[160:161],v[160:161],v[226:227] op_sel:[0,0] op_sel_hi:[1,0]
v_pk_mul_f32 v[162:163],v[162:163],v[226:227] op_sel:[0,0] op_sel_hi:[1,0]
v_pk_mul_f32 v[164:165],v[164:165],v[226:227] op_sel:[0,0] op_sel_hi:[1,0]
v_pk_mul_f32 v[166:167],v[166:167],v[226:227] op_sel:[0,0] op_sel_hi:[1,0]
cv 160,160,161
cv 161,162,163
cv 162,164,165
cv 163,166,167
buffer_store_dwordx4 v[160:163],v244,s[8:11],s13 offen offset:0 sc0 sc1
v_pk_mul_f32 v[168:169],v[168:169],v[226:227] op_sel:[0,0] op_sel_hi:[1,0]
v_pk_mul_f32 v[170:171],v[170:171],v[226:227] op_sel:[0,0] op_sel_hi:[1,0]
v_pk_mul_f32 v[172:173],v[172:173],v[226:227] op_sel:[0,0] op_sel_hi:[1,0]
v_pk_mul_f32 v[174:175],v[174:175],v[226:227] op_sel:[0,0] op_sel_hi:[1,0]
cv 168,168,169
cv 169,170,171
cv 170,172,173
cv 171,174,175
buffer_store_dwordx4 v[168:171],v244,s[8:11],s13 offen offset:2048 sc0 sc1
v_pk_mul_f32 v[176:177],v[176:177],v[226:227] op_sel:[0,0] op_sel_hi:[1,0]
v_pk_mul_f32 v[178:179],v[178:179],v[226:227] op_sel:[0,0] op_sel_hi:[1,0]
v_pk_mul_f32 v[180:181],v[180:181],v[226:227] op_sel:[0,0] op_sel_hi:[1,0]
v_pk_mul_f32 v[182:183],v[182:183],v[226:227] op_sel:[0,0] op_sel_hi:[1,0]
cv 176,176,177
cv 177,178,179
cv 178,180,181
v_mfma_f32_32x32x16_bf16 v[160:175],v[236:239],v[236:239],0
cv 179,182,183
buffer_store_dwordx4 v[176:179],v244,s[8:11],s86 offen offset:0 sc0 sc1
v_pk_mul_f32 v[184:185],v[184:185],v[226:227] op_sel:[0,0] op_sel_hi:[1,0]
v_pk_mul_f32 v[186:187],v[186:187],v[226:227] op_sel:[0,0] op_sel_hi:[1,0]
v_pk_mul_f32 v[188:189],v[188:189],v[226:227] op_sel:[0,0] op_sel_hi:[1,0]
v_pk_mul_f32 v[190:191],v[190:191],v[226:227] op_sel:[0,0] op_sel_hi:[1,0]
cv 184,184,185
cv 185,186,187
cv 186,188,189
cv 187,190,191
buffer_store_dwordx4 v[184:187],v244,s[8:11],s86 offen offset:2048 sc0 sc1
v_pk_mul_f32 v[192:193],v[192:193],v[226:227] op_sel:[0,1] op_sel_hi:[1,1]
v_pk_mul_f32 v[194:195],v[194:195],v[226:227] op_sel:[0,1] op_sel_hi:[1,1]
v_pk_mul_f32 v[196:197],v[196:197],v[226:227] op_sel:[0,1] op_sel_hi:[1,1]
v_pk_mul_f32 v[198:199],v[198:199],v[226:227] op_sel:[0,1] op_sel_hi:[1,1]
cv 192,192,193
cv 193,194,195
cv 194,196,197
v_mfma_f32_32x32x16_bf16 v[176:191],v[236:239],v[236:239],0
cv 195,198,199
buffer_store_dwordx4 v[192:195],v244,s[8:11],s13 offen offset:1024 sc0 sc1
v_pk_mul_f32 v[200:201],v[200:201],v[226:227] op_sel:[0,1] op_sel_hi:[1,1]
v_pk_mul_f32 v[202:203],v[202:203],v[226:227] op_sel:[0,1] op_sel_hi:[1,1]
v_pk_mul_f32 v[204:205],v[204:205],v[226:227] op_sel:[0,1] op_sel_hi:[1,1]
v_pk_mul_f32 v[206:207],v[206:207],v[226:227] op_sel:[0,1] op_sel_hi:[1,1]
cv 200,200,201
cv 201,202,203
cv 202,204,205
cv 203,206,207
buffer_store_dwordx4 v[200:203],v244,s[8:11],s13 offen offset:3072 sc0 sc1
v_pk_mul_f32 v[208:209],v[208:209],v[226:227] op_sel:[0,1] op_sel_hi:[1,1]
v_pk_mul_f32 v[210:211],v[210:211],v[226:227] op_sel:[0,1] op_sel_hi:[1,1]
v_pk_mul_f32 v[212:213],v[212:213],v[226:227] op_sel:[0,1] op_sel_hi:[1,1]
v_pk_mul_f32 v[214:215],v[214:215],v[226:227] op_sel:[0,1] op_sel_hi:[1,1]
cv 208,208,209
cv 209,210,211
cv 210,212,213
v_mfma_f32_32x32x16_bf16 v[192:207],v[236:239],v[236:239],0
cv 211,214,215
buffer_store_dwordx4 v[208:211],v244,s[8:11],s86 offen offset:1024 sc0 sc1
v_pk_mul_f32 v[216:217],v[216:217],v[226:227] op_sel:[0,1] op_sel_hi:[1,1]
v_pk_mul_f32 v[218:219],v[218:219],v[226:227] op_sel:[0,1] op_sel_hi:[1,1]
v_pk_mul_f32 v[220:221],v[220:221],v[226:227] op_sel:[0,1] op_sel_hi:[1,1]
v_pk_mul_f32 v[222:223],v[222:223],v[226:227] op_sel:[0,1] op_sel_hi:[1,1]
cv 216,216,217
cv 217,218,219
cv 218,220,221
cv 219,222,223
buffer_store_dwordx4 v[216:219],v244,s[8:11],s86 offen offset:3072 sc0 sc1
s_nop 1
v_mfma_f32_32x32x16_bf16 v[208:223],v[236:239],v[236:239],0
L_eva_r2:
s_cmp_lg_u32 s81,14
s_cbranch_scc1 L_loop2
s_add_u32 s81,s81,1
s_bitcmp1_b32 170,s20
s_cbranch_scc1 L_odd
s_branch L_last
.fill 276,4,0xbf800000
L_cw3:
s_mov_b64 exec,-1
s_waitcnt lgkmcnt(0)
s_barrier
cvaa 0,192,128,232
mvaa 0,200,132
s_add_u32 s81,s81,1
ex 78
mvaa 0,208,136
s_add_u32 s44,s44,1024
ex 79
mvaa 0,216,140
cv 72,72,73
cv 73,74,75
cvaa 4,196,128,232
ex 104
mvaa 4,204,132
ex 105
mvaa 4,212,136
s_mov_b32 m0,s68
ex 106
mvaa 4,220,140
ra 64,225,32768
cv 74,76,77
cvaa 32,224,128,232
ds 249,8,76,0
mvaa 32,232,132
ex 107
mvaa 32,240,136
ra 68,225,33792
cv 75,78,79
mvaa 32,248,140
ex 108
cvaa 36,228,128,232
ex 109
mvaa 36,236,132
ra 72,225,34816
cv 76,104,105
mvaa 36,244,136
ex 110
mvaa 36,252,140
ds 249,8,76,1024
cvaa 8,192,144,232
ra 76,225,35840
cv 77,106,107
mvaa 8,200,148
ex 111
mvaa 8,208,152
ex 80
mvaa 8,216,156
ra 80,225,36864
cv 78,108,109
cvaa 12,196,144,232
ex 81
mvaa 12,204,148
ra 84,225,37888
cv 79,110,111
mvaa 12,212,152
ds 249,8,76,2048
mvaa 12,220,156
ex 82
cvaa 40,224,144,232
ex 83
mvaa 40,232,148
ra 88,225,38912
cv 80,80,81
mvaa 40,240,152
ex 84
mvaa 40,248,156
ra 92,225,39936
cv 81,82,83
cvaa 44,228,144,232
ex 85
mvaa 44,236,148
ds 249,8,76,3072
mvaa 44,244,152
ex 86
mvaa 44,252,156
ra 96,225,40960
cv 82,84,85
cvaa 16,192,160,232
ex 87
mvaa 16,200,164
ex 112
mvaa 16,208,168
ra 100,225,41984
cv 83,86,87
mvaa 16,216,172
s_mov_b32 m0,s69
ex 113
cvaa 20,196,160,232
ra 104,225,43008
cv 84,112,113
mvaa 20,204,164
ds 249,8,77,0
mvaa 20,212,168
ex 114
mvaa 20,220,172
ex 115
cvaa 48,224,160,232
ra 108,225,44032
cv 85,114,115
mvaa 48,232,164
ex 116
mvaa 48,240,168
ra 112,225,45056
mvaa 48,248,172
ex 117
cvaa 52,228,160,232
ds 249,8,77,1024
mvaa 52,236,164
ex 118
mvaa 52,244,168
ra 116,225,46080
cv 86,116,117
mvaa 52,252,172
ex 119
cvaa 24,192,176,232
ra 120,225,47104
cv 87,118,119
mvaa 24,200,180
ex 88
mvaa 24,208,184
ex 89
mvaa 24,216,188
ds 249,8,77,2048
cvaa 28,196,176,232
ra 124,225,48128
cv 88,88,89
mvaa 28,204,180
ex 90
mvaa 28,212,184
ex 91
mvaa 28,220,188
ex 92
cvaa 56,224,176,232
ex 93
mvaa 56,232,180
ex 94
mvaa 56,240,184
ds 249,8,77,3072
mvaa 56,248,188
ex 95
cvaa 60,228,176,232
ex 120
mvaa 60,236,180
ex 121
mvaa 60,244,184
mvaa 60,252,188
s_waitcnt vmcnt(8) lgkmcnt(0)
s_barrier
maav 0,64,64
cv 89,90,91
cv 90,92,93
maav 0,96,68
ex 122
maav 4,68,64
ex 123
maav 4,100,68
ex 124
maav 8,72,64
s_mov_b32 m0,s74
ex 125
maav 8,104,68
ds 255,8,78,0
maav 12,76,64
cv 91,94,95
cv 92,120,121
maav 12,108,68
ex 126
v_smfmac_f32_16x16x64_bf16 v[144:147],v[228:231],v[64:71],v248
cv 93,122,123
maav 16,80,64
ex 127
maav 16,112,68
cv 94,124,125
cv 95,126,127
maav 20,84,64
maav 20,116,68
maav 24,88,64
buffer_load_dword v247,v245,s[36:39],s85 offen
maav 24,120,68
ra 192,224,32768
maav 28,92,64
maav 28,124,68
ra 196,224,33792
maav 32,64,72
maav 32,96,76
ds 255,8,78,1024
maav 36,68,72
ra 200,224,34816
maav 36,100,76
s_add_u32 s85,s85,131072
maav 40,72,72
maav 40,104,76
ra 204,224,35840
maav 44,76,72
maav 44,108,76
ds 255,8,78,2048
v_smfmac_f32_16x16x64_bf16 v[148:151],v[228:231],v[72:79],v248
ra 208,224,36864
maav 48,80,72
maav 48,112,76
maav 52,84,72
ra 212,224,37888
maav 52,116,76
ex 0
maav 56,88,72
ra 216,224,38912
maav 56,120,76
ds 255,8,78,3072
maav 60,92,72
ex 1
maav 60,124,76
ex 2
zvav 160,64,80
ra 220,224,39936
cv 0,0,1
mvav 160,96,84
ex 3
zvav 164,68,80
ra 224,224,40960
cv 1,2,3
mvav 164,100,84
s_mov_b32 m0,s75
ex 4
zvav 168,72,80
ds 255,8,79,0
mvav 168,104,84
ra 228,224,41984
zvav 172,76,80
s_add_u32 s46,s46,16384
ex 5
mvav 172,108,84
s_add_u32 s47,s47,32768
ex 6
v_smfmac_f32_16x16x64_bf16 v[152:155],v[228:231],v[80:87],v248
ra 232,224,43008
zvav 176,80,80
ds 255,8,79,1024
mvav 176,112,84
s_add_u32 s95,s47,32768
ex 7
zvav 180,84,80
ra 236,224,44032
cv 2,4,5
mvav 180,116,84
ex 32
zvav 184,88,80
ex 33
mvav 184,120,84
ra 240,224,45056
cv 3,6,7
zvav 188,92,80
ex 34
mvav 188,124,84
ds 255,8,79,2048
mvav 192,64,88
ra 244,224,46080
cv 4,32,33
mvav 192,96,92
ex 35
mvav 196,68,88
ex 36
mvav 196,100,92
ra 248,224,47104
cv 5,34,35
mvav 200,72,88
ex 37
mvav 200,104,92
ex 38
mvav 204,76,88
ds 255,8,79,3072
mvav 204,108,92
s_cmp_ge_i32 s47,s46
ex 39
v_smfmac_f32_16x16x64_bf16 v[156:159],v[228:231],v[88:95],v248
ra 252,224,48128
mvav 208,80,88
s_cselect_b32 s78,s95,s46
ex 8
mvav 208,112,92
s_cselect_b32 s79,s95,268435456
ex 9
mvav 212,84,88
s_add_u32 s79,s79,16384
ex 10
mvav 212,116,92
ex 11
mvav 216,88,88
cv 6,36,37
cv 7,38,39
mvav 216,120,92
ex 12
mvav 220,92,88
ex 13
mvav 220,124,92
s_waitcnt vmcnt(16) lgkmcnt(0)
s_barrier
cvaa 64,192,128,232
s_bitcmp0_b32 s82,s81
s_cbranch_scc1 L_loop3
s_bitcmp1_b32 s15,s81
s_cbranch_scc1 L_short3
.p2align 6
L_loop3:
mvaa 64,200,132
s_add_u32 s81,s81,1
ex 14
mvaa 64,208,136
s_add_u32 s44,s44,1024
ex 15
mvaa 64,216,140
cv 8,8,9
cv 9,10,11
cvaa 68,196,128,232
ex 40
mvaa 68,204,132
ex 41
mvaa 68,212,136
s_mov_b32 m0,s70
ex 42
mvaa 68,220,140
ra 64,225,0
cv 10,12,13
cvaa 96,224,128,232
ds 249,8,78,0
mvaa 96,232,132
ex 43
mvaa 96,240,136
ra 68,225,1024
cv 11,14,15
mvaa 96,248,140
ex 44
cvaa 100,228,128,232
ex 45
mvaa 100,236,132
ra 72,225,2048
cv 12,40,41
mvaa 100,244,136
ex 46
mvaa 100,252,140
ds 249,8,78,1024
cvaa 72,192,144,232
ra 76,225,3072
cv 13,42,43
mvaa 72,200,148
ex 47
mvaa 72,208,152
ex 16
mvaa 72,216,156
ra 80,225,4096
cv 14,44,45
cvaa 76,196,144,232
ex 17
mvaa 76,204,148
ra 84,225,5120
cv 15,46,47
mvaa 76,212,152
ds 249,8,78,2048
mvaa 76,220,156
ex 18
cvaa 104,224,144,232
ex 19
mvaa 104,232,148
ra 88,225,6144
cv 16,16,17
mvaa 104,240,152
ex 20
mvaa 104,248,156
ra 92,225,7168
cv 17,18,19
cvaa 108,228,144,232
ex 21
mvaa 108,236,148
ds 249,8,78,3072
mvaa 108,244,152
ex 22
mvaa 108,252,156
ra 96,225,8192
cv 18,20,21
cvaa 80,192,160,232
ex 23
mvaa 80,200,164
ex 48
mvaa 80,208,168
ra 100,225,9216
cv 19,22,23
mvaa 80,216,172
s_mov_b32 m0,s71
ex 49
cvaa 84,196,160,232
ra 104,225,10240
cv 20,48,49
mvaa 84,204,164
ds 249,8,79,0
mvaa 84,212,168
ex 50
mvaa 84,220,172
ex 51
cvaa 112,224,160,232
ra 108,225,11264
cv 21,50,51
mvaa 112,232,164
ex 52
mvaa 112,240,168
ra 112,225,12288
mvaa 112,248,172
ex 53
cvaa 116,228,160,232
ds 249,8,79,1024
mvaa 116,236,164
ex 54
mvaa 116,244,168
ra 116,225,13312
cv 22,52,53
mvaa 116,252,172
ex 55
cvaa 88,192,176,232
ra 120,225,14336
cv 23,54,55
mvaa 88,200,180
ex 24
mvaa 88,208,184
ex 25
mvaa 88,216,188
ds 249,8,79,2048
cvaa 92,196,176,232
ra 124,225,15360
cv 24,24,25
mvaa 92,204,180
ex 26
mvaa 92,212,184
ex 27
mvaa 92,220,188
ex 28
cvaa 120,224,176,232
ex 29
mvaa 120,232,180
ex 30
mvaa 120,240,184
ds 249,8,79,3072
mvaa 120,248,188
ex 31
cvaa 124,228,176,232
ex 56
mvaa 124,236,180
ex 57
mvaa 124,244,184
buffer_store_dwordx4 v[250:253],v244,s[8:11],s44 offen sc0 sc1
mvaa 124,252,188
s_waitcnt vmcnt(16) lgkmcnt(0)
s_barrier
maav 0,64,0
cv 25,26,27
cv 26,28,29
maav 0,96,4
ex 58
maav 4,68,0
ex 59
maav 4,100,4
ex 60
maav 8,72,0
s_mov_b32 m0,s72
ex 61
maav 8,104,4
ds 255,8,76,0
maav 12,76,0
s_cmp_lg_u32 s80,s81
cv 27,30,31
cv 28,56,57
maav 12,108,4
ex 62
v_smfmac_f32_16x16x64_bf16 v[144:147],v[228:231],v[0:7],v248
cv 29,58,59
maav 16,80,0
ex 63
maav 16,112,4
cv 30,60,61
cv 31,62,63
maav 20,84,0
maav 20,116,4
s_cbranch_scc1 L_evb_r3
v_add_u32_e32 v224,s94,v224
s_lshl_b32 s86,s12,12
s_add_u32 s86,s86,131072
v_add_u32_e32 v236,s86,v244
ra 160,236,0
ra 164,236,1024
ra 168,236,2048
ra 172,236,3072
ra 176,236,16384
ra 180,236,17408
ra 184,236,18432
ra 188,236,19456
L_evb_r3:
maav 24,88,0
buffer_load_dword v247,v246,s[24:27],s49 offen
maav 24,120,4
ra 192,224,0
maav 28,92,0
maav 28,124,4
ra 196,224,1024
maav 32,64,8
maav 32,96,12
ds 255,8,76,1024
maav 36,68,8
ra 200,224,2048
maav 36,100,12
s_add_u32 s49,s49,32768
maav 40,72,8
maav 40,104,12
ra 204,224,3072
maav 44,76,8
maav 44,108,12
ds 255,8,76,2048
v_smfmac_f32_16x16x64_bf16 v[148:151],v[228:231],v[8:15],v248
ra 208,224,4096
maav 48,80,8
maav 48,112,12
maav 52,84,8
ra 212,224,5120
maav 52,116,12
ex 64
maav 56,88,8
ra 216,224,6144
maav 56,120,12
ds 255,8,76,3072
maav 60,92,8
ex 65
maav 60,124,12
ex 66
mvav 160,64,16
ra 220,224,7168
cv 64,64,65
mvav 160,96,20
ex 67
mvav 164,68,16
ra 224,224,8192
cv 65,66,67
mvav 164,100,20
s_mov_b32 m0,s73
ex 68
mvav 168,72,16
ds 255,8,77,0
mvav 168,104,20
ra 228,224,9216
mvav 172,76,16
s_add_u32 s46,s46,16384
ex 69
mvav 172,108,20
s_add_u32 s47,s47,32768
ex 70
v_smfmac_f32_16x16x64_bf16 v[152:155],v[228:231],v[16:23],v248
ra 232,224,10240
mvav 176,80,16
ds 255,8,77,1024
mvav 176,112,20
s_add_u32 s95,s47,32768
ex 71
mvav 180,84,16
ra 236,224,11264
cv 66,68,69
mvav 180,116,20
ex 96
mvav 184,88,16
ex 97
mvav 184,120,20
ra 240,224,12288
cv 67,70,71
mvav 188,92,16
ex 98
mvav 188,124,20
ds 255,8,77,2048
mvav 192,64,24
ra 244,224,13312
cv 68,96,97
mvav 192,96,28
ex 99
mvav 196,68,24
ex 100
mvav 196,100,28
ra 248,224,14336
cv 69,98,99
mvav 200,72,24
ex 101
mvav 200,104,28
ex 102
mvav 204,76,24
ds 255,8,77,3072
mvav 204,108,28
s_cmp_ge_i32 s47,s46
ex 103
v_smfmac_f32_16x16x64_bf16 v[156:159],v[228:231],v[24:31],v248
ra 252,224,15360
mvav 208,80,24
s_cselect_b32 s76,s95,s46
ex 72
mvav 208,112,28
s_cselect_b32 s77,s95,268435456
ex 73
mvav 212,84,24
s_add_u32 s77,s77,16384
ex 74
mvav 212,116,28
ex 75
mvav 216,88,24
cv 70,100,101
cv 71,102,103
mvav 216,120,28
ex 76
mvav 220,92,24
ex 77
mvav 220,124,28
s_waitcnt vmcnt(16) lgkmcnt(0)
s_barrier
cvaa 0,192,128,232
mvaa 0,200,132
s_add_u32 s81,s81,1
ex 78
mvaa 0,208,136
s_add_u32 s44,s44,1024
ex 79
mvaa 0,216,140
cv 72,72,73
cv 73,74,75
cvaa 4,196,128,232
ex 104
mvaa 4,204,132
ex 105
mvaa 4,212,136
s_mov_b32 m0,s68
ex 106
mvaa 4,220,140
ra 64,225,32768
cv 74,76,77
cvaa 32,224,128,232
ds 249,8,76,0
mvaa 32,232,132
ex 107
mvaa 32,240,136
ra 68,225,33792
cv 75,78,79
mvaa 32,248,140
ex 108
cvaa 36,228,128,232
ex 109
mvaa 36,236,132
ra 72,225,34816
cv 76,104,105
mvaa 36,244,136
ex 110
mvaa 36,252,140
ds 249,8,76,1024
cvaa 8,192,144,232
ra 76,225,35840
cv 77,106,107
mvaa 8,200,148
ex 111
mvaa 8,208,152
ex 80
mvaa 8,216,156
ra 80,225,36864
cv 78,108,109
cvaa 12,196,144,232
ex 81
mvaa 12,204,148
ra 84,225,37888
cv 79,110,111
mvaa 12,212,152
ds 249,8,76,2048
mvaa 12,220,156
ex 82
cvaa 40,224,144,232
ex 83
mvaa 40,232,148
ra 88,225,38912
cv 80,80,81
mvaa 40,240,152
ex 84
mvaa 40,248,156
ra 92,225,39936
cv 81,82,83
cvaa 44,228,144,232
ex 85
mvaa 44,236,148
ds 249,8,76,3072
mvaa 44,244,152
ex 86
mvaa 44,252,156
ra 96,225,40960
cv 82,84,85
cvaa 16,192,160,232
ex 87
mvaa 16,200,164
ex 112
mvaa 16,208,168
ra 100,225,41984
cv 83,86,87
mvaa 16,216,172
s_mov_b32 m0,s69
ex 113
cvaa 20,196,160,232
ra 104,225,43008
cv 84,112,113
mvaa 20,204,164
ds 249,8,77,0
mvaa 20,212,168
ex 114
mvaa 20,220,172
ex 115
cvaa 48,224,160,232
ra 108,225,44032
cv 85,114,115
mvaa 48,232,164
ex 116
mvaa 48,240,168
ra 112,225,45056
mvaa 48,248,172
ex 117
cvaa 52,228,160,232
ds 249,8,77,1024
mvaa 52,236,164
ex 118
mvaa 52,244,168
ra 116,225,46080
cv 86,116,117
mvaa 52,252,172
ex 119
cvaa 24,192,176,232
ra 120,225,47104
cv 87,118,119
mvaa 24,200,180
ex 88
mvaa 24,208,184
ex 89
mvaa 24,216,188
ds 249,8,77,2048
cvaa 28,196,176,232
ra 124,225,48128
cv 88,88,89
mvaa 28,204,180
ex 90
mvaa 28,212,184
ex 91
mvaa 28,220,188
ex 92
cvaa 56,224,176,232
ex 93
mvaa 56,232,180
ex 94
mvaa 56,240,184
ds 249,8,77,3072
mvaa 56,248,188
ex 95
cvaa 60,228,176,232
ex 120
mvaa 60,236,180
ex 121
mvaa 60,244,184
buffer_store_dwordx4 v[250:253],v244,s[8:11],s44 offen sc0 sc1
mvaa 60,252,188
s_waitcnt vmcnt(16) lgkmcnt(0)
s_barrier
maav 0,64,64
cv 89,90,91
cv 90,92,93
maav 0,96,68
ex 122
maav 4,68,64
ex 123
maav 4,100,68
ex 124
maav 8,72,64
s_mov_b32 m0,s74
ex 125
maav 8,104,68
ds 255,8,78,0
maav 12,76,64
cv 91,94,95
cv 92,120,121
maav 12,108,68
ex 126
v_smfmac_f32_16x16x64_bf16 v[144:147],v[228:231],v[64:71],v248
cv 93,122,123
maav 16,80,64
ex 127
maav 16,112,68
cv 94,124,125
cv 95,126,127
maav 20,84,64
maav 20,116,68
maav 24,88,64
buffer_load_dword v247,v245,s[36:39],s85 offen
maav 24,120,68
ra 192,224,32768
maav 28,92,64
maav 28,124,68
ra 196,224,33792
maav 32,64,72
maav 32,96,76
ds 255,8,78,1024
maav 36,68,72
ra 200,224,34816
maav 36,100,76
s_add_u32 s85,s85,131072
maav 40,72,72
maav 40,104,76
ra 204,224,35840
maav 44,76,72
maav 44,108,76
ds 255,8,78,2048
v_smfmac_f32_16x16x64_bf16 v[148:151],v[228:231],v[72:79],v248
ra 208,224,36864
maav 48,80,72
maav 48,112,76
maav 52,84,72
ra 212,224,37888
maav 52,116,76
ex 0
maav 56,88,72
ra 216,224,38912
maav 56,120,76
ds 255,8,78,3072
maav 60,92,72
ex 1
maav 60,124,76
ex 2
mvav 160,64,80
ra 220,224,39936
cv 0,0,1
mvav 160,96,84
ex 3
mvav 164,68,80
ra 224,224,40960
cv 1,2,3
mvav 164,100,84
s_mov_b32 m0,s75
ex 4
mvav 168,72,80
ds 255,8,79,0
mvav 168,104,84
ra 228,224,41984
mvav 172,76,80
s_add_u32 s46,s46,16384
ex 5
mvav 172,108,84
s_add_u32 s47,s47,32768
ex 6
v_smfmac_f32_16x16x64_bf16 v[152:155],v[228:231],v[80:87],v248
ra 232,224,43008
mvav 176,80,80
ds 255,8,79,1024
mvav 176,112,84
s_add_u32 s95,s47,32768
ex 7
mvav 180,84,80
ra 236,224,44032
cv 2,4,5
mvav 180,116,84
ex 32
mvav 184,88,80
ex 33
mvav 184,120,84
ra 240,224,45056
cv 3,6,7
mvav 188,92,80
ex 34
mvav 188,124,84
ds 255,8,79,2048
mvav 192,64,88
ra 244,224,46080
cv 4,32,33
mvav 192,96,92
ex 35
mvav 196,68,88
ex 36
mvav 196,100,92
ra 248,224,47104
cv 5,34,35
mvav 200,72,88
ex 37
mvav 200,104,92
ex 38
mvav 204,76,88
ds 255,8,79,3072
mvav 204,108,92
s_cmp_ge_i32 s47,s46
ex 39
v_smfmac_f32_16x16x64_bf16 v[156:159],v[228:231],v[88:95],v248
ra 252,224,48128
mvav 208,80,88
s_cselect_b32 s78,s95,s46
ex 8
mvav 208,112,92
s_cselect_b32 s79,s95,268435456
ex 9
mvav 212,84,88
s_add_u32 s79,s79,16384
ex 10
mvav 212,116,92
ex 11
mvav 216,88,88
cv 6,36,37
cv 7,38,39
mvav 216,120,92
)"
R"(ex 12
mvav 220,92,88
ex 13
mvav 220,124,92
s_waitcnt vmcnt(16) lgkmcnt(0)
s_barrier
cvaa 64,192,128,232
s_bitcmp0_b32 s82,s81
s_cbranch_scc1 L_loop3
s_bitcmp0_b32 s15,s81
s_cbranch_scc1 L_eva_r3
L_short3:
v_add_f32_e32 v226,v152,v153
v_add_f32_e32 v227,v156,v157
v_subrev_u32_e32 v236,0x23800000,v226
v_subrev_u32_e32 v237,0x23800000,v227
v_max_u32_e32 v236,v236,v237
v_cmp_lt_u32_e64 s[40:41],s48,v236
v_rcp_f32_e32 v226,v226
v_rcp_f32_e32 v227,v227
s_add_u32 s86,s13,4096
v_mov_b32_e32 v236,0
v_mov_b32_e32 v237,0
v_mov_b32_e32 v238,0
v_mov_b32_e32 v239,0
v_mov_b64_e32 v[152:153],0
v_mov_b64_e32 v[156:157],0
v_mul_f32_e32 v226,v242,v226
v_mul_f32_e32 v227,v243,v227
v_add_u32_e32 v225,s94,v225
v_pk_mul_f32 v[160:161],v[160:161],v[226:227] op_sel:[0,0] op_sel_hi:[1,0]
v_pk_mul_f32 v[162:163],v[162:163],v[226:227] op_sel:[0,0] op_sel_hi:[1,0]
v_pk_mul_f32 v[164:165],v[164:165],v[226:227] op_sel:[0,0] op_sel_hi:[1,0]
v_pk_mul_f32 v[166:167],v[166:167],v[226:227] op_sel:[0,0] op_sel_hi:[1,0]
cv 160,160,161
cv 161,162,163
cv 162,164,165
cv 163,166,167
buffer_store_dwordx4 v[160:163],v244,s[8:11],s13 offen offset:0 sc0 sc1
v_pk_mul_f32 v[168:169],v[168:169],v[226:227] op_sel:[0,0] op_sel_hi:[1,0]
v_pk_mul_f32 v[170:171],v[170:171],v[226:227] op_sel:[0,0] op_sel_hi:[1,0]
v_pk_mul_f32 v[172:173],v[172:173],v[226:227] op_sel:[0,0] op_sel_hi:[1,0]
v_pk_mul_f32 v[174:175],v[174:175],v[226:227] op_sel:[0,0] op_sel_hi:[1,0]
cv 168,168,169
cv 169,170,171
cv 170,172,173
cv 171,174,175
buffer_store_dwordx4 v[168:171],v244,s[8:11],s13 offen offset:2048 sc0 sc1
v_pk_mul_f32 v[176:177],v[176:177],v[226:227] op_sel:[0,0] op_sel_hi:[1,0]
v_pk_mul_f32 v[178:179],v[178:179],v[226:227] op_sel:[0,0] op_sel_hi:[1,0]
v_pk_mul_f32 v[180:181],v[180:181],v[226:227] op_sel:[0,0] op_sel_hi:[1,0]
v_pk_mul_f32 v[182:183],v[182:183],v[226:227] op_sel:[0,0] op_sel_hi:[1,0]
cv 176,176,177
cv 177,178,179
cv 178,180,181
v_mfma_f32_32x32x16_bf16 v[160:175],v[236:239],v[236:239],0
cv 179,182,183
buffer_store_dwordx4 v[176:179],v244,s[8:11],s86 offen offset:0 sc0 sc1
v_pk_mul_f32 v[184:185],v[184:185],v[226:227] op_sel:[0,0] op_sel_hi:[1,0]
v_pk_mul_f32 v[186:187],v[186:187],v[226:227] op_sel:[0,0] op_sel_hi:[1,0]
v_pk_mul_f32 v[188:189],v[188:189],v[226:227] op_sel:[0,0] op_sel_hi:[1,0]
v_pk_mul_f32 v[190:191],v[190:191],v[226:227] op_sel:[0,0] op_sel_hi:[1,0]
cv 184,184,185
cv 185,186,187
cv 186,188,189
cv 187,190,191
buffer_store_dwordx4 v[184:187],v244,s[8:11],s86 offen offset:2048 sc0 sc1
v_pk_mul_f32 v[192:193],v[192:193],v[226:227] op_sel:[0,1] op_sel_hi:[1,1]
v_pk_mul_f32 v[194:195],v[194:195],v[226:227] op_sel:[0,1] op_sel_hi:[1,1]
v_pk_mul_f32 v[196:197],v[196:197],v[226:227] op_sel:[0,1] op_sel_hi:[1,1]
v_pk_mul_f32 v[198:199],v[198:199],v[226:227] op_sel:[0,1] op_sel_hi:[1,1]
cv 192,192,193
cv 193,194,195
cv 194,196,197
v_mfma_f32_32x32x16_bf16 v[176:191],v[236:239],v[236:239],0
cv 195,198,199
buffer_store_dwordx4 v[192:195],v244,s[8:11],s13 offen offset:1024 sc0 sc1
v_pk_mul_f32 v[200:201],v[200:201],v[226:227] op_sel:[0,1] op_sel_hi:[1,1]
v_pk_mul_f32 v[202:203],v[202:203],v[226:227] op_sel:[0,1] op_sel_hi:[1,1]
v_pk_mul_f32 v[204:205],v[204:205],v[226:227] op_sel:[0,1] op_sel_hi:[1,1]
v_pk_mul_f32 v[206:207],v[206:207],v[226:227] op_sel:[0,1] op_sel_hi:[1,1]
cv 200,200,201
cv 201,202,203
cv 202,204,205
cv 203,206,207
buffer_store_dwordx4 v[200:203],v244,s[8:11],s13 offen offset:3072 sc0 sc1
v_pk_mul_f32 v[208:209],v[208:209],v[226:227] op_sel:[0,1] op_sel_hi:[1,1]
v_pk_mul_f32 v[210:211],v[210:211],v[226:227] op_sel:[0,1] op_sel_hi:[1,1]
v_pk_mul_f32 v[212:213],v[212:213],v[226:227] op_sel:[0,1] op_sel_hi:[1,1]
v_pk_mul_f32 v[214:215],v[214:215],v[226:227] op_sel:[0,1] op_sel_hi:[1,1]
cv 208,208,209
cv 209,210,211
cv 210,212,213
v_mfma_f32_32x32x16_bf16 v[192:207],v[236:239],v[236:239],0
cv 211,214,215
buffer_store_dwordx4 v[208:211],v244,s[8:11],s86 offen offset:1024 sc0 sc1
v_pk_mul_f32 v[216:217],v[216:217],v[226:227] op_sel:[0,1] op_sel_hi:[1,1]
v_pk_mul_f32 v[218:219],v[218:219],v[226:227] op_sel:[0,1] op_sel_hi:[1,1]
v_pk_mul_f32 v[220:221],v[220:221],v[226:227] op_sel:[0,1] op_sel_hi:[1,1]
v_pk_mul_f32 v[222:223],v[222:223],v[226:227] op_sel:[0,1] op_sel_hi:[1,1]
cv 216,216,217
cv 217,218,219
cv 218,220,221
cv 219,222,223
buffer_store_dwordx4 v[216:219],v244,s[8:11],s86 offen offset:3072 sc0 sc1
s_nop 1
v_mfma_f32_32x32x16_bf16 v[208:223],v[236:239],v[236:239],0
L_eva_r3:
s_cmp_lg_u32 s81,14
s_cbranch_scc1 L_loop3
s_add_u32 s81,s81,1
s_bitcmp1_b32 170,s20
s_cbranch_scc1 L_odd
L_last:
L_vs1:
s_cbranch_execz L_vs5
s_lshl_b32 s86,s21,2
s_sub_u32 s87,s13,s67
s_add_u32 s86,s86,s12
mvaa 64,200,132
s_lshl_b32 s86,s86,8
v_add_u32_e32 v252,s87,v244
s_add_u32 s86,s86,L_last-L_att_pc+1024
v_lshl_add_u32 v226,v244,3,s86
s_lshl_b32 s86,s12,1
s_bfe_u32 s87,s21,65539
mvaa 64,208,136
s_add_u32 s86,s86,s87
v_min_u32_e32 v226,L_att_end-L_att_pc-4,v226
s_lshl_b32 s86,s86,7
s_add_u32 s86,s86,L_pend2-L_att_pc
v_writelane_b32 v226,s86,2
mvaa 64,216,140
s_mov_b64 exec,7
global_load_dword v253,v226,s[96:97]
s_mov_b64 exec,-1
cvaa 68,196,128,232
s_add_u32 m0,s52,32768
v_and_b32_e32 v224,16,v244
v_lshlrev_b32_e32 v224,5,v224
mvaa 68,204,132
s_xor_b32 s51,s62,0
v_lshrrev_b32_e32 v239,4,v244
v_lshrrev_b32_e32 v236,3,v239
mvaa 68,212,136
s_lshl_b32 s87,s12,13
v_and_b32_e32 v237,112,v244
v_and_b32_e32 v238,48,v239
mvaa 68,220,140
s_xor_b32 s87,s87,8192
ra 64,225,0
v_lshlrev_b32_e32 v238,1,v238
cvaa 96,224,128,232
buffer_load_dword v253,v224,s[8:11],s53 offen sc1
mvaa 96,232,132
s_mov_b32 s84,6
ra 68,225,1024
v_xor_b32_e32 v237,v237,v238
mvaa 96,240,136
s_add_u32 s81,s81,1
ra 72,225,2048
v_lshl_add_u32 v245,v236,11,v237
mvaa 96,248,140
s_mov_b32 s84,4
v_add_u32_e32 v246,15360,v245
v_add_u32_e32 v249,30720,v245
cvaa 100,228,128,232
s_andn2_b32 s69,64,s54
ra 76,225,3072
s_waitcnt vmcnt(9)
v_add_u32_e32 v247,46080,v245
mvaa 100,236,132
dl 245,24,51,0
mvaa 100,244,136
s_add_u32 s88,s14,4096
ra 80,225,4096
cv 8,8,9
mvaa 100,252,140
s_lshl_b32 s35,s19,1
ra 84,225,5120
cv 9,10,11
cvaa 72,192,144,232
s_lshr_b32 s33,40569,s35
ra 88,225,6144
cv 10,12,13
mvaa 72,200,148
s_and_b32 s33,s33,3
ra 92,225,7168
v_add_u32_e32 v250,4096,v252
mvaa 72,208,152
L_vs2:
s_cbranch_execz L_vs6
dl 246,24,51,1024
mvaa 72,216,156
s_lshl_b32 s33,s33,13
ra 96,225,8192
v_mov_b32_e32 v251,4
cvaa 76,196,144,232
s_lshr_b32 s34,59294,s35
ra 100,225,9216
s_and_b32 s34,s34,3
mvaa 76,204,148
s_lshl_b32 s34,s34,13
ra 104,225,10240
s_xor_b32 s35,s33,s34
mvaa 76,212,152
s_and_b32 s98,s19,3
ra 108,225,11264
s_lshl_b32 s98,s98,13
mvaa 76,220,156
dl 249,24,51,2048
cvaa 104,224,144,232
s_mov_b32 s74,0x300003
ra 112,225,12288
s_mov_b32 s75,0x30000300
mvaa 104,232,148
s_lshl_b32 s71,s12,15
ra 116,225,13312
s_add_u32 s71,s71,s54
mvaa 104,240,152
s_mov_b64 s[44:45],s[30:31]
ra 120,225,14336
s_mov_b32 s46,4194304
mvaa 104,248,156
s_mov_b32 s47,131072
ex 14
cvaa 108,228,144,232
dl 247,24,51,3072
mvaa 108,236,148
s_add_u32 m0,s52,49152
s_xor_b32 s51,s63,0
ra 124,225,15360
mvaa 108,244,152
s_lshl_b32 s70,s23,11
ex 15
mvaa 108,252,156
s_add_u32 s70,s70,s71
ex 40
cvaa 80,192,160,232
ex 41
mvaa 80,200,164
dl 245,24,51,0
mvaa 80,208,168
ex 42
mvaa 80,216,172
ex 43
cvaa 84,196,160,232
ex 44
mvaa 84,204,164
ex 45
mvaa 84,212,168
dl 246,24,51,1024
mvaa 84,220,172
ex 46
cvaa 112,224,160,232
ex 47
mvaa 112,232,164
ex 16
mvaa 112,240,168
ex 17
mvaa 112,248,172
dl 249,24,51,2048
cvaa 116,228,160,232
ex 18
mvaa 116,236,164
ex 19
mvaa 116,244,168
ex 20
mvaa 116,252,172
ex 21
cvaa 88,192,176,232
dl 247,24,51,3072
s_waitcnt vmcnt(8)
v_cmp_ne_u32_e32 vcc,4,v253
mvaa 88,200,180
s_cbranch_vccnz L_spbe
L_sprbe:
ex 22
mvaa 88,208,184
s_xor_b32 s50,s66,262144
ex 23
mvaa 88,216,188
ex 48
cvaa 92,196,176,232
ex 49
mvaa 92,204,180
la 128,252,50,0
mvaa 92,212,184
s_waitcnt lgkmcnt(0)
s_barrier
mvaa 92,220,188
s_mov_b32 m0,s72
ex 50
cvaa 120,224,176,232
s_lshl_b32 s72,s22,11
ex 51
mvaa 120,232,180
s_add_u32 s72,s72,s71
ex 52
mvaa 120,240,184
ds 255,8,76,0
mvaa 120,248,188
s_add_u32 s71,s70,131072
ex 53
cvaa 124,228,176,232
ex 54
mvaa 124,236,180
ex 55
mvaa 124,244,184
ex 24
mvaa 124,252,188
ds 255,8,76,1024
maav 0,64,0
s_cmp_lg_u32 s19,7
s_cbranch_scc1 L_j7e
s_nop 1
s_lshl_b32 s86,s12,12
s_add_u32 s86,s86,131072
v_add_u32_e32 v236,s86,v244
ra 160,236,0
ra 164,236,1024
ra 168,236,2048
ra 172,236,3072
ra 176,236,16384
ra 180,236,17408
ra 184,236,18432
ra 188,236,19456
v_add_u32_e32 v224,s94,v224
s_waitcnt lgkmcnt(0)
L_j7e:
ex 25
maav 0,96,4
ex 26
maav 4,68,0
ex 27
maav 4,100,4
cv 11,14,15
cv 12,40,41
maav 8,72,0
ds 255,8,76,2048
maav 8,104,4
cv 13,42,43
cv 14,44,45
maav 12,76,0
cv 15,46,47
cv 16,16,17
maav 12,108,4
ex 28
v_smfmac_f32_16x16x64_bf16 v[144:147],v[228:231],v[0:7],v248
cv 17,18,19
maav 16,80,0
ds 255,8,76,3072
maav 16,112,4
s_mov_b32 m0,s73
ex 29
maav 20,84,0
s_add_u32 s73,s72,131072
ex 30
maav 20,116,4
ex 31
maav 24,88,0
ex 56
maav 24,120,4
ds 255,8,77,0
maav 28,92,0
ex 57
maav 28,124,4
ex 58
maav 32,64,8
ex 59
maav 32,96,12
ex 60
maav 36,68,8
ds 255,8,77,1024
maav 36,100,12
cv 18,20,21
cv 19,22,23
maav 40,72,8
cv 20,48,49
cv 21,50,51
maav 40,104,12
cv 22,52,53
cv 23,54,55
maav 44,76,8
ex 61
maav 44,108,12
ds 255,8,77,2048
v_smfmac_f32_16x16x64_bf16 v[148:151],v[228:231],v[8:15],v248
cv 24,24,25
maav 48,80,8
ex 62
maav 48,112,12
ex 63
maav 52,84,8
ra 192,244,0
cv 25,26,27
maav 52,116,12
ds 255,8,77,3072
maav 56,88,8
cv 26,28,29
cv 27,30,31
maav 56,120,12
cv 28,56,57
cv 29,58,59
maav 60,92,8
cv 30,60,61
cv 31,62,63
maav 60,124,12
ex 64
mvav 160,64,16
la 132,252,50,2048
mvav 160,96,20
ex 65
mvav 164,68,16
ex 66
mvav 164,100,20
ex 67
mvav 168,72,16
ex 68
mvav 168,104,20
la 136,250,50,0
mvav 172,76,16
ex 69
mvav 172,108,20
ex 70
v_smfmac_f32_16x16x64_bf16 v[152:155],v[228:231],v[16:23],v248
ra 200,244,2048
mvav 176,80,16
ra 208,244,4096
cv 64,64,65
mvav 176,112,20
la 140,250,50,2048
mvav 180,84,16
ra 216,244,6144
cv 65,66,67
mvav 180,116,20
ex 71
mvav 184,88,16
ex 96
mvav 184,120,20
s_barrier
ex 97
mvav 188,92,16
la 156,250,50,3072
mvav 188,124,20
s_add_u32 m0,s52,131072
ra 196,244,1024
s_xor_b32 s51,s62,1048576
mvav 192,64,24
ra 204,244,3072
cv 66,68,69
mvav 192,96,28
ra 212,244,5120
cv 67,70,71
mvav 196,68,24
ra 220,244,7168
cv 68,96,97
mvav 196,100,28
la 152,250,50,1024
mvav 200,72,24
ex 98
mvav 200,104,28
ex 99
mvav 204,76,24
ra 224,244,8192
cv 69,98,99
mvav 204,108,28
ra 232,244,10240
v_add_u32_e32 v6,s87,v244
v_smfmac_f32_16x16x64_bf16 v[156:159],v[228:231],v[24:31],v248
ra 240,244,12288
mvav 208,80,24
la 148,252,50,3072
mvav 208,112,28
s_xor_b32 s87,s52,4096
ra 248,244,14336
v_add_u32_e32 v7,98304,v6
mvav 212,84,24
s_add_u32 s87,s87,150528
ra 228,244,9216
v_add_u32_e32 v5,65536,v244
mvav 212,116,28
ra 236,244,11264
v_add_u32_e32 v32,s87,v244
mvav 216,88,24
ra 244,244,13312
v_and_b32_e32 v224,960,v244
mvav 216,120,28
la 144,252,50,1024
mvav 220,92,24
s_add_u32 s50,s50,s55
ra 252,244,15360
mvav 220,124,28
ra 64,225,32768
s_waitcnt lgkmcnt(8)
cvaa 24,192,176,232
dl 245,24,51,0
mvaa 24,200,180
ra 96,225,40960
mvaa 24,208,184
ra 68,225,33792
mvaa 24,216,188
dl 246,24,51,1024
cvaa 16,192,160,128
ra 100,225,41984
mvaa 16,200,164
ra 72,225,34816
mvaa 16,208,168
dl 249,24,51,2048
mvaa 16,216,172
ra 104,225,43008
cvaa 28,196,176,232
ra 76,225,35840
mvaa 28,204,180
dl 247,24,51,3072
mvaa 28,212,184
s_add_u32 m0,s52,147456
s_xor_b32 s51,s63,1048576
ra 108,225,44032
mvaa 28,220,188
ra 80,225,36864
cvaa 20,196,160,132
dl 245,24,51,0
mvaa 20,204,164
ra 112,225,45056
mvaa 20,212,168
ra 192,244,16384
mvaa 20,220,172
dl 246,24,51,1024
cvaa 56,224,176,232
ra 200,244,18432
s_waitcnt lgkmcnt(7)
mvaa 56,232,180
ra 208,244,20480
mvaa 56,240,184
dl 249,24,51,2048
mvaa 56,248,188
ra 216,244,22528
cvaa 48,224,160,136
ra 196,244,17408
mvaa 48,232,164
ra 204,244,19456
mvaa 48,240,168
ra 212,244,21504
mvaa 48,248,172
ra 220,244,23552
cvaa 60,228,176,232
ra 224,244,24576
mvaa 60,236,180
ra 232,244,26624
mvaa 60,244,184
ra 240,244,28672
mvaa 60,252,188
ra 248,244,30720
cvaa 52,228,160,140
ra 84,225,37888
mvaa 52,236,164
ra 116,225,46080
mvaa 52,244,168
la 160,252,50,0
ra 228,244,25600
mvaa 52,252,172
ra 236,244,27648
s_waitcnt lgkmcnt(10)
cvaa 8,192,176,128
ra 244,244,29696
mvaa 8,200,180
la 164,252,50,2048
ra 252,244,31744
mvaa 8,208,184
ra 88,225,38912
mvaa 8,216,188
ra 120,225,47104
cvaa 12,196,176,132
la 168,250,50,0
mvaa 12,204,180
ra 92,225,39936
ra 124,225,48128
s_waitcnt lgkmcnt(9)
mvaa 12,212,184
ex 100
mvaa 12,220,188
la 172,250,50,2048
cvaa 40,224,176,136
ex 101
mvaa 40,232,180
ex 102
mvaa 40,240,184
ex 103
mvaa 40,248,188
cv 70,100,101
cv 71,102,103
s_waitcnt lgkmcnt(4)
cvaa 44,228,176,140
ex 72
mvaa 44,236,180
s_waitcnt vmcnt(18) lgkmcnt(0)
s_barrier
ex 73
mvaa 44,244,184
la 176,252,50,1024
mvaa 44,252,188
ex 74
maav 0,64,64
ex 75
maav 0,96,68
la 188,250,50,3072
maav 4,68,64
ra 192,5,0
cv 72,72,73
maav 4,100,68
ra 196,5,1024
cv 73,74,75
maav 8,72,64
la 184,250,50,1024
ds_write_b128 v6,a[0:3] offset:0
maav 8,104,68
ex 76
maav 12,76,64
ex 77
maav 12,108,68
la 180,252,50,3072
ds_write_b128 v6,a[4:7] offset:1024
v_smfmac_f32_16x16x64_bf16 v[144:147],v[228:231],v[64:71],v248
cv 74,76,77
maav 16,80,64
ex 78
maav 16,112,68
ds_write_b128 v6,a[8:11] offset:2048
ex 79
maav 20,84,64
ex 104
maav 20,116,68
ds_write_b128 v6,a[12:15] offset:3072
ex 105
maav 24,88,64
ex 106
ex 107
ex 108
maav 24,120,68
ds_write_b128 v6,a[16:19] offset:4096
cv 75,78,79
maav 28,92,64
ex 109
ex 110
ex 111
maav 28,124,68
cv 76,104,105
cv 77,106,107
cv 78,108,109
cv 79,110,111
maav 32,64,72
ds_write_b128 v6,a[20:23] offset:5120
maav 32,96,76
ex 80
maav 36,68,72
ex 81
maav 36,100,76
ds_write_b128 v6,a[24:27] offset:6144
maav 40,72,72
ex 82
maav 40,104,76
ex 83
maav 44,76,72
ds_write_b128 v6,a[28:31] offset:7168
maav 44,108,76
cv 80,80,81
cv 81,82,83
v_smfmac_f32_16x16x64_bf16 v[148:151],v[228:231],v[72:79],v248
ex 84
maav 48,80,72
ds_write_b128 v7,a[32:35] offset:0
maav 48,112,76
ex 85
ex 86
ex 87
maav 52,84,72
ex 112
ex 113
maav 52,116,76
ds_write_b128 v7,a[36:39] offset:1024
cv 82,84,85
maav 56,88,72
ex 114
ex 115
ex 116
maav 56,120,76
ex 117
ex 118
ex 119
maav 60,92,72
ds_write_b128 v7,a[40:43] offset:2048
cv 83,86,87
maav 60,124,76
cv 84,112,113
cv 85,114,115
cv 86,116,117
cv 87,118,119
mvav 160,64,80
ex 88
mvav 160,96,84
ds_write_b128 v7,a[44:47] offset:3072
mvav 164,68,80
v_add_f32_e32 v130,v144,v145
v_add_f32_e32 v131,v148,v149
mvav 164,100,84
ex 89
mvav 168,72,80
ds_write_b128 v7,a[48:51] offset:4096
mvav 168,104,84
ex 90
mvav 172,76,80
ex 91
mvav 172,108,84
ds_write_b128 v32,v[128:131]
v_smfmac_f32_16x16x64_bf16 v[152:155],v[228:231],v[80:87],v248
ex 92
ex 93
mvav 176,80,80
ds_write_b128 v7,a[52:55] offset:5120
ex 94
mvav 176,112,84
ra 200,5,2048
ra 204,5,3072
ex 95
cv 88,88,89
mvav 180,84,80
ra 208,5,4096
ra 212,5,5120
cv 89,90,91
cv 90,92,93
ex 120
mvav 180,116,84
ds_write_b128 v7,a[56:59] offset:6144
ex 121
mvav 184,88,80
ex 122
ex 123
cv 91,94,95
mvav 184,120,84
ex 124
ex 125
mvav 188,92,80
ds_write_b128 v7,a[60:63] offset:7168
ex 126
mvav 188,124,84
ra 216,5,6144
ra 220,5,7168
ra 224,5,8192
ra 232,5,10240
ex 127
cv 92,120,121
mvav 192,64,88
ra 240,5,12288
ra 248,5,14336
ra 228,5,9216
ra 236,5,11264
ra 244,5,13312
ra 252,5,15360
cv 93,122,123
cv 94,124,125
cv 95,126,127
v_xor_b32_e32 v6,8192,v6
s_nop 0
mvav 192,96,92
ex 16
mvav 196,68,88
s_waitcnt lgkmcnt(10)
s_xor_b32 s87,s87,0
mvav 196,100,92
s_xor_b32 s50,s66,294912
s_barrier
v_xor_b32_e32 v7,8192,v7
mvav 200,72,88
la 4,252,50,0
ra 64,5,16384
ra 96,5,24576
rv 64,6,0
rv 68,6,1024
rv 72,6,2048
mvav 200,104,92
L_vs3:
s_cbranch_execz L_vs7
rv 76,6,3072
rv 80,6,4096
rv 84,6,5120
ex 17
ex 18
mvav 204,76,88
rv 96,7,0
rv 100,7,1024
ex 19
mvav 204,108,92
la 12,252,50,2048
rv 104,7,2048
rv 108,7,3072
cv 16,16,17
v_smfmac_f32_16x16x64_bf16 v[156:159],v[228:231],v[88:95],v248
rv 112,7,4096
rv 116,7,5120
ex 20
ex 21
mvav 208,80,88
rv 120,7,6144
rv 124,7,7168
ex 22
ex 23
mvav 208,112,92
la 20,250,50,0
ra 68,5,17408
ra 100,5,25600
cv 17,18,19
cv 18,20,21
mvav 212,84,88
ra 72,5,18432
ra 104,5,26624
ex 48
ex 49
mvav 212,116,92
ra 76,5,19456
ra 108,5,27648
ex 50
ex 51
mvav 216,88,88
la 28,250,50,2048
ra 80,5,20480
ra 112,5,28672
ra 84,5,21504
mvav 216,120,92
ra 116,5,29696
cv 19,22,23
ex 52
ex 53
mvav 220,92,88
ra 88,5,22528
cv 20,48,49
ex 54
ex 55
mvav 220,124,92
la 60,250,50,3072
ra 120,5,30720
cv 21,50,51
cv 22,52,53
cv 23,54,55
s_bitcmp1_b32 s82,s81
s_cbranch_scc1 L_pend2
L_ret_eb4:
mvav 160,192,16
ra 92,5,23552
rv 88,6,6144
ra 124,5,31744
rv 92,6,7168
ex 24
s_waitcnt lgkmcnt(10)
mvav 160,224,20
ex 25
ex 26
v_smfmac_f32_16x16x64_bf16 v[152:155],v[228:231],v[16:23],v248
ex 27
v_xor_b32_e32 v32,4096,v32
mvav 164,196,16
la 52,250,50,1024
ds_read_b128 v[236:239],v32
cv 24,24,25
ex 28
mvav 164,228,20
ex 29
cv 25,26,27
ex 30
ex 31
mvav 168,200,16
ex 56
ex 57
mvav 168,232,20
la 44,252,50,3072
cv 26,28,29
ex 58
ex 59
mvav 172,204,16
ex 60
ex 61
ex 62
mvav 172,236,20
s_waitcnt lgkmcnt(0)
s_barrier
cv 27,30,31
ex 63
mvav 176,208,16
la 36,252,50,1024
cv 28,56,57
cv 29,58,59
cv 30,60,61
cv 31,62,63
mvav 176,240,20
s_add_u32 m0,s52,147456
ex 8
s_xor_b32 s51,s63,1048576
ex 9
ex 10
mvav 180,212,16
s_add_u32 s50,s50,s55
ex 11
cv 8,8,9
cv 9,10,11
ex 12
mvav 180,244,20
dl 247,24,51,3072
ex 13
ex 14
mvav 184,216,16
s_add_u32 m0,s52,0
ex 15
ex 40
ex 41
mvav 184,248,20
s_xor_b32 s51,s64,0
ex 42
ex 43
ex 44
mvav 188,220,16
la 0,252,50,0
ex 45
ex 46
mvav 188,252,20
ex 47
cv 10,12,13
cv 11,14,15
cv 12,40,41
cv 13,42,43
cv 14,44,45
v_smfmac_f32_16x16x64_bf16 v[156:159],v[228:231],v[24:31],v248
cv 15,46,47
v_bfe_u32 v225,v244,1,5
s_nop 0
v_smfmac_f32_16x16x64_bf16 v[156:159],v[228:231],v[8:15],v248
v_lshl_add_u32 v224,v224,1,v225
v_bfe_u32 v225,v244,7,2
v_lshl_add_u32 v224,v225,5,v224
mvav 192,192,24
la 8,252,50,2048
v_xor_b32_e32 v224,s69,v224
v_add_f32_e32 v152,v152,v153
v_add_f32_e32 v137,v152,v238
v_subrev_u32_e32 v236,0x23800000,v137
mvav 192,224,28
v_rcp_f32_e32 v137,v137
mvav 192,64,8
tv 128,224,32768
v_mul_f32_e32 v232,v240,v137
v_mul_f32_e32 v64,v232,v64
v_mul_f32_e32 v65,v232,v65
v_mul_f32_e32 v66,v232,v66
mvav 192,96,12
la 16,250,50,0
v_mul_f32_e32 v67,v232,v67
v_mul_f32_e32 v68,v232,v68
v_mul_f32_e32 v69,v232,v69
v_mul_f32_e32 v70,v232,v70
mvav 196,196,24
v_fmac_f32_e32 v64,v232,v160
v_fmac_f32_e32 v65,v232,v161
v_mul_f32_e32 v71,v232,v71
mvav 196,228,28
v_fmac_f32_e32 v66,v232,v162
v_fmac_f32_e32 v67,v232,v163
v_fmac_f32_e32 v68,v232,v164
v_fmac_f32_e32 v69,v232,v165
v_fmac_f32_e32 v70,v232,v166
mvav 196,68,8
la 24,250,50,2048
v_fmac_f32_e32 v71,v232,v167
cv 160,64,65
cv 161,66,67
cv 162,68,69
cv 163,70,71
mvav 196,100,12
tv 130,224,34816
tv 132,224,36864
mvav 200,200,24
tv 134,224,38912
tv 144,224,40960
v_mul_f32_e32 v72,v232,v72
v_mul_f32_e32 v73,v232,v73
v_mul_f32_e32 v74,v232,v74
mvav 200,232,28
buffer_store_dwordx4 v[160:163],v244,s[8:11],s14 offen offset:0 sc0 sc1
v_mul_f32_e32 v75,v232,v75
v_mul_f32_e32 v76,v232,v76
v_mul_f32_e32 v77,v232,v77
v_mul_f32_e32 v78,v232,v78
mvav 200,72,8
v_fmac_f32_e32 v72,v232,v168
v_fmac_f32_e32 v73,v232,v169
v_mul_f32_e32 v79,v232,v79
mvav 200,104,12
v_fmac_f32_e32 v74,v232,v170
v_fmac_f32_e32 v75,v232,v171
v_fmac_f32_e32 v76,v232,v172
v_fmac_f32_e32 v77,v232,v173
v_fmac_f32_e32 v78,v232,v174
)"
R"(mvav 204,204,24
la 56,250,50,3072
v_fmac_f32_e32 v79,v232,v175
cv 168,72,73
cv 169,74,75
cv 170,76,77
cv 171,78,79
mvav 204,236,28
tv 146,224,43008
tv 148,224,45056
mvav 204,76,8
tv 150,224,47104
v_mul_f32_e32 v80,v232,v80
v_mul_f32_e32 v81,v232,v81
v_mul_f32_e32 v82,v232,v82
mvav 204,108,12
buffer_store_dwordx4 v[168:171],v244,s[8:11],s14 offen offset:2048 sc0 sc1
v_mul_f32_e32 v83,v232,v83
v_mul_f32_e32 v84,v232,v84
v_mul_f32_e32 v85,v232,v85
v_mul_f32_e32 v86,v232,v86
v_mul_f32_e32 v87,v232,v87
mvav 208,208,24
v_fmac_f32_e32 v80,v232,v176
v_fmac_f32_e32 v81,v232,v177
v_fmac_f32_e32 v82,v232,v178
mvav 208,240,28
v_fmac_f32_e32 v83,v232,v179
v_fmac_f32_e32 v84,v232,v180
v_fmac_f32_e32 v85,v232,v181
cv 176,80,81
cv 177,82,83
mvav 208,80,8
la 48,250,50,1024
v_fmac_f32_e32 v86,v232,v182
v_fmac_f32_e32 v87,v232,v183
cv 178,84,85
cv 179,86,87
mvav 208,112,12
v_mul_f32_e32 v88,v232,v88
v_mul_f32_e32 v89,v232,v89
v_mul_f32_e32 v90,v232,v90
mvav 212,212,24
v_mul_f32_e32 v91,v232,v91
v_mul_f32_e32 v92,v232,v92
v_mul_f32_e32 v93,v232,v93
v_mul_f32_e32 v94,v232,v94
v_mul_f32_e32 v95,v232,v95
mvav 212,244,28
buffer_store_dwordx4 v[176:179],v244,s[8:11],s88 offen offset:0 sc0 sc1
v_fmac_f32_e32 v88,v232,v184
v_fmac_f32_e32 v89,v232,v185
v_fmac_f32_e32 v90,v232,v186
v_fmac_f32_e32 v91,v232,v187
cv 184,88,89
mvav 212,84,8
v_fmac_f32_e32 v92,v232,v188
cv 185,90,91
mvav 212,116,12
v_fmac_f32_e32 v93,v232,v189
v_fmac_f32_e32 v94,v232,v190
v_fmac_f32_e32 v95,v232,v191
cv 186,92,93
cv 187,94,95
mvav 216,216,24
la 40,252,50,3072
v_add_f32_e32 v156,v156,v157
v_add_f32_e32 v141,v156,v239
v_subrev_u32_e32 v237,0x23800000,v141
v_rcp_f32_e32 v141,v141
mvav 216,248,28
v_mul_f32_e32 v233,v241,v141
v_mul_f32_e32 v96,v233,v96
mvav 216,88,8
v_mul_f32_e32 v97,v233,v97
v_mul_f32_e32 v98,v233,v98
v_mul_f32_e32 v99,v233,v99
v_mul_f32_e32 v100,v233,v100
mvav 216,120,12
buffer_store_dwordx4 v[184:187],v244,s[8:11],s88 offen offset:2048 sc0 sc1
v_fmac_f32_e32 v96,v233,v192
v_fmac_f32_e32 v97,v233,v193
v_mul_f32_e32 v101,v233,v101
v_mul_f32_e32 v102,v233,v102
v_mul_f32_e32 v103,v233,v103
mvav 220,220,24
v_fmac_f32_e32 v98,v233,v194
v_fmac_f32_e32 v99,v233,v195
mvav 220,252,28
v_fmac_f32_e32 v100,v233,v196
v_fmac_f32_e32 v101,v233,v197
cv 192,96,97
cv 193,98,99
mvav 220,92,8
la 32,252,50,1024
v_fmac_f32_e32 v102,v233,v198
v_fmac_f32_e32 v103,v233,v199
cv 194,100,101
cv 195,102,103
mvav 220,124,12
v_xor_b32_e32 v225,32,v224
v_xor_b32_e32 v226,64,v224
s_waitcnt lgkmcnt(0)
zvva 0,128,128
tv 138,225,34816
tv 142,225,38912
tv 152,225,40960
s_waitcnt vmcnt(36)
mvva 0,132,132
tv 136,225,32768
tv 154,225,43008
mvva 0,144,136
tv 140,225,36864
tv 158,225,47104
mvva 0,148,140
tv 156,225,45056
v_mul_f32_e32 v104,v233,v104
v_mul_f32_e32 v105,v233,v105
v_mul_f32_e32 v106,v233,v106
zvva 4,148,156
buffer_store_dwordx4 v[192:195],v244,s[8:11],s14 offen offset:1024 sc0 sc1
v_mul_f32_e32 v107,v233,v107
v_mul_f32_e32 v108,v233,v108
v_mul_f32_e32 v109,v233,v109
v_mul_f32_e32 v110,v233,v110
mvva 4,144,152
v_fmac_f32_e32 v104,v233,v200
v_fmac_f32_e32 v105,v233,v201
v_fmac_f32_e32 v106,v233,v202
v_mul_f32_e32 v111,v233,v111
mvva 4,132,148
v_fmac_f32_e32 v107,v233,v203
v_fmac_f32_e32 v108,v233,v204
v_fmac_f32_e32 v109,v233,v205
cv 200,104,105
cv 201,106,107
mvva 4,128,144
v_fmac_f32_e32 v110,v233,v206
v_fmac_f32_e32 v111,v233,v207
cv 202,108,109
cv 203,110,111
s_waitcnt vmcnt(16)
zvva 8,128,160
v_cmp_lt_u32_e64 s[42:43],s48,v236
v_mul_f32_e32 v112,v233,v112
s_or_b64 s[40:41],s[40:41],s[42:43]
v_cmp_lt_u32_e64 s[42:43],s48,v237
mvva 8,132,164
s_or_b64 s[40:41],s[40:41],s[42:43]
v_mul_f32_e32 v113,v233,v113
v_mul_f32_e32 v114,v233,v114
v_mul_f32_e32 v115,v233,v115
mvva 8,144,168
dl 245,36,51,0
v_mul_f32_e32 v116,v233,v116
v_mul_f32_e32 v117,v233,v117
v_mul_f32_e32 v118,v233,v118
v_mul_f32_e32 v119,v233,v119
v_mul_f32_e32 v120,v233,v120
mvva 8,148,172
v_fmac_f32_e32 v112,v233,v208
v_fmac_f32_e32 v113,v233,v209
v_fmac_f32_e32 v114,v233,v210
v_fmac_f32_e32 v115,v233,v211
zvva 12,148,188
v_fmac_f32_e32 v116,v233,v212
v_fmac_f32_e32 v117,v233,v213
cv 208,112,113
cv 209,114,115
mvva 12,144,184
v_fmac_f32_e32 v118,v233,v214
v_fmac_f32_e32 v119,v233,v215
cv 210,116,117
cv 211,118,119
mvva 12,132,180
v_mul_f32_e32 v121,v233,v121
v_mul_f32_e32 v122,v233,v122
v_mul_f32_e32 v123,v233,v123
mvva 12,128,176
v_mul_f32_e32 v124,v233,v124
v_mul_f32_e32 v125,v233,v125
v_mul_f32_e32 v126,v233,v126
v_mul_f32_e32 v127,v233,v127
s_waitcnt lgkmcnt(0)
zvva 28,136,176
buffer_store_dwordx4 v[200:203],v244,s[8:11],s14 offen offset:3072 sc0 sc1
tv 144,226,40960
v_fmac_f32_e32 v120,v233,v216
v_fmac_f32_e32 v121,v233,v217
v_fmac_f32_e32 v122,v233,v218
v_fmac_f32_e32 v123,v233,v219
mvva 28,140,180
tv 128,226,32768
tv 130,226,34816
cv 216,120,121
cv 217,122,123
mvva 28,152,184
tv 132,226,36864
tv 134,226,38912
v_fmac_f32_e32 v124,v233,v220
v_fmac_f32_e32 v125,v233,v221
v_fmac_f32_e32 v126,v233,v222
mvva 28,156,188
tv 146,226,43008
v_fmac_f32_e32 v127,v233,v223
cv 218,124,125
cv 219,126,127
zvva 24,156,172
tv 148,226,45056
tv 150,226,47104
mvva 24,152,168
v_xor_b32_e32 v227,96,v224
v_add_u32_e32 v232,131072,v224
mvva 24,140,164
buffer_store_dwordx4 v[208:211],v244,s[8:11],s88 offen offset:1024 sc0 sc1
mvva 24,136,160
v_add_u32_e32 v233,131072,v225
v_add_u32_e32 v234,131072,v226
zvva 20,136,144
v_add_u32_e32 v235,131072,v227
v_mov_b32_e32 v242,83914752
mvva 20,140,148
v_add_u32_e32 v228,65536,v224
v_add_u32_e32 v229,65536,v225
mvva 20,152,152
v_add_u32_e32 v230,65536,v226
v_add_u32_e32 v231,65536,v227
mvva 20,156,156
v_and_b32_e32 v243,240,v244
v_lshlrev_b32_e32 v243,7,v243
zvva 16,156,140
buffer_store_dwordx4 v[216:219],v244,s[8:11],s88 offen offset:3072 sc0 sc1
s_cmp_lg_u64 s[40:41],0
s_cbranch_scc1 L_redo
L_redo_r4:
mvva 16,152,136
tv 156,227,45056
v_mov_b32_e32 v236,16256
mvva 16,140,132
tv 158,227,47104
v_and_b32_e32 v248,256,v244
mvva 16,136,128
tv 152,227,40960
v_lshrrev_b32_e32 v248,3,v248
s_waitcnt lgkmcnt(3)
zvva 32,128,128
tv 140,227,36864
v_add_u32_e32 v243,v243,v248
mvva 32,132,132
tv 136,227,32768
tv 138,227,34816
mvva 32,144,136
dl 246,36,51,1024
tv 142,227,38912
mvva 32,148,140
tv 154,227,43008
v_and_b32_e32 v248,512,v244
zvva 36,148,156
v_lshrrev_b32_e32 v248,5,v248
v_add_u32_e32 v243,v243,v248
mvva 36,144,152
v_and_b32_e32 v248,16,v244
v_lshlrev_b32_e32 v248,v248,v236
mvva 36,132,148
v_cndmask_b32_e64 v238,0,v248,s[74:75]
s_mov_b32 s74,0xc0000c
mvva 36,128,144
s_mov_b32 s75,0xc0000c00
v_mov_b32_e32 v240,0
v_cndmask_b32_e64 v239,0,v248,s[74:75]
zvva 40,128,160
dl 249,36,51,2048
mvva 40,132,164
v_mov_b32_e32 v241,0
v_mov_b32_e32 v236,0
mvva 40,144,168
v_mov_b32_e32 v237,0
mvva 40,148,172
zvva 44,148,188
mvva 44,144,184
mvva 44,132,180
dl 247,36,51,3072
s_add_u32 m0,s52,16384
s_xor_b32 s51,s65,0
mvva 44,128,176
tv 144,224,57344
s_waitcnt lgkmcnt(1)
zvva 60,136,176
tv 132,224,53248
tv 134,224,55296
mvva 60,140,180
tv 128,224,49152
tv 130,224,51200
mvva 60,152,184
mvva 60,156,188
tv 146,224,59392
zvva 56,156,172
dl 245,36,51,0
tv 148,224,61440
tv 150,224,63488
mvva 56,152,168
mvva 56,140,164
mvva 56,136,160
zvva 52,136,144
mvva 52,140,148
mvva 52,152,152
dl 246,36,51,1024
mvva 52,156,156
zvva 48,156,140
mvva 48,152,136
mvva 48,140,132
tv 156,225,61440
mvva 48,136,128
tv 152,225,57344
s_waitcnt lgkmcnt(2)
zvva 64,128,128
dl 249,36,51,2048
tv 140,225,53248
mvva 64,132,132
tv 136,225,49152
tv 138,225,51200
mvva 64,144,136
tv 142,225,55296
mvva 64,148,140
tv 154,225,59392
zvva 68,148,156
tv 158,225,63488
mvva 68,144,152
mvva 68,132,148
dl 247,36,51,3072
s_lshl_b32 s51,s33,5
s_add_u32 m0,s52,98304
s_xor_b32 s51,s64,s51
mvva 68,128,144
zvva 72,128,160
mvva 72,132,164
s_waitcnt vmcnt(18)
s_mov_b32 s90,0
mvva 72,144,168
s_xor_b32 s50,s67,0
mvva 72,148,172
zvva 76,148,188
dl 245,36,51,0
mvva 76,144,184
mvva 76,132,180
tv 148,226,61440
mvva 76,128,176
tv 144,226,57344
s_waitcnt lgkmcnt(2)
zvva 92,136,176
tv 132,226,53248
mvva 92,140,180
tv 128,226,49152
tv 130,226,51200
mvva 92,152,184
dl 246,36,51,1024
tv 134,226,55296
mvva 92,156,188
tv 146,226,59392
zvva 88,156,172
tv 150,226,63488
mvva 88,152,168
mvva 88,140,164
mvva 88,136,160
zvva 84,136,144
dl 249,36,51,2048
mvva 84,140,148
mvva 84,152,152
mvva 84,156,156
zvva 80,156,140
mvva 80,152,136
mvva 80,140,132
dl 247,36,51,3072
mvva 80,136,128
s_lshl_b32 s51,s33,5
tv 152,227,57344
s_add_u32 m0,s52,114688
s_waitcnt lgkmcnt(1)
zvva 96,128,128
s_xor_b32 s51,s65,s51
tv 140,227,53248
tv 142,227,55296
mvva 96,132,132
tv 136,227,49152
tv 138,227,51200
mvva 96,144,136
mvva 96,148,140
tv 154,227,59392
zvva 100,148,156
lv 164,252,50,0
tv 156,227,61440
tv 158,227,63488
mvva 100,144,152
mvva 100,132,148
mvva 100,128,144
zvva 104,128,160
mvva 104,132,164
mvva 104,144,168
lv 172,252,50,2048
mvva 104,148,172
zvva 108,148,188
mvva 108,144,184
mvva 108,132,180
mvva 108,128,176
s_barrier
s_waitcnt lgkmcnt(0)
zvva 124,136,176
lv 180,250,50,0
tv 132,232,4096
tv 144,232,8192
tv 146,232,10240
mvva 124,140,180
L_vs4:
s_cbranch_execz L_vs8
tv 128,232,0
tv 130,232,2048
mvva 124,152,184
tv 134,232,6144
mvva 124,156,188
tv 148,232,12288
tv 150,232,14336
zvva 120,156,172
mvva 120,152,168
mvva 120,140,164
lv 188,250,50,2048
mvva 120,136,160
zvva 116,136,144
mvva 116,140,148
mvva 116,152,152
mvva 116,156,156
zvva 112,156,140
lv 220,250,50,3072
mvva 112,152,136
mvva 112,140,132
tv 156,233,12288
mvva 112,136,128
tv 152,233,8192
s_waitcnt lgkmcnt(2)
mvva 0,128,4
tv 140,233,4096
mvva 0,132,12
tv 136,233,0
tv 138,233,2048
mvva 0,144,20
lv 212,250,50,1024
tv 142,233,6144
mvva 0,148,28
tv 154,233,10240
mvva 4,148,60
tv 158,233,14336
mvva 4,144,52
mvva 4,132,44
mvva 4,128,36
mvva 8,128,0
lv 204,252,50,3072
mvva 8,132,8
mvva 8,144,16
mvva 8,148,24
mvva 12,148,56
s_waitcnt vmcnt(27)
mvva 12,144,48
s_waitcnt vmcnt(25)
mvva 12,132,40
lv 196,252,50,1024
s_waitcnt vmcnt(24)
mvva 12,128,32
tv 144,234,8192
s_waitcnt lgkmcnt(1)
mvva 28,136,32
tv 132,234,4096
tv 134,234,6144
mvva 28,140,40
tv 128,234,0
tv 130,234,2048
mvva 28,152,48
mvva 28,156,56
tv 146,234,10240
mvva 24,156,24
dl 245,36,51,0
s_waitcnt vmcnt(20)
s_barrier
tv 148,234,12288
tv 150,234,14336
mvva 24,152,16
mvva 24,140,8
mvva 24,136,0
mvva 20,136,36
mvva 20,140,44
mvva 20,152,52
s_cmp_eq_u32 s12,0
s_cselect_b64 exec,1,0
buffer_store_dword v251,off,s[8:11],s61 sc0 sc1
s_mov_b64 exec,-1
mvva 20,156,60
mvva 16,156,28
mvva 16,152,20
mvva 16,140,12
tv 156,235,12288
mvva 16,136,4
tv 152,235,8192
s_waitcnt lgkmcnt(2)
mvva 32,128,4
dl 246,36,51,1024
tv 140,235,4096
mvva 32,132,12
tv 136,235,0
tv 138,235,2048
mvva 32,144,20
tv 142,235,6144
mvva 32,148,28
tv 154,235,10240
mvva 36,148,60
tv 158,235,14336
mvva 36,144,52
mvva 36,132,44
dl 249,36,51,2048
mvva 36,128,36
mvva 40,128,0
mvva 40,132,8
mvva 40,144,16
mvva 40,148,24
mvva 44,148,56
dl 247,36,51,3072
s_lshl_b32 s51,s34,5
s_add_u32 m0,s52,65536
s_xor_b32 s51,s64,s51
mvva 44,144,48
tv 148,232,28672
mvva 44,132,40
s_xor_b32 s53,s60,s33
tv 150,232,30720
mvva 44,128,32
tv 144,232,24576
s_waitcnt lgkmcnt(3)
mvva 60,136,32
tv 132,232,20480
mvva 60,140,40
tv 128,232,16384
tv 130,232,18432
mvva 60,152,48
dl 245,36,51,0
tv 134,232,22528
mvva 60,156,56
tv 146,232,26624
mvva 56,156,24
mvva 56,152,16
mvva 56,140,8
mvva 56,136,0
mvva 52,136,36
buffer_load_dword v253,v242,s[8:11],s53 offen sc1
mvva 52,140,44
mvva 52,152,52
mvva 52,156,60
mvva 48,156,28
mvva 48,152,20
mvva 48,140,12
dl 246,36,51,1024
mvva 48,136,4
tv 152,233,24576
s_waitcnt lgkmcnt(1)
mvva 64,128,4
tv 140,233,20480
tv 142,233,22528
mvva 64,132,12
tv 136,233,16384
tv 138,233,18432
mvva 64,144,20
mvva 64,148,28
tv 154,233,26624
mvva 68,148,60
dl 249,36,51,2048
tv 156,233,28672
tv 158,233,30720
mvva 68,144,52
mvva 68,132,44
mvva 68,128,36
mvva 72,128,0
mvva 72,132,8
mvva 72,144,16
dl 247,36,51,3072
s_lshl_b32 s51,s34,5
s_add_u32 m0,s52,81920
s_xor_b32 s51,s65,s51
mvva 72,148,24
mvva 76,148,56
mvva 76,144,48
mvva 76,132,40
tv 148,234,28672
mvva 76,128,32
tv 144,234,24576
s_waitcnt lgkmcnt(2)
mvva 92,136,32
dl 245,36,51,0
tv 132,234,20480
mvva 92,140,40
tv 128,234,16384
tv 130,234,18432
mvva 92,152,48
tv 134,234,22528
mvva 92,156,56
tv 146,234,26624
mvva 88,156,24
tv 150,234,30720
mvva 88,152,16
mvva 88,140,8
dl 246,36,51,1024
mvva 88,136,0
mvva 84,136,36
mvva 84,140,44
mvva 84,152,52
mvva 84,156,60
mvva 80,156,28
dl 249,36,51,2048
mvva 80,152,20
mvva 80,140,12
tv 156,235,28672
mvva 80,136,4
tv 152,235,24576
s_waitcnt lgkmcnt(2)
mvva 96,128,4
tv 140,235,20480
mvva 96,132,12
tv 136,235,16384
tv 138,235,18432
mvva 96,144,20
dl 247,36,51,3072
s_lshl_b32 s51,s35,5
s_add_u32 m0,s52,32768
s_xor_b32 s51,s64,s51
tv 142,235,22528
mvva 96,148,28
tv 154,235,26624
mvva 100,148,60
tv 158,235,30720
mvva 100,144,52
mvva 100,132,44
mvva 100,128,36
mvva 104,128,0
dl 245,36,51,0
mvva 104,132,8
mvva 104,144,16
mvva 104,148,24
mvva 108,148,56
mvva 108,144,48
mvva 108,132,40
dl 246,36,51,1024
mvva 108,128,32
s_waitcnt vmcnt(27)
s_barrier
s_waitcnt lgkmcnt(0)
mvva 124,136,32
tv 132,224,4096
tv 134,224,6144
tv 144,224,8192
mvva 124,140,40
tv 128,224,0
tv 130,224,2048
mvva 124,152,48
mvva 124,156,56
tv 146,224,10240
mvva 120,156,24
dl 249,36,51,2048
tv 148,224,12288
tv 150,224,14336
mvva 120,152,16
mvva 120,140,8
mvva 120,136,0
mvva 116,136,36
mvva 116,140,44
mvva 116,152,52
dl 247,36,51,3072
s_lshl_b32 s51,s35,5
s_add_u32 m0,s52,49152
s_xor_b32 s51,s65,s51
mvva 116,156,60
mvva 112,156,28
mvva 112,152,20
s_waitcnt vmcnt(11)
v_cmp_ne_u32_e32 vcc,4,v253
mvva 112,140,12
dl 245,36,51,0
s_cbranch_vccnz L_spa3e
L_spra3e:
mvva 112,136,4
s_xor_b32 s50,s67,s33
tv 152,225,8192
s_waitcnt lgkmcnt(1)
mvvv 0,128,164
tv 140,225,4096
tv 142,225,6144
mvvv 0,132,172
tv 136,225,0
tv 138,225,2048
mvvv 0,144,180
dl 246,36,51,1024
mvvv 0,148,188
tv 154,225,10240
mvvv 4,148,220
tv 156,225,12288
tv 158,225,14336
mvvv 4,144,212
mvvv 4,132,204
la 196,252,50,0
mvvv 4,128,196
mvvv 8,128,160
s_xor_b32 s53,s60,s34
mvvv 8,132,168
mvvv 8,144,176
la 204,252,50,2048
mvvv 8,148,184
mvvv 12,148,216
mvvv 12,144,208
mvvv 12,132,200
buffer_load_dword v253,v242,s[8:11],s53 offen sc1
mvvv 12,128,192
tv 144,226,8192
s_waitcnt lgkmcnt(1)
mvvv 28,136,192
tv 132,226,4096
tv 134,226,6144
mvvv 28,140,200
tv 128,226,0
tv 130,226,2048
mvvv 28,152,208
la 212,250,50,0
mvvv 28,156,216
tv 146,226,10240
mvvv 24,156,184
tv 148,226,12288
tv 150,226,14336
mvvv 24,152,176
mvvv 24,140,168
la 220,250,50,2048
mvvv 24,136,160
mvvv 20,136,196
mvvv 20,140,204
mvvv 20,152,212
la 252,250,50,3072
mvvv 20,156,220
mvvv 16,156,188
mvvv 16,152,180
mvvv 16,140,172
la 244,250,50,1024
mvvv 16,136,164
tv 152,227,8192
s_waitcnt lgkmcnt(1)
mvvv 32,128,164
tv 140,227,4096
tv 142,227,6144
mvvv 32,132,172
tv 136,227,0
tv 138,227,2048
mvvv 32,144,180
la 236,252,50,3072
mvvv 32,148,188
tv 154,227,10240
mvvv 36,148,220
tv 156,227,12288
tv 158,227,14336
mvvv 36,144,212
mvvv 36,132,204
la 228,252,50,1024
s_add_u32 s50,s50,s55
mvvv 36,128,196
mvvv 40,128,160
mvvv 40,132,168
mvvv 40,144,176
la 192,252,50,0
mvvv 40,148,184
mvvv 44,148,216
mvvv 44,144,208
mvvv 44,132,200
la 200,252,50,2048
mvvv 44,128,192
tv 144,224,24576
s_waitcnt lgkmcnt(1)
mvvv 60,136,192
tv 132,224,20480
tv 134,224,22528
mvvv 60,140,200
tv 128,224,16384
tv 130,224,18432
mvvv 60,152,208
la 208,250,50,0
mvvv 60,156,216
tv 146,224,26624
mvvv 56,156,184
tv 148,224,28672
tv 150,224,30720
mvvv 56,152,176
mvvv 56,140,168
la 216,250,50,2048
mvvv 56,136,160
mvvv 52,136,196
mvvv 52,140,204
mvvv 52,152,212
la 248,250,50,3072
mvvv 52,156,220
mvvv 48,156,188
mvvv 48,152,180
mvvv 48,140,172
la 240,250,50,1024
mvvv 48,136,164
tv 152,225,24576
s_waitcnt lgkmcnt(1)
mvvv 64,128,164
tv 140,225,20480
tv 142,225,22528
mvvv 64,132,172
tv 136,225,16384
tv 138,225,18432
mvvv 64,144,180
la 232,252,50,3072
mvvv 64,148,188
tv 154,225,26624
mvvv 68,148,220
tv 156,225,28672
tv 158,225,30720
mvvv 68,144,212
mvvv 68,132,204
la 224,252,50,1024
mvvv 68,128,196
mvvv 72,128,160
mvvv 72,132,168
mvvv 72,144,176
dl 249,36,51,2048
mvvv 72,148,184
mvvv 76,148,216
mvvv 76,144,208
mvvv 76,132,200
dl 247,36,51,3072
s_lshl_b32 s51,s98,5
s_xor_b32 s51,s64,s51
s_add_u32 m0,s52,131072
s_xor_b32 s51,s51,4194304
mvvv 76,128,192
tv 144,226,24576
s_waitcnt lgkmcnt(1)
mvvv 92,136,192
tv 132,226,20480
tv 134,226,22528
mvvv 92,140,200
tv 128,226,16384
tv 130,226,18432
mvvv 92,152,208
dl 245,36,51,0
mvvv 92,156,216
tv 146,226,26624
mvvv 88,156,184
tv 148,226,28672
tv 150,226,30720
mvvv 88,152,176
mvvv 88,140,168
dl 246,36,51,1024
mvvv 88,136,160
s_waitcnt vmcnt(18)
v_cmp_ne_u32_e32 vcc,4,v253
mvvv 84,136,196
s_cbranch_vccnz L_spa4e
L_spra4e:
mvvv 84,140,204
s_xor_b32 s50,s67,s34
mvvv 84,152,212
dl 249,36,51,2048
mvvv 84,156,220
mvvv 80,156,188
mvvv 80,152,180
mvvv 80,140,172
la 68,252,50,0
mvvv 80,136,164
tv 152,227,24576
s_waitcnt lgkmcnt(1)
mvvv 96,128,164
tv 140,227,20480
tv 142,227,22528
mvvv 96,132,172
tv 136,227,16384
tv 138,227,18432
mvvv 96,144,180
la 76,252,50,2048
s_xor_b32 s53,s60,s35
mvvv 96,148,188
tv 154,227,26624
mvvv 100,148,220
tv 156,227,28672
tv 158,227,30720
mvvv 100,144,212
mvvv 100,132,204
la 84,250,50,0
mvvv 100,128,196
mvvv 104,128,160
mvvv 104,132,168
mvvv 104,144,176
buffer_load_dword v253,v242,s[8:11],s53 offen sc1
mvvv 104,148,184
mvvv 108,148,216
mvvv 108,144,208
mvvv 108,132,200
la 92,250,50,2048
mvvv 108,128,192
s_barrier
s_waitcnt lgkmcnt(0)
mvvv 124,136,192
tv 132,228,36864
tv 134,228,38912
tv 144,228,40960
mvvv 124,140,200
tv 128,228,32768
tv 130,228,34816
mvvv 124,152,208
la 124,250,50,3072
mvvv 124,156,216
tv 146,228,43008
mvvv 120,156,184
tv 148,228,45056
tv 150,228,47104
mvvv 120,152,176
mvvv 120,140,168
la 116,250,50,1024
mvvv 120,136,160
mvvv 116,136,196
mvvv 116,140,204
mvvv 116,152,212
la 108,252,50,3072
mvvv 116,156,220
mvvv 112,156,188
mvvv 112,152,180
mvvv 112,140,172
la 100,252,50,1024
s_add_u32 s50,s50,s55
mvvv 112,136,164
tv 152,229,40960
s_waitcnt lgkmcnt(1)
mvva 0,128,196
tv 140,229,36864
tv 142,229,38912
mvva 0,132,204
tv 136,229,32768
tv 138,229,34816
s_waitcnt vmcnt(23)
mvva 0,144,212
la 64,252,50,0
mvva 0,148,220
tv 154,229,43008
mvva 4,148,252
tv 156,229,45056
tv 158,229,47104
mvva 4,144,244
mvva 4,132,236
la 72,252,50,2048
s_waitcnt vmcnt(23)
mvva 4,128,228
L_vs5:
s_cbranch_execz L_cw0
mvva 8,128,192
s_waitcnt vmcnt(22)
mvva 8,132,200
s_waitcnt vmcnt(21)
mvva 8,144,208
la 80,250,50,0
s_waitcnt vmcnt(21)
mvva 8,148,216
s_waitcnt vmcnt(20)
mvva 12,148,248
s_waitcnt vmcnt(19)
mvva 12,144,240
s_waitcnt vmcnt(18)
mvva 12,132,232
la 88,250,50,2048
s_waitcnt vmcnt(18)
mvva 12,128,224
tv 144,230,40960
s_waitcnt lgkmcnt(1)
mvva 28,136,224
tv 132,230,36864
tv 134,230,38912
mvva 28,140,232
tv 128,230,32768
tv 130,230,34816
mvva 28,152,240
la 120,250,50,3072
mvva 28,156,248
tv 146,230,43008
mvva 24,156,216
tv 148,230,45056
tv 150,230,47104
mvva 24,152,208
mvva 24,140,200
la 112,250,50,1024
mvva 24,136,192
mvva 20,136,228
mvva 20,140,236
mvva 20,152,244
la 104,252,50,3072
mvva 20,156,252
mvva 16,156,220
mvva 16,152,212
mvva 16,140,204
la 96,252,50,1024
mvva 16,136,196
)"
R"(tv 152,231,40960
s_waitcnt lgkmcnt(1)
mvva 32,128,196
tv 140,231,36864
tv 142,231,38912
mvva 32,132,204
tv 136,231,32768
tv 138,231,34816
mvva 32,144,212
dl 247,36,51,3072
s_lshl_b32 s51,s98,5
s_xor_b32 s51,s65,s51
s_add_u32 m0,s52,147456
s_xor_b32 s51,s51,4194304
mvva 32,148,220
tv 154,231,43008
mvva 36,148,252
tv 156,231,45056
tv 158,231,47104
mvva 36,144,244
mvva 36,132,236
dl 245,36,51,0
mvva 36,128,228
mvva 40,128,192
mvva 40,132,200
mvva 40,144,208
dl 246,36,51,1024
mvva 40,148,216
mvva 44,148,248
mvva 44,144,240
mvva 44,132,232
dl 249,36,51,2048
mvva 44,128,224
tv 144,228,57344
s_waitcnt lgkmcnt(1)
mvva 60,136,224
tv 132,228,53248
tv 134,228,55296
mvva 60,140,232
tv 128,228,49152
tv 130,228,51200
s_waitcnt vmcnt(17)
v_cmp_ne_u32_e32 vcc,4,v253
mvva 60,152,240
dl 247,36,51,3072
s_lshl_b32 s51,s98,5
s_xor_b32 s51,s64,s51
s_add_u32 m0,s52,0
s_xor_b32 s51,s51,4456448
s_cbranch_vccnz L_spa5e
L_spra5e:
mvva 60,156,248
s_xor_b32 s50,s67,s35
tv 146,228,59392
mvva 56,156,216
tv 148,228,61440
tv 150,228,63488
mvva 56,152,208
mvva 56,140,200
dl 245,36,51,0
mvva 56,136,192
mvva 52,136,228
mvva 52,140,236
mvva 52,152,244
la 132,252,50,0
mvva 52,156,252
mvva 48,156,220
s_xor_b32 s53,s60,s98
mvva 48,152,212
s_xor_b32 s53,s53,512
mvva 48,140,204
la 140,252,50,2048
mvva 48,136,196
tv 152,229,57344
s_waitcnt lgkmcnt(1)
mvva 64,128,196
tv 140,229,53248
tv 142,229,55296
mvva 64,132,204
tv 136,229,49152
tv 138,229,51200
mvva 64,144,212
buffer_load_dword v253,v242,s[8:11],s53 offen sc1
mvva 64,148,220
tv 154,229,59392
mvva 68,148,252
tv 156,229,61440
tv 158,229,63488
mvva 68,144,244
mvva 68,132,236
la 148,250,50,0
mvva 68,128,228
mvva 72,128,192
mvva 72,132,200
mvva 72,144,208
la 156,250,50,2048
mvva 72,148,216
mvva 76,148,248
mvva 76,144,240
mvva 76,132,232
la 188,250,50,3072
mvva 76,128,224
tv 144,230,57344
s_waitcnt lgkmcnt(1)
mvva 92,136,224
tv 132,230,53248
tv 134,230,55296
mvva 92,140,232
tv 128,230,49152
tv 130,230,51200
mvva 92,152,240
la 180,250,50,1024
mvva 92,156,248
tv 146,230,59392
mvva 88,156,216
tv 148,230,61440
tv 150,230,63488
mvva 88,152,208
mvva 88,140,200
la 172,252,50,3072
mvva 88,136,192
mvva 84,136,228
mvva 84,140,236
mvva 84,152,244
la 164,252,50,1024
s_add_u32 s50,s50,s55
mvva 84,156,252
mvva 80,156,220
mvva 80,152,212
mvva 80,140,204
la 128,252,50,0
mvva 80,136,196
tv 152,231,57344
s_waitcnt lgkmcnt(1)
mvva 96,128,196
tv 140,231,53248
tv 142,231,55296
mvva 96,132,204
tv 136,231,49152
tv 138,231,51200
mvva 96,144,212
la 136,252,50,2048
mvva 96,148,220
tv 154,231,59392
mvva 100,148,252
tv 156,231,61440
tv 158,231,63488
mvva 100,144,244
mvva 100,132,236
la 144,250,50,0
mvva 100,128,228
mvva 104,128,192
mvva 104,132,200
mvva 104,144,208
la 152,250,50,2048
mvva 104,148,216
mvva 108,148,248
mvva 108,144,240
mvva 108,132,232
la 184,250,50,3072
mvva 108,128,224
s_barrier
s_waitcnt lgkmcnt(0)
mvva 124,136,224
tv 132,228,4096
tv 134,228,6144
tv 144,228,8192
mvva 124,140,232
tv 128,228,0
tv 130,228,2048
mvva 124,152,240
la 176,250,50,1024
mvva 124,156,248
tv 146,228,10240
mvva 120,156,216
tv 148,228,12288
tv 150,228,14336
mvva 120,152,208
mvva 120,140,200
la 168,252,50,3072
mvva 120,136,192
mvva 116,136,228
mvva 116,140,236
mvva 116,152,244
la 160,252,50,1024
mvva 116,156,252
mvva 112,156,220
mvva 112,152,212
mvva 112,140,204
dl 246,36,51,1024
mvva 112,136,196
tv 152,229,8192
s_waitcnt lgkmcnt(1)
mvva 0,128,68
tv 140,229,4096
tv 142,229,6144
mvva 0,132,76
tv 136,229,0
tv 138,229,2048
mvva 0,144,84
dl 249,36,51,2048
s_waitcnt vmcnt(25)
mvva 0,148,92
tv 154,229,10240
mvva 4,148,124
tv 156,229,12288
tv 158,229,14336
mvva 4,144,116
mvva 4,132,108
dl 247,36,51,3072
s_lshl_b32 s51,s98,5
s_xor_b32 s51,s65,s51
s_add_u32 m0,s52,16384
s_xor_b32 s51,s51,4456448
mvva 4,128,100
mvva 8,128,64
mvva 8,132,72
mvva 8,144,80
dl 245,36,51,0
mvva 8,148,88
s_waitcnt vmcnt(18)
v_cmp_ne_u32_e32 vcc,4,v253
mvva 12,148,120
s_cbranch_vccnz L_spa6e
L_spra6e:
mvva 12,144,112
s_xor_b32 s50,s67,s98
mvva 12,132,104
dl 246,36,51,1024
s_xor_b32 s50,s50,131072
mvva 12,128,96
tv 144,230,8192
s_waitcnt lgkmcnt(1)
mvva 28,136,96
tv 132,230,4096
tv 134,230,6144
mvva 28,140,104
tv 128,230,0
tv 130,230,2048
mvva 28,152,112
la 4,252,50,0
mvva 28,156,120
tv 146,230,10240
mvva 24,156,88
tv 148,230,12288
tv 150,230,14336
mvva 24,152,80
mvva 24,140,72
la 12,252,50,2048
s_xor_b32 s53,s60,s98
s_xor_b32 s53,s53,8704
mvva 24,136,64
mvva 20,136,100
mvva 20,140,108
mvva 20,152,116
la 20,250,50,0
mvva 20,156,124
mvva 16,156,92
mvva 16,152,84
mvva 16,140,76
buffer_load_dword v253,v242,s[8:11],s53 offen sc1
mvva 16,136,68
tv 152,231,8192
s_waitcnt lgkmcnt(1)
mvva 32,128,68
tv 140,231,4096
tv 142,231,6144
mvva 32,132,76
tv 136,231,0
tv 138,231,2048
mvva 32,144,84
la 28,250,50,2048
mvva 32,148,92
tv 154,231,10240
mvva 36,148,124
tv 156,231,12288
tv 158,231,14336
mvva 36,144,116
mvva 36,132,108
la 60,250,50,3072
mvva 36,128,100
mvva 40,128,64
mvva 40,132,72
mvva 40,144,80
la 52,250,50,1024
mvva 40,148,88
mvva 44,148,120
mvva 44,144,112
mvva 44,132,104
la 44,252,50,3072
mvva 44,128,96
tv 144,228,24576
s_waitcnt lgkmcnt(1)
mvva 60,136,96
tv 132,228,20480
tv 134,228,22528
mvva 60,140,104
tv 128,228,16384
tv 130,228,18432
mvva 60,152,112
la 36,252,50,1024
s_add_u32 s50,s50,s55
mvva 60,156,120
tv 146,228,26624
mvva 56,156,88
tv 148,228,28672
tv 150,228,30720
mvva 56,152,80
mvva 56,140,72
la 0,252,50,0
mvva 56,136,64
mvva 52,136,100
mvva 52,140,108
mvva 52,152,116
la 8,252,50,2048
mvva 52,156,124
mvva 48,156,92
mvva 48,152,84
mvva 48,140,76
la 16,250,50,0
mvva 48,136,68
tv 152,229,24576
s_waitcnt lgkmcnt(1)
mvva 64,128,68
tv 140,229,20480
tv 142,229,22528
mvva 64,132,76
tv 136,229,16384
tv 138,229,18432
mvva 64,144,84
la 24,250,50,2048
mvva 64,148,92
tv 154,229,26624
mvva 68,148,124
tv 156,229,28672
tv 158,229,30720
mvva 68,144,116
mvva 68,132,108
la 56,250,50,3072
mvva 68,128,100
mvva 72,128,64
mvva 72,132,72
mvva 72,144,80
la 48,250,50,1024
mvva 72,148,88
mvva 76,148,120
mvva 76,144,112
mvva 76,132,104
la 40,252,50,3072
mvva 76,128,96
tv 144,230,24576
s_waitcnt lgkmcnt(1)
mvva 92,136,96
tv 132,230,20480
tv 134,230,22528
mvva 92,140,104
tv 128,230,16384
tv 130,230,18432
mvva 92,152,112
la 32,252,50,1024
mvva 92,156,120
tv 146,230,26624
mvva 88,156,88
tv 148,230,28672
tv 150,230,30720
mvva 88,152,80
mvva 88,140,72
dl 249,36,51,2048
mvva 88,136,64
mvva 84,136,100
mvva 84,140,108
mvva 84,152,116
dl 247,36,51,3072
s_lshl_b32 s51,s98,5
s_xor_b32 s51,s64,s51
s_add_u32 m0,s52,98304
s_xor_b32 s51,s51,4718592
mvva 84,156,124
mvva 80,156,92
mvva 80,152,84
mvva 80,140,76
dl 245,36,51,0
mvva 80,136,68
tv 152,231,24576
s_waitcnt lgkmcnt(1)
mvva 96,128,68
tv 140,231,20480
tv 142,231,22528
mvva 96,132,76
tv 136,231,16384
tv 138,231,18432
mvva 96,144,84
dl 246,36,51,1024
mvva 96,148,92
tv 154,231,26624
mvva 100,148,124
tv 156,231,28672
tv 158,231,30720
mvva 100,144,116
s_waitcnt vmcnt(17)
v_cmp_ne_u32_e32 vcc,4,v253
mvva 100,132,108
dl 249,36,51,2048
s_cbranch_vccnz L_spa7e
L_spra7e:
mvva 100,128,100
s_xor_b32 s50,s67,s98
mvva 104,128,64
s_xor_b32 s50,s50,139264
mvva 104,132,72
mvva 104,144,80
dl 247,36,51,3072
mvva 104,148,88
mvva 108,148,120
mvva 108,144,112
mvva 108,132,104
lv 164,252,50,0
mvva 108,128,96
s_barrier
s_waitcnt lgkmcnt(0)
mvva 124,136,96
s_xor_b32 s53,s60,s98
tv 132,224,36864
tv 134,224,38912
tv 144,224,40960
s_xor_b32 s53,s53,16896
mvva 124,140,104
s_lshl_b32 s51,s98,5
tv 128,224,32768
tv 130,224,34816
mvva 124,152,112
lv 172,252,50,2048
mvva 124,156,120
s_xor_b32 s51,s65,s51
tv 146,224,43008
s_add_u32 m0,s52,114688
mvva 120,156,88
s_xor_b32 s51,s51,4718592
tv 148,224,45056
tv 150,224,47104
mvva 120,152,80
mvva 120,140,72
buffer_load_dword v253,v242,s[8:11],s53 offen sc1
mvva 120,136,64
mvva 116,136,100
mvva 116,140,108
mvva 116,152,116
lv 180,250,50,0
mvva 116,156,124
mvva 112,156,92
mvva 112,152,84
mvva 112,140,76
lv 188,250,50,2048
mvva 112,136,68
tv 152,225,40960
s_waitcnt lgkmcnt(1)
mvva 0,128,132
tv 140,225,36864
tv 142,225,38912
mvva 0,132,140
tv 136,225,32768
tv 138,225,34816
mvva 0,144,148
lv 220,250,50,3072
mvva 0,148,156
tv 154,225,43008
mvva 4,148,188
tv 156,225,45056
tv 158,225,47104
mvva 4,144,180
mvva 4,132,172
lv 212,250,50,1024
mvva 4,128,164
mvva 8,128,128
mvva 8,132,136
mvva 8,144,144
lv 204,252,50,3072
mvva 8,148,152
mvva 12,148,184
mvva 12,144,176
mvva 12,132,168
lv 196,252,50,1024
s_add_u32 s50,s50,s55
mvva 12,128,160
tv 144,226,40960
s_waitcnt lgkmcnt(1)
mvva 28,136,160
tv 132,226,36864
tv 134,226,38912
mvva 28,140,168
tv 128,226,32768
tv 130,226,34816
mvva 28,152,176
lv 160,252,50,0
mvva 28,156,184
tv 146,226,43008
mvva 24,156,152
L_vs6:
s_cbranch_execz L_cw1
tv 148,226,45056
tv 150,226,47104
mvva 24,152,144
mvva 24,140,136
lv 168,252,50,2048
mvva 24,136,128
mvva 20,136,164
mvva 20,140,172
mvva 20,152,180
lv 176,250,50,0
mvva 20,156,188
mvva 16,156,156
mvva 16,152,148
mvva 16,140,140
lv 184,250,50,2048
mvva 16,136,132
tv 152,227,40960
s_waitcnt lgkmcnt(1)
mvva 32,128,132
tv 140,227,36864
tv 142,227,38912
mvva 32,132,140
tv 136,227,32768
tv 138,227,34816
mvva 32,144,148
lv 216,250,50,3072
mvva 32,148,156
tv 154,227,43008
mvva 36,148,188
tv 156,227,45056
tv 158,227,47104
mvva 36,144,180
mvva 36,132,172
lv 208,250,50,1024
mvva 36,128,164
mvva 40,128,128
mvva 40,132,136
mvva 40,144,144
lv 200,252,50,3072
mvva 40,148,152
mvva 44,148,184
mvva 44,144,176
mvva 44,132,168
lv 192,252,50,1024
mvva 44,128,160
tv 144,224,57344
s_waitcnt lgkmcnt(1)
mvva 60,136,160
tv 132,224,53248
tv 134,224,55296
mvva 60,140,168
tv 128,224,49152
tv 130,224,51200
mvva 60,152,176
dl 245,36,51,0
mvva 60,156,184
tv 146,224,59392
mvva 56,156,152
tv 148,224,61440
tv 150,224,63488
mvva 56,152,144
mvva 56,140,136
dl 246,36,51,1024
mvva 56,136,128
mvva 52,136,164
mvva 52,140,172
mvva 52,152,180
dl 249,36,51,2048
mvva 52,156,188
mvva 48,156,156
mvva 48,152,148
mvva 48,140,140
dl 247,36,51,3072
s_lshl_b32 s51,s98,5
s_xor_b32 s51,s64,s51
s_add_u32 m0,s52,65536
s_xor_b32 s51,s51,4980736
mvva 48,136,132
tv 152,225,57344
s_waitcnt vmcnt(18)
v_cmp_ne_u32_e32 vcc,4,v253
s_waitcnt lgkmcnt(1)
mvva 64,128,132
s_cbranch_vccnz L_spa8e
L_spra8e:
tv 140,225,53248
tv 142,225,55296
mvva 64,132,140
s_xor_b32 s50,s67,s98
tv 136,225,49152
s_xor_b32 s50,s50,147456
tv 138,225,51200
mvva 64,144,148
dl 245,36,51,0
mvva 64,148,156
tv 154,225,59392
mvva 68,148,188
tv 156,225,61440
tv 158,225,63488
mvva 68,144,180
mvva 68,132,172
la 196,252,50,0
mvva 68,128,164
mvva 72,128,128
mvva 72,132,136
mvva 72,144,144
la 204,252,50,2048
s_xor_b32 s53,s60,s98
s_xor_b32 s53,s53,25088
mvva 72,148,152
mvva 76,148,184
mvva 76,144,176
mvva 76,132,168
la 212,250,50,0
mvva 76,128,160
tv 144,226,57344
s_waitcnt lgkmcnt(1)
mvva 92,136,160
tv 132,226,53248
tv 134,226,55296
mvva 92,140,168
tv 128,226,49152
tv 130,226,51200
mvva 92,152,176
buffer_load_dword v253,v242,s[8:11],s53 offen sc1
mvva 92,156,184
tv 146,226,59392
mvva 88,156,152
tv 148,226,61440
tv 150,226,63488
mvva 88,152,144
mvva 88,140,136
la 220,250,50,2048
mvva 88,136,128
mvva 84,136,164
mvva 84,140,172
mvva 84,152,180
la 252,250,50,3072
mvva 84,156,188
mvva 80,156,156
mvva 80,152,148
mvva 80,140,140
la 244,250,50,1024
mvva 80,136,132
tv 152,227,57344
s_waitcnt lgkmcnt(1)
mvva 96,128,132
tv 140,227,53248
tv 142,227,55296
mvva 96,132,140
tv 136,227,49152
tv 138,227,51200
mvva 96,144,148
la 236,252,50,3072
mvva 96,148,156
tv 154,227,59392
mvva 100,148,188
tv 156,227,61440
tv 158,227,63488
mvva 100,144,180
mvva 100,132,172
la 228,252,50,1024
s_add_u32 s50,s50,s55
mvva 100,128,164
mvva 104,128,128
mvva 104,132,136
mvva 104,144,144
la 192,252,50,0
mvva 104,148,152
mvva 108,148,184
mvva 108,144,176
mvva 108,132,168
la 200,252,50,2048
mvva 108,128,160
s_barrier
s_waitcnt lgkmcnt(0)
mvva 124,136,160
tv 132,232,4096
tv 134,232,6144
tv 144,232,8192
mvva 124,140,168
tv 128,232,0
tv 130,232,2048
mvva 124,152,176
la 208,250,50,0
mvva 124,156,184
tv 146,232,10240
mvva 120,156,152
tv 148,232,12288
tv 150,232,14336
mvva 120,152,144
mvva 120,140,136
la 216,250,50,2048
mvva 120,136,128
mvva 116,136,164
mvva 116,140,172
mvva 116,152,180
la 248,250,50,3072
mvva 116,156,188
mvva 112,156,156
mvva 112,152,148
mvva 112,140,140
la 240,250,50,1024
mvva 112,136,132
tv 152,233,8192
s_waitcnt lgkmcnt(1)
mvva 0,128,4
tv 140,233,4096
tv 142,233,6144
mvva 0,132,12
tv 136,233,0
tv 138,233,2048
mvva 0,144,20
la 232,252,50,3072
mvva 0,148,28
tv 154,233,10240
mvva 4,148,60
tv 156,233,12288
tv 158,233,14336
mvva 4,144,52
mvva 4,132,44
la 224,252,50,1024
mvva 4,128,36
mvva 8,128,0
mvva 8,132,8
mvva 8,144,16
dl 246,36,51,1024
mvva 8,148,24
mvva 12,148,56
mvva 12,144,48
mvva 12,132,40
dl 249,36,51,2048
mvva 12,128,32
tv 144,234,8192
s_waitcnt lgkmcnt(1)
mvva 28,136,32
tv 132,234,4096
tv 134,234,6144
mvva 28,140,40
tv 128,234,0
tv 130,234,2048
mvva 28,152,48
dl 247,36,51,3072
s_lshl_b32 s51,s98,5
s_xor_b32 s51,s64,s51
s_add_u32 m0,s52,81920
s_xor_b32 s51,s51,6029312
mvva 28,156,56
tv 146,234,10240
mvva 24,156,24
tv 148,234,12288
tv 150,234,14336
mvva 24,152,16
mvva 24,140,8
dl 245,36,51,0
mvva 24,136,0
mvva 20,136,36
mvva 20,140,44
s_waitcnt vmcnt(17)
v_cmp_ne_u32_e32 vcc,4,v253
mvva 20,152,52
dl 246,36,51,1024
s_cbranch_vccnz L_spa9e
L_spra9e:
mvva 20,156,60
s_xor_b32 s50,s67,s98
mvva 16,156,28
s_xor_b32 s50,s50,155648
mvva 16,152,20
mvva 16,140,12
dl 249,36,51,2048
mvva 16,136,4
tv 152,235,8192
s_waitcnt lgkmcnt(1)
mvva 32,128,4
tv 140,235,4096
tv 142,235,6144
mvva 32,132,12
tv 136,235,0
tv 138,235,2048
mvva 32,144,20
dl 247,36,51,3072
mvva 32,148,28
s_lshl_b32 s51,s98,5
tv 154,235,10240
s_xor_b32 s51,s65,s51
mvva 36,148,60
s_xor_b32 s53,s60,s98
tv 156,235,12288
tv 158,235,14336
s_xor_b32 s53,s53,25216
mvva 36,144,52
s_add_u32 m0,s52,32768
s_xor_b32 s51,s51,4980736
mvva 36,132,44
la 68,252,50,0
mvva 36,128,36
mvva 40,128,0
mvva 40,132,8
mvva 40,144,16
buffer_load_dword v253,v242,s[8:11],s53 offen sc1
mvva 40,148,24
mvva 44,148,56
mvva 44,144,48
mvva 44,132,40
la 76,252,50,2048
mvva 44,128,32
tv 144,232,24576
s_waitcnt lgkmcnt(1)
mvva 60,136,32
tv 132,232,20480
tv 134,232,22528
mvva 60,140,40
tv 128,232,16384
tv 130,232,18432
mvva 60,152,48
la 84,250,50,0
mvva 60,156,56
tv 146,232,26624
mvva 56,156,24
tv 148,232,28672
tv 150,232,30720
mvva 56,152,16
mvva 56,140,8
la 92,250,50,2048
mvva 56,136,0
mvva 52,136,36
mvva 52,140,44
mvva 52,152,52
la 124,250,50,3072
mvva 52,156,60
mvva 48,156,28
mvva 48,152,20
mvva 48,140,12
la 116,250,50,1024
mvva 48,136,4
tv 152,233,24576
s_waitcnt lgkmcnt(1)
mvva 64,128,4
tv 140,233,20480
tv 142,233,22528
mvva 64,132,12
tv 136,233,16384
tv 138,233,18432
mvva 64,144,20
la 108,252,50,3072
mvva 64,148,28
tv 154,233,26624
mvva 68,148,60
tv 156,233,28672
tv 158,233,30720
mvva 68,144,52
mvva 68,132,44
la 100,252,50,1024
s_add_u32 s50,s50,s55
mvva 68,128,36
mvva 72,128,0
mvva 72,132,8
mvva 72,144,16
la 64,252,50,0
mvva 72,148,24
mvva 76,148,56
mvva 76,144,48
mvva 76,132,40
la 72,252,50,2048
mvva 76,128,32
tv 144,224,8192
s_waitcnt lgkmcnt(1)
mvva 92,136,32
tv 132,224,4096
tv 134,224,6144
mvva 92,140,40
tv 128,224,0
tv 130,224,2048
mvva 92,152,48
la 80,250,50,0
mvva 92,156,56
tv 146,224,10240
mvva 88,156,24
tv 148,224,12288
tv 150,224,14336
mvva 88,152,16
mvva 88,140,8
la 88,250,50,2048
mvva 88,136,0
mvva 84,136,36
mvva 84,140,44
mvva 84,152,52
la 120,250,50,3072
mvva 84,156,60
mvva 80,156,28
mvva 80,152,20
mvva 80,140,12
la 112,250,50,1024
mvva 80,136,4
tv 152,225,8192
s_waitcnt lgkmcnt(1)
mvvv 0,128,164
tv 140,225,4096
tv 142,225,6144
mvvv 0,132,172
tv 136,225,0
tv 138,225,2048
mvvv 0,144,180
la 104,252,50,3072
mvvv 0,148,188
tv 154,225,10240
mvvv 4,148,220
tv 156,225,12288
tv 158,225,14336
mvvv 4,144,212
mvvv 4,132,204
la 96,252,50,1024
mvvv 4,128,196
mvvv 8,128,160
mvvv 8,132,168
mvvv 8,144,176
dl 245,36,51,0
mvvv 8,148,184
mvvv 12,148,216
mvvv 12,144,208
mvvv 12,132,200
dl 246,36,51,1024
mvvv 12,128,192
s_barrier
tv 144,226,8192
s_waitcnt lgkmcnt(1)
mvvv 28,136,192
tv 132,226,4096
tv 134,226,6144
mvvv 28,140,200
tv 128,226,0
tv 130,226,2048
mvvv 28,152,208
dl 249,36,51,2048
mvvv 28,156,216
tv 146,226,10240
s_waitcnt vmcnt(18)
v_cmp_ne_u32_e32 vcc,4,v253
mvvv 24,156,184
s_cbranch_vccnz L_spa10e
L_spra10e:
tv 148,226,12288
tv 150,226,14336
mvvv 24,152,176
s_xor_b32 s50,s67,s98
v_and_b32_e32 v242,48,v244
s_xor_b32 s50,s50,188416
mvvv 24,140,168
dl 247,36,51,3072
mvvv 24,136,160
s_lshl_b32 s51,s98,5
v_lshlrev_b32_e32 v242,3,v242
s_xor_b32 s51,s65,s51
mvvv 20,136,196
s_add_u32 m0,s52,49152
s_xor_b32 s51,s51,6029312
mvvv 20,140,204
mvvv 20,152,212
la 132,252,50,0
mvvv 20,156,220
mvvv 16,156,188
s_andn2_b32 s53,s61,384
mvvv 16,152,180
mvvv 16,140,172
la 140,252,50,2048
mvvv 16,136,164
tv 152,227,8192
s_waitcnt lgkmcnt(1)
mvvv 32,128,164
tv 140,227,4096
tv 142,227,6144
mvvv 32,132,172
tv 136,227,0
tv 138,227,2048
mvvv 32,144,180
buffer_load_dword v253,v242,s[8:11],s53 offen sc1
mvvv 32,148,188
tv 154,227,10240
mvvv 36,148,220
tv 156,227,12288
tv 158,227,14336
mvvv 36,144,212
mvvv 36,132,204
la 148,250,50,0
mvvv 36,128,196
mvvv 40,128,160
mvvv 40,132,168
mvvv 40,144,176
la 156,250,50,2048
mvvv 40,148,184
mvvv 44,148,216
mvvv 44,144,208
mvvv 44,132,200
la 188,250,50,3072
mvvv 44,128,192
tv 144,224,24576
s_waitcnt lgkmcnt(1)
mvvv 60,136,192
tv 132,224,20480
tv 134,224,22528
)"
R"(mvvv 60,140,200
tv 128,224,16384
tv 130,224,18432
mvvv 60,152,208
la 180,250,50,1024
mvvv 60,156,216
tv 146,224,26624
mvvv 56,156,184
tv 148,224,28672
tv 150,224,30720
mvvv 56,152,176
mvvv 56,140,168
la 172,252,50,3072
mvvv 56,136,160
mvvv 52,136,196
mvvv 52,140,204
mvvv 52,152,212
la 164,252,50,1024
s_add_u32 s50,s50,s55
mvvv 52,156,220
mvvv 48,156,188
mvvv 48,152,180
mvvv 48,140,172
la 128,252,50,0
mvvv 48,136,164
tv 152,225,24576
s_waitcnt lgkmcnt(1)
mvvv 64,128,164
tv 140,225,20480
tv 142,225,22528
mvvv 64,132,172
tv 136,225,16384
tv 138,225,18432
mvvv 64,144,180
la 136,252,50,2048
mvvv 64,148,188
L_vs7:
s_cbranch_execz L_cw2
tv 154,225,26624
mvvv 68,148,220
tv 156,225,28672
tv 158,225,30720
mvvv 68,144,212
mvvv 68,132,204
la 144,250,50,0
mvvv 68,128,196
mvvv 72,128,160
mvvv 72,132,168
mvvv 72,144,176
la 152,250,50,2048
mvvv 72,148,184
mvvv 76,148,216
mvvv 76,144,208
mvvv 76,132,200
la 184,250,50,3072
mvvv 76,128,192
tv 144,228,40960
s_waitcnt lgkmcnt(1)
mvvv 92,136,192
tv 132,228,36864
tv 134,228,38912
mvvv 92,140,200
tv 128,228,32768
tv 130,228,34816
mvvv 92,152,208
la 176,250,50,1024
mvvv 92,156,216
tv 146,228,43008
mvvv 88,156,184
tv 148,228,45056
tv 150,228,47104
mvvv 88,152,176
mvvv 88,140,168
la 168,252,50,3072
mvvv 88,136,160
mvvv 84,136,196
mvvv 84,140,204
mvvv 84,152,212
la 160,252,50,1024
mvvv 84,156,220
mvvv 80,156,188
mvvv 80,152,180
mvvv 80,140,172
dl 245,36,51,0
mvvv 80,136,164
tv 152,229,40960
s_waitcnt lgkmcnt(1)
mvva 0,128,196
tv 140,229,36864
tv 142,229,38912
mvva 0,132,204
tv 136,229,32768
tv 138,229,34816
mvva 0,144,212
dl 246,36,51,1024
mvva 0,148,220
tv 154,229,43008
mvva 4,148,252
tv 156,229,45056
tv 158,229,47104
mvva 4,144,244
mvva 4,132,236
dl 249,36,51,2048
mvva 4,128,228
mvva 8,128,192
mvva 8,132,200
mvva 8,144,208
dl 247,36,51,3072
mvva 8,148,216
s_waitcnt vmcnt(18)
v_cmp_ne_u32_e32 vcc,4,v253
mvva 12,148,248
s_cbranch_vccnz L_sppe
L_sprpe:
mvva 12,144,240
mvva 12,132,232
tv 148,230,45056
mvva 12,128,224
tv 144,230,40960
s_waitcnt lgkmcnt(2)
mvva 28,136,224
tv 132,230,36864
mvva 28,140,232
tv 128,230,32768
tv 130,230,34816
mvva 28,152,240
tv 134,230,38912
mvva 28,156,248
tv 146,230,43008
mvva 24,156,216
tv 150,230,47104
mvva 24,152,208
mvva 24,140,200
mvva 24,136,192
mvva 20,136,228
mvva 20,140,236
mvva 20,152,244
mvva 20,156,252
mvva 16,156,220
mvva 16,152,212
mvva 16,140,204
tv 156,231,45056
mvva 16,136,196
tv 152,231,40960
s_waitcnt lgkmcnt(2)
mvva 32,128,196
tv 140,231,36864
mvva 32,132,204
tv 136,231,32768
tv 138,231,34816
mvva 32,144,212
tv 142,231,38912
mvva 32,148,220
tv 154,231,43008
mvva 36,148,252
tv 158,231,47104
mvva 36,144,244
mvva 36,132,236
mvva 36,128,228
mvva 40,128,192
mvva 40,132,200
mvva 40,144,208
mvva 40,148,216
mvva 44,148,248
mvva 44,144,240
mvva 44,132,232
mvva 44,128,224
s_barrier
s_waitcnt lgkmcnt(0)
mvva 60,136,224
tv 132,228,53248
tv 144,228,57344
mvva 60,140,232
tv 128,228,49152
tv 130,228,51200
mvva 60,152,240
tv 134,228,55296
mvva 60,156,248
tv 146,228,59392
mvva 56,156,216
tv 148,228,61440
tv 150,228,63488
mvva 56,152,208
mvva 56,140,200
mvva 56,136,192
mvva 52,136,228
mvva 52,140,236
mvva 52,152,244
mvva 52,156,252
mvva 48,156,220
mvva 48,152,212
mvva 48,140,204
tv 156,229,61440
mvva 48,136,196
tv 152,229,57344
s_waitcnt lgkmcnt(2)
mvva 64,128,196
tv 140,229,53248
mvva 64,132,204
tv 136,229,49152
tv 138,229,51200
mvva 64,144,212
tv 142,229,55296
mvva 64,148,220
tv 154,229,59392
mvva 68,148,252
tv 158,229,63488
mvva 68,144,244
mvva 68,132,236
mvva 68,128,228
mvva 72,128,192
mvva 72,132,200
mvva 72,144,208
mvva 72,148,216
mvva 76,148,248
mvva 76,144,240
mvva 76,132,232
tv 148,228,12288
mvva 76,128,224
tv 144,228,8192
s_waitcnt lgkmcnt(2)
mvva 92,136,224
tv 132,228,4096
mvva 92,140,232
tv 128,228,0
tv 130,228,2048
mvva 92,152,240
tv 134,228,6144
mvva 92,156,248
tv 146,228,10240
mvva 88,156,216
tv 150,228,14336
mvva 88,152,208
mvva 88,140,200
mvva 88,136,192
mvva 84,136,228
mvva 84,140,236
mvva 84,152,244
mvva 84,156,252
mvva 80,156,220
mvva 80,152,212
mvva 80,140,204
tv 156,229,12288
mvva 80,136,196
tv 152,229,8192
s_waitcnt lgkmcnt(2)
mvva 0,128,68
tv 140,229,4096
mvva 0,132,76
tv 136,229,0
tv 138,229,2048
mvva 0,144,84
tv 142,229,6144
mvva 0,148,92
tv 154,229,10240
mvva 4,148,124
tv 158,229,14336
mvva 4,144,116
mvva 4,132,108
mvva 4,128,100
mvva 8,128,64
mvva 8,132,72
mvva 8,144,80
mvva 8,148,88
mvva 12,148,120
mvva 12,144,112
mvva 12,132,104
tv 148,228,28672
mvva 12,128,96
tv 144,228,24576
s_waitcnt lgkmcnt(2)
mvva 28,136,96
tv 132,228,20480
mvva 28,140,104
tv 128,228,16384
tv 130,228,18432
mvva 28,152,112
tv 134,228,22528
mvva 28,156,120
tv 146,228,26624
mvva 24,156,88
tv 150,228,30720
mvva 24,152,80
mvva 24,140,72
mvva 24,136,64
mvva 20,136,100
mvva 20,140,108
mvva 20,152,116
mvva 20,156,124
mvva 16,156,92
mvva 16,152,84
mvva 16,140,76
tv 156,229,28672
mvva 16,136,68
tv 152,229,24576
s_waitcnt lgkmcnt(2)
mvva 0,128,132
tv 140,229,20480
mvva 0,132,140
tv 136,229,16384
tv 138,229,18432
s_waitcnt vmcnt(4)
mvva 0,144,148
tv 142,229,22528
mvva 0,148,156
tv 154,229,26624
mvva 4,148,188
tv 158,229,30720
mvva 4,144,180
mvva 4,132,172
mvva 4,128,164
mvva 8,128,128
mvva 8,132,136
cv 0,0,1
cv 1,2,3
mvva 8,144,144
mvva 8,148,152
mvva 12,148,184
mvva 12,144,176
cv 4,4,5
cv 5,6,7
mvva 12,132,168
tv 148,230,12288
mvva 12,128,160
tv 144,230,8192
cv 8,8,9
s_waitcnt lgkmcnt(2)
mvva 28,136,160
tv 132,230,4096
cv 9,10,11
mvva 28,140,168
tv 128,230,0
tv 130,230,2048
mvva 28,152,176
tv 134,230,6144
cv 12,12,13
mvva 28,156,184
tv 146,230,10240
cv 13,14,15
mvva 24,156,152
tv 150,230,14336
mvva 24,152,144
mvva 24,140,136
mvva 24,136,128
mvva 20,136,164
cv 14,28,29
cv 15,30,31
mvva 20,140,172
buffer_store_dwordx4 v[12:15],v244,s[8:11],s56 offen offset:3072 sc0 sc1
mvva 20,152,180
mvva 20,156,188
mvva 16,156,156
cv 10,24,25
cv 11,26,27
mvva 16,152,148
buffer_store_dwordx4 v[8:11],v244,s[8:11],s56 offen offset:2048 sc0 sc1
mvva 16,140,140
tv 156,231,12288
mvva 16,136,132
cv 6,20,21
cv 7,22,23
s_waitcnt lgkmcnt(1)
mvva 32,128,68
tv 140,231,4096
mvva 32,132,76
buffer_store_dwordx4 v[4:7],v244,s[8:11],s56 offen offset:1024 sc0 sc1
tv 136,231,0
tv 138,231,2048
mvva 32,144,84
tv 142,231,6144
cv 2,16,17
mvva 32,148,92
tv 152,231,8192
tv 154,231,10240
mvva 36,148,124
tv 158,231,14336
cv 3,18,19
mvva 36,144,116
buffer_store_dwordx4 v[0:3],v244,s[8:11],s56 offen offset:0 sc0 sc1
mvva 36,132,108
mvva 36,128,100
mvva 40,128,64
mvva 40,132,72
mvva 40,144,80
mvva 40,148,88
mvva 44,148,120
mvva 44,144,112
mvva 44,132,104
tv 148,230,28672
mvva 44,128,96
s_waitcnt vmcnt(4)
s_barrier
tv 144,230,24576
s_waitcnt lgkmcnt(2)
mvva 60,136,96
tv 132,230,20480
mvva 60,140,104
tv 128,230,16384
tv 130,230,18432
mvva 60,152,112
tv 134,230,22528
mvva 60,156,120
tv 146,230,26624
mvva 56,156,88
tv 150,230,30720
mvva 56,152,80
mvva 56,140,72
mvva 56,136,64
mvva 52,136,100
mvva 52,140,108
mvva 52,152,116
mvva 52,156,124
mvva 48,156,92
mvva 48,152,84
mvva 48,140,76
tv 156,231,28672
mvva 48,136,68
tv 152,231,24576
s_waitcnt lgkmcnt(2)
mvva 32,128,132
tv 140,231,20480
mvva 32,132,140
tv 136,231,16384
tv 138,231,18432
mvva 32,144,148
tv 142,231,22528
mvva 32,148,156
tv 154,231,26624
mvva 36,148,188
tv 158,231,30720
mvva 36,144,180
mvva 36,132,172
mvva 36,128,164
mvva 40,128,128
mvva 40,132,136
cv 32,32,33
cv 33,34,35
mvva 40,144,144
mvva 40,148,152
mvva 44,148,184
mvva 44,144,176
cv 36,36,37
cv 37,38,39
mvva 44,132,168
tv 148,224,45056
mvva 44,128,160
tv 144,224,40960
cv 40,40,41
s_waitcnt lgkmcnt(2)
mvva 60,136,160
tv 132,224,36864
cv 41,42,43
mvva 60,140,168
tv 128,224,32768
tv 130,224,34816
mvva 60,152,176
tv 134,224,38912
cv 44,44,45
mvva 60,156,184
tv 146,224,43008
cv 45,46,47
mvva 56,156,152
tv 150,224,47104
mvva 56,152,144
mvva 56,140,136
mvva 56,136,128
mvva 52,136,164
cv 46,60,61
cv 47,62,63
mvva 52,140,172
buffer_store_dwordx4 v[44:47],v244,s[8:11],s57 offen offset:3072 sc0 sc1
mvva 52,152,180
mvva 52,156,188
mvva 48,156,156
cv 42,56,57
cv 43,58,59
mvva 48,152,148
buffer_store_dwordx4 v[40:43],v244,s[8:11],s57 offen offset:2048 sc0 sc1
mvva 48,140,140
tv 156,225,45056
mvva 48,136,132
cv 38,52,53
cv 39,54,55
s_waitcnt lgkmcnt(1)
mvva 64,128,68
tv 140,225,36864
mvva 64,132,76
buffer_store_dwordx4 v[36:39],v244,s[8:11],s57 offen offset:1024 sc0 sc1
tv 136,225,32768
tv 138,225,34816
mvva 64,144,84
tv 142,225,38912
cv 34,48,49
mvva 64,148,92
tv 152,225,40960
tv 154,225,43008
mvva 68,148,124
tv 158,225,47104
cv 35,50,51
mvva 68,144,116
buffer_store_dwordx4 v[32:35],v244,s[8:11],s57 offen offset:0 sc0 sc1
mvva 68,132,108
mvva 68,128,100
mvva 72,128,64
mvva 72,132,72
mvva 72,144,80
mvva 72,148,88
mvva 76,148,120
mvva 76,144,112
mvva 76,132,104
tv 148,224,61440
mvva 76,128,96
tv 144,224,57344
s_waitcnt lgkmcnt(2)
mvva 92,136,96
tv 132,224,53248
mvva 92,140,104
tv 128,224,49152
tv 130,224,51200
mvva 92,152,112
tv 134,224,55296
mvva 92,156,120
tv 146,224,59392
mvva 88,156,88
tv 150,224,63488
mvva 88,152,80
mvva 88,140,72
mvva 88,136,64
mvva 84,136,100
mvva 84,140,108
mvva 84,152,116
mvva 84,156,124
mvva 80,156,92
mvva 80,152,84
mvva 80,140,76
tv 156,225,61440
mvva 80,136,68
tv 152,225,57344
s_waitcnt lgkmcnt(2)
mvva 64,128,132
tv 140,225,53248
mvva 64,132,140
tv 136,225,49152
tv 138,225,51200
mvva 64,144,148
tv 142,225,55296
mvva 64,148,156
tv 154,225,59392
mvva 68,148,188
tv 158,225,63488
mvva 68,144,180
mvva 68,132,172
mvva 68,128,164
s_xor_b32 s82,s59,12
mvva 72,128,128
s_mul_i32 s82,s82,12288
cv 64,64,65
s_add_u32 s82,s82,0
mvva 72,132,136
lv 16,244,82,0
mvva 72,144,144
cv 65,66,67
cv 68,68,69
mvva 72,148,152
L_vs8:
s_cbranch_execz L_cw3
cv 69,70,71
mvva 76,148,184
mvva 76,144,176
lv 20,244,82,1024
mvva 76,132,168
tv 148,234,28672
tv 150,234,30720
mvva 76,128,160
cv 72,72,73
cv 73,74,75
s_waitcnt lgkmcnt(2)
mvva 92,136,160
tv 132,234,20480
mvva 92,140,168
lv 24,244,82,2048
tv 128,234,16384
tv 130,234,18432
mvva 92,152,176
tv 134,234,22528
cv 76,76,77
mvva 92,156,184
tv 144,234,24576
tv 146,234,26624
mvva 88,156,152
cv 77,78,79
mvva 88,152,144
lv 28,244,82,3072
mvva 88,140,136
mvva 88,136,128
cv 78,92,93
cv 79,94,95
mvva 84,136,164
mvva 84,140,172
buffer_store_dwordx4 v[76:79],v244,s[8:11],s58 offen offset:3072 sc0 sc1
mvva 84,152,180
mvva 84,156,188
mvva 80,156,156
cv 74,88,89
cv 75,90,91
mvva 80,152,148
buffer_store_dwordx4 v[72:75],v244,s[8:11],s58 offen offset:2048 sc0 sc1
mvva 80,140,140
tv 156,235,28672
mvva 80,136,132
cv 70,84,85
cv 71,86,87
s_waitcnt lgkmcnt(1)
mvva 96,128,4
tv 140,235,20480
mvva 96,132,12
buffer_store_dwordx4 v[68:71],v244,s[8:11],s58 offen offset:1024 sc0 sc1
tv 136,235,16384
tv 138,235,18432
mvva 96,144,20
tv 142,235,22528
cv 66,80,81
mvva 96,148,28
tv 152,235,24576
tv 154,235,26624
mvva 100,148,60
tv 158,235,30720
cv 67,82,83
mvva 100,144,52
buffer_store_dwordx4 v[64:67],v244,s[8:11],s58 offen offset:0 sc0 sc1
mvva 100,132,44
mvva 100,128,36
mvva 104,128,0
mvva 104,132,8
mvva 104,144,16
mvva 104,148,24
mvva 108,148,56
mvva 108,144,48
mvva 108,132,40
tv 148,226,28672
mvva 108,128,32
tv 144,226,24576
s_waitcnt lgkmcnt(2)
mvva 124,136,32
tv 132,226,20480
mvva 124,140,40
tv 128,226,16384
tv 130,226,18432
mvva 124,152,48
tv 134,226,22528
mvva 124,156,56
tv 146,226,26624
mvva 120,156,24
tv 150,226,30720
mvva 120,152,16
mvva 120,140,8
mvva 120,136,0
mvva 116,136,36
mvva 116,140,44
s_waitcnt vmcnt(7)
v_max_u32_e32 v248,v16,v17
mvva 116,152,52
v_max3_u32 v248,v248,v18,v19
s_waitcnt vmcnt(6)
v_max3_u32 v248,v248,v20,v21
v_max3_u32 v248,v248,v22,v23
mvva 116,156,60
s_waitcnt vmcnt(5)
v_max3_u32 v248,v248,v24,v25
v_max3_u32 v248,v248,v26,v27
s_waitcnt vmcnt(4)
v_max3_u32 v248,v248,v28,v29
mvva 112,156,28
v_max3_u32 v248,v248,v30,v31
v_cmp_eq_u32_e32 vcc,-1,v248
mvva 112,152,20
s_cbranch_vccnz L_spx0e
L_sprx0e:
mvva 112,140,12
tv 156,227,28672
tv 158,227,30720
v_lshlrev_b32_e32 v251,16,v18
v_and_b32_e32 v255,4294901760,v18
mvva 112,136,4
tv 152,227,24576
tv 154,227,26624
s_waitcnt lgkmcnt(4)
mvvv 96,128,164
v_lshlrev_b32_e32 v253,16,v19
v_and_b32_e32 v242,4294901760,v19
mvvv 96,132,172
tv 136,227,16384
tv 138,227,18432
mvvv 96,144,180
tv 140,227,20480
tv 142,227,22528
v_add_f32_e32 v112,v112,v251
mvvv 96,148,188
v_add_f32_e32 v113,v113,v255
v_add_f32_e32 v114,v114,v253
mvvv 100,148,220
v_add_f32_e32 v115,v115,v242
v_lshlrev_b32_e32 v251,16,v22
mvvv 100,144,212
v_and_b32_e32 v255,4294901760,v22
v_lshlrev_b32_e32 v253,16,v23
mvvv 100,132,204
v_and_b32_e32 v242,4294901760,v23
v_add_f32_e32 v116,v116,v251
v_add_f32_e32 v117,v117,v255
mvvv 100,128,196
v_add_f32_e32 v118,v118,v253
v_add_f32_e32 v119,v119,v242
mvvv 104,128,160
v_lshlrev_b32_e32 v251,16,v26
v_and_b32_e32 v255,4294901760,v26
mvvv 104,132,168
v_lshlrev_b32_e32 v253,16,v27
v_and_b32_e32 v242,4294901760,v27
mvvv 104,144,176
v_add_f32_e32 v120,v120,v251
v_add_f32_e32 v121,v121,v255
v_add_f32_e32 v122,v122,v253
mvvv 104,148,184
v_add_f32_e32 v123,v123,v242
v_lshlrev_b32_e32 v251,16,v30
mvvv 108,148,216
v_and_b32_e32 v255,4294901760,v30
v_lshlrev_b32_e32 v253,16,v31
mvvv 108,144,208
v_and_b32_e32 v242,4294901760,v31
v_add_f32_e32 v124,v124,v251
mvvv 108,132,200
v_add_f32_e32 v125,v125,v255
v_add_f32_e32 v126,v126,v253
v_add_f32_e32 v127,v127,v242
mvvv 108,128,192
tv 144,230,57344
v_lshlrev_b32_e32 v251,16,v16
s_waitcnt lgkmcnt(1)
mvvv 124,136,192
tv 132,230,53248
v_and_b32_e32 v255,4294901760,v16
mvvv 124,140,200
tv 128,230,49152
tv 130,230,51200
mvvv 124,152,208
tv 134,230,55296
v_lshlrev_b32_e32 v253,16,v17
mvvv 124,156,216
tv 146,230,59392
v_and_b32_e32 v242,4294901760,v17
mvvv 120,156,184
tv 148,230,61440
tv 150,230,63488
mvvv 120,152,176
v_add_f32_e32 v96,v96,v251
v_add_f32_e32 v97,v97,v255
mvvv 120,140,168
v_add_f32_e32 v98,v98,v253
v_add_f32_e32 v99,v99,v242
mvvv 120,136,160
v_lshlrev_b32_e32 v251,16,v20
v_and_b32_e32 v255,4294901760,v20
mvvv 116,136,196
v_lshlrev_b32_e32 v253,16,v21
v_and_b32_e32 v242,4294901760,v21
mvvv 116,140,204
v_add_f32_e32 v100,v100,v251
v_add_f32_e32 v101,v101,v255
mvvv 116,152,212
v_add_f32_e32 v102,v102,v253
v_add_f32_e32 v103,v103,v242
mvvv 116,156,220
v_lshlrev_b32_e32 v251,16,v24
v_and_b32_e32 v255,4294901760,v24
mvvv 112,156,188
v_lshlrev_b32_e32 v253,16,v25
v_and_b32_e32 v242,4294901760,v25
mvvv 112,152,180
v_add_f32_e32 v104,v104,v251
v_add_f32_e32 v105,v105,v255
mvvv 112,140,172
tv 156,231,61440
v_add_f32_e32 v106,v106,v253
mvvv 112,136,164
tv 152,231,57344
v_add_f32_e32 v107,v107,v242
s_waitcnt lgkmcnt(2)
mvva 96,128,196
tv 140,231,53248
v_lshlrev_b32_e32 v251,16,v28
mvva 96,132,204
tv 136,231,49152
tv 138,231,51200
mvva 96,144,212
tv 142,231,55296
v_and_b32_e32 v255,4294901760,v28
mvva 96,148,220
tv 154,231,59392
v_lshlrev_b32_e32 v253,16,v29
mvva 100,148,252
tv 158,231,63488
v_and_b32_e32 v242,4294901760,v29
mvva 100,144,244
v_add_f32_e32 v108,v108,v251
v_add_f32_e32 v109,v109,v255
mvva 100,132,236
v_add_f32_e32 v110,v110,v253
v_add_f32_e32 v111,v111,v242
mvva 100,128,228
mvva 104,128,192
mvva 104,132,200
mvva 104,144,208
mvva 104,148,216
mvva 108,148,248
mvva 108,144,240
mvva 108,132,232
tv 148,226,45056
mvva 108,128,224
tv 144,226,40960
s_waitcnt lgkmcnt(2)
mvva 124,136,224
s_xor_b32 s83,s59,8
tv 132,226,36864
s_mul_i32 s83,s83,12288
mvva 124,140,232
s_add_u32 s83,s83,4096
tv 128,226,32768
tv 130,226,34816
mvva 124,152,240
lv 48,244,83,0
tv 134,226,38912
mvva 124,156,248
tv 146,226,43008
mvva 120,156,216
lv 52,244,83,1024
tv 150,226,47104
mvva 120,152,208
mvva 120,140,200
lv 56,244,83,2048
mvva 120,136,192
mvva 116,136,228
lv 60,244,83,3072
mvva 116,140,236
mvva 116,152,244
s_xor_b32 s84,s59,4
mvva 116,156,252
s_mul_i32 s84,s84,12288
mvva 112,156,220
s_add_u32 s84,s84,8192
mvva 112,152,212
lv 80,244,84,0
mvva 112,140,204
tv 156,227,45056
mvva 112,136,196
lv 84,244,84,1024
tv 152,227,40960
s_waitcnt lgkmcnt(2)
mvva 96,128,68
tv 140,227,36864
mvva 96,132,76
lv 88,244,84,2048
tv 136,227,32768
tv 138,227,34816
mvva 96,144,84
tv 142,227,38912
mvva 96,148,92
lv 92,244,84,3072
tv 154,227,43008
mvva 100,148,124
tv 158,227,47104
mvva 100,144,116
mvva 100,132,108
mvva 100,128,100
mvva 104,128,64
mvva 104,132,72
mvva 104,144,80
mvva 104,148,88
mvva 108,148,120
mvva 108,144,112
mvva 108,132,104
tv 148,226,61440
mvva 108,128,96
tv 144,226,57344
s_waitcnt lgkmcnt(2)
mvva 124,136,96
tv 132,226,53248
mvva 124,140,104
tv 128,226,49152
tv 130,226,51200
mvva 124,152,112
tv 134,226,55296
mvva 124,156,120
tv 146,226,59392
mvva 120,156,88
tv 150,226,63488
mvva 120,152,80
mvva 120,140,72
mvva 120,136,64
mvva 116,136,100
mvva 116,140,108
mvva 116,152,116
mvva 116,156,124
mvva 112,156,92
mvva 112,152,84
mvva 112,140,76
tv 156,227,61440
mvva 112,136,68
tv 152,227,57344
s_waitcnt lgkmcnt(2)
mvva 96,128,132
tv 140,227,53248
mvva 96,132,140
tv 136,227,49152
tv 138,227,51200
mvva 96,144,148
tv 142,227,55296
mvva 96,148,156
tv 154,227,59392
mvva 100,148,188
tv 158,227,63488
mvva 100,144,180
mvva 100,132,172
mvva 100,128,164
mvva 104,128,128
mvva 104,132,136
mvva 104,144,144
mvva 104,148,152
s_waitcnt vmcnt(7)
v_max_u32_e32 v248,v48,v49
mvva 108,148,184
v_max3_u32 v248,v248,v50,v51
s_waitcnt vmcnt(6)
v_max3_u32 v248,v248,v52,v53
mvva 108,144,176
v_max3_u32 v248,v248,v54,v55
s_waitcnt vmcnt(5)
v_max3_u32 v248,v248,v56,v57
mvva 108,132,168
v_max3_u32 v248,v248,v58,v59
s_waitcnt vmcnt(4)
v_max3_u32 v248,v248,v60,v61
mvva 108,128,160
v_max3_u32 v248,v248,v62,v63
v_cmp_eq_u32_e32 vcc,-1,v248
s_waitcnt lgkmcnt(0)
mvva 124,136,160
s_cbranch_vccnz L_spx1e
L_sprx1e:
mvva 124,140,168
mvva 124,152,176
mvva 124,156,184
s_waitcnt vmcnt(3)
v_max_u32_e32 v248,v80,v81
mvva 120,156,152
v_max3_u32 v248,v248,v82,v83
s_waitcnt vmcnt(2)
v_max3_u32 v248,v248,v84,v85
mvva 120,152,144
v_max3_u32 v248,v248,v86,v87
s_waitcnt vmcnt(1)
v_max3_u32 v248,v248,v88,v89
mvva 120,140,136
v_max3_u32 v248,v248,v90,v91
s_waitcnt vmcnt(0)
v_max3_u32 v248,v248,v92,v93
mvva 120,136,128
v_max3_u32 v248,v248,v94,v95
v_cmp_eq_u32_e32 vcc,-1,v248
mvva 116,136,164
s_cbranch_vccnz L_spx2e
L_sprx2e:
mvva 116,140,172
mvva 116,152,180
mvva 116,156,188
mvva 112,156,156
mvva 112,152,148
mvva 112,140,140
mvva 112,136,132
mvvv 108,238,60
mvvv 108,238,92
mvvv 124,236,60
mvvv 124,236,92
mvvv 104,238,56
mvvv 104,238,88
mvvv 120,236,56
mvvv 120,236,88
mvvv 100,238,52
mvvv 100,238,84
cv 108,108,109
cv 109,110,111
cv 110,124,125
cv 111,126,127
cv 104,104,105
v_permlane16_swap_b32_e32 v108,v110
mvvv 116,236,52
v_permlane16_swap_b32_e32 v109,v111
cv 105,106,107
cv 106,120,121
)"
R"(cv 107,122,123
mvvv 116,236,84
buffer_store_dwordx4 v[108:111],v243,s[44:47],s73 offen
v_permlane16_swap_b32_e32 v104,v106
v_permlane16_swap_b32_e32 v105,v107
mvvv 96,238,48
cv 100,100,101
cv 101,102,103
mvvv 96,238,80
buffer_store_dwordx4 v[104:107],v243,s[44:47],s72 offen
cv 102,116,117
cv 103,118,119
mvvv 112,236,48
v_permlane16_swap_b32_e32 v100,v102
v_permlane16_swap_b32_e32 v101,v103
mvvv 112,236,80
buffer_store_dwordx4 v[100:103],v243,s[44:47],s71 offen
cv 96,96,97
cv 97,98,99
s_nop 4
cv 98,112,113
cv 99,114,115
s_nop 0
v_permlane16_swap_b32_e32 v96,v98
v_permlane16_swap_b32_e32 v97,v99
buffer_store_dwordx4 v[96:99],v243,s[44:47],s70 offen
s_endpgm
L_odd:
L_vs9:
s_cbranch_execz L_vs13
s_lshl_b32 s86,s21,2
s_sub_u32 s87,s13,s67
s_add_u32 s86,s86,s12
mvaa 64,200,132
s_lshl_b32 s86,s86,8
v_add_u32_e32 v252,s87,v244
s_add_u32 s86,s86,L_odd-L_att_pc+1024
v_lshl_add_u32 v226,v244,3,s86
s_lshl_b32 s86,s12,1
s_bfe_u32 s87,s21,65539
mvaa 64,208,136
s_add_u32 s86,s86,s87
v_min_u32_e32 v226,L_att_end-L_att_pc-4,v226
s_lshl_b32 s86,s86,7
s_add_u32 s86,s86,L_pend2-L_att_pc
v_writelane_b32 v226,s86,2
mvaa 64,216,140
s_mov_b64 exec,7
global_load_dword v253,v226,s[96:97]
s_mov_b64 exec,-1
cvaa 68,196,128,232
s_add_u32 m0,s52,32768
v_and_b32_e32 v224,16,v244
v_lshlrev_b32_e32 v224,5,v224
mvaa 68,204,132
s_xor_b32 s51,s62,0
v_lshrrev_b32_e32 v239,4,v244
v_lshrrev_b32_e32 v236,3,v239
mvaa 68,212,136
s_lshl_b32 s87,s12,13
v_and_b32_e32 v237,112,v244
v_and_b32_e32 v238,48,v239
mvaa 68,220,140
s_xor_b32 s87,s87,8192
ra 64,225,0
v_lshlrev_b32_e32 v238,1,v238
cvaa 96,224,128,232
buffer_load_dword v253,v224,s[8:11],s53 offen sc1
mvaa 96,232,132
s_mov_b32 s84,7
ra 68,225,1024
v_xor_b32_e32 v237,v237,v238
mvaa 96,240,136
s_add_u32 s81,s81,1
ra 72,225,2048
v_lshl_add_u32 v245,v236,11,v237
mvaa 96,248,140
s_mov_b32 s84,5
v_add_u32_e32 v246,15360,v245
v_add_u32_e32 v249,30720,v245
cvaa 100,228,128,232
s_andn2_b32 s69,64,s54
ra 76,225,3072
s_waitcnt vmcnt(9)
v_add_u32_e32 v247,46080,v245
mvaa 100,236,132
dl 245,24,51,0
mvaa 100,244,136
s_add_u32 s88,s14,4096
ra 80,225,4096
cv 8,8,9
mvaa 100,252,140
s_lshl_b32 s35,s19,1
ra 84,225,5120
cv 9,10,11
cvaa 72,192,144,232
s_lshr_b32 s33,40569,s35
ra 88,225,6144
cv 10,12,13
mvaa 72,200,148
s_and_b32 s33,s33,3
ra 92,225,7168
v_add_u32_e32 v250,4096,v252
mvaa 72,208,152
dl 246,24,51,1024
mvaa 72,216,156
s_lshl_b32 s33,s33,13
ra 96,225,8192
v_mov_b32_e32 v251,4
cvaa 76,196,144,232
s_lshr_b32 s34,59294,s35
ra 100,225,9216
s_and_b32 s34,s34,3
mvaa 76,204,148
s_lshl_b32 s34,s34,13
ra 104,225,10240
s_xor_b32 s35,s33,s34
mvaa 76,212,152
s_and_b32 s98,s19,3
ra 108,225,11264
s_lshl_b32 s98,s98,13
mvaa 76,220,156
dl 249,24,51,2048
cvaa 104,224,144,232
s_mov_b32 s74,0x300003
ra 112,225,12288
s_mov_b32 s75,0x30000300
mvaa 104,232,148
s_lshl_b32 s71,s12,15
ra 116,225,13312
s_add_u32 s71,s71,s54
mvaa 104,240,152
s_mov_b64 s[44:45],s[30:31]
ra 120,225,14336
s_mov_b32 s46,4194304
mvaa 104,248,156
s_mov_b32 s47,131072
ex 14
cvaa 108,228,144,232
dl 247,24,51,3072
mvaa 108,236,148
s_add_u32 m0,s52,49152
s_xor_b32 s51,s63,0
ra 124,225,15360
mvaa 108,244,152
s_lshl_b32 s70,s23,11
ex 15
mvaa 108,252,156
s_add_u32 s70,s70,s71
ex 40
cvaa 80,192,160,232
L_vs10:
s_cbranch_execz L_vs14
ex 41
mvaa 80,200,164
dl 245,24,51,0
mvaa 80,208,168
ex 42
mvaa 80,216,172
ex 43
cvaa 84,196,160,232
ex 44
mvaa 84,204,164
ex 45
mvaa 84,212,168
dl 246,24,51,1024
mvaa 84,220,172
ex 46
cvaa 112,224,160,232
ex 47
mvaa 112,232,164
ex 16
mvaa 112,240,168
ex 17
mvaa 112,248,172
dl 249,24,51,2048
cvaa 116,228,160,232
ex 18
mvaa 116,236,164
ex 19
mvaa 116,244,168
ex 20
mvaa 116,252,172
ex 21
cvaa 88,192,176,232
dl 247,24,51,3072
s_waitcnt vmcnt(8)
v_cmp_ne_u32_e32 vcc,4,v253
mvaa 88,200,180
s_cbranch_vccnz L_spbo
L_sprbo:
ex 22
mvaa 88,208,184
s_xor_b32 s50,s66,262144
ex 23
mvaa 88,216,188
ex 48
cvaa 92,196,176,232
ex 49
mvaa 92,204,180
la 128,252,50,0
mvaa 92,212,184
s_waitcnt lgkmcnt(0)
s_barrier
mvaa 92,220,188
s_mov_b32 m0,s72
ex 50
cvaa 120,224,176,232
s_lshl_b32 s72,s22,11
ex 51
mvaa 120,232,180
s_add_u32 s72,s72,s71
ex 52
mvaa 120,240,184
ds 255,8,76,0
mvaa 120,248,188
s_add_u32 s71,s70,131072
ex 53
cvaa 124,228,176,232
ex 54
mvaa 124,236,180
ex 55
mvaa 124,244,184
ex 24
mvaa 124,252,188
ds 255,8,76,1024
maav 0,64,0
s_cmp_lg_u32 s19,7
s_cbranch_scc1 L_j7o
s_nop 1
s_lshl_b32 s86,s12,12
s_add_u32 s86,s86,131072
v_add_u32_e32 v236,s86,v244
ra 160,236,0
ra 164,236,1024
ra 168,236,2048
ra 172,236,3072
ra 176,236,16384
ra 180,236,17408
ra 184,236,18432
ra 188,236,19456
v_add_u32_e32 v224,s94,v224
s_waitcnt lgkmcnt(0)
L_j7o:
ex 25
maav 0,96,4
ex 26
maav 4,68,0
ex 27
maav 4,100,4
cv 11,14,15
cv 12,40,41
maav 8,72,0
ds 255,8,76,2048
maav 8,104,4
cv 13,42,43
cv 14,44,45
maav 12,76,0
cv 15,46,47
cv 16,16,17
maav 12,108,4
ex 28
v_smfmac_f32_16x16x64_bf16 v[144:147],v[228:231],v[0:7],v248
cv 17,18,19
maav 16,80,0
ds 255,8,76,3072
maav 16,112,4
s_mov_b32 m0,s73
ex 29
maav 20,84,0
s_add_u32 s73,s72,131072
ex 30
maav 20,116,4
ex 31
maav 24,88,0
ex 56
maav 24,120,4
ds 255,8,77,0
maav 28,92,0
ex 57
maav 28,124,4
ex 58
maav 32,64,8
ex 59
maav 32,96,12
ex 60
maav 36,68,8
ds 255,8,77,1024
maav 36,100,12
cv 18,20,21
cv 19,22,23
maav 40,72,8
cv 20,48,49
cv 21,50,51
maav 40,104,12
cv 22,52,53
cv 23,54,55
maav 44,76,8
ex 61
maav 44,108,12
ds 255,8,77,2048
v_smfmac_f32_16x16x64_bf16 v[148:151],v[228:231],v[8:15],v248
cv 24,24,25
maav 48,80,8
ex 62
maav 48,112,12
ex 63
maav 52,84,8
ra 192,244,0
cv 25,26,27
maav 52,116,12
ds 255,8,77,3072
maav 56,88,8
cv 26,28,29
cv 27,30,31
maav 56,120,12
cv 28,56,57
cv 29,58,59
maav 60,92,8
cv 30,60,61
cv 31,62,63
maav 60,124,12
ex 64
mvav 160,64,16
la 132,252,50,2048
mvav 160,96,20
ex 65
mvav 164,68,16
ex 66
mvav 164,100,20
ex 67
mvav 168,72,16
ex 68
mvav 168,104,20
la 136,250,50,0
mvav 172,76,16
ex 69
mvav 172,108,20
ex 70
v_smfmac_f32_16x16x64_bf16 v[152:155],v[228:231],v[16:23],v248
ra 200,244,2048
mvav 176,80,16
ra 208,244,4096
cv 64,64,65
mvav 176,112,20
la 140,250,50,2048
mvav 180,84,16
ra 216,244,6144
cv 65,66,67
mvav 180,116,20
ex 71
mvav 184,88,16
ex 96
mvav 184,120,20
s_barrier
ex 97
mvav 188,92,16
la 156,250,50,3072
mvav 188,124,20
s_add_u32 m0,s52,131072
ra 196,244,1024
s_xor_b32 s51,s62,1048576
mvav 192,64,24
ra 204,244,3072
cv 66,68,69
mvav 192,96,28
ra 212,244,5120
cv 67,70,71
mvav 196,68,24
ra 220,244,7168
cv 68,96,97
mvav 196,100,28
la 152,250,50,1024
mvav 200,72,24
ex 98
mvav 200,104,28
ex 99
mvav 204,76,24
ra 224,244,8192
cv 69,98,99
mvav 204,108,28
ra 232,244,10240
v_add_u32_e32 v6,s87,v244
v_smfmac_f32_16x16x64_bf16 v[156:159],v[228:231],v[24:31],v248
ra 240,244,12288
mvav 208,80,24
la 148,252,50,3072
mvav 208,112,28
s_xor_b32 s87,s52,4096
ra 248,244,14336
v_add_u32_e32 v7,98304,v6
mvav 212,84,24
s_add_u32 s87,s87,150528
ra 228,244,9216
v_add_u32_e32 v5,65536,v244
mvav 212,116,28
ra 236,244,11264
v_add_u32_e32 v32,s87,v244
mvav 216,88,24
ra 244,244,13312
v_and_b32_e32 v224,960,v244
mvav 216,120,28
la 144,252,50,1024
mvav 220,92,24
s_add_u32 s50,s50,s55
ra 252,244,15360
mvav 220,124,28
ra 64,225,32768
s_waitcnt lgkmcnt(8)
cvaa 24,192,176,232
dl 245,24,51,0
mvaa 24,200,180
ra 96,225,40960
mvaa 24,208,184
ra 68,225,33792
mvaa 24,216,188
dl 246,24,51,1024
cvaa 16,192,160,128
ra 100,225,41984
mvaa 16,200,164
ra 72,225,34816
mvaa 16,208,168
dl 249,24,51,2048
mvaa 16,216,172
ra 104,225,43008
cvaa 28,196,176,232
ra 76,225,35840
mvaa 28,204,180
dl 247,24,51,3072
mvaa 28,212,184
s_add_u32 m0,s52,147456
s_xor_b32 s51,s63,1048576
ra 108,225,44032
mvaa 28,220,188
ra 80,225,36864
cvaa 20,196,160,132
dl 245,24,51,0
mvaa 20,204,164
ra 112,225,45056
mvaa 20,212,168
ra 192,244,16384
mvaa 20,220,172
dl 246,24,51,1024
cvaa 56,224,176,232
ra 200,244,18432
s_waitcnt lgkmcnt(7)
mvaa 56,232,180
ra 208,244,20480
mvaa 56,240,184
dl 249,24,51,2048
mvaa 56,248,188
ra 216,244,22528
cvaa 48,224,160,136
ra 196,244,17408
mvaa 48,232,164
ra 204,244,19456
mvaa 48,240,168
ra 212,244,21504
mvaa 48,248,172
ra 220,244,23552
cvaa 60,228,176,232
ra 224,244,24576
mvaa 60,236,180
ra 232,244,26624
mvaa 60,244,184
ra 240,244,28672
mvaa 60,252,188
ra 248,244,30720
cvaa 52,228,160,140
ra 84,225,37888
mvaa 52,236,164
ra 116,225,46080
mvaa 52,244,168
la 160,252,50,0
ra 228,244,25600
mvaa 52,252,172
ra 236,244,27648
s_waitcnt lgkmcnt(10)
cvaa 8,192,176,128
ra 244,244,29696
mvaa 8,200,180
la 164,252,50,2048
ra 252,244,31744
mvaa 8,208,184
ra 88,225,38912
mvaa 8,216,188
ra 120,225,47104
cvaa 12,196,176,132
la 168,250,50,0
mvaa 12,204,180
ra 92,225,39936
ra 124,225,48128
s_waitcnt lgkmcnt(9)
mvaa 12,212,184
ex 100
mvaa 12,220,188
la 172,250,50,2048
cvaa 40,224,176,136
ex 101
mvaa 40,232,180
ex 102
mvaa 40,240,184
ex 103
mvaa 40,248,188
cv 70,100,101
cv 71,102,103
s_waitcnt lgkmcnt(4)
cvaa 44,228,176,140
ex 72
mvaa 44,236,180
s_waitcnt vmcnt(18) lgkmcnt(0)
s_barrier
ex 73
mvaa 44,244,184
la 176,252,50,1024
mvaa 44,252,188
ex 74
maav 0,64,64
ex 75
maav 0,96,68
la 188,250,50,3072
maav 4,68,64
ra 192,5,0
cv 72,72,73
maav 4,100,68
ra 196,5,1024
cv 73,74,75
maav 8,72,64
la 184,250,50,1024
ds_write_b128 v6,a[0:3] offset:0
maav 8,104,68
ex 76
maav 12,76,64
ex 77
maav 12,108,68
la 180,252,50,3072
ds_write_b128 v6,a[4:7] offset:1024
v_smfmac_f32_16x16x64_bf16 v[144:147],v[228:231],v[64:71],v248
cv 74,76,77
maav 16,80,64
ex 78
maav 16,112,68
ds_write_b128 v6,a[8:11] offset:2048
ex 79
maav 20,84,64
ex 104
maav 20,116,68
ds_write_b128 v6,a[12:15] offset:3072
ex 105
maav 24,88,64
ex 106
ex 107
ex 108
maav 24,120,68
ds_write_b128 v6,a[16:19] offset:4096
cv 75,78,79
maav 28,92,64
ex 109
ex 110
ex 111
maav 28,124,68
cv 76,104,105
cv 77,106,107
cv 78,108,109
cv 79,110,111
maav 32,64,72
ds_write_b128 v6,a[20:23] offset:5120
maav 32,96,76
ex 80
maav 36,68,72
ex 81
maav 36,100,76
ds_write_b128 v6,a[24:27] offset:6144
maav 40,72,72
ex 82
maav 40,104,76
ex 83
maav 44,76,72
ds_write_b128 v6,a[28:31] offset:7168
maav 44,108,76
cv 80,80,81
cv 81,82,83
v_smfmac_f32_16x16x64_bf16 v[148:151],v[228:231],v[72:79],v248
ex 84
maav 48,80,72
ds_write_b128 v7,a[32:35] offset:0
maav 48,112,76
ex 85
ex 86
ex 87
maav 52,84,72
ex 112
ex 113
maav 52,116,76
ds_write_b128 v7,a[36:39] offset:1024
cv 82,84,85
maav 56,88,72
ex 114
ex 115
ex 116
maav 56,120,76
ex 117
ex 118
ex 119
maav 60,92,72
ds_write_b128 v7,a[40:43] offset:2048
cv 83,86,87
maav 60,124,76
cv 84,112,113
cv 85,114,115
cv 86,116,117
cv 87,118,119
mvav 160,64,80
ex 88
mvav 160,96,84
ds_write_b128 v7,a[44:47] offset:3072
mvav 164,68,80
v_add_f32_e32 v130,v144,v145
v_add_f32_e32 v131,v148,v149
mvav 164,100,84
ex 89
mvav 168,72,80
ds_write_b128 v7,a[48:51] offset:4096
mvav 168,104,84
ex 90
mvav 172,76,80
ex 91
mvav 172,108,84
ds_write_b128 v32,v[128:131]
v_smfmac_f32_16x16x64_bf16 v[152:155],v[228:231],v[80:87],v248
ex 92
ex 93
mvav 176,80,80
ds_write_b128 v7,a[52:55] offset:5120
ex 94
mvav 176,112,84
ra 200,5,2048
ra 204,5,3072
ex 95
cv 88,88,89
mvav 180,84,80
ra 208,5,4096
ra 212,5,5120
cv 89,90,91
cv 90,92,93
ex 120
mvav 180,116,84
ds_write_b128 v7,a[56:59] offset:6144
ex 121
mvav 184,88,80
ex 122
ex 123
cv 91,94,95
mvav 184,120,84
ex 124
ex 125
mvav 188,92,80
ds_write_b128 v7,a[60:63] offset:7168
ex 126
mvav 188,124,84
ra 216,5,6144
ra 220,5,7168
ra 224,5,8192
ra 232,5,10240
ex 127
cv 92,120,121
mvav 192,64,88
ra 240,5,12288
ra 248,5,14336
ra 228,5,9216
ra 236,5,11264
ra 244,5,13312
ra 252,5,15360
cv 93,122,123
cv 94,124,125
cv 95,126,127
v_xor_b32_e32 v6,8192,v6
s_nop 0
mvav 192,96,92
ex 16
mvav 196,68,88
s_waitcnt lgkmcnt(10)
s_xor_b32 s87,s87,0
mvav 196,100,92
s_xor_b32 s50,s66,294912
s_barrier
v_xor_b32_e32 v7,8192,v7
mvav 200,72,88
la 4,252,50,0
ra 64,5,16384
ra 96,5,24576
rv 64,6,0
rv 68,6,1024
rv 72,6,2048
mvav 200,104,92
rv 76,6,3072
rv 80,6,4096
rv 84,6,5120
ex 17
ex 18
mvav 204,76,88
rv 96,7,0
rv 100,7,1024
ex 19
mvav 204,108,92
la 12,252,50,2048
rv 104,7,2048
rv 108,7,3072
cv 16,16,17
v_smfmac_f32_16x16x64_bf16 v[156:159],v[228:231],v[88:95],v248
rv 112,7,4096
rv 116,7,5120
ex 20
ex 21
mvav 208,80,88
rv 120,7,6144
rv 124,7,7168
ex 22
ex 23
mvav 208,112,92
la 20,250,50,0
ra 68,5,17408
ra 100,5,25600
cv 17,18,19
cv 18,20,21
mvav 212,84,88
ra 72,5,18432
ra 104,5,26624
ex 48
ex 49
mvav 212,116,92
ra 76,5,19456
ra 108,5,27648
ex 50
ex 51
mvav 216,88,88
la 28,250,50,2048
ra 80,5,20480
ra 112,5,28672
ra 84,5,21504
mvav 216,120,92
L_vs11:
s_cbranch_execz L_vs15
ra 116,5,29696
cv 19,22,23
ex 52
ex 53
mvav 220,92,88
ra 88,5,22528
cv 20,48,49
ex 54
ex 55
mvav 220,124,92
la 60,250,50,3072
ra 120,5,30720
cv 21,50,51
cv 22,52,53
cv 23,54,55
s_bitcmp1_b32 s82,s81
s_cbranch_scc1 L_pend2
L_ret_eb5:
mvav 160,192,16
ra 92,5,23552
rv 88,6,6144
ra 124,5,31744
rv 92,6,7168
ex 24
s_waitcnt lgkmcnt(10)
mvav 160,224,20
ex 25
ex 26
v_smfmac_f32_16x16x64_bf16 v[152:155],v[228:231],v[16:23],v248
ex 27
v_xor_b32_e32 v32,4096,v32
mvav 164,196,16
la 52,250,50,1024
ds_read_b128 v[236:239],v32
cv 24,24,25
ex 28
mvav 164,228,20
ex 29
cv 25,26,27
ex 30
ex 31
mvav 168,200,16
ex 56
ex 57
mvav 168,232,20
la 44,252,50,3072
cv 26,28,29
ex 58
ex 59
mvav 172,204,16
ex 60
ex 61
ex 62
mvav 172,236,20
s_waitcnt lgkmcnt(0)
s_barrier
cv 27,30,31
ex 63
mvav 176,208,16
la 36,252,50,1024
cv 28,56,57
cv 29,58,59
cv 30,60,61
cv 31,62,63
mvav 176,240,20
s_add_u32 m0,s52,147456
ex 8
s_xor_b32 s51,s63,1048576
ex 9
ex 10
mvav 180,212,16
s_add_u32 s50,s50,s55
ex 11
cv 8,8,9
cv 9,10,11
ex 12
mvav 180,244,20
dl 247,24,51,3072
ex 13
ex 14
mvav 184,216,16
s_add_u32 m0,s52,0
ex 15
ex 40
ex 41
mvav 184,248,20
s_xor_b32 s51,s64,0
ex 42
ex 43
ex 44
mvav 188,220,16
la 0,252,50,0
ex 45
ex 46
mvav 188,252,20
ex 47
cv 10,12,13
cv 11,14,15
cv 12,40,41
cv 13,42,43
cv 14,44,45
v_smfmac_f32_16x16x64_bf16 v[156:159],v[228:231],v[24:31],v248
cv 15,46,47
v_bfe_u32 v225,v244,1,5
s_nop 0
v_smfmac_f32_16x16x64_bf16 v[156:159],v[228:231],v[8:15],v248
v_lshl_add_u32 v224,v224,1,v225
v_bfe_u32 v225,v244,7,2
v_lshl_add_u32 v224,v225,5,v224
mvav 192,192,24
la 8,252,50,2048
v_xor_b32_e32 v224,s69,v224
v_add_f32_e32 v152,v152,v153
v_add_f32_e32 v137,v152,v238
v_subrev_u32_e32 v236,0x23800000,v137
mvav 192,224,28
v_rcp_f32_e32 v137,v137
mvav 192,64,8
tv 128,224,32768
v_mul_f32_e32 v232,v240,v137
v_mul_f32_e32 v64,v232,v64
v_mul_f32_e32 v65,v232,v65
v_mul_f32_e32 v66,v232,v66
mvav 192,96,12
la 16,250,50,0
v_mul_f32_e32 v67,v232,v67
v_mul_f32_e32 v68,v232,v68
v_mul_f32_e32 v69,v232,v69
v_mul_f32_e32 v70,v232,v70
mvav 196,196,24
v_fmac_f32_e32 v64,v232,v160
v_fmac_f32_e32 v65,v232,v161
v_mul_f32_e32 v71,v232,v71
mvav 196,228,28
v_fmac_f32_e32 v66,v232,v162
v_fmac_f32_e32 v67,v232,v163
v_fmac_f32_e32 v68,v232,v164
v_fmac_f32_e32 v69,v232,v165
v_fmac_f32_e32 v70,v232,v166
mvav 196,68,8
la 24,250,50,2048
v_fmac_f32_e32 v71,v232,v167
cv 160,64,65
cv 161,66,67
cv 162,68,69
cv 163,70,71
mvav 196,100,12
tv 130,224,34816
tv 132,224,36864
mvav 200,200,24
tv 134,224,38912
tv 144,224,40960
v_mul_f32_e32 v72,v232,v72
v_mul_f32_e32 v73,v232,v73
v_mul_f32_e32 v74,v232,v74
mvav 200,232,28
buffer_store_dwordx4 v[160:163],v244,s[8:11],s14 offen offset:0 sc0 sc1
v_mul_f32_e32 v75,v232,v75
v_mul_f32_e32 v76,v232,v76
v_mul_f32_e32 v77,v232,v77
v_mul_f32_e32 v78,v232,v78
mvav 200,72,8
v_fmac_f32_e32 v72,v232,v168
v_fmac_f32_e32 v73,v232,v169
v_mul_f32_e32 v79,v232,v79
mvav 200,104,12
v_fmac_f32_e32 v74,v232,v170
v_fmac_f32_e32 v75,v232,v171
v_fmac_f32_e32 v76,v232,v172
v_fmac_f32_e32 v77,v232,v173
v_fmac_f32_e32 v78,v232,v174
mvav 204,204,24
la 56,250,50,3072
v_fmac_f32_e32 v79,v232,v175
cv 168,72,73
cv 169,74,75
cv 170,76,77
cv 171,78,79
mvav 204,236,28
tv 146,224,43008
tv 148,224,45056
mvav 204,76,8
tv 150,224,47104
v_mul_f32_e32 v80,v232,v80
v_mul_f32_e32 v81,v232,v81
v_mul_f32_e32 v82,v232,v82
mvav 204,108,12
buffer_store_dwordx4 v[168:171],v244,s[8:11],s14 offen offset:2048 sc0 sc1
v_mul_f32_e32 v83,v232,v83
v_mul_f32_e32 v84,v232,v84
v_mul_f32_e32 v85,v232,v85
v_mul_f32_e32 v86,v232,v86
v_mul_f32_e32 v87,v232,v87
mvav 208,208,24
v_fmac_f32_e32 v80,v232,v176
v_fmac_f32_e32 v81,v232,v177
v_fmac_f32_e32 v82,v232,v178
mvav 208,240,28
v_fmac_f32_e32 v83,v232,v179
v_fmac_f32_e32 v84,v232,v180
v_fmac_f32_e32 v85,v232,v181
cv 176,80,81
cv 177,82,83
mvav 208,80,8
la 48,250,50,1024
v_fmac_f32_e32 v86,v232,v182
v_fmac_f32_e32 v87,v232,v183
cv 178,84,85
cv 179,86,87
mvav 208,112,12
v_mul_f32_e32 v88,v232,v88
v_mul_f32_e32 v89,v232,v89
v_mul_f32_e32 v90,v232,v90
mvav 212,212,24
v_mul_f32_e32 v91,v232,v91
v_mul_f32_e32 v92,v232,v92
v_mul_f32_e32 v93,v232,v93
v_mul_f32_e32 v94,v232,v94
v_mul_f32_e32 v95,v232,v95
mvav 212,244,28
buffer_store_dwordx4 v[176:179],v244,s[8:11],s88 offen offset:0 sc0 sc1
v_fmac_f32_e32 v88,v232,v184
v_fmac_f32_e32 v89,v232,v185
v_fmac_f32_e32 v90,v232,v186
v_fmac_f32_e32 v91,v232,v187
cv 184,88,89
mvav 212,84,8
v_fmac_f32_e32 v92,v232,v188
cv 185,90,91
mvav 212,116,12
v_fmac_f32_e32 v93,v232,v189
v_fmac_f32_e32 v94,v232,v190
v_fmac_f32_e32 v95,v232,v191
cv 186,92,93
cv 187,94,95
mvav 216,216,24
la 40,252,50,3072
v_add_f32_e32 v156,v156,v157
v_add_f32_e32 v141,v156,v239
v_subrev_u32_e32 v237,0x23800000,v141
v_rcp_f32_e32 v141,v141
mvav 216,248,28
v_mul_f32_e32 v233,v241,v141
v_mul_f32_e32 v96,v233,v96
mvav 216,88,8
v_mul_f32_e32 v97,v233,v97
v_mul_f32_e32 v98,v233,v98
v_mul_f32_e32 v99,v233,v99
v_mul_f32_e32 v100,v233,v100
mvav 216,120,12
buffer_store_dwordx4 v[184:187],v244,s[8:11],s88 offen offset:2048 sc0 sc1
v_fmac_f32_e32 v96,v233,v192
v_fmac_f32_e32 v97,v233,v193
v_mul_f32_e32 v101,v233,v101
v_mul_f32_e32 v102,v233,v102
v_mul_f32_e32 v103,v233,v103
mvav 220,220,24
)"
R"(v_fmac_f32_e32 v98,v233,v194
v_fmac_f32_e32 v99,v233,v195
mvav 220,252,28
v_fmac_f32_e32 v100,v233,v196
v_fmac_f32_e32 v101,v233,v197
cv 192,96,97
cv 193,98,99
mvav 220,92,8
la 32,252,50,1024
v_fmac_f32_e32 v102,v233,v198
v_fmac_f32_e32 v103,v233,v199
cv 194,100,101
cv 195,102,103
mvav 220,124,12
v_xor_b32_e32 v225,32,v224
v_xor_b32_e32 v226,64,v224
s_waitcnt lgkmcnt(0)
zvva 0,128,128
tv 138,225,34816
tv 142,225,38912
tv 152,225,40960
s_waitcnt vmcnt(36)
mvva 0,132,132
tv 136,225,32768
tv 154,225,43008
mvva 0,144,136
tv 140,225,36864
tv 158,225,47104
mvva 0,148,140
tv 156,225,45056
v_mul_f32_e32 v104,v233,v104
v_mul_f32_e32 v105,v233,v105
v_mul_f32_e32 v106,v233,v106
zvva 4,148,156
buffer_store_dwordx4 v[192:195],v244,s[8:11],s14 offen offset:1024 sc0 sc1
v_mul_f32_e32 v107,v233,v107
v_mul_f32_e32 v108,v233,v108
v_mul_f32_e32 v109,v233,v109
v_mul_f32_e32 v110,v233,v110
mvva 4,144,152
v_fmac_f32_e32 v104,v233,v200
v_fmac_f32_e32 v105,v233,v201
v_fmac_f32_e32 v106,v233,v202
v_mul_f32_e32 v111,v233,v111
mvva 4,132,148
v_fmac_f32_e32 v107,v233,v203
v_fmac_f32_e32 v108,v233,v204
v_fmac_f32_e32 v109,v233,v205
cv 200,104,105
cv 201,106,107
mvva 4,128,144
v_fmac_f32_e32 v110,v233,v206
v_fmac_f32_e32 v111,v233,v207
cv 202,108,109
cv 203,110,111
s_waitcnt vmcnt(16)
zvva 8,128,160
v_cmp_lt_u32_e64 s[42:43],s48,v236
v_mul_f32_e32 v112,v233,v112
s_or_b64 s[40:41],s[40:41],s[42:43]
v_cmp_lt_u32_e64 s[42:43],s48,v237
mvva 8,132,164
s_or_b64 s[40:41],s[40:41],s[42:43]
v_mul_f32_e32 v113,v233,v113
v_mul_f32_e32 v114,v233,v114
v_mul_f32_e32 v115,v233,v115
mvva 8,144,168
dl 245,36,51,0
v_mul_f32_e32 v116,v233,v116
v_mul_f32_e32 v117,v233,v117
v_mul_f32_e32 v118,v233,v118
v_mul_f32_e32 v119,v233,v119
v_mul_f32_e32 v120,v233,v120
mvva 8,148,172
v_fmac_f32_e32 v112,v233,v208
v_fmac_f32_e32 v113,v233,v209
v_fmac_f32_e32 v114,v233,v210
v_fmac_f32_e32 v115,v233,v211
zvva 12,148,188
v_fmac_f32_e32 v116,v233,v212
v_fmac_f32_e32 v117,v233,v213
cv 208,112,113
cv 209,114,115
mvva 12,144,184
v_fmac_f32_e32 v118,v233,v214
v_fmac_f32_e32 v119,v233,v215
cv 210,116,117
cv 211,118,119
mvva 12,132,180
v_mul_f32_e32 v121,v233,v121
v_mul_f32_e32 v122,v233,v122
v_mul_f32_e32 v123,v233,v123
mvva 12,128,176
v_mul_f32_e32 v124,v233,v124
v_mul_f32_e32 v125,v233,v125
v_mul_f32_e32 v126,v233,v126
v_mul_f32_e32 v127,v233,v127
s_waitcnt lgkmcnt(0)
zvva 28,136,176
buffer_store_dwordx4 v[200:203],v244,s[8:11],s14 offen offset:3072 sc0 sc1
tv 144,226,40960
v_fmac_f32_e32 v120,v233,v216
v_fmac_f32_e32 v121,v233,v217
v_fmac_f32_e32 v122,v233,v218
v_fmac_f32_e32 v123,v233,v219
mvva 28,140,180
tv 128,226,32768
tv 130,226,34816
cv 216,120,121
cv 217,122,123
mvva 28,152,184
tv 132,226,36864
tv 134,226,38912
v_fmac_f32_e32 v124,v233,v220
v_fmac_f32_e32 v125,v233,v221
v_fmac_f32_e32 v126,v233,v222
mvva 28,156,188
tv 146,226,43008
v_fmac_f32_e32 v127,v233,v223
cv 218,124,125
cv 219,126,127
zvva 24,156,172
tv 148,226,45056
tv 150,226,47104
mvva 24,152,168
v_xor_b32_e32 v227,96,v224
v_add_u32_e32 v232,131072,v224
mvva 24,140,164
buffer_store_dwordx4 v[208:211],v244,s[8:11],s88 offen offset:1024 sc0 sc1
mvva 24,136,160
v_add_u32_e32 v233,131072,v225
v_add_u32_e32 v234,131072,v226
zvva 20,136,144
v_add_u32_e32 v235,131072,v227
v_mov_b32_e32 v242,83914752
mvva 20,140,148
v_add_u32_e32 v228,65536,v224
v_add_u32_e32 v229,65536,v225
mvva 20,152,152
v_add_u32_e32 v230,65536,v226
v_add_u32_e32 v231,65536,v227
mvva 20,156,156
v_and_b32_e32 v243,240,v244
v_lshlrev_b32_e32 v243,7,v243
zvva 16,156,140
buffer_store_dwordx4 v[216:219],v244,s[8:11],s88 offen offset:3072 sc0 sc1
s_cmp_lg_u64 s[40:41],0
s_cbranch_scc1 L_redo
L_redo_r5:
mvva 16,152,136
tv 156,227,45056
v_mov_b32_e32 v236,16256
mvva 16,140,132
tv 158,227,47104
v_and_b32_e32 v248,256,v244
mvva 16,136,128
tv 152,227,40960
v_lshrrev_b32_e32 v248,3,v248
s_waitcnt lgkmcnt(3)
zvva 32,128,128
tv 140,227,36864
v_add_u32_e32 v243,v243,v248
mvva 32,132,132
tv 136,227,32768
tv 138,227,34816
mvva 32,144,136
dl 246,36,51,1024
tv 142,227,38912
mvva 32,148,140
tv 154,227,43008
v_and_b32_e32 v248,512,v244
zvva 36,148,156
v_lshrrev_b32_e32 v248,5,v248
v_add_u32_e32 v243,v243,v248
mvva 36,144,152
v_and_b32_e32 v248,16,v244
v_lshlrev_b32_e32 v248,v248,v236
mvva 36,132,148
v_cndmask_b32_e64 v238,0,v248,s[74:75]
s_mov_b32 s74,0xc0000c
mvva 36,128,144
s_mov_b32 s75,0xc0000c00
v_mov_b32_e32 v240,0
v_cndmask_b32_e64 v239,0,v248,s[74:75]
zvva 40,128,160
dl 249,36,51,2048
mvva 40,132,164
v_mov_b32_e32 v241,0
v_mov_b32_e32 v236,0
mvva 40,144,168
v_mov_b32_e32 v237,0
mvva 40,148,172
zvva 44,148,188
mvva 44,144,184
mvva 44,132,180
dl 247,36,51,3072
s_add_u32 m0,s52,16384
s_xor_b32 s51,s65,0
mvva 44,128,176
tv 144,224,57344
s_waitcnt lgkmcnt(1)
zvva 60,136,176
tv 132,224,53248
tv 134,224,55296
mvva 60,140,180
tv 128,224,49152
tv 130,224,51200
mvva 60,152,184
mvva 60,156,188
tv 146,224,59392
zvva 56,156,172
dl 245,36,51,0
tv 148,224,61440
tv 150,224,63488
mvva 56,152,168
mvva 56,140,164
mvva 56,136,160
zvva 52,136,144
mvva 52,140,148
mvva 52,152,152
dl 246,36,51,1024
mvva 52,156,156
zvva 48,156,140
mvva 48,152,136
mvva 48,140,132
tv 156,225,61440
mvva 48,136,128
tv 152,225,57344
s_waitcnt lgkmcnt(2)
zvva 64,128,128
dl 249,36,51,2048
tv 140,225,53248
mvva 64,132,132
tv 136,225,49152
tv 138,225,51200
mvva 64,144,136
tv 142,225,55296
mvva 64,148,140
tv 154,225,59392
zvva 68,148,156
tv 158,225,63488
mvva 68,144,152
mvva 68,132,148
dl 247,36,51,3072
s_lshl_b32 s51,s33,5
s_add_u32 m0,s52,98304
s_xor_b32 s51,s64,s51
mvva 68,128,144
zvva 72,128,160
mvva 72,132,164
s_waitcnt vmcnt(18)
s_mov_b32 s90,0
mvva 72,144,168
s_xor_b32 s50,s67,0
mvva 72,148,172
zvva 76,148,188
dl 245,36,51,0
mvva 76,144,184
mvva 76,132,180
tv 148,226,61440
mvva 76,128,176
tv 144,226,57344
s_waitcnt lgkmcnt(2)
zvva 92,136,176
tv 132,226,53248
mvva 92,140,180
tv 128,226,49152
tv 130,226,51200
mvva 92,152,184
dl 246,36,51,1024
tv 134,226,55296
mvva 92,156,188
tv 146,226,59392
zvva 88,156,172
tv 150,226,63488
mvva 88,152,168
mvva 88,140,164
mvva 88,136,160
zvva 84,136,144
dl 249,36,51,2048
mvva 84,140,148
mvva 84,152,152
mvva 84,156,156
zvva 80,156,140
mvva 80,152,136
mvva 80,140,132
dl 247,36,51,3072
mvva 80,136,128
s_lshl_b32 s51,s33,5
tv 152,227,57344
s_add_u32 m0,s52,114688
s_waitcnt lgkmcnt(1)
zvva 96,128,128
s_xor_b32 s51,s65,s51
tv 140,227,53248
tv 142,227,55296
mvva 96,132,132
tv 136,227,49152
tv 138,227,51200
mvva 96,144,136
mvva 96,148,140
tv 154,227,59392
zvva 100,148,156
lv 164,252,50,0
tv 156,227,61440
tv 158,227,63488
mvva 100,144,152
mvva 100,132,148
mvva 100,128,144
zvva 104,128,160
mvva 104,132,164
mvva 104,144,168
lv 172,252,50,2048
mvva 104,148,172
zvva 108,148,188
mvva 108,144,184
mvva 108,132,180
mvva 108,128,176
s_barrier
s_waitcnt lgkmcnt(0)
zvva 124,136,176
lv 180,250,50,0
tv 132,232,4096
tv 144,232,8192
tv 146,232,10240
mvva 124,140,180
tv 128,232,0
tv 130,232,2048
mvva 124,152,184
tv 134,232,6144
mvva 124,156,188
tv 148,232,12288
tv 150,232,14336
zvva 120,156,172
mvva 120,152,168
mvva 120,140,164
lv 188,250,50,2048
mvva 120,136,160
zvva 116,136,144
mvva 116,140,148
mvva 116,152,152
mvva 116,156,156
zvva 112,156,140
lv 220,250,50,3072
mvva 112,152,136
mvva 112,140,132
tv 156,233,12288
mvva 112,136,128
tv 152,233,8192
s_waitcnt lgkmcnt(2)
mvva 0,128,4
tv 140,233,4096
mvva 0,132,12
tv 136,233,0
tv 138,233,2048
mvva 0,144,20
lv 212,250,50,1024
tv 142,233,6144
mvva 0,148,28
tv 154,233,10240
mvva 4,148,60
tv 158,233,14336
mvva 4,144,52
mvva 4,132,44
mvva 4,128,36
L_vs12:
s_cbranch_execz L_cw3
mvva 8,128,0
lv 204,252,50,3072
mvva 8,132,8
mvva 8,144,16
mvva 8,148,24
mvva 12,148,56
s_waitcnt vmcnt(27)
mvva 12,144,48
s_waitcnt vmcnt(25)
mvva 12,132,40
lv 196,252,50,1024
s_waitcnt vmcnt(24)
mvva 12,128,32
tv 144,234,8192
s_waitcnt lgkmcnt(1)
mvva 28,136,32
tv 132,234,4096
tv 134,234,6144
mvva 28,140,40
tv 128,234,0
tv 130,234,2048
mvva 28,152,48
mvva 28,156,56
tv 146,234,10240
mvva 24,156,24
dl 245,36,51,0
s_waitcnt vmcnt(20)
s_barrier
tv 148,234,12288
tv 150,234,14336
mvva 24,152,16
mvva 24,140,8
mvva 24,136,0
mvva 20,136,36
mvva 20,140,44
mvva 20,152,52
s_cmp_eq_u32 s12,0
s_cselect_b64 exec,1,0
buffer_store_dword v251,off,s[8:11],s61 sc0 sc1
s_mov_b64 exec,-1
mvva 20,156,60
mvva 16,156,28
mvva 16,152,20
mvva 16,140,12
tv 156,235,12288
mvva 16,136,4
tv 152,235,8192
s_waitcnt lgkmcnt(2)
mvva 32,128,4
dl 246,36,51,1024
tv 140,235,4096
mvva 32,132,12
tv 136,235,0
tv 138,235,2048
mvva 32,144,20
tv 142,235,6144
mvva 32,148,28
tv 154,235,10240
mvva 36,148,60
tv 158,235,14336
mvva 36,144,52
mvva 36,132,44
dl 249,36,51,2048
mvva 36,128,36
mvva 40,128,0
mvva 40,132,8
mvva 40,144,16
mvva 40,148,24
mvva 44,148,56
dl 247,36,51,3072
s_lshl_b32 s51,s34,5
s_add_u32 m0,s52,65536
s_xor_b32 s51,s64,s51
mvva 44,144,48
tv 148,232,28672
mvva 44,132,40
s_xor_b32 s53,s60,s33
tv 150,232,30720
mvva 44,128,32
tv 144,232,24576
s_waitcnt lgkmcnt(3)
mvva 60,136,32
tv 132,232,20480
mvva 60,140,40
tv 128,232,16384
tv 130,232,18432
mvva 60,152,48
dl 245,36,51,0
tv 134,232,22528
mvva 60,156,56
tv 146,232,26624
mvva 56,156,24
mvva 56,152,16
mvva 56,140,8
mvva 56,136,0
mvva 52,136,36
buffer_load_dword v253,v242,s[8:11],s53 offen sc1
mvva 52,140,44
mvva 52,152,52
mvva 52,156,60
mvva 48,156,28
mvva 48,152,20
mvva 48,140,12
dl 246,36,51,1024
mvva 48,136,4
tv 152,233,24576
s_waitcnt lgkmcnt(1)
mvva 64,128,4
tv 140,233,20480
tv 142,233,22528
mvva 64,132,12
tv 136,233,16384
tv 138,233,18432
mvva 64,144,20
mvva 64,148,28
tv 154,233,26624
mvva 68,148,60
dl 249,36,51,2048
tv 156,233,28672
tv 158,233,30720
mvva 68,144,52
mvva 68,132,44
mvva 68,128,36
mvva 72,128,0
mvva 72,132,8
mvva 72,144,16
dl 247,36,51,3072
s_lshl_b32 s51,s34,5
s_add_u32 m0,s52,81920
s_xor_b32 s51,s65,s51
mvva 72,148,24
mvva 76,148,56
mvva 76,144,48
mvva 76,132,40
tv 148,234,28672
mvva 76,128,32
tv 144,234,24576
s_waitcnt lgkmcnt(2)
mvva 92,136,32
dl 245,36,51,0
tv 132,234,20480
mvva 92,140,40
tv 128,234,16384
tv 130,234,18432
mvva 92,152,48
tv 134,234,22528
mvva 92,156,56
tv 146,234,26624
mvva 88,156,24
tv 150,234,30720
mvva 88,152,16
mvva 88,140,8
dl 246,36,51,1024
mvva 88,136,0
mvva 84,136,36
mvva 84,140,44
mvva 84,152,52
mvva 84,156,60
mvva 80,156,28
dl 249,36,51,2048
mvva 80,152,20
mvva 80,140,12
tv 156,235,28672
mvva 80,136,4
tv 152,235,24576
s_waitcnt lgkmcnt(2)
mvva 96,128,4
tv 140,235,20480
mvva 96,132,12
tv 136,235,16384
tv 138,235,18432
mvva 96,144,20
dl 247,36,51,3072
s_lshl_b32 s51,s35,5
s_add_u32 m0,s52,32768
s_xor_b32 s51,s64,s51
tv 142,235,22528
mvva 96,148,28
tv 154,235,26624
mvva 100,148,60
tv 158,235,30720
mvva 100,144,52
mvva 100,132,44
mvva 100,128,36
mvva 104,128,0
dl 245,36,51,0
mvva 104,132,8
mvva 104,144,16
mvva 104,148,24
mvva 108,148,56
mvva 108,144,48
mvva 108,132,40
dl 246,36,51,1024
mvva 108,128,32
s_waitcnt vmcnt(27)
s_barrier
s_waitcnt lgkmcnt(0)
mvva 124,136,32
tv 132,224,4096
tv 134,224,6144
tv 144,224,8192
mvva 124,140,40
tv 128,224,0
tv 130,224,2048
mvva 124,152,48
mvva 124,156,56
tv 146,224,10240
mvva 120,156,24
dl 249,36,51,2048
tv 148,224,12288
tv 150,224,14336
mvva 120,152,16
mvva 120,140,8
mvva 120,136,0
mvva 116,136,36
mvva 116,140,44
mvva 116,152,52
dl 247,36,51,3072
s_lshl_b32 s51,s35,5
s_add_u32 m0,s52,49152
s_xor_b32 s51,s65,s51
mvva 116,156,60
mvva 112,156,28
mvva 112,152,20
s_waitcnt vmcnt(11)
v_cmp_ne_u32_e32 vcc,4,v253
mvva 112,140,12
dl 245,36,51,0
s_cbranch_vccnz L_spa3o
L_spra3o:
mvva 112,136,4
s_xor_b32 s50,s67,s33
tv 152,225,8192
s_waitcnt lgkmcnt(1)
mvvv 0,128,164
tv 140,225,4096
tv 142,225,6144
mvvv 0,132,172
tv 136,225,0
tv 138,225,2048
mvvv 0,144,180
dl 246,36,51,1024
mvvv 0,148,188
tv 154,225,10240
mvvv 4,148,220
tv 156,225,12288
tv 158,225,14336
mvvv 4,144,212
mvvv 4,132,204
la 196,252,50,0
mvvv 4,128,196
mvvv 8,128,160
s_xor_b32 s53,s60,s34
mvvv 8,132,168
mvvv 8,144,176
la 204,252,50,2048
mvvv 8,148,184
mvvv 12,148,216
mvvv 12,144,208
mvvv 12,132,200
buffer_load_dword v253,v242,s[8:11],s53 offen sc1
mvvv 12,128,192
tv 144,226,8192
s_waitcnt lgkmcnt(1)
mvvv 28,136,192
tv 132,226,4096
tv 134,226,6144
mvvv 28,140,200
tv 128,226,0
tv 130,226,2048
mvvv 28,152,208
la 212,250,50,0
mvvv 28,156,216
tv 146,226,10240
mvvv 24,156,184
tv 148,226,12288
tv 150,226,14336
mvvv 24,152,176
mvvv 24,140,168
la 220,250,50,2048
mvvv 24,136,160
mvvv 20,136,196
mvvv 20,140,204
mvvv 20,152,212
la 252,250,50,3072
mvvv 20,156,220
mvvv 16,156,188
mvvv 16,152,180
mvvv 16,140,172
la 244,250,50,1024
mvvv 16,136,164
tv 152,227,8192
s_waitcnt lgkmcnt(1)
mvvv 32,128,164
tv 140,227,4096
tv 142,227,6144
mvvv 32,132,172
tv 136,227,0
tv 138,227,2048
mvvv 32,144,180
la 236,252,50,3072
mvvv 32,148,188
tv 154,227,10240
mvvv 36,148,220
tv 156,227,12288
tv 158,227,14336
mvvv 36,144,212
mvvv 36,132,204
la 228,252,50,1024
s_add_u32 s50,s50,s55
mvvv 36,128,196
mvvv 40,128,160
mvvv 40,132,168
mvvv 40,144,176
la 192,252,50,0
mvvv 40,148,184
mvvv 44,148,216
mvvv 44,144,208
mvvv 44,132,200
la 200,252,50,2048
mvvv 44,128,192
tv 144,224,24576
s_waitcnt lgkmcnt(1)
mvvv 60,136,192
tv 132,224,20480
tv 134,224,22528
mvvv 60,140,200
tv 128,224,16384
tv 130,224,18432
mvvv 60,152,208
la 208,250,50,0
mvvv 60,156,216
tv 146,224,26624
mvvv 56,156,184
tv 148,224,28672
tv 150,224,30720
mvvv 56,152,176
mvvv 56,140,168
la 216,250,50,2048
mvvv 56,136,160
mvvv 52,136,196
mvvv 52,140,204
mvvv 52,152,212
la 248,250,50,3072
mvvv 52,156,220
mvvv 48,156,188
mvvv 48,152,180
mvvv 48,140,172
la 240,250,50,1024
mvvv 48,136,164
tv 152,225,24576
s_waitcnt lgkmcnt(1)
mvvv 64,128,164
tv 140,225,20480
tv 142,225,22528
mvvv 64,132,172
tv 136,225,16384
tv 138,225,18432
mvvv 64,144,180
la 232,252,50,3072
mvvv 64,148,188
tv 154,225,26624
mvvv 68,148,220
tv 156,225,28672
tv 158,225,30720
mvvv 68,144,212
mvvv 68,132,204
la 224,252,50,1024
mvvv 68,128,196
mvvv 72,128,160
mvvv 72,132,168
mvvv 72,144,176
dl 249,36,51,2048
mvvv 72,148,184
mvvv 76,148,216
mvvv 76,144,208
mvvv 76,132,200
dl 247,36,51,3072
s_lshl_b32 s51,s98,5
s_xor_b32 s51,s64,s51
s_add_u32 m0,s52,131072
s_xor_b32 s51,s51,4194304
mvvv 76,128,192
tv 144,226,24576
s_waitcnt lgkmcnt(1)
mvvv 92,136,192
tv 132,226,20480
tv 134,226,22528
mvvv 92,140,200
tv 128,226,16384
tv 130,226,18432
mvvv 92,152,208
dl 245,36,51,0
mvvv 92,156,216
tv 146,226,26624
mvvv 88,156,184
tv 148,226,28672
tv 150,226,30720
mvvv 88,152,176
mvvv 88,140,168
dl 246,36,51,1024
mvvv 88,136,160
s_waitcnt vmcnt(18)
v_cmp_ne_u32_e32 vcc,4,v253
mvvv 84,136,196
s_cbranch_vccnz L_spa4o
L_spra4o:
mvvv 84,140,204
s_xor_b32 s50,s67,s34
mvvv 84,152,212
dl 249,36,51,2048
mvvv 84,156,220
mvvv 80,156,188
mvvv 80,152,180
mvvv 80,140,172
la 68,252,50,0
mvvv 80,136,164
tv 152,227,24576
s_waitcnt lgkmcnt(1)
mvvv 96,128,164
tv 140,227,20480
tv 142,227,22528
mvvv 96,132,172
tv 136,227,16384
tv 138,227,18432
mvvv 96,144,180
la 76,252,50,2048
s_xor_b32 s53,s60,s35
mvvv 96,148,188
tv 154,227,26624
mvvv 100,148,220
tv 156,227,28672
tv 158,227,30720
mvvv 100,144,212
mvvv 100,132,204
la 84,250,50,0
mvvv 100,128,196
mvvv 104,128,160
mvvv 104,132,168
mvvv 104,144,176
buffer_load_dword v253,v242,s[8:11],s53 offen sc1
mvvv 104,148,184
mvvv 108,148,216
mvvv 108,144,208
mvvv 108,132,200
la 92,250,50,2048
mvvv 108,128,192
s_barrier
s_waitcnt lgkmcnt(0)
mvvv 124,136,192
tv 132,228,36864
tv 134,228,38912
tv 144,228,40960
mvvv 124,140,200
tv 128,228,32768
tv 130,228,34816
mvvv 124,152,208
la 124,250,50,3072
mvvv 124,156,216
tv 146,228,43008
mvvv 120,156,184
tv 148,228,45056
tv 150,228,47104
mvvv 120,152,176
mvvv 120,140,168
la 116,250,50,1024
mvvv 120,136,160
mvvv 116,136,196
mvvv 116,140,204
mvvv 116,152,212
la 108,252,50,3072
mvvv 116,156,220
mvvv 112,156,188
mvvv 112,152,180
mvvv 112,140,172
la 100,252,50,1024
s_add_u32 s50,s50,s55
mvvv 112,136,164
tv 152,229,40960
s_waitcnt lgkmcnt(1)
mvva 0,128,196
tv 140,229,36864
tv 142,229,38912
mvva 0,132,204
tv 136,229,32768
tv 138,229,34816
s_waitcnt vmcnt(23)
mvva 0,144,212
la 64,252,50,0
mvva 0,148,220
tv 154,229,43008
mvva 4,148,252
tv 156,229,45056
tv 158,229,47104
mvva 4,144,244
mvva 4,132,236
la 72,252,50,2048
s_waitcnt vmcnt(23)
mvva 4,128,228
mvva 8,128,192
s_waitcnt vmcnt(22)
mvva 8,132,200
s_waitcnt vmcnt(21)
mvva 8,144,208
la 80,250,50,0
s_waitcnt vmcnt(21)
mvva 8,148,216
s_waitcnt vmcnt(20)
mvva 12,148,248
s_waitcnt vmcnt(19)
mvva 12,144,240
s_waitcnt vmcnt(18)
mvva 12,132,232
la 88,250,50,2048
s_waitcnt vmcnt(18)
mvva 12,128,224
tv 144,230,40960
s_waitcnt lgkmcnt(1)
mvva 28,136,224
tv 132,230,36864
tv 134,230,38912
mvva 28,140,232
tv 128,230,32768
tv 130,230,34816
mvva 28,152,240
la 120,250,50,3072
mvva 28,156,248
tv 146,230,43008
mvva 24,156,216
tv 148,230,45056
tv 150,230,47104
mvva 24,152,208
mvva 24,140,200
la 112,250,50,1024
mvva 24,136,192
mvva 20,136,228
mvva 20,140,236
mvva 20,152,244
la 104,252,50,3072
mvva 20,156,252
mvva 16,156,220
mvva 16,152,212
mvva 16,140,204
L_vs13:
s_cbranch_execz L_cw0
la 96,252,50,1024
mvva 16,136,196
tv 152,231,40960
s_waitcnt lgkmcnt(1)
mvva 32,128,196
tv 140,231,36864
tv 142,231,38912
mvva 32,132,204
tv 136,231,32768
tv 138,231,34816
mvva 32,144,212
dl 247,36,51,3072
s_lshl_b32 s51,s98,5
s_xor_b32 s51,s65,s51
s_add_u32 m0,s52,147456
s_xor_b32 s51,s51,4194304
mvva 32,148,220
tv 154,231,43008
mvva 36,148,252
tv 156,231,45056
tv 158,231,47104
mvva 36,144,244
mvva 36,132,236
dl 245,36,51,0
mvva 36,128,228
mvva 40,128,192
mvva 40,132,200
mvva 40,144,208
dl 246,36,51,1024
mvva 40,148,216
mvva 44,148,248
mvva 44,144,240
mvva 44,132,232
dl 249,36,51,2048
mvva 44,128,224
tv 144,228,57344
s_waitcnt lgkmcnt(1)
mvva 60,136,224
tv 132,228,53248
tv 134,228,55296
mvva 60,140,232
tv 128,228,49152
tv 130,228,51200
s_waitcnt vmcnt(17)
v_cmp_ne_u32_e32 vcc,4,v253
mvva 60,152,240
dl 247,36,51,3072
s_lshl_b32 s51,s98,5
s_xor_b32 s51,s64,s51
s_add_u32 m0,s52,0
s_xor_b32 s51,s51,4456448
s_cbranch_vccnz L_spa5o
L_spra5o:
mvva 60,156,248
s_xor_b32 s50,s67,s35
tv 146,228,59392
mvva 56,156,216
tv 148,228,61440
tv 150,228,63488
mvva 56,152,208
mvva 56,140,200
dl 245,36,51,0
mvva 56,136,192
mvva 52,136,228
mvva 52,140,236
mvva 52,152,244
la 132,252,50,0
mvva 52,156,252
mvva 48,156,220
s_xor_b32 s53,s60,s98
mvva 48,152,212
s_xor_b32 s53,s53,512
mvva 48,140,204
la 140,252,50,2048
mvva 48,136,196
tv 152,229,57344
s_waitcnt lgkmcnt(1)
mvva 64,128,196
tv 140,229,53248
tv 142,229,55296
mvva 64,132,204
tv 136,229,49152
tv 138,229,51200
mvva 64,144,212
buffer_load_dword v253,v242,s[8:11],s53 offen sc1
mvva 64,148,220
tv 154,229,59392
mvva 68,148,252
)"
R"(tv 156,229,61440
tv 158,229,63488
mvva 68,144,244
mvva 68,132,236
la 148,250,50,0
mvva 68,128,228
mvva 72,128,192
mvva 72,132,200
mvva 72,144,208
la 156,250,50,2048
mvva 72,148,216
mvva 76,148,248
mvva 76,144,240
mvva 76,132,232
la 188,250,50,3072
mvva 76,128,224
tv 144,230,57344
s_waitcnt lgkmcnt(1)
mvva 92,136,224
tv 132,230,53248
tv 134,230,55296
mvva 92,140,232
tv 128,230,49152
tv 130,230,51200
mvva 92,152,240
la 180,250,50,1024
mvva 92,156,248
tv 146,230,59392
mvva 88,156,216
tv 148,230,61440
tv 150,230,63488
mvva 88,152,208
mvva 88,140,200
la 172,252,50,3072
mvva 88,136,192
mvva 84,136,228
mvva 84,140,236
mvva 84,152,244
la 164,252,50,1024
s_add_u32 s50,s50,s55
mvva 84,156,252
mvva 80,156,220
mvva 80,152,212
mvva 80,140,204
la 128,252,50,0
mvva 80,136,196
tv 152,231,57344
s_waitcnt lgkmcnt(1)
mvva 96,128,196
tv 140,231,53248
tv 142,231,55296
mvva 96,132,204
tv 136,231,49152
tv 138,231,51200
mvva 96,144,212
la 136,252,50,2048
mvva 96,148,220
tv 154,231,59392
mvva 100,148,252
tv 156,231,61440
tv 158,231,63488
mvva 100,144,244
mvva 100,132,236
la 144,250,50,0
mvva 100,128,228
mvva 104,128,192
mvva 104,132,200
mvva 104,144,208
la 152,250,50,2048
mvva 104,148,216
mvva 108,148,248
mvva 108,144,240
mvva 108,132,232
la 184,250,50,3072
mvva 108,128,224
s_barrier
s_waitcnt lgkmcnt(0)
mvva 124,136,224
tv 132,228,4096
tv 134,228,6144
tv 144,228,8192
mvva 124,140,232
tv 128,228,0
tv 130,228,2048
mvva 124,152,240
la 176,250,50,1024
mvva 124,156,248
tv 146,228,10240
mvva 120,156,216
tv 148,228,12288
tv 150,228,14336
mvva 120,152,208
mvva 120,140,200
la 168,252,50,3072
mvva 120,136,192
mvva 116,136,228
mvva 116,140,236
mvva 116,152,244
la 160,252,50,1024
mvva 116,156,252
mvva 112,156,220
mvva 112,152,212
mvva 112,140,204
dl 246,36,51,1024
mvva 112,136,196
tv 152,229,8192
s_waitcnt lgkmcnt(1)
mvva 0,128,68
tv 140,229,4096
tv 142,229,6144
mvva 0,132,76
tv 136,229,0
tv 138,229,2048
mvva 0,144,84
dl 249,36,51,2048
s_waitcnt vmcnt(25)
mvva 0,148,92
tv 154,229,10240
mvva 4,148,124
tv 156,229,12288
tv 158,229,14336
mvva 4,144,116
mvva 4,132,108
dl 247,36,51,3072
s_lshl_b32 s51,s98,5
s_xor_b32 s51,s64,s51
s_add_u32 m0,s52,16384
s_xor_b32 s51,s51,4718592
mvva 4,128,100
mvva 8,128,64
mvva 8,132,72
mvva 8,144,80
dl 245,36,51,0
mvva 8,148,88
s_waitcnt vmcnt(18)
v_cmp_ne_u32_e32 vcc,4,v253
mvva 12,148,120
s_cbranch_vccnz L_spa6o
L_spra6o:
mvva 12,144,112
s_xor_b32 s50,s67,s98
mvva 12,132,104
dl 246,36,51,1024
s_xor_b32 s50,s50,131072
mvva 12,128,96
tv 144,230,8192
s_waitcnt lgkmcnt(1)
mvva 28,136,96
tv 132,230,4096
tv 134,230,6144
mvva 28,140,104
tv 128,230,0
tv 130,230,2048
mvva 28,152,112
la 4,252,50,0
mvva 28,156,120
tv 146,230,10240
mvva 24,156,88
tv 148,230,12288
tv 150,230,14336
mvva 24,152,80
mvva 24,140,72
la 12,252,50,2048
s_xor_b32 s53,s60,s98
s_xor_b32 s53,s53,8704
mvva 24,136,64
mvva 20,136,100
mvva 20,140,108
mvva 20,152,116
la 20,250,50,0
mvva 20,156,124
mvva 16,156,92
mvva 16,152,84
mvva 16,140,76
buffer_load_dword v253,v242,s[8:11],s53 offen sc1
mvva 16,136,68
tv 152,231,8192
s_waitcnt lgkmcnt(1)
mvva 32,128,68
tv 140,231,4096
tv 142,231,6144
mvva 32,132,76
tv 136,231,0
tv 138,231,2048
mvva 32,144,84
la 28,250,50,2048
mvva 32,148,92
tv 154,231,10240
mvva 36,148,124
tv 156,231,12288
tv 158,231,14336
mvva 36,144,116
mvva 36,132,108
la 60,250,50,3072
mvva 36,128,100
mvva 40,128,64
mvva 40,132,72
mvva 40,144,80
la 52,250,50,1024
mvva 40,148,88
mvva 44,148,120
mvva 44,144,112
mvva 44,132,104
la 44,252,50,3072
mvva 44,128,96
tv 144,228,24576
s_waitcnt lgkmcnt(1)
mvva 60,136,96
tv 132,228,20480
tv 134,228,22528
mvva 60,140,104
tv 128,228,16384
tv 130,228,18432
mvva 60,152,112
la 36,252,50,1024
s_add_u32 s50,s50,s55
mvva 60,156,120
tv 146,228,26624
mvva 56,156,88
tv 148,228,28672
tv 150,228,30720
mvva 56,152,80
mvva 56,140,72
la 0,252,50,0
mvva 56,136,64
mvva 52,136,100
mvva 52,140,108
mvva 52,152,116
la 8,252,50,2048
mvva 52,156,124
mvva 48,156,92
mvva 48,152,84
mvva 48,140,76
la 16,250,50,0
mvva 48,136,68
tv 152,229,24576
s_waitcnt lgkmcnt(1)
mvva 64,128,68
tv 140,229,20480
tv 142,229,22528
mvva 64,132,76
tv 136,229,16384
tv 138,229,18432
mvva 64,144,84
la 24,250,50,2048
mvva 64,148,92
tv 154,229,26624
mvva 68,148,124
tv 156,229,28672
tv 158,229,30720
mvva 68,144,116
mvva 68,132,108
la 56,250,50,3072
mvva 68,128,100
mvva 72,128,64
mvva 72,132,72
mvva 72,144,80
la 48,250,50,1024
mvva 72,148,88
mvva 76,148,120
mvva 76,144,112
mvva 76,132,104
la 40,252,50,3072
mvva 76,128,96
tv 144,224,40960
s_waitcnt lgkmcnt(1)
mvva 92,136,96
tv 132,224,36864
tv 134,224,38912
mvva 92,140,104
tv 128,224,32768
tv 130,224,34816
mvva 92,152,112
la 32,252,50,1024
mvva 92,156,120
tv 146,224,43008
mvva 88,156,88
tv 148,224,45056
tv 150,224,47104
mvva 88,152,80
mvva 88,140,72
dl 249,36,51,2048
mvva 88,136,64
mvva 84,136,100
mvva 84,140,108
mvva 84,152,116
dl 247,36,51,3072
s_lshl_b32 s51,s98,5
s_xor_b32 s51,s65,s51
s_add_u32 m0,s52,98304
s_xor_b32 s51,s51,4456448
mvva 84,156,124
mvva 80,156,92
mvva 80,152,84
mvva 80,140,76
dl 245,36,51,0
mvva 80,136,68
tv 152,225,40960
s_waitcnt lgkmcnt(1)
mvva 0,128,132
tv 140,225,36864
tv 142,225,38912
mvva 0,132,140
tv 136,225,32768
tv 138,225,34816
s_waitcnt vmcnt(25)
mvva 0,144,148
dl 246,36,51,1024
mvva 0,148,156
tv 154,225,43008
mvva 4,148,188
tv 156,225,45056
tv 158,225,47104
mvva 4,144,180
s_waitcnt vmcnt(17)
v_cmp_ne_u32_e32 vcc,4,v253
mvva 4,132,172
dl 249,36,51,2048
s_cbranch_vccnz L_spa7o
L_spra7o:
mvva 4,128,164
s_xor_b32 s50,s67,s98
mvva 8,128,128
s_xor_b32 s50,s50,139264
mvva 8,132,136
mvva 8,144,144
dl 247,36,51,3072
mvva 8,148,152
mvva 12,148,184
mvva 12,144,176
mvva 12,132,168
lv 164,252,50,0
mvva 12,128,160
s_barrier
tv 144,226,40960
s_lshl_b32 s51,s98,5
s_waitcnt lgkmcnt(1)
mvva 28,136,160
s_xor_b32 s53,s60,s98
tv 132,226,36864
tv 134,226,38912
s_xor_b32 s53,s53,16896
mvva 28,140,168
s_xor_b32 s51,s65,s51
tv 128,226,32768
tv 130,226,34816
mvva 28,152,176
lv 172,252,50,2048
mvva 28,156,184
s_add_u32 m0,s52,114688
tv 146,226,43008
s_xor_b32 s51,s51,4718592
mvva 24,156,152
tv 148,226,45056
tv 150,226,47104
mvva 24,152,144
mvva 24,140,136
buffer_load_dword v253,v242,s[8:11],s53 offen sc1
mvva 24,136,128
mvva 20,136,164
mvva 20,140,172
mvva 20,152,180
lv 180,250,50,0
mvva 20,156,188
mvva 16,156,156
mvva 16,152,148
mvva 16,140,140
lv 188,250,50,2048
mvva 16,136,132
tv 152,227,40960
s_waitcnt lgkmcnt(1)
mvva 32,128,132
tv 140,227,36864
tv 142,227,38912
mvva 32,132,140
tv 136,227,32768
tv 138,227,34816
mvva 32,144,148
lv 220,250,50,3072
mvva 32,148,156
tv 154,227,43008
mvva 36,148,188
tv 156,227,45056
tv 158,227,47104
mvva 36,144,180
mvva 36,132,172
lv 212,250,50,1024
mvva 36,128,164
mvva 40,128,128
mvva 40,132,136
mvva 40,144,144
lv 204,252,50,3072
mvva 40,148,152
mvva 44,148,184
mvva 44,144,176
mvva 44,132,168
lv 196,252,50,1024
s_add_u32 s50,s50,s55
mvva 44,128,160
tv 144,224,57344
s_waitcnt lgkmcnt(1)
mvva 60,136,160
tv 132,224,53248
tv 134,224,55296
mvva 60,140,168
tv 128,224,49152
tv 130,224,51200
mvva 60,152,176
lv 160,252,50,0
mvva 60,156,184
tv 146,224,59392
mvva 56,156,152
tv 148,224,61440
tv 150,224,63488
mvva 56,152,144
mvva 56,140,136
lv 168,252,50,2048
mvva 56,136,128
mvva 52,136,164
mvva 52,140,172
mvva 52,152,180
lv 176,250,50,0
mvva 52,156,188
mvva 48,156,156
mvva 48,152,148
mvva 48,140,140
lv 184,250,50,2048
mvva 48,136,132
tv 152,225,57344
s_waitcnt lgkmcnt(1)
mvva 64,128,132
tv 140,225,53248
tv 142,225,55296
mvva 64,132,140
tv 136,225,49152
tv 138,225,51200
mvva 64,144,148
lv 216,250,50,3072
mvva 64,148,156
tv 154,225,59392
mvva 68,148,188
tv 156,225,61440
tv 158,225,63488
mvva 68,144,180
mvva 68,132,172
lv 208,250,50,1024
mvva 68,128,164
mvva 72,128,128
mvva 72,132,136
mvva 72,144,144
lv 200,252,50,3072
mvva 72,148,152
L_vs14:
s_cbranch_execz L_cw1
mvva 76,148,184
mvva 76,144,176
mvva 76,132,168
lv 192,252,50,1024
mvva 76,128,160
tv 144,232,8192
s_waitcnt lgkmcnt(1)
mvva 92,136,160
tv 132,232,4096
tv 134,232,6144
mvva 92,140,168
tv 128,232,0
tv 130,232,2048
mvva 92,152,176
dl 245,36,51,0
mvva 92,156,184
tv 146,232,10240
mvva 88,156,152
tv 148,232,12288
tv 150,232,14336
mvva 88,152,144
mvva 88,140,136
dl 246,36,51,1024
mvva 88,136,128
mvva 84,136,164
mvva 84,140,172
mvva 84,152,180
dl 249,36,51,2048
mvva 84,156,188
mvva 80,156,156
mvva 80,152,148
mvva 80,140,140
dl 247,36,51,3072
mvva 80,136,132
tv 152,233,8192
s_waitcnt vmcnt(18)
v_cmp_ne_u32_e32 vcc,4,v253
s_waitcnt lgkmcnt(1)
mvva 0,128,4
s_cbranch_vccnz L_spa8o
L_spra8o:
tv 140,233,4096
s_xor_b32 s50,s67,s98
mvva 0,132,12
s_xor_b32 s50,s50,147456
tv 136,233,0
tv 138,233,2048
mvva 0,144,20
la 196,252,50,0
tv 142,233,6144
mvva 0,148,28
tv 154,233,10240
v_and_b32_e32 v242,48,v244
mvva 4,148,60
tv 156,233,12288
tv 158,233,14336
mvva 4,144,52
v_lshlrev_b32_e32 v242,3,v242
mvva 4,132,44
la 204,252,50,2048
mvva 4,128,36
mvva 8,128,0
s_andn2_b32 s53,s61,384
mvva 8,132,8
mvva 8,144,16
buffer_load_dword v253,v242,s[8:11],s53 offen sc1
mvva 8,148,24
mvva 12,148,56
mvva 12,144,48
mvva 12,132,40
la 212,250,50,0
mvva 12,128,32
tv 144,234,8192
s_waitcnt lgkmcnt(1)
mvva 28,136,32
tv 132,234,4096
mvva 28,140,40
tv 128,234,0
tv 130,234,2048
mvva 28,152,48
la 220,250,50,2048
tv 134,234,6144
mvva 28,156,56
tv 146,234,10240
mvva 24,156,24
tv 148,234,12288
tv 150,234,14336
mvva 24,152,16
mvva 24,140,8
la 252,250,50,3072
mvva 24,136,0
mvva 20,136,36
mvva 20,140,44
mvva 20,152,52
la 244,250,50,1024
mvva 20,156,60
mvva 16,156,28
mvva 16,152,20
mvva 16,140,12
la 236,252,50,3072
mvva 16,136,4
tv 152,235,8192
s_waitcnt lgkmcnt(1)
mvva 32,128,4
tv 140,235,4096
mvva 32,132,12
tv 136,235,0
tv 138,235,2048
mvva 32,144,20
la 228,252,50,1024
tv 142,235,6144
mvva 32,148,28
s_add_u32 s50,s50,s55
tv 154,235,10240
mvva 36,148,60
tv 156,235,12288
tv 158,235,14336
mvva 36,144,52
mvva 36,132,44
la 192,252,50,0
mvva 36,128,36
mvva 40,128,0
mvva 40,132,8
mvva 40,144,16
la 200,252,50,2048
mvva 40,148,24
mvva 44,148,56
mvva 44,144,48
mvva 44,132,40
la 208,250,50,0
mvva 44,128,32
s_barrier
s_waitcnt lgkmcnt(0)
mvva 60,136,32
tv 132,232,20480
tv 144,232,24576
mvva 60,140,40
tv 128,232,16384
tv 130,232,18432
mvva 60,152,48
la 216,250,50,2048
tv 134,232,22528
mvva 60,156,56
tv 146,232,26624
mvva 56,156,24
tv 148,232,28672
tv 150,232,30720
mvva 56,152,16
mvva 56,140,8
la 248,250,50,3072
mvva 56,136,0
mvva 52,136,36
mvva 52,140,44
mvva 52,152,52
la 240,250,50,1024
mvva 52,156,60
mvva 48,156,28
mvva 48,152,20
mvva 48,140,12
la 232,252,50,3072
mvva 48,136,4
tv 152,233,24576
s_waitcnt lgkmcnt(1)
mvva 64,128,4
tv 140,233,20480
mvva 64,132,12
tv 136,233,16384
tv 138,233,18432
mvva 64,144,20
la 224,252,50,1024
tv 142,233,22528
mvva 64,148,28
tv 154,233,26624
mvva 68,148,60
tv 156,233,28672
tv 158,233,30720
mvva 68,144,52
mvva 68,132,44
mvva 68,128,36
mvva 72,128,0
mvva 72,132,8
mvva 72,144,16
mvva 72,148,24
mvva 76,148,56
mvva 76,144,48
mvva 76,132,40
tv 148,224,12288
mvva 76,128,32
tv 144,224,8192
s_waitcnt lgkmcnt(2)
mvva 92,136,32
tv 132,224,4096
mvva 92,140,40
tv 128,224,0
tv 130,224,2048
mvva 92,152,48
tv 134,224,6144
mvva 92,156,56
tv 146,224,10240
mvva 88,156,24
tv 150,224,14336
mvva 88,152,16
mvva 88,140,8
mvva 88,136,0
s_waitcnt vmcnt(14)
v_cmp_ne_u32_e32 vcc,4,v253
mvva 84,136,36
s_cbranch_vccnz L_sppo
L_sprpo:
mvva 84,140,44
mvva 84,152,52
mvva 84,156,60
mvva 80,156,28
mvva 80,152,20
mvva 80,140,12
tv 156,225,12288
mvva 80,136,4
tv 152,225,8192
s_waitcnt lgkmcnt(2)
mvvv 0,128,164
tv 140,225,4096
mvvv 0,132,172
tv 136,225,0
tv 138,225,2048
mvvv 0,144,180
tv 142,225,6144
mvvv 0,148,188
tv 154,225,10240
mvvv 4,148,220
tv 158,225,14336
mvvv 4,144,212
mvvv 4,132,204
mvvv 4,128,196
mvvv 8,128,160
mvvv 8,132,168
mvvv 8,144,176
mvvv 8,148,184
mvvv 12,148,216
mvvv 12,144,208
mvvv 12,132,200
tv 148,224,28672
mvvv 12,128,192
tv 144,224,24576
s_waitcnt lgkmcnt(2)
mvvv 28,136,192
tv 132,224,20480
mvvv 28,140,200
tv 128,224,16384
tv 130,224,18432
mvvv 28,152,208
tv 134,224,22528
mvvv 28,156,216
tv 146,224,26624
mvvv 24,156,184
tv 150,224,30720
mvvv 24,152,176
mvvv 24,140,168
mvvv 24,136,160
mvvv 20,136,196
mvvv 20,140,204
mvvv 20,152,212
mvvv 20,156,220
mvvv 16,156,188
mvvv 16,152,180
mvvv 16,140,172
tv 156,225,28672
mvvv 16,136,164
tv 152,225,24576
s_waitcnt lgkmcnt(2)
mvva 0,128,196
tv 140,225,20480
mvva 0,132,204
tv 136,225,16384
tv 138,225,18432
s_waitcnt vmcnt(8)
mvva 0,144,212
tv 142,225,22528
mvva 0,148,220
tv 154,225,26624
mvva 4,148,252
tv 158,225,30720
mvva 4,144,244
mvva 4,132,236
mvva 4,128,228
s_waitcnt vmcnt(6)
mvva 8,128,192
cv 0,0,1
cv 1,2,3
mvva 8,132,200
s_waitcnt vmcnt(5)
mvva 8,144,208
s_waitcnt vmcnt(4)
mvva 8,148,216
cv 4,4,5
cv 5,6,7
s_waitcnt vmcnt(3)
mvva 12,148,248
s_waitcnt vmcnt(2)
mvva 12,144,240
s_waitcnt vmcnt(1)
mvva 12,132,232
tv 148,226,12288
cv 8,8,9
s_waitcnt vmcnt(0)
mvva 12,128,224
tv 144,226,8192
cv 9,10,11
s_waitcnt lgkmcnt(2)
mvva 28,136,224
tv 132,226,4096
mvva 28,140,232
tv 128,226,0
tv 130,226,2048
mvva 28,152,240
tv 134,226,6144
cv 12,12,13
mvva 28,156,248
tv 146,226,10240
cv 13,14,15
mvva 24,156,216
tv 150,226,14336
mvva 24,152,208
mvva 24,140,200
mvva 24,136,192
mvva 20,136,228
cv 14,28,29
cv 15,30,31
mvva 20,140,236
buffer_store_dwordx4 v[12:15],v244,s[8:11],s56 offen offset:3072 sc0 sc1
mvva 20,152,244
mvva 20,156,252
mvva 16,156,220
cv 10,24,25
cv 11,26,27
mvva 16,152,212
buffer_store_dwordx4 v[8:11],v244,s[8:11],s56 offen offset:2048 sc0 sc1
mvva 16,140,204
tv 156,227,12288
mvva 16,136,196
cv 6,20,21
cv 7,22,23
s_waitcnt lgkmcnt(1)
mvvv 32,128,164
tv 140,227,4096
mvvv 32,132,172
buffer_store_dwordx4 v[4:7],v244,s[8:11],s56 offen offset:1024 sc0 sc1
tv 136,227,0
tv 138,227,2048
mvvv 32,144,180
tv 142,227,6144
cv 2,16,17
mvvv 32,148,188
tv 152,227,8192
tv 154,227,10240
mvvv 36,148,220
tv 158,227,14336
cv 3,18,19
mvvv 36,144,212
buffer_store_dwordx4 v[0:3],v244,s[8:11],s56 offen offset:0 sc0 sc1
mvvv 36,132,204
mvvv 36,128,196
mvvv 40,128,160
mvvv 40,132,168
mvvv 40,144,176
mvvv 40,148,184
mvvv 44,148,216
mvvv 44,144,208
mvvv 44,132,200
tv 148,226,28672
mvvv 44,128,192
s_barrier
tv 144,226,24576
s_waitcnt lgkmcnt(2)
mvvv 60,136,192
tv 132,226,20480
mvvv 60,140,200
tv 128,226,16384
tv 130,226,18432
mvvv 60,152,208
tv 134,226,22528
mvvv 60,156,216
tv 146,226,26624
mvvv 56,156,184
tv 150,226,30720
mvvv 56,152,176
mvvv 56,140,168
mvvv 56,136,160
mvvv 52,136,196
mvvv 52,140,204
mvvv 52,152,212
mvvv 52,156,220
mvvv 48,156,188
mvvv 48,152,180
mvvv 48,140,172
tv 156,227,28672
mvvv 48,136,164
tv 152,227,24576
s_waitcnt lgkmcnt(2)
mvva 32,128,196
tv 140,227,20480
mvva 32,132,204
tv 136,227,16384
tv 138,227,18432
mvva 32,144,212
tv 142,227,22528
mvva 32,148,220
tv 154,227,26624
mvva 36,148,252
tv 158,227,30720
mvva 36,144,244
mvva 36,132,236
mvva 36,128,228
mvva 40,128,192
mvva 40,132,200
cv 32,32,33
cv 33,34,35
mvva 40,144,208
mvva 40,148,216
mvva 44,148,248
mvva 44,144,240
cv 36,36,37
cv 37,38,39
mvva 44,132,232
tv 148,228,45056
mvva 44,128,224
tv 144,228,40960
cv 40,40,41
s_waitcnt lgkmcnt(2)
mvva 60,136,224
tv 132,228,36864
cv 41,42,43
mvva 60,140,232
tv 128,228,32768
tv 130,228,34816
mvva 60,152,240
tv 134,228,38912
cv 44,44,45
mvva 60,156,248
tv 146,228,43008
cv 45,46,47
mvva 56,156,216
tv 150,228,47104
mvva 56,152,208
mvva 56,140,200
mvva 56,136,192
mvva 52,136,228
cv 46,60,61
cv 47,62,63
mvva 52,140,236
buffer_store_dwordx4 v[44:47],v244,s[8:11],s57 offen offset:3072 sc0 sc1
mvva 52,152,244
mvva 52,156,252
mvva 48,156,220
cv 42,56,57
cv 43,58,59
mvva 48,152,212
buffer_store_dwordx4 v[40:43],v244,s[8:11],s57 offen offset:2048 sc0 sc1
mvva 48,140,204
tv 156,229,45056
mvva 48,136,196
cv 38,52,53
cv 39,54,55
s_waitcnt lgkmcnt(1)
mvvv 64,128,164
tv 140,229,36864
mvvv 64,132,172
buffer_store_dwordx4 v[36:39],v244,s[8:11],s57 offen offset:1024 sc0 sc1
tv 136,229,32768
tv 138,229,34816
mvvv 64,144,180
tv 142,229,38912
cv 34,48,49
mvvv 64,148,188
tv 152,229,40960
tv 154,229,43008
mvvv 68,148,220
tv 158,229,47104
cv 35,50,51
mvvv 68,144,212
buffer_store_dwordx4 v[32:35],v244,s[8:11],s57 offen offset:0 sc0 sc1
mvvv 68,132,204
mvvv 68,128,196
mvvv 72,128,160
mvvv 72,132,168
mvvv 72,144,176
mvvv 72,148,184
mvvv 76,148,216
mvvv 76,144,208
mvvv 76,132,200
tv 148,228,61440
mvvv 76,128,192
tv 144,228,57344
s_waitcnt lgkmcnt(2)
mvvv 92,136,192
tv 132,228,53248
mvvv 92,140,200
tv 128,228,49152
tv 130,228,51200
mvvv 92,152,208
tv 134,228,55296
mvvv 92,156,216
tv 146,228,59392
mvvv 88,156,184
tv 150,228,63488
mvvv 88,152,176
mvvv 88,140,168
mvvv 88,136,160
mvvv 84,136,196
mvvv 84,140,204
mvvv 84,152,212
mvvv 84,156,220
mvvv 80,156,188
mvvv 80,152,180
mvvv 80,140,172
tv 156,229,61440
mvvv 80,136,164
tv 152,229,57344
s_waitcnt lgkmcnt(2)
mvva 64,128,196
tv 140,229,53248
mvva 64,132,204
tv 136,229,49152
tv 138,229,51200
mvva 64,144,212
tv 142,229,55296
mvva 64,148,220
tv 154,229,59392
mvva 68,148,252
tv 158,229,63488
mvva 68,144,244
mvva 68,132,236
mvva 68,128,228
L_vs15:
s_cbranch_execz L_cw2
s_xor_b32 s82,s59,12
mvva 72,128,192
s_mul_i32 s82,s82,12288
cv 64,64,65
s_add_u32 s82,s82,0
mvva 72,132,200
lv 16,244,82,0
mvva 72,144,208
cv 65,66,67
cv 68,68,69
mvva 72,148,216
cv 69,70,71
)"
R"(mvva 76,148,248
mvva 76,144,240
lv 20,244,82,1024
mvva 76,132,232
tv 148,230,28672
tv 150,230,30720
mvva 76,128,224
cv 72,72,73
cv 73,74,75
s_waitcnt lgkmcnt(2)
mvva 92,136,224
tv 132,230,20480
mvva 92,140,232
lv 24,244,82,2048
tv 128,230,16384
tv 130,230,18432
mvva 92,152,240
tv 134,230,22528
cv 76,76,77
mvva 92,156,248
tv 144,230,24576
tv 146,230,26624
mvva 88,156,216
cv 77,78,79
mvva 88,152,208
lv 28,244,82,3072
mvva 88,140,200
mvva 88,136,192
cv 78,92,93
cv 79,94,95
mvva 84,136,228
mvva 84,140,236
buffer_store_dwordx4 v[76:79],v244,s[8:11],s58 offen offset:3072 sc0 sc1
mvva 84,152,244
mvva 84,156,252
mvva 80,156,220
cv 74,88,89
cv 75,90,91
mvva 80,152,212
buffer_store_dwordx4 v[72:75],v244,s[8:11],s58 offen offset:2048 sc0 sc1
mvva 80,140,204
tv 156,231,28672
mvva 80,136,196
cv 70,84,85
cv 71,86,87
s_waitcnt lgkmcnt(1)
mvva 96,128,68
tv 140,231,20480
mvva 96,132,76
buffer_store_dwordx4 v[68:71],v244,s[8:11],s58 offen offset:1024 sc0 sc1
tv 136,231,16384
tv 138,231,18432
mvva 96,144,84
tv 142,231,22528
cv 66,80,81
mvva 96,148,92
tv 152,231,24576
tv 154,231,26624
mvva 100,148,124
tv 158,231,30720
cv 67,82,83
mvva 100,144,116
buffer_store_dwordx4 v[64:67],v244,s[8:11],s58 offen offset:0 sc0 sc1
mvva 100,132,108
mvva 100,128,100
mvva 104,128,64
mvva 104,132,72
mvva 104,144,80
mvva 104,148,88
mvva 108,148,120
mvva 108,144,112
mvva 108,132,104
tv 148,226,61440
mvva 108,128,96
tv 144,226,57344
s_waitcnt lgkmcnt(2)
mvva 124,136,96
tv 132,226,53248
mvva 124,140,104
tv 128,226,49152
tv 130,226,51200
mvva 124,152,112
tv 134,226,55296
mvva 124,156,120
tv 146,226,59392
mvva 120,156,88
tv 150,226,63488
mvva 120,152,80
mvva 120,140,72
mvva 120,136,64
mvva 116,136,100
mvva 116,140,108
s_waitcnt vmcnt(7)
v_max_u32_e32 v248,v16,v17
mvva 116,152,116
v_max3_u32 v248,v248,v18,v19
s_waitcnt vmcnt(6)
v_max3_u32 v248,v248,v20,v21
v_max3_u32 v248,v248,v22,v23
mvva 116,156,124
s_waitcnt vmcnt(5)
v_max3_u32 v248,v248,v24,v25
v_max3_u32 v248,v248,v26,v27
s_waitcnt vmcnt(4)
v_max3_u32 v248,v248,v28,v29
mvva 112,156,92
v_max3_u32 v248,v248,v30,v31
v_cmp_eq_u32_e32 vcc,-1,v248
mvva 112,152,84
s_cbranch_vccnz L_spx0o
L_sprx0o:
mvva 112,140,76
tv 156,227,61440
tv 158,227,63488
v_lshlrev_b32_e32 v251,16,v18
v_and_b32_e32 v255,4294901760,v18
mvva 112,136,68
tv 152,227,57344
tv 154,227,59392
s_waitcnt lgkmcnt(4)
mvva 96,128,132
v_lshlrev_b32_e32 v253,16,v19
v_and_b32_e32 v242,4294901760,v19
mvva 96,132,140
tv 136,227,49152
tv 138,227,51200
mvva 96,144,148
tv 140,227,53248
tv 142,227,55296
v_add_f32_e32 v112,v112,v251
mvva 96,148,156
v_add_f32_e32 v113,v113,v255
v_add_f32_e32 v114,v114,v253
mvva 100,148,188
v_add_f32_e32 v115,v115,v242
v_lshlrev_b32_e32 v251,16,v22
mvva 100,144,180
v_and_b32_e32 v255,4294901760,v22
v_lshlrev_b32_e32 v253,16,v23
mvva 100,132,172
v_and_b32_e32 v242,4294901760,v23
v_add_f32_e32 v116,v116,v251
v_add_f32_e32 v117,v117,v255
mvva 100,128,164
v_add_f32_e32 v118,v118,v253
v_add_f32_e32 v119,v119,v242
mvva 104,128,128
v_lshlrev_b32_e32 v251,16,v26
v_and_b32_e32 v255,4294901760,v26
mvva 104,132,136
v_lshlrev_b32_e32 v253,16,v27
v_and_b32_e32 v242,4294901760,v27
mvva 104,144,144
v_add_f32_e32 v120,v120,v251
v_add_f32_e32 v121,v121,v255
v_add_f32_e32 v122,v122,v253
mvva 104,148,152
v_add_f32_e32 v123,v123,v242
v_lshlrev_b32_e32 v251,16,v30
mvva 108,148,184
v_and_b32_e32 v255,4294901760,v30
v_lshlrev_b32_e32 v253,16,v31
mvva 108,144,176
v_and_b32_e32 v242,4294901760,v31
v_add_f32_e32 v124,v124,v251
mvva 108,132,168
v_add_f32_e32 v125,v125,v255
v_add_f32_e32 v126,v126,v253
v_add_f32_e32 v127,v127,v242
mvva 108,128,160
tv 144,234,24576
v_lshlrev_b32_e32 v251,16,v16
s_waitcnt lgkmcnt(1)
mvva 124,136,160
tv 132,234,20480
v_and_b32_e32 v255,4294901760,v16
mvva 124,140,168
tv 128,234,16384
tv 130,234,18432
mvva 124,152,176
tv 134,234,22528
v_lshlrev_b32_e32 v253,16,v17
mvva 124,156,184
tv 146,234,26624
v_and_b32_e32 v242,4294901760,v17
mvva 120,156,152
tv 148,234,28672
tv 150,234,30720
mvva 120,152,144
v_add_f32_e32 v96,v96,v251
v_add_f32_e32 v97,v97,v255
mvva 120,140,136
v_add_f32_e32 v98,v98,v253
v_add_f32_e32 v99,v99,v242
mvva 120,136,128
v_lshlrev_b32_e32 v251,16,v20
v_and_b32_e32 v255,4294901760,v20
mvva 116,136,164
v_lshlrev_b32_e32 v253,16,v21
v_and_b32_e32 v242,4294901760,v21
mvva 116,140,172
v_add_f32_e32 v100,v100,v251
v_add_f32_e32 v101,v101,v255
mvva 116,152,180
v_add_f32_e32 v102,v102,v253
v_add_f32_e32 v103,v103,v242
mvva 116,156,188
v_lshlrev_b32_e32 v251,16,v24
v_and_b32_e32 v255,4294901760,v24
mvva 112,156,156
v_lshlrev_b32_e32 v253,16,v25
v_and_b32_e32 v242,4294901760,v25
mvva 112,152,148
v_add_f32_e32 v104,v104,v251
v_add_f32_e32 v105,v105,v255
mvva 112,140,140
tv 156,235,28672
v_add_f32_e32 v106,v106,v253
mvva 112,136,132
tv 152,235,24576
v_add_f32_e32 v107,v107,v242
s_waitcnt lgkmcnt(2)
mvva 96,128,4
tv 140,235,20480
v_lshlrev_b32_e32 v251,16,v28
mvva 96,132,12
tv 136,235,16384
tv 138,235,18432
mvva 96,144,20
tv 142,235,22528
v_and_b32_e32 v255,4294901760,v28
mvva 96,148,28
tv 154,235,26624
v_lshlrev_b32_e32 v253,16,v29
mvva 100,148,60
tv 158,235,30720
v_and_b32_e32 v242,4294901760,v29
mvva 100,144,52
v_add_f32_e32 v108,v108,v251
v_add_f32_e32 v109,v109,v255
mvva 100,132,44
v_add_f32_e32 v110,v110,v253
v_add_f32_e32 v111,v111,v242
mvva 100,128,36
mvva 104,128,0
mvva 104,132,8
mvva 104,144,16
mvva 104,148,24
mvva 108,148,56
mvva 108,144,48
mvva 108,132,40
tv 148,230,45056
mvva 108,128,32
tv 144,230,40960
s_waitcnt lgkmcnt(2)
mvva 124,136,32
s_xor_b32 s83,s59,8
tv 132,230,36864
s_mul_i32 s83,s83,12288
mvva 124,140,40
s_add_u32 s83,s83,4096
tv 128,230,32768
tv 130,230,34816
mvva 124,152,48
lv 48,244,83,0
tv 134,230,38912
mvva 124,156,56
tv 146,230,43008
mvva 120,156,24
lv 52,244,83,1024
tv 150,230,47104
mvva 120,152,16
mvva 120,140,8
lv 56,244,83,2048
mvva 120,136,0
mvva 116,136,36
lv 60,244,83,3072
mvva 116,140,44
mvva 116,152,52
s_xor_b32 s84,s59,4
mvva 116,156,60
s_mul_i32 s84,s84,12288
mvva 112,156,28
s_add_u32 s84,s84,8192
mvva 112,152,20
lv 80,244,84,0
mvva 112,140,12
tv 156,231,45056
mvva 112,136,4
lv 84,244,84,1024
tv 152,231,40960
s_waitcnt lgkmcnt(2)
mvvv 96,128,164
tv 140,231,36864
mvvv 96,132,172
lv 88,244,84,2048
tv 136,231,32768
tv 138,231,34816
mvvv 96,144,180
tv 142,231,38912
mvvv 96,148,188
lv 92,244,84,3072
tv 154,231,43008
mvvv 100,148,220
tv 158,231,47104
mvvv 100,144,212
mvvv 100,132,204
mvvv 100,128,196
mvvv 104,128,160
mvvv 104,132,168
mvvv 104,144,176
mvvv 104,148,184
mvvv 108,148,216
mvvv 108,144,208
mvvv 108,132,200
tv 148,230,61440
mvvv 108,128,192
tv 144,230,57344
s_waitcnt lgkmcnt(2)
mvvv 124,136,192
tv 132,230,53248
mvvv 124,140,200
tv 128,230,49152
tv 130,230,51200
mvvv 124,152,208
tv 134,230,55296
mvvv 124,156,216
tv 146,230,59392
mvvv 120,156,184
tv 150,230,63488
mvvv 120,152,176
mvvv 120,140,168
mvvv 120,136,160
mvvv 116,136,196
mvvv 116,140,204
mvvv 116,152,212
mvvv 116,156,220
mvvv 112,156,188
mvvv 112,152,180
mvvv 112,140,172
tv 156,231,61440
mvvv 112,136,164
tv 152,231,57344
s_waitcnt lgkmcnt(2)
mvva 96,128,196
tv 140,231,53248
mvva 96,132,204
tv 136,231,49152
tv 138,231,51200
mvva 96,144,212
tv 142,231,55296
mvva 96,148,220
tv 154,231,59392
mvva 100,148,252
tv 158,231,63488
mvva 100,144,244
mvva 100,132,236
mvva 100,128,228
mvva 104,128,192
mvva 104,132,200
mvva 104,144,208
mvva 104,148,216
s_waitcnt vmcnt(7)
v_max_u32_e32 v248,v48,v49
mvva 108,148,248
v_max3_u32 v248,v248,v50,v51
s_waitcnt vmcnt(6)
v_max3_u32 v248,v248,v52,v53
mvva 108,144,240
v_max3_u32 v248,v248,v54,v55
s_waitcnt vmcnt(5)
v_max3_u32 v248,v248,v56,v57
mvva 108,132,232
v_max3_u32 v248,v248,v58,v59
s_waitcnt vmcnt(4)
v_max3_u32 v248,v248,v60,v61
mvva 108,128,224
v_max3_u32 v248,v248,v62,v63
v_cmp_eq_u32_e32 vcc,-1,v248
s_waitcnt lgkmcnt(0)
mvva 124,136,224
s_cbranch_vccnz L_spx1o
L_sprx1o:
mvva 124,140,232
mvva 124,152,240
mvva 124,156,248
s_waitcnt vmcnt(3)
v_max_u32_e32 v248,v80,v81
mvva 120,156,216
v_max3_u32 v248,v248,v82,v83
s_waitcnt vmcnt(2)
v_max3_u32 v248,v248,v84,v85
mvva 120,152,208
v_max3_u32 v248,v248,v86,v87
s_waitcnt vmcnt(1)
v_max3_u32 v248,v248,v88,v89
mvva 120,140,200
v_max3_u32 v248,v248,v90,v91
s_waitcnt vmcnt(0)
v_max3_u32 v248,v248,v92,v93
mvva 120,136,192
v_max3_u32 v248,v248,v94,v95
v_cmp_eq_u32_e32 vcc,-1,v248
mvva 116,136,228
s_cbranch_vccnz L_spx2o
L_sprx2o:
mvva 116,140,236
mvva 116,152,244
mvva 116,156,252
mvva 112,156,220
mvva 112,152,212
mvva 112,140,204
mvva 112,136,196
mvvv 108,238,60
mvvv 108,238,92
mvvv 124,236,60
mvvv 124,236,92
mvvv 104,238,56
mvvv 104,238,88
mvvv 120,236,56
mvvv 120,236,88
mvvv 100,238,52
mvvv 100,238,84
cv 108,108,109
cv 109,110,111
cv 110,124,125
cv 111,126,127
cv 104,104,105
v_permlane16_swap_b32_e32 v108,v110
mvvv 116,236,52
v_permlane16_swap_b32_e32 v109,v111
cv 105,106,107
cv 106,120,121
cv 107,122,123
mvvv 116,236,84
buffer_store_dwordx4 v[108:111],v243,s[44:47],s73 offen
v_permlane16_swap_b32_e32 v104,v106
v_permlane16_swap_b32_e32 v105,v107
mvvv 96,238,48
cv 100,100,101
cv 101,102,103
mvvv 96,238,80
buffer_store_dwordx4 v[104:107],v243,s[44:47],s72 offen
cv 102,116,117
cv 103,118,119
mvvv 112,236,48
v_permlane16_swap_b32_e32 v100,v102
v_permlane16_swap_b32_e32 v101,v103
mvvv 112,236,80
buffer_store_dwordx4 v[100:103],v243,s[44:47],s71 offen
cv 96,96,97
cv 97,98,99
s_nop 4
cv 98,112,113
cv 99,114,115
s_nop 0
v_permlane16_swap_b32_e32 v96,v98
v_permlane16_swap_b32_e32 v97,v99
buffer_store_dwordx4 v[96:99],v243,s[44:47],s70 offen
L_oddx:
s_endpgm
L_pend2:
s_nop 7
v_add_f32_e32 v226,v152,v153
v_add_f32_e32 v227,v156,v157
v_subrev_u32_e32 v236,0x23800000,v226
v_subrev_u32_e32 v237,0x23800000,v227
v_max_u32_e32 v236,v236,v237
v_cmp_lt_u32_e64 s[40:41],s48,v236
v_rcp_f32_e32 v226,v226
v_rcp_f32_e32 v227,v227
s_add_u32 s86,s13,4096
v_mov_b32_e32 v236,0
v_mov_b32_e32 v237,0
v_mov_b32_e32 v238,0
v_mov_b32_e32 v239,0
v_mov_b64_e32 v[152:153],0
v_mov_b64_e32 v[156:157],0
v_mul_f32_e32 v226,v242,v226
v_mul_f32_e32 v227,v243,v227
v_add_u32_e32 v225,s94,v225
v_pk_mul_f32 v[160:161],v[160:161],v[226:227] op_sel:[0,0] op_sel_hi:[1,0]
v_pk_mul_f32 v[162:163],v[162:163],v[226:227] op_sel:[0,0] op_sel_hi:[1,0]
v_pk_mul_f32 v[164:165],v[164:165],v[226:227] op_sel:[0,0] op_sel_hi:[1,0]
v_pk_mul_f32 v[166:167],v[166:167],v[226:227] op_sel:[0,0] op_sel_hi:[1,0]
cv 160,160,161
cv 161,162,163
cv 162,164,165
cv 163,166,167
buffer_store_dwordx4 v[160:163],v244,s[8:11],s13 offen offset:0 sc0 sc1
v_pk_mul_f32 v[168:169],v[168:169],v[226:227] op_sel:[0,0] op_sel_hi:[1,0]
v_pk_mul_f32 v[170:171],v[170:171],v[226:227] op_sel:[0,0] op_sel_hi:[1,0]
v_pk_mul_f32 v[172:173],v[172:173],v[226:227] op_sel:[0,0] op_sel_hi:[1,0]
v_pk_mul_f32 v[174:175],v[174:175],v[226:227] op_sel:[0,0] op_sel_hi:[1,0]
cv 168,168,169
cv 169,170,171
cv 170,172,173
cv 171,174,175
buffer_store_dwordx4 v[168:171],v244,s[8:11],s13 offen offset:2048 sc0 sc1
v_pk_mul_f32 v[176:177],v[176:177],v[226:227] op_sel:[0,0] op_sel_hi:[1,0]
v_pk_mul_f32 v[178:179],v[178:179],v[226:227] op_sel:[0,0] op_sel_hi:[1,0]
v_pk_mul_f32 v[180:181],v[180:181],v[226:227] op_sel:[0,0] op_sel_hi:[1,0]
v_pk_mul_f32 v[182:183],v[182:183],v[226:227] op_sel:[0,0] op_sel_hi:[1,0]
cv 176,176,177
cv 177,178,179
cv 178,180,181
v_mfma_f32_32x32x16_bf16 v[160:175],v[236:239],v[236:239],0
cv 179,182,183
buffer_store_dwordx4 v[176:179],v244,s[8:11],s86 offen offset:0 sc0 sc1
v_pk_mul_f32 v[184:185],v[184:185],v[226:227] op_sel:[0,0] op_sel_hi:[1,0]
v_pk_mul_f32 v[186:187],v[186:187],v[226:227] op_sel:[0,0] op_sel_hi:[1,0]
v_pk_mul_f32 v[188:189],v[188:189],v[226:227] op_sel:[0,0] op_sel_hi:[1,0]
v_pk_mul_f32 v[190:191],v[190:191],v[226:227] op_sel:[0,0] op_sel_hi:[1,0]
cv 184,184,185
cv 185,186,187
cv 186,188,189
cv 187,190,191
buffer_store_dwordx4 v[184:187],v244,s[8:11],s86 offen offset:2048 sc0 sc1
v_pk_mul_f32 v[192:193],v[192:193],v[226:227] op_sel:[0,1] op_sel_hi:[1,1]
v_pk_mul_f32 v[194:195],v[194:195],v[226:227] op_sel:[0,1] op_sel_hi:[1,1]
v_pk_mul_f32 v[196:197],v[196:197],v[226:227] op_sel:[0,1] op_sel_hi:[1,1]
v_pk_mul_f32 v[198:199],v[198:199],v[226:227] op_sel:[0,1] op_sel_hi:[1,1]
cv 192,192,193
cv 193,194,195
cv 194,196,197
v_mfma_f32_32x32x16_bf16 v[176:191],v[236:239],v[236:239],0
cv 195,198,199
buffer_store_dwordx4 v[192:195],v244,s[8:11],s13 offen offset:1024 sc0 sc1
v_pk_mul_f32 v[200:201],v[200:201],v[226:227] op_sel:[0,1] op_sel_hi:[1,1]
v_pk_mul_f32 v[202:203],v[202:203],v[226:227] op_sel:[0,1] op_sel_hi:[1,1]
v_pk_mul_f32 v[204:205],v[204:205],v[226:227] op_sel:[0,1] op_sel_hi:[1,1]
v_pk_mul_f32 v[206:207],v[206:207],v[226:227] op_sel:[0,1] op_sel_hi:[1,1]
cv 200,200,201
cv 201,202,203
cv 202,204,205
cv 203,206,207
buffer_store_dwordx4 v[200:203],v244,s[8:11],s13 offen offset:3072 sc0 sc1
v_pk_mul_f32 v[208:209],v[208:209],v[226:227] op_sel:[0,1] op_sel_hi:[1,1]
v_pk_mul_f32 v[210:211],v[210:211],v[226:227] op_sel:[0,1] op_sel_hi:[1,1]
v_pk_mul_f32 v[212:213],v[212:213],v[226:227] op_sel:[0,1] op_sel_hi:[1,1]
v_pk_mul_f32 v[214:215],v[214:215],v[226:227] op_sel:[0,1] op_sel_hi:[1,1]
cv 208,208,209
cv 209,210,211
cv 210,212,213
v_mfma_f32_32x32x16_bf16 v[192:207],v[236:239],v[236:239],0
cv 211,214,215
buffer_store_dwordx4 v[208:211],v244,s[8:11],s86 offen offset:1024 sc0 sc1
v_pk_mul_f32 v[216:217],v[216:217],v[226:227] op_sel:[0,1] op_sel_hi:[1,1]
v_pk_mul_f32 v[218:219],v[218:219],v[226:227] op_sel:[0,1] op_sel_hi:[1,1]
v_pk_mul_f32 v[220:221],v[220:221],v[226:227] op_sel:[0,1] op_sel_hi:[1,1]
v_pk_mul_f32 v[222:223],v[222:223],v[226:227] op_sel:[0,1] op_sel_hi:[1,1]
cv 216,216,217
cv 217,218,219
cv 218,220,221
cv 219,222,223
buffer_store_dwordx4 v[216:219],v244,s[8:11],s86 offen offset:3072 sc0 sc1
s_nop 1
v_mfma_f32_32x32x16_bf16 v[208:223],v[236:239],v[236:239],0
s_nop 7
s_nop 1
s_cmp_eq_u32 s84,4
s_cbranch_scc1 L_ret_eb4
s_cmp_lg_u32 s80,s81
s_branch L_ret_eb5
L_redo:
s_waitcnt vmcnt(0) lgkmcnt(0)
s_nop 15
s_nop 3
s_load_dwordx2 s[76:77],%0,0
s_load_dwordx2 s[80:81],%0,16
s_mov_b32 s78,4194304
s_mov_b32 s79,131072
s_mov_b32 s82,8388608
s_mov_b32 s83,131072
s_mov_b32 s86,0
s_waitcnt lgkmcnt(0)
L_rd_t:
s_nop 15
s_nop 3
s_cmp_lt_u32 s86,2
s_cselect_b32 s88,s23,s22
s_cselect_b32 s95,s13,s14
s_and_b32 s89,s86,1
s_lshl_b32 s90,s89,10
s_add_u32 s95,s95,s90
s_lshl_b32 s89,s89,6
s_add_u32 s88,s88,s89
s_lshl_b32 s89,s12,4
s_add_u32 s88,s88,s89
v_bfe_u32 v213,v244,4,4
v_lshrrev_b32_e32 v214,8,v244
v_add_u32_e32 v210,s88,v213
v_lshlrev_b32_e32 v208,11,v210
v_lshl_add_u32 v208,v214,4,v208
v_lshlrev_b32_e32 v209,16,v214
v_lshl_add_u32 v209,v213,1,v209
s_lshl_b32 s89,s18,8
v_add_u32_e32 v209,s89,v209
v_lshlrev_b32_e32 v214,2,v214
v_sub_u32_e32 v210,v210,v214
v_accvgpr_write_b32 a192,0
v_accvgpr_write_b32 a193,0
v_accvgpr_write_b32 a194,0
v_accvgpr_write_b32 a195,0
v_accvgpr_write_b32 a196,0
v_accvgpr_write_b32 a197,0
v_accvgpr_write_b32 a198,0
v_accvgpr_write_b32 a199,0
v_accvgpr_write_b32 a200,0
v_accvgpr_write_b32 a201,0
v_accvgpr_write_b32 a202,0
v_accvgpr_write_b32 a203,0
v_accvgpr_write_b32 a204,0
v_accvgpr_write_b32 a205,0
v_accvgpr_write_b32 a206,0
v_accvgpr_write_b32 a207,0
v_accvgpr_write_b32 a208,0
v_accvgpr_write_b32 a209,0
v_accvgpr_write_b32 a210,0
v_accvgpr_write_b32 a211,0
v_accvgpr_write_b32 a212,0
v_accvgpr_write_b32 a213,0
v_accvgpr_write_b32 a214,0
v_accvgpr_write_b32 a215,0
v_accvgpr_write_b32 a216,0
v_accvgpr_write_b32 a217,0
v_accvgpr_write_b32 a218,0
v_accvgpr_write_b32 a219,0
v_accvgpr_write_b32 a220,0
v_accvgpr_write_b32 a221,0
v_accvgpr_write_b32 a222,0
v_accvgpr_write_b32 a223,0
s_mov_b32 s87,0
L_rd_q:
s_nop 15
s_nop 3
s_lshl_b32 s89,s87,6
s_lshl_b32 s90,s87,18
buffer_load_dwordx4 v[200:203],v208,s[76:79],s89 offen
buffer_load_ushort v160,v209,s[80:83],s90 offen offset:0
buffer_load_ushort v164,v209,s[80:83],s90 offen offset:32
buffer_load_ushort v168,v209,s[80:83],s90 offen offset:64
buffer_load_ushort v172,v209,s[80:83],s90 offen offset:96
buffer_load_ushort v176,v209,s[80:83],s90 offen offset:128
buffer_load_ushort v180,v209,s[80:83],s90 offen offset:160
buffer_load_ushort v184,v209,s[80:83],s90 offen offset:192
buffer_load_ushort v188,v209,s[80:83],s90 offen offset:224
s_add_u32 s90,s90,8192
buffer_load_ushort v192,v209,s[80:83],s90 offen offset:0
buffer_load_ushort v193,v209,s[80:83],s90 offen offset:32
buffer_load_ushort v194,v209,s[80:83],s90 offen offset:64
buffer_load_ushort v195,v209,s[80:83],s90 offen offset:96
buffer_load_ushort v196,v209,s[80:83],s90 offen offset:128
buffer_load_ushort v197,v209,s[80:83],s90 offen offset:160
buffer_load_ushort v198,v209,s[80:83],s90 offen offset:192
buffer_load_ushort v199,v209,s[80:83],s90 offen offset:224
s_add_u32 s90,s90,8192
s_waitcnt vmcnt(0)
v_lshl_or_b32 v160,v192,16,v160
v_lshl_or_b32 v164,v193,16,v164
v_lshl_or_b32 v168,v194,16,v168
v_lshl_or_b32 v172,v195,16,v172
v_lshl_or_b32 v176,v196,16,v176
v_lshl_or_b32 v180,v197,16,v180
v_lshl_or_b32 v184,v198,16,v184
v_lshl_or_b32 v188,v199,16,v188
buffer_load_ushort v161,v209,s[80:83],s90 offen offset:0
buffer_load_ushort v165,v209,s[80:83],s90 offen offset:32
buffer_load_ushort v169,v209,s[80:83],s90 offen offset:64
buffer_load_ushort v173,v209,s[80:83],s90 offen offset:96
buffer_load_ushort v177,v209,s[80:83],s90 offen offset:128
buffer_load_ushort v181,v209,s[80:83],s90 offen offset:160
buffer_load_ushort v185,v209,s[80:83],s90 offen offset:192
buffer_load_ushort v189,v209,s[80:83],s90 offen offset:224
s_add_u32 s90,s90,8192
buffer_load_ushort v192,v209,s[80:83],s90 offen offset:0
buffer_load_ushort v193,v209,s[80:83],s90 offen offset:32
buffer_load_ushort v194,v209,s[80:83],s90 offen offset:64
buffer_load_ushort v195,v209,s[80:83],s90 offen offset:96
buffer_load_ushort v196,v209,s[80:83],s90 offen offset:128
buffer_load_ushort v197,v209,s[80:83],s90 offen offset:160
buffer_load_ushort v198,v209,s[80:83],s90 offen offset:192
buffer_load_ushort v199,v209,s[80:83],s90 offen offset:224
s_add_u32 s90,s90,8192
s_waitcnt vmcnt(0)
v_lshl_or_b32 v161,v192,16,v161
v_lshl_or_b32 v165,v193,16,v165
v_lshl_or_b32 v169,v194,16,v169
v_lshl_or_b32 v173,v195,16,v173
v_lshl_or_b32 v177,v196,16,v177
v_lshl_or_b32 v181,v197,16,v181
v_lshl_or_b32 v185,v198,16,v185
v_lshl_or_b32 v189,v199,16,v189
buffer_load_ushort v162,v209,s[80:83],s90 offen offset:0
buffer_load_ushort v166,v209,s[80:83],s90 offen offset:32
buffer_load_ushort v170,v209,s[80:83],s90 offen offset:64
buffer_load_ushort v174,v209,s[80:83],s90 offen offset:96
buffer_load_ushort v178,v209,s[80:83],s90 offen offset:128
buffer_load_ushort v182,v209,s[80:83],s90 offen offset:160
buffer_load_ushort v186,v209,s[80:83],s90 offen offset:192
buffer_load_ushort v190,v209,s[80:83],s90 offen offset:224
s_add_u32 s90,s90,8192
buffer_load_ushort v192,v209,s[80:83],s90 offen offset:0
buffer_load_ushort v193,v209,s[80:83],s90 offen offset:32
buffer_load_ushort v194,v209,s[80:83],s90 offen offset:64
buffer_load_ushort v195,v209,s[80:83],s90 offen offset:96
buffer_load_ushort v196,v209,s[80:83],s90 offen offset:128
buffer_load_ushort v197,v209,s[80:83],s90 offen offset:160
buffer_load_ushort v198,v209,s[80:83],s90 offen offset:192
buffer_load_ushort v199,v209,s[80:83],s90 offen offset:224
s_add_u32 s90,s90,8192
s_waitcnt vmcnt(0)
v_lshl_or_b32 v162,v192,16,v162
v_lshl_or_b32 v166,v193,16,v166
v_lshl_or_b32 v170,v194,16,v170
v_lshl_or_b32 v174,v195,16,v174
v_lshl_or_b32 v178,v196,16,v178
v_lshl_or_b32 v182,v197,16,v182
v_lshl_or_b32 v186,v198,16,v186
v_lshl_or_b32 v190,v199,16,v190
buffer_load_ushort v163,v209,s[80:83],s90 offen offset:0
buffer_load_ushort v167,v209,s[80:83],s90 offen offset:32
buffer_load_ushort v171,v209,s[80:83],s90 offen offset:64
buffer_load_ushort v175,v209,s[80:83],s90 offen offset:96
buffer_load_ushort v179,v209,s[80:83],s90 offen offset:128
buffer_load_ushort v183,v209,s[80:83],s90 offen offset:160
buffer_load_ushort v187,v209,s[80:83],s90 offen offset:192
buffer_load_ushort v191,v209,s[80:83],s90 offen offset:224
s_add_u32 s90,s90,8192
buffer_load_ushort v192,v209,s[80:83],s90 offen offset:0
buffer_load_ushort v193,v209,s[80:83],s90 offen offset:32
buffer_load_ushort v194,v209,s[80:83],s90 offen offset:64
buffer_load_ushort v195,v209,s[80:83],s90 offen offset:96
buffer_load_ushort v196,v209,s[80:83],s90 offen offset:128
buffer_load_ushort v197,v209,s[80:83],s90 offen offset:160
buffer_load_ushort v198,v209,s[80:83],s90 offen offset:192
buffer_load_ushort v199,v209,s[80:83],s90 offen offset:224
s_add_u32 s90,s90,8192
s_waitcnt vmcnt(0)
v_lshl_or_b32 v163,v192,16,v163
v_lshl_or_b32 v167,v193,16,v167
v_lshl_or_b32 v171,v194,16,v171
v_lshl_or_b32 v175,v195,16,v175
v_lshl_or_b32 v179,v196,16,v179
v_lshl_or_b32 v183,v197,16,v183
v_lshl_or_b32 v187,v198,16,v187
v_lshl_or_b32 v191,v199,16,v191
mavv 192,160,200
mavv 196,164,200
mavv 200,168,200
mavv 204,172,200
mavv 208,176,200
mavv 212,180,200
mavv 216,184,200
mavv 220,188,200
s_add_u32 s87,s87,1
s_cmp_lt_u32 s87,32
s_cbranch_scc1 L_rd_q
s_nop 15
s_nop 3
v_accvgpr_read_b32 v192,a192
v_accvgpr_read_b32 v193,a193
v_accvgpr_read_b32 v194,a194
v_accvgpr_read_b32 v195,a195
v_accvgpr_read_b32 v196,a196
v_accvgpr_read_b32 v197,a197
v_accvgpr_read_b32 v198,a198
v_accvgpr_read_b32 v199,a199
v_mul_f32_e32 v192,0x3e0293ee,v192
v_mul_f32_e32 v193,0x3e0293ee,v193
v_mul_f32_e32 v194,0x3e0293ee,v194
v_mul_f32_e32 v195,0x3e0293ee,v195
v_mul_f32_e32 v196,0x3e0293ee,v196
v_mul_f32_e32 v197,0x3e0293ee,v197
v_mul_f32_e32 v198,0x3e0293ee,v198
v_mul_f32_e32 v199,0x3e0293ee,v199
cv 192,192,193
cv 193,194,195
cv 194,196,197
cv 195,198,199
v_accvgpr_write_b32 a224,v192
v_accvgpr_write_b32 a225,v193
v_accvgpr_write_b32 a226,v194
v_accvgpr_write_b32 a227,v195
v_accvgpr_read_b32 v192,a200
v_accvgpr_read_b32 v193,a201
v_accvgpr_read_b32 v194,a202
v_accvgpr_read_b32 v195,a203
v_accvgpr_read_b32 v196,a204
v_accvgpr_read_b32 v197,a205
v_accvgpr_read_b32 v198,a206
v_accvgpr_read_b32 v199,a207
v_mul_f32_e32 v192,0x3e0293ee,v192
v_mul_f32_e32 v193,0x3e0293ee,v193
v_mul_f32_e32 v194,0x3e0293ee,v194
v_mul_f32_e32 v195,0x3e0293ee,v195
v_mul_f32_e32 v196,0x3e0293ee,v196
v_mul_f32_e32 v197,0x3e0293ee,v197
v_mul_f32_e32 v198,0x3e0293ee,v198
v_mul_f32_e32 v199,0x3e0293ee,v199
cv 192,192,193
cv 193,194,195
cv 194,196,197
cv 195,198,199
v_accvgpr_write_b32 a228,v192
v_accvgpr_write_b32 a229,v193
v_accvgpr_write_b32 a230,v194
v_accvgpr_write_b32 a231,v195
v_accvgpr_read_b32 v192,a208
v_accvgpr_read_b32 v193,a209
v_accvgpr_read_b32 v194,a210
v_accvgpr_read_b32 v195,a211
v_accvgpr_read_b32 v196,a212
v_accvgpr_read_b32 v197,a213
v_accvgpr_read_b32 v198,a214
v_accvgpr_read_b32 v199,a215
v_mul_f32_e32 v192,0x3e0293ee,v192
v_mul_f32_e32 v193,0x3e0293ee,v193
v_mul_f32_e32 v194,0x3e0293ee,v194
v_mul_f32_e32 v195,0x3e0293ee,v195
v_mul_f32_e32 v196,0x3e0293ee,v196
v_mul_f32_e32 v197,0x3e0293ee,v197
v_mul_f32_e32 v198,0x3e0293ee,v198
v_mul_f32_e32 v199,0x3e0293ee,v199
cv 192,192,193
cv 193,194,195
cv 194,196,197
cv 195,198,199
v_accvgpr_write_b32 a232,v192
v_accvgpr_write_b32 a233,v193
v_accvgpr_write_b32 a234,v194
v_accvgpr_write_b32 a235,v195
v_accvgpr_read_b32 v192,a216
v_accvgpr_read_b32 v193,a217
v_accvgpr_read_b32 v194,a218
v_accvgpr_read_b32 v195,a219
v_accvgpr_read_b32 v196,a220
v_accvgpr_read_b32 v197,a221
v_accvgpr_read_b32 v198,a222
v_accvgpr_read_b32 v199,a223
v_mul_f32_e32 v192,0x3e0293ee,v192
v_mul_f32_e32 v193,0x3e0293ee,v193
v_mul_f32_e32 v194,0x3e0293ee,v194
v_mul_f32_e32 v195,0x3e0293ee,v195
v_mul_f32_e32 v196,0x3e0293ee,v196
v_mul_f32_e32 v197,0x3e0293ee,v197
v_mul_f32_e32 v198,0x3e0293ee,v198
v_mul_f32_e32 v199,0x3e0293ee,v199
cv 192,192,193
cv 193,194,195
cv 194,196,197
cv 195,198,199
v_accvgpr_write_b32 a236,v192
v_accvgpr_write_b32 a237,v193
v_accvgpr_write_b32 a238,v194
v_accvgpr_write_b32 a239,v195
v_mov_b32_e32 v160,0
v_mov_b32_e32 v161,0
v_mov_b32_e32 v162,0
v_mov_b32_e32 v163,0
v_mov_b32_e32 v164,0
v_mov_b32_e32 v165,0
v_mov_b32_e32 v166,0
v_mov_b32_e32 v167,0
v_mov_b32_e32 v168,0
v_mov_b32_e32 v169,0
v_mov_b32_e32 v170,0
v_mov_b32_e32 v171,0
v_mov_b32_e32 v172,0
v_mov_b32_e32 v173,0
v_mov_b32_e32 v174,0
v_mov_b32_e32 v175,0
v_mov_b32_e32 v176,0
v_mov_b32_e32 v177,0
v_mov_b32_e32 v178,0
v_mov_b32_e32 v179,0
v_mov_b32_e32 v180,0
v_mov_b32_e32 v181,0
v_mov_b32_e32 v182,0
v_mov_b32_e32 v183,0
v_mov_b32_e32 v184,0
v_mov_b32_e32 v185,0
v_mov_b32_e32 v186,0
v_mov_b32_e32 v187,0
v_mov_b32_e32 v188,0
v_mov_b32_e32 v189,0
v_mov_b32_e32 v190,0
v_mov_b32_e32 v191,0
v_mov_b32_e32 v216,0xf149f2ca
v_mov_b32_e32 v211,v216
v_mov_b32_e32 v212,0
s_lshr_b32 s91,s88,5
s_mov_b32 s87,0
L_rd_a:
s_nop 15
s_nop 3
s_mul_i32 s89,s18,524288
s_lshl_b32 s90,s87,13
s_add_u32 s89,s89,s90
s_add_u32 s90,s89,16777216
la 64,244,90,0
la 68,244,90,1024
la 72,244,90,2048
la 76,244,90,3072
s_add_u32 s90,s89,16781312
la 80,244,90,0
la 84,244,90,1024
la 88,244,90,2048
la 92,244,90,3072
s_add_u32 s90,s89,33554432
la 96,244,90,0
la 100,244,90,1024
la 104,244,90,2048
la 108,244,90,3072
s_add_u32 s90,s89,33558528
la 112,244,90,0
la 116,244,90,1024
la 120,244,90,2048
la 124,244,90,3072
s_waitcnt vmcnt(0)
zvaa 192,64,224
mvaa 192,72,228
mvaa 192,80,232
mvaa 192,88,236
zvaa 196,68,224
mvaa 196,76,228
mvaa 196,84,232
mvaa 196,92,236
v_cmp_gt_i32_e32 vcc,0,v210
s_nop 2
v_cndmask_b32_e32 v192,v192,v216,vcc
v_cmp_gt_i32_e32 vcc,1,v210
s_nop 1
v_cndmask_b32_e32 v193,v193,v216,vcc
v_cmp_gt_i32_e32 vcc,2,v210
s_nop 1
v_cndmask_b32_e32 v194,v194,v216,vcc
v_cmp_gt_i32_e32 vcc,3,v210
s_nop 1
v_cndmask_b32_e32 v195,v195,v216,vcc
v_cmp_gt_i32_e32 vcc,16,v210
s_nop 1
v_cndmask_b32_e32 v196,v196,v216,vcc
v_cmp_gt_i32_e32 vcc,17,v210
s_nop 1
v_cndmask_b32_e32 v197,v197,v216,vcc
v_cmp_gt_i32_e32 vcc,18,v210
s_nop 1
v_cndmask_b32_e32 v198,v198,v216,vcc
v_cmp_gt_i32_e32 vcc,19,v210
s_nop 1
v_cndmask_b32_e32 v199,v199,v216,vcc
v_max3_f32 v213,v192,v193,v194
v_max3_f32 v214,v195,v196,v197
v_max3_f32 v213,v213,v198,v199
v_max_f32_e32 v213,v213,v214
v_mov_b32_e32 v214,v213
s_nop 1
v_permlane16_swap_b32_e32 v213,v214
v_max_f32_e32 v213,v213,v214
v_mov_b32_e32 v214,v213
s_nop 1
v_permlane32_swap_b32_e32 v213,v214
v_max_f32_e32 v213,v213,v214
v_max_f32_e32 v213,v213,v211
v_sub_f32_e32 v215,v211,v213
ex 215
v_mov_b32_e32 v211,v213
v_sub_f32_e32 v192,v192,v211
v_sub_f32_e32 v193,v193,v211
v_sub_f32_e32 v194,v194,v211
v_sub_f32_e32 v195,v195,v211
v_sub_f32_e32 v196,v196,v211
v_sub_f32_e32 v197,v197,v211
v_sub_f32_e32 v198,v198,v211
v_sub_f32_e32 v199,v199,v211
ex 192
ex 193
ex 194
ex 195
ex 196
ex 197
ex 198
ex 199
v_mul_f32_e32 v212,v215,v212
v_add_f32_e32 v212,v212,v192
v_add_f32_e32 v212,v212,v193
v_add_f32_e32 v212,v212,v194
v_add_f32_e32 v212,v212,v195
v_add_f32_e32 v212,v212,v196
v_add_f32_e32 v212,v212,v197
v_add_f32_e32 v212,v212,v198
v_add_f32_e32 v212,v212,v199
cv 192,192,193
cv 193,194,195
cv 194,196,197
cv 195,198,199
v_mul_f32_e32 v160,v215,v160
v_mul_f32_e32 v161,v215,v161
v_mul_f32_e32 v162,v215,v162
)"
R"(v_mul_f32_e32 v163,v215,v163
v_mul_f32_e32 v164,v215,v164
v_mul_f32_e32 v165,v215,v165
v_mul_f32_e32 v166,v215,v166
v_mul_f32_e32 v167,v215,v167
v_mul_f32_e32 v168,v215,v168
v_mul_f32_e32 v169,v215,v169
v_mul_f32_e32 v170,v215,v170
v_mul_f32_e32 v171,v215,v171
v_mul_f32_e32 v172,v215,v172
v_mul_f32_e32 v173,v215,v173
v_mul_f32_e32 v174,v215,v174
v_mul_f32_e32 v175,v215,v175
v_mul_f32_e32 v176,v215,v176
v_mul_f32_e32 v177,v215,v177
v_mul_f32_e32 v178,v215,v178
v_mul_f32_e32 v179,v215,v179
v_mul_f32_e32 v180,v215,v180
v_mul_f32_e32 v181,v215,v181
v_mul_f32_e32 v182,v215,v182
v_mul_f32_e32 v183,v215,v183
v_mul_f32_e32 v184,v215,v184
v_mul_f32_e32 v185,v215,v185
v_mul_f32_e32 v186,v215,v186
v_mul_f32_e32 v187,v215,v187
v_mul_f32_e32 v188,v215,v188
v_mul_f32_e32 v189,v215,v189
v_mul_f32_e32 v190,v215,v190
v_mul_f32_e32 v191,v215,v191
mvav 160,96,192
mvav 164,100,192
mvav 168,104,192
mvav 172,108,192
mvav 176,112,192
mvav 180,116,192
mvav 184,120,192
mvav 188,124,192
v_subrev_u32_e32 v210,32,v210
s_add_u32 s87,s87,1
s_cmp_le_u32 s87,s91
s_cbranch_scc1 L_rd_a
s_nop 15
s_nop 3
s_lshl_b32 s89,s88,2
s_add_u32 s89,s89,83886080
v_bfe_u32 v213,v244,2,6
buffer_load_dword v215,v213,s[8:11],s89 offen sc1
v_mov_b32_e32 v214,v212
s_nop 1
v_permlane16_swap_b32_e32 v212,v214
v_add_f32_e32 v212,v212,v214
v_mov_b32_e32 v214,v212
s_nop 1
v_permlane32_swap_b32_e32 v212,v214
v_add_f32_e32 v212,v212,v214
v_rcp_f32_e32 v212,v212
s_waitcnt vmcnt(0)
v_mul_f32_e32 v215,v215,v212
v_mul_f32_e32 v160,v215,v160
v_mul_f32_e32 v161,v215,v161
v_mul_f32_e32 v162,v215,v162
v_mul_f32_e32 v163,v215,v163
v_mul_f32_e32 v164,v215,v164
v_mul_f32_e32 v165,v215,v165
v_mul_f32_e32 v166,v215,v166
v_mul_f32_e32 v167,v215,v167
v_mul_f32_e32 v168,v215,v168
v_mul_f32_e32 v169,v215,v169
v_mul_f32_e32 v170,v215,v170
v_mul_f32_e32 v171,v215,v171
v_mul_f32_e32 v172,v215,v172
v_mul_f32_e32 v173,v215,v173
v_mul_f32_e32 v174,v215,v174
v_mul_f32_e32 v175,v215,v175
v_mul_f32_e32 v176,v215,v176
v_mul_f32_e32 v177,v215,v177
v_mul_f32_e32 v178,v215,v178
v_mul_f32_e32 v179,v215,v179
v_mul_f32_e32 v180,v215,v180
v_mul_f32_e32 v181,v215,v181
v_mul_f32_e32 v182,v215,v182
v_mul_f32_e32 v183,v215,v183
v_mul_f32_e32 v184,v215,v184
v_mul_f32_e32 v185,v215,v185
v_mul_f32_e32 v186,v215,v186
v_mul_f32_e32 v187,v215,v187
v_mul_f32_e32 v188,v215,v188
v_mul_f32_e32 v189,v215,v189
v_mul_f32_e32 v190,v215,v190
v_mul_f32_e32 v191,v215,v191
s_add_u32 s89,s95,4096
cv 160,160,161
cv 161,162,163
cv 162,164,165
cv 163,166,167
buffer_store_dwordx4 v[160:163],v244,s[8:11],s95 offen offset:0 sc0 sc1
cv 168,168,169
cv 169,170,171
cv 170,172,173
cv 171,174,175
buffer_store_dwordx4 v[168:171],v244,s[8:11],s95 offen offset:2048 sc0 sc1
cv 176,176,177
cv 177,178,179
cv 178,180,181
cv 179,182,183
buffer_store_dwordx4 v[176:179],v244,s[8:11],s89 offen offset:0 sc0 sc1
cv 184,184,185
cv 185,186,187
cv 186,188,189
cv 187,190,191
buffer_store_dwordx4 v[184:187],v244,s[8:11],s89 offen offset:2048 sc0 sc1
s_waitcnt vmcnt(0)
v_mov_b32_e32 v192,v160
v_accvgpr_read_b32 v160,a240
v_accvgpr_write_b32 a240,v192
v_mov_b32_e32 v193,v161
v_accvgpr_read_b32 v161,a241
v_accvgpr_write_b32 a241,v193
v_mov_b32_e32 v194,v162
v_accvgpr_read_b32 v162,a242
v_accvgpr_write_b32 a242,v194
v_mov_b32_e32 v195,v163
v_accvgpr_read_b32 v163,a243
v_accvgpr_write_b32 a243,v195
v_mov_b32_e32 v200,v168
v_accvgpr_read_b32 v168,a244
v_accvgpr_write_b32 a244,v200
v_mov_b32_e32 v201,v169
v_accvgpr_read_b32 v169,a245
v_accvgpr_write_b32 a245,v201
v_mov_b32_e32 v202,v170
v_accvgpr_read_b32 v170,a246
v_accvgpr_write_b32 a246,v202
v_mov_b32_e32 v203,v171
v_accvgpr_read_b32 v171,a247
v_accvgpr_write_b32 a247,v203
v_mov_b32_e32 v208,v176
v_accvgpr_read_b32 v176,a248
v_accvgpr_write_b32 a248,v208
v_mov_b32_e32 v209,v177
v_accvgpr_read_b32 v177,a249
v_accvgpr_write_b32 a249,v209
v_mov_b32_e32 v210,v178
v_accvgpr_read_b32 v178,a250
v_accvgpr_write_b32 a250,v210
v_mov_b32_e32 v211,v179
v_accvgpr_read_b32 v179,a251
v_accvgpr_write_b32 a251,v211
v_mov_b32_e32 v216,v184
v_accvgpr_read_b32 v184,a252
v_accvgpr_write_b32 a252,v216
v_mov_b32_e32 v217,v185
v_accvgpr_read_b32 v185,a253
v_accvgpr_write_b32 a253,v217
v_mov_b32_e32 v218,v186
v_accvgpr_read_b32 v186,a254
v_accvgpr_write_b32 a254,v218
v_mov_b32_e32 v219,v187
v_accvgpr_read_b32 v187,a255
v_accvgpr_write_b32 a255,v219
s_add_u32 s86,s86,1
s_cmp_lt_u32 s86,4
s_cbranch_scc1 L_rd_t
s_nop 15
s_nop 3
s_nop 1
s_cmp_eq_u32 s84,4
s_cbranch_scc1 L_redo_r4
s_cmp_lg_u32 s80,s81
s_branch L_redo_r5
L_spbe:
s_mov_b32 s91,65536
L_splbe:
s_sleep 2
buffer_load_dword v253,v224,s[8:11],s53 offen sc1
s_waitcnt vmcnt(0)
v_cmp_ne_u32_e32 vcc,4,v253
s_nop 1
s_cbranch_vccz L_sprbe
s_sub_u32 s91,s91,1
s_cbranch_scc0 L_splbe
s_branch L_sprbe
L_spa3e:
s_mov_b32 s91,65536
L_spla3e:
s_sleep 2
buffer_load_dword v253,v242,s[8:11],s53 offen sc1
s_waitcnt vmcnt(0)
v_cmp_ne_u32_e32 vcc,4,v253
s_nop 1
s_cbranch_vccz L_spra3e
s_sub_u32 s91,s91,1
s_cbranch_scc0 L_spla3e
s_branch L_spra3e
L_spa4e:
s_mov_b32 s91,65536
L_spla4e:
s_sleep 2
buffer_load_dword v253,v242,s[8:11],s53 offen sc1
s_waitcnt vmcnt(0)
v_cmp_ne_u32_e32 vcc,4,v253
s_nop 1
s_cbranch_vccz L_spra4e
s_sub_u32 s91,s91,1
s_cbranch_scc0 L_spla4e
s_branch L_spra4e
L_spa5e:
s_mov_b32 s91,65536
L_spla5e:
s_sleep 2
buffer_load_dword v253,v242,s[8:11],s53 offen sc1
s_waitcnt vmcnt(0)
v_cmp_ne_u32_e32 vcc,4,v253
s_nop 1
s_cbranch_vccz L_spra5e
s_sub_u32 s91,s91,1
s_cbranch_scc0 L_spla5e
s_branch L_spra5e
L_spa6e:
s_mov_b32 s91,65536
L_spla6e:
s_sleep 2
buffer_load_dword v253,v242,s[8:11],s53 offen sc1
s_waitcnt vmcnt(0)
v_cmp_ne_u32_e32 vcc,4,v253
s_nop 1
s_cbranch_vccz L_spra6e
s_sub_u32 s91,s91,1
s_cbranch_scc0 L_spla6e
s_branch L_spra6e
L_spa7e:
s_mov_b32 s91,65536
L_spla7e:
s_sleep 2
buffer_load_dword v253,v242,s[8:11],s53 offen sc1
s_waitcnt vmcnt(0)
v_cmp_ne_u32_e32 vcc,4,v253
s_nop 1
s_cbranch_vccz L_spra7e
s_sub_u32 s91,s91,1
s_cbranch_scc0 L_spla7e
s_branch L_spra7e
L_spa8e:
s_mov_b32 s91,65536
L_spla8e:
s_sleep 2
buffer_load_dword v253,v242,s[8:11],s53 offen sc1
s_waitcnt vmcnt(0)
v_cmp_ne_u32_e32 vcc,4,v253
s_nop 1
s_cbranch_vccz L_spra8e
s_sub_u32 s91,s91,1
s_cbranch_scc0 L_spla8e
s_branch L_spra8e
L_spa9e:
s_mov_b32 s91,65536
L_spla9e:
s_sleep 2
buffer_load_dword v253,v242,s[8:11],s53 offen sc1
s_waitcnt vmcnt(0)
v_cmp_ne_u32_e32 vcc,4,v253
s_nop 1
s_cbranch_vccz L_spra9e
s_sub_u32 s91,s91,1
s_cbranch_scc0 L_spla9e
s_branch L_spra9e
L_spa10e:
s_mov_b32 s91,65536
L_spla10e:
s_sleep 2
buffer_load_dword v253,v242,s[8:11],s53 offen sc1
s_waitcnt vmcnt(0)
v_cmp_ne_u32_e32 vcc,4,v253
s_nop 1
s_cbranch_vccz L_spra10e
s_sub_u32 s91,s91,1
s_cbranch_scc0 L_spla10e
s_branch L_spra10e
L_sppe:
s_mov_b32 s91,65536
L_splpe:
s_sleep 2
buffer_load_dword v253,v242,s[8:11],s53 offen sc1
s_waitcnt vmcnt(0)
v_cmp_ne_u32_e32 vcc,4,v253
s_nop 1
s_cbranch_vccz L_sprpe
s_sub_u32 s91,s91,1
s_cbranch_scc0 L_splpe
s_branch L_sprpe
L_spx0e:
s_mov_b32 s91,65536
L_splx0e:
lv 16,244,82,0
lv 20,244,82,1024
lv 24,244,82,2048
lv 28,244,82,3072
s_waitcnt vmcnt(0)
v_max_u32_e32 v248,v16,v17
v_max3_u32 v248,v248,v18,v19
v_max3_u32 v248,v248,v20,v21
v_max3_u32 v248,v248,v22,v23
v_max3_u32 v248,v248,v24,v25
v_max3_u32 v248,v248,v26,v27
v_max3_u32 v248,v248,v28,v29
v_max3_u32 v248,v248,v30,v31
v_cmp_eq_u32_e32 vcc,-1,v248
s_nop 1
s_cbranch_vccz L_sprx0e
s_sub_u32 s91,s91,1
s_cbranch_scc0 L_splx0e
s_branch L_sprx0e
L_spx1e:
s_mov_b32 s91,65536
L_splx1e:
lv 48,244,83,0
lv 52,244,83,1024
lv 56,244,83,2048
lv 60,244,83,3072
s_waitcnt vmcnt(0)
v_max_u32_e32 v248,v48,v49
v_max3_u32 v248,v248,v50,v51
v_max3_u32 v248,v248,v52,v53
v_max3_u32 v248,v248,v54,v55
v_max3_u32 v248,v248,v56,v57
v_max3_u32 v248,v248,v58,v59
v_max3_u32 v248,v248,v60,v61
v_max3_u32 v248,v248,v62,v63
v_cmp_eq_u32_e32 vcc,-1,v248
s_nop 1
s_cbranch_vccz L_sprx1e
s_sub_u32 s91,s91,1
s_cbranch_scc0 L_splx1e
s_branch L_sprx1e
L_spx2e:
s_mov_b32 s91,65536
L_splx2e:
lv 80,244,84,0
lv 84,244,84,1024
lv 88,244,84,2048
lv 92,244,84,3072
s_waitcnt vmcnt(0)
v_max_u32_e32 v248,v80,v81
v_max3_u32 v248,v248,v82,v83
v_max3_u32 v248,v248,v84,v85
v_max3_u32 v248,v248,v86,v87
v_max3_u32 v248,v248,v88,v89
v_max3_u32 v248,v248,v90,v91
v_max3_u32 v248,v248,v92,v93
v_max3_u32 v248,v248,v94,v95
v_cmp_eq_u32_e32 vcc,-1,v248
s_nop 1
s_cbranch_vccz L_sprx2e
s_sub_u32 s91,s91,1
s_cbranch_scc0 L_splx2e
s_branch L_sprx2e
L_spbo:
s_mov_b32 s91,65536
L_splbo:
s_sleep 2
buffer_load_dword v253,v224,s[8:11],s53 offen sc1
s_waitcnt vmcnt(0)
v_cmp_ne_u32_e32 vcc,4,v253
s_nop 1
s_cbranch_vccz L_sprbo
s_sub_u32 s91,s91,1
s_cbranch_scc0 L_splbo
s_branch L_sprbo
L_spa3o:
s_mov_b32 s91,65536
L_spla3o:
s_sleep 2
buffer_load_dword v253,v242,s[8:11],s53 offen sc1
s_waitcnt vmcnt(0)
v_cmp_ne_u32_e32 vcc,4,v253
s_nop 1
s_cbranch_vccz L_spra3o
s_sub_u32 s91,s91,1
s_cbranch_scc0 L_spla3o
s_branch L_spra3o
L_spa4o:
s_mov_b32 s91,65536
L_spla4o:
s_sleep 2
buffer_load_dword v253,v242,s[8:11],s53 offen sc1
s_waitcnt vmcnt(0)
v_cmp_ne_u32_e32 vcc,4,v253
s_nop 1
s_cbranch_vccz L_spra4o
s_sub_u32 s91,s91,1
s_cbranch_scc0 L_spla4o
s_branch L_spra4o
L_spa5o:
s_mov_b32 s91,65536
L_spla5o:
s_sleep 2
buffer_load_dword v253,v242,s[8:11],s53 offen sc1
s_waitcnt vmcnt(0)
v_cmp_ne_u32_e32 vcc,4,v253
s_nop 1
s_cbranch_vccz L_spra5o
s_sub_u32 s91,s91,1
s_cbranch_scc0 L_spla5o
s_branch L_spra5o
L_spa6o:
s_mov_b32 s91,65536
L_spla6o:
s_sleep 2
buffer_load_dword v253,v242,s[8:11],s53 offen sc1
s_waitcnt vmcnt(0)
v_cmp_ne_u32_e32 vcc,4,v253
s_nop 1
s_cbranch_vccz L_spra6o
s_sub_u32 s91,s91,1
s_cbranch_scc0 L_spla6o
s_branch L_spra6o
L_spa7o:
s_mov_b32 s91,65536
L_spla7o:
s_sleep 2
buffer_load_dword v253,v242,s[8:11],s53 offen sc1
s_waitcnt vmcnt(0)
v_cmp_ne_u32_e32 vcc,4,v253
s_nop 1
s_cbranch_vccz L_spra7o
s_sub_u32 s91,s91,1
s_cbranch_scc0 L_spla7o
s_branch L_spra7o
L_spa8o:
s_mov_b32 s91,65536
L_spla8o:
s_sleep 2
buffer_load_dword v253,v242,s[8:11],s53 offen sc1
s_waitcnt vmcnt(0)
v_cmp_ne_u32_e32 vcc,4,v253
s_nop 1
s_cbranch_vccz L_spra8o
s_sub_u32 s91,s91,1
s_cbranch_scc0 L_spla8o
s_branch L_spra8o
L_sppo:
s_mov_b32 s91,65536
L_splpo:
s_sleep 2
buffer_load_dword v253,v242,s[8:11],s53 offen sc1
s_waitcnt vmcnt(0)
v_cmp_ne_u32_e32 vcc,4,v253
s_nop 1
s_cbranch_vccz L_sprpo
s_sub_u32 s91,s91,1
s_cbranch_scc0 L_splpo
s_branch L_sprpo
L_spx0o:
s_mov_b32 s91,65536
L_splx0o:
lv 16,244,82,0
lv 20,244,82,1024
lv 24,244,82,2048
lv 28,244,82,3072
s_waitcnt vmcnt(0)
v_max_u32_e32 v248,v16,v17
v_max3_u32 v248,v248,v18,v19
v_max3_u32 v248,v248,v20,v21
v_max3_u32 v248,v248,v22,v23
v_max3_u32 v248,v248,v24,v25
v_max3_u32 v248,v248,v26,v27
v_max3_u32 v248,v248,v28,v29
v_max3_u32 v248,v248,v30,v31
v_cmp_eq_u32_e32 vcc,-1,v248
s_nop 1
s_cbranch_vccz L_sprx0o
s_sub_u32 s91,s91,1
s_cbranch_scc0 L_splx0o
s_branch L_sprx0o
L_spx1o:
s_mov_b32 s91,65536
L_splx1o:
lv 48,244,83,0
lv 52,244,83,1024
lv 56,244,83,2048
lv 60,244,83,3072
s_waitcnt vmcnt(0)
v_max_u32_e32 v248,v48,v49
v_max3_u32 v248,v248,v50,v51
v_max3_u32 v248,v248,v52,v53
v_max3_u32 v248,v248,v54,v55
v_max3_u32 v248,v248,v56,v57
v_max3_u32 v248,v248,v58,v59
v_max3_u32 v248,v248,v60,v61
v_max3_u32 v248,v248,v62,v63
v_cmp_eq_u32_e32 vcc,-1,v248
s_nop 1
s_cbranch_vccz L_sprx1o
s_sub_u32 s91,s91,1
s_cbranch_scc0 L_splx1o
s_branch L_sprx1o
L_spx2o:
s_mov_b32 s91,65536
L_splx2o:
lv 80,244,84,0
lv 84,244,84,1024
lv 88,244,84,2048
lv 92,244,84,3072
s_waitcnt vmcnt(0)
v_max_u32_e32 v248,v80,v81
v_max3_u32 v248,v248,v82,v83
v_max3_u32 v248,v248,v84,v85
v_max3_u32 v248,v248,v86,v87
v_max3_u32 v248,v248,v88,v89
v_max3_u32 v248,v248,v90,v91
v_max3_u32 v248,v248,v92,v93
v_max3_u32 v248,v248,v94,v95
v_cmp_eq_u32_e32 vcc,-1,v248
s_nop 1
s_cbranch_vccz L_sprx2o
s_sub_u32 s91,s91,1
s_cbranch_scc0 L_splx2o
s_branch L_sprx2o
L_att_end:
s_endpgm
.p2align 12
.purgem cv
.purgem cvaa
.purgem cvvv
.purgem dl
.purgem ds
.purgem ex
.purgem la
.purgem lv
.purgem maaa
.purgem maav
.purgem mavv
.purgem mvaa
.purgem mvav
.purgem mvva
.purgem mvvv
.purgem ra
.purgem rv
.purgem ta
.purgem tv
.purgem zaaa
.purgem zaav
.purgem zvaa
.purgem zvav
.purgem zvva
.purgem zvvv
)"
        :: "s"(__builtin_amdgcn_kernarg_segment_ptr()), "s"(blockIdx.x), "v"(tid)
        : "v0", "v1", "v2", "v3", "v4", "v5", "v6", "v7", "v8", "v9", "v10", "v11", "v12", "v13", "v14", "v15", "v16", "v17", "v18", "v19", "v20", "v21", "v22", "v23", "v24", "v25", "v26", "v27", "v28", "v29", "v30", "v31", "v32", "v33", "v34", "v35", "v36", "v37", "v38", "v39", "v40", "v41", "v42", "v43", "v44", "v45", "v46", "v47", "v48", "v49", "v50", "v51", "v52", "v53", "v54", "v55", "v56", "v57", "v58", "v59", "v60", "v61", "v62", "v63", "v64", "v65", "v66", "v67", "v68", "v69", "v70", "v71", "v72", "v73", "v74", "v75", "v76", "v77", "v78", "v79", "v80", "v81", "v82", "v83", "v84", "v85", "v86", "v87", "v88", "v89", "v90", "v91", "v92", "v93", "v94", "v95", "v96", "v97", "v98", "v99", "v100", "v101", "v102", "v103", "v104", "v105", "v106", "v107", "v108", "v109", "v110", "v111", "v112", "v113", "v114", "v115", "v116", "v117", "v118", "v119", "v120", "v121", "v122", "v123", "v124", "v125", "v126", "v127", "v128", "v129", "v130", "v131", "v132", "v133", "v134", "v135", "v136", "v137", "v138", "v139", "v140", "v141", "v142", "v143", "v144", "v145", "v146", "v147", "v148", "v149", "v150", "v151", "v152", "v153", "v154", "v155", "v156", "v157", "v158", "v159", "v160", "v161", "v162", "v163", "v164", "v165", "v166", "v167", "v168", "v169", "v170", "v171", "v172", "v173", "v174", "v175", "v176", "v177", "v178", "v179", "v180", "v181", "v182", "v183", "v184", "v185", "v186", "v187", "v188", "v189", "v190", "v191", "v192", "v193", "v194", "v195", "v196", "v197", "v198", "v199", "v200", "v201", "v202", "v203", "v204", "v205", "v206", "v207", "v208", "v209", "v210", "v211", "v212", "v213", "v214", "v215", "v216", "v217", "v218", "v219", "v220", "v221", "v222", "v223", "v224", "v225", "v226", "v227", "v228", "v229", "v230", "v231", "v232", "v233", "v234", "v235", "v236", "v237", "v238", "v239", "v240", "v241", "v242", "v243", "v244", "v245", "v246", "v247", "v248", "v249", "v250", "v251", "v252", "v253", "v254", "a0", "a1", "a2", "a3", "a4", "a5", "a6", "a7", "a8", "a9", "a10", "a11", "a12", "a13", "a14", "a15", "a16", "a17", "a18", "a19", "a20", "a21", "a22", "a23", "a24", "a25", "a26", "a27", "a28", "a29", "a30", "a31", "a32", "a33", "a34", "a35", "a36", "a37", "a38", "a39", "a40", "a41", "a42", "a43", "a44", "a45", "a46", "a47", "a48", "a49", "a50", "a51", "a52", "a53", "a54", "a55", "a56", "a57", "a58", "a59", "a60", "a61", "a62", "a63", "a64", "a65", "a66", "a67", "a68", "a69", "a70", "a71", "a72", "a73", "a74", "a75", "a76", "a77", "a78", "a79", "a80", "a81", "a82", "a83", "a84", "a85", "a86", "a87", "a88", "a89", "a90", "a91", "a92", "a93", "a94", "a95", "a96", "a97", "a98", "a99", "a100", "a101", "a102", "a103", "a104", "a105", "a106", "a107", "a108", "a109", "a110", "a111", "a112", "a113", "a114", "a115", "a116", "a117", "a118", "a119", "a120", "a121", "a122", "a123", "a124", "a125", "a126", "a127", "a128", "a129", "a130", "a131", "a132", "a133", "a134", "a135", "a136", "a137", "a138", "a139", "a140", "a141", "a142", "a143", "a144", "a145", "a146", "a147", "a148", "a149", "a150", "a151", "a152", "a153", "a154", "a155", "a156", "a157", "a158", "a159", "a160", "a161", "a162", "a163", "a164", "a165", "a166", "a167", "a168", "a169", "a170", "a171", "a172", "a173", "a174", "a175", "a176", "a177", "a178", "a179", "a180", "a181", "a182", "a183", "a184", "a185", "a186", "a187", "a188", "a189", "a190", "a191", "a192", "a193", "a194", "a195", "a196", "a197", "a198", "a199", "a200", "a201", "a202", "a203", "a204", "a205", "a206", "a207", "a208", "a209", "a210", "a211", "a212", "a213", "a214", "a215", "a216", "a217", "a218", "a219", "a220", "a221", "a222", "a223", "a224", "a225", "a226", "a227", "a228", "a229", "a230", "a231", "a232", "a233", "a234", "a235", "a236", "a237", "a238", "a239", "a240", "a241", "a242", "a243", "a244", "a245", "a246", "a247", "a248", "a249", "a250", "a251", "a252", "a253", "a254", "a255", "s4", "s5", "s6", "s7", "s8", "s9", "s10", "s11", "s12", "s13", "s14", "s15", "s16", "s17", "s18", "s19", "s20", "s21", "s22", "s23", "s24", "s25", "s26", "s27", "s28", "s29", "s30", "s31", "s32", "s33", "s34", "s35", "s36", "s37", "s38", "s39", "s40", "s41", "s42", "s43", "s44", "s45", "s46", "s47", "s48", "s49", "s50", "s51", "s52", "s53", "s54", "s55", "s56", "s57", "s58", "s59", "s60", "s61", "s62", "s63", "s64", "s65", "s66", "s67", "s68", "s69", "s70", "s71", "s72", "s73", "s74", "s75", "s76", "s77", "s78", "s79", "s80", "s81", "s82", "s83", "s84", "s85", "s86", "s87", "s88", "s89", "s90", "s91", "s92", "s93", "s94", "s95", "s96", "s97", "s98", "s99", "s100", "s101", "vcc", "scc", "m0", "memory");
}

extern "C" void kernel_launch(void* const* d_in, const int* in_sizes, int n_in, void* d_out, int out_size, void* d_ws, size_t ws_size, hipStream_t stream) {
    (void)in_sizes; (void)n_in; (void)out_size; (void)ws_size;
    char* ws = (char*)d_ws + 0ull;
    Args a;
    a.p[0] = d_in[0]; a.p[1] = d_in[1]; a.p[2] = d_in[2]; a.p[3] = d_in[3]; a.p[4] = d_in[4]; a.p[5] = d_in[5]; a.p[6] = d_in[7]; a.p[7] = d_in[8]; a.p[8] = d_in[9]; a.p[9] = d_out; a.p[10] = ws;
    mega<<<256, 256, 0, stream>>>(a);
}
```

```cpp
#include <hip/hip_runtime.h>
#include <hip/hip_bf16.h>
#include <stdint.h>
#include <type_traits>

#define LAS __attribute__((address_space(3)))
struct Args { const void* p[11]; };
__global__ __launch_bounds__(256, 1)
void mega(Args args) {
    __shared__ __attribute__((aligned(1024))) char smem[163840];
    const unsigned tid = threadIdx.x + (unsigned)(uintptr_t)(LAS char*)smem;
    asm volatile(
R"(.macro cv a,b,c
v_cvt_pk_bf16_f32 v\a,v\b,v\c
.endm
.macro cvaa a,b,c,d
v_mfma_f32_16x16x32_bf16 v[\a:\a+3],a[\b:\b+3],a[\c:\c+3],v[\d:\d+3]
.endm
.macro cvvv a,b,c,d
v_mfma_f32_16x16x32_bf16 v[\a:\a+3],v[\b:\b+3],v[\c:\c+3],v[\d:\d+3]
.endm
.macro dl a,b,c,d
buffer_load_dwordx4 v\a,s[\b:\b+3],s\c offen offset:\d lds
.endm
.macro ds a,b,c,d
buffer_load_dwordx4 v\a,s[\b:\b+3],s\c offen offset:\d sc1 lds
.endm
.macro ex a
v_exp_f32_e32 v\a,v\a
.endm
.macro la a,b,c,d
buffer_load_dwordx4 a[\a:\a+3],v\b,s[8:11],s\c offen offset:\d sc1
.endm
.macro lv a,b,c,d
buffer_load_dwordx4 v[\a:\a+3],v\b,s[8:11],s\c offen offset:\d sc1
.endm
.macro maaa a,b,c
v_mfma_f32_16x16x32_bf16 a[\a:\a+3],a[\b:\b+3],a[\c:\c+3],a[\a:\a+3]
.endm
.macro maav a,b,c
v_mfma_f32_16x16x32_bf16 a[\a:\a+3],a[\b:\b+3],v[\c:\c+3],a[\a:\a+3]
.endm
.macro mavv a,b,c
v_mfma_f32_16x16x32_bf16 a[\a:\a+3],v[\b:\b+3],v[\c:\c+3],a[\a:\a+3]
.endm
.macro mvaa a,b,c
v_mfma_f32_16x16x32_bf16 v[\a:\a+3],a[\b:\b+3],a[\c:\c+3],v[\a:\a+3]
.endm
.macro mvav a,b,c
v_mfma_f32_16x16x32_bf16 v[\a:\a+3],a[\b:\b+3],v[\c:\c+3],v[\a:\a+3]
.endm
.macro mvva a,b,c
v_mfma_f32_16x16x32_bf16 v[\a:\a+3],v[\b:\b+3],a[\c:\c+3],v[\a:\a+3]
.endm
.macro mvvv a,b,c
v_mfma_f32_16x16x32_bf16 v[\a:\a+3],v[\b:\b+3],v[\c:\c+3],v[\a:\a+3]
.endm
.macro ra a,b,c
ds_read_b128 a[\a:\a+3],v\b offset:\c
.endm
.macro rv a,b,c
ds_read_b128 v[\a:\a+3],v\b offset:\c
.endm
.macro ta a,b,c
ds_read_b64_tr_b16 a[\a:\a+1],v\b offset:\c
.endm
.macro tv a,b,c
ds_read_b64_tr_b16 v[\a:\a+1],v\b offset:\c
.endm
.macro zaaa a,b,c
v_mfma_f32_16x16x32_bf16 a[\a:\a+3],a[\b:\b+3],a[\c:\c+3],0
.endm
.macro zaav a,b,c
v_mfma_f32_16x16x32_bf16 a[\a:\a+3],a[\b:\b+3],v[\c:\c+3],0
.endm
.macro zvaa a,b,c
v_mfma_f32_16x16x32_bf16 v[\a:\a+3],a[\b:\b+3],a[\c:\c+3],0
.endm
.macro zvav a,b,c
v_mfma_f32_16x16x32_bf16 v[\a:\a+3],a[\b:\b+3],v[\c:\c+3],0
.endm
.macro zvva a,b,c
v_mfma_f32_16x16x32_bf16 v[\a:\a+3],v[\b:\b+3],a[\c:\c+3],0
.endm
.macro zvvv a,b,c
v_mfma_f32_16x16x32_bf16 v[\a:\a+3],v[\b:\b+3],v[\c:\c+3],0
.endm
L_begin:
s_load_dwordx2 s[28:29],%0,72
s_load_dwordx16 s[68:83],%0,0
s_load_dwordx2 s[84:85],%0,64
s_load_dwordx2 s[8:9],%0,80
v_lshrrev_b32_e32 v246,6,%2
v_and_b32_e32 v247,63,%2
v_lshlrev_b32_e32 v244,4,v247
v_readfirstlane_b32 s12,v246
s_and_b32 s20,%1,7
s_lshr_b32 s21,%1,3
s_getpc_b64 s[96:97]
L_att_pc:
s_lshl_b32 s86,s21,2
s_add_u32 s86,s86,s12
s_mul_i32 s86,s86,256
v_lshl_add_u32 v246,v247,7,s86
v_min_u32_e32 v246,L_att_end-L_att_pc-4,v246
s_mov_b64 exec,3
global_load_dword a188,v246,s[96:97]
s_mov_b64 exec,-1
s_and_b32 s19,s21,7
s_lshr_b32 s86,s21,3
s_lshl_b32 s18,s20,2
s_add_u32 s18,s18,s86
v_lshrrev_b32_e32 v248,3,v247
v_and_b32_e32 v250,112,v244
v_and_b32_e32 v249,48,v247
v_xor_b32_e32 v249,v249,v250
v_and_b32_e32 v251,16,v247
v_lshlrev_b32_e32 v251,1,v251
v_xor_b32_e32 v250,v250,v251
v_lshl_add_u32 v224,v248,10,v249
v_xor_b32_e32 v225,64,v224
v_add_u32_e32 v227,21504,v225
v_add_u32_e32 v226,14336,v224
v_add_u32_e32 v225,7168,v225
v_lshl_add_u32 v228,v248,13,v250
v_xor_b32_e32 v229,64,v228
v_add_u32_e32 v231,193536,v229
v_add_u32_e32 v230,129024,v228
v_add_u32_e32 v229,64512,v229
s_lshl_b32 s86,s19,2
s_add_u32 s86,s86,s12
s_lshl_b32 s52,s86,16
s_bfe_u32 s87,s21,65539
s_lshl_b32 s88,s20,1
s_add_u32 s87,s87,s88
s_lshl_b32 s87,s87,2
s_add_u32 s87,s87,s12
s_lshl_b32 s53,s87,7
s_mov_b32 s54,s53
s_mov_b32 s39,131072
s_mov_b32 s43,131072
s_mov_b32 s47,131072
s_mov_b32 s51,131072
s_mov_b32 s38,2097152
s_mov_b32 s42,4194304
s_mov_b32 s46,4194304
s_mov_b32 s56,32768
s_mov_b32 s60,-32640
s_mov_b32 s57,262144
s_mov_b32 s58,262144
s_bfe_u32 s91,s21,65536
s_lshl_b32 s91,s91,18
s_add_u32 s53,s53,s91
s_mov_b32 s54,s53
s_lshl_b32 s91,s91,1
s_sub_u32 s57,s57,s91
s_add_u32 s58,s58,s91
s_lshl_b32 s98,s12,13
s_lshl_b32 s62,s12,12
s_add_u32 s62,s62,98304
s_lshl_b32 s35,%1,2
s_add_u32 s35,s35,83896320
v_lshrrev_b32_e32 v48,2,v247
v_and_b32_e32 v49,3,v247
v_lshlrev_b32_e32 v49,7,v49
v_lshl_add_u32 v48,v48,13,v49
s_lshl_b32 s89,s86,14
s_and_b32 s90,s53,7680
s_add_u32 s90,s90,s89
s_lshr_b32 s89,s21,3
s_lshl_b32 s89,s89,2
s_add_u32 s89,s89,s12
s_lshl_b32 s89,s89,14
s_lshl_b32 s91,s19,18
s_add_u32 s89,s89,s91
s_cmp_ge_u32 s21,16
s_waitcnt lgkmcnt(0)
s_mov_b64 s[36:37],s[70:71]
s_cselect_b64 s[40:41],s[76:77],s[74:75]
s_mov_b64 s[44:45],s[40:41]
s_bfm_b64 exec,8,0
buffer_load_dword a189,v48,s[40:43],s90 offen
s_mov_b64 exec,-1
s_and_b32 s9,s9,65535
s_mov_b32 s10,83951616
s_mov_b32 s11,131072
buffer_load_dword v44,off,s[8:11],s35 sc1
buffer_load_dword v45,off,s[8:11],s35 offset:1024 sc1
s_add_u32 m0,s62,0
s_nop 0
buffer_load_dwordx4 v228,s[40:43],s53 offen offset:0 nt lds
buffer_load_dwordx4 v229,s[40:43],s53 offen offset:1024 nt lds
s_add_u32 s53,s53,s57
buffer_load_dwordx4 v230,s[44:47],s54 offen offset:2048 nt lds
buffer_load_dwordx4 v231,s[44:47],s54 offen offset:3072 nt lds
s_add_u32 s54,s54,s57
s_add_u32 m0,s98,0
s_nop 0
dl 224,36,52,0
dl 225,36,52,1024
dl 226,36,52,2048
dl 227,36,52,3072
s_add_u32 s52,s52,s56
s_add_u32 m0,s98,4096
s_nop 0
dl 224,36,52,0
dl 225,36,52,1024
dl 226,36,52,2048
dl 227,36,52,3072
s_add_u32 s52,s52,s60
s_add_u32 s61,s98,65536
s_mov_b32 s63,0
s_lshl_b32 s88,%1,2
s_add_u32 s88,s88,s12
s_lshl_b32 s88,s88,12
s_add_u32 s30,s68,s88
s_addc_u32 s31,s69,0
v_mov_b32_e32 v40,v247
v_and_b32_e32 v246,15,v40
v_lshlrev_b32_e32 v248,3,v40
v_xor_b32_e32 v248,v248,v40
v_and_b32_e32 v248,112,v248
v_lshl_add_u32 v232,v246,7,v248
s_and_b32 s86,s98,16384
v_add_u32_e32 v232,s86,v232
s_bfe_u32 s86,s21,65536
s_lshl_b32 s86,s86,6
v_xor_b32_e32 v232,s86,v232
v_xor_b32_e32 v233,64,v232
v_and_b32_e32 v246,12,v40
v_lshlrev_b32_e32 v246,5,v246
v_and_b32_e32 v248,3,v40
v_lshl_add_u32 v246,v248,3,v246
v_and_b32_e32 v248,24,v40
v_lshl_add_u32 v246,v248,2,v246
s_and_b32 s87,s98,8192
s_add_u32 s87,s87,98304
v_add_u32_e32 v246,s87,v246
v_and_b32_e32 v248,48,v40
v_lshl_add_u32 v234,v248,6,v246
v_xor_b32_e32 v235,32,v234
v_xor_b32_e32 v236,64,v234
v_xor_b32_e32 v237,96,v234
s_lshl_b32 s86,s19,1
s_add_u32 s86,s86,2
s_lshl_b32 s15,1,s86
s_xor_b32 s87,s19,15
s_lshl_b32 s22,s87,7
s_lshl_b32 s23,s19,7
s_lshl_b32 s87,s87,2
s_lshl_b32 s88,s19,2
s_add_u32 s87,s87,s12
s_add_u32 s88,s88,s12
s_lshl_b32 s90,s18,13
s_add_u32 s90,s90,62914560
s_mul_i32 s14,s87,327680
s_add_u32 s14,s14,s90
s_mul_i32 s13,s88,327680
s_add_u32 s13,s13,s90
s_mul_i32 s90,s18,524288
s_lshl_b32 s91,s12,12
s_add_u32 s90,s90,s91
s_add_u32 s90,s90,16777216
v_add_u32_e32 v249,s90,v244
v_add_u32_e32 v255,16777216,v249
v_accvgpr_write_b32 a240,v249
v_accvgpr_write_b32 a241,v255
s_or_b32 s86,%1,s12
s_cselect_b32 s65,0,4
v_lshrrev_b32_e32 v241,3,v40
v_lshlrev_b32_e32 v241,27,v241
v_lshl_add_u32 v241,v40,7,v241
s_cmp_lt_u32 s12,2
s_cselect_b64 s[48:49],s[72:73],s[80:81]
s_cselect_b32 s50,8,6
s_lshl_b32 s50,s50,20
s_and_b32 s86,s12,1
s_lshl_b32 s87,%1,1
s_add_u32 s86,s86,s87
s_lshl_b32 s55,s86,10
s_mov_b32 s59,524288
s_cmp_lt_u32 s12,2
s_cselect_b32 s87,12,9
v_cmp_gt_u32_e32 vcc,s87,v40
v_lshlrev_b32_e32 v241,7,v40
v_mov_b32_e32 v41,134217728
s_lshl_b32 s87,s87,7
s_mul_i32 s55,s86,s87
s_lshl_b32 s59,s87,9
v_cndmask_b32_e32 v241,v41,v241,vcc
s_cmp_lt_u32 s12,2
s_cbranch_scc1 L_lx
s_cmp_eq_u32 s12,2
s_cselect_b32 s87,18,12
v_cmp_gt_u32_e32 vcc,s87,v40
v_lshlrev_b32_e32 v241,7,v40
s_mul_i32 s55,%1,2304
v_cndmask_b32_e32 v241,v41,v241,vcc
s_cmp_eq_u32 s12,2
s_cbranch_scc1 L_lx
s_lshl_b32 s55,%1,14
s_add_u32 s86,s55,16384
s_mov_b64 s[48:49],s[68:69]
s_mov_b32 s50,s86
s_mov_b32 s55,s55
s_mov_b32 s59,1536
L_lx:
s_mov_b32 s33,s50
s_mov_b32 s34,s59
s_mov_b32 s50,0
s_mov_b32 s59,0
v_and_b32_e32 v246,15,v40
v_and_b32_e32 v248,48,v40
v_lshlrev_b32_e32 v249,2,v40
v_and_b32_e32 v249,32,v249
v_xor_b32_e32 v248,v248,v249
v_lshl_add_u32 v240,v246,6,v248
s_lshr_b32 s86,s12,1
s_lshl_b32 s86,s86,10
s_add_u32 s86,s86,112640
v_add_u32_e32 v240,s86,v240
v_and_b32_e32 v246,12,v40
v_lshlrev_b32_e32 v246,5,v246
v_and_b32_e32 v248,3,v40
v_lshl_add_u32 v246,v248,3,v246
v_and_b32_e32 v248,24,v40
v_lshl_add_u32 v246,v248,2,v246
v_add_u32_e32 v246,98304,v246
v_and_b32_e32 v248,32,v40
v_lshl_add_u32 v238,v248,5,v246
v_xor_b32_e32 v239,32,v238
v_lshrrev_b32_e32 v248,3,v40
v_and_b32_e32 v250,112,v244
v_and_b32_e32 v249,48,v40
v_xor_b32_e32 v249,v249,v250
v_and_b32_e32 v251,16,v40
v_lshlrev_b32_e32 v251,1,v251
v_xor_b32_e32 v250,v250,v251
v_lshl_add_u32 v48,v248,11,v249
v_xor_b32_e32 v49,64,v48
v_add_u32_e32 v51,46080,v49
v_add_u32_e32 v50,30720,v48
v_add_u32_e32 v49,15360,v49
v_mov_b32_e32 v52,v228
v_mov_b32_e32 v53,v229
v_mov_b32_e32 v54,v230
v_mov_b32_e32 v55,v231
s_cmp_lt_u32 s12,2
s_cbranch_scc1 L_pqx
v_mul_u32_u24_e32 v246,6144,v248
v_and_b32_e32 v251,48,v250
v_add_u32_e32 v246,v246,v251
v_and_b32_e32 v251,64,v250
v_lshl_add_u32 v52,v251,5,v246
v_xor_b32_e32 v252,64,v251
v_lshl_add_u32 v53,v252,5,v246
v_add_u32_e32 v55,144384,v53
v_add_u32_e32 v54,96256,v52
v_add_u32_e32 v53,48128,v53
s_cmp_eq_u32 s12,2
s_cbranch_scc1 L_pqx
v_mul_u32_u24_e32 v251,768,v251
v_add_u32_e32 v246,v246,v251
v_add_u32_e32 v52,4096,v246
v_add_u32_e32 v53,101376,v246
v_lshrrev_b32_e32 v252,2,v40
v_and_b32_e32 v253,32,v40
v_and_b32_e32 v250,48,v244
v_xor_b32_e32 v253,v253,v250
v_lshl_add_u32 v246,v252,11,v253
s_lshl_b32 s86,s22,11
s_sub_u32 s86,s86,34816
v_add_u32_e32 v54,s86,v246
s_lshl_b32 s86,s23,11
s_sub_u32 s86,s86,35840
v_add_u32_e32 v55,s86,v246
v_max_i32_e32 v55,0,v55
L_pqx:
v_accvgpr_write_b32 a242,v48
v_accvgpr_write_b32 a243,v49
v_accvgpr_write_b32 a244,v50
v_accvgpr_write_b32 a245,v51
v_accvgpr_write_b32 a246,v52
v_accvgpr_write_b32 a247,v53
v_accvgpr_write_b32 a248,v54
v_accvgpr_write_b32 a249,v55
s_add_u32 m0,s62,16384
s_nop 0
buffer_load_dwordx4 v228,s[40:43],s53 offen offset:0 nt lds
buffer_load_dwordx4 v229,s[40:43],s53 offen offset:1024 nt lds
s_add_u32 s53,s53,s58
buffer_load_dwordx4 v230,s[44:47],s54 offen offset:2048 nt lds
buffer_load_dwordx4 v231,s[44:47],s54 offen offset:3072 nt lds
s_add_u32 s54,s54,s58
s_add_u32 m0,s98,32768
s_nop 0
dl 224,36,52,0
dl 225,36,52,1024
dl 226,36,52,2048
dl 227,36,52,3072
s_add_u32 s52,s52,s56
s_waitcnt vmcnt(20)
s_cmp_lg_u32 s12,0
s_cbranch_scc1 L_rqx
v_readfirstlane_b32 s32,v44
v_readfirstlane_b32 s86,v45
s_add_u32 s32,s32,1
s_cmp_eq_u32 s32,s86
s_addc_u32 s32,s32,0
v_mov_b32_e32 v44,s32
v_mov_b32_e32 v45,0
v_lshlrev_b32_e32 v46,3,v244
v_mov_b32_e32 v47,0
s_lshl_b32 s86,%1,7
s_add_u32 s87,s86,83914752
s_and_b32 s86,s86,16383
s_add_u32 s86,s86,83898368
s_mov_b64 exec,1
buffer_store_dword v44,off,s[8:11],s35 sc0 sc1
v_mov_b32_e32 v48,0
v_mov_b32_e32 v49,0
buffer_store_dwordx4 v[46:49],off,s[8:11],s86 sc0 sc1
buffer_store_dwordx4 v[46:49],off,s[8:11],s87 sc0 sc1
s_cmp_lg_u32 %1,0
s_cbranch_scc1 L_rqy
s_mov_b64 exec,65535
s_mov_b32 s86,83894272
buffer_store_dword v45,v46,s[8:11],s86 offen sc0 sc1
L_rqy:
s_mov_b64 exec,-1
L_rqx:
L_vr0:
s_cmp_ge_u32 s21,16
s_cbranch_scc1 L_vtile
s_waitcnt vmcnt(8)
s_barrier
ta 32,234,0
ta 34,234,512
ra 0,232,0
ta 36,235,0
ta 38,235,512
ra 4,232,2048
ra 8,232,4096
ra 12,232,6144
ra 16,232,8192
ra 20,232,10240
ra 24,232,12288
ra 28,232,14336
s_add_u32 m0,s62,32768
ta 40,236,0
buffer_load_dwordx4 v228,s[40:43],s53 offen offset:0 nt lds
ta 42,236,512
ta 44,237,0
ta 46,237,512
ta 48,234,4096
ta 50,234,4608
ta 52,235,4096
ta 54,235,4608
ta 56,236,4096
ta 58,236,4608
ta 60,237,4096
ta 62,237,4608
s_cmp_ge_u32 s63,65536
s_cselect_b32 s99,-2,1
s_lshl_b32 s99,s99,15
s_add_u32 s63,s63,s99
v_add_u32_e32 v232,s99,v232
s_mov_b32 s64,1
s_waitcnt lgkmcnt(15)
zaaa 128,32,0
zaaa 132,36,0
zaaa 140,36,4
zaaa 136,32,4
buffer_load_dwordx4 v229,s[40:43],s53 offen offset:1024 nt lds
zvaa 32,32,8
s_add_u32 s53,s53,s57
zvaa 36,36,8
zvaa 44,36,12
zvaa 40,32,12
buffer_load_dwordx4 v230,s[44:47],s54 offen offset:2048 nt lds
zvaa 96,32,16
zvaa 100,36,16
s_waitcnt lgkmcnt(0)
zvaa 108,36,20
zvaa 104,32,20
buffer_load_dwordx4 v231,s[44:47],s54 offen offset:3072 nt lds
zvaa 160,32,24
s_add_u32 m0,s98,36864
s_add_u32 s54,s54,s57
zvaa 164,36,24
zvaa 172,36,28
zvaa 168,32,28
dl 224,36,52,0
zvaa 184,40,28
zvaa 188,44,28
zvaa 180,44,24
zvaa 176,40,24
dl 225,36,52,1024
zvaa 120,40,20
zvaa 124,44,20
zvaa 116,44,16
zvaa 112,40,16
dl 226,36,52,2048
zvaa 56,40,12
s_waitcnt vmcnt(11)
s_barrier
zvaa 60,44,12
ta 96,234,16384
zvaa 52,44,8
ta 98,234,16896
zvaa 48,40,8
dl 227,36,52,3072
zaaa 152,40,4
s_add_u32 m0,s62,49152
ra 64,233,0
s_add_u32 s52,s52,s60
zaaa 156,44,4
ta 100,235,16384
zaaa 148,44,0
ta 102,235,16896
zaaa 144,40,0
buffer_load_dwordx4 v228,s[40:43],s53 offen offset:0 nt lds
zvaa 0,48,0
ra 68,233,2048
zvaa 4,52,0
ta 104,236,16384
zvaa 12,52,4
ta 106,236,16896
zvaa 8,48,4
buffer_load_dwordx4 v229,s[40:43],s53 offen offset:1024 nt lds
zvaa 64,48,8
s_add_u32 s53,s53,s58
ra 72,233,4096
zvaa 68,52,8
ta 108,237,16384
zvaa 76,52,12
ta 110,237,16896
zvaa 72,48,12
buffer_load_dwordx4 v230,s[44:47],s54 offen offset:2048 nt lds
zvaa 128,48,16
ra 76,233,6144
zvaa 132,52,16
ta 112,234,20480
zvaa 140,52,20
ta 114,234,20992
zvaa 136,48,20
buffer_load_dwordx4 v231,s[44:47],s54 offen offset:3072 nt lds
zvaa 192,48,24
s_add_u32 m0,s61,0
ra 80,233,8192
s_add_u32 s54,s54,s58
zvaa 196,52,24
ta 116,235,20480
zvaa 204,52,28
ta 118,235,20992
zvaa 200,48,28
dl 224,36,52,0
zvaa 216,56,28
ra 84,233,10240
zvaa 220,60,28
ta 120,236,20480
zvaa 212,60,24
ta 122,236,20992
zvaa 208,56,24
dl 225,36,52,1024
ra 88,233,12288
zvaa 152,56,20
ta 124,237,20480
zvaa 156,60,20
ta 126,237,20992
zvaa 148,60,16
ra 92,233,14336
zvaa 144,56,16
dl 226,36,52,2048
zvaa 88,56,12
zvaa 92,60,12
zvaa 84,60,8
zvaa 80,56,8
dl 227,36,52,3072
zvaa 24,56,4
s_add_u32 s52,s52,s56
zvaa 28,60,4
zvaa 20,60,0
zvaa 16,56,0
buffer_load_dword a184,v241,s[48:51],s55 offen
s_add_u32 s55,s55,s59
s_waitcnt vmcnt(9)
s_waitcnt lgkmcnt(0)
s_barrier
L_k_head:
maaa 128,96,64
ta 32,234,32768
maaa 132,100,64
s_add_u32 m0,s62,0
ta 34,234,33280
maaa 140,100,68
buffer_load_dwordx4 v228,s[40:43],s53 offen offset:0 nt lds
maaa 136,96,68
mvaa 32,96,72
ra 0,232,0
mvaa 36,100,72
mvaa 44,100,76
ta 36,235,32768
mvaa 40,96,76
buffer_load_dwordx4 v229,s[40:43],s53 offen offset:1024 nt lds
mvaa 96,96,80
s_add_u32 s53,s53,s57
mvaa 100,100,80
ta 38,235,33280
mvaa 108,100,84
mvaa 104,96,84
ra 4,232,2048
mvaa 160,96,88
buffer_load_dwordx4 v230,s[44:47],s54 offen offset:2048 nt lds
mvaa 164,100,88
mvaa 172,100,92
ta 40,236,32768
mvaa 168,96,92
mvaa 184,104,92
ta 42,236,33280
mvaa 188,108,92
buffer_load_dwordx4 v231,s[44:47],s54 offen offset:3072 nt lds
mvaa 180,108,88
s_add_u32 s54,s54,s57
mvaa 176,104,88
ra 8,232,4096
mvaa 120,104,84
mvaa 124,108,84
ta 44,237,32768
mvaa 116,108,80
s_add_u32 m0,s61,4096
mvaa 112,104,80
dl 224,36,52,0
mvaa 56,104,76
ta 46,237,33280
mvaa 60,108,76
mvaa 52,108,72
ra 12,232,6144
mvaa 48,104,72
maaa 152,104,68
ta 48,234,36864
maaa 156,108,68
dl 225,36,52,1024
maaa 148,108,64
ta 50,234,37376
maaa 144,104,64
mvaa 0,112,64
ra 16,232,8192
mvaa 4,116,64
mvaa 12,116,68
ta 52,235,36864
mvaa 8,112,68
dl 226,36,52,2048
mvaa 64,112,72
mvaa 68,116,72
ta 54,235,37376
mvaa 76,116,76
mvaa 72,112,76
ra 20,232,10240
mvaa 128,112,80
mvaa 132,116,80
dl 227,36,52,3072
mvaa 140,116,84
s_add_u32 s52,s52,s60
ta 56,236,36864
mvaa 136,112,84
v_add_u32_e32 v233,s99,v233
mvaa 192,112,88
ta 58,236,37376
s_sub_u32 s61,s63,s99
mvaa 196,116,88
s_add_u32 s61,s61,s98
mvaa 204,116,92
ra 24,232,12288
mvaa 200,112,92
buffer_load_dword a185,v241,s[48:51],s55 offen
s_add_u32 s55,s55,s59
mvaa 216,120,92
mvaa 220,124,92
ta 60,237,36864
mvaa 212,124,88
mvaa 208,120,88
ta 62,237,37376
mvaa 152,120,84
mvaa 156,124,84
ra 28,232,14336
mvaa 148,124,80
mvaa 144,120,80
mvaa 88,120,76
mvaa 92,124,76
mvaa 84,124,72
mvaa 80,120,72
mvaa 24,120,68
mvaa 28,124,68
mvaa 20,124,64
s_waitcnt vmcnt(10) lgkmcnt(0)
s_barrier
mvaa 16,120,64
maaa 128,32,0
ta 96,234,49152
maaa 132,36,0
s_add_u32 m0,s62,16384
ta 98,234,49664
maaa 140,36,4
buffer_load_dwordx4 v228,s[40:43],s53 offen offset:0 nt lds
maaa 136,32,4
mvaa 32,32,8
ra 64,233,0
mvaa 36,36,8
mvaa 44,36,12
ta 100,235,49152
mvaa 40,32,12
buffer_load_dwordx4 v229,s[40:43],s53 offen offset:1024 nt lds
mvaa 96,32,16
s_add_u32 s53,s53,s58
mvaa 100,36,16
ta 102,235,49664
mvaa 108,36,20
mvaa 104,32,20
ra 68,233,2048
mvaa 160,32,24
buffer_load_dwordx4 v230,s[44:47],s54 offen offset:2048 nt lds
mvaa 164,36,24
mvaa 172,36,28
ta 104,236,49152
mvaa 168,32,28
mvaa 184,40,28
ta 106,236,49664
mvaa 188,44,28
buffer_load_dwordx4 v231,s[44:47],s54 offen offset:3072 nt lds
mvaa 180,44,24
s_add_u32 s54,s54,s58
mvaa 176,40,24
ra 72,233,4096
mvaa 120,40,20
mvaa 124,44,20
ta 108,237,49152
mvaa 116,44,16
s_add_u32 m0,s61,0
mvaa 112,40,16
dl 224,36,52,0
mvaa 56,40,12
ta 110,237,49664
mvaa 60,44,12
mvaa 52,44,8
ra 76,233,6144
mvaa 48,40,8
maaa 152,40,4
ta 112,234,53248
maaa 156,44,4
dl 225,36,52,1024
maaa 148,44,0
ta 114,234,53760
maaa 144,40,0
mvaa 0,48,0
ra 80,233,8192
mvaa 4,52,0
mvaa 12,52,4
ta 116,235,53248
mvaa 8,48,4
dl 226,36,52,2048
mvaa 64,48,8
mvaa 68,52,8
ta 118,235,53760
mvaa 76,52,12
mvaa 72,48,12
ra 84,233,10240
mvaa 128,48,16
mvaa 132,52,16
dl 227,36,52,3072
mvaa 140,52,20
s_add_u32 s52,s52,s56
ta 120,236,53248
mvaa 136,48,20
s_cmp_ge_u32 s63,65536
s_cselect_b32 s99,-2,1
s_lshl_b32 s99,s99,15
mvaa 192,48,24
ta 122,236,53760
s_add_u32 s63,s63,s99
mvaa 196,52,24
v_add_u32_e32 v232,s99,v232
mvaa 204,52,28
ra 88,233,12288
mvaa 200,48,28
buffer_load_dword a186,v241,s[48:51],s55 offen
s_add_u32 s55,s55,s59
mvaa 216,56,28
mvaa 220,60,28
ta 124,237,53248
mvaa 212,60,24
mvaa 208,56,24
ta 126,237,53760
mvaa 152,56,20
mvaa 156,60,20
ra 92,233,14336
mvaa 148,60,16
mvaa 144,56,16
mvaa 88,56,12
mvaa 92,60,12
mvaa 84,60,8
mvaa 80,56,8
mvaa 24,56,4
mvaa 28,60,4
mvaa 20,60,0
s_waitcnt vmcnt(10) lgkmcnt(0)
s_barrier
mvaa 16,56,0
maaa 128,96,64
ta 32,234,0
maaa 132,100,64
s_add_u32 m0,s62,32768
ta 34,234,512
maaa 140,100,68
buffer_load_dwordx4 v228,s[40:43],s53 offen offset:0 nt lds
maaa 136,96,68
mvaa 32,96,72
ra 0,232,0
mvaa 36,100,72
mvaa 44,100,76
ta 36,235,0
mvaa 40,96,76
buffer_load_dwordx4 v229,s[40:43],s53 offen offset:1024 nt lds
mvaa 96,96,80
s_add_u32 s53,s53,s57
mvaa 100,100,80
ta 38,235,512
mvaa 108,100,84
mvaa 104,96,84
ra 4,232,2048
mvaa 160,96,88
buffer_load_dwordx4 v230,s[44:47],s54 offen offset:2048 nt lds
mvaa 164,100,88
mvaa 172,100,92
ta 40,236,0
mvaa 168,96,92
mvaa 184,104,92
ta 42,236,512
mvaa 188,108,92
buffer_load_dwordx4 v231,s[44:47],s54 offen offset:3072 nt lds
mvaa 180,108,88
s_add_u32 s54,s54,s57
mvaa 176,104,88
ra 8,232,4096
mvaa 120,104,84
mvaa 124,108,84
ta 44,237,0
mvaa 116,108,80
s_add_u32 m0,s61,4096
mvaa 112,104,80
dl 224,36,52,0
mvaa 56,104,76
ta 46,237,512
mvaa 60,108,76
mvaa 52,108,72
ra 12,232,6144
mvaa 48,104,72
maaa 152,104,68
ta 48,234,4096
maaa 156,108,68
dl 225,36,52,1024
maaa 148,108,64
ta 50,234,4608
maaa 144,104,64
mvaa 0,112,64
ra 16,232,8192
mvaa 4,116,64
mvaa 12,116,68
ta 52,235,4096
mvaa 8,112,68
dl 226,36,52,2048
mvaa 64,112,72
mvaa 68,116,72
ta 54,235,4608
mvaa 76,116,76
mvaa 72,112,76
ra 20,232,10240
mvaa 128,112,80
mvaa 132,116,80
dl 227,36,52,3072
mvaa 140,116,84
s_add_u32 s52,s52,s60
ta 56,236,4096
mvaa 136,112,84
v_add_u32_e32 v233,s99,v233
mvaa 192,112,88
ta 58,236,4608
s_sub_u32 s61,s63,s99
mvaa 196,116,88
s_add_u32 s61,s61,s98
mvaa 204,116,92
ra 24,232,12288
mvaa 200,112,92
buffer_load_dword a187,v241,s[48:51],s55 offen
s_add_u32 s55,s55,s59
mvaa 216,120,92
mvaa 220,124,92
ta 60,237,4096
mvaa 212,124,88
mvaa 208,120,88
ta 62,237,4608
mvaa 152,120,84
mvaa 156,124,84
ra 28,232,14336
mvaa 148,124,80
mvaa 144,120,80
mvaa 88,120,76
mvaa 92,124,76
mvaa 84,124,72
mvaa 80,120,72
mvaa 24,120,68
mvaa 28,124,68
s_cmp_eq_u32 s64,3
mvaa 20,124,64
s_waitcnt vmcnt(10) lgkmcnt(0)
s_barrier
mvaa 16,120,64
s_cbranch_scc1 L_k_exit
maaa 128,32,0
ta 96,234,16384
maaa 132,36,0
s_add_u32 m0,s62,49152
ta 98,234,16896
maaa 140,36,4
buffer_load_dwordx4 v228,s[40:43],s53 offen offset:0 nt lds
maaa 136,32,4
mvaa 32,32,8
ra 64,233,0
mvaa 36,36,8
mvaa 44,36,12
ta 100,235,16384
mvaa 40,32,12
buffer_load_dwordx4 v229,s[40:43],s53 offen offset:1024 nt lds
mvaa 96,32,16
s_add_u32 s53,s53,s58
mvaa 100,36,16
ta 102,235,16896
mvaa 108,36,20
mvaa 104,32,20
ra 68,233,2048
mvaa 160,32,24
buffer_load_dwordx4 v230,s[44:47],s54 offen offset:2048 nt lds
mvaa 164,36,24
mvaa 172,36,28
ta 104,236,16384
mvaa 168,32,28
mvaa 184,40,28
ta 106,236,16896
mvaa 188,44,28
buffer_load_dwordx4 v231,s[44:47],s54 offen offset:3072 nt lds
mvaa 180,44,24
s_add_u32 s54,s54,s58
mvaa 176,40,24
ra 72,233,4096
s_bitcmp1_b32 s65,s64
s_cbranch_scc1 L_k_ev
L_k_evr:
mvaa 120,40,20
mvaa 124,44,20
ta 108,237,16384
mvaa 116,44,16
s_add_u32 m0,s61,0
mvaa 112,40,16
dl 224,36,52,0
mvaa 56,40,12
ta 110,237,16896
mvaa 60,44,12
mvaa 52,44,8
ra 76,233,6144
mvaa 48,40,8
maaa 152,40,4
ta 112,234,20480
maaa 156,44,4
dl 225,36,52,1024
maaa 148,44,0
ta 114,234,20992
maaa 144,40,0
mvaa 0,48,0
ra 80,233,8192
mvaa 4,52,0
mvaa 12,52,4
ta 116,235,20480
mvaa 8,48,4
dl 226,36,52,2048
mvaa 64,48,8
mvaa 68,52,8
ta 118,235,20992
mvaa 76,52,12
mvaa 72,48,12
ra 84,233,10240
mvaa 128,48,16
mvaa 132,52,16
dl 227,36,52,3072
mvaa 140,52,20
s_add_u32 s52,s52,s56
ta 120,236,20480
mvaa 136,48,20
s_cmp_ge_u32 s63,65536
s_cselect_b32 s99,-2,1
s_lshl_b32 s99,s99,15
mvaa 192,48,24
ta 122,236,20992
s_add_u32 s63,s63,s99
mvaa 196,52,24
v_add_u32_e32 v232,s99,v232
mvaa 204,52,28
ra 88,233,12288
mvaa 200,48,28
buffer_load_dword a184,v241,s[48:51],s55 offen
s_add_u32 s55,s55,s59
mvaa 216,56,28
mvaa 220,60,28
)"
R"(ta 124,237,20480
mvaa 212,60,24
mvaa 208,56,24
ta 126,237,20992
mvaa 152,56,20
mvaa 156,60,20
ra 92,233,14336
mvaa 148,60,16
mvaa 144,56,16
mvaa 88,56,12
mvaa 92,60,12
mvaa 84,60,8
mvaa 80,56,8
mvaa 24,56,4
mvaa 28,60,4
s_mov_b32 s50,s33
s_mov_b32 s59,s34
s_lshl_b32 s86,s21,2
s_add_u32 s86,s86,s12
s_mul_i32 s86,s86,384
s_add_u32 s86,s86,4096
v_lshl_add_u32 v253,v244,3,s86
s_mov_b64 exec,7
global_load_dword a188,v253,s[96:97]
s_mov_b64 exec,-1
s_add_u32 s64,s64,1
mvaa 20,60,0
s_waitcnt vmcnt(10) lgkmcnt(0)
s_barrier
mvaa 16,56,0
s_branch L_k_head
L_k_exit:
global_load_dwordx4 a[160:163],v244,s[30:31] offset:0
s_add_u32 m0,s62,49152
maaa 128,32,0
s_cmp_lt_u32 s12,2
s_cselect_b32 s86,s22,s23
ta 96,234,16384
v_accvgpr_read_b32 v224,a242
maaa 132,36,0
s_and_b32 s87,s12,1
ta 98,234,16896
s_lshl_b32 s86,s86,11
maaa 140,36,4
s_lshl_b32 s87,s87,17
ra 64,233,0
s_add_u32 s86,s86,s87
maaa 136,32,4
s_mov_b64 s[36:37],s[68:69]
s_mov_b32 s38,4194304
s_mov_b32 s52,s86
mvaa 32,32,8
buffer_load_dwordx4 v228,s[40:43],s53 offen offset:0 nt lds
mvaa 36,36,8
s_mov_b32 s56,65536
ta 100,235,16384
v_accvgpr_read_b32 v225,a243
mvaa 44,36,12
s_lshl_b32 s88,s18,1
ta 102,235,16896
v_accvgpr_read_b32 v226,a244
mvaa 40,32,12
s_add_u32 s88,s88,s12
ra 68,233,2048
v_accvgpr_read_b32 v227,a245
mvaa 96,32,16
s_mov_b32 s91,196608
ra 72,233,4096
s_lshl_b32 s88,s88,7
mvaa 100,36,16
buffer_load_dwordx4 v229,s[40:43],s53 offen offset:1024 nt lds
mvaa 108,36,20
s_lshl_b32 s89,s18,6
ra 76,233,6144
s_mov_b32 s90,6291456
mvaa 104,32,20
s_cmp_lt_u32 s12,2
s_cselect_b32 s57,262144,s91
ra 80,233,8192
s_add_u32 s53,s53,s58
mvaa 160,32,24
s_cmp_lt_u32 s12,2
s_cselect_b32 s42,8388608,s90
ra 84,233,10240
v_accvgpr_read_b32 v228,a246
mvaa 164,36,24
s_cmp_lt_u32 s12,2
s_cselect_b32 s53,s88,s89
ra 88,233,12288
v_accvgpr_read_b32 v229,a247
mvaa 172,36,28
buffer_load_dwordx4 v230,s[44:47],s54 offen offset:2048 nt lds
mvaa 168,32,28
s_bitcmp1_b32 s21,0
s_cselect_b32 s90,s57,0
ra 92,233,14336
s_add_u32 s53,s53,s90
mvaa 184,40,28
s_cmp_lt_u32 s12,2
s_cselect_b64 s[40:41],s[72:73],s[80:81]
ta 104,236,16384
v_accvgpr_read_b32 v230,a248
mvaa 188,44,28
s_lshl_b32 s90,s90,1
ta 106,236,16896
s_mov_b32 s60,-65408
mvaa 180,44,24
s_and_b32 s89,s98,8192
ta 108,237,16384
s_add_u32 s91,s89,65536
mvaa 176,40,24
buffer_load_dwordx4 v231,s[44:47],s54 offen offset:3072 nt lds
mvaa 120,40,20
s_add_u32 m0,s98,65536
ta 110,237,16896
s_add_u32 s54,s54,s58
mvaa 124,44,20
s_cmp_eq_u32 s12,3
s_cselect_b64 s[44:45],s[68:69],s[40:41]
ta 112,234,20480
v_accvgpr_read_b32 v231,a249
mvaa 116,44,16
s_cmp_eq_u32 s12,3
s_cselect_b32 s46,0,s42
ta 114,234,20992
s_add_u32 s58,s57,s90
mvaa 112,40,16
s_cmp_eq_u32 s12,3
s_cselect_b32 s54,0,s53
ta 116,235,20480
s_sub_u32 s57,s57,s90
mvaa 56,40,12
global_load_dwordx4 a[168:171],v244,s[30:31] offset:2048
mvaa 60,44,12
s_add_u32 s90,s89,65536
ta 118,235,20992
s_bfe_u32 s88,s21,65539
mvaa 52,44,8
s_lshl_b32 s88,s88,1
ta 120,236,20480
s_lshl_b32 s87,s20,2
mvaa 48,40,8
s_add_u32 s88,s88,s87
ta 122,236,20992
s_and_b32 s87,s12,1
maaa 152,40,4
s_add_u32 s88,s88,s87
ta 124,237,20480
s_mul_i32 s88,s88,524288
maaa 156,44,4
global_load_dwordx4 a[176:179],v244,s[84:85] offset:0
maaa 148,44,0
s_and_b32 s87,s21,7
ta 126,237,20992
s_add_u32 s86,s88,16777216
maaa 144,40,0
s_lshl_b32 s87,s87,16
s_mov_b32 s26,32768
s_add_u32 s86,s86,s87
mvaa 0,48,0
s_lshr_b32 s87,s12,1
s_mov_b32 s27,131072
s_lshl_b32 s87,s87,15
mvaa 4,52,0
s_add_u32 s86,s86,s87
s_lshl_b32 s87,s12,3
s_add_u32 s61,s98,32768
mvaa 12,52,4
global_load_dwordx4 a[164:167],v244,s[30:31] offset:1024
mvaa 8,48,4
s_add_u32 s24,s8,s86
s_addc_u32 s25,s9,0
s_lshl_b32 s86,%1,5
s_add_u32 s86,s86,s87
mvaa 64,48,8
s_add_u32 s86,s86,83886080
s_mov_b32 s63,65536
s_mov_b32 s67,2048
mvaa 68,52,8
s_cmp_ge_u32 s63,65536
s_cselect_b32 s99,-2,1
s_lshl_b32 s99,s99,15
s_mov_b32 s66,8
mvaa 76,52,12
s_cmp_lg_u32 s65,0
s_cbranch_scc1 L_k_ec1
L_k_ecr1:
s_cmp_eq_u32 s12,0
s_cselect_b32 s65,96,0
mvaa 72,48,12
global_load_dwordx4 a[172:175],v244,s[30:31] offset:3072
mvaa 128,48,16
s_mov_b32 s30,1048577
s_mov_b32 s31,268435712
s_mov_b32 s88,4096
mvaa 132,52,16
s_mov_b32 s64,1
s_or_b32 s65,s65,128
mvaa 140,52,20
s_bitcmp1_b32 s12,0
s_cselect_b64 vcc,-1,0
mvaa 136,48,20
mvaa 192,48,24
global_load_dwordx4 a[180:183],v244,s[84:85] offset:1024
mvaa 196,52,24
mvaa 204,52,28
mvaa 200,48,28
mvaa 216,56,28
mvaa 220,60,28
dl 224,36,52,0
mvaa 212,60,24
mvaa 208,56,24
mvaa 152,56,20
mvaa 156,60,20
mvaa 148,60,16
dl 225,36,52,1024
mvaa 144,56,16
mvaa 88,56,12
s_waitcnt vmcnt(13) lgkmcnt(0)
s_barrier
mvaa 92,60,12
ta 32,234,32768
mvaa 84,60,8
ta 34,234,33280
mvaa 80,56,8
dl 226,36,52,2048
mvaa 24,56,4
ta 36,235,32768
mvaa 28,60,4
ta 38,235,33280
mvaa 20,60,0
ta 40,236,32768
mvaa 16,56,0
ta 42,236,33280
maaa 128,96,64
dl 227,36,52,3072
maaa 132,100,64
s_add_u32 s52,s52,s56
s_add_u32 m0,s98,69632
ta 44,237,32768
maaa 140,100,68
ta 46,237,33280
maaa 136,96,68
ta 48,234,36864
mvaa 32,96,72
ta 50,234,37376
mvaa 36,100,72
dl 224,36,52,0
mvaa 44,100,76
ta 52,235,36864
mvaa 40,96,76
ta 54,235,37376
mvaa 96,96,80
ra 20,232,43008
mvaa 100,100,80
ra 24,232,45056
mvaa 108,100,84
dl 225,36,52,1024
mvaa 104,96,84
ra 28,232,47104
mvaa 160,96,88
ra 16,232,40960
mvaa 164,100,88
ra 0,232,32768
mvaa 172,100,92
ra 4,232,34816
mvaa 168,96,92
dl 226,36,52,2048
mvaa 184,104,92
ra 12,232,38912
mvaa 188,108,92
ra 8,232,36864
v_add_u32_e32 v232,s90,v232
mvaa 180,108,88
ta 60,237,36864
mvaa 176,104,88
ta 62,237,37376
mvaa 120,104,84
dl 227,36,52,3072
mvaa 124,108,84
s_add_u32 m0,s62,0
ta 56,236,36864
s_add_u32 s52,s52,s60
mvaa 116,108,80
ta 58,236,37376
mvaa 112,104,80
mvaa 56,104,76
mvaa 60,108,76
dl 228,40,53,0
mvaa 52,108,72
mvaa 48,104,72
maaa 152,104,68
maaa 156,108,68
maaa 148,108,64
dl 229,40,53,1024
maaa 144,104,64
s_add_u32 s53,s53,s57
mvaa 0,112,64
mvaa 4,116,64
mvaa 12,116,68
mvaa 8,112,68
dl 230,44,54,2048
mvaa 64,112,72
mvaa 68,116,72
mvaa 76,116,76
mvaa 72,112,76
mvaa 128,112,80
dl 231,44,54,3072
mvaa 132,116,80
s_add_u32 m0,s62,16384
s_add_u32 s54,s54,s57
mvaa 140,116,84
mvaa 136,112,84
mvaa 192,112,88
mvaa 196,116,88
dl 228,40,53,0
mvaa 204,116,92
mvaa 200,112,92
mvaa 216,120,92
mvaa 220,124,92
mvaa 212,124,88
dl 229,40,53,1024
mvaa 208,120,88
s_waitcnt vmcnt(19) lgkmcnt(0)
s_barrier
mvaa 152,120,84
s_add_u32 s53,s53,s58
ta 96,234,49152
mvaa 156,124,84
ra 92,233,47104
mvaa 148,124,80
ta 98,234,49664
mvaa 144,120,80
dl 230,44,54,2048
mvaa 88,120,76
ra 88,233,45056
mvaa 92,124,76
ta 100,235,49152
mvaa 84,124,72
ta 112,234,53248
mvaa 80,120,72
ta 116,235,53248
mvaa 24,120,68
dl 231,44,54,3072
mvaa 28,124,68
s_add_u32 m0,s98,0
ta 118,235,53760
s_add_u32 s54,s54,s58
mvaa 20,124,64
ta 114,234,53760
v_subrev_u32_e32 v234,s89,v234
mvaa 16,120,64
ta 102,235,49664
v_subrev_u32_e32 v235,s89,v235
maaa 128,32,0
ta 124,237,53248
maaa 132,36,0
dl 224,36,52,0
maaa 140,36,4
ta 126,237,53760
maaa 136,32,4
ta 120,236,53248
mvaa 32,32,8
ta 122,236,53760
mvaa 36,36,8
ta 104,236,49152
mvaa 44,36,12
dl 225,36,52,1024
mvaa 40,32,12
ta 106,236,49664
v_subrev_u32_e32 v236,s89,v236
mvaa 56,40,12
ta 108,237,49152
mvaa 60,44,12
ta 110,237,49664
v_subrev_u32_e32 v237,s89,v237
mvaa 52,44,8
ra 84,233,43008
mvaa 48,40,8
dl 226,36,52,2048
maaa 152,40,4
ra 76,233,38912
maaa 156,44,4
ra 80,233,40960
maaa 148,44,0
ra 68,233,34816
maaa 144,40,0
ra 72,233,36864
mvaa 0,48,0
dl 227,36,52,3072
mvaa 4,52,0
s_add_u32 s52,s52,s56
s_add_u32 m0,s98,4096
ra 64,233,32768
mvaa 12,52,4
v_add_u32_e32 v233,s91,v233
mvaa 8,48,4
mvaa 24,56,4
mvaa 28,60,4
dl 224,36,52,0
mvaa 20,60,0
mvaa 16,56,0
mvaa 220,60,28
s_waitcnt lgkmcnt(2)
mvaa 220,124,92
mvaa 216,120,92
dl 225,36,52,1024
mvaa 216,56,28
mvaa 212,60,24
mvaa 212,124,88
mvaa 208,120,88
mvaa 208,56,24
dl 226,36,52,2048
s_waitcnt vmcnt(25)
zvaa 246,160,176
mvaa 204,52,28
cv 223,222,223
cv 222,220,221
mvaa 204,116,92
cv 221,218,219
zvaa 250,168,176
mvaa 200,112,92
cv 220,216,217
cv 219,214,215
mvaa 200,48,28
cv 218,212,213
cv 217,210,211
mvaa 196,52,24
dl 227,36,52,3072
mvaa 196,116,88
s_add_u32 m0,s62,32768
cv 216,208,209
cv 215,206,207
mvaa 192,112,88
s_add_u32 s52,s52,s60
cv 214,204,205
cv 213,202,203
mvaa 192,48,24
cv 212,200,201
s_waitcnt vmcnt(24)
mvaa 246,164,180
mvaa 250,172,180
mvaa 188,44,28
cv 211,198,199
cv 210,196,197
mvaa 188,108,92
dl 228,40,53,0
mvaa 184,104,92
cv 209,194,195
v_cndmask_b32_e64 v246,0,v246,s[30:31]
v_cndmask_b32_e64 v250,0,v250,s[30:31]
s_lshl_b64 s[30:31],s[30:31],1
v_cndmask_b32_e64 v246,v246,v247,s[30:31]
v_cndmask_b32_e64 v250,v250,v251,s[30:31]
s_lshl_b64 s[30:31],s[30:31],1
mvaa 184,40,28
v_cndmask_b32_e64 v246,v246,v248,s[30:31]
v_cndmask_b32_e64 v250,v250,v252,s[30:31]
s_lshl_b64 s[30:31],s[30:31],1
v_cndmask_b32_e64 v246,v246,v249,s[30:31]
v_cndmask_b32_e64 v250,v250,v253,s[30:31]
mvaa 180,44,24
v_add_f32_dpp v246,v246,v246 quad_perm:[1,0,3,2] row_mask:0xf bank_mask:0xf
v_add_f32_dpp v250,v250,v250 quad_perm:[1,0,3,2] row_mask:0xf bank_mask:0xf
mvaa 180,108,88
v_add_f32_dpp v246,v246,v246 quad_perm:[2,3,0,1] row_mask:0xf bank_mask:0xf
v_add_f32_dpp v250,v250,v250 quad_perm:[2,3,0,1] row_mask:0xf bank_mask:0xf
mvaa 176,104,88
dl 229,40,53,1024
mvaa 176,40,24
s_add_u32 s53,s53,s57
v_add_f32_dpp v246,v246,v246 row_half_mirror row_mask:0xf bank_mask:0xf
v_add_f32_dpp v250,v250,v250 row_half_mirror row_mask:0xf bank_mask:0xf
mvaa 172,36,28
v_add_f32_dpp v246,v246,v246 row_mirror row_mask:0xf bank_mask:0xf
v_add_f32_dpp v250,v250,v250 row_mirror row_mask:0xf bank_mask:0xf
mvaa 172,100,92
cv 208,192,193
cv 207,190,191
mvaa 168,96,92
v_permlane16_swap_b32_e32 v246,v250
mvaa 168,32,28
dl 230,44,54,2048
v_add_f32_e32 v246,v246,v250
v_mov_b32_e32 v250,v246
mvaa 164,36,24
cv 206,188,189
cv 205,186,187
mvaa 164,100,88
v_permlane32_swap_b32_e32 v246,v250
mvaa 160,96,88
v_add_f32_e32 v246,v246,v250
v_lshrrev_b32_e32 v250,6,v244
mvaa 160,32,24
v_mul_f32_e32 v246,0xbfb8aa3b,v246
cv 204,184,185
mvaa 156,60,20
dl 231,44,54,3072
ex 246
mvaa 156,124,84
s_add_u32 s54,s54,s57
v_add_f32_e32 v246,1.0,v246
cv 203,182,183
mvaa 152,120,84
v_rcp_f32_e32 v246,v246
mvaa 152,56,20
cv 202,180,181
cv 201,178,179
mvaa 148,60,16
cv 200,176,177
cv 199,174,175
mvaa 148,124,80
buffer_load_dword a242,v241,s[48:51],s55 offen
mvaa 144,120,80
s_add_u32 s55,s55,s59
cv 198,172,173
cv 197,170,171
mvaa 144,56,16
s_waitcnt vmcnt(17) lgkmcnt(0)
s_barrier
s_add_u32 m0,s62,49152
mvaa 140,52,20
cv 196,168,169
cv 195,166,167
mvaa 140,116,84
cv 194,164,165
cv 193,162,163
mvaa 136,112,84
dl 228,40,53,0
mvaa 136,48,20
cv 192,160,161
cv 191,158,159
mvaa 132,52,16
ra 4,232,0
cv 190,156,157
mvaa 132,116,80
ta 88,238,12288
cv 189,154,155
mvaa 128,112,80
ta 90,238,12800
cv 188,152,153
mvaa 128,48,16
dl 229,40,53,1024
mvaa 124,44,20
s_add_u32 s53,s53,s58
ta 92,239,12288
cv 187,150,151
mvaa 124,108,84
ta 94,239,12800
cv 186,148,149
mvaa 120,104,84
cv 185,146,147
cv 184,144,145
mvaa 120,40,20
cv 183,142,143
cv 182,140,141
mvaa 116,44,16
dl 230,44,54,2048
mvaa 116,108,80
cv 181,138,139
cv 180,136,137
mvaa 112,104,80
cv 179,134,135
cv 178,132,133
mvaa 112,40,16
cv 177,130,131
cv 176,128,129
mvaa 108,36,20
cv 175,126,127
cv 174,124,125
mvaa 108,100,84
dl 231,44,54,3072
mvaa 104,96,84
s_add_u32 m0,s98,32768
cv 173,122,123
cv 172,120,121
mvaa 104,32,20
s_add_u32 s54,s54,s58
ta 44,235,0
cv 171,118,119
mvaa 100,36,16
ta 46,235,512
cv 170,116,117
mvaa 100,100,80
ta 40,234,0
cv 169,114,115
mvaa 96,96,80
dl 224,36,52,0
mvaa 96,32,16
ta 42,234,512
cv 168,112,113
mvaa 92,60,12
cv 167,110,111
cv 166,108,109
mvaa 92,124,76
ta 84,237,8192
cv 165,106,107
mvaa 88,120,76
ta 86,237,8704
cv 164,104,105
mvaa 88,56,12
dl 225,36,52,1024
mvaa 84,60,8
cv 163,102,103
cv 162,100,101
mvaa 84,124,72
ta 80,236,8192
cv 161,98,99
mvaa 80,120,72
ra 16,232,6144
cv 160,96,97
mvaa 80,56,8
ta 82,236,8704
cv 159,94,95
mvaa 76,52,12
dl 226,36,52,2048
mvaa 76,116,76
ta 60,235,4096
cv 158,92,93
mvaa 72,112,76
ta 62,235,4608
cv 157,90,91
mvaa 72,48,12
ta 56,234,4096
cv 156,88,89
mvaa 68,52,8
ta 58,234,4608
cv 151,78,79
mvaa 68,116,72
dl 227,36,52,3072
mvaa 64,112,72
s_add_u32 m0,s62,0
ra 12,232,4096
s_add_u32 s52,s52,s56
mvaa 64,48,8
ta 52,237,0
cv 150,76,77
mvaa 60,108,76
ta 54,237,512
cv 147,70,71
mvaa 56,104,76
ra 8,232,2048
cv 146,68,69
mvaa 52,108,72
buffer_load_dword a243,v241,s[48:51],s55 offen
mvaa 48,104,72
s_add_u32 s55,s55,s59
ta 48,236,0
cv 145,66,67
mvaa 44,100,76
ta 50,236,512
cv 144,64,65
mvaa 40,96,76
cv 143,62,63
cv 142,60,61
mvaa 36,100,72
cv 141,58,59
cv 140,56,57
mvaa 32,96,72
buffer_load_dword a244,v241,s[48:51],s55 offen
mvaa 28,124,68
s_add_u32 s55,s55,s59
cv 139,54,55
cv 138,52,53
mvaa 24,120,68
cv 137,50,51
cv 136,48,49
mvaa 20,124,64
cv 135,46,47
cv 134,44,45
mvaa 16,120,64
ta 76,235,8192
cv 133,42,43
mvaa 12,116,68
buffer_load_dword a245,v241,s[48:51],s55 offen
mvaa 8,112,68
s_add_u32 s55,s55,s59
ta 78,235,8704
cv 132,40,41
mvaa 4,116,64
cv 131,38,39
cv 130,36,37
mvaa 0,112,64
ta 72,234,8192
cv 129,34,35
maaa 156,108,68
ta 74,234,8704
cv 128,32,33
maaa 152,104,68
s_mov_b64 exec,65537
buffer_store_dword v246,v250,s[8:11],s86 offen sc0 sc1
s_mov_b64 exec,-1
maaa 148,108,64
cv 127,30,31
v_accvgpr_read_b32 v63,a159
maaa 144,104,64
v_accvgpr_read_b32 v62,a158
v_accvgpr_read_b32 v61,a157
maaa 140,100,68
v_accvgpr_read_b32 v60,a156
v_accvgpr_read_b32 v59,a155
maaa 136,96,68
v_accvgpr_read_b32 v58,a154
v_accvgpr_read_b32 v57,a153
maaa 132,100,64
v_accvgpr_read_b32 v56,a152
cv 56,56,57
maaa 128,96,64
ta 68,237,4096
ta 70,237,4608
s_waitcnt lgkmcnt(4)
zaaa 236,40,4
cv 57,58,59
cv 58,60,61
zaaa 220,44,4
ta 64,236,4096
ta 66,236,4608
zaaa 216,44,8
cv 59,62,63
v_accvgpr_read_b32 v55,a151
zaaa 232,40,8
buffer_store_dwordx4 v[56:59],v244,s[24:27],0 offen offset:3072 sc0 sc1
zaaa 228,40,12
v_accvgpr_read_b32 v54,a150
v_accvgpr_read_b32 v53,a149
zaaa 212,44,12
v_accvgpr_read_b32 v52,a148
v_accvgpr_read_b32 v51,a147
zaaa 208,44,16
v_accvgpr_read_b32 v50,a146
v_accvgpr_read_b32 v49,a145
zaaa 224,40,16
v_accvgpr_read_b32 v48,a144
cv 48,48,49
zaaa 192,48,16
cv 49,50,51
cv 50,52,53
zaaa 176,52,16
cv 51,54,55
s_waitcnt vmcnt(26) lgkmcnt(0)
s_barrier
zaaa 180,52,12
dl 228,40,53,0
zaaa 196,48,12
ra 24,233,0
v_accvgpr_read_b32 v47,a143
zaaa 200,48,8
ra 28,233,2048
v_accvgpr_read_b32 v46,a142
v_accvgpr_read_b32 v45,a141
zaaa 184,52,8
ra 32,233,4096
ra 36,233,6144
zaaa 188,52,4
v_accvgpr_read_b32 v43,a139
v_accvgpr_read_b32 v42,a138
zaaa 204,48,4
dl 229,40,53,1024
ta 96,236,24576
zaaa 172,56,4
s_add_u32 s53,s53,s57
ta 98,236,25088
v_accvgpr_read_b32 v41,a137
zaaa 156,60,4
ta 100,237,24576
v_accvgpr_read_b32 v39,a135
zaaa 152,60,8
ta 102,237,25088
ta 104,238,28672
zaaa 168,56,8
v_accvgpr_read_b32 v38,a134
v_accvgpr_read_b32 v35,a131
zaaa 164,56,12
buffer_store_dwordx4 v[48:51],v244,s[24:27],0 offen offset:2048 sc0 sc1
ta 106,238,29184
zaaa 148,60,12
ta 108,239,28672
v_accvgpr_read_b32 v34,a130
zaaa 144,60,16
ta 110,239,29184
v_accvgpr_read_b32 v33,a129
zaaa 160,56,16
ta 44,235,16384
ta 46,235,16896
ta 40,234,16384
ta 42,234,16896
ta 52,237,16384
ta 54,237,16896
ta 48,236,16384
ta 50,236,16896
ta 60,235,20480
ta 62,235,20992
ta 56,234,20480
ta 58,234,20992
v_accvgpr_read_b32 v32,a128
zaaa 128,64,16
v_accvgpr_read_b32 v37,a133
v_accvgpr_read_b32 v36,a132
zaaa 112,68,16
dl 230,44,54,2048
zaaa 116,68,12
v_accvgpr_read_b32 v40,a136
v_accvgpr_read_b32 v44,a140
zaaa 132,64,12
cv 251,42,43
cv 250,40,41
zaaa 136,64,8
cv 253,46,47
cv 252,44,45
zaaa 120,68,8
cv 126,28,29
cv 125,26,27
zaaa 124,68,4
dl 231,44,54,3072
zaaa 140,64,4
s_add_u32 m0,s61,4096
cv 249,38,39
cv 247,34,35
zvaa 40,72,4
s_add_u32 s54,s54,s57
ta 68,237,20480
cv 246,32,33
zvaa 44,76,4
s_add_u32 s61,s63,s98
ta 70,237,20992
cv 248,36,37
zvaa 52,76,8
s_add_u32 s63,s63,s99
ta 64,236,20480
cv 124,24,25
zvaa 48,72,8
dl 224,36,52,0
zvaa 56,72,12
ta 66,236,20992
cv 123,22,23
zvaa 60,76,12
cv 121,18,19
cv 120,16,17
zvaa 68,76,16
cv 122,20,21
cv 119,14,15
zvaa 64,72,16
cv 118,12,13
cv 117,10,11
zvaa 32,80,16
dl 225,36,52,1024
ta 76,235,24576
ta 78,235,25088
zvaa 36,84,16
ta 72,234,24576
ta 74,234,25088
zvaa 28,84,12
cv 116,8,9
cv 153,82,83
zvaa 24,80,12
cv 152,80,81
cv 155,86,87
zvaa 16,80,8
cv 154,84,85
cv 149,74,75
zvaa 20,84,8
dl 226,36,52,2048
zvaa 12,84,4
cv 148,72,73
v_add_u32_e32 v232,s99,v232
zvaa 8,80,4
cv 115,6,7
cv 114,4,5
zvaa 76,4,88
cv 113,2,3
cv 112,0,1
zvaa 96,4,92
zvaa 100,8,92
dl 227,36,52,3072
zvaa 80,8,88
s_add_u32 s52,s52,s60
zvaa 84,12,88
zvaa 104,12,92
zvaa 108,16,92
s_waitcnt vmcnt(23) lgkmcnt(0)
s_barrier
zvaa 88,16,88
buffer_store_dwordx4 v[250:253],v244,s[24:27],0 offen offset:1024 sc0 sc1
buffer_store_dwordx4 v[246:249],v244,s[24:27],0 offen offset:0 sc0 sc1
s_branch L_kvx
L_vtile:
s_waitcnt vmcnt(8)
s_barrier
ta 32,234,0
ta 34,234,512
ra 0,232,0
ta 36,235,0
ta 38,235,512
ra 4,232,2048
ra 8,232,4096
ra 12,232,6144
ra 16,232,8192
ra 20,232,10240
ra 24,232,12288
ra 28,232,14336
s_add_u32 m0,s62,32768
ta 40,236,0
buffer_load_dwordx4 v228,s[40:43],s53 offen offset:0 nt lds
ta 42,236,512
ta 44,237,0
ta 46,237,512
ta 48,234,4096
ta 50,234,4608
ta 52,235,4096
ta 54,235,4608
ta 56,236,4096
ta 58,236,4608
ta 60,237,4096
ta 62,237,4608
s_cmp_ge_u32 s63,65536
s_cselect_b32 s99,-2,1
s_lshl_b32 s99,s99,15
s_add_u32 s63,s63,s99
v_add_u32_e32 v232,s99,v232
s_mov_b32 s64,1
s_waitcnt lgkmcnt(15)
zaaa 128,0,32
zaaa 136,0,36
zaaa 140,4,36
zaaa 132,4,32
buffer_load_dwordx4 v229,s[40:43],s53 offen offset:1024 nt lds
zvaa 32,8,32
s_add_u32 s53,s53,s57
zvaa 40,8,36
zvaa 44,12,36
zvaa 36,12,32
buffer_load_dwordx4 v230,s[44:47],s54 offen offset:2048 nt lds
zvaa 96,16,32
zvaa 104,16,36
s_waitcnt lgkmcnt(0)
zvaa 108,20,36
zvaa 100,20,32
buffer_load_dwordx4 v231,s[44:47],s54 offen offset:3072 nt lds
zvaa 160,24,32
s_add_u32 m0,s98,36864
s_add_u32 s54,s54,s57
zvaa 168,24,36
zvaa 172,28,36
zvaa 164,28,32
dl 224,36,52,0
zvaa 180,28,40
zvaa 188,28,44
zvaa 184,24,44
zvaa 176,24,40
dl 225,36,52,1024
zvaa 116,20,40
zvaa 124,20,44
zvaa 120,16,44
zvaa 112,16,40
dl 226,36,52,2048
zvaa 52,12,40
s_waitcnt vmcnt(11)
s_barrier
zvaa 60,12,44
ta 96,234,16384
zvaa 56,8,44
ta 98,234,16896
zvaa 48,8,40
dl 227,36,52,3072
zaaa 148,4,40
s_add_u32 m0,s62,49152
ra 64,233,0
s_add_u32 s52,s52,s60
zaaa 156,4,44
ta 100,235,16384
zaaa 152,0,44
ta 102,235,16896
zaaa 144,0,40
buffer_load_dwordx4 v228,s[40:43],s53 offen offset:0 nt lds
zvaa 0,0,48
ra 68,233,2048
zvaa 8,0,52
ta 104,236,16384
zvaa 12,4,52
ta 106,236,16896
zvaa 4,4,48
buffer_load_dwordx4 v229,s[40:43],s53 offen offset:1024 nt lds
zvaa 64,8,48
s_add_u32 s53,s53,s58
ra 72,233,4096
zvaa 72,8,52
ta 108,237,16384
zvaa 76,12,52
ta 110,237,16896
zvaa 68,12,48
buffer_load_dwordx4 v230,s[44:47],s54 offen offset:2048 nt lds
zvaa 128,16,48
ra 76,233,6144
zvaa 136,16,52
ta 112,234,20480
zvaa 140,20,52
ta 114,234,20992
zvaa 132,20,48
buffer_load_dwordx4 v231,s[44:47],s54 offen offset:3072 nt lds
zvaa 192,24,48
)"
R"(s_add_u32 m0,s61,0
ra 80,233,8192
s_add_u32 s54,s54,s58
zvaa 200,24,52
ta 116,235,20480
zvaa 204,28,52
ta 118,235,20992
zvaa 196,28,48
dl 224,36,52,0
zvaa 212,28,56
ra 84,233,10240
zvaa 220,28,60
ta 120,236,20480
zvaa 216,24,60
ta 122,236,20992
zvaa 208,24,56
dl 225,36,52,1024
ra 88,233,12288
zvaa 148,20,56
ta 124,237,20480
zvaa 156,20,60
ta 126,237,20992
zvaa 152,16,60
ra 92,233,14336
zvaa 144,16,56
dl 226,36,52,2048
zvaa 84,12,56
zvaa 92,12,60
zvaa 88,8,60
zvaa 80,8,56
dl 227,36,52,3072
zvaa 20,4,56
s_add_u32 s52,s52,s56
zvaa 28,4,60
zvaa 24,0,60
zvaa 16,0,56
buffer_load_dword a184,v241,s[48:51],s55 offen
s_add_u32 s55,s55,s59
s_waitcnt vmcnt(9)
s_waitcnt lgkmcnt(0)
s_barrier
L_v_head:
maaa 128,64,96
ta 32,234,32768
maaa 136,64,100
s_add_u32 m0,s62,0
ta 34,234,33280
maaa 140,68,100
buffer_load_dwordx4 v228,s[40:43],s53 offen offset:0 nt lds
maaa 132,68,96
mvaa 32,72,96
ra 0,232,0
mvaa 40,72,100
mvaa 44,76,100
ta 36,235,32768
mvaa 36,76,96
buffer_load_dwordx4 v229,s[40:43],s53 offen offset:1024 nt lds
mvaa 96,80,96
s_add_u32 s53,s53,s57
mvaa 104,80,100
ta 38,235,33280
mvaa 108,84,100
mvaa 100,84,96
ra 4,232,2048
mvaa 160,88,96
buffer_load_dwordx4 v230,s[44:47],s54 offen offset:2048 nt lds
mvaa 168,88,100
mvaa 172,92,100
ta 40,236,32768
mvaa 164,92,96
mvaa 180,92,104
ta 42,236,33280
mvaa 188,92,108
buffer_load_dwordx4 v231,s[44:47],s54 offen offset:3072 nt lds
mvaa 184,88,108
s_add_u32 s54,s54,s57
mvaa 176,88,104
ra 8,232,4096
mvaa 116,84,104
mvaa 124,84,108
ta 44,237,32768
mvaa 120,80,108
s_add_u32 m0,s61,4096
mvaa 112,80,104
dl 224,36,52,0
mvaa 52,76,104
ta 46,237,33280
mvaa 60,76,108
mvaa 56,72,108
ra 12,232,6144
mvaa 48,72,104
maaa 148,68,104
ta 48,234,36864
maaa 156,68,108
dl 225,36,52,1024
maaa 152,64,108
ta 50,234,37376
maaa 144,64,104
mvaa 0,64,112
ra 16,232,8192
mvaa 8,64,116
mvaa 12,68,116
ta 52,235,36864
mvaa 4,68,112
dl 226,36,52,2048
mvaa 64,72,112
mvaa 72,72,116
ta 54,235,37376
mvaa 76,76,116
mvaa 68,76,112
ra 20,232,10240
mvaa 128,80,112
mvaa 136,80,116
dl 227,36,52,3072
mvaa 140,84,116
s_add_u32 s52,s52,s60
ta 56,236,36864
mvaa 132,84,112
v_add_u32_e32 v233,s99,v233
mvaa 192,88,112
ta 58,236,37376
s_sub_u32 s61,s63,s99
mvaa 200,88,116
s_add_u32 s61,s61,s98
mvaa 204,92,116
ra 24,232,12288
mvaa 196,92,112
buffer_load_dword a185,v241,s[48:51],s55 offen
s_add_u32 s55,s55,s59
mvaa 212,92,120
mvaa 220,92,124
ta 60,237,36864
mvaa 216,88,124
mvaa 208,88,120
ta 62,237,37376
mvaa 148,84,120
mvaa 156,84,124
ra 28,232,14336
mvaa 152,80,124
mvaa 144,80,120
mvaa 84,76,120
mvaa 92,76,124
mvaa 88,72,124
mvaa 80,72,120
mvaa 20,68,120
mvaa 28,68,124
mvaa 24,64,124
s_waitcnt vmcnt(10) lgkmcnt(0)
s_barrier
mvaa 16,64,120
maaa 128,0,32
ta 96,234,49152
maaa 136,0,36
s_add_u32 m0,s62,16384
ta 98,234,49664
maaa 140,4,36
buffer_load_dwordx4 v228,s[40:43],s53 offen offset:0 nt lds
maaa 132,4,32
mvaa 32,8,32
ra 64,233,0
mvaa 40,8,36
mvaa 44,12,36
ta 100,235,49152
mvaa 36,12,32
buffer_load_dwordx4 v229,s[40:43],s53 offen offset:1024 nt lds
mvaa 96,16,32
s_add_u32 s53,s53,s58
mvaa 104,16,36
ta 102,235,49664
mvaa 108,20,36
mvaa 100,20,32
ra 68,233,2048
mvaa 160,24,32
buffer_load_dwordx4 v230,s[44:47],s54 offen offset:2048 nt lds
mvaa 168,24,36
mvaa 172,28,36
ta 104,236,49152
mvaa 164,28,32
mvaa 180,28,40
ta 106,236,49664
mvaa 188,28,44
buffer_load_dwordx4 v231,s[44:47],s54 offen offset:3072 nt lds
mvaa 184,24,44
s_add_u32 s54,s54,s58
mvaa 176,24,40
ra 72,233,4096
mvaa 116,20,40
mvaa 124,20,44
ta 108,237,49152
mvaa 120,16,44
s_add_u32 m0,s61,0
mvaa 112,16,40
dl 224,36,52,0
mvaa 52,12,40
ta 110,237,49664
mvaa 60,12,44
mvaa 56,8,44
ra 76,233,6144
mvaa 48,8,40
maaa 148,4,40
ta 112,234,53248
maaa 156,4,44
dl 225,36,52,1024
maaa 152,0,44
ta 114,234,53760
maaa 144,0,40
mvaa 0,0,48
ra 80,233,8192
mvaa 8,0,52
mvaa 12,4,52
ta 116,235,53248
mvaa 4,4,48
dl 226,36,52,2048
mvaa 64,8,48
mvaa 72,8,52
ta 118,235,53760
mvaa 76,12,52
mvaa 68,12,48
ra 84,233,10240
mvaa 128,16,48
mvaa 136,16,52
dl 227,36,52,3072
mvaa 140,20,52
s_add_u32 s52,s52,s56
ta 120,236,53248
mvaa 132,20,48
s_cmp_ge_u32 s63,65536
s_cselect_b32 s99,-2,1
s_lshl_b32 s99,s99,15
mvaa 192,24,48
ta 122,236,53760
s_add_u32 s63,s63,s99
mvaa 200,24,52
v_add_u32_e32 v232,s99,v232
mvaa 204,28,52
ra 88,233,12288
mvaa 196,28,48
buffer_load_dword a186,v241,s[48:51],s55 offen
s_add_u32 s55,s55,s59
mvaa 212,28,56
mvaa 220,28,60
ta 124,237,53248
mvaa 216,24,60
mvaa 208,24,56
ta 126,237,53760
mvaa 148,20,56
mvaa 156,20,60
ra 92,233,14336
mvaa 152,16,60
mvaa 144,16,56
mvaa 84,12,56
mvaa 92,12,60
mvaa 88,8,60
mvaa 80,8,56
mvaa 20,4,56
mvaa 28,4,60
mvaa 24,0,60
s_waitcnt vmcnt(10) lgkmcnt(0)
s_barrier
mvaa 16,0,56
maaa 128,64,96
ta 32,234,0
maaa 136,64,100
s_add_u32 m0,s62,32768
ta 34,234,512
maaa 140,68,100
buffer_load_dwordx4 v228,s[40:43],s53 offen offset:0 nt lds
maaa 132,68,96
mvaa 32,72,96
ra 0,232,0
mvaa 40,72,100
mvaa 44,76,100
ta 36,235,0
mvaa 36,76,96
buffer_load_dwordx4 v229,s[40:43],s53 offen offset:1024 nt lds
mvaa 96,80,96
s_add_u32 s53,s53,s57
mvaa 104,80,100
ta 38,235,512
mvaa 108,84,100
mvaa 100,84,96
ra 4,232,2048
mvaa 160,88,96
buffer_load_dwordx4 v230,s[44:47],s54 offen offset:2048 nt lds
mvaa 168,88,100
mvaa 172,92,100
ta 40,236,0
mvaa 164,92,96
mvaa 180,92,104
ta 42,236,512
mvaa 188,92,108
buffer_load_dwordx4 v231,s[44:47],s54 offen offset:3072 nt lds
mvaa 184,88,108
s_add_u32 s54,s54,s57
mvaa 176,88,104
ra 8,232,4096
mvaa 116,84,104
mvaa 124,84,108
ta 44,237,0
mvaa 120,80,108
s_add_u32 m0,s61,4096
mvaa 112,80,104
dl 224,36,52,0
mvaa 52,76,104
ta 46,237,512
mvaa 60,76,108
mvaa 56,72,108
ra 12,232,6144
mvaa 48,72,104
maaa 148,68,104
ta 48,234,4096
maaa 156,68,108
dl 225,36,52,1024
maaa 152,64,108
ta 50,234,4608
maaa 144,64,104
mvaa 0,64,112
ra 16,232,8192
mvaa 8,64,116
mvaa 12,68,116
ta 52,235,4096
mvaa 4,68,112
dl 226,36,52,2048
mvaa 64,72,112
mvaa 72,72,116
ta 54,235,4608
mvaa 76,76,116
mvaa 68,76,112
ra 20,232,10240
mvaa 128,80,112
mvaa 136,80,116
dl 227,36,52,3072
mvaa 140,84,116
s_add_u32 s52,s52,s60
ta 56,236,4096
mvaa 132,84,112
v_add_u32_e32 v233,s99,v233
mvaa 192,88,112
ta 58,236,4608
s_sub_u32 s61,s63,s99
mvaa 200,88,116
s_add_u32 s61,s61,s98
mvaa 204,92,116
ra 24,232,12288
mvaa 196,92,112
buffer_load_dword a187,v241,s[48:51],s55 offen
s_add_u32 s55,s55,s59
mvaa 212,92,120
mvaa 220,92,124
ta 60,237,4096
mvaa 216,88,124
mvaa 208,88,120
ta 62,237,4608
mvaa 148,84,120
mvaa 156,84,124
ra 28,232,14336
mvaa 152,80,124
mvaa 144,80,120
mvaa 84,76,120
mvaa 92,76,124
mvaa 88,72,124
mvaa 80,72,120
mvaa 20,68,120
mvaa 28,68,124
s_cmp_eq_u32 s64,3
mvaa 24,64,124
s_waitcnt vmcnt(10) lgkmcnt(0)
s_barrier
mvaa 16,64,120
s_cbranch_scc1 L_v_exit
maaa 128,0,32
ta 96,234,16384
maaa 136,0,36
s_add_u32 m0,s62,49152
ta 98,234,16896
maaa 140,4,36
buffer_load_dwordx4 v228,s[40:43],s53 offen offset:0 nt lds
maaa 132,4,32
mvaa 32,8,32
ra 64,233,0
mvaa 40,8,36
mvaa 44,12,36
ta 100,235,16384
mvaa 36,12,32
buffer_load_dwordx4 v229,s[40:43],s53 offen offset:1024 nt lds
mvaa 96,16,32
s_add_u32 s53,s53,s58
mvaa 104,16,36
ta 102,235,16896
mvaa 108,20,36
mvaa 100,20,32
ra 68,233,2048
mvaa 160,24,32
buffer_load_dwordx4 v230,s[44:47],s54 offen offset:2048 nt lds
mvaa 168,24,36
mvaa 172,28,36
ta 104,236,16384
mvaa 164,28,32
mvaa 180,28,40
ta 106,236,16896
mvaa 188,28,44
buffer_load_dwordx4 v231,s[44:47],s54 offen offset:3072 nt lds
mvaa 184,24,44
s_add_u32 s54,s54,s58
mvaa 176,24,40
ra 72,233,4096
s_bitcmp1_b32 s65,s64
s_cbranch_scc1 L_v_ev
L_v_evr:
mvaa 116,20,40
mvaa 124,20,44
ta 108,237,16384
mvaa 120,16,44
s_add_u32 m0,s61,0
mvaa 112,16,40
dl 224,36,52,0
mvaa 52,12,40
ta 110,237,16896
mvaa 60,12,44
mvaa 56,8,44
ra 76,233,6144
mvaa 48,8,40
maaa 148,4,40
ta 112,234,20480
maaa 156,4,44
dl 225,36,52,1024
maaa 152,0,44
ta 114,234,20992
maaa 144,0,40
mvaa 0,0,48
ra 80,233,8192
mvaa 8,0,52
mvaa 12,4,52
ta 116,235,20480
mvaa 4,4,48
dl 226,36,52,2048
mvaa 64,8,48
mvaa 72,8,52
ta 118,235,20992
mvaa 76,12,52
mvaa 68,12,48
ra 84,233,10240
mvaa 128,16,48
mvaa 136,16,52
dl 227,36,52,3072
mvaa 140,20,52
s_add_u32 s52,s52,s56
ta 120,236,20480
mvaa 132,20,48
s_cmp_ge_u32 s63,65536
s_cselect_b32 s99,-2,1
s_lshl_b32 s99,s99,15
mvaa 192,24,48
ta 122,236,20992
s_add_u32 s63,s63,s99
mvaa 200,24,52
v_add_u32_e32 v232,s99,v232
mvaa 204,28,52
ra 88,233,12288
mvaa 196,28,48
buffer_load_dword a184,v241,s[48:51],s55 offen
s_add_u32 s55,s55,s59
mvaa 212,28,56
mvaa 220,28,60
ta 124,237,20480
mvaa 216,24,60
mvaa 208,24,56
ta 126,237,20992
mvaa 148,20,56
mvaa 156,20,60
ra 92,233,14336
mvaa 152,16,60
mvaa 144,16,56
mvaa 84,12,56
mvaa 92,12,60
mvaa 88,8,60
mvaa 80,8,56
mvaa 20,4,56
mvaa 28,4,60
s_mov_b32 s50,s33
s_mov_b32 s59,s34
s_lshl_b32 s86,s21,2
s_add_u32 s86,s86,s12
s_mul_i32 s86,s86,384
s_add_u32 s86,s86,4096
v_lshl_add_u32 v253,v244,3,s86
s_mov_b64 exec,7
global_load_dword a188,v253,s[96:97]
s_mov_b64 exec,-1
s_add_u32 s64,s64,1
mvaa 24,0,60
s_waitcnt vmcnt(10) lgkmcnt(0)
s_barrier
mvaa 16,0,56
s_branch L_v_head
L_v_exit:
global_load_dwordx4 a[160:163],v244,s[30:31] offset:0
s_add_u32 m0,s62,49152
maaa 128,0,32
s_cmp_lt_u32 s12,2
s_cselect_b32 s86,s22,s23
ta 96,234,16384
v_accvgpr_read_b32 v224,a242
maaa 136,0,36
s_and_b32 s87,s12,1
ta 98,234,16896
s_lshl_b32 s86,s86,11
maaa 140,4,36
s_lshl_b32 s87,s87,17
ra 64,233,0
s_add_u32 s86,s86,s87
maaa 132,4,32
s_mov_b64 s[36:37],s[68:69]
s_mov_b32 s38,4194304
s_mov_b32 s52,s86
mvaa 32,8,32
buffer_load_dwordx4 v228,s[40:43],s53 offen offset:0 nt lds
mvaa 40,8,36
s_mov_b32 s56,65536
ta 100,235,16384
v_accvgpr_read_b32 v225,a243
mvaa 44,12,36
s_lshl_b32 s88,s18,1
ta 102,235,16896
v_accvgpr_read_b32 v226,a244
mvaa 36,12,32
s_add_u32 s88,s88,s12
ra 68,233,2048
v_accvgpr_read_b32 v227,a245
mvaa 96,16,32
s_mov_b32 s91,196608
ra 72,233,4096
s_lshl_b32 s88,s88,7
mvaa 104,16,36
buffer_load_dwordx4 v229,s[40:43],s53 offen offset:1024 nt lds
mvaa 108,20,36
s_lshl_b32 s89,s18,6
ra 76,233,6144
s_mov_b32 s90,6291456
mvaa 100,20,32
s_cmp_lt_u32 s12,2
s_cselect_b32 s57,262144,s91
ra 80,233,8192
s_add_u32 s53,s53,s58
mvaa 160,24,32
s_cmp_lt_u32 s12,2
s_cselect_b32 s42,8388608,s90
ra 84,233,10240
v_accvgpr_read_b32 v228,a246
mvaa 168,24,36
s_cmp_lt_u32 s12,2
s_cselect_b32 s53,s88,s89
ra 88,233,12288
v_accvgpr_read_b32 v229,a247
mvaa 172,28,36
buffer_load_dwordx4 v230,s[44:47],s54 offen offset:2048 nt lds
mvaa 164,28,32
s_bitcmp1_b32 s21,0
s_cselect_b32 s90,s57,0
ra 92,233,14336
s_add_u32 s53,s53,s90
mvaa 180,28,40
s_cmp_lt_u32 s12,2
s_cselect_b64 s[40:41],s[72:73],s[80:81]
ta 104,236,16384
v_accvgpr_read_b32 v230,a248
mvaa 188,28,44
s_lshl_b32 s90,s90,1
ta 106,236,16896
s_mov_b32 s60,-65408
mvaa 184,24,44
s_and_b32 s89,s98,8192
ta 108,237,16384
s_add_u32 s91,s89,65536
mvaa 176,24,40
buffer_load_dwordx4 v231,s[44:47],s54 offen offset:3072 nt lds
mvaa 116,20,40
s_add_u32 m0,s98,65536
ta 110,237,16896
s_add_u32 s54,s54,s58
mvaa 124,20,44
s_cmp_eq_u32 s12,3
s_cselect_b64 s[44:45],s[68:69],s[40:41]
ta 112,234,20480
v_accvgpr_read_b32 v231,a249
mvaa 120,16,44
s_cmp_eq_u32 s12,3
s_cselect_b32 s46,0,s42
ta 114,234,20992
s_add_u32 s58,s57,s90
mvaa 112,16,40
s_cmp_eq_u32 s12,3
s_cselect_b32 s54,0,s53
ta 116,235,20480
s_sub_u32 s57,s57,s90
mvaa 52,12,40
global_load_dwordx4 a[168:171],v244,s[30:31] offset:2048
mvaa 60,12,44
s_add_u32 s90,s89,65536
ta 118,235,20992
s_bfe_u32 s88,s21,65539
mvaa 56,8,44
s_lshl_b32 s88,s88,1
ta 120,236,20480
s_lshl_b32 s87,s20,2
mvaa 48,8,40
s_add_u32 s88,s88,s87
ta 122,236,20992
s_and_b32 s87,s12,1
maaa 148,4,40
s_add_u32 s88,s88,s87
ta 124,237,20480
s_mul_i32 s88,s88,524288
maaa 156,4,44
global_load_dwordx4 a[176:179],v244,s[84:85] offset:0
maaa 152,0,44
s_and_b32 s87,s21,7
ta 126,237,20992
s_add_u32 s86,s88,33554432
maaa 144,0,40
s_lshl_b32 s87,s87,16
s_mov_b32 s26,32768
s_add_u32 s86,s86,s87
mvaa 0,0,48
s_lshr_b32 s87,s12,1
s_mov_b32 s27,131072
s_lshl_b32 s87,s87,15
mvaa 8,0,52
s_add_u32 s86,s86,s87
s_lshl_b32 s87,s12,3
s_add_u32 s61,s98,32768
mvaa 12,4,52
global_load_dwordx4 a[164:167],v244,s[30:31] offset:1024
mvaa 4,4,48
s_add_u32 s24,s8,s86
s_addc_u32 s25,s9,0
s_lshl_b32 s86,%1,5
s_add_u32 s86,s86,s87
mvaa 64,8,48
s_add_u32 s86,s86,83886080
s_mov_b32 s63,65536
s_mov_b32 s67,2048
mvaa 72,8,52
s_cmp_ge_u32 s63,65536
s_cselect_b32 s99,-2,1
s_lshl_b32 s99,s99,15
s_mov_b32 s66,8
mvaa 76,12,52
s_cmp_eq_u32 s12,0
s_cselect_b32 s65,96,0
s_mov_b32 s88,4096
s_mov_b32 s64,1
mvaa 68,12,48
global_load_dwordx4 a[172:175],v244,s[30:31] offset:3072
mvaa 128,16,48
s_mov_b32 s30,1048577
s_mov_b32 s31,268435712
s_or_b32 s65,s65,128
mvaa 136,16,52
s_bitcmp1_b32 s12,0
s_cselect_b64 vcc,-1,0
mvaa 140,20,52
mvaa 132,20,48
mvaa 192,24,48
global_load_dwordx4 a[180:183],v244,s[84:85] offset:1024
mvaa 200,24,52
mvaa 204,28,52
mvaa 196,28,48
mvaa 212,28,56
mvaa 220,28,60
dl 224,36,52,0
mvaa 216,24,60
mvaa 208,24,56
mvaa 148,20,56
mvaa 156,20,60
mvaa 152,16,60
dl 225,36,52,1024
mvaa 144,16,56
mvaa 84,12,56
s_waitcnt vmcnt(13) lgkmcnt(0)
s_barrier
mvaa 92,12,60
ta 32,234,32768
mvaa 88,8,60
ta 34,234,33280
mvaa 80,8,56
dl 226,36,52,2048
mvaa 20,4,56
ta 36,235,32768
mvaa 28,4,60
ta 38,235,33280
mvaa 24,0,60
ta 40,236,32768
mvaa 16,0,56
ta 42,236,33280
maaa 128,64,96
dl 227,36,52,3072
maaa 136,64,100
s_add_u32 s52,s52,s56
s_add_u32 m0,s98,69632
ta 44,237,32768
maaa 140,68,100
ta 46,237,33280
maaa 132,68,96
ta 48,234,36864
mvaa 32,72,96
ta 50,234,37376
mvaa 40,72,100
dl 224,36,52,0
mvaa 44,76,100
ta 52,235,36864
mvaa 36,76,96
ta 54,235,37376
mvaa 96,80,96
ra 20,232,43008
mvaa 104,80,100
ra 24,232,45056
mvaa 108,84,100
dl 225,36,52,1024
mvaa 100,84,96
ra 28,232,47104
mvaa 160,88,96
ra 16,232,40960
mvaa 168,88,100
ra 0,232,32768
mvaa 172,92,100
ra 4,232,34816
mvaa 164,92,96
dl 226,36,52,2048
mvaa 180,92,104
ra 12,232,38912
mvaa 188,92,108
ra 8,232,36864
v_add_u32_e32 v232,s90,v232
mvaa 184,88,108
ta 60,237,36864
mvaa 176,88,104
ta 62,237,37376
mvaa 116,84,104
dl 227,36,52,3072
mvaa 124,84,108
s_add_u32 m0,s62,0
ta 56,236,36864
s_add_u32 s52,s52,s60
mvaa 120,80,108
ta 58,236,37376
mvaa 112,80,104
mvaa 52,76,104
mvaa 60,76,108
dl 228,40,53,0
mvaa 56,72,108
mvaa 48,72,104
maaa 148,68,104
maaa 156,68,108
maaa 152,64,108
dl 229,40,53,1024
maaa 144,64,104
s_add_u32 s53,s53,s57
mvaa 0,64,112
mvaa 8,64,116
mvaa 12,68,116
mvaa 4,68,112
dl 230,44,54,2048
mvaa 64,72,112
mvaa 72,72,116
mvaa 76,76,116
mvaa 68,76,112
mvaa 128,80,112
dl 231,44,54,3072
mvaa 136,80,116
s_add_u32 m0,s62,16384
s_add_u32 s54,s54,s57
mvaa 140,84,116
mvaa 132,84,112
mvaa 192,88,112
mvaa 200,88,116
dl 228,40,53,0
mvaa 204,92,116
mvaa 196,92,112
mvaa 212,92,120
mvaa 220,92,124
mvaa 216,88,124
dl 229,40,53,1024
mvaa 208,88,120
s_waitcnt vmcnt(19) lgkmcnt(0)
s_barrier
mvaa 148,84,120
s_add_u32 s53,s53,s58
ta 96,234,49152
mvaa 156,84,124
ra 92,233,47104
mvaa 152,80,124
ta 98,234,49664
mvaa 144,80,120
dl 230,44,54,2048
mvaa 84,76,120
ra 88,233,45056
mvaa 92,76,124
ta 100,235,49152
mvaa 88,72,124
ta 102,235,49664
mvaa 80,72,120
ta 116,235,53248
mvaa 20,68,120
dl 231,44,54,3072
mvaa 28,68,124
s_add_u32 m0,s98,0
ta 118,235,53760
v_subrev_u32_e32 v235,s89,v235
mvaa 24,64,124
s_add_u32 s54,s54,s58
ta 104,236,49152
mvaa 16,64,120
ta 106,236,49664
maaa 128,0,32
ta 124,237,53248
maaa 136,0,36
dl 224,36,52,0
maaa 140,4,36
ta 126,237,53760
maaa 132,4,32
ta 120,236,53248
mvaa 32,8,32
ta 122,236,53760
v_subrev_u32_e32 v236,s89,v236
mvaa 40,8,36
ta 112,234,53248
mvaa 44,12,36
dl 225,36,52,1024
mvaa 36,12,32
ta 114,234,53760
v_subrev_u32_e32 v234,s89,v234
mvaa 52,12,40
ta 108,237,49152
mvaa 60,12,44
ta 110,237,49664
v_subrev_u32_e32 v237,s89,v237
mvaa 56,8,44
ra 84,233,43008
mvaa 48,8,40
dl 226,36,52,2048
maaa 148,4,40
ra 76,233,38912
maaa 156,4,44
ra 80,233,40960
maaa 152,0,44
ra 68,233,34816
maaa 144,0,40
ra 72,233,36864
mvaa 0,0,48
dl 227,36,52,3072
mvaa 8,0,52
s_add_u32 s52,s52,s56
s_add_u32 m0,s98,4096
ra 64,233,32768
mvaa 12,4,52
v_add_u32_e32 v233,s91,v233
mvaa 4,4,48
mvaa 20,4,56
mvaa 28,4,60
dl 224,36,52,0
mvaa 24,0,60
mvaa 16,0,56
mvaa 220,28,60
s_waitcnt lgkmcnt(2)
mvaa 220,92,124
mvaa 216,88,124
dl 225,36,52,1024
mvaa 216,24,60
mvaa 212,28,56
mvaa 212,92,120
mvaa 208,88,120
mvaa 208,24,56
dl 226,36,52,2048
s_waitcnt vmcnt(25)
zvaa 246,160,176
mvaa 204,28,52
cv 223,222,223
cv 222,220,221
mvaa 204,92,116
cv 221,218,219
zvaa 250,168,176
mvaa 200,88,116
cv 220,216,217
cv 219,214,215
mvaa 200,24,52
cv 218,212,213
cv 217,210,211
mvaa 196,28,48
dl 227,36,52,3072
mvaa 196,92,112
s_add_u32 m0,s62,32768
cv 216,208,209
cv 215,206,207
mvaa 192,88,112
s_add_u32 s52,s52,s60
cv 214,204,205
cv 213,202,203
mvaa 192,24,48
cv 212,200,201
s_waitcnt vmcnt(24)
mvaa 246,164,180
mvaa 250,172,180
mvaa 188,28,44
cv 211,198,199
cv 210,196,197
mvaa 188,92,108
dl 228,40,53,0
mvaa 184,88,108
cv 209,194,195
v_cndmask_b32_e64 v246,0,v246,s[30:31]
v_cndmask_b32_e64 v250,0,v250,s[30:31]
s_lshl_b64 s[30:31],s[30:31],1
v_cndmask_b32_e64 v246,v246,v247,s[30:31]
v_cndmask_b32_e64 v250,v250,v251,s[30:31]
s_lshl_b64 s[30:31],s[30:31],1
mvaa 184,24,44
v_cndmask_b32_e64 v246,v246,v248,s[30:31]
v_cndmask_b32_e64 v250,v250,v252,s[30:31]
s_lshl_b64 s[30:31],s[30:31],1
v_cndmask_b32_e64 v246,v246,v249,s[30:31]
v_cndmask_b32_e64 v250,v250,v253,s[30:31]
mvaa 180,28,40
v_add_f32_dpp v246,v246,v246 quad_perm:[1,0,3,2] row_mask:0xf bank_mask:0xf
v_add_f32_dpp v250,v250,v250 quad_perm:[1,0,3,2] row_mask:0xf bank_mask:0xf
mvaa 180,92,104
v_add_f32_dpp v246,v246,v246 quad_perm:[2,3,0,1] row_mask:0xf bank_mask:0xf
v_add_f32_dpp v250,v250,v250 quad_perm:[2,3,0,1] row_mask:0xf bank_mask:0xf
mvaa 176,88,104
dl 229,40,53,1024
mvaa 176,24,40
s_add_u32 s53,s53,s57
v_add_f32_dpp v246,v246,v246 row_half_mirror row_mask:0xf bank_mask:0xf
v_add_f32_dpp v250,v250,v250 row_half_mirror row_mask:0xf bank_mask:0xf
mvaa 172,28,36
v_add_f32_dpp v246,v246,v246 row_mirror row_mask:0xf bank_mask:0xf
v_add_f32_dpp v250,v250,v250 row_mirror row_mask:0xf bank_mask:0xf
mvaa 172,92,100
cv 208,192,193
cv 207,190,191
mvaa 168,88,100
v_permlane16_swap_b32_e32 v246,v250
mvaa 168,24,36
dl 230,44,54,2048
v_add_f32_e32 v246,v246,v250
v_mov_b32_e32 v250,v246
mvaa 164,28,32
cv 206,188,189
cv 205,186,187
mvaa 164,92,96
v_permlane32_swap_b32_e32 v246,v250
mvaa 160,88,96
v_add_f32_e32 v246,v246,v250
v_lshrrev_b32_e32 v250,6,v244
mvaa 160,24,32
v_mul_f32_e32 v246,0xbfb8aa3b,v246
cv 204,184,185
mvaa 156,20,60
dl 231,44,54,3072
ex 246
mvaa 156,84,124
s_add_u32 s54,s54,s57
v_add_f32_e32 v246,1.0,v246
cv 203,182,183
mvaa 152,80,124
v_rcp_f32_e32 v246,v246
mvaa 152,16,60
cv 202,180,181
cv 201,178,179
mvaa 148,20,56
cv 200,176,177
cv 199,174,175
mvaa 148,84,120
buffer_load_dword a242,v241,s[48:51],s55 offen
mvaa 144,80,120
s_add_u32 s55,s55,s59
cv 198,172,173
cv 197,170,171
mvaa 144,16,56
s_waitcnt vmcnt(17) lgkmcnt(0)
s_barrier
s_add_u32 m0,s62,49152
mvaa 140,20,52
cv 196,168,169
cv 195,166,167
mvaa 140,84,116
cv 194,164,165
cv 193,162,163
mvaa 136,80,116
dl 228,40,53,0
mvaa 136,16,52
cv 192,160,161
cv 191,158,159
mvaa 132,20,48
ra 4,232,0
cv 190,156,157
mvaa 132,84,112
ta 88,238,12288
cv 189,154,155
mvaa 128,80,112
)"
R"(ta 90,238,12800
cv 188,152,153
mvaa 128,16,48
dl 229,40,53,1024
mvaa 124,20,44
s_add_u32 s53,s53,s58
ta 92,239,12288
cv 187,150,151
mvaa 124,84,108
ta 94,239,12800
cv 186,148,149
mvaa 120,80,108
cv 185,146,147
cv 184,144,145
mvaa 120,16,44
cv 183,142,143
cv 182,140,141
mvaa 116,20,40
dl 230,44,54,2048
mvaa 116,84,104
cv 181,138,139
cv 180,136,137
mvaa 112,80,104
cv 179,134,135
cv 178,132,133
mvaa 112,16,40
cv 177,130,131
cv 176,128,129
mvaa 108,20,36
cv 175,126,127
cv 174,124,125
mvaa 108,84,100
dl 231,44,54,3072
mvaa 104,80,100
s_add_u32 m0,s98,32768
ta 44,235,0
cv 173,122,123
mvaa 104,16,36
s_add_u32 s54,s54,s58
ta 46,235,512
cv 172,120,121
mvaa 100,20,32
cv 171,118,119
cv 170,116,117
mvaa 100,84,96
ta 40,234,0
cv 169,114,115
mvaa 96,80,96
dl 224,36,52,0
mvaa 96,16,32
ta 42,234,512
cv 168,112,113
mvaa 92,12,60
cv 167,110,111
cv 166,108,109
mvaa 92,76,124
cv 165,106,107
cv 164,104,105
mvaa 88,72,124
ta 84,237,8192
cv 163,102,103
mvaa 88,8,60
dl 225,36,52,1024
mvaa 84,12,56
ta 86,237,8704
cv 162,100,101
mvaa 84,76,120
ta 80,236,8192
cv 161,98,99
mvaa 80,72,120
ra 16,232,6144
cv 160,96,97
mvaa 80,8,56
ta 82,236,8704
cv 159,94,95
mvaa 76,12,52
dl 226,36,52,2048
mvaa 76,76,116
ta 60,235,4096
cv 158,92,93
mvaa 72,72,116
ta 62,235,4608
cv 157,90,91
mvaa 72,8,52
ta 56,234,4096
cv 156,88,89
mvaa 68,12,48
ta 58,234,4608
cv 151,78,79
mvaa 68,76,112
dl 227,36,52,3072
mvaa 64,72,112
s_add_u32 m0,s62,0
ta 52,237,0
s_add_u32 s52,s52,s56
mvaa 64,8,48
ra 12,232,4096
cv 150,76,77
mvaa 60,76,108
ta 54,237,512
cv 147,70,71
mvaa 56,72,108
ra 8,232,2048
cv 146,68,69
mvaa 52,76,104
buffer_load_dword a243,v241,s[48:51],s55 offen
mvaa 48,72,104
s_add_u32 s55,s55,s59
ta 48,236,0
cv 145,66,67
mvaa 44,76,100
ta 50,236,512
cv 144,64,65
mvaa 40,72,100
cv 143,62,63
cv 142,60,61
mvaa 36,76,96
cv 141,58,59
cv 140,56,57
mvaa 32,72,96
buffer_load_dword a244,v241,s[48:51],s55 offen
mvaa 28,68,124
s_add_u32 s55,s55,s59
cv 139,54,55
cv 138,52,53
mvaa 24,64,124
cv 137,50,51
cv 136,48,49
mvaa 20,68,120
cv 135,46,47
cv 134,44,45
mvaa 16,64,120
cv 133,42,43
cv 132,40,41
mvaa 12,68,116
buffer_load_dword a245,v241,s[48:51],s55 offen
mvaa 8,64,116
s_add_u32 s55,s55,s59
ta 76,235,8192
cv 131,38,39
mvaa 4,68,112
ta 78,235,8704
cv 130,36,37
mvaa 0,64,112
ta 72,234,8192
cv 129,34,35
maaa 156,68,108
ta 74,234,8704
cv 128,32,33
maaa 152,64,108
s_mov_b64 exec,65537
buffer_store_dword v246,v250,s[8:11],s86 offen sc0 sc1
s_mov_b64 exec,-1
maaa 148,68,104
cv 127,30,31
v_accvgpr_read_b32 v63,a159
maaa 144,64,104
v_accvgpr_read_b32 v62,a158
v_accvgpr_read_b32 v61,a157
maaa 140,68,100
v_accvgpr_read_b32 v60,a156
v_accvgpr_read_b32 v59,a155
maaa 136,64,100
v_accvgpr_read_b32 v58,a154
v_accvgpr_read_b32 v57,a153
maaa 132,68,96
v_accvgpr_read_b32 v56,a152
cv 56,56,57
maaa 128,64,96
cv 57,58,59
cv 58,60,61
s_waitcnt lgkmcnt(2)
zaaa 236,40,4
ta 68,237,4096
ta 70,237,4608
zaaa 220,44,4
ta 64,236,4096
ta 66,236,4608
zaaa 216,44,8
cv 59,62,63
v_accvgpr_read_b32 v55,a151
zaaa 232,40,8
buffer_store_dwordx4 v[56:59],v244,s[24:27],0 offen offset:3072 sc0 sc1
zaaa 228,40,12
v_accvgpr_read_b32 v54,a150
v_accvgpr_read_b32 v53,a149
zaaa 212,44,12
v_accvgpr_read_b32 v52,a148
v_accvgpr_read_b32 v51,a147
zaaa 208,44,16
v_accvgpr_read_b32 v50,a146
v_accvgpr_read_b32 v49,a145
zaaa 224,40,16
v_accvgpr_read_b32 v48,a144
cv 48,48,49
zaaa 192,48,16
cv 49,50,51
cv 50,52,53
zaaa 176,52,16
cv 51,54,55
s_waitcnt vmcnt(26) lgkmcnt(0)
s_barrier
zaaa 180,52,12
dl 228,40,53,0
zaaa 196,48,12
ra 24,233,0
v_accvgpr_read_b32 v47,a143
zaaa 200,48,8
ra 28,233,2048
v_accvgpr_read_b32 v46,a142
v_accvgpr_read_b32 v45,a141
zaaa 184,52,8
ra 32,233,4096
ra 36,233,6144
zaaa 188,52,4
v_accvgpr_read_b32 v43,a139
v_accvgpr_read_b32 v42,a138
zaaa 204,48,4
dl 229,40,53,1024
ta 96,236,24576
zaaa 172,56,4
s_add_u32 s53,s53,s57
ta 98,236,25088
v_accvgpr_read_b32 v41,a137
zaaa 156,60,4
ta 100,237,24576
v_accvgpr_read_b32 v39,a135
zaaa 152,60,8
ta 102,237,25088
ta 104,238,28672
zaaa 168,56,8
v_accvgpr_read_b32 v38,a134
v_accvgpr_read_b32 v35,a131
zaaa 164,56,12
buffer_store_dwordx4 v[48:51],v244,s[24:27],0 offen offset:2048 sc0 sc1
ta 106,238,29184
zaaa 148,60,12
ta 108,239,28672
v_accvgpr_read_b32 v34,a130
zaaa 144,60,16
ta 110,239,29184
v_accvgpr_read_b32 v33,a129
zaaa 160,56,16
ta 44,235,16384
ta 46,235,16896
ta 40,234,16384
ta 42,234,16896
ta 52,237,16384
ta 54,237,16896
ta 48,236,16384
ta 50,236,16896
ta 60,235,20480
ta 62,235,20992
ta 56,234,20480
ta 58,234,20992
v_accvgpr_read_b32 v32,a128
zaaa 128,64,16
v_accvgpr_read_b32 v37,a133
v_accvgpr_read_b32 v36,a132
zaaa 112,68,16
dl 230,44,54,2048
zaaa 116,68,12
v_accvgpr_read_b32 v40,a136
v_accvgpr_read_b32 v44,a140
zaaa 132,64,12
cv 251,42,43
cv 250,40,41
zaaa 136,64,8
cv 253,46,47
cv 252,44,45
zaaa 120,68,8
cv 126,28,29
cv 125,26,27
zaaa 124,68,4
dl 231,44,54,3072
zaaa 140,64,4
s_add_u32 m0,s61,4096
cv 249,38,39
cv 247,34,35
zvaa 40,72,4
s_add_u32 s54,s54,s57
ta 68,237,20480
cv 246,32,33
zvaa 44,76,4
s_add_u32 s61,s63,s98
ta 70,237,20992
cv 248,36,37
zvaa 52,76,8
s_add_u32 s63,s63,s99
ta 64,236,20480
cv 124,24,25
zvaa 48,72,8
dl 224,36,52,0
zvaa 56,72,12
ta 66,236,20992
cv 123,22,23
zvaa 60,76,12
cv 121,18,19
cv 120,16,17
zvaa 68,76,16
cv 122,20,21
cv 119,14,15
zvaa 64,72,16
cv 118,12,13
cv 117,10,11
zvaa 32,80,16
dl 225,36,52,1024
ta 76,235,24576
ta 78,235,25088
zvaa 36,84,16
ta 72,234,24576
ta 74,234,25088
zvaa 28,84,12
cv 116,8,9
cv 153,82,83
zvaa 24,80,12
cv 152,80,81
cv 155,86,87
zvaa 16,80,8
cv 154,84,85
cv 149,74,75
zvaa 20,84,8
dl 226,36,52,2048
zvaa 12,84,4
cv 148,72,73
v_add_u32_e32 v232,s99,v232
zvaa 8,80,4
cv 115,6,7
cv 114,4,5
zvaa 76,4,88
cv 113,2,3
cv 112,0,1
zvaa 96,4,92
zvaa 100,8,92
dl 227,36,52,3072
zvaa 80,8,88
s_add_u32 s52,s52,s60
zvaa 84,12,88
zvaa 104,12,92
zvaa 108,16,92
s_waitcnt vmcnt(23) lgkmcnt(0)
s_barrier
zvaa 88,16,88
buffer_store_dwordx4 v[250:253],v244,s[24:27],0 offen offset:1024 sc0 sc1
buffer_store_dwordx4 v[246:249],v244,s[24:27],0 offen offset:0 sc0 sc1
L_kvx:
L_p_exit:
s_add_u32 m0,s62,16384
ra 4,232,0
s_lshl_b32 s86,s21,2
maaa 236,40,24
dl 228,40,53,0
maaa 220,44,24
s_add_u32 s86,s86,s12
ra 8,232,2048
s_lshl_b32 s86,s86,8
maaa 216,44,28
s_add_u32 s86,s86,L_p_exit-L_att_pc+2048
ra 12,232,4096
v_lshl_add_u32 v253,v244,3,s86
maaa 232,40,28
s_mov_b32 s67,4096
ra 16,232,6144
s_and_b32 s87,s12,1
maaa 228,40,32
s_cmp_lt_u32 s12,2
s_cselect_b32 s94,s22,s23
ta 80,236,40960
s_lshr_b32 s94,s94,6
maaa 212,44,32
s_add_u32 s94,s94,s87
ta 82,236,41472
v_mov_b32_e32 v248,-1
maaa 208,44,36
dl 229,40,53,1024
maaa 224,40,36
s_add_u32 s53,s53,s58
ta 84,237,40960
v_mov_b32_e32 v249,-1
maaa 192,48,36
s_lshl_b32 s87,s18,5
ta 86,237,41472
s_add_u32 s94,s94,s87
maaa 176,52,36
s_lshl_b32 s94,s94,11
ta 88,238,45056
s_add_u32 s94,s94,12582912
maaa 180,52,32
s_bfe_u32 s88,s21,65539
ta 90,238,45568
v_mov_b32_e32 v250,-1
maaa 196,48,32
s_lshl_b32 s87,s20,1
ta 92,239,45056
v_mov_b32_e32 v251,-1
maaa 200,48,28
dl 230,44,54,2048
maaa 184,52,28
s_add_u32 s88,s88,s87
ta 94,239,45568
v_add_u32_e32 v233,s99,v233
maaa 188,52,24
s_cmp_ge_u32 s63,65536
s_cselect_b32 s99,-2,1
ta 44,235,32768
s_lshl_b32 s99,s99,15
maaa 204,48,24
s_lshl_b32 s88,s88,7
ta 46,235,33280
v_add_u32_e32 v232,s99,v232
maaa 172,56,24
s_add_u32 s85,s88,83894272
ta 40,234,32768
v_mov_b32_e32 v247,1
maaa 156,60,24
s_lshl_b32 s91,s12,4
ta 42,234,33280
v_bfe_u32 v252,v244,4,4
maaa 152,60,28
dl 231,44,54,3072
maaa 168,56,28
s_add_u32 m0,s61,0
ta 52,237,32768
s_add_u32 s54,s54,s58
maaa 164,56,32
s_mov_b32 s64,0x55505555
ta 54,237,33280
v_bfe_u32 v241,v244,6,4
maaa 148,60,32
s_mov_b32 s65,0x50005500
ta 48,236,32768
v_and_b32_e32 v241,12,v241
maaa 144,60,36
s_mov_b32 s66,0x50000
ta 50,236,33280
v_sub_u32_e32 v252,v252,v241
maaa 160,56,36
s_cmp_lt_u32 s12,2
s_cselect_b32 s90,s22,s23
v_add_u32_e32 v252,s91,v252
v_mov_b32_e32 v241,0xf149f2ca
maaa 128,64,36
s_mov_b64 exec,3
global_load_dword a250,v253,s[96:97]
s_mov_b64 exec,-1
maaa 112,68,36
s_mul_i32 s33,s90,10240
ta 60,235,36864
v_add_u32_e32 v253,10240,v253
maaa 116,68,32
s_cmp_lt_u32 s12,2
s_cselect_b32 s91,32768,0
ta 62,235,37376
s_add_u32 s91,s91,98304
maaa 132,64,32
s_lshl_b32 s95,s12,12
ta 56,234,36864
v_accvgpr_read_b32 v255,a241
maaa 136,64,28
s_add_u32 s70,s95,32768
ta 58,234,37376
s_add_u32 s72,s95,65536
maaa 120,68,28
s_mov_b64 s[30:31],s[28:29]
s_mov_b64 s[28:29],s[78:79]
v_cvt_f32_i32_e32 v252,v252
maaa 124,68,24
buffer_store_dwordx4 v[112:115],v244,s[24:27],s67 offen offset:0 sc0 sc1
maaa 140,64,24
s_add_u32 s68,s95,0
v_cndmask_b32_e64 v112,v241,0,s[64:65]
s_mov_b32 s64,0xaaa0aaaa
mvaa 40,72,24
s_mov_b32 s65,0xa000aa00
ta 68,237,36864
v_cndmask_b32_e64 v113,v241,0,s[64:65]
mvaa 44,76,24
s_mov_b32 s64,0x55405554
ta 70,237,37376
s_mov_b32 s65,0x40005400
mvaa 52,76,28
s_add_u32 s69,s95,16384
ta 64,236,36864
v_cndmask_b32_e64 v114,v241,0,s[64:65]
mvaa 48,72,28
s_mov_b32 s64,0xaa80aaa8
ta 66,236,37376
s_mov_b32 s65,0x8000a800
mvaa 56,72,32
dl 224,36,52,0
mvaa 60,76,32
s_add_u32 s74,s95,98304
v_cndmask_b32_e64 v115,v241,0,s[64:65]
v_mul_f32_e32 v252,0x7149f2ca,v252
mvaa 68,76,36
s_add_u32 s71,s95,49152
s_add_u32 s73,s95,81920
v_and_b32_e32 v245,48,v244
mvaa 64,72,36
s_lshl_b32 s59,%1,2
v_lshlrev_b32_e32 v245,3,v245
s_lshl_b32 s84,%1,2
mvaa 32,96,36
s_add_u32 s59,s59,s12
ta 76,235,40960
s_add_u32 s75,s95,114688
mvaa 36,100,36
s_add_u32 s84,s84,s12
ta 78,235,41472
s_mul_i32 s84,s84,12288
mvaa 28,100,32
dl 225,36,52,1024
mvaa 24,96,32
s_sub_u32 s55,s14,s13
ta 72,234,40960
s_mov_b32 s48,0x38000000
mvaa 16,96,28
s_mov_b32 s81,1
ta 74,234,41472
s_mov_b32 s84,0
mvaa 20,100,28
mvaa 12,100,24
mvaa 8,96,24
mvaa 76,24,104
dl 226,36,52,2048
mvaa 96,24,108
mvaa 100,28,108
mvaa 80,28,104
mvaa 84,32,104
mvaa 104,32,108
mvaa 108,36,108
dl 227,36,52,3072
mvaa 88,36,104
s_waitcnt vmcnt(20) lgkmcnt(0)
s_barrier
maaa 236,40,4
s_add_u32 m0,s62,32768
ra 24,233,0
s_add_u32 s52,s52,s56
maaa 220,44,4
ra 28,233,2048
maaa 216,44,8
ta 96,236,57344
maaa 232,40,8
ta 98,236,57856
maaa 228,40,12
dl 228,40,53,0
maaa 212,44,12
ta 100,237,57344
maaa 208,44,16
ta 102,237,57856
maaa 224,40,16
ra 32,233,4096
maaa 192,48,16
ta 108,239,61440
maaa 176,52,16
ta 110,239,61952
maaa 180,52,12
buffer_store_dwordx4 v[248:251],v244,s[8:11],s94 offen offset:0 sc0 sc1
maaa 196,48,12
ra 36,233,6144
v_add_u32_e32 v233,s99,v233
maaa 200,48,8
ta 104,238,61440
maaa 184,52,8
ta 106,238,61952
maaa 188,52,4
ta 44,235,49152
maaa 204,48,4
ta 46,235,49664
maaa 172,56,4
buffer_store_dwordx4 v[116:119],v244,s[24:27],s67 offen offset:1024 sc0 sc1
maaa 156,60,4
ta 40,234,49152
maaa 152,60,8
ta 42,234,49664
maaa 168,56,8
ta 52,237,49152
maaa 164,56,12
ta 54,237,49664
maaa 148,60,12
ta 48,236,49152
maaa 144,60,16
dl 229,40,53,1024
maaa 160,56,16
s_add_u32 s53,s53,s57
ta 50,236,49664
maaa 128,64,16
ta 60,235,53248
maaa 112,68,16
ta 62,235,53760
maaa 116,68,12
ta 56,234,53248
maaa 132,64,12
ta 58,234,53760
maaa 136,64,8
dl 230,44,54,2048
maaa 120,68,8
maaa 124,68,4
maaa 140,64,4
mvaa 40,72,4
ta 68,237,53248
mvaa 44,76,4
ta 70,237,53760
mvaa 52,76,8
dl 231,44,54,3072
mvaa 48,72,8
s_add_u32 m0,s61,4096
ta 64,236,53248
s_add_u32 s54,s54,s57
mvaa 56,72,12
s_add_u32 s61,s63,s98
ta 66,236,53760
s_add_u32 s63,s63,s99
mvaa 60,76,12
s_cmp_ge_u32 s63,65536
s_cselect_b32 s99,-2,1
s_lshl_b32 s99,s99,15
mvaa 68,76,16
mvaa 64,72,16
mvaa 32,80,16
dl 224,36,52,0
mvaa 36,84,16
ta 76,235,57344
mvaa 28,84,12
ta 78,235,57856
mvaa 24,80,12
ta 72,234,57344
mvaa 16,80,8
ta 74,234,57856
mvaa 20,84,8
mvaa 12,84,4
dl 225,36,52,1024
mvaa 8,80,4
mvaa 76,4,88
mvaa 96,4,92
mvaa 100,8,92
mvaa 80,8,88
mvaa 84,12,88
dl 226,36,52,2048
mvaa 104,12,92
mvaa 108,16,92
s_waitcnt vmcnt(21) lgkmcnt(0)
s_barrier
mvaa 88,16,88
ra 4,232,0
maaa 236,40,24
ra 8,232,2048
maaa 220,44,24
ta 80,236,8192
maaa 216,44,28
buffer_store_dwordx4 v[120:123],v244,s[24:27],s67 offen offset:2048 sc0 sc1
maaa 232,40,28
ta 82,236,8704
maaa 228,40,32
ta 84,237,8192
maaa 212,44,32
ta 86,237,8704
maaa 208,44,36
ra 12,232,4096
maaa 224,40,36
ta 92,239,12288
maaa 192,48,36
buffer_store_dwordx4 v[248:251],v244,s[8:11],s94 offen offset:1024 sc0 sc1
maaa 176,52,36
ta 94,239,12800
v_and_b32_e32 v250,240,v244
maaa 180,52,32
ra 16,232,6144
v_add_u32_e32 v232,s99,v232
maaa 196,48,32
ta 88,238,12288
v_accvgpr_read_b32 v249,a240
maaa 200,48,28
ta 90,238,12800
maaa 184,52,28
ta 44,235,0
maaa 188,52,24
dl 227,36,52,3072
maaa 204,48,24
s_add_u32 m0,s62,49152
ta 46,235,512
s_add_u32 s52,s52,s60
maaa 172,56,24
ta 40,234,0
maaa 156,60,24
ta 42,234,512
maaa 152,60,28
ta 52,237,0
maaa 168,56,28
ta 54,237,512
maaa 164,56,32
dl 228,40,53,0
maaa 148,60,32
ta 48,236,0
maaa 144,60,36
ta 50,236,512
maaa 160,56,36
maaa 128,64,36
ta 60,235,4096
maaa 112,68,36
ta 62,235,4608
maaa 116,68,32
dl 229,40,53,1024
maaa 132,64,32
s_add_u32 s53,s53,s58
ta 56,234,4096
maaa 136,64,28
ta 58,234,4608
maaa 120,68,28
maaa 124,68,24
maaa 140,64,24
mvaa 40,72,24
dl 230,44,54,2048
mvaa 44,76,24
ta 68,237,4096
mvaa 52,76,28
ta 70,237,4608
mvaa 48,72,28
ta 64,236,4096
mvaa 56,72,32
ta 66,236,4608
mvaa 60,76,32
mvaa 68,76,36
buffer_store_dwordx4 v[124:127],v244,s[24:27],s67 offen offset:3072 sc0 sc1
mvaa 64,72,36
s_mov_b32 s67,8192
v_mov_b32_e32 v124,-1
v_mov_b32_e32 v125,-1
mvaa 32,96,36
ta 76,235,8192
v_mov_b32_e32 v126,-1
mvaa 36,100,36
ta 78,235,8704
v_mov_b32_e32 v127,-1
mvaa 28,100,32
ta 72,234,8192
mvaa 24,96,32
ta 74,234,8704
mvaa 16,96,28
dl 231,44,54,3072
mvaa 20,100,28
s_add_u32 m0,s61,0
s_add_u32 s54,s54,s58
mvaa 12,100,24
mvaa 8,96,24
mvaa 76,24,104
mvaa 96,24,108
mvaa 100,28,108
dl 224,36,52,0
mvaa 80,28,104
mvaa 84,32,104
mvaa 104,32,108
mvaa 108,36,108
s_waitcnt vmcnt(18) lgkmcnt(0)
s_barrier
mvaa 88,36,104
ra 24,233,0
maaa 236,40,4
dl 225,36,52,1024
maaa 220,44,4
ra 28,233,2048
maaa 216,44,8
ta 96,236,24576
maaa 232,40,8
ta 98,236,25088
maaa 228,40,12
ta 100,237,24576
maaa 212,44,12
ta 102,237,25088
maaa 208,44,16
dl 226,36,52,2048
maaa 224,40,16
ra 32,233,4096
maaa 192,48,16
ta 108,239,28672
maaa 176,52,16
ta 110,239,29184
maaa 180,52,12
ra 36,233,6144
v_add_u32_e32 v233,s99,v233
maaa 196,48,12
ta 104,238,28672
maaa 200,48,8
dl 227,36,52,3072
maaa 184,52,8
s_add_u32 m0,s62,0
ta 106,238,29184
s_add_u32 s52,s52,s56
maaa 188,52,4
ta 44,235,16384
maaa 204,48,4
ta 46,235,16896
maaa 172,56,4
ta 40,234,16384
maaa 156,60,4
ta 42,234,16896
maaa 152,60,8
dl 228,40,53,0
maaa 168,56,8
ta 52,237,16384
maaa 164,56,12
ta 54,237,16896
maaa 148,60,12
ta 48,236,16384
maaa 144,60,16
ta 50,236,16896
maaa 160,56,16
maaa 128,64,16
buffer_store_dwordx4 v[128:131],v244,s[24:27],s67 offen offset:0 sc0 sc1
maaa 112,68,16
ta 60,235,20480
v_add_f32_e32 v128,0x70c9f2ca,v252
maaa 116,68,12
ta 62,235,20992
v_add_f32_e32 v129,0xf0c9f2ca,v252
maaa 132,64,12
ta 56,234,20480
v_add_f32_e32 v130,0xf1977617,v252
maaa 136,64,8
ta 58,234,20992
v_add_f32_e32 v131,0xf1fc6f7c,v252
maaa 120,68,8
v_min_f32_e32 v128,0xc1a00000,v128
v_min_f32_e32 v129,0xc1a00000,v129
maaa 124,68,4
dl 229,40,53,1024
maaa 140,64,4
s_add_u32 s53,s53,s57
v_min_f32_e32 v130,0xc1a00000,v130
v_min_f32_e32 v131,0xc1a00000,v131
mvaa 40,72,4
ta 68,237,20480
mvaa 44,76,4
ta 70,237,20992
mvaa 52,76,8
ta 64,236,20480
mvaa 48,72,8
ta 66,236,20992
mvaa 56,72,12
dl 230,44,54,2048
mvaa 60,76,12
mvaa 68,76,16
mvaa 64,72,16
mvaa 32,80,16
ta 76,235,24576
mvaa 36,84,16
ta 78,235,25088
mvaa 28,84,12
dl 231,44,54,3072
mvaa 24,80,12
s_add_u32 m0,s61,4096
ta 72,234,24576
s_add_u32 s54,s54,s57
mvaa 16,80,8
s_add_u32 s61,s63,s98
ta 74,234,25088
s_add_u32 s63,s63,s99
mvaa 20,84,8
s_cmp_ge_u32 s63,65536
s_cselect_b32 s99,-2,1
s_lshl_b32 s99,s99,15
mvaa 12,84,4
mvaa 8,80,4
mvaa 76,4,88
dl 224,36,52,0
mvaa 96,4,92
mvaa 100,8,92
mvaa 80,8,88
mvaa 84,12,88
mvaa 104,12,92
mvaa 108,16,92
dl 225,36,52,1024
mvaa 88,16,88
s_waitcnt vmcnt(16) lgkmcnt(0)
s_barrier
maaa 236,40,24
ra 4,232,0
maaa 220,44,24
ra 8,232,2048
maaa 216,44,28
ta 80,236,40960
maaa 232,40,28
ta 82,236,41472
maaa 228,40,32
buffer_load_dword v246,off,s[8:11],s35 offset:1024 sc1
maaa 212,44,32
ta 84,237,40960
maaa 208,44,36
ta 86,237,41472
maaa 224,40,36
ra 12,232,4096
maaa 192,48,36
ta 92,239,45056
maaa 176,52,36
ta 94,239,45568
maaa 180,52,32
buffer_store_dwordx4 v[132:135],v244,s[24:27],s67 offen offset:1024 sc0 sc1
maaa 196,48,32
ra 16,232,6144
v_add_u32_e32 v232,s99,v232
maaa 200,48,28
ta 88,238,45056
v_add_f32_e32 v132,0xf343a333,v252
maaa 184,52,28
ta 90,238,45568
v_add_f32_e32 v133,0xf3504260,v252
maaa 188,52,24
ta 44,235,32768
v_add_f32_e32 v134,0xf35ce18d,v252
maaa 204,48,24
ta 46,235,33280
v_min_f32_e32 v132,0xc1a00000,v132
maaa 172,56,24
dl 226,36,52,2048
maaa 156,60,24
ta 40,234,32768
v_add_f32_e32 v135,0xf36980b9,v252
maaa 152,60,28
ta 42,234,33280
v_min_f32_e32 v133,0xc1a00000,v133
maaa 168,56,28
ta 52,237,32768
v_min_f32_e32 v134,0xc1a00000,v134
maaa 164,56,32
ta 54,237,33280
v_min_f32_e32 v135,0xc1a00000,v135
maaa 148,60,32
ta 48,236,32768
maaa 144,60,36
dl 227,36,52,3072
maaa 160,56,36
s_add_u32 m0,s62,16384
ta 50,236,33280
s_add_u32 s52,s52,s60
maaa 128,64,36
ta 60,235,36864
maaa 112,68,36
ta 62,235,37376
maaa 116,68,32
ta 56,234,36864
maaa 132,64,32
ta 58,234,37376
maaa 136,64,28
dl 228,40,53,0
maaa 120,68,28
maaa 124,68,24
maaa 140,64,24
mvaa 40,72,24
ta 68,237,36864
mvaa 44,76,24
ta 70,237,37376
mvaa 52,76,28
dl 229,40,53,1024
mvaa 48,72,28
s_add_u32 s53,s53,s58
ta 64,236,36864
mvaa 56,72,32
ta 66,236,37376
mvaa 60,76,32
mvaa 68,76,36
mvaa 64,72,36
mvaa 32,96,36
dl 230,44,54,2048
mvaa 36,100,36
ta 76,235,40960
mvaa 28,100,32
ta 78,235,41472
mvaa 24,96,32
ta 72,234,40960
mvaa 16,96,28
ta 74,234,41472
mvaa 20,100,28
mvaa 12,100,24
buffer_store_dwordx4 v[136:139],v244,s[24:27],s67 offen offset:2048 sc0 sc1
mvaa 8,96,24
mvaa 76,24,104
v_add_f32_e32 v136,0xf3c6caff,v252
v_add_f32_e32 v137,0xf3cd1a95,v252
mvaa 96,24,108
v_add_f32_e32 v138,0xf3d36a2b,v252
v_add_f32_e32 v139,0xf3d9b9c2,v252
mvaa 100,28,108
v_min_f32_e32 v136,0xc1a00000,v136
v_min_f32_e32 v137,0xc1a00000,v137
mvaa 80,28,104
v_min_f32_e32 v138,0xc1a00000,v138
v_min_f32_e32 v139,0xc1a00000,v139
mvaa 84,32,104
dl 231,44,54,3072
mvaa 104,32,108
s_add_u32 m0,s61,0
s_add_u32 s54,s54,s58
mvaa 108,36,108
s_waitcnt vmcnt(16) lgkmcnt(0)
s_barrier
mvaa 88,36,104
ra 24,233,0
maaa 236,40,4
ra 28,233,2048
)"
R"(maaa 220,44,4
ta 96,236,57344
maaa 216,44,8
dl 224,36,52,0
maaa 232,40,8
ta 98,236,57856
maaa 228,40,12
ta 100,237,57344
maaa 212,44,12
ta 102,237,57856
maaa 208,44,16
ra 32,233,4096
maaa 224,40,16
ta 108,239,61440
maaa 192,48,16
dl 225,36,52,1024
maaa 176,52,16
ta 110,239,61952
maaa 180,52,12
ra 36,233,6144
v_add_u32_e32 v233,s99,v233
maaa 196,48,12
ta 104,238,61440
maaa 200,48,8
ta 106,238,61952
maaa 184,52,8
ta 44,235,49152
maaa 188,52,4
dl 226,36,52,2048
maaa 204,48,4
ta 46,235,49664
maaa 172,56,4
ta 40,234,49152
maaa 156,60,4
ta 42,234,49664
maaa 152,60,8
ta 52,237,49152
maaa 168,56,8
ta 54,237,49664
maaa 164,56,12
dl 227,36,52,3072
maaa 148,60,12
s_add_u32 m0,s62,32768
ta 48,236,49152
s_add_u32 s52,s52,s56
maaa 144,60,16
ta 50,236,49664
maaa 160,56,16
s_waitcnt vmcnt(12)
v_readfirstlane_b32 s87,v246
maaa 128,64,16
s_cmp_eq_u32 s12,0
s_cselect_b32 s89,s32,s87
ta 60,235,53248
maaa 112,68,16
s_cmp_lg_u32 s87,s89
s_cbranch_scc1 L_q_ack
L_q_acked:
ta 62,235,53760
s_and_b32 s87,s12,1
maaa 116,68,12
dl 228,40,53,0
maaa 132,64,12
s_lshl_b32 s88,s87,6
ta 56,234,53248
s_add_u32 s90,s90,s88
maaa 136,64,8
s_cmp_eq_u32 s90,0
s_cselect_b64 s[64:65],-1,0
ta 58,234,53760
s_lshl_b32 s88,s87,10
maaa 120,68,8
s_lshl_b32 s87,s87,14
s_add_u32 s33,s33,s88
s_add_u32 s91,s91,s87
maaa 124,68,4
s_lshr_b32 s87,s18,1
v_add_u32_e32 v251,s91,v244
s_lshl_b32 s87,s87,4
maaa 140,64,4
s_lshl_b32 s88,s18,11
s_lshr_b32 s91,s21,3
s_add_u32 s88,s33,s88
mvaa 40,72,4
buffer_store_dwordx4 v[140:143],v244,s[24:27],s67 offen offset:3072 sc0 sc1
mvaa 44,76,4
s_mov_b32 s67,12288
ta 68,237,53248
s_add_u32 s33,s88,63176704
mvaa 52,76,8
s_add_u32 s34,s88,63504384
ta 70,237,53760
s_add_u32 s50,s88,63832064
mvaa 48,72,8
s_add_u32 s51,s88,64159744
ta 64,236,53248
s_and_b32 s88,s20,4
mvaa 56,72,12
s_and_b32 s89,s20,3
ta 66,236,53760
s_add_u32 s91,s91,s88
mvaa 60,76,12
s_lshl_b32 s88,s89,6
v_add_f32_e32 v140,0xf415e232,v252
v_add_f32_e32 v141,0xf41909fd,v252
mvaa 68,76,16
dl 229,40,53,1024
mvaa 64,72,16
s_add_u32 s53,s53,s57
v_add_f32_e32 v142,0xf41c31c8,v252
v_min_f32_e32 v140,0xc1a00000,v140
mvaa 32,80,16
ta 76,235,57344
v_add_f32_e32 v143,0xf41f5993,v252
mvaa 36,84,16
ta 78,235,57856
v_bfe_i32 v252,v244,4,1
mvaa 28,84,12
ta 72,234,57344
v_min_f32_e32 v141,0xc1a00000,v141
mvaa 24,80,12
ta 74,234,57856
v_min_f32_e32 v142,0xc1a00000,v142
mvaa 16,80,8
dl 230,44,54,2048
mvaa 20,84,8
v_min_f32_e32 v143,0xc1a00000,v143
v_xor_b32_e32 v252,65535,v252
mvaa 12,84,4
v_lshrrev_b32_e32 v246,6,v244
v_lshl_add_u32 v245,v246,22,v245
mvaa 8,80,4
v_lshlrev_b32_e32 v246,16,v244
mvaa 76,4,88
mvaa 96,4,92
mvaa 100,8,92
dl 231,44,54,3072
mvaa 80,8,88
s_add_u32 m0,s61,4096
s_add_u32 s54,s54,s57
s_add_u32 s61,s63,s98
mvaa 84,12,88
s_add_u32 s63,s63,s99
mvaa 104,12,92
s_cmp_ge_u32 s63,65536
s_cselect_b32 s99,-2,1
s_lshl_b32 s99,s99,15
mvaa 108,16,92
mvaa 88,16,88
s_waitcnt lgkmcnt(0)
maaa 236,40,24
dl 224,36,52,0
maaa 220,44,24
maaa 216,44,28
maaa 232,40,28
s_waitcnt vmcnt(15)
s_barrier
maaa 228,40,32
ra 4,232,0
maaa 212,44,32
ra 8,232,2048
maaa 208,44,36
dl 225,36,52,1024
maaa 224,40,36
ta 80,236,8192
maaa 192,48,36
ta 82,236,8704
maaa 176,52,36
ta 84,237,8192
maaa 180,52,32
ta 86,237,8704
maaa 196,48,32
ra 12,232,4096
maaa 200,48,28
dl 226,36,52,2048
maaa 184,52,28
ta 92,239,12288
maaa 188,52,24
ra 16,232,6144
v_add_u32_e32 v232,s99,v232
maaa 204,48,24
ta 94,239,12800
maaa 172,56,24
ta 88,238,12288
maaa 156,60,24
ta 90,238,12800
maaa 152,60,28
buffer_store_dwordx4 v[144:147],v244,s[24:27],s67 offen offset:0 sc0 sc1
maaa 168,56,28
ta 44,235,0
maaa 164,56,32
ta 46,235,512
maaa 148,60,32
ta 40,234,0
maaa 144,60,36
ta 42,234,512
maaa 160,56,36
ta 52,237,0
maaa 128,64,36
dl 227,36,52,3072
maaa 112,68,36
s_add_u32 m0,s62,49152
ta 54,237,512
s_add_u32 s52,s52,s60
maaa 116,68,32
ta 48,236,0
maaa 132,64,32
ta 50,236,512
maaa 136,64,28
ta 60,235,4096
maaa 120,68,28
ta 62,235,4608
maaa 124,68,24
dl 228,40,53,0
maaa 140,64,24
ta 56,234,4096
mvaa 40,72,24
ta 58,234,4608
mvaa 44,76,24
ta 68,237,4096
mvaa 52,76,28
ta 70,237,4608
mvaa 48,72,28
ta 64,236,4096
mvaa 56,72,32
dl 229,40,53,1024
mvaa 60,76,32
s_add_u32 s53,s53,s58
ta 66,236,4608
mvaa 68,76,36
mvaa 64,72,36
mvaa 32,96,36
ta 76,235,8192
mvaa 36,100,36
ta 78,235,8704
mvaa 28,100,32
dl 230,44,54,2048
mvaa 24,96,32
ta 72,234,8192
mvaa 16,96,28
ta 74,234,8704
mvaa 20,100,28
mvaa 12,100,24
mvaa 8,96,24
mvaa 76,24,104
dl 231,44,54,3072
mvaa 96,24,108
s_add_u32 m0,s61,0
s_add_u32 s54,s54,s58
mvaa 100,28,108
mvaa 80,28,104
mvaa 84,32,104
mvaa 104,32,108
mvaa 108,36,108
buffer_store_dwordx4 v[148:151],v244,s[24:27],s67 offen offset:1024 sc0 sc1
mvaa 88,36,104
s_waitcnt lgkmcnt(0)
maaa 236,40,4
maaa 220,44,4
s_waitcnt vmcnt(15)
s_barrier
maaa 216,44,8
ra 24,233,0
maaa 232,40,8
ra 28,233,2048
maaa 228,40,12
s_cmp_eq_u32 s12,0
s_cselect_b64 exec,1,0
buffer_atomic_add v247,off,s[8:11],s85 sc1
s_mov_b64 exec,-1
maaa 212,44,12
ta 96,236,24576
v_mov_b32_e32 v247,32
maaa 208,44,16
ta 98,236,25088
maaa 224,40,16
ta 100,237,24576
maaa 192,48,16
ta 102,237,25088
maaa 176,52,16
ra 32,233,4096
maaa 180,52,12
dl 224,36,52,0
maaa 196,48,12
ta 108,239,28672
maaa 200,48,8
ta 110,239,29184
maaa 184,52,8
ra 36,233,6144
v_add_u32_e32 v233,s99,v233
maaa 188,52,4
ta 104,238,28672
maaa 204,48,4
ta 106,238,29184
maaa 172,56,4
dl 225,36,52,1024
maaa 156,60,4
ta 44,235,16384
maaa 152,60,8
ta 46,235,16896
maaa 168,56,8
ta 40,234,16384
maaa 164,56,12
ta 42,234,16896
maaa 148,60,12
ta 52,237,16384
maaa 144,60,16
dl 226,36,52,2048
maaa 160,56,16
ta 54,237,16896
maaa 128,64,16
ta 48,236,16384
maaa 112,68,16
ta 50,236,16896
maaa 116,68,12
ta 60,235,20480
maaa 132,64,12
ta 62,235,20992
maaa 136,64,8
dl 227,36,52,3072
maaa 120,68,8
s_add_u32 m0,s62,0
ta 56,234,20480
s_add_u32 s52,s52,s56
maaa 124,68,4
ta 58,234,20992
maaa 140,64,4
mvaa 40,72,4
ta 68,237,20480
mvaa 44,76,4
ta 70,237,20992
mvaa 52,76,8
dl 228,40,53,0
mvaa 48,72,8
ta 64,236,20480
mvaa 56,72,12
ta 66,236,20992
mvaa 60,76,12
mvaa 68,76,16
mvaa 64,72,16
mvaa 32,80,16
buffer_store_dwordx4 v[152:155],v244,s[24:27],s67 offen offset:2048 sc0 sc1
mvaa 36,84,16
ta 76,235,24576
mvaa 28,84,12
ta 78,235,25088
mvaa 24,80,12
ta 72,234,24576
mvaa 16,80,8
ta 74,234,25088
mvaa 20,84,8
mvaa 12,84,4
dl 229,40,53,1024
mvaa 8,80,4
s_add_u32 s53,s53,s57
mvaa 76,4,88
mvaa 96,4,92
mvaa 100,8,92
mvaa 80,8,88
mvaa 84,12,88
dl 230,44,54,2048
mvaa 104,12,92
mvaa 108,16,92
mvaa 88,16,88
s_waitcnt lgkmcnt(0)
maaa 236,40,24
maaa 220,44,24
maaa 216,44,28
dl 231,44,54,3072
maaa 232,40,28
s_add_u32 m0,s61,4096
s_add_u32 s54,s54,s57
s_add_u32 s61,s63,s98
maaa 228,40,32
s_add_u32 s63,s63,s99
maaa 212,44,32
s_waitcnt vmcnt(14)
s_barrier
maaa 208,44,36
s_cmp_ge_u32 s63,65536
s_cselect_b32 s99,-2,1
ra 4,232,0
s_lshl_b32 s99,s99,15
maaa 224,40,36
ra 8,232,2048
maaa 192,48,36
dl 224,36,52,0
maaa 176,52,36
ta 80,236,40960
maaa 180,52,32
ta 82,236,41472
maaa 196,48,32
ta 84,237,40960
maaa 200,48,28
ta 86,237,41472
maaa 184,52,28
ra 12,232,4096
maaa 188,52,24
dl 225,36,52,1024
maaa 204,48,24
ta 92,239,45056
maaa 172,56,24
ta 94,239,45568
maaa 156,60,24
ra 16,232,6144
v_add_u32_e32 v232,s99,v232
maaa 152,60,28
ta 88,238,45056
maaa 168,56,28
ta 90,238,45568
maaa 164,56,32
dl 226,36,52,2048
maaa 148,60,32
ta 44,235,32768
maaa 144,60,36
ta 46,235,33280
maaa 160,56,36
ta 40,234,32768
maaa 128,64,36
ta 42,234,33280
maaa 112,68,36
ta 52,237,32768
maaa 116,68,32
buffer_store_dwordx4 v[156:159],v244,s[24:27],s67 offen offset:3072 sc0 sc1
maaa 132,64,32
s_mov_b32 s67,16384
ta 54,237,33280
maaa 136,64,28
ta 48,236,32768
maaa 120,68,28
ta 50,236,33280
maaa 124,68,24
ta 60,235,36864
maaa 140,64,24
ta 62,235,37376
mvaa 40,72,24
dl 227,36,52,3072
mvaa 44,76,24
s_add_u32 m0,s62,16384
ta 56,234,36864
s_add_u32 s52,s52,s60
mvaa 52,76,28
ta 58,234,37376
mvaa 48,72,28
ta 68,237,36864
mvaa 56,72,32
ta 70,237,37376
mvaa 60,76,32
ta 64,236,36864
mvaa 68,76,36
dl 228,40,53,0
mvaa 64,72,36
ta 66,236,37376
mvaa 32,96,36
ta 76,235,40960
mvaa 36,100,36
ta 78,235,41472
mvaa 28,100,32
ta 72,234,40960
mvaa 24,96,32
ta 74,234,41472
mvaa 16,96,28
dl 229,40,53,1024
mvaa 20,100,28
s_add_u32 s53,s53,s58
mvaa 12,100,24
mvaa 8,96,24
mvaa 76,24,104
mvaa 96,24,108
mvaa 100,28,108
dl 230,44,54,2048
mvaa 80,28,104
mvaa 84,32,104
mvaa 104,32,108
mvaa 108,36,108
mvaa 88,36,104
s_waitcnt lgkmcnt(0)
maaa 236,40,4
dl 231,44,54,3072
maaa 220,44,4
s_add_u32 m0,s61,0
s_add_u32 s54,s54,s58
maaa 216,44,8
maaa 232,40,8
maaa 228,40,12
maaa 212,44,12
maaa 208,44,16
buffer_store_dwordx4 v[160:163],v244,s[24:27],s67 offen offset:0 sc0 sc1
maaa 224,40,16
maaa 192,48,16
maaa 176,52,16
maaa 180,52,12
s_waitcnt vmcnt(15)
s_barrier
maaa 196,48,12
dl 224,36,52,0
maaa 200,48,8
ra 24,233,0
maaa 184,52,8
ra 28,233,2048
maaa 188,52,4
ra 32,233,4096
maaa 204,48,4
ta 96,236,57344
maaa 172,56,4
dl 225,36,52,1024
maaa 156,60,4
ta 98,236,57856
maaa 152,60,8
ta 100,237,57344
maaa 168,56,8
ta 102,237,57856
maaa 164,56,12
ta 108,239,61440
maaa 148,60,12
ta 110,239,61952
maaa 144,60,16
dl 226,36,52,2048
maaa 160,56,16
ra 36,233,6144
v_add_u32_e32 v233,s99,v233
maaa 128,64,16
ta 104,238,61440
maaa 112,68,16
ta 106,238,61952
maaa 116,68,12
ta 44,235,49152
maaa 132,64,12
dl 227,36,52,3072
maaa 136,64,8
s_add_u32 m0,s62,32768
ta 46,235,49664
s_add_u32 s52,s52,s56
maaa 120,68,8
ta 40,234,49152
maaa 124,68,4
ta 42,234,49664
maaa 140,64,4
ta 52,237,49152
mvaa 40,72,4
dl 228,40,53,0
mvaa 44,76,4
ta 54,237,49664
mvaa 52,76,8
ta 48,236,49152
mvaa 48,72,8
ta 50,236,49664
mvaa 56,72,12
ta 60,235,53248
ta 62,235,53760
mvaa 60,76,12
dl 229,40,53,1024
mvaa 68,76,16
s_add_u32 s53,s53,s57
ta 56,234,53248
mvaa 64,72,16
ta 58,234,53760
mvaa 32,80,16
ta 68,237,53248
mvaa 36,84,16
ta 70,237,53760
ta 64,236,53248
mvaa 28,84,12
mvaa 24,80,12
dl 230,44,54,2048
ta 66,236,53760
mvaa 16,80,8
ta 76,235,57344
mvaa 20,84,8
ta 78,235,57856
mvaa 12,84,4
ta 72,234,57344
ta 74,234,57856
mvaa 8,80,4
mvaa 76,4,88
buffer_store_dwordx4 v[164:167],v244,s[24:27],s67 offen offset:1024 sc0 sc1
mvaa 96,4,92
mvaa 100,8,92
mvaa 80,8,88
mvaa 84,12,88
mvaa 104,12,92
dl 231,44,54,3072
mvaa 108,16,92
s_add_u32 m0,s61,4096
s_add_u32 s54,s54,s57
s_add_u32 s61,s63,s98
mvaa 88,16,88
s_add_u32 s63,s63,s99
s_waitcnt lgkmcnt(2)
maaa 236,40,24
s_cmp_ge_u32 s63,65536
s_cselect_b32 s99,-2,1
s_lshl_b32 s99,s99,15
maaa 220,44,24
maaa 216,44,28
dl 224,36,52,0
maaa 232,40,28
maaa 228,40,32
maaa 212,44,32
maaa 208,44,36
maaa 224,40,36
maaa 192,48,36
dl 225,36,52,1024
maaa 176,52,36
maaa 180,52,32
maaa 196,48,32
maaa 200,48,28
maaa 184,52,28
dl 226,36,52,2048
maaa 188,52,24
s_waitcnt vmcnt(17) lgkmcnt(0)
s_barrier
maaa 204,48,24
ra 4,232,0
maaa 172,56,24
ra 8,232,2048
maaa 156,60,24
ra 12,232,4096
maaa 152,60,28
dl 227,36,52,3072
maaa 168,56,28
s_add_u32 m0,s62,49152
ra 16,232,6144
s_add_u32 s52,s52,s60
maaa 164,56,32
ta 80,236,8192
v_add_u32_e32 v232,s99,v232
maaa 148,60,32
ta 82,236,8704
maaa 144,60,36
ta 84,237,8192
ta 86,237,8704
maaa 160,56,36
dl 228,40,53,0
maaa 128,64,36
ta 88,238,12288
maaa 112,68,36
ta 90,238,12800
maaa 116,68,32
ta 92,239,12288
maaa 132,64,32
ta 94,239,12800
ta 44,235,0
maaa 136,64,28
maaa 120,68,28
dl 229,40,53,1024
ta 46,235,512
maaa 124,68,24
s_add_u32 s53,s53,s58
ta 40,234,0
maaa 140,64,24
ta 42,234,512
mvaa 40,72,24
ta 52,237,0
mvaa 44,76,24
ta 54,237,512
ta 48,236,0
mvaa 52,76,28
buffer_store_dwordx4 v[168:171],v244,s[24:27],s67 offen offset:2048 sc0 sc1
mvaa 48,72,28
ta 50,236,512
mvaa 56,72,32
ta 60,235,4096
mvaa 60,76,32
ta 62,235,4608
mvaa 68,76,36
ta 56,234,4096
ta 58,234,4608
mvaa 64,72,36
dl 230,44,54,2048
mvaa 32,96,36
ta 68,237,4096
mvaa 36,100,36
ta 70,237,4608
mvaa 28,100,32
ta 64,236,4096
mvaa 24,96,32
ta 66,236,4608
ta 76,235,8192
mvaa 16,96,28
dl 231,44,54,3072
mvaa 20,100,28
s_add_u32 m0,s61,0
ta 78,235,8704
s_add_u32 s54,s54,s58
mvaa 12,100,24
ta 72,234,8192
ta 74,234,8704
mvaa 8,96,24
mvaa 76,24,104
mvaa 96,24,108
dl 224,36,52,0
mvaa 100,28,108
mvaa 80,28,104
mvaa 84,32,104
mvaa 104,32,108
mvaa 108,36,108
mvaa 88,36,104
dl 225,36,52,1024
s_waitcnt lgkmcnt(2)
maaa 236,40,4
maaa 220,44,4
maaa 216,44,8
maaa 232,40,8
maaa 228,40,12
dl 226,36,52,2048
maaa 212,44,12
maaa 208,44,16
maaa 224,40,16
maaa 192,48,16
s_waitcnt vmcnt(17) lgkmcnt(0)
s_barrier
maaa 176,52,16
dl 227,36,52,3072
maaa 180,52,12
s_add_u32 m0,s62,0
ra 24,233,0
s_add_u32 s52,s52,s56
maaa 196,48,12
ra 28,233,2048
maaa 200,48,8
ta 96,236,24576
maaa 184,52,8
ta 98,236,25088
maaa 188,52,4
buffer_store_dwordx4 v[172:175],v244,s[24:27],s67 offen offset:3072 sc0 sc1
maaa 204,48,4
s_mov_b32 s67,20480
ta 100,237,24576
maaa 172,56,4
ta 102,237,25088
maaa 156,60,4
ra 32,233,4096
maaa 152,60,8
ta 108,239,28672
maaa 168,56,8
ta 110,239,29184
maaa 164,56,12
dl 228,40,53,0
maaa 148,60,12
ra 36,233,6144
v_add_u32_e32 v233,s99,v233
maaa 144,60,16
ta 104,238,28672
maaa 160,56,16
ta 106,238,29184
maaa 128,64,16
ta 44,235,16384
maaa 112,68,16
s_mov_b64 exec,3
global_load_dword a250,v253,s[96:97]
s_mov_b64 exec,-1
maaa 116,68,12
ta 46,235,16896
maaa 132,64,12
ta 40,234,16384
maaa 136,64,8
ta 42,234,16896
maaa 120,68,8
ta 52,237,16384
maaa 124,68,4
dl 229,40,53,1024
maaa 140,64,4
s_add_u32 s53,s53,s57
ta 54,237,16896
mvaa 40,72,4
ta 48,236,16384
mvaa 44,76,4
ta 50,236,16896
mvaa 52,76,8
ta 60,235,20480
mvaa 48,72,8
dl 230,44,54,2048
mvaa 56,72,12
ta 62,235,20992
mvaa 60,76,12
ta 56,234,20480
mvaa 68,76,16
ta 58,234,20992
mvaa 64,72,16
ta 68,237,20480
ta 70,237,20992
mvaa 32,80,16
mvaa 36,84,16
dl 231,44,54,3072
ta 64,236,20480
mvaa 28,84,12
s_add_u32 m0,s61,4096
ta 66,236,20992
s_add_u32 s54,s54,s57
mvaa 24,80,12
s_add_u32 s61,s63,s98
ta 76,235,24576
s_add_u32 s63,s63,s99
mvaa 16,80,8
s_cmp_ge_u32 s63,65536
s_cselect_b32 s99,-2,1
ta 78,235,25088
s_lshl_b32 s99,s99,15
mvaa 20,84,8
ta 72,234,24576
ta 74,234,25088
mvaa 12,84,4
dl 224,36,52,0
mvaa 8,80,4
mvaa 76,4,88
mvaa 96,4,92
mvaa 100,8,92
mvaa 80,8,88
dl 225,36,52,1024
mvaa 84,12,88
mvaa 104,12,92
mvaa 108,16,92
mvaa 88,16,88
s_waitcnt vmcnt(17) lgkmcnt(0)
s_barrier
maaa 236,40,24
buffer_store_dwordx4 v[176:179],v244,s[24:27],s67 offen offset:0 sc0 sc1
maaa 220,44,24
ra 4,232,0
maaa 216,44,28
ra 8,232,2048
maaa 232,40,28
ta 80,236,40960
maaa 228,40,32
ta 82,236,41472
maaa 212,44,32
ta 84,237,40960
maaa 208,44,36
dl 226,36,52,2048
maaa 224,40,36
ta 86,237,41472
maaa 192,48,36
ra 12,232,4096
maaa 176,52,36
ta 92,239,45056
maaa 180,52,32
ta 94,239,45568
maaa 196,48,32
dl 227,36,52,3072
maaa 200,48,28
s_add_u32 m0,s62,16384
ra 16,232,6144
s_add_u32 s52,s52,s60
maaa 184,52,28
ta 88,238,45056
v_add_u32_e32 v232,s99,v232
maaa 188,52,24
ta 90,238,45568
maaa 204,48,24
ta 44,235,32768
maaa 172,56,24
dl 228,40,53,0
maaa 156,60,24
ta 46,235,33280
maaa 152,60,28
ta 40,234,32768
maaa 168,56,28
ta 42,234,33280
maaa 164,56,32
ta 52,237,32768
maaa 148,60,32
dl 229,40,53,1024
maaa 144,60,36
s_add_u32 s53,s53,s58
ta 54,237,33280
maaa 160,56,36
ta 48,236,32768
maaa 128,64,36
ta 50,236,33280
maaa 112,68,36
ta 60,235,36864
maaa 116,68,32
dl 230,44,54,2048
maaa 132,64,32
ta 62,235,37376
maaa 136,64,28
ta 56,234,36864
maaa 120,68,28
ta 58,234,37376
maaa 124,68,24
maaa 140,64,24
mvaa 40,72,24
dl 231,44,54,3072
mvaa 44,76,24
s_add_u32 m0,s61,0
ta 68,237,36864
s_add_u32 s54,s54,s58
mvaa 52,76,28
ta 70,237,37376
mvaa 48,72,28
ta 64,236,36864
mvaa 56,72,32
ta 66,236,37376
mvaa 60,76,32
dl 224,36,52,0
mvaa 68,76,36
mvaa 64,72,36
mvaa 32,96,36
ta 76,235,40960
mvaa 36,100,36
ta 78,235,41472
mvaa 28,100,32
buffer_store_dwordx4 v[180:183],v244,s[24:27],s67 offen offset:1024 sc0 sc1
mvaa 24,96,32
ta 72,234,40960
mvaa 16,96,28
ta 74,234,41472
mvaa 20,100,28
mvaa 12,100,24
mvaa 8,96,24
dl 225,36,52,1024
mvaa 76,24,104
mvaa 96,24,108
mvaa 100,28,108
mvaa 80,28,104
mvaa 84,32,104
mvaa 104,32,108
dl 226,36,52,2048
mvaa 108,36,108
s_waitcnt vmcnt(19) lgkmcnt(0)
s_barrier
mvaa 88,36,104
ra 24,233,0
maaa 236,40,4
ra 28,233,2048
maaa 220,44,4
ta 96,236,57344
maaa 216,44,8
dl 227,36,52,3072
maaa 232,40,8
s_add_u32 m0,s62,32768
ta 98,236,57856
s_add_u32 s52,s52,s56
maaa 228,40,12
ta 100,237,57344
maaa 212,44,12
ra 32,233,4096
maaa 208,44,16
ta 102,237,57856
maaa 224,40,16
dl 228,40,53,0
maaa 192,48,16
ta 108,239,61440
maaa 176,52,16
ta 110,239,61952
maaa 180,52,12
ra 36,233,6144
v_add_u32_e32 v233,s99,v233
maaa 196,48,12
ta 104,238,61440
maaa 200,48,8
dl 229,40,53,1024
maaa 184,52,8
s_add_u32 s53,s53,s57
ta 106,238,61952
maaa 188,52,4
ta 44,235,49152
maaa 204,48,4
ta 46,235,49664
maaa 172,56,4
ta 40,234,49152
maaa 156,60,4
ta 42,234,49664
maaa 152,60,8
dl 230,44,54,2048
maaa 168,56,8
ta 52,237,49152
maaa 164,56,12
ta 54,237,49664
maaa 148,60,12
ta 48,236,49152
maaa 144,60,16
ta 50,236,49664
maaa 160,56,16
buffer_store_dwordx4 v[184:187],v244,s[24:27],s67 offen offset:2048 sc0 sc1
maaa 128,64,16
ta 60,235,53248
maaa 112,68,16
ta 62,235,53760
maaa 116,68,12
ta 56,234,53248
maaa 132,64,12
ta 58,234,53760
maaa 136,64,8
dl 231,44,54,3072
maaa 120,68,8
s_add_u32 m0,s61,4096
s_add_u32 s54,s54,s57
s_add_u32 s61,s63,s98
maaa 124,68,4
s_add_u32 s63,s63,s99
maaa 140,64,4
s_cmp_ge_u32 s63,65536
s_cselect_b32 s99,-2,1
s_lshl_b32 s99,s99,15
ta 68,237,53248
mvaa 40,72,4
ta 70,237,53760
mvaa 44,76,4
)"
R"(dl 224,36,52,0
mvaa 52,76,8
ta 64,236,53248
mvaa 48,72,8
ta 66,236,53760
mvaa 56,72,12
mvaa 60,76,12
mvaa 68,76,16
mvaa 64,72,16
dl 225,36,52,1024
mvaa 32,80,16
ta 76,235,57344
ta 78,235,57856
mvaa 36,84,16
ta 72,234,57344
ta 74,234,57856
mvaa 28,84,12
mvaa 24,80,12
mvaa 16,80,8
dl 226,36,52,2048
mvaa 20,84,8
mvaa 12,84,4
mvaa 8,80,4
mvaa 76,4,88
mvaa 96,4,92
dl 227,36,52,3072
mvaa 100,8,92
s_add_u32 m0,s62,49152
s_add_u32 s52,s52,s60
mvaa 80,8,88
mvaa 84,12,88
mvaa 104,12,92
mvaa 108,16,92
s_waitcnt vmcnt(18) lgkmcnt(0)
s_barrier
mvaa 88,16,88
dl 228,40,53,0
maaa 236,40,24
ra 4,232,0
maaa 220,44,24
ra 8,232,2048
maaa 216,44,28
ta 80,236,8192
maaa 232,40,28
ta 82,236,8704
maaa 228,40,32
dl 229,40,53,1024
maaa 212,44,32
s_add_u32 s53,s53,s58
ta 84,237,8192
maaa 208,44,36
ta 86,237,8704
maaa 224,40,36
ra 12,232,4096
maaa 192,48,36
ta 92,239,12288
maaa 176,52,36
buffer_store_dwordx4 v[188:191],v244,s[24:27],s67 offen offset:3072 sc0 sc1
maaa 180,52,32
s_mov_b32 s67,24576
ta 94,239,12800
maaa 196,48,32
ra 16,232,6144
v_add_u32_e32 v232,s99,v232
maaa 200,48,28
ta 88,238,12288
maaa 184,52,28
ta 90,238,12800
maaa 188,52,24
dl 230,44,54,2048
maaa 204,48,24
ta 44,235,0
maaa 172,56,24
ta 46,235,512
maaa 156,60,24
ta 40,234,0
maaa 152,60,28
ta 42,234,512
maaa 168,56,28
dl 231,44,54,3072
maaa 164,56,32
s_add_u32 m0,s61,0
ta 52,237,0
s_add_u32 s54,s54,s58
maaa 148,60,32
ta 54,237,512
maaa 144,60,36
ta 48,236,0
maaa 160,56,36
ta 50,236,512
maaa 128,64,36
dl 224,36,52,0
maaa 112,68,36
ta 60,235,4096
maaa 116,68,32
ta 62,235,4608
maaa 132,64,32
ta 56,234,4096
maaa 136,64,28
ta 58,234,4608
maaa 120,68,28
maaa 124,68,24
dl 225,36,52,1024
maaa 140,64,24
mvaa 40,72,24
ta 68,237,4096
mvaa 44,76,24
ta 70,237,4608
mvaa 52,76,28
ta 64,236,4096
mvaa 48,72,28
dl 226,36,52,2048
mvaa 56,72,32
ta 66,236,4608
mvaa 60,76,32
mvaa 68,76,36
mvaa 64,72,36
mvaa 32,96,36
dl 227,36,52,3072
ta 76,235,8192
ta 78,235,8704
mvaa 36,100,36
s_add_u32 m0,s62,0
ta 72,234,8192
ta 74,234,8704
mvaa 28,100,32
s_add_u32 s52,s52,s56
mvaa 24,96,32
mvaa 16,96,28
mvaa 20,100,28
mvaa 12,100,24
buffer_store_dwordx4 v[192:195],v244,s[24:27],s67 offen offset:0 sc0 sc1
mvaa 8,96,24
mvaa 76,24,104
mvaa 96,24,108
mvaa 100,28,108
mvaa 80,28,104
mvaa 84,32,104
mvaa 104,32,108
mvaa 108,36,108
s_waitcnt vmcnt(19) lgkmcnt(0)
s_barrier
mvaa 88,36,104
dl 228,40,53,0
maaa 236,40,4
ra 24,233,0
maaa 220,44,4
ra 28,233,2048
maaa 216,44,8
ta 96,236,24576
maaa 232,40,8
ta 98,236,25088
maaa 228,40,12
ta 100,237,24576
v_accvgpr_read_b32 v0,a236
maaa 212,44,12
dl 229,40,53,1024
maaa 208,44,16
s_add_u32 s53,s53,s57
ta 102,237,25088
v_accvgpr_read_b32 v1,a237
maaa 224,40,16
ra 32,233,4096
v_accvgpr_read_b32 v2,a238
maaa 192,48,16
ta 108,239,28672
v_accvgpr_read_b32 v3,a239
maaa 176,52,16
ta 110,239,29184
v_accvgpr_read_b32 v4,a220
maaa 180,52,12
dl 230,44,54,2048
maaa 196,48,12
ra 36,233,6144
v_accvgpr_read_b32 v5,a221
maaa 200,48,8
ta 104,238,28672
v_accvgpr_read_b32 v6,a222
maaa 184,52,8
ta 106,238,29184
v_accvgpr_read_b32 v7,a223
maaa 188,52,4
ta 44,235,16384
v_add_u32_e32 v233,s99,v233
maaa 204,48,4
ta 46,235,16896
maaa 172,56,4
dl 231,44,54,3072
maaa 156,60,4
s_add_u32 m0,s61,4096
ta 40,234,16384
s_add_u32 s54,s54,s57
maaa 152,60,8
s_add_u32 s61,s63,s98
ta 42,234,16896
s_add_u32 s63,s63,s99
maaa 168,56,8
s_cmp_ge_u32 s63,65536
s_cselect_b32 s99,-2,1
ta 52,237,16384
s_lshl_b32 s99,s99,15
maaa 164,56,12
ta 54,237,16896
maaa 148,60,12
dl 224,36,52,0
maaa 144,60,16
ta 48,236,16384
maaa 160,56,16
ta 50,236,16896
maaa 128,64,16
ta 60,235,20480
maaa 112,68,16
ta 62,235,20992
maaa 116,68,12
dl 225,36,52,1024
maaa 132,64,12
ta 56,234,20480
maaa 136,64,8
ta 58,234,20992
maaa 120,68,8
maaa 124,68,4
maaa 140,64,4
buffer_store_dwordx4 v[196:199],v244,s[24:27],s67 offen offset:1024 sc0 sc1
mvaa 40,72,4
ta 68,237,20480
mvaa 44,76,4
ta 70,237,20992
mvaa 52,76,8
ta 64,236,20480
mvaa 48,72,8
ta 66,236,20992
mvaa 56,72,12
dl 226,36,52,2048
mvaa 60,76,12
mvaa 68,76,16
mvaa 64,72,16
mvaa 32,80,16
ta 76,235,24576
ta 78,235,25088
mvaa 36,84,16
ta 72,234,24576
ta 74,234,25088
mvaa 28,84,12
dl 227,36,52,3072
mvaa 24,80,12
s_add_u32 m0,s62,16384
s_add_u32 s52,s52,s60
mvaa 16,80,8
mvaa 20,84,8
mvaa 12,84,4
mvaa 8,80,4
mvaa 76,4,88
mvaa 96,4,92
mvaa 100,8,92
mvaa 80,8,88
mvaa 84,12,88
mvaa 104,12,92
mvaa 108,16,92
s_waitcnt vmcnt(19) lgkmcnt(0)
s_barrier
mvaa 88,16,88
dl 228,40,53,0
mvaa 0,40,24
ra 4,232,0
mvaa 4,44,24
ra 8,232,2048
maaa 216,44,28
ta 80,236,40960
maaa 232,40,28
ta 82,236,41472
maaa 228,40,32
dl 229,40,53,1024
maaa 212,44,32
s_add_u32 s53,s53,s58
ta 84,237,40960
v_accvgpr_read_b32 v92,a216
maaa 208,44,36
ra 12,232,4096
v_accvgpr_read_b32 v93,a217
maaa 224,40,36
ta 86,237,41472
v_accvgpr_read_b32 v94,a218
maaa 192,48,36
ta 92,239,45056
v_accvgpr_read_b32 v95,a219
maaa 176,52,36
dl 230,44,54,2048
maaa 180,52,32
ta 94,239,45568
v_accvgpr_read_b32 v72,a232
maaa 196,48,32
ra 16,232,6144
v_accvgpr_read_b32 v73,a233
maaa 200,48,28
ta 88,238,45056
v_accvgpr_read_b32 v74,a234
maaa 184,52,28
ta 90,238,45568
v_accvgpr_read_b32 v75,a235
maaa 188,52,24
ta 44,235,32768
v_add_u32_e32 v232,s99,v232
maaa 204,48,24
buffer_store_dwordx4 v[200:203],v244,s[24:27],s67 offen offset:2048 sc0 sc1
maaa 172,56,24
ta 46,235,33280
maaa 156,60,24
ta 40,234,32768
maaa 152,60,28
ta 42,234,33280
maaa 168,56,28
ta 52,237,32768
maaa 164,56,32
dl 231,44,54,3072
maaa 148,60,32
s_add_u32 m0,s61,0
ta 54,237,33280
s_add_u32 s54,s54,s58
maaa 144,60,36
ta 48,236,32768
maaa 160,56,36
ta 50,236,33280
maaa 128,64,36
ta 60,235,36864
maaa 112,68,36
dl 224,36,52,0
maaa 116,68,32
ta 62,235,37376
maaa 132,64,32
ta 56,234,36864
maaa 136,64,28
ta 58,234,37376
maaa 120,68,28
maaa 124,68,24
dl 225,36,52,1024
maaa 140,64,24
mvaa 40,72,24
ta 68,237,36864
mvaa 44,76,24
ta 70,237,37376
mvaa 52,76,28
ta 64,236,36864
mvaa 48,72,28
dl 226,36,52,2048
mvaa 56,72,32
ta 66,236,37376
mvaa 60,76,32
mvaa 68,76,36
mvaa 64,72,36
mvaa 32,96,36
ta 76,235,40960
mvaa 36,100,36
dl 227,36,52,3072
mvaa 28,100,32
s_add_u32 m0,s62,32768
ta 78,235,41472
s_add_u32 s52,s52,s56
mvaa 24,96,32
ta 72,234,40960
mvaa 16,96,28
ta 74,234,41472
mvaa 20,100,28
mvaa 12,100,24
mvaa 8,96,24
mvaa 76,24,104
mvaa 96,24,108
mvaa 100,28,108
mvaa 80,28,104
mvaa 84,32,104
mvaa 104,32,108
mvaa 108,36,108
buffer_store_dwordx4 v[204:207],v244,s[24:27],s67 offen offset:3072 sc0 sc1
mvaa 88,36,104
s_waitcnt vmcnt(20) lgkmcnt(0)
s_barrier
mvaa 0,40,4
s_mov_b32 s67,28672
ra 24,233,0
mvaa 4,44,4
ra 28,233,2048
mvaa 92,44,8
ta 96,236,57344
mvaa 72,40,8
dl 228,40,53,0
maaa 228,40,12
ta 98,236,57856
maaa 212,44,12
ta 100,237,57344
maaa 208,44,16
ta 102,237,57856
maaa 224,40,16
ra 32,233,4096
maaa 192,48,16
dl 229,40,53,1024
maaa 176,52,16
s_add_u32 s53,s53,s57
ta 108,239,61440
v_accvgpr_read_b32 v144,a228
maaa 180,52,12
ta 110,239,61952
v_accvgpr_read_b32 v145,a229
maaa 196,48,12
ra 36,233,6144
v_accvgpr_read_b32 v146,a230
maaa 200,48,8
ta 104,238,61440
v_accvgpr_read_b32 v147,a231
maaa 184,52,8
ta 106,238,61952
v_accvgpr_read_b32 v148,a212
maaa 188,52,4
dl 230,44,54,2048
maaa 204,48,4
ta 44,235,49152
v_accvgpr_read_b32 v149,a213
maaa 172,56,4
ta 46,235,49664
v_accvgpr_read_b32 v150,a214
maaa 156,60,4
ta 40,234,49152
v_accvgpr_read_b32 v151,a215
maaa 152,60,8
ta 42,234,49664
v_add_u32_e32 v233,s99,v233
maaa 168,56,8
dl 231,44,54,3072
maaa 164,56,12
s_add_u32 m0,s61,4096
ta 52,237,49152
s_add_u32 s54,s54,s57
maaa 148,60,12
s_add_u32 s61,s63,s98
ta 54,237,49664
s_add_u32 s63,s63,s99
maaa 144,60,16
s_cmp_ge_u32 s63,65536
s_cselect_b32 s99,-2,1
ta 48,236,49152
s_lshl_b32 s99,s99,15
maaa 160,56,16
ta 50,236,49664
maaa 128,64,16
dl 224,36,52,0
maaa 112,68,16
ta 60,235,53248
maaa 116,68,12
ta 62,235,53760
maaa 132,64,12
ta 56,234,53248
maaa 136,64,8
ta 58,234,53760
maaa 120,68,8
dl 225,36,52,1024
maaa 124,68,4
maaa 140,64,4
mvaa 40,72,4
ta 68,237,53248
mvaa 44,76,4
ta 70,237,53760
mvaa 52,76,8
dl 226,36,52,2048
mvaa 48,72,8
ta 64,236,53248
mvaa 56,72,12
ta 66,236,53760
mvaa 60,76,12
mvaa 68,76,16
mvaa 64,72,16
mvaa 32,80,16
buffer_store_dwordx4 v[208:211],v244,s[24:27],s67 offen offset:0 sc0 sc1
ta 76,235,57344
ta 78,235,57856
mvaa 36,84,16
ta 72,234,57344
ta 74,234,57856
mvaa 28,84,12
mvaa 24,80,12
mvaa 16,80,8
mvaa 20,84,8
dl 227,36,52,3072
mvaa 12,84,4
s_add_u32 m0,s62,49152
s_add_u32 s52,s52,s60
mvaa 8,80,4
mvaa 76,4,88
mvaa 96,4,92
mvaa 100,8,92
mvaa 80,8,88
mvaa 84,12,88
mvaa 104,12,92
mvaa 108,16,92
s_waitcnt vmcnt(19) lgkmcnt(0)
s_barrier
mvaa 88,16,88
dl 228,40,53,0
mvaa 0,40,24
ra 4,232,0
mvaa 4,44,24
ra 8,232,2048
mvaa 92,44,28
ta 80,236,8192
mvaa 72,40,28
ta 82,236,8704
mvaa 144,40,32
ta 84,237,8192
mvaa 148,44,32
dl 229,40,53,1024
maaa 208,44,36
s_add_u32 s53,s53,s58
ta 86,237,8704
maaa 224,40,36
ra 12,232,4096
maaa 192,48,36
ta 92,239,12288
maaa 176,52,36
ra 16,232,6144
v_accvgpr_read_b32 v156,a208
maaa 180,52,32
dl 230,44,54,2048
maaa 196,48,32
ta 94,239,12800
v_accvgpr_read_b32 v157,a209
maaa 200,48,28
ta 88,238,12288
v_accvgpr_read_b32 v158,a210
maaa 184,52,28
ta 90,238,12800
v_accvgpr_read_b32 v159,a211
maaa 188,52,24
ta 44,235,0
v_accvgpr_read_b32 v152,a224
maaa 204,48,24
dl 231,44,54,3072
maaa 172,56,24
s_add_u32 m0,s61,0
ta 46,235,512
v_accvgpr_read_b32 v153,a225
maaa 156,60,24
s_add_u32 s54,s54,s58
ta 40,234,0
v_accvgpr_read_b32 v154,a226
maaa 152,60,28
ta 42,234,512
v_accvgpr_read_b32 v155,a227
maaa 168,56,28
ta 52,237,0
v_add_u32_e32 v232,s99,v232
maaa 164,56,32
dl 224,36,52,0
maaa 148,60,32
ta 54,237,512
maaa 144,60,36
ta 48,236,0
maaa 160,56,36
ta 50,236,512
maaa 128,64,36
ta 60,235,4096
maaa 112,68,36
ta 62,235,4608
maaa 116,68,32
buffer_store_dwordx4 v[212:215],v244,s[24:27],s67 offen offset:1024 sc0 sc1
maaa 132,64,32
ta 56,234,4096
maaa 136,64,28
ta 58,234,4608
maaa 120,68,28
maaa 124,68,24
maaa 140,64,24
dl 225,36,52,1024
mvaa 40,72,24
ta 68,237,4096
mvaa 44,76,24
ta 70,237,4608
mvaa 52,76,28
ta 64,236,4096
mvaa 48,72,28
ta 66,236,4608
mvaa 56,72,32
dl 226,36,52,2048
mvaa 60,76,32
mvaa 68,76,36
mvaa 64,72,36
mvaa 32,96,36
ta 76,235,8192
ta 78,235,8704
mvaa 36,100,36
dl 227,36,52,3072
ta 72,234,8192
ta 74,234,8704
mvaa 28,100,32
s_add_u32 m0,s62,0
s_add_u32 s52,s52,s56
mvaa 24,96,32
mvaa 16,96,28
mvaa 20,100,28
mvaa 12,100,24
mvaa 8,96,24
mvaa 76,24,104
mvaa 96,24,108
mvaa 100,28,108
mvaa 80,28,104
mvaa 84,32,104
mvaa 104,32,108
mvaa 108,36,108
s_waitcnt vmcnt(19) lgkmcnt(0)
s_barrier
mvaa 88,36,104
dl 228,40,53,0
mvaa 0,40,4
ra 24,233,0
mvaa 4,44,4
ra 28,233,2048
mvaa 92,44,8
ta 96,236,24576
mvaa 72,40,8
ta 98,236,25088
mvaa 144,40,12
dl 229,40,53,1024
mvaa 148,44,12
s_add_u32 s53,s53,s57
ta 100,237,24576
mvaa 156,44,16
ta 102,237,25088
mvaa 152,40,16
ra 32,233,4096
maaa 192,48,16
ta 108,239,28672
maaa 176,52,16
buffer_store_dwordx4 v[216:219],v244,s[24:27],s67 offen offset:2048 sc0 sc1
maaa 180,52,12
ta 110,239,29184
maaa 196,48,12
ra 36,233,6144
v_add_u32_e32 v233,s99,v233
maaa 200,48,8
ta 104,238,28672
v_accvgpr_read_b32 v160,a192
maaa 184,52,8
ta 106,238,29184
v_accvgpr_read_b32 v161,a193
maaa 188,52,4
dl 230,44,54,2048
maaa 204,48,4
ta 44,235,16384
v_accvgpr_read_b32 v162,a194
maaa 172,56,4
ta 46,235,16896
v_accvgpr_read_b32 v163,a195
maaa 156,60,4
ta 40,234,16384
v_accvgpr_read_b32 v164,a176
maaa 152,60,8
ta 42,234,16896
v_accvgpr_read_b32 v165,a177
maaa 168,56,8
dl 231,44,54,3072
maaa 164,56,12
s_add_u32 m0,s61,4096
ta 52,237,16384
v_accvgpr_read_b32 v166,a178
maaa 148,60,12
s_add_u32 s54,s54,s57
ta 54,237,16896
v_accvgpr_read_b32 v167,a179
maaa 144,60,16
s_add_u32 s61,s63,s98
ta 48,236,16384
s_add_u32 s63,s63,s99
maaa 160,56,16
s_cmp_ge_u32 s63,65536
s_cselect_b32 s99,-2,1
ta 50,236,16896
s_lshl_b32 s99,s99,15
maaa 128,64,16
ta 60,235,20480
maaa 112,68,16
dl 224,36,52,0
maaa 116,68,12
ta 62,235,20992
maaa 132,64,12
ta 56,234,20480
maaa 136,64,8
ta 58,234,20992
maaa 120,68,8
maaa 124,68,4
dl 225,36,52,1024
maaa 140,64,4
mvaa 40,72,4
ta 68,237,20480
mvaa 44,76,4
ta 70,237,20992
mvaa 52,76,8
ta 64,236,20480
mvaa 48,72,8
dl 226,36,52,2048
mvaa 56,72,12
ta 66,236,20992
mvaa 60,76,12
mvaa 68,76,16
mvaa 64,72,16
mvaa 32,80,16
dl 227,36,52,3072
ta 76,235,24576
ta 78,235,25088
mvaa 36,84,16
s_add_u32 m0,s62,16384
ta 72,234,24576
ta 74,234,25088
mvaa 28,84,12
s_add_u32 s52,s52,s60
mvaa 24,80,12
mvaa 16,80,8
mvaa 20,84,8
mvaa 12,84,4
buffer_store_dwordx4 v[220:223],v244,s[24:27],s67 offen offset:3072 sc0 sc1
mvaa 8,80,4
s_mov_b32 s67,0x5550055
s_mov_b32 s26,2097152
v_cndmask_b32_e64 v116,v241,0,s[66:67]
mvaa 76,4,88
s_mov_b32 s66,0xa0000
s_mov_b32 s67,0xaaa00aa
v_cndmask_b32_e64 v117,v241,0,s[66:67]
mvaa 96,4,92
s_mov_b32 s66,0x150001
v_cndmask_b32_e64 v120,v116,v241,s[64:65]
s_mov_b32 s67,0x15550155
mvaa 100,8,92
s_mov_b32 s27,131072
v_cndmask_b32_e64 v118,v241,0,s[66:67]
v_cndmask_b32_e64 v121,v117,v241,s[64:65]
mvaa 80,8,88
s_mov_b32 s66,0x2a0002
s_mov_b32 s67,0x2aaa02aa
v_cndmask_b32_e64 v119,v241,0,s[66:67]
mvaa 84,12,88
s_lshr_b32 s66,s18,2
v_cndmask_b32_e64 v122,v118,v241,s[64:65]
v_cndmask_b32_e64 v123,v119,v241,s[64:65]
mvaa 104,12,92
s_lshl_b32 s66,s66,13
v_cmp_eq_u32_e64 s[64:65],s87,v250
v_lshlrev_b32_e32 v250,3,v250
mvaa 108,16,92
s_waitcnt vmcnt(19) lgkmcnt(0)
s_barrier
mvaa 88,16,88
dl 228,40,53,0
mvaa 0,40,24
s_lshl_b32 s67,s18,13
ra 4,232,0
v_add_u32_e32 v250,83894272,v250
mvaa 4,44,24
s_mov_b64 s[24:25],s[82:83]
ra 8,232,2048
v_cndmask_b32_e64 v242,31,-1,s[64:65]
mvaa 92,44,28
s_cmp_eq_u32 s12,0
s_cselect_b32 s82,0,0
ta 80,236,40960
v_and_b32_e32 v243,528,v242
mvaa 72,40,28
s_or_b32 s82,s82,s15
ta 82,236,41472
s_mov_b32 s83,0
mvaa 144,40,32
s_or_b32 s82,s82,16384
ta 84,237,40960
mvaa 148,44,32
dl 229,40,53,1024
mvaa 156,44,36
s_add_u32 s53,s53,s58
ta 86,237,41472
mvaa 152,40,36
ra 12,232,4096
mvaa 160,48,36
ta 92,239,45056
mvaa 164,52,36
ta 94,239,45568
maaa 180,52,32
dl 230,44,54,2048
maaa 196,48,32
ra 16,232,6144
v_add_u32_e32 v232,s99,v232
maaa 200,48,28
ta 88,238,45056
maaa 184,52,28
ta 90,238,45568
v_accvgpr_read_b32 v172,a180
maaa 188,52,24
ta 44,235,32768
v_accvgpr_read_b32 v173,a181
maaa 204,48,24
ta 46,235,33280
v_accvgpr_read_b32 v174,a182
maaa 172,56,24
dl 231,44,54,3072
maaa 156,60,24
s_add_u32 m0,s61,0
ta 40,234,32768
v_accvgpr_read_b32 v175,a183
maaa 152,60,28
s_add_u32 s54,s54,s58
ta 42,234,33280
v_accvgpr_read_b32 v168,a196
maaa 168,56,28
ta 52,237,32768
v_accvgpr_read_b32 v169,a197
maaa 164,56,32
ta 54,237,33280
v_accvgpr_read_b32 v170,a198
maaa 148,60,32
dl 224,36,52,0
maaa 144,60,36
ta 48,236,32768
v_accvgpr_read_b32 v171,a199
maaa 160,56,36
ta 50,236,33280
maaa 128,64,36
ta 60,235,36864
maaa 112,68,36
ta 62,235,37376
maaa 116,68,32
dl 225,36,52,1024
maaa 132,64,32
ta 56,234,36864
maaa 136,64,28
ta 58,234,37376
maaa 120,68,28
maaa 124,68,24
maaa 140,64,24
dl 226,36,52,2048
mvaa 40,72,24
ta 68,237,36864
mvaa 44,76,24
ta 70,237,37376
mvaa 52,76,28
ta 64,236,36864
mvaa 48,72,28
ta 66,236,37376
mvaa 56,72,32
mvaa 60,76,32
dl 227,36,52,3072
mvaa 68,76,36
s_add_u32 m0,s62,32768
s_add_u32 s52,s52,s56
mvaa 64,72,36
mvaa 32,96,36
ta 76,235,40960
ta 78,235,41472
mvaa 36,100,36
ta 72,234,40960
ta 74,234,41472
mvaa 28,100,32
mvaa 24,96,32
mvaa 16,96,28
mvaa 20,100,28
mvaa 12,100,24
mvaa 8,96,24
mvaa 76,24,104
mvaa 96,24,108
mvaa 100,28,108
mvaa 80,28,104
mvaa 84,32,104
mvaa 104,32,108
mvaa 108,36,108
s_waitcnt vmcnt(18) lgkmcnt(0)
s_barrier
mvaa 88,36,104
dl 228,40,53,0
mvaa 0,40,4
ra 24,233,0
mvaa 4,44,4
ra 28,233,2048
mvaa 92,44,8
ta 96,236,57344
mvaa 72,40,8
ta 98,236,57856
mvaa 144,40,12
dl 229,40,53,1024
mvaa 148,44,12
s_add_u32 s53,s53,s57
ta 100,237,57344
mvaa 156,44,16
ta 102,237,57856
mvaa 152,40,16
ra 32,233,4096
mvaa 160,48,16
ta 108,239,61440
mvaa 164,52,16
dl 230,44,54,2048
mvaa 172,52,12
ra 36,233,6144
v_add_u32_e32 v233,s99,v233
mvaa 168,48,12
ta 110,239,61952
maaa 200,48,8
ta 104,238,61440
maaa 184,52,8
ta 106,238,61952
maaa 188,52,4
ta 44,235,49152
maaa 204,48,4
dl 231,44,54,3072
maaa 172,56,4
s_add_u32 m0,s61,4096
ta 46,235,49664
v_accvgpr_read_b32 v176,a200
maaa 156,60,4
s_add_u32 s54,s54,s57
ta 40,234,49152
v_accvgpr_read_b32 v177,a201
maaa 152,60,8
s_add_u32 s61,s63,s98
ta 42,234,49664
v_accvgpr_read_b32 v178,a202
maaa 168,56,8
s_add_u32 s63,s63,s99
ta 52,237,49152
v_accvgpr_read_b32 v179,a203
maaa 164,56,12
s_mov_b64 exec,3
global_load_dword a250,v253,s[96:97]
s_mov_b64 exec,-1
maaa 148,60,12
s_cmp_ge_u32 s63,65536
s_cselect_b32 s99,-2,1
ta 54,237,49664
v_accvgpr_read_b32 v180,a184
maaa 144,60,16
s_lshl_b32 s99,s99,15
ta 48,236,49152
v_accvgpr_read_b32 v181,a185
maaa 160,56,16
ta 50,236,49664
v_accvgpr_read_b32 v182,a186
maaa 128,64,16
ta 60,235,53248
v_accvgpr_read_b32 v183,a187
maaa 112,68,16
dl 224,36,52,0
maaa 116,68,12
ta 62,235,53760
v_add_u32_e32 v253,24576,v253
maaa 132,64,12
ta 56,234,53248
maaa 136,64,8
ta 58,234,53760
maaa 120,68,8
maaa 124,68,4
dl 225,36,52,1024
maaa 140,64,4
mvaa 40,72,4
ta 68,237,53248
mvaa 44,76,4
ta 70,237,53760
mvaa 52,76,8
ta 64,236,53248
mvaa 48,72,8
ta 66,236,53760
mvaa 56,72,12
dl 226,36,52,2048
mvaa 60,76,12
mvaa 68,76,16
mvaa 64,72,16
mvaa 32,80,16
ta 76,235,57344
ta 78,235,57856
mvaa 36,84,16
dl 227,36,52,3072
ta 72,234,57344
ta 74,234,57856
mvaa 28,84,12
s_add_u32 m0,s62,49152
s_add_u32 s52,s52,s60
mvaa 24,80,12
mvaa 16,80,8
mvaa 20,84,8
mvaa 12,84,4
mvaa 8,80,4
mvaa 76,4,88
mvaa 96,4,92
mvaa 100,8,92
mvaa 80,8,88
mvaa 84,12,88
mvaa 104,12,92
mvaa 108,16,92
s_waitcnt vmcnt(17) lgkmcnt(0)
s_barrier
mvaa 88,16,88
s_cmp_eq_u32 s12,0
s_cselect_b64 exec,1,0
buffer_atomic_add v247,off,s[8:11],s85 sc1
s_mov_b64 exec,-1
mvaa 0,40,24
ra 4,232,0
v_bfe_u32 v247,v244,2,6
mvaa 4,44,24
ra 8,232,2048
mvaa 92,44,28
)"
R"(ta 80,236,8192
mvaa 72,40,28
ta 82,236,8704
mvaa 144,40,32
dl 228,40,53,0
mvaa 148,44,32
ta 84,237,8192
mvaa 156,44,36
ta 86,237,8704
mvaa 152,40,36
ra 12,232,4096
mvaa 160,48,36
ta 92,239,12288
mvaa 164,52,36
dl 229,40,53,1024
mvaa 172,52,32
s_add_u32 s53,s53,s58
ra 16,232,6144
v_add_u32_e32 v232,s99,v232
mvaa 168,48,32
ta 94,239,12800
mvaa 176,48,28
ta 88,238,12288
mvaa 180,52,28
ta 90,238,12800
maaa 188,52,24
dl 230,44,54,2048
maaa 204,48,24
ta 44,235,0
maaa 172,56,24
ta 46,235,512
maaa 156,60,24
ta 40,234,0
maaa 152,60,28
ta 42,234,512
v_accvgpr_read_b32 v188,a188
maaa 168,56,28
dl 231,44,54,3072
maaa 164,56,32
s_add_u32 m0,s61,0
ta 52,237,0
v_accvgpr_read_b32 v189,a189
maaa 148,60,32
s_add_u32 s54,s54,s58
ta 54,237,512
v_accvgpr_read_b32 v190,a190
maaa 144,60,36
ta 48,236,0
v_accvgpr_read_b32 v191,a191
maaa 160,56,36
ta 50,236,512
v_accvgpr_read_b32 v184,a204
maaa 128,64,36
dl 224,36,52,0
maaa 112,68,36
ta 60,235,4096
v_accvgpr_read_b32 v185,a205
maaa 116,68,32
ta 62,235,4608
v_accvgpr_read_b32 v186,a206
maaa 132,64,32
ta 56,234,4096
v_accvgpr_read_b32 v187,a207
maaa 136,64,28
ta 58,234,4608
maaa 120,68,28
dl 225,36,52,1024
maaa 124,68,24
maaa 140,64,24
mvaa 40,72,24
ta 68,237,4096
mvaa 44,76,24
ta 70,237,4608
mvaa 52,76,28
dl 226,36,52,2048
mvaa 48,72,28
ta 64,236,4096
mvaa 56,72,32
ta 66,236,4608
mvaa 60,76,32
mvaa 68,76,36
mvaa 64,72,36
dl 227,36,52,3072
mvaa 32,96,36
s_add_u32 m0,s62,0
ta 76,235,8192
ta 78,235,8704
mvaa 36,100,36
s_add_u32 s52,s52,s56
ta 72,234,8192
ta 74,234,8704
mvaa 28,100,32
mvaa 24,96,32
mvaa 16,96,28
s_waitcnt vmcnt(12)
s_mov_b64 exec,3
global_load_dword a250,v253,s[96:97]
s_mov_b64 exec,-1
mvaa 20,100,28
v_lshrrev_b32_e32 v253,1,v244
mvaa 12,100,24
mvaa 8,96,24
mvaa 76,24,104
mvaa 96,24,108
mvaa 100,28,108
mvaa 80,28,104
mvaa 84,32,104
mvaa 104,32,108
mvaa 108,36,108
s_waitcnt lgkmcnt(0)
s_barrier
mvaa 88,36,104
dl 228,40,53,0
mvaa 0,40,4
ra 24,233,0
mvaa 4,44,4
ra 28,233,2048
mvaa 92,44,8
ta 96,236,24576
mvaa 72,40,8
ta 98,236,25088
mvaa 144,40,12
dl 229,40,53,1024
mvaa 148,44,12
s_add_u32 s53,s53,s57
ta 100,237,24576
mvaa 156,44,16
ta 102,237,25088
mvaa 152,40,16
ra 32,233,4096
mvaa 160,48,16
ta 108,239,28672
mvaa 164,52,16
dl 230,44,54,2048
mvaa 172,52,12
ta 110,239,29184
mvaa 168,48,12
ra 36,233,6144
v_add_u32_e32 v233,s99,v233
mvaa 176,48,8
ta 104,238,28672
mvaa 180,52,8
ta 106,238,29184
mvaa 188,52,4
dl 231,44,54,3072
mvaa 184,48,4
s_add_u32 m0,s61,4096
ta 44,235,16384
s_add_u32 s54,s54,s57
maaa 172,56,4
s_add_u32 s61,s63,s98
ta 46,235,16896
s_add_u32 s63,s63,s99
maaa 156,60,4
s_cmp_ge_u32 s63,65536
s_cselect_b32 s99,-2,1
ta 40,234,16384
s_lshl_b32 s99,s99,15
maaa 152,60,8
ta 42,234,16896
v_accvgpr_read_b32 v192,a172
maaa 168,56,8
dl 224,36,52,0
maaa 164,56,12
ta 52,237,16384
v_accvgpr_read_b32 v193,a173
maaa 148,60,12
ta 54,237,16896
v_accvgpr_read_b32 v194,a174
maaa 144,60,16
ta 48,236,16384
v_accvgpr_read_b32 v195,a175
maaa 160,56,16
ta 50,236,16896
v_accvgpr_read_b32 v196,a156
maaa 128,64,16
dl 225,36,52,1024
maaa 112,68,16
ta 60,235,20480
v_accvgpr_read_b32 v197,a157
maaa 116,68,12
ta 62,235,20992
v_accvgpr_read_b32 v198,a158
maaa 132,64,12
ta 56,234,20480
v_accvgpr_read_b32 v199,a159
maaa 136,64,8
ta 58,234,20992
maaa 120,68,8
dl 226,36,52,2048
maaa 124,68,4
maaa 140,64,4
mvaa 40,72,4
ta 68,237,20480
mvaa 44,76,4
ta 70,237,20992
mvaa 52,76,8
dl 227,36,52,3072
mvaa 48,72,8
s_add_u32 m0,s62,16384
ta 64,236,20480
s_add_u32 s52,s52,s60
mvaa 56,72,12
ta 66,236,20992
mvaa 60,76,12
mvaa 68,76,16
mvaa 64,72,16
mvaa 32,80,16
ta 76,235,24576
ta 78,235,25088
mvaa 36,84,16
ta 72,234,24576
ta 74,234,25088
mvaa 28,84,12
mvaa 24,80,12
mvaa 16,80,8
mvaa 20,84,8
mvaa 12,84,4
mvaa 8,80,4
mvaa 76,4,88
mvaa 96,4,92
mvaa 100,8,92
mvaa 80,8,88
mvaa 84,12,88
mvaa 104,12,92
mvaa 108,16,92
s_waitcnt vmcnt(17) lgkmcnt(0)
s_barrier
mvaa 88,16,88
dl 228,40,53,0
mvaa 0,40,24
ra 4,232,0
mvaa 4,44,24
ra 8,232,2048
mvaa 92,44,28
ta 80,236,40960
mvaa 72,40,28
ta 82,236,41472
mvaa 144,40,32
dl 229,40,53,1024
mvaa 148,44,32
s_add_u32 s53,s53,s58
ta 84,237,40960
mvaa 156,44,36
ta 86,237,41472
mvaa 152,40,36
ra 12,232,4096
mvaa 160,48,36
ta 92,239,45056
mvaa 164,52,36
dl 230,44,54,2048
mvaa 172,52,32
ta 94,239,45568
mvaa 168,48,32
ra 16,232,6144
v_add_u32_e32 v232,s99,v232
mvaa 176,48,28
ta 88,238,45056
mvaa 180,52,28
ta 90,238,45568
mvaa 188,52,24
dl 231,44,54,3072
mvaa 184,48,24
s_add_u32 m0,s61,0
ta 44,235,32768
s_add_u32 s54,s54,s58
mvaa 192,56,24
ta 46,235,33280
mvaa 196,60,24
ta 40,234,32768
maaa 152,60,28
ta 42,234,33280
maaa 168,56,28
dl 224,36,52,0
maaa 164,56,32
ta 52,237,32768
maaa 148,60,32
ta 54,237,33280
maaa 144,60,36
ta 48,236,32768
v_accvgpr_read_b32 v204,a152
maaa 160,56,36
ta 50,236,33280
v_accvgpr_read_b32 v205,a153
maaa 128,64,36
dl 225,36,52,1024
maaa 112,68,36
ta 60,235,36864
v_accvgpr_read_b32 v206,a154
maaa 116,68,32
ta 62,235,37376
v_accvgpr_read_b32 v207,a155
maaa 132,64,32
ta 56,234,36864
v_accvgpr_read_b32 v200,a168
maaa 136,64,28
ta 58,234,37376
v_accvgpr_read_b32 v201,a169
maaa 120,68,28
dl 226,36,52,2048
maaa 124,68,24
v_accvgpr_read_b32 v202,a170
v_accvgpr_read_b32 v203,a171
maaa 140,64,24
mvaa 40,72,24
ta 68,237,36864
mvaa 44,76,24
ta 70,237,37376
mvaa 52,76,28
dl 227,36,52,3072
mvaa 48,72,28
s_add_u32 m0,s62,32768
ta 64,236,36864
s_add_u32 s52,s52,s56
mvaa 56,72,32
s_mul_i32 s56,s59,12288
ta 66,236,37376
mvaa 60,76,32
mvaa 68,76,36
mvaa 64,72,36
mvaa 32,96,36
ta 76,235,40960
ta 78,235,41472
mvaa 36,100,36
ta 72,234,40960
ta 74,234,41472
mvaa 28,100,32
mvaa 24,96,32
mvaa 16,96,28
mvaa 20,100,28
mvaa 12,100,24
mvaa 8,96,24
mvaa 76,24,104
mvaa 96,24,108
mvaa 100,28,108
mvaa 80,28,104
mvaa 84,32,104
mvaa 104,32,108
mvaa 108,36,108
s_waitcnt vmcnt(16) lgkmcnt(0)
s_barrier
mvaa 88,36,104
dl 228,40,53,0
mvaa 0,40,4
ra 24,233,0
mvaa 4,44,4
ra 28,233,2048
mvaa 92,44,8
ta 96,236,57344
mvaa 72,40,8
ta 98,236,57856
mvaa 144,40,12
dl 229,40,53,1024
mvaa 148,44,12
s_add_u32 s53,s53,s57
ta 100,237,57344
mvaa 156,44,16
ta 102,237,57856
mvaa 152,40,16
ra 32,233,4096
mvaa 160,48,16
ta 108,239,61440
mvaa 164,52,16
dl 230,44,54,2048
mvaa 172,52,12
ta 110,239,61952
mvaa 168,48,12
ra 36,233,6144
v_add_u32_e32 v233,s99,v233
mvaa 176,48,8
ta 104,238,61440
mvaa 180,52,8
ta 106,238,61952
mvaa 188,52,4
dl 231,44,54,3072
mvaa 184,48,4
s_add_u32 m0,s61,4096
ta 44,235,49152
s_add_u32 s54,s54,s57
mvaa 192,56,4
s_add_u32 s61,s63,s98
ta 46,235,49664
s_add_u32 s63,s63,s99
mvaa 196,60,4
s_mov_b32 s98,s94
ta 40,234,49152
s_mov_b32 s99,s98
mvaa 204,60,8
s_and_b32 s94,s12,1
ta 42,234,49664
s_lshl_b32 s94,s94,14
mvaa 200,56,8
dl 224,36,52,0
maaa 164,56,12
s_add_u32 s57,s56,4096
ta 52,237,49152
v_mov_b32_e32 v224,v244
maaa 148,60,12
ta 54,237,49664
maaa 144,60,16
ta 48,236,49152
maaa 160,56,16
ta 50,236,49664
v_accvgpr_read_b32 v208,a164
maaa 128,64,16
dl 225,36,52,1024
maaa 112,68,16
ta 60,235,53248
v_accvgpr_read_b32 v209,a165
maaa 116,68,12
ta 62,235,53760
v_accvgpr_read_b32 v210,a166
maaa 132,64,12
ta 56,234,53248
v_accvgpr_read_b32 v211,a167
maaa 136,64,8
ta 58,234,53760
v_accvgpr_read_b32 v212,a148
maaa 120,68,8
dl 226,36,52,2048
maaa 124,68,4
v_accvgpr_read_b32 v213,a149
v_accvgpr_read_b32 v214,a150
maaa 140,64,4
v_accvgpr_read_b32 v215,a151
v_add_u32_e32 v225,65536,v224
mvaa 40,72,4
ta 68,237,53248
v_mov_b32_e32 v226,1065369472
mvaa 44,76,4
ta 70,237,53760
mvaa 52,76,8
dl 227,36,52,3072
mvaa 48,72,8
s_add_u32 m0,s62,49152
ta 64,236,53248
s_add_u32 s52,s52,s60
mvaa 56,72,12
s_lshl_b32 s60,%1,7
ta 66,236,53760
s_mov_b64 s[36:37],s[28:29]
mvaa 60,76,12
s_mov_b32 s38,8388608
s_mov_b32 s39,131072
s_add_u32 s61,s60,83914752
mvaa 68,76,16
s_lshl_b32 s52,s12,12
mvaa 64,72,16
mvaa 32,80,16
ta 76,235,57344
ta 78,235,57856
mvaa 36,84,16
ta 72,234,57344
ta 74,234,57856
mvaa 28,84,12
mvaa 24,80,12
mvaa 16,80,8
mvaa 20,84,8
mvaa 12,84,4
mvaa 8,80,4
mvaa 76,4,88
mvaa 96,4,92
mvaa 100,8,92
mvaa 80,8,88
mvaa 84,12,88
mvaa 104,12,92
mvaa 108,16,92
s_waitcnt vmcnt(16) lgkmcnt(0)
s_barrier
mvaa 88,16,88
dl 228,40,53,0
mvaa 0,40,24
ra 4,232,0
mvaa 4,44,24
ra 8,232,2048
mvaa 92,44,28
ta 80,236,8192
mvaa 72,40,28
ta 82,236,8704
mvaa 144,40,32
dl 229,40,53,1024
mvaa 148,44,32
s_lshl_b32 s43,s19,15
ta 84,237,8192
s_add_u32 s42,s43,16384
mvaa 156,44,36
s_add_u32 s53,s53,s58
ta 86,237,8704
s_mov_b32 s40,858993459
mvaa 152,40,36
s_mov_b32 s41,858993459
ra 12,232,4096
v_cndmask_b32_e64 v228,0,v226,s[40:41]
mvaa 160,48,36
ta 92,239,12288
v_cndmask_b32_e64 v229,0,v226,s[40:41]
mvaa 164,52,36
buffer_load_dword v248,v250,s[8:11],0 offen sc1
mvaa 172,52,32
ta 94,239,12800
mvaa 168,48,32
ra 16,232,6144
mvaa 176,48,28
ta 88,238,12288
mvaa 180,52,28
ta 90,238,12800
mvaa 188,52,24
dl 230,44,54,2048
mvaa 184,48,24
ta 44,235,0
v_cndmask_b32_e64 v230,0,v226,s[40:41]
mvaa 192,56,24
ta 46,235,512
mvaa 196,60,24
ta 40,234,0
mvaa 204,60,28
ta 42,234,512
mvaa 200,56,28
dl 231,44,54,3072
mvaa 208,56,32
s_add_u32 s54,s54,s58
ta 52,237,0
s_mov_b32 s46,16384
mvaa 212,60,32
s_sub_u32 s47,32768,s43
ta 54,237,512
v_cndmask_b32_e64 v231,0,v226,s[40:41]
maaa 144,60,36
s_mov_b32 s40,0xf0000
ta 48,236,0
s_mov_b32 s41,0xfff00ff
maaa 160,56,36
s_add_u32 s47,s47,32768
ta 50,236,512
v_cndmask_b32_e64 v226,0,v252,s[40:41]
maaa 128,64,36
s_mov_b32 s40,0x3f0003
ta 60,235,4096
v_accvgpr_read_b32 v220,a144
maaa 112,68,36
s_mov_b32 s41,0x3fff03ff
ta 62,235,4608
v_accvgpr_read_b32 v221,a145
maaa 116,68,32
s_lshr_b32 s45,s15,1
ta 56,234,4096
v_accvgpr_read_b32 v222,a146
maaa 132,64,32
s_add_u32 s58,s56,8192
ta 58,234,4608
v_accvgpr_read_b32 v223,a147
maaa 136,64,28
s_ff1_i32_b32 s80,s45
v_accvgpr_read_b32 v216,a160
v_accvgpr_read_b32 v217,a161
maaa 120,68,28
s_sub_u32 s44,s56,2048
v_accvgpr_read_b32 v218,a162
v_accvgpr_read_b32 v219,a163
maaa 124,68,24
v_cndmask_b32_e64 v227,0,v252,s[40:41]
s_mov_b32 s40,0xfff0ffff
maaa 140,64,24
s_mov_b32 s41,0xf000ff00
mvaa 40,72,24
ta 68,237,4096
mvaa 44,76,24
ta 70,237,4608
mvaa 52,76,28
ta 64,236,4096
mvaa 48,72,28
ta 66,236,4608
mvaa 56,72,32
mvaa 60,76,32
mvaa 68,76,36
mvaa 64,72,36
mvaa 32,96,36
ta 76,235,8192
ta 78,235,8704
mvaa 36,100,36
ta 72,234,8192
ta 74,234,8704
mvaa 28,100,32
mvaa 24,96,32
mvaa 16,96,28
mvaa 20,100,28
mvaa 12,100,24
mvaa 8,96,24
mvaa 76,24,104
mvaa 96,24,108
mvaa 100,28,108
mvaa 80,28,104
mvaa 84,32,104
mvaa 104,32,108
mvaa 108,36,108
s_waitcnt vmcnt(13) lgkmcnt(0)
s_barrier
mvaa 88,36,104
ra 0,233,0
ta 144,234,16384
ta 146,234,16896
ta 164,235,16384
mvaa 0,40,4
ta 166,235,16896
mvaa 4,44,4
ra 28,233,2048
mvaa 92,44,8
ra 96,233,4096
mvaa 72,40,8
ta 168,236,16384
ta 170,236,16896
mvaa 144,40,12
ta 188,237,16384
mvaa 148,44,12
ta 190,237,16896
mvaa 156,44,16
ta 192,234,20480
mvaa 152,40,16
ta 194,234,20992
ta 212,235,20480
mvaa 160,48,16
ta 214,235,20992
mvaa 164,52,16
ta 216,236,20480
mvaa 172,52,12
ta 218,236,20992
mvaa 168,48,12
ta 236,237,20480
ta 238,237,20992
mvaa 176,48,8
ta 244,235,24576
mvaa 180,52,8
ta 246,235,25088
mvaa 188,52,4
ra 108,233,6144
mvaa 184,48,4
mvaa 192,56,4
ta 52,239,28672
ta 54,239,29184
mvaa 196,60,4
mvaa 204,60,8
mvaa 200,56,8
mvaa 208,56,12
mvaa 212,60,12
mvaa 220,60,16
mvaa 216,56,16
maaa 128,64,16
maaa 112,68,16
s_waitcnt vmcnt(0)
s_barrier
ra 20,232,32768
ta 148,234,32768
ta 150,234,33280
ra 24,233,32768
ta 152,234,49152
maaa 116,68,12
ta 154,234,49664
ta 156,235,49152
ta 158,235,49664
ta 160,235,32768
ta 162,235,33280
ra 32,232,34816
ra 36,233,34816
maaa 132,64,12
ra 100,232,36864
ra 104,233,36864
ta 172,236,32768
ta 174,236,33280
ta 176,236,49152
maaa 136,64,8
ta 178,236,49664
ta 180,237,49152
v_and_b32_e32 v248,v242,v248
v_cmp_ne_u32_e64 s[64:65],v243,v248
maaa 120,68,8
ta 182,237,49664
ta 184,237,32768
maaa 124,68,4
s_cmp_lg_u64 s[64:65],0
s_cbranch_scc1 L_q_spin
L_q_ok:
ta 186,237,33280
ta 196,234,36864
s_mov_b32 m0,s95
maaa 140,64,4
ds 249,8,42,0
ta 198,234,37376
ta 200,234,53248
mvaa 40,72,4
s_and_b32 s87,s60,7552
ta 202,234,53760
ta 204,235,53248
mvaa 44,76,4
s_add_u32 s53,s87,83898368
ta 206,235,53760
ta 208,235,36864
mvaa 52,76,8
s_lshl_b32 s87,s91,8
ta 210,235,37376
ta 220,236,36864
mvaa 48,72,8
s_add_u32 s54,s87,s88
ta 222,236,37376
ta 224,236,53248
mvaa 56,72,12
ds 249,8,42,1024
ta 226,236,53760
ta 228,237,53248
mvaa 60,76,12
s_lshl_b32 s87,s12,16
ta 230,237,53760
ta 232,237,36864
mvaa 68,76,16
s_add_u32 s87,s87,s54
ta 234,237,37376
ta 240,235,40960
mvaa 64,72,16
s_andn2_b32 s87,s87,64
ta 242,235,41472
ra 44,232,38912
mvaa 32,80,16
s_lshl_b32 s88,s66,5
ra 40,233,38912
ta 48,239,45056
mvaa 36,84,16
ds 249,8,42,2048
ta 50,239,45568
ta 60,239,61440
mvaa 28,84,12
s_lshl_b32 s86,s21,2
ta 62,239,61952
ta 56,238,61440
mvaa 24,80,12
s_add_u32 s63,s87,s88
ta 58,238,61952
ta 68,238,45056
mvaa 16,80,8
s_lshl_b32 s88,s67,5
ta 70,238,45568
ta 64,238,28672
mvaa 20,84,8
s_add_u32 s86,s86,s12
ta 66,238,29184
ta 76,237,24576
mvaa 12,84,4
ds 249,8,42,3072
ta 78,237,25088
ta 72,237,40960
mvaa 8,80,4
s_add_u32 s65,s87,s88
ta 74,237,41472
s_and_b32 s87,s20,4
mvaa 76,4,88
s_lshr_b32 s85,s86,1
ta 84,237,57344
ta 86,237,57856
s_lshl_b32 s87,s87,8
s_lshl_b32 s85,s85,11
s_and_b32 s89,s86,1
s_and_b32 s88,s20,3
s_add_u32 s85,s85,s87
mvaa 96,4,92
s_lshl_b32 s89,s89,9
s_lshr_b32 s49,s86,3
s_add_u32 s85,s85,s89
s_lshl_b32 s89,s88,20
s_add_u32 s85,s85,s89
s_lshl_b32 s49,s49,11
s_and_b32 s89,s86,7
s_add_u32 s49,s49,s87
mvaa 100,8,92
s_lshl_b32 s89,s89,7
ta 80,236,57344
ta 82,236,57856
s_add_u32 s49,s49,s89
s_lshl_b32 s89,s88,18
s_add_u32 s49,s49,s89
s_lshl_b32 s89,s12,6
s_lshl_b32 s35,s90,2
mvaa 80,8,88
s_add_u32 s89,s89,83886080
s_lshl_b32 s90,s22,2
s_add_u32 s90,s90,s89
ta 4,236,40960
ta 6,236,41472
mvaa 84,12,88
buffer_load_dword v240,v247,s[8:11],s90 offen offset:0 sc1
mvaa 104,12,92
s_lshl_b32 s91,s23,2
ta 8,236,24576
ta 10,236,25088
mvaa 108,16,92
s_add_u32 s91,s91,s89
s_mov_b32 s86,268435456
v_mov_b32_e32 v232,0xc1a00000
mvaa 88,16,88
s_add_u32 s35,s35,83886080
ta 12,234,24576
ta 14,234,25088
s_waitcnt lgkmcnt(8)
mvaa 0,144,0
s_bfe_u32 s87,s98,327691
s_cselect_b32 s87,2048,0
ta 92,234,40960
ta 94,234,41472
mvaa 0,148,20
buffer_load_dword v241,v247,s[8:11],s90 offen offset:256 sc1
ta 16,234,57344
ta 18,234,57856
ta 88,235,57344
ta 90,235,57856
mvaa 0,152,24
s_sub_u32 s98,s98,s87
v_mov_b32_e32 v233,0xc1a00000
v_mov_b32_e32 v234,0xc1a00000
mvaa 4,156,24
s_xor_b32 s62,s63,128
v_mov_b32_e32 v235,0xc1a00000
v_cndmask_b32_e64 v250,0,v252,s[40:41]
mvaa 4,160,20
s_mov_b32 s40,0xffc0fffc
s_mov_b32 s41,0xc000fc00
v_mul_f32_e32 v0,0x3e0293ee,v0
mvaa 4,164,0
s_xor_b32 s64,s65,128
v_mul_f32_e32 v1,0x3e0293ee,v1
v_mul_f32_e32 v2,0x3e0293ee,v2
mvaa 92,164,28
buffer_load_dword v242,v247,s[8:11],s91 offen offset:0 sc1
mvaa 92,160,32
v_mul_f32_e32 v3,0x3e0293ee,v3
cv 0,0,1
mvaa 92,156,36
v_mul_f32_e32 v4,0x3e0293ee,v4
cv 1,2,3
mvaa 72,152,36
v_mul_f32_e32 v5,0x3e0293ee,v5
s_waitcnt lgkmcnt(0)
s_barrier
s_mov_b32 m0,s70
mvaa 72,148,32
v_mul_f32_e32 v6,0x3e0293ee,v6
v_mul_f32_e32 v7,0x3e0293ee,v7
mvaa 72,144,28
ds 249,8,43,0
mvaa 144,144,96
cv 2,4,5
cv 3,6,7
mvaa 144,148,100
v_mul_f32_e32 v92,0x3e0293ee,v92
v_mul_f32_e32 v93,0x3e0293ee,v93
mvaa 144,152,104
v_mul_f32_e32 v94,0x3e0293ee,v94
v_mul_f32_e32 v95,0x3e0293ee,v95
mvaa 148,156,104
v_mul_f32_e32 v72,0x3e0293ee,v72
v_mul_f32_e32 v73,0x3e0293ee,v73
mvaa 148,160,100
ds 249,8,43,1024
mvaa 148,164,96
v_mul_f32_e32 v74,0x3e0293ee,v74
v_mul_f32_e32 v75,0x3e0293ee,v75
mvaa 156,164,108
cv 72,72,73
cv 73,74,75
mvaa 156,160,44
cv 74,92,93
cv 75,94,95
mvaa 156,156,40
v_mul_f32_e32 v144,0x3e0293ee,v144
v_mul_f32_e32 v145,0x3e0293ee,v145
mvaa 152,152,40
ds 249,8,43,2048
mvaa 152,148,44
v_mul_f32_e32 v146,0x3e0293ee,v146
v_mul_f32_e32 v147,0x3e0293ee,v147
mvaa 152,144,108
cv 144,144,145
v_mul_f32_e32 v148,0x3e0293ee,v148
mvaa 160,168,108
cv 145,146,147
v_mul_f32_e32 v149,0x3e0293ee,v149
mvaa 160,172,44
v_mul_f32_e32 v150,0x3e0293ee,v150
v_mul_f32_e32 v151,0x3e0293ee,v151
mvaa 160,176,40
ds 249,8,43,3072
mvaa 164,180,40
s_mov_b32 m0,s72
cv 146,148,149
cv 147,150,151
mvaa 164,184,44
v_mul_f32_e32 v156,0x3e0293ee,v156
v_mul_f32_e32 v157,0x3e0293ee,v157
mvaa 164,188,108
v_mul_f32_e32 v158,0x3e0293ee,v158
v_mul_f32_e32 v152,0x3e0293ee,v152
mvaa 172,188,96
v_mul_f32_e32 v159,0x3e0293ee,v159
v_mul_f32_e32 v153,0x3e0293ee,v153
mvaa 172,184,100
ds 255,8,42,0
mvaa 172,180,104
v_mul_f32_e32 v154,0x3e0293ee,v154
v_mul_f32_e32 v155,0x3e0293ee,v155
mvaa 168,176,104
cv 152,152,153
cv 153,154,155
mvaa 168,172,100
ds_write_b128 v251,v[0:3] offset:0
mvaa 168,168,96
cv 154,156,157
cv 155,158,159
mvaa 176,168,28
ds 255,8,42,1024
mvaa 176,172,32
v_mul_f32_e32 v160,0x3e0293ee,v160
v_mul_f32_e32 v161,0x3e0293ee,v161
mvaa 176,176,36
v_mul_f32_e32 v162,0x3e0293ee,v162
v_mul_f32_e32 v163,0x3e0293ee,v163
mvaa 180,180,36
cv 160,160,161
cv 161,162,163
mvaa 180,184,32
v_mul_f32_e32 v164,0x3e0293ee,v164
v_mul_f32_e32 v165,0x3e0293ee,v165
mvaa 180,188,28
ds 255,8,42,2048
ds_write_b128 v251,v[72:75] offset:4096
mvaa 188,188,0
v_mul_f32_e32 v166,0x3e0293ee,v166
v_mul_f32_e32 v167,0x3e0293ee,v167
mvaa 188,184,20
cv 162,164,165
cv 163,166,167
mvaa 188,180,24
v_mul_f32_e32 v172,0x3e0293ee,v172
v_mul_f32_e32 v173,0x3e0293ee,v173
mvaa 184,176,24
v_mul_f32_e32 v174,0x3e0293ee,v174
v_mul_f32_e32 v175,0x3e0293ee,v175
mvaa 184,172,20
ds 255,8,42,3072
mvaa 184,168,0
v_mul_f32_e32 v168,0x3e0293ee,v168
v_mul_f32_e32 v169,0x3e0293ee,v169
mvaa 192,192,0
ds_write_b128 v251,v[144:147] offset:8192
mvaa 192,196,20
v_mul_f32_e32 v170,0x3e0293ee,v170
v_mul_f32_e32 v171,0x3e0293ee,v171
mvaa 192,200,24
cv 168,168,169
cv 169,170,171
mvaa 196,204,24
buffer_load_dword v243,v247,s[8:11],s91 offen offset:256 sc1
mvaa 196,208,20
cv 170,172,173
cv 171,174,175
mvaa 196,212,0
v_mul_f32_e32 v176,0x3e0293ee,v176
v_mul_f32_e32 v177,0x3e0293ee,v177
mvaa 204,212,28
v_mul_f32_e32 v178,0x3e0293ee,v178
v_mul_f32_e32 v179,0x3e0293ee,v179
mvaa 204,208,32
ds_write_b128 v251,v[152:155] offset:12288
mvaa 204,204,36
cv 176,176,177
cv 177,178,179
mvaa 200,200,36
v_mul_f32_e32 v180,0x3e0293ee,v180
v_mul_f32_e32 v181,0x3e0293ee,v181
mvaa 200,196,32
v_mul_f32_e32 v182,0x3e0293ee,v182
v_mul_f32_e32 v183,0x3e0293ee,v183
mvaa 200,192,28
cv 178,180,181
cv 179,182,183
mvaa 208,192,96
v_mul_f32_e32 v188,0x3e0293ee,v188
v_mul_f32_e32 v189,0x3e0293ee,v189
mvaa 208,196,100
v_mul_f32_e32 v190,0x3e0293ee,v190
v_mul_f32_e32 v191,0x3e0293ee,v191
mvaa 208,200,104
ds_write_b128 v251,v[160:163] offset:13312
mvaa 212,204,104
v_mul_f32_e32 v184,0x3e0293ee,v184
v_mul_f32_e32 v185,0x3e0293ee,v185
mvaa 212,208,100
v_mul_f32_e32 v186,0x3e0293ee,v186
v_mul_f32_e32 v187,0x3e0293ee,v187
mvaa 212,212,96
cv 184,184,185
cv 185,186,187
mvaa 220,212,108
cv 186,188,189
cv 187,190,191
)"
R"(mvaa 220,208,44
v_mul_f32_e32 v192,0x3e0293ee,v192
v_mul_f32_e32 v193,0x3e0293ee,v193
mvaa 220,204,40
v_mul_f32_e32 v194,0x3e0293ee,v194
v_mul_f32_e32 v195,0x3e0293ee,v195
mvaa 216,200,40
ds_write_b128 v251,v[168:171] offset:9216
mvaa 216,196,44
cv 192,192,193
cv 193,194,195
mvaa 216,192,108
v_mul_f32_e32 v196,0x3e0293ee,v196
v_mul_f32_e32 v197,0x3e0293ee,v197
maaa 128,216,108
s_waitcnt vmcnt(12)
s_barrier
maaa 128,220,44
v_mul_f32_e32 v198,0x3e0293ee,v198
v_mul_f32_e32 v199,0x3e0293ee,v199
maaa 128,224,40
cv 194,196,197
cv 195,198,199
maaa 112,228,40
v_mul_f32_e32 v204,0x3e0293ee,v204
v_mul_f32_e32 v205,0x3e0293ee,v205
maaa 112,232,44
ds_write_b128 v251,v[176:179] offset:5120
maaa 112,236,108
v_mul_f32_e32 v206,0x3e0293ee,v206
v_mul_f32_e32 v207,0x3e0293ee,v207
maaa 116,236,96
v_mul_f32_e32 v200,0x3e0293ee,v200
v_mul_f32_e32 v201,0x3e0293ee,v201
maaa 116,232,100
v_mul_f32_e32 v202,0x3e0293ee,v202
v_mul_f32_e32 v203,0x3e0293ee,v203
maaa 116,228,104
cv 200,200,201
cv 201,202,203
maaa 132,224,104
cv 202,204,205
cv 203,206,207
maaa 132,220,100
v_mul_f32_e32 v208,0x3e0293ee,v208
v_mul_f32_e32 v209,0x3e0293ee,v209
maaa 132,216,96
ds_write_b128 v251,v[184:187] offset:1024
maaa 136,216,28
v_mul_f32_e32 v210,0x3e0293ee,v210
v_mul_f32_e32 v211,0x3e0293ee,v211
maaa 136,220,32
cv 208,208,209
cv 209,210,211
maaa 136,224,36
v_mul_f32_e32 v212,0x3e0293ee,v212
v_mul_f32_e32 v213,0x3e0293ee,v213
maaa 120,228,36
v_mul_f32_e32 v214,0x3e0293ee,v214
v_mul_f32_e32 v215,0x3e0293ee,v215
maaa 120,232,32
cv 210,212,213
cv 211,214,215
maaa 120,236,28
v_mul_f32_e32 v220,0x3e0293ee,v220
v_mul_f32_e32 v221,0x3e0293ee,v221
maaa 124,236,0
ds_write_b128 v251,v[192:195] offset:2048
maaa 124,232,20
v_mul_f32_e32 v216,0x3e0293ee,v216
v_mul_f32_e32 v217,0x3e0293ee,v217
maaa 124,228,24
v_mul_f32_e32 v222,0x3e0293ee,v222
v_mul_f32_e32 v223,0x3e0293ee,v223
maaa 140,224,24
v_mul_f32_e32 v218,0x3e0293ee,v218
v_mul_f32_e32 v219,0x3e0293ee,v219
maaa 140,220,20
cv 216,216,217
cv 217,218,219
maaa 140,216,0
cv 218,220,221
cv 219,222,223
mvaa 88,108,64
v_accvgpr_read_b32 v0,a128
v_accvgpr_read_b32 v1,a129
mvaa 88,44,68
ds_write_b128 v251,v[200:203] offset:6144
mvaa 88,40,56
v_mul_f32_e32 v0,0x3e0293ee,v0
v_mul_f32_e32 v1,0x3e0293ee,v1
mvaa 108,40,60
v_accvgpr_read_b32 v2,a130
v_accvgpr_read_b32 v3,a131
mvaa 108,44,48
v_mul_f32_e32 v2,0x3e0293ee,v2
v_mul_f32_e32 v3,0x3e0293ee,v3
cv 88,88,89
cv 89,90,91
mvaa 108,108,52
buffer_store_dwordx2 v[88:89],v253,s[8:11],s99 offen offset:1024 sc0 sc1
mvaa 36,76,108
cv 0,0,1
v_accvgpr_read_b32 v4,a112
mvaa 36,72,44
v_accvgpr_read_b32 v5,a113
v_accvgpr_read_b32 v6,a114
mvaa 36,84,40
ds_write_b128 v251,v[208:211] offset:10240
mvaa 32,80,40
cv 108,108,109
cv 109,110,111
mvaa 32,4,44
buffer_store_dwordx2 v[108:109],v253,s[8:11],s99 offen offset:1536 sc0 sc1
mvaa 32,8,108
cv 1,2,3
v_mul_f32_e32 v4,0x3e0293ee,v4
mvaa 64,12,108
v_mul_f32_e32 v5,0x3e0293ee,v5
v_accvgpr_read_b32 v7,a115
mvaa 64,92,44
v_mul_f32_e32 v6,0x3e0293ee,v6
v_mul_f32_e32 v7,0x3e0293ee,v7
mvaa 64,16,40
cv 32,32,33
cv 33,34,35
cv 34,36,37
cv 35,38,39
mvaa 68,88,40
buffer_store_dwordx4 v[32:35],v244,s[8:11],s99 offen sc0 sc1
ds_write_b128 v251,v[216:219] offset:14336
mvaa 68,240,44
cv 2,4,5
cv 3,6,7
mvaa 68,244,108
v_accvgpr_read_b32 v92,a116
v_accvgpr_read_b32 v93,a117
mvaa 60,244,96
ra 192,224,0
v_accvgpr_read_b32 v94,a118
mvaa 60,240,100
ra 196,224,1024
v_mul_f32_e32 v92,0x3e0293ee,v92
mvaa 60,88,104
ra 200,224,2048
v_mul_f32_e32 v93,0x3e0293ee,v93
mvaa 56,16,104
v_mul_f32_e32 v94,0x3e0293ee,v94
v_accvgpr_read_b32 v95,a119
mvaa 56,92,100
ds_write_b128 v251,v[0:3] offset:15360
mvaa 56,12,96
v_accvgpr_read_b32 v72,a132
v_accvgpr_read_b32 v73,a133
mvaa 24,8,96
v_accvgpr_read_b32 v74,a134
v_mul_f32_e32 v95,0x3e0293ee,v95
mvaa 24,4,100
v_mul_f32_e32 v72,0x3e0293ee,v72
v_mul_f32_e32 v73,0x3e0293ee,v73
mvaa 24,80,104
v_mul_f32_e32 v74,0x3e0293ee,v74
v_accvgpr_read_b32 v75,a135
mvaa 28,84,104
v_mul_f32_e32 v75,0x3e0293ee,v75
cv 72,72,73
mvaa 28,72,100
ra 204,224,3072
cv 73,74,75
cv 74,92,93
mvaa 28,76,96
ra 208,224,4096
cv 75,94,95
mvaa 20,76,28
ds_write_b128 v251,v[72:75] offset:11264
mvaa 20,72,32
v_accvgpr_read_b32 v144,a136
v_accvgpr_read_b32 v145,a137
mvaa 20,84,36
v_accvgpr_read_b32 v146,a138
v_accvgpr_read_b32 v147,a139
mvaa 16,80,36
ra 212,224,5120
ra 248,224,14336
ra 252,224,15360
v_mul_f32_e32 v144,0x3e0293ee,v144
v_mul_f32_e32 v145,0x3e0293ee,v145
v_mul_f32_e32 v146,0x3e0293ee,v146
v_mul_f32_e32 v147,0x3e0293ee,v147
mvaa 16,4,32
ra 236,224,11264
cv 144,144,145
v_accvgpr_read_b32 v148,a120
v_accvgpr_read_b32 v149,a121
v_accvgpr_read_b32 v150,a122
cv 145,146,147
mvaa 16,8,28
v_mul_f32_e32 v148,0x3e0293ee,v148
v_mul_f32_e32 v149,0x3e0293ee,v149
v_mul_f32_e32 v150,0x3e0293ee,v150
v_accvgpr_read_b32 v151,a123
v_mul_f32_e32 v151,0x3e0293ee,v151
cv 146,148,149
mvaa 48,12,28
ra 228,224,9216
cv 147,150,151
mvaa 48,92,32
ds_write_b128 v251,v[144:147] offset:7168
mvaa 48,16,36
v_accvgpr_read_b32 v156,a124
v_accvgpr_read_b32 v157,a125
mvaa 52,88,36
ra 232,224,10240
ra 216,224,6144
ra 220,224,7168
ra 224,224,8192
v_accvgpr_read_b32 v158,a126
v_accvgpr_read_b32 v159,a127
mvaa 52,240,32
v_accvgpr_read_b32 v152,a140
v_mul_f32_e32 v156,0x3e0293ee,v156
v_mul_f32_e32 v157,0x3e0293ee,v157
v_mul_f32_e32 v158,0x3e0293ee,v158
v_mul_f32_e32 v159,0x3e0293ee,v159
mvaa 52,244,28
v_mul_f32_e32 v152,0x3e0293ee,v152
v_accvgpr_read_b32 v153,a141
v_mul_f32_e32 v153,0x3e0293ee,v153
v_accvgpr_read_b32 v154,a142
v_accvgpr_read_b32 v155,a143
mvaa 44,244,0
v_mul_f32_e32 v154,0x3e0293ee,v154
v_mul_f32_e32 v155,0x3e0293ee,v155
cv 152,152,153
cv 153,154,155
cv 154,156,157
mvaa 44,240,20
ra 244,224,13312
cv 155,158,159
mvaa 44,88,24
ds_write_b128 v251,v[152:155] offset:3072
mvaa 40,16,24
cv 64,64,65
cv 56,56,57
mvaa 40,92,20
cv 57,58,59
cv 58,60,61
mvaa 40,12,0
ra 240,224,12288
cv 59,62,63
mvaa 8,8,0
cv 16,16,17
cv 17,18,19
mvaa 8,4,20
cv 48,48,49
cv 194,44,45
mvaa 8,80,24
cv 195,46,47
cv 192,40,41
mvaa 12,84,24
cv 193,42,43
cv 49,50,51
mvaa 12,72,20
s_cmp_eq_u32 s19,0
s_cselect_b32 s76,32768,0
s_cselect_b32 s77,49152,s86
s_cselect_b32 s78,65536,s46
s_cselect_b32 s79,81920,s86
mvaa 12,76,0
s_add_u32 s86,s95,98304
cv 8,8,9
v_add_u32_e32 v236,s86,v244
mvaa 76,0,64
s_add_u32 s46,s46,16384
v_xor_b32_e32 v237,4096,v236
s_waitcnt lgkmcnt(0)
s_barrier
s_mov_b32 m0,s68
mvaa 76,20,68
ds 249,8,76,0
ra 128,237,32768
ra 132,237,33792
mvaa 76,24,56
s_add_u32 s95,s47,32768
ra 136,237,34816
cv 9,10,11
mvaa 96,24,60
ra 140,237,35840
ra 144,237,49152
mvaa 96,20,48
cv 10,12,13
cv 11,14,15
mvaa 96,0,52
ra 148,237,50176
cv 50,52,53
mvaa 100,28,52
ds 249,8,76,1024
ra 152,237,51200
ra 156,237,52224
mvaa 100,32,48
ra 176,236,16384
cv 51,54,55
mvaa 100,36,60
ra 180,236,17408
cv 18,20,21
mvaa 80,36,56
ra 184,236,18432
cv 19,22,23
mvaa 80,32,68
ra 188,236,19456
cv 24,24,25
mvaa 80,28,64
ds 249,8,76,2048
ra 160,236,0
mvaa 84,96,64
ra 164,236,1024
cv 25,26,27
mvaa 84,100,68
ra 168,236,2048
ra 172,236,3072
mvaa 84,104,56
cv 26,28,29
cv 27,30,31
mvaa 104,104,60
cv 65,66,67
cv 66,68,69
mvaa 104,100,48
ds 249,8,76,3072
mvaa 104,96,52
s_mov_b32 m0,s69
cv 67,70,71
v_cndmask_b32_e64 v252,0,v252,s[40:41]
cvvv 164,8,192,112
cv 70,76,77
cv 71,78,79
cvvv 168,8,48,116
ds 249,8,77,0
cvvv 172,16,48,112
cv 94,96,97
v_mov_b32_e32 v76,v88
cvvv 176,16,56,116
cv 95,98,99
s_waitcnt vmcnt(8) lgkmcnt(0)
s_barrier
cvvv 180,24,56,112
ds 249,8,77,1024
cvvv 184,24,64,116
ra 0,224,32768
cv 96,100,101
cvvv 188,32,64,112
ra 4,224,33792
cv 97,102,103
cvaa 0,192,128,232
buffer_load_dword v17,v247,s[8:11],s35 offen offset:64 sc1
mvaa 0,200,132
ra 8,224,34816
cv 72,80,81
mvaa 0,208,136
ra 12,224,35840
cv 73,82,83
mvaa 0,216,140
ds 249,8,77,2048
cvaa 4,196,128,232
ra 16,224,36864
cv 74,84,85
mvaa 4,204,132
ra 20,224,37888
v_max3_f32 v172,v168,v169,v172
mvaa 4,212,136
ds 249,8,77,3072
mvaa 4,220,140
s_mov_b32 m0,s74
ra 24,224,38912
v_max_f32_e32 v172,v172,v173
cvaa 32,224,128,232
ra 28,224,39936
v_max3_f32 v174,v170,v171,v174
mvaa 32,232,132
buffer_load_dword v19,v247,s[8:11],s35 offen offset:192 sc1
mvaa 32,240,136
ra 32,224,40960
v_max_f32_e32 v174,v174,v175
mvaa 32,248,140
ra 36,224,41984
ra 40,224,43008
cvaa 36,228,128,232
buffer_load_dword v18,v247,s[8:11],s35 offen offset:128 sc1
ra 44,224,44032
mvaa 36,236,132
ra 48,224,45056
v_max_f32_e32 v148,v172,v174
mvaa 36,244,136
ra 56,224,47104
ra 60,224,48128
mvaa 36,252,140
buffer_load_dword v16,v247,s[8:11],s35 offen offset:0 sc1
ra 52,224,46080
cvaa 8,192,144,232
v_mov_b32_e32 v149,v148
cv 75,86,87
mvaa 8,200,148
v_permlane16_swap_b32_e32 v148,v149
mvaa 8,208,152
ds 255,8,43,0
mvaa 8,216,156
v_max_f32_e32 v148,v148,v149
v_mov_b32_e32 v149,v148
cvaa 12,196,144,232
cv 98,104,105
v_max3_f32 v180,v176,v177,v180
mvaa 12,204,148
ds 255,8,43,1024
mvaa 12,212,152
v_permlane32_swap_b32_e32 v148,v149
mvaa 12,220,156
v_max_f32_e32 v148,v148,v149
s_waitcnt lgkmcnt(0)
s_barrier
cvaa 40,224,144,232
ds 255,8,43,2048
mvaa 40,232,148
ra 64,225,0
v_mul_f32_e32 v148,0xbe8293ee,v148
mvaa 40,240,152
ra 68,225,1024
v_fmamk_f32 v172,v172,0x3e8293ee,v148
mvaa 40,248,156
ds 255,8,43,3072
cvaa 44,228,144,232
s_mov_b32 m0,s70
ra 72,225,2048
v_fmamk_f32 v174,v174,0x3e8293ee,v148
mvaa 44,236,148
ra 76,225,3072
v_max3_f32 v182,v178,v179,v182
mvaa 44,244,152
ds 249,8,78,0
mvaa 44,252,156
ra 80,225,4096
v_max_f32_e32 v180,v180,v181
cvaa 24,192,176,128
ra 84,225,5120
v_max_f32_e32 v182,v182,v183
mvaa 24,200,180
ds 249,8,78,1024
mvaa 24,208,184
ra 88,225,6144
v_max_f32_e32 v152,v180,v182
mvaa 24,216,188
ra 92,225,7168
v_mov_b32_e32 v153,v152
cvaa 28,196,176,132
ds 249,8,78,2048
mvaa 28,204,180
ra 96,225,8192
v_max3_f32 v188,v184,v185,v188
mvaa 28,212,184
ra 100,225,9216
ra 104,225,10240
mvaa 28,220,188
ds 249,8,78,3072
ra 108,225,11264
cvaa 56,224,176,136
s_mov_b32 m0,s71
ra 112,225,12288
v_max3_f32 v190,v186,v187,v190
mvaa 56,232,180
ra 116,225,13312
ra 120,225,14336
mvaa 56,240,184
ds 249,8,79,0
ra 124,225,15360
mvaa 56,248,188
ex 172
cvaa 60,228,176,140
ex 174
mvaa 60,236,180
ds 249,8,79,1024
mvaa 60,244,184
v_permlane16_swap_b32_e32 v152,v153
mvaa 60,252,188
cv 172,172,172
cv 174,174,174
cvaa 80,0,160,128
ds 249,8,79,2048
mvaa 80,8,164
v_and_b32_e32 v168,v226,v172
v_max_f32_e32 v152,v152,v153
mvaa 80,16,168
v_and_b32_e32 v169,v227,v174
v_and_b32_e32 v170,v250,v172
v_and_b32_e32 v171,v252,v174
mvaa 80,24,172
ds 249,8,79,3072
v_mov_b32_e32 v153,v152
cvaa 84,4,160,132
v_max_f32_e32 v188,v188,v189
v_permlane32_swap_b32_e32 v152,v153
v_max_f32_e32 v152,v152,v153
mvaa 84,12,164
v_mul_f32_e32 v152,0xbe8293ee,v152
v_fmamk_f32 v180,v180,0x3e8293ee,v152
mvaa 84,20,168
v_fmamk_f32 v182,v182,0x3e8293ee,v152
v_max_f32_e32 v190,v190,v191
mvaa 84,28,172
ex 180
cvaa 112,32,160,136
ex 182
v_max_f32_e32 v156,v188,v190
mvaa 112,40,164
v_mov_b32_e32 v157,v156
cv 180,180,180
mvaa 112,48,168
v_permlane16_swap_b32_e32 v156,v157
mvaa 112,56,172
cv 182,182,182
v_and_b32_e32 v176,v226,v180
v_max_f32_e32 v156,v156,v157
s_waitcnt vmcnt(15) lgkmcnt(0)
s_barrier
s_mov_b32 m0,s72
cvaa 116,36,160,140
ds 255,8,76,0
v_and_b32_e32 v177,v227,v182
mvaa 116,44,164
v_and_b32_e32 v178,v250,v180
v_and_b32_e32 v179,v252,v182
v_mov_b32_e32 v157,v156
mvaa 116,52,168
cv 99,106,107
v_permlane32_swap_b32_e32 v156,v157
v_max_f32_e32 v156,v156,v157
mvaa 116,60,172
ds 255,8,76,1024
v_mul_f32_e32 v156,0xbe8293ee,v156
zvvv 148,228,168
v_fmamk_f32 v188,v188,0x3e8293ee,v156
ex 188
zvvv 200,70,168
v_fmamk_f32 v190,v190,0x3e8293ee,v156
ex 190
zvvv 204,94,168
ds 255,8,76,2048
zvvv 152,228,176
cv 188,188,188
cv 190,190,190
v_and_b32_e32 v184,v226,v188
zvvv 208,72,176
v_and_b32_e32 v185,v227,v190
v_and_b32_e32 v186,v250,v188
v_and_b32_e32 v187,v252,v190
zvvv 212,96,176
ds 255,8,76,3072
s_mov_b32 m0,s73
v_mov_b32_e32 v77,v89
zvvv 156,228,184
v_mov_b32_e32 v100,v108
v_mov_b32_e32 v101,v109
zvvv 216,74,184
ex 0
zvvv 220,98,184
ds 255,8,77,0
cvaa 64,0,128,232
ex 1
mvaa 64,8,132
v_mov_b32_e32 v170,v70
v_mov_b32_e32 v171,v71
mvaa 64,16,136
ds 255,8,77,1024
mvaa 64,24,140
ex 2
cvaa 68,4,128,232
ex 3
mvaa 68,12,132
ds 255,8,77,2048
mvaa 68,20,136
ex 4
mvaa 68,28,140
ex 5
cvaa 96,32,128,232
ds 255,8,77,3072
mvaa 96,40,132
ex 6
mvaa 96,48,136
ex 7
mvaa 96,56,140
ex 32
cvaa 100,36,128,232
ex 33
mvaa 100,44,132
ex 34
mvaa 100,52,136
v_mov_b32_e32 v160,v120
ex 35
mvaa 100,60,140
v_mov_b32_e32 v161,v121
ex 36
cvaa 72,0,144,232
v_mov_b32_e32 v162,v122
ex 37
mvaa 72,8,148
v_mov_b32_e32 v163,v123
ex 38
mvaa 72,16,152
v_mov_b32_e32 v175,v95
ex 39
mvaa 72,24,156
v_mov_b32_e32 v174,v94
ex 8
cvaa 76,4,144,232
cv 0,0,1
ex 9
mvaa 76,12,148
cv 1,2,3
ex 10
mvaa 76,20,152
cv 2,4,5
ex 11
mvaa 76,28,156
cv 3,6,7
ex 12
cvaa 104,32,144,232
cv 4,32,33
ex 13
mvaa 104,40,148
v_sub_f32_e32 v17,1.0,v17
ex 14
mvaa 104,48,152
v_rcp_f32_e32 v148,v148
s_waitcnt vmcnt(21)
v_sub_f32_e32 v19,1.0,v19
mvaa 104,56,156
v_mul_f32_e32 v148,v148,v17
v_mul_f32_e32 v200,v148,v200
v_sub_f32_e32 v18,1.0,v18
cvaa 108,36,144,232
v_mul_f32_e32 v201,v148,v201
v_mul_f32_e32 v202,v148,v202
v_mul_f32_e32 v203,v148,v203
mvaa 108,44,148
v_mul_f32_e32 v204,v148,v204
cv 200,200,201
cv 201,202,203
mvaa 108,52,152
buffer_load_dwordx4 v[176:179],v244,s[8:11],s98 offen sc1
v_mul_f32_e32 v205,v148,v205
mvaa 108,60,156
v_mul_f32_e32 v206,v148,v206
v_mul_f32_e32 v207,v148,v207
cv 202,204,205
cvaa 88,0,176,232
cv 203,206,207
v_rcp_f32_e32 v152,v152
mvaa 88,8,180
buffer_load_dwordx2 v[168:169],v253,s[8:11],s98 offen offset:1024 sc1
v_mul_f32_e32 v152,v152,v18
mvaa 88,16,184
v_mul_f32_e32 v208,v152,v208
v_mul_f32_e32 v209,v152,v209
v_mul_f32_e32 v210,v152,v210
mvaa 88,24,188
v_mul_f32_e32 v211,v152,v211
cv 208,208,209
cv 209,210,211
cvaa 92,4,176,232
buffer_load_dwordx2 v[172:173],v253,s[8:11],s98 offen offset:1536 sc1
v_mul_f32_e32 v212,v152,v212
mvaa 92,12,180
v_mul_f32_e32 v213,v152,v213
v_mul_f32_e32 v214,v152,v214
v_mul_f32_e32 v215,v152,v215
mvaa 92,20,184
cv 210,212,213
v_rcp_f32_e32 v156,v156
mvaa 92,28,188
buffer_store_dwordx4 v[200:203],v244,s[8:11],s34 offen sc0 sc1
cv 211,214,215
cvaa 120,32,176,232
v_mul_f32_e32 v156,v156,v19
v_mul_f32_e32 v216,v156,v216
v_mul_f32_e32 v217,v156,v217
mvaa 120,40,180
v_mul_f32_e32 v218,v156,v218
v_mul_f32_e32 v219,v156,v219
cv 216,216,217
mvaa 120,48,184
buffer_store_dwordx4 v[208:211],v244,s[8:11],s50 offen sc0 sc1
cv 217,218,219
mvaa 120,56,188
v_mul_f32_e32 v220,v156,v220
v_mul_f32_e32 v221,v156,v221
v_mul_f32_e32 v222,v156,v222
cvaa 124,36,176,232
v_mul_f32_e32 v223,v156,v223
cv 218,220,221
cv 219,222,223
mvaa 124,44,180
buffer_store_dwordx4 v[216:219],v244,s[8:11],s51 offen sc0 sc1
cv 5,34,35
mvaa 124,52,184
cv 6,36,37
ex 15
mvaa 124,60,188
s_cmp_lg_u32 s19,0
s_cbranch_scc1 L_j0
s_nop 1
s_lshl_b32 s86,s12,12
s_add_u32 s86,s86,131072
v_add_u32_e32 v236,s86,v244
ra 160,236,0
ra 164,236,1024
ra 168,236,2048
ra 172,236,3072
ra 176,236,16384
ra 180,236,17408
ra 184,236,18432
ra 188,236,19456
v_add_u32_e32 v224,s94,v224
s_waitcnt lgkmcnt(0)
L_j0:
cv 7,38,39
ex 40
zaav 0,64,0
buffer_load_dword v247,v246,s[24:27],s49 offen
maav 0,96,4
s_add_u32 s49,s49,32768
ex 41
zaav 4,68,0
s_lshl_b32 s86,s21,2
ex 42
v_bfe_i32 v248,v244,4,1
maav 4,100,4
buffer_load_dword v247,v245,s[36:39],s85 offen
v_and_b32_e32 v248,2863311530,v248
zaav 8,72,0
s_add_u32 s86,s86,s12
ex 43
v_or_b32_e32 v248,1145324612,v248
maav 8,104,4
s_lshl_b32 s86,s86,8
ex 44
v_mov_b64_e32 v[144:145],0
zaav 12,76,0
buffer_load_dword v247,v246,s[24:27],s49 offen
cv 8,8,9
maav 12,108,4
s_add_u32 s85,s85,131072
cv 9,10,11
ex 45
v_smfmac_f32_16x16x64_bf16 v[144:147],v[228:231],v[0:7],v248
cv 10,12,13
ex 46
zaav 16,80,0
s_add_u32 s49,s49,32768
cv 11,14,15
ex 47
maav 16,112,4
s_waitcnt vmcnt(8)
v_max3_u32 v180,v176,v177,v178
cv 12,40,41
zaav 20,84,0
s_waitcnt vmcnt(7)
v_max3_u32 v180,v180,v179,v168
s_waitcnt vmcnt(6)
v_max3_u32 v180,v180,v169,v172
v_max_u32_e32 v180,v180,v173
v_cmp_eq_u32_e64 s[40:41],-1,v180
maav 20,116,4
s_cmp_lg_u64 s[40:41],0
s_cbranch_scc1 L_bx
L_bxr:
s_bitcmp1_b32 170,s20
s_cselect_b32 s87,L_odd-L_last,0
s_add_u32 s86,s86,s87
s_add_u32 s86,s86,L_last-L_att_pc
mvvv 160,176,192
cv 13,42,43
zaav 24,88,0
cv 14,44,45
v_lshl_add_u32 v238,v244,3,s86
v_min_u32_e32 v238,L_att_end-L_att_pc-4,v238
cv 15,46,47
maav 24,120,4
s_mov_b64 exec,3
global_load_dword v239,v238,s[96:97]
s_mov_b64 exec,-1
zaav 28,92,0
v_max3_f32 v164,v160,v161,v164
v_max_f32_e32 v164,v164,v165
v_max3_f32 v166,v162,v163,v166
v_max_f32_e32 v166,v166,v167
v_max_f32_e32 v180,v164,v166
v_mov_b32_e32 v181,v180
maav 28,124,4
ex 24
v_permlane16_swap_b32_e32 v180,v181
v_max_f32_e32 v180,v180,v181
v_mov_b32_e32 v181,v180
zaav 32,64,8
ex 25
v_permlane32_swap_b32_e32 v180,v181
v_max_f32_e32 v180,v180,v181
v_mul_f32_e32 v180,0xbe8293ee,v180
v_fmamk_f32 v164,v164,0x3e8293ee,v180
maav 32,96,12
ex 164
v_fmamk_f32 v166,v166,0x3e8293ee,v180
ex 166
cv 164,164,164
cv 166,166,166
zaav 36,68,8
v_and_b32_e32 v160,v226,v164
v_and_b32_e32 v161,v227,v166
v_and_b32_e32 v162,v250,v164
v_and_b32_e32 v163,v252,v166
maav 36,100,12
v_sub_f32_e32 v16,1.0,v16
zvvv 180,228,160
zaav 40,72,8
ex 26
zvvv 184,168,160
maav 40,104,12
ex 27
zvvv 188,172,160
zaav 44,76,8
v_mov_b64_e32 v[148:149],0
v_rcp_f32_e32 v180,v180
maav 44,108,12
v_mul_f32_e32 v180,v180,v16
v_mul_f32_e32 v184,v180,v184
v_mul_f32_e32 v185,v180,v185
v_mul_f32_e32 v186,v180,v186
v_mul_f32_e32 v187,v180,v187
v_mul_f32_e32 v188,v180,v188
v_mul_f32_e32 v189,v180,v189
v_smfmac_f32_16x16x64_bf16 v[148:151],v[228:231],v[8:15],v248
v_mul_f32_e32 v190,v180,v190
v_mul_f32_e32 v191,v180,v191
cv 184,184,185
cv 185,186,187
cv 186,188,189
cv 187,190,191
zaav 48,80,8
buffer_store_dwordx4 v[184:187],v244,s[8:11],s33 offen sc0 sc1
cv 24,24,25
cv 25,26,27
ex 28
maav 48,112,12
ex 29
ex 30
ex 31
cv 26,28,29
zaav 52,84,8
cv 27,30,31
ex 56
ex 57
ex 58
maav 52,116,12
ex 59
ex 60
zaav 56,88,8
ra 192,224,0
ra 196,224,1024
ex 61
maav 56,120,12
ra 200,224,2048
cv 28,56,57
ex 62
zaav 60,92,8
ra 204,224,3072
ra 208,224,4096
ex 63
maav 60,124,12
ra 212,224,5120
cv 29,58,59
cv 30,60,61
cv 31,62,63
zvav 192,64,24
ra 216,224,6144
ra 220,224,7168
ex 64
mvav 192,96,28
ra 224,224,8192
v_mov_b32_e32 v251,-1
ex 65
zvav 196,68,24
ra 228,224,9216
ra 232,224,10240
ex 66
mvav 196,100,28
ra 236,224,11264
v_mov_b64_e32 v[152:153],0
ex 67
zvav 200,72,24
ra 240,224,12288
ra 244,224,13312
v_mov_b64_e32 v[156:157],0
cv 64,64,65
mvav 200,104,28
ra 248,224,14336
ra 252,224,15360
ex 68
zvav 204,76,24
ex 69
ex 70
mvav 204,108,28
cv 65,66,67
ex 71
cv 66,68,69
v_smfmac_f32_16x16x64_bf16 v[156:159],v[228:231],v[24:31],v248
ex 96
ex 97
zvav 208,80,24
ex 98
ex 99
mvav 208,112,28
cv 67,70,71
cv 68,96,97
ex 100
zvav 212,84,24
ex 101
ex 102
mvav 212,116,28
cv 69,98,99
ex 103
cv 70,100,101
zvav 216,88,24
ex 72
ex 73
mvav 216,120,28
ex 74
ex 75
zvav 220,92,24
ex 76
ex 77
mvav 220,124,28
s_cmp_ge_i32 s47,s46
s_cselect_b32 s76,s95,s46
s_cselect_b32 s77,s95,268435456
s_add_u32 s77,s77,16384
v_mov_b32_e32 v250,-1
v_mov_b32_e32 v252,-1
v_mov_b32_e32 v253,-1
cv 71,102,103
s_mov_b64 exec,0
s_bitcmp1_b32 170,s20
s_cbranch_scc1 L_vbO
s_cmp_eq_u32 s12,0
s_cbranch_scc1 L_vs1
s_cmp_eq_u32 s12,1
s_cbranch_scc1 L_vs2
s_cmp_eq_u32 s12,2
s_cbranch_scc1 L_vs3
s_branch L_vs4
L_vbO:
s_cmp_eq_u32 s12,0
s_cbranch_scc1 L_vs9
s_cmp_eq_u32 s12,1
s_cbranch_scc1 L_vs10
s_cmp_eq_u32 s12,2
s_cbranch_scc1 L_vs11
s_branch L_vs12
L_cw0:
s_mov_b64 exec,-1
s_waitcnt lgkmcnt(0)
s_barrier
cvaa 0,192,128,232
mvaa 0,200,132
s_add_u32 s81,s81,1
ex 78
mvaa 0,208,136
s_add_u32 s44,s44,1024
ex 79
mvaa 0,216,140
cv 72,72,73
cv 73,74,75
cvaa 4,196,128,232
s_mov_b32 m0,s68
ex 104
mvaa 4,204,132
ds 249,8,76,0
mvaa 4,212,136
ex 105
mvaa 4,220,140
ex 106
cvaa 32,224,128,232
ra 64,225,32768
cv 74,76,77
mvaa 32,232,132
ex 107
mvaa 32,240,136
ra 68,225,33792
cv 75,78,79
mvaa 32,248,140
ex 108
cvaa 36,228,128,232
ds 249,8,76,1024
mvaa 36,236,132
ex 109
)"
R"(mvaa 36,244,136
ra 72,225,34816
cv 76,104,105
mvaa 36,252,140
ex 110
cvaa 8,192,144,232
ra 76,225,35840
cv 77,106,107
mvaa 8,200,148
ex 111
mvaa 8,208,152
ex 80
mvaa 8,216,156
ds 249,8,76,2048
cvaa 12,196,144,232
ra 80,225,36864
cv 78,108,109
mvaa 12,204,148
ex 81
mvaa 12,212,152
ra 84,225,37888
cv 79,110,111
mvaa 12,220,156
ex 82
cvaa 40,224,144,232
ex 83
mvaa 40,232,148
ra 88,225,38912
cv 80,80,81
mvaa 40,240,152
ds 249,8,76,3072
mvaa 40,248,156
ex 84
cvaa 44,228,144,232
ra 92,225,39936
cv 81,82,83
mvaa 44,236,148
ex 85
mvaa 44,244,152
ex 86
mvaa 44,252,156
ra 96,225,40960
cv 82,84,85
cvaa 16,192,160,232
ex 87
mvaa 16,200,164
s_mov_b32 m0,s69
ex 112
mvaa 16,208,168
ds 249,8,77,0
mvaa 16,216,172
ra 100,225,41984
cv 83,86,87
cvaa 20,196,160,232
ex 113
mvaa 20,204,164
ra 104,225,43008
cv 84,112,113
mvaa 20,212,168
ex 114
mvaa 20,220,172
ex 115
cvaa 48,224,160,232
ra 108,225,44032
cv 85,114,115
mvaa 48,232,164
ds 249,8,77,1024
mvaa 48,240,168
ex 116
mvaa 48,248,172
ra 112,225,45056
cvaa 52,228,160,232
ex 117
mvaa 52,236,164
ex 118
mvaa 52,244,168
ra 116,225,46080
cv 86,116,117
mvaa 52,252,172
ex 119
cvaa 24,192,176,232
ds 249,8,77,2048
mvaa 24,200,180
ra 120,225,47104
cv 87,118,119
mvaa 24,208,184
ex 88
mvaa 24,216,188
ex 89
cvaa 28,196,176,232
ra 124,225,48128
cv 88,88,89
mvaa 28,204,180
ex 90
mvaa 28,212,184
ex 91
mvaa 28,220,188
ds 249,8,77,3072
cvaa 56,224,176,232
ex 92
mvaa 56,232,180
ex 93
mvaa 56,240,184
ex 94
mvaa 56,248,188
cvaa 60,228,176,232
ex 95
mvaa 60,236,180
ex 120
mvaa 60,244,184
ex 121
mvaa 60,252,188
s_waitcnt vmcnt(8) lgkmcnt(0)
s_barrier
maav 0,64,64
cv 89,90,91
cv 90,92,93
maav 0,96,68
s_mov_b32 m0,s74
ex 122
maav 4,68,64
ds 255,8,78,0
maav 4,100,68
ex 123
maav 8,72,64
ex 124
maav 8,104,68
ex 125
maav 12,76,64
cv 91,94,95
cv 92,120,121
maav 12,108,68
ex 126
v_smfmac_f32_16x16x64_bf16 v[144:147],v[228:231],v[64:71],v248
cv 93,122,123
maav 16,80,64
ex 127
maav 16,112,68
buffer_load_dword v247,v245,s[36:39],s85 offen
maav 20,84,64
cv 94,124,125
cv 95,126,127
maav 20,116,68
s_lshl_b32 s86,s19,3
s_lshr_b32 s87,s18,2
s_add_u32 s86,s86,s87
maav 24,88,64
s_lshl_b32 s86,s86,7
s_add_u32 s86,s86,83898368
v_mov_b32_e32 v238,1
maav 24,120,68
ra 192,224,32768
maav 28,92,64
ds 255,8,78,1024
maav 28,124,68
s_mov_b64 exec,1
buffer_atomic_add v238,off,s[8:11],s86 sc1
s_mov_b64 exec,-1
maav 32,64,72
ra 196,224,33792
maav 32,96,76
s_add_u32 s85,s85,131072
maav 36,68,72
ra 200,224,34816
maav 36,100,76
maav 40,72,72
ds 255,8,78,2048
maav 40,104,76
maav 44,76,72
ra 204,224,35840
maav 44,108,76
v_smfmac_f32_16x16x64_bf16 v[148:151],v[228:231],v[72:79],v248
ra 208,224,36864
maav 48,80,72
maav 48,112,76
maav 52,84,72
ds 255,8,78,3072
maav 52,116,76
ra 212,224,37888
maav 56,88,72
ex 0
maav 56,120,76
ra 216,224,38912
maav 60,92,72
ex 1
maav 60,124,76
s_mov_b32 m0,s75
ex 2
zvav 160,64,80
ra 220,224,39936
cv 0,0,1
mvav 160,96,84
ds 255,8,79,0
zvav 164,68,80
ex 3
mvav 164,100,84
ra 224,224,40960
cv 1,2,3
zvav 168,72,80
ex 4
mvav 168,104,84
ra 228,224,41984
zvav 172,76,80
ds 255,8,79,1024
mvav 172,108,84
s_add_u32 s46,s46,16384
ex 5
v_smfmac_f32_16x16x64_bf16 v[152:155],v[228:231],v[80:87],v248
ra 232,224,43008
zvav 176,80,80
s_add_u32 s47,s47,32768
ex 6
mvav 176,112,84
s_add_u32 s95,s47,32768
ex 7
zvav 180,84,80
ra 236,224,44032
cv 2,4,5
mvav 180,116,84
ex 32
zvav 184,88,80
ds 255,8,79,2048
mvav 184,120,84
ex 33
zvav 188,92,80
ra 240,224,45056
cv 3,6,7
mvav 188,124,84
ex 34
mvav 192,64,88
ra 244,224,46080
cv 4,32,33
mvav 192,96,92
ex 35
mvav 196,68,88
ex 36
mvav 196,100,92
ds 255,8,79,3072
mvav 200,72,88
ra 248,224,47104
cv 5,34,35
mvav 200,104,92
s_cmp_ge_i32 s47,s46
ex 37
mvav 204,76,88
s_cselect_b32 s78,s95,s46
ex 38
mvav 204,108,92
s_cselect_b32 s79,s95,268435456
ex 39
v_smfmac_f32_16x16x64_bf16 v[156:159],v[228:231],v[88:95],v248
ra 252,224,48128
mvav 208,80,88
s_add_u32 s79,s79,16384
ex 8
mvav 208,112,92
ex 9
mvav 212,84,88
ex 10
mvav 212,116,92
ex 11
mvav 216,88,88
cv 6,36,37
cv 7,38,39
mvav 216,120,92
ex 12
mvav 220,92,88
ex 13
mvav 220,124,92
s_waitcnt vmcnt(16) lgkmcnt(0)
s_barrier
cvaa 64,192,128,232
s_bitcmp0_b32 s82,s81
s_cbranch_scc1 L_loop0
s_bitcmp1_b32 s15,s81
s_cbranch_scc1 L_short0
.p2align 6
L_loop0:
mvaa 64,200,132
s_add_u32 s81,s81,1
ex 14
mvaa 64,208,136
s_add_u32 s44,s44,1024
ex 15
mvaa 64,216,140
cv 8,8,9
cv 9,10,11
cvaa 68,196,128,232
s_mov_b32 m0,s70
ex 40
mvaa 68,204,132
ds 249,8,78,0
mvaa 68,212,136
ex 41
mvaa 68,220,140
ex 42
cvaa 96,224,128,232
ra 64,225,0
cv 10,12,13
mvaa 96,232,132
ex 43
mvaa 96,240,136
ra 68,225,1024
cv 11,14,15
mvaa 96,248,140
ex 44
cvaa 100,228,128,232
ds 249,8,78,1024
mvaa 100,236,132
ex 45
mvaa 100,244,136
ra 72,225,2048
cv 12,40,41
mvaa 100,252,140
ex 46
cvaa 72,192,144,232
ra 76,225,3072
cv 13,42,43
mvaa 72,200,148
ex 47
mvaa 72,208,152
ex 16
mvaa 72,216,156
ds 249,8,78,2048
cvaa 76,196,144,232
ra 80,225,4096
cv 14,44,45
mvaa 76,204,148
ex 17
mvaa 76,212,152
ra 84,225,5120
cv 15,46,47
mvaa 76,220,156
ex 18
cvaa 104,224,144,232
ex 19
mvaa 104,232,148
ra 88,225,6144
cv 16,16,17
mvaa 104,240,152
ds 249,8,78,3072
mvaa 104,248,156
ex 20
cvaa 108,228,144,232
ra 92,225,7168
cv 17,18,19
mvaa 108,236,148
ex 21
mvaa 108,244,152
ex 22
mvaa 108,252,156
ra 96,225,8192
cv 18,20,21
cvaa 80,192,160,232
ex 23
mvaa 80,200,164
s_mov_b32 m0,s71
ex 48
mvaa 80,208,168
ds 249,8,79,0
mvaa 80,216,172
ra 100,225,9216
cv 19,22,23
cvaa 84,196,160,232
ex 49
mvaa 84,204,164
ra 104,225,10240
cv 20,48,49
mvaa 84,212,168
ex 50
mvaa 84,220,172
ex 51
cvaa 112,224,160,232
ra 108,225,11264
cv 21,50,51
mvaa 112,232,164
ds 249,8,79,1024
mvaa 112,240,168
ex 52
mvaa 112,248,172
ra 112,225,12288
cvaa 116,228,160,232
ex 53
mvaa 116,236,164
ex 54
mvaa 116,244,168
ra 116,225,13312
cv 22,52,53
mvaa 116,252,172
ex 55
cvaa 88,192,176,232
ds 249,8,79,2048
mvaa 88,200,180
ra 120,225,14336
cv 23,54,55
mvaa 88,208,184
ex 24
mvaa 88,216,188
ex 25
cvaa 92,196,176,232
ra 124,225,15360
cv 24,24,25
mvaa 92,204,180
ex 26
mvaa 92,212,184
ex 27
mvaa 92,220,188
ds 249,8,79,3072
cvaa 120,224,176,232
ex 28
mvaa 120,232,180
ex 29
mvaa 120,240,184
ex 30
mvaa 120,248,188
buffer_store_dwordx4 v[250:253],v244,s[8:11],s44 offen sc0 sc1
cvaa 124,228,176,232
ex 31
mvaa 124,236,180
ex 56
mvaa 124,244,184
ex 57
mvaa 124,252,188
s_waitcnt vmcnt(16) lgkmcnt(0)
s_barrier
maav 0,64,0
cv 25,26,27
cv 26,28,29
maav 0,96,4
s_mov_b32 m0,s72
ex 58
maav 4,68,0
ds 255,8,76,0
maav 4,100,4
ex 59
maav 8,72,0
ex 60
maav 8,104,4
ex 61
maav 12,76,0
s_cmp_lg_u32 s80,s81
cv 27,30,31
cv 28,56,57
maav 12,108,4
ex 62
v_smfmac_f32_16x16x64_bf16 v[144:147],v[228:231],v[0:7],v248
cv 29,58,59
maav 16,80,0
ex 63
maav 16,112,4
buffer_load_dword v247,v246,s[24:27],s49 offen
maav 20,84,0
cv 30,60,61
cv 31,62,63
maav 20,116,4
maav 24,88,0
s_cbranch_scc1 L_evb_r0
v_add_u32_e32 v224,s94,v224
s_lshl_b32 s86,s12,12
s_add_u32 s86,s86,131072
v_add_u32_e32 v236,s86,v244
ra 160,236,0
ra 164,236,1024
ra 168,236,2048
ra 172,236,3072
ra 176,236,16384
ra 180,236,17408
ra 184,236,18432
ra 188,236,19456
L_evb_r0:
maav 24,120,4
ra 192,224,0
maav 28,92,0
ds 255,8,76,1024
maav 28,124,4
maav 32,64,8
ra 196,224,1024
maav 32,96,12
s_add_u32 s49,s49,32768
maav 36,68,8
ra 200,224,2048
maav 36,100,12
maav 40,72,8
ds 255,8,76,2048
maav 40,104,12
maav 44,76,8
ra 204,224,3072
maav 44,108,12
v_smfmac_f32_16x16x64_bf16 v[148:151],v[228:231],v[8:15],v248
ra 208,224,4096
maav 48,80,8
maav 48,112,12
maav 52,84,8
ds 255,8,76,3072
maav 52,116,12
ra 212,224,5120
maav 56,88,8
ex 64
maav 56,120,12
ra 216,224,6144
maav 60,92,8
ex 65
maav 60,124,12
s_mov_b32 m0,s73
ex 66
mvav 160,64,16
ra 220,224,7168
cv 64,64,65
mvav 160,96,20
ds 255,8,77,0
mvav 164,68,16
ex 67
mvav 164,100,20
ra 224,224,8192
cv 65,66,67
mvav 168,72,16
ex 68
mvav 168,104,20
ra 228,224,9216
mvav 172,76,16
ds 255,8,77,1024
mvav 172,108,20
s_add_u32 s46,s46,16384
ex 69
v_smfmac_f32_16x16x64_bf16 v[152:155],v[228:231],v[16:23],v248
ra 232,224,10240
mvav 176,80,16
s_add_u32 s47,s47,32768
ex 70
mvav 176,112,20
s_add_u32 s95,s47,32768
ex 71
mvav 180,84,16
ra 236,224,11264
cv 66,68,69
mvav 180,116,20
ex 96
mvav 184,88,16
ds 255,8,77,2048
mvav 184,120,20
ex 97
mvav 188,92,16
ra 240,224,12288
cv 67,70,71
mvav 188,124,20
ex 98
mvav 192,64,24
ra 244,224,13312
cv 68,96,97
mvav 192,96,28
ex 99
mvav 196,68,24
ex 100
mvav 196,100,28
ds 255,8,77,3072
mvav 200,72,24
ra 248,224,14336
cv 69,98,99
mvav 200,104,28
s_cmp_ge_i32 s47,s46
ex 101
mvav 204,76,24
s_cselect_b32 s76,s95,s46
ex 102
mvav 204,108,28
s_cselect_b32 s77,s95,268435456
ex 103
v_smfmac_f32_16x16x64_bf16 v[156:159],v[228:231],v[24:31],v248
ra 252,224,15360
mvav 208,80,24
s_add_u32 s77,s77,16384
ex 72
mvav 208,112,28
ex 73
mvav 212,84,24
ex 74
mvav 212,116,28
ex 75
mvav 216,88,24
cv 70,100,101
cv 71,102,103
mvav 216,120,28
ex 76
mvav 220,92,24
ex 77
mvav 220,124,28
s_waitcnt vmcnt(16) lgkmcnt(0)
s_barrier
cvaa 0,192,128,232
mvaa 0,200,132
s_add_u32 s81,s81,1
ex 78
mvaa 0,208,136
s_add_u32 s44,s44,1024
ex 79
mvaa 0,216,140
cv 72,72,73
cv 73,74,75
cvaa 4,196,128,232
s_mov_b32 m0,s68
ex 104
mvaa 4,204,132
ds 249,8,76,0
mvaa 4,212,136
ex 105
mvaa 4,220,140
ex 106
cvaa 32,224,128,232
ra 64,225,32768
cv 74,76,77
mvaa 32,232,132
ex 107
mvaa 32,240,136
ra 68,225,33792
cv 75,78,79
mvaa 32,248,140
ex 108
cvaa 36,228,128,232
ds 249,8,76,1024
mvaa 36,236,132
ex 109
mvaa 36,244,136
ra 72,225,34816
cv 76,104,105
mvaa 36,252,140
ex 110
cvaa 8,192,144,232
ra 76,225,35840
cv 77,106,107
mvaa 8,200,148
ex 111
mvaa 8,208,152
ex 80
mvaa 8,216,156
ds 249,8,76,2048
cvaa 12,196,144,232
ra 80,225,36864
cv 78,108,109
mvaa 12,204,148
ex 81
mvaa 12,212,152
ra 84,225,37888
cv 79,110,111
mvaa 12,220,156
ex 82
cvaa 40,224,144,232
ex 83
mvaa 40,232,148
ra 88,225,38912
cv 80,80,81
mvaa 40,240,152
ds 249,8,76,3072
mvaa 40,248,156
ex 84
cvaa 44,228,144,232
ra 92,225,39936
cv 81,82,83
mvaa 44,236,148
ex 85
mvaa 44,244,152
ex 86
mvaa 44,252,156
ra 96,225,40960
cv 82,84,85
cvaa 16,192,160,232
ex 87
mvaa 16,200,164
s_mov_b32 m0,s69
ex 112
mvaa 16,208,168
ds 249,8,77,0
mvaa 16,216,172
ra 100,225,41984
cv 83,86,87
cvaa 20,196,160,232
ex 113
mvaa 20,204,164
ra 104,225,43008
cv 84,112,113
mvaa 20,212,168
ex 114
mvaa 20,220,172
ex 115
cvaa 48,224,160,232
ra 108,225,44032
cv 85,114,115
mvaa 48,232,164
ds 249,8,77,1024
mvaa 48,240,168
ex 116
mvaa 48,248,172
ra 112,225,45056
cvaa 52,228,160,232
ex 117
mvaa 52,236,164
ex 118
mvaa 52,244,168
ra 116,225,46080
cv 86,116,117
mvaa 52,252,172
ex 119
cvaa 24,192,176,232
ds 249,8,77,2048
mvaa 24,200,180
ra 120,225,47104
cv 87,118,119
mvaa 24,208,184
ex 88
mvaa 24,216,188
ex 89
cvaa 28,196,176,232
ra 124,225,48128
cv 88,88,89
mvaa 28,204,180
ex 90
mvaa 28,212,184
ex 91
mvaa 28,220,188
ds 249,8,77,3072
cvaa 56,224,176,232
ex 92
mvaa 56,232,180
ex 93
mvaa 56,240,184
ex 94
mvaa 56,248,188
buffer_store_dwordx4 v[250:253],v244,s[8:11],s44 offen sc0 sc1
cvaa 60,228,176,232
ex 95
mvaa 60,236,180
ex 120
mvaa 60,244,184
ex 121
mvaa 60,252,188
s_waitcnt vmcnt(16) lgkmcnt(0)
s_barrier
maav 0,64,64
cv 89,90,91
cv 90,92,93
maav 0,96,68
s_mov_b32 m0,s74
ex 122
maav 4,68,64
ds 255,8,78,0
maav 4,100,68
ex 123
maav 8,72,64
ex 124
maav 8,104,68
ex 125
maav 12,76,64
cv 91,94,95
cv 92,120,121
maav 12,108,68
ex 126
v_smfmac_f32_16x16x64_bf16 v[144:147],v[228:231],v[64:71],v248
cv 93,122,123
maav 16,80,64
ex 127
maav 16,112,68
buffer_load_dword v247,v245,s[36:39],s85 offen
maav 20,84,64
cv 94,124,125
cv 95,126,127
maav 20,116,68
maav 24,88,64
maav 24,120,68
ra 192,224,32768
maav 28,92,64
ds 255,8,78,1024
maav 28,124,68
maav 32,64,72
ra 196,224,33792
maav 32,96,76
s_add_u32 s85,s85,131072
maav 36,68,72
ra 200,224,34816
maav 36,100,76
maav 40,72,72
ds 255,8,78,2048
maav 40,104,76
maav 44,76,72
ra 204,224,35840
maav 44,108,76
v_smfmac_f32_16x16x64_bf16 v[148:151],v[228:231],v[72:79],v248
ra 208,224,36864
maav 48,80,72
maav 48,112,76
maav 52,84,72
ds 255,8,78,3072
maav 52,116,76
ra 212,224,37888
maav 56,88,72
ex 0
maav 56,120,76
ra 216,224,38912
maav 60,92,72
ex 1
maav 60,124,76
s_mov_b32 m0,s75
ex 2
mvav 160,64,80
ra 220,224,39936
cv 0,0,1
mvav 160,96,84
ds 255,8,79,0
mvav 164,68,80
ex 3
mvav 164,100,84
ra 224,224,40960
cv 1,2,3
mvav 168,72,80
ex 4
mvav 168,104,84
ra 228,224,41984
mvav 172,76,80
ds 255,8,79,1024
mvav 172,108,84
s_add_u32 s46,s46,16384
ex 5
v_smfmac_f32_16x16x64_bf16 v[152:155],v[228:231],v[80:87],v248
ra 232,224,43008
mvav 176,80,80
s_add_u32 s47,s47,32768
ex 6
mvav 176,112,84
s_add_u32 s95,s47,32768
ex 7
mvav 180,84,80
ra 236,224,44032
cv 2,4,5
mvav 180,116,84
ex 32
mvav 184,88,80
ds 255,8,79,2048
mvav 184,120,84
ex 33
mvav 188,92,80
ra 240,224,45056
cv 3,6,7
mvav 188,124,84
ex 34
mvav 192,64,88
ra 244,224,46080
cv 4,32,33
mvav 192,96,92
ex 35
mvav 196,68,88
ex 36
mvav 196,100,92
ds 255,8,79,3072
mvav 200,72,88
ra 248,224,47104
cv 5,34,35
mvav 200,104,92
s_cmp_ge_i32 s47,s46
ex 37
mvav 204,76,88
s_cselect_b32 s78,s95,s46
ex 38
mvav 204,108,92
s_cselect_b32 s79,s95,268435456
ex 39
v_smfmac_f32_16x16x64_bf16 v[156:159],v[228:231],v[88:95],v248
ra 252,224,48128
mvav 208,80,88
s_add_u32 s79,s79,16384
ex 8
mvav 208,112,92
ex 9
mvav 212,84,88
ex 10
mvav 212,116,92
ex 11
mvav 216,88,88
cv 6,36,37
cv 7,38,39
mvav 216,120,92
ex 12
mvav 220,92,88
ex 13
mvav 220,124,92
s_waitcnt vmcnt(16) lgkmcnt(0)
s_barrier
cvaa 64,192,128,232
s_bitcmp0_b32 s82,s81
s_cbranch_scc1 L_loop0
s_bitcmp0_b32 s15,s81
s_cbranch_scc1 L_eva_r0
L_short0:
v_add_f32_e32 v226,v152,v153
v_add_f32_e32 v227,v156,v157
v_subrev_u32_e32 v236,0x23800000,v226
v_subrev_u32_e32 v237,0x23800000,v227
v_max_u32_e32 v236,v236,v237
v_cmp_lt_u32_e64 s[40:41],s48,v236
v_rcp_f32_e32 v226,v226
v_rcp_f32_e32 v227,v227
s_add_u32 s86,s13,4096
v_mov_b32_e32 v236,0
v_mov_b32_e32 v237,0
v_mov_b32_e32 v238,0
v_mov_b32_e32 v239,0
v_mov_b64_e32 v[152:153],0
v_mov_b64_e32 v[156:157],0
v_mul_f32_e32 v226,v242,v226
v_mul_f32_e32 v227,v243,v227
v_add_u32_e32 v225,s94,v225
v_pk_mul_f32 v[160:161],v[160:161],v[226:227] op_sel:[0,0] op_sel_hi:[1,0]
v_pk_mul_f32 v[162:163],v[162:163],v[226:227] op_sel:[0,0] op_sel_hi:[1,0]
v_pk_mul_f32 v[164:165],v[164:165],v[226:227] op_sel:[0,0] op_sel_hi:[1,0]
v_pk_mul_f32 v[166:167],v[166:167],v[226:227] op_sel:[0,0] op_sel_hi:[1,0]
cv 160,160,161
cv 161,162,163
cv 162,164,165
cv 163,166,167
buffer_store_dwordx4 v[160:163],v244,s[8:11],s13 offen offset:0 sc0 sc1
v_pk_mul_f32 v[168:169],v[168:169],v[226:227] op_sel:[0,0] op_sel_hi:[1,0]
v_pk_mul_f32 v[170:171],v[170:171],v[226:227] op_sel:[0,0] op_sel_hi:[1,0]
v_pk_mul_f32 v[172:173],v[172:173],v[226:227] op_sel:[0,0] op_sel_hi:[1,0]
v_pk_mul_f32 v[174:175],v[174:175],v[226:227] op_sel:[0,0] op_sel_hi:[1,0]
cv 168,168,169
cv 169,170,171
cv 170,172,173
cv 171,174,175
buffer_store_dwordx4 v[168:171],v244,s[8:11],s13 offen offset:2048 sc0 sc1
v_pk_mul_f32 v[176:177],v[176:177],v[226:227] op_sel:[0,0] op_sel_hi:[1,0]
v_pk_mul_f32 v[178:179],v[178:179],v[226:227] op_sel:[0,0] op_sel_hi:[1,0]
v_pk_mul_f32 v[180:181],v[180:181],v[226:227] op_sel:[0,0] op_sel_hi:[1,0]
v_pk_mul_f32 v[182:183],v[182:183],v[226:227] op_sel:[0,0] op_sel_hi:[1,0]
cv 176,176,177
cv 177,178,179
cv 178,180,181
v_mfma_f32_32x32x16_bf16 v[160:175],v[236:239],v[236:239],0
cv 179,182,183
buffer_store_dwordx4 v[176:179],v244,s[8:11],s86 offen offset:0 sc0 sc1
v_pk_mul_f32 v[184:185],v[184:185],v[226:227] op_sel:[0,0] op_sel_hi:[1,0]
v_pk_mul_f32 v[186:187],v[186:187],v[226:227] op_sel:[0,0] op_sel_hi:[1,0]
v_pk_mul_f32 v[188:189],v[188:189],v[226:227] op_sel:[0,0] op_sel_hi:[1,0]
v_pk_mul_f32 v[190:191],v[190:191],v[226:227] op_sel:[0,0] op_sel_hi:[1,0]
cv 184,184,185
cv 185,186,187
cv 186,188,189
cv 187,190,191
buffer_store_dwordx4 v[184:187],v244,s[8:11],s86 offen offset:2048 sc0 sc1
v_pk_mul_f32 v[192:193],v[192:193],v[226:227] op_sel:[0,1] op_sel_hi:[1,1]
v_pk_mul_f32 v[194:195],v[194:195],v[226:227] op_sel:[0,1] op_sel_hi:[1,1]
v_pk_mul_f32 v[196:197],v[196:197],v[226:227] op_sel:[0,1] op_sel_hi:[1,1]
v_pk_mul_f32 v[198:199],v[198:199],v[226:227] op_sel:[0,1] op_sel_hi:[1,1]
cv 192,192,193
cv 193,194,195
cv 194,196,197
v_mfma_f32_32x32x16_bf16 v[176:191],v[236:239],v[236:239],0
cv 195,198,199
buffer_store_dwordx4 v[192:195],v244,s[8:11],s13 offen offset:1024 sc0 sc1
v_pk_mul_f32 v[200:201],v[200:201],v[226:227] op_sel:[0,1] op_sel_hi:[1,1]
v_pk_mul_f32 v[202:203],v[202:203],v[226:227] op_sel:[0,1] op_sel_hi:[1,1]
v_pk_mul_f32 v[204:205],v[204:205],v[226:227] op_sel:[0,1] op_sel_hi:[1,1]
v_pk_mul_f32 v[206:207],v[206:207],v[226:227] op_sel:[0,1] op_sel_hi:[1,1]
cv 200,200,201
cv 201,202,203
cv 202,204,205
cv 203,206,207
buffer_store_dwordx4 v[200:203],v244,s[8:11],s13 offen offset:3072 sc0 sc1
v_pk_mul_f32 v[208:209],v[208:209],v[226:227] op_sel:[0,1] op_sel_hi:[1,1]
v_pk_mul_f32 v[210:211],v[210:211],v[226:227] op_sel:[0,1] op_sel_hi:[1,1]
v_pk_mul_f32 v[212:213],v[212:213],v[226:227] op_sel:[0,1] op_sel_hi:[1,1]
v_pk_mul_f32 v[214:215],v[214:215],v[226:227] op_sel:[0,1] op_sel_hi:[1,1]
cv 208,208,209
cv 209,210,211
cv 210,212,213
v_mfma_f32_32x32x16_bf16 v[192:207],v[236:239],v[236:239],0
cv 211,214,215
buffer_store_dwordx4 v[208:211],v244,s[8:11],s86 offen offset:1024 sc0 sc1
v_pk_mul_f32 v[216:217],v[216:217],v[226:227] op_sel:[0,1] op_sel_hi:[1,1]
v_pk_mul_f32 v[218:219],v[218:219],v[226:227] op_sel:[0,1] op_sel_hi:[1,1]
v_pk_mul_f32 v[220:221],v[220:221],v[226:227] op_sel:[0,1] op_sel_hi:[1,1]
v_pk_mul_f32 v[222:223],v[222:223],v[226:227] op_sel:[0,1] op_sel_hi:[1,1]
cv 216,216,217
cv 217,218,219
cv 218,220,221
cv 219,222,223
buffer_store_dwordx4 v[216:219],v244,s[8:11],s86 offen offset:3072 sc0 sc1
s_nop 1
v_mfma_f32_32x32x16_bf16 v[208:223],v[236:239],v[236:239],0
L_eva_r0:
s_cmp_lg_u32 s81,14
s_cbranch_scc1 L_loop0
s_add_u32 s81,s81,1
s_bitcmp1_b32 170,s20
s_cbranch_scc1 L_odd
s_branch L_last
L_k_ec1:
buffer_store_dwordx4 a[252:255],v244,s[8:11],s35 offen offset:1024 sc0 sc1
s_branch L_k_ecr1
L_q_ack:
s_mov_b32 s86,65536
L_q_ack2:
s_sleep 8
buffer_load_dword v246,off,s[8:11],s35 offset:1024 sc1
)"
R"(s_waitcnt vmcnt(0)
v_readfirstlane_b32 s87,v246
s_nop 0
s_cmp_eq_u32 s87,s32
s_cbranch_scc1 L_q_acked
s_sub_u32 s86,s86,1
s_cbranch_scc0 L_q_ack2
s_branch L_q_acked
L_q_spin:
s_mov_b32 s86,65536
L_q_spin1:
s_sleep 8
buffer_load_dword v248,v250,s[8:11],0 offen sc1
s_or_b32 s87,%1,s12
s_cbranch_scc1 L_q_spin2
buffer_load_dwordx4 a[252:255],v244,s[8:11],s35 offen sc1
s_waitcnt vmcnt(0)
buffer_store_dwordx4 a[252:255],v244,s[8:11],s35 offen offset:1024 sc0 sc1
L_q_spin2:
s_waitcnt vmcnt(0)
v_and_b32_e32 v248,v242,v248
v_cmp_ne_u32_e64 s[64:65],v243,v248
s_nop 1
s_cmp_eq_u64 s[64:65],0
s_cbranch_scc1 L_q_ok
s_sub_u32 s86,s86,1
s_cbranch_scc0 L_q_spin1
s_branch L_q_ok
L_bx:
s_mov_b32 s87,65536
L_bx1:
s_sleep 2
buffer_load_dwordx4 v[176:179],v244,s[8:11],s98 offen sc1
buffer_load_dwordx2 v[168:169],v253,s[8:11],s98 offen offset:1024 sc1
buffer_load_dwordx2 v[172:173],v253,s[8:11],s98 offen offset:1536 sc1
s_waitcnt vmcnt(0)
v_max3_u32 v180,v176,v177,v178
v_max3_u32 v180,v180,v179,v168
v_max3_u32 v180,v180,v169,v172
v_max_u32_e32 v180,v180,v173
v_cmp_eq_u32_e64 s[40:41],-1,v180
s_nop 1
s_cmp_eq_u64 s[40:41],0
s_cbranch_scc1 L_bxr
s_sub_u32 s87,s87,1
s_cbranch_scc0 L_bx1
s_branch L_bxr
L_k_ev:
L_v_ev:
buffer_load_dwordx4 a[252:255],v244,s[8:11],s35 offen sc1
s_branch L_k_evr
.fill 194,4,0xbf800000
L_cw1:
s_mov_b64 exec,-1
s_waitcnt lgkmcnt(0)
s_barrier
cvaa 0,192,128,232
mvaa 0,200,132
s_add_u32 s81,s81,1
ex 78
mvaa 0,208,136
s_add_u32 s44,s44,1024
ex 79
mvaa 0,216,140
cv 72,72,73
cv 73,74,75
cvaa 4,196,128,232
ex 104
mvaa 4,204,132
s_mov_b32 m0,s68
ex 105
mvaa 4,212,136
ds 249,8,76,0
mvaa 4,220,140
ex 106
cvaa 32,224,128,232
ra 64,225,32768
cv 74,76,77
mvaa 32,232,132
ex 107
mvaa 32,240,136
ra 68,225,33792
cv 75,78,79
mvaa 32,248,140
ex 108
cvaa 36,228,128,232
ex 109
mvaa 36,236,132
ds 249,8,76,1024
mvaa 36,244,136
ra 72,225,34816
cv 76,104,105
mvaa 36,252,140
ex 110
cvaa 8,192,144,232
ra 76,225,35840
cv 77,106,107
mvaa 8,200,148
ex 111
mvaa 8,208,152
ex 80
mvaa 8,216,156
ra 80,225,36864
cv 78,108,109
cvaa 12,196,144,232
ds 249,8,76,2048
mvaa 12,204,148
ex 81
mvaa 12,212,152
ra 84,225,37888
cv 79,110,111
mvaa 12,220,156
ex 82
cvaa 40,224,144,232
ex 83
mvaa 40,232,148
ra 88,225,38912
cv 80,80,81
mvaa 40,240,152
ex 84
mvaa 40,248,156
ds 249,8,76,3072
cvaa 44,228,144,232
ra 92,225,39936
cv 81,82,83
mvaa 44,236,148
ex 85
mvaa 44,244,152
ex 86
mvaa 44,252,156
ra 96,225,40960
cv 82,84,85
cvaa 16,192,160,232
ex 87
mvaa 16,200,164
s_mov_b32 m0,s69
ex 112
mvaa 16,208,168
ra 100,225,41984
cv 83,86,87
mvaa 16,216,172
ds 249,8,77,0
cvaa 20,196,160,232
ex 113
mvaa 20,204,164
ra 104,225,43008
cv 84,112,113
mvaa 20,212,168
ex 114
mvaa 20,220,172
ex 115
cvaa 48,224,160,232
ra 108,225,44032
cv 85,114,115
mvaa 48,232,164
ex 116
mvaa 48,240,168
ds 249,8,77,1024
mvaa 48,248,172
ra 112,225,45056
cvaa 52,228,160,232
ex 117
mvaa 52,236,164
ex 118
mvaa 52,244,168
ra 116,225,46080
cv 86,116,117
mvaa 52,252,172
ex 119
cvaa 24,192,176,232
ra 120,225,47104
cv 87,118,119
mvaa 24,200,180
ds 249,8,77,2048
mvaa 24,208,184
ex 88
mvaa 24,216,188
ex 89
cvaa 28,196,176,232
ra 124,225,48128
cv 88,88,89
mvaa 28,204,180
ex 90
mvaa 28,212,184
ex 91
mvaa 28,220,188
ex 92
cvaa 56,224,176,232
ds 249,8,77,3072
mvaa 56,232,180
ex 93
mvaa 56,240,184
ex 94
mvaa 56,248,188
ex 95
cvaa 60,228,176,232
mvaa 60,236,180
ex 120
mvaa 60,244,184
ex 121
mvaa 60,252,188
s_waitcnt vmcnt(8) lgkmcnt(0)
s_barrier
maav 0,64,64
cv 89,90,91
cv 90,92,93
maav 0,96,68
ex 122
maav 4,68,64
s_mov_b32 m0,s74
ex 123
maav 4,100,68
ds 255,8,78,0
maav 8,72,64
ex 124
maav 8,104,68
ex 125
maav 12,76,64
cv 91,94,95
cv 92,120,121
maav 12,108,68
ex 126
v_smfmac_f32_16x16x64_bf16 v[144:147],v[228:231],v[64:71],v248
cv 93,122,123
maav 16,80,64
ex 127
maav 16,112,68
cv 94,124,125
cv 95,126,127
maav 20,84,64
buffer_load_dword v247,v245,s[36:39],s85 offen
maav 20,116,68
maav 24,88,64
maav 24,120,68
ra 192,224,32768
maav 28,92,64
maav 28,124,68
ds 255,8,78,1024
maav 32,64,72
ra 196,224,33792
maav 32,96,76
s_add_u32 s85,s85,131072
maav 36,68,72
ra 200,224,34816
maav 36,100,76
maav 40,72,72
maav 40,104,76
ds 255,8,78,2048
maav 44,76,72
ra 204,224,35840
maav 44,108,76
v_smfmac_f32_16x16x64_bf16 v[148:151],v[228:231],v[72:79],v248
ra 208,224,36864
maav 48,80,72
maav 48,112,76
maav 52,84,72
ra 212,224,37888
maav 52,116,76
ds 255,8,78,3072
maav 56,88,72
ex 0
maav 56,120,76
ra 216,224,38912
maav 60,92,72
ex 1
maav 60,124,76
ex 2
zvav 160,64,80
ra 220,224,39936
cv 0,0,1
mvav 160,96,84
s_mov_b32 m0,s75
ex 3
zvav 164,68,80
ds 255,8,79,0
mvav 164,100,84
ra 224,224,40960
cv 1,2,3
zvav 168,72,80
ex 4
mvav 168,104,84
ra 228,224,41984
zvav 172,76,80
s_add_u32 s46,s46,16384
ex 5
mvav 172,108,84
ds 255,8,79,1024
v_smfmac_f32_16x16x64_bf16 v[152:155],v[228:231],v[80:87],v248
ra 232,224,43008
zvav 176,80,80
s_add_u32 s47,s47,32768
ex 6
mvav 176,112,84
s_add_u32 s95,s47,32768
ex 7
zvav 180,84,80
ra 236,224,44032
cv 2,4,5
mvav 180,116,84
ex 32
zvav 184,88,80
ex 33
mvav 184,120,84
ds 255,8,79,2048
zvav 188,92,80
ra 240,224,45056
cv 3,6,7
mvav 188,124,84
ex 34
mvav 192,64,88
ra 244,224,46080
cv 4,32,33
mvav 192,96,92
ex 35
mvav 196,68,88
ex 36
mvav 196,100,92
ra 248,224,47104
cv 5,34,35
mvav 200,72,88
ds 255,8,79,3072
mvav 200,104,92
s_cmp_ge_i32 s47,s46
ex 37
mvav 204,76,88
s_cselect_b32 s78,s95,s46
ex 38
mvav 204,108,92
s_cselect_b32 s79,s95,268435456
ex 39
v_smfmac_f32_16x16x64_bf16 v[156:159],v[228:231],v[88:95],v248
ra 252,224,48128
mvav 208,80,88
s_add_u32 s79,s79,16384
ex 8
mvav 208,112,92
ex 9
mvav 212,84,88
ex 10
mvav 212,116,92
ex 11
mvav 216,88,88
cv 6,36,37
cv 7,38,39
mvav 216,120,92
ex 12
mvav 220,92,88
ex 13
mvav 220,124,92
s_waitcnt vmcnt(16) lgkmcnt(0)
s_barrier
cvaa 64,192,128,232
s_bitcmp0_b32 s82,s81
s_cbranch_scc1 L_loop1
s_bitcmp1_b32 s15,s81
s_cbranch_scc1 L_short1
.p2align 6
L_loop1:
mvaa 64,200,132
s_add_u32 s81,s81,1
ex 14
mvaa 64,208,136
s_add_u32 s44,s44,1024
ex 15
mvaa 64,216,140
cv 8,8,9
cv 9,10,11
cvaa 68,196,128,232
ex 40
mvaa 68,204,132
s_mov_b32 m0,s70
ex 41
mvaa 68,212,136
ds 249,8,78,0
mvaa 68,220,140
ex 42
cvaa 96,224,128,232
ra 64,225,0
cv 10,12,13
mvaa 96,232,132
ex 43
mvaa 96,240,136
ra 68,225,1024
cv 11,14,15
mvaa 96,248,140
ex 44
cvaa 100,228,128,232
ex 45
mvaa 100,236,132
ds 249,8,78,1024
mvaa 100,244,136
ra 72,225,2048
cv 12,40,41
mvaa 100,252,140
ex 46
cvaa 72,192,144,232
ra 76,225,3072
cv 13,42,43
mvaa 72,200,148
ex 47
mvaa 72,208,152
ex 16
mvaa 72,216,156
ra 80,225,4096
cv 14,44,45
cvaa 76,196,144,232
ds 249,8,78,2048
mvaa 76,204,148
ex 17
mvaa 76,212,152
ra 84,225,5120
cv 15,46,47
mvaa 76,220,156
ex 18
cvaa 104,224,144,232
ex 19
mvaa 104,232,148
ra 88,225,6144
cv 16,16,17
mvaa 104,240,152
ex 20
mvaa 104,248,156
ds 249,8,78,3072
cvaa 108,228,144,232
ra 92,225,7168
cv 17,18,19
mvaa 108,236,148
ex 21
mvaa 108,244,152
ex 22
mvaa 108,252,156
ra 96,225,8192
cv 18,20,21
cvaa 80,192,160,232
ex 23
mvaa 80,200,164
s_mov_b32 m0,s71
ex 48
mvaa 80,208,168
ra 100,225,9216
cv 19,22,23
mvaa 80,216,172
ds 249,8,79,0
cvaa 84,196,160,232
ex 49
mvaa 84,204,164
ra 104,225,10240
cv 20,48,49
mvaa 84,212,168
ex 50
mvaa 84,220,172
ex 51
cvaa 112,224,160,232
ra 108,225,11264
cv 21,50,51
mvaa 112,232,164
ex 52
mvaa 112,240,168
ds 249,8,79,1024
mvaa 112,248,172
ra 112,225,12288
cvaa 116,228,160,232
ex 53
mvaa 116,236,164
ex 54
mvaa 116,244,168
ra 116,225,13312
cv 22,52,53
mvaa 116,252,172
ex 55
cvaa 88,192,176,232
ra 120,225,14336
cv 23,54,55
mvaa 88,200,180
ds 249,8,79,2048
mvaa 88,208,184
ex 24
mvaa 88,216,188
ex 25
cvaa 92,196,176,232
ra 124,225,15360
cv 24,24,25
mvaa 92,204,180
ex 26
mvaa 92,212,184
ex 27
mvaa 92,220,188
ex 28
cvaa 120,224,176,232
ds 249,8,79,3072
mvaa 120,232,180
ex 29
mvaa 120,240,184
ex 30
mvaa 120,248,188
ex 31
cvaa 124,228,176,232
buffer_store_dwordx4 v[250:253],v244,s[8:11],s44 offen sc0 sc1
mvaa 124,236,180
ex 56
mvaa 124,244,184
ex 57
mvaa 124,252,188
s_waitcnt vmcnt(16) lgkmcnt(0)
s_barrier
maav 0,64,0
cv 25,26,27
cv 26,28,29
maav 0,96,4
ex 58
maav 4,68,0
s_mov_b32 m0,s72
ex 59
maav 4,100,4
ds 255,8,76,0
maav 8,72,0
ex 60
maav 8,104,4
ex 61
maav 12,76,0
s_cmp_lg_u32 s80,s81
cv 27,30,31
cv 28,56,57
maav 12,108,4
ex 62
v_smfmac_f32_16x16x64_bf16 v[144:147],v[228:231],v[0:7],v248
cv 29,58,59
maav 16,80,0
ex 63
maav 16,112,4
cv 30,60,61
cv 31,62,63
maav 20,84,0
buffer_load_dword v247,v246,s[24:27],s49 offen
maav 20,116,4
maav 24,88,0
s_cbranch_scc1 L_evb_r1
v_add_u32_e32 v224,s94,v224
s_lshl_b32 s86,s12,12
s_add_u32 s86,s86,131072
v_add_u32_e32 v236,s86,v244
ra 160,236,0
ra 164,236,1024
ra 168,236,2048
ra 172,236,3072
ra 176,236,16384
ra 180,236,17408
ra 184,236,18432
ra 188,236,19456
L_evb_r1:
maav 24,120,4
ra 192,224,0
maav 28,92,0
maav 28,124,4
ds 255,8,76,1024
maav 32,64,8
ra 196,224,1024
maav 32,96,12
s_add_u32 s49,s49,32768
maav 36,68,8
ra 200,224,2048
maav 36,100,12
maav 40,72,8
maav 40,104,12
ds 255,8,76,2048
maav 44,76,8
ra 204,224,3072
maav 44,108,12
v_smfmac_f32_16x16x64_bf16 v[148:151],v[228:231],v[8:15],v248
ra 208,224,4096
maav 48,80,8
maav 48,112,12
maav 52,84,8
ra 212,224,5120
maav 52,116,12
ds 255,8,76,3072
maav 56,88,8
ex 64
maav 56,120,12
ra 216,224,6144
maav 60,92,8
ex 65
maav 60,124,12
ex 66
mvav 160,64,16
ra 220,224,7168
cv 64,64,65
mvav 160,96,20
s_mov_b32 m0,s73
ex 67
mvav 164,68,16
ds 255,8,77,0
mvav 164,100,20
ra 224,224,8192
cv 65,66,67
mvav 168,72,16
ex 68
mvav 168,104,20
ra 228,224,9216
mvav 172,76,16
s_add_u32 s46,s46,16384
ex 69
mvav 172,108,20
ds 255,8,77,1024
v_smfmac_f32_16x16x64_bf16 v[152:155],v[228:231],v[16:23],v248
ra 232,224,10240
mvav 176,80,16
s_add_u32 s47,s47,32768
ex 70
mvav 176,112,20
s_add_u32 s95,s47,32768
ex 71
mvav 180,84,16
ra 236,224,11264
cv 66,68,69
mvav 180,116,20
ex 96
mvav 184,88,16
ex 97
mvav 184,120,20
ds 255,8,77,2048
mvav 188,92,16
ra 240,224,12288
cv 67,70,71
mvav 188,124,20
ex 98
mvav 192,64,24
ra 244,224,13312
cv 68,96,97
mvav 192,96,28
ex 99
mvav 196,68,24
ex 100
mvav 196,100,28
ra 248,224,14336
cv 69,98,99
mvav 200,72,24
ds 255,8,77,3072
mvav 200,104,28
s_cmp_ge_i32 s47,s46
ex 101
mvav 204,76,24
s_cselect_b32 s76,s95,s46
ex 102
mvav 204,108,28
s_cselect_b32 s77,s95,268435456
ex 103
v_smfmac_f32_16x16x64_bf16 v[156:159],v[228:231],v[24:31],v248
ra 252,224,15360
mvav 208,80,24
s_add_u32 s77,s77,16384
ex 72
mvav 208,112,28
ex 73
mvav 212,84,24
ex 74
mvav 212,116,28
ex 75
mvav 216,88,24
cv 70,100,101
cv 71,102,103
mvav 216,120,28
ex 76
mvav 220,92,24
ex 77
mvav 220,124,28
s_waitcnt vmcnt(16) lgkmcnt(0)
s_barrier
cvaa 0,192,128,232
mvaa 0,200,132
s_add_u32 s81,s81,1
ex 78
mvaa 0,208,136
s_add_u32 s44,s44,1024
ex 79
mvaa 0,216,140
cv 72,72,73
cv 73,74,75
cvaa 4,196,128,232
ex 104
mvaa 4,204,132
s_mov_b32 m0,s68
ex 105
mvaa 4,212,136
ds 249,8,76,0
mvaa 4,220,140
ex 106
cvaa 32,224,128,232
ra 64,225,32768
cv 74,76,77
mvaa 32,232,132
ex 107
mvaa 32,240,136
ra 68,225,33792
cv 75,78,79
mvaa 32,248,140
ex 108
cvaa 36,228,128,232
ex 109
mvaa 36,236,132
ds 249,8,76,1024
mvaa 36,244,136
ra 72,225,34816
cv 76,104,105
mvaa 36,252,140
ex 110
cvaa 8,192,144,232
ra 76,225,35840
cv 77,106,107
mvaa 8,200,148
ex 111
mvaa 8,208,152
ex 80
mvaa 8,216,156
ra 80,225,36864
cv 78,108,109
cvaa 12,196,144,232
ds 249,8,76,2048
mvaa 12,204,148
ex 81
mvaa 12,212,152
ra 84,225,37888
cv 79,110,111
mvaa 12,220,156
ex 82
cvaa 40,224,144,232
ex 83
mvaa 40,232,148
ra 88,225,38912
cv 80,80,81
mvaa 40,240,152
ex 84
mvaa 40,248,156
ds 249,8,76,3072
cvaa 44,228,144,232
ra 92,225,39936
cv 81,82,83
mvaa 44,236,148
ex 85
mvaa 44,244,152
ex 86
mvaa 44,252,156
ra 96,225,40960
cv 82,84,85
cvaa 16,192,160,232
ex 87
mvaa 16,200,164
s_mov_b32 m0,s69
ex 112
mvaa 16,208,168
ra 100,225,41984
cv 83,86,87
mvaa 16,216,172
ds 249,8,77,0
cvaa 20,196,160,232
ex 113
mvaa 20,204,164
ra 104,225,43008
cv 84,112,113
mvaa 20,212,168
ex 114
mvaa 20,220,172
ex 115
cvaa 48,224,160,232
ra 108,225,44032
cv 85,114,115
mvaa 48,232,164
ex 116
mvaa 48,240,168
ds 249,8,77,1024
mvaa 48,248,172
ra 112,225,45056
cvaa 52,228,160,232
ex 117
mvaa 52,236,164
ex 118
mvaa 52,244,168
ra 116,225,46080
cv 86,116,117
mvaa 52,252,172
ex 119
cvaa 24,192,176,232
ra 120,225,47104
cv 87,118,119
mvaa 24,200,180
ds 249,8,77,2048
mvaa 24,208,184
ex 88
mvaa 24,216,188
ex 89
cvaa 28,196,176,232
ra 124,225,48128
cv 88,88,89
mvaa 28,204,180
ex 90
mvaa 28,212,184
ex 91
mvaa 28,220,188
ex 92
cvaa 56,224,176,232
ds 249,8,77,3072
mvaa 56,232,180
ex 93
mvaa 56,240,184
ex 94
mvaa 56,248,188
ex 95
cvaa 60,228,176,232
buffer_store_dwordx4 v[250:253],v244,s[8:11],s44 offen sc0 sc1
mvaa 60,236,180
ex 120
mvaa 60,244,184
ex 121
mvaa 60,252,188
s_waitcnt vmcnt(16) lgkmcnt(0)
s_barrier
maav 0,64,64
cv 89,90,91
cv 90,92,93
maav 0,96,68
ex 122
maav 4,68,64
s_mov_b32 m0,s74
ex 123
maav 4,100,68
ds 255,8,78,0
maav 8,72,64
ex 124
maav 8,104,68
ex 125
maav 12,76,64
cv 91,94,95
cv 92,120,121
maav 12,108,68
ex 126
v_smfmac_f32_16x16x64_bf16 v[144:147],v[228:231],v[64:71],v248
cv 93,122,123
maav 16,80,64
ex 127
maav 16,112,68
cv 94,124,125
cv 95,126,127
maav 20,84,64
buffer_load_dword v247,v245,s[36:39],s85 offen
maav 20,116,68
maav 24,88,64
maav 24,120,68
ra 192,224,32768
maav 28,92,64
maav 28,124,68
ds 255,8,78,1024
maav 32,64,72
ra 196,224,33792
maav 32,96,76
s_add_u32 s85,s85,131072
maav 36,68,72
ra 200,224,34816
maav 36,100,76
maav 40,72,72
maav 40,104,76
ds 255,8,78,2048
maav 44,76,72
ra 204,224,35840
maav 44,108,76
v_smfmac_f32_16x16x64_bf16 v[148:151],v[228:231],v[72:79],v248
ra 208,224,36864
maav 48,80,72
maav 48,112,76
maav 52,84,72
ra 212,224,37888
maav 52,116,76
ds 255,8,78,3072
maav 56,88,72
ex 0
maav 56,120,76
ra 216,224,38912
maav 60,92,72
ex 1
maav 60,124,76
ex 2
mvav 160,64,80
ra 220,224,39936
cv 0,0,1
mvav 160,96,84
s_mov_b32 m0,s75
ex 3
mvav 164,68,80
ds 255,8,79,0
mvav 164,100,84
ra 224,224,40960
cv 1,2,3
mvav 168,72,80
ex 4
mvav 168,104,84
ra 228,224,41984
mvav 172,76,80
s_add_u32 s46,s46,16384
ex 5
mvav 172,108,84
ds 255,8,79,1024
v_smfmac_f32_16x16x64_bf16 v[152:155],v[228:231],v[80:87],v248
ra 232,224,43008
mvav 176,80,80
s_add_u32 s47,s47,32768
ex 6
mvav 176,112,84
s_add_u32 s95,s47,32768
ex 7
mvav 180,84,80
ra 236,224,44032
cv 2,4,5
mvav 180,116,84
ex 32
mvav 184,88,80
ex 33
mvav 184,120,84
ds 255,8,79,2048
mvav 188,92,80
ra 240,224,45056
cv 3,6,7
mvav 188,124,84
ex 34
mvav 192,64,88
ra 244,224,46080
cv 4,32,33
mvav 192,96,92
ex 35
mvav 196,68,88
ex 36
mvav 196,100,92
ra 248,224,47104
cv 5,34,35
mvav 200,72,88
ds 255,8,79,3072
mvav 200,104,92
s_cmp_ge_i32 s47,s46
ex 37
mvav 204,76,88
s_cselect_b32 s78,s95,s46
ex 38
mvav 204,108,92
s_cselect_b32 s79,s95,268435456
ex 39
v_smfmac_f32_16x16x64_bf16 v[156:159],v[228:231],v[88:95],v248
ra 252,224,48128
mvav 208,80,88
s_add_u32 s79,s79,16384
ex 8
mvav 208,112,92
ex 9
mvav 212,84,88
ex 10
mvav 212,116,92
ex 11
mvav 216,88,88
cv 6,36,37
cv 7,38,39
mvav 216,120,92
ex 12
mvav 220,92,88
ex 13
mvav 220,124,92
s_waitcnt vmcnt(16) lgkmcnt(0)
s_barrier
cvaa 64,192,128,232
s_bitcmp0_b32 s82,s81
s_cbranch_scc1 L_loop1
s_bitcmp0_b32 s15,s81
s_cbranch_scc1 L_eva_r1
L_short1:
v_add_f32_e32 v226,v152,v153
v_add_f32_e32 v227,v156,v157
v_subrev_u32_e32 v236,0x23800000,v226
v_subrev_u32_e32 v237,0x23800000,v227
v_max_u32_e32 v236,v236,v237
v_cmp_lt_u32_e64 s[40:41],s48,v236
v_rcp_f32_e32 v226,v226
v_rcp_f32_e32 v227,v227
s_add_u32 s86,s13,4096
v_mov_b32_e32 v236,0
v_mov_b32_e32 v237,0
v_mov_b32_e32 v238,0
v_mov_b32_e32 v239,0
v_mov_b64_e32 v[152:153],0
v_mov_b64_e32 v[156:157],0
v_mul_f32_e32 v226,v242,v226
v_mul_f32_e32 v227,v243,v227
v_add_u32_e32 v225,s94,v225
v_pk_mul_f32 v[160:161],v[160:161],v[226:227] op_sel:[0,0] op_sel_hi:[1,0]
v_pk_mul_f32 v[162:163],v[162:163],v[226:227] op_sel:[0,0] op_sel_hi:[1,0]
v_pk_mul_f32 v[164:165],v[164:165],v[226:227] op_sel:[0,0] op_sel_hi:[1,0]
v_pk_mul_f32 v[166:167],v[166:167],v[226:227] op_sel:[0,0] op_sel_hi:[1,0]
cv 160,160,161
cv 161,162,163
cv 162,164,165
cv 163,166,167
buffer_store_dwordx4 v[160:163],v244,s[8:11],s13 offen offset:0 sc0 sc1
v_pk_mul_f32 v[168:169],v[168:169],v[226:227] op_sel:[0,0] op_sel_hi:[1,0]
v_pk_mul_f32 v[170:171],v[170:171],v[226:227] op_sel:[0,0] op_sel_hi:[1,0]
v_pk_mul_f32 v[172:173],v[172:173],v[226:227] op_sel:[0,0] op_sel_hi:[1,0]
)"
R"(v_pk_mul_f32 v[174:175],v[174:175],v[226:227] op_sel:[0,0] op_sel_hi:[1,0]
cv 168,168,169
cv 169,170,171
cv 170,172,173
cv 171,174,175
buffer_store_dwordx4 v[168:171],v244,s[8:11],s13 offen offset:2048 sc0 sc1
v_pk_mul_f32 v[176:177],v[176:177],v[226:227] op_sel:[0,0] op_sel_hi:[1,0]
v_pk_mul_f32 v[178:179],v[178:179],v[226:227] op_sel:[0,0] op_sel_hi:[1,0]
v_pk_mul_f32 v[180:181],v[180:181],v[226:227] op_sel:[0,0] op_sel_hi:[1,0]
v_pk_mul_f32 v[182:183],v[182:183],v[226:227] op_sel:[0,0] op_sel_hi:[1,0]
cv 176,176,177
cv 177,178,179
cv 178,180,181
v_mfma_f32_32x32x16_bf16 v[160:175],v[236:239],v[236:239],0
cv 179,182,183
buffer_store_dwordx4 v[176:179],v244,s[8:11],s86 offen offset:0 sc0 sc1
v_pk_mul_f32 v[184:185],v[184:185],v[226:227] op_sel:[0,0] op_sel_hi:[1,0]
v_pk_mul_f32 v[186:187],v[186:187],v[226:227] op_sel:[0,0] op_sel_hi:[1,0]
v_pk_mul_f32 v[188:189],v[188:189],v[226:227] op_sel:[0,0] op_sel_hi:[1,0]
v_pk_mul_f32 v[190:191],v[190:191],v[226:227] op_sel:[0,0] op_sel_hi:[1,0]
cv 184,184,185
cv 185,186,187
cv 186,188,189
cv 187,190,191
buffer_store_dwordx4 v[184:187],v244,s[8:11],s86 offen offset:2048 sc0 sc1
v_pk_mul_f32 v[192:193],v[192:193],v[226:227] op_sel:[0,1] op_sel_hi:[1,1]
v_pk_mul_f32 v[194:195],v[194:195],v[226:227] op_sel:[0,1] op_sel_hi:[1,1]
v_pk_mul_f32 v[196:197],v[196:197],v[226:227] op_sel:[0,1] op_sel_hi:[1,1]
v_pk_mul_f32 v[198:199],v[198:199],v[226:227] op_sel:[0,1] op_sel_hi:[1,1]
cv 192,192,193
cv 193,194,195
cv 194,196,197
v_mfma_f32_32x32x16_bf16 v[176:191],v[236:239],v[236:239],0
cv 195,198,199
buffer_store_dwordx4 v[192:195],v244,s[8:11],s13 offen offset:1024 sc0 sc1
v_pk_mul_f32 v[200:201],v[200:201],v[226:227] op_sel:[0,1] op_sel_hi:[1,1]
v_pk_mul_f32 v[202:203],v[202:203],v[226:227] op_sel:[0,1] op_sel_hi:[1,1]
v_pk_mul_f32 v[204:205],v[204:205],v[226:227] op_sel:[0,1] op_sel_hi:[1,1]
v_pk_mul_f32 v[206:207],v[206:207],v[226:227] op_sel:[0,1] op_sel_hi:[1,1]
cv 200,200,201
cv 201,202,203
cv 202,204,205
cv 203,206,207
buffer_store_dwordx4 v[200:203],v244,s[8:11],s13 offen offset:3072 sc0 sc1
v_pk_mul_f32 v[208:209],v[208:209],v[226:227] op_sel:[0,1] op_sel_hi:[1,1]
v_pk_mul_f32 v[210:211],v[210:211],v[226:227] op_sel:[0,1] op_sel_hi:[1,1]
v_pk_mul_f32 v[212:213],v[212:213],v[226:227] op_sel:[0,1] op_sel_hi:[1,1]
v_pk_mul_f32 v[214:215],v[214:215],v[226:227] op_sel:[0,1] op_sel_hi:[1,1]
cv 208,208,209
cv 209,210,211
cv 210,212,213
v_mfma_f32_32x32x16_bf16 v[192:207],v[236:239],v[236:239],0
cv 211,214,215
buffer_store_dwordx4 v[208:211],v244,s[8:11],s86 offen offset:1024 sc0 sc1
v_pk_mul_f32 v[216:217],v[216:217],v[226:227] op_sel:[0,1] op_sel_hi:[1,1]
v_pk_mul_f32 v[218:219],v[218:219],v[226:227] op_sel:[0,1] op_sel_hi:[1,1]
v_pk_mul_f32 v[220:221],v[220:221],v[226:227] op_sel:[0,1] op_sel_hi:[1,1]
v_pk_mul_f32 v[222:223],v[222:223],v[226:227] op_sel:[0,1] op_sel_hi:[1,1]
cv 216,216,217
cv 217,218,219
cv 218,220,221
cv 219,222,223
buffer_store_dwordx4 v[216:219],v244,s[8:11],s86 offen offset:3072 sc0 sc1
s_nop 1
v_mfma_f32_32x32x16_bf16 v[208:223],v[236:239],v[236:239],0
L_eva_r1:
s_cmp_lg_u32 s81,14
s_cbranch_scc1 L_loop1
s_add_u32 s81,s81,1
s_bitcmp1_b32 170,s20
s_cbranch_scc1 L_odd
s_branch L_last
.fill 276,4,0xbf800000
L_cw2:
s_mov_b64 exec,-1
s_waitcnt lgkmcnt(0)
s_barrier
cvaa 0,192,128,232
mvaa 0,200,132
s_add_u32 s81,s81,1
ex 78
mvaa 0,208,136
s_add_u32 s44,s44,1024
ex 79
mvaa 0,216,140
cv 72,72,73
cv 73,74,75
cvaa 4,196,128,232
ex 104
mvaa 4,204,132
ex 105
mvaa 4,212,136
s_mov_b32 m0,s68
ex 106
mvaa 4,220,140
ds 249,8,76,0
cvaa 32,224,128,232
ra 64,225,32768
cv 74,76,77
mvaa 32,232,132
ex 107
mvaa 32,240,136
ra 68,225,33792
cv 75,78,79
mvaa 32,248,140
ex 108
cvaa 36,228,128,232
ex 109
mvaa 36,236,132
ra 72,225,34816
cv 76,104,105
mvaa 36,244,136
ds 249,8,76,1024
mvaa 36,252,140
ex 110
cvaa 8,192,144,232
ra 76,225,35840
cv 77,106,107
mvaa 8,200,148
ex 111
mvaa 8,208,152
ex 80
mvaa 8,216,156
ra 80,225,36864
cv 78,108,109
cvaa 12,196,144,232
ex 81
mvaa 12,204,148
ds 249,8,76,2048
mvaa 12,212,152
ra 84,225,37888
cv 79,110,111
mvaa 12,220,156
ex 82
cvaa 40,224,144,232
ex 83
mvaa 40,232,148
ra 88,225,38912
cv 80,80,81
mvaa 40,240,152
ex 84
mvaa 40,248,156
ra 92,225,39936
cv 81,82,83
cvaa 44,228,144,232
ds 249,8,76,3072
mvaa 44,236,148
ex 85
mvaa 44,244,152
ex 86
mvaa 44,252,156
ra 96,225,40960
cv 82,84,85
cvaa 16,192,160,232
ex 87
mvaa 16,200,164
ex 112
mvaa 16,208,168
ra 100,225,41984
cv 83,86,87
mvaa 16,216,172
s_mov_b32 m0,s69
ex 113
cvaa 20,196,160,232
ds 249,8,77,0
mvaa 20,204,164
ra 104,225,43008
cv 84,112,113
mvaa 20,212,168
ex 114
mvaa 20,220,172
ex 115
cvaa 48,224,160,232
ra 108,225,44032
cv 85,114,115
mvaa 48,232,164
ex 116
mvaa 48,240,168
ra 112,225,45056
mvaa 48,248,172
ds 249,8,77,1024
cvaa 52,228,160,232
ex 117
mvaa 52,236,164
ex 118
mvaa 52,244,168
ra 116,225,46080
cv 86,116,117
mvaa 52,252,172
ex 119
cvaa 24,192,176,232
ra 120,225,47104
cv 87,118,119
mvaa 24,200,180
ex 88
mvaa 24,208,184
ds 249,8,77,2048
mvaa 24,216,188
ex 89
cvaa 28,196,176,232
ra 124,225,48128
cv 88,88,89
mvaa 28,204,180
ex 90
mvaa 28,212,184
ex 91
mvaa 28,220,188
ex 92
cvaa 56,224,176,232
ex 93
mvaa 56,232,180
ds 249,8,77,3072
mvaa 56,240,184
ex 94
mvaa 56,248,188
ex 95
cvaa 60,228,176,232
ex 120
mvaa 60,236,180
mvaa 60,244,184
ex 121
mvaa 60,252,188
s_waitcnt vmcnt(8) lgkmcnt(0)
s_barrier
maav 0,64,64
cv 89,90,91
cv 90,92,93
maav 0,96,68
ex 122
maav 4,68,64
ex 123
maav 4,100,68
s_mov_b32 m0,s74
ex 124
maav 8,72,64
ds 255,8,78,0
maav 8,104,68
ex 125
maav 12,76,64
cv 91,94,95
cv 92,120,121
maav 12,108,68
ex 126
v_smfmac_f32_16x16x64_bf16 v[144:147],v[228:231],v[64:71],v248
cv 93,122,123
maav 16,80,64
ex 127
maav 16,112,68
cv 94,124,125
cv 95,126,127
maav 20,84,64
maav 20,116,68
buffer_load_dword v247,v245,s[36:39],s85 offen
maav 24,88,64
maav 24,120,68
ra 192,224,32768
maav 28,92,64
maav 28,124,68
ra 196,224,33792
maav 32,64,72
ds 255,8,78,1024
maav 32,96,76
s_add_u32 s85,s85,131072
maav 36,68,72
ra 200,224,34816
maav 36,100,76
maav 40,72,72
maav 40,104,76
ra 204,224,35840
maav 44,76,72
ds 255,8,78,2048
maav 44,108,76
v_smfmac_f32_16x16x64_bf16 v[148:151],v[228:231],v[72:79],v248
ra 208,224,36864
maav 48,80,72
maav 48,112,76
maav 52,84,72
ra 212,224,37888
maav 52,116,76
ex 0
maav 56,88,72
ds 255,8,78,3072
maav 56,120,76
ra 216,224,38912
maav 60,92,72
ex 1
maav 60,124,76
ex 2
zvav 160,64,80
ra 220,224,39936
cv 0,0,1
mvav 160,96,84
s_mov_b32 m0,s75
ex 3
zvav 164,68,80
ra 224,224,40960
cv 1,2,3
mvav 164,100,84
ds 255,8,79,0
zvav 168,72,80
ex 4
mvav 168,104,84
ra 228,224,41984
zvav 172,76,80
s_add_u32 s46,s46,16384
ex 5
mvav 172,108,84
s_add_u32 s47,s47,32768
ex 6
v_smfmac_f32_16x16x64_bf16 v[152:155],v[228:231],v[80:87],v248
ra 232,224,43008
zvav 176,80,80
ds 255,8,79,1024
mvav 176,112,84
s_add_u32 s95,s47,32768
ex 7
zvav 180,84,80
ra 236,224,44032
cv 2,4,5
mvav 180,116,84
ex 32
zvav 184,88,80
ex 33
mvav 184,120,84
ra 240,224,45056
cv 3,6,7
zvav 188,92,80
ds 255,8,79,2048
mvav 188,124,84
ex 34
mvav 192,64,88
ra 244,224,46080
cv 4,32,33
mvav 192,96,92
ex 35
mvav 196,68,88
ex 36
mvav 196,100,92
ra 248,224,47104
cv 5,34,35
mvav 200,72,88
ex 37
mvav 200,104,92
ds 255,8,79,3072
mvav 204,76,88
s_cmp_ge_i32 s47,s46
ex 38
mvav 204,108,92
s_cselect_b32 s78,s95,s46
ex 39
v_smfmac_f32_16x16x64_bf16 v[156:159],v[228:231],v[88:95],v248
ra 252,224,48128
mvav 208,80,88
s_cselect_b32 s79,s95,268435456
ex 8
mvav 208,112,92
s_add_u32 s79,s79,16384
ex 9
mvav 212,84,88
ex 10
mvav 212,116,92
ex 11
mvav 216,88,88
cv 6,36,37
cv 7,38,39
mvav 216,120,92
ex 12
mvav 220,92,88
ex 13
mvav 220,124,92
s_waitcnt vmcnt(16) lgkmcnt(0)
s_barrier
cvaa 64,192,128,232
s_bitcmp0_b32 s82,s81
s_cbranch_scc1 L_loop2
s_bitcmp1_b32 s15,s81
s_cbranch_scc1 L_short2
.p2align 6
L_loop2:
mvaa 64,200,132
s_add_u32 s81,s81,1
ex 14
mvaa 64,208,136
s_add_u32 s44,s44,1024
ex 15
mvaa 64,216,140
cv 8,8,9
cv 9,10,11
cvaa 68,196,128,232
ex 40
mvaa 68,204,132
ex 41
mvaa 68,212,136
s_mov_b32 m0,s70
ex 42
mvaa 68,220,140
ds 249,8,78,0
cvaa 96,224,128,232
ra 64,225,0
cv 10,12,13
mvaa 96,232,132
ex 43
mvaa 96,240,136
ra 68,225,1024
cv 11,14,15
mvaa 96,248,140
ex 44
cvaa 100,228,128,232
ex 45
mvaa 100,236,132
ra 72,225,2048
cv 12,40,41
mvaa 100,244,136
ds 249,8,78,1024
mvaa 100,252,140
ex 46
cvaa 72,192,144,232
ra 76,225,3072
cv 13,42,43
mvaa 72,200,148
ex 47
mvaa 72,208,152
ex 16
mvaa 72,216,156
ra 80,225,4096
cv 14,44,45
cvaa 76,196,144,232
ex 17
mvaa 76,204,148
ds 249,8,78,2048
mvaa 76,212,152
ra 84,225,5120
cv 15,46,47
mvaa 76,220,156
ex 18
cvaa 104,224,144,232
ex 19
mvaa 104,232,148
ra 88,225,6144
cv 16,16,17
mvaa 104,240,152
ex 20
mvaa 104,248,156
ra 92,225,7168
cv 17,18,19
cvaa 108,228,144,232
ds 249,8,78,3072
mvaa 108,236,148
ex 21
mvaa 108,244,152
ex 22
mvaa 108,252,156
ra 96,225,8192
cv 18,20,21
cvaa 80,192,160,232
ex 23
mvaa 80,200,164
ex 48
mvaa 80,208,168
ra 100,225,9216
cv 19,22,23
mvaa 80,216,172
s_mov_b32 m0,s71
ex 49
cvaa 84,196,160,232
ds 249,8,79,0
mvaa 84,204,164
ra 104,225,10240
cv 20,48,49
mvaa 84,212,168
ex 50
mvaa 84,220,172
ex 51
cvaa 112,224,160,232
ra 108,225,11264
cv 21,50,51
mvaa 112,232,164
ex 52
mvaa 112,240,168
ra 112,225,12288
mvaa 112,248,172
ds 249,8,79,1024
cvaa 116,228,160,232
ex 53
mvaa 116,236,164
ex 54
mvaa 116,244,168
ra 116,225,13312
cv 22,52,53
mvaa 116,252,172
ex 55
cvaa 88,192,176,232
ra 120,225,14336
cv 23,54,55
mvaa 88,200,180
ex 24
mvaa 88,208,184
ds 249,8,79,2048
mvaa 88,216,188
ex 25
cvaa 92,196,176,232
ra 124,225,15360
cv 24,24,25
mvaa 92,204,180
ex 26
mvaa 92,212,184
ex 27
mvaa 92,220,188
ex 28
cvaa 120,224,176,232
ex 29
mvaa 120,232,180
ds 249,8,79,3072
mvaa 120,240,184
ex 30
mvaa 120,248,188
ex 31
cvaa 124,228,176,232
ex 56
mvaa 124,236,180
buffer_store_dwordx4 v[250:253],v244,s[8:11],s44 offen sc0 sc1
mvaa 124,244,184
ex 57
mvaa 124,252,188
s_waitcnt vmcnt(16) lgkmcnt(0)
s_barrier
maav 0,64,0
cv 25,26,27
cv 26,28,29
maav 0,96,4
ex 58
maav 4,68,0
ex 59
maav 4,100,4
s_mov_b32 m0,s72
ex 60
maav 8,72,0
ds 255,8,76,0
maav 8,104,4
ex 61
maav 12,76,0
s_cmp_lg_u32 s80,s81
cv 27,30,31
cv 28,56,57
maav 12,108,4
ex 62
v_smfmac_f32_16x16x64_bf16 v[144:147],v[228:231],v[0:7],v248
cv 29,58,59
maav 16,80,0
ex 63
maav 16,112,4
cv 30,60,61
cv 31,62,63
maav 20,84,0
maav 20,116,4
buffer_load_dword v247,v246,s[24:27],s49 offen
maav 24,88,0
s_cbranch_scc1 L_evb_r2
v_add_u32_e32 v224,s94,v224
s_lshl_b32 s86,s12,12
s_add_u32 s86,s86,131072
v_add_u32_e32 v236,s86,v244
ra 160,236,0
ra 164,236,1024
ra 168,236,2048
ra 172,236,3072
ra 176,236,16384
ra 180,236,17408
ra 184,236,18432
ra 188,236,19456
L_evb_r2:
maav 24,120,4
ra 192,224,0
maav 28,92,0
maav 28,124,4
ra 196,224,1024
maav 32,64,8
ds 255,8,76,1024
maav 32,96,12
s_add_u32 s49,s49,32768
maav 36,68,8
ra 200,224,2048
maav 36,100,12
maav 40,72,8
maav 40,104,12
ra 204,224,3072
maav 44,76,8
ds 255,8,76,2048
maav 44,108,12
v_smfmac_f32_16x16x64_bf16 v[148:151],v[228:231],v[8:15],v248
ra 208,224,4096
maav 48,80,8
maav 48,112,12
maav 52,84,8
ra 212,224,5120
maav 52,116,12
ex 64
maav 56,88,8
ds 255,8,76,3072
maav 56,120,12
ra 216,224,6144
maav 60,92,8
ex 65
maav 60,124,12
ex 66
mvav 160,64,16
ra 220,224,7168
cv 64,64,65
mvav 160,96,20
s_mov_b32 m0,s73
ex 67
mvav 164,68,16
ra 224,224,8192
cv 65,66,67
mvav 164,100,20
ds 255,8,77,0
mvav 168,72,16
ex 68
mvav 168,104,20
ra 228,224,9216
mvav 172,76,16
s_add_u32 s46,s46,16384
ex 69
mvav 172,108,20
s_add_u32 s47,s47,32768
ex 70
v_smfmac_f32_16x16x64_bf16 v[152:155],v[228:231],v[16:23],v248
ra 232,224,10240
mvav 176,80,16
ds 255,8,77,1024
mvav 176,112,20
s_add_u32 s95,s47,32768
ex 71
mvav 180,84,16
ra 236,224,11264
cv 66,68,69
mvav 180,116,20
ex 96
mvav 184,88,16
ex 97
mvav 184,120,20
ra 240,224,12288
cv 67,70,71
mvav 188,92,16
ds 255,8,77,2048
mvav 188,124,20
ex 98
mvav 192,64,24
ra 244,224,13312
cv 68,96,97
mvav 192,96,28
ex 99
mvav 196,68,24
ex 100
mvav 196,100,28
ra 248,224,14336
cv 69,98,99
mvav 200,72,24
ex 101
mvav 200,104,28
ds 255,8,77,3072
mvav 204,76,24
s_cmp_ge_i32 s47,s46
ex 102
mvav 204,108,28
s_cselect_b32 s76,s95,s46
ex 103
v_smfmac_f32_16x16x64_bf16 v[156:159],v[228:231],v[24:31],v248
ra 252,224,15360
mvav 208,80,24
s_cselect_b32 s77,s95,268435456
ex 72
mvav 208,112,28
s_add_u32 s77,s77,16384
ex 73
mvav 212,84,24
ex 74
mvav 212,116,28
ex 75
mvav 216,88,24
cv 70,100,101
cv 71,102,103
mvav 216,120,28
ex 76
mvav 220,92,24
ex 77
mvav 220,124,28
s_waitcnt vmcnt(16) lgkmcnt(0)
s_barrier
cvaa 0,192,128,232
mvaa 0,200,132
s_add_u32 s81,s81,1
ex 78
mvaa 0,208,136
s_add_u32 s44,s44,1024
ex 79
mvaa 0,216,140
cv 72,72,73
cv 73,74,75
cvaa 4,196,128,232
ex 104
mvaa 4,204,132
ex 105
mvaa 4,212,136
s_mov_b32 m0,s68
ex 106
mvaa 4,220,140
ds 249,8,76,0
cvaa 32,224,128,232
ra 64,225,32768
cv 74,76,77
mvaa 32,232,132
ex 107
mvaa 32,240,136
ra 68,225,33792
cv 75,78,79
mvaa 32,248,140
ex 108
cvaa 36,228,128,232
ex 109
mvaa 36,236,132
ra 72,225,34816
cv 76,104,105
mvaa 36,244,136
ds 249,8,76,1024
mvaa 36,252,140
ex 110
cvaa 8,192,144,232
ra 76,225,35840
cv 77,106,107
mvaa 8,200,148
ex 111
mvaa 8,208,152
ex 80
mvaa 8,216,156
ra 80,225,36864
cv 78,108,109
cvaa 12,196,144,232
ex 81
mvaa 12,204,148
ds 249,8,76,2048
mvaa 12,212,152
ra 84,225,37888
cv 79,110,111
mvaa 12,220,156
ex 82
cvaa 40,224,144,232
ex 83
mvaa 40,232,148
ra 88,225,38912
cv 80,80,81
mvaa 40,240,152
ex 84
mvaa 40,248,156
ra 92,225,39936
cv 81,82,83
cvaa 44,228,144,232
ds 249,8,76,3072
mvaa 44,236,148
ex 85
mvaa 44,244,152
ex 86
mvaa 44,252,156
ra 96,225,40960
cv 82,84,85
cvaa 16,192,160,232
ex 87
mvaa 16,200,164
ex 112
mvaa 16,208,168
ra 100,225,41984
cv 83,86,87
mvaa 16,216,172
s_mov_b32 m0,s69
ex 113
cvaa 20,196,160,232
ds 249,8,77,0
mvaa 20,204,164
ra 104,225,43008
cv 84,112,113
mvaa 20,212,168
ex 114
mvaa 20,220,172
ex 115
cvaa 48,224,160,232
ra 108,225,44032
cv 85,114,115
mvaa 48,232,164
ex 116
mvaa 48,240,168
ra 112,225,45056
mvaa 48,248,172
ds 249,8,77,1024
cvaa 52,228,160,232
ex 117
mvaa 52,236,164
ex 118
mvaa 52,244,168
ra 116,225,46080
cv 86,116,117
mvaa 52,252,172
ex 119
cvaa 24,192,176,232
ra 120,225,47104
cv 87,118,119
mvaa 24,200,180
ex 88
mvaa 24,208,184
ds 249,8,77,2048
mvaa 24,216,188
ex 89
cvaa 28,196,176,232
ra 124,225,48128
cv 88,88,89
mvaa 28,204,180
ex 90
mvaa 28,212,184
ex 91
mvaa 28,220,188
ex 92
cvaa 56,224,176,232
ex 93
mvaa 56,232,180
ds 249,8,77,3072
mvaa 56,240,184
ex 94
mvaa 56,248,188
ex 95
cvaa 60,228,176,232
ex 120
mvaa 60,236,180
buffer_store_dwordx4 v[250:253],v244,s[8:11],s44 offen sc0 sc1
mvaa 60,244,184
ex 121
mvaa 60,252,188
s_waitcnt vmcnt(16) lgkmcnt(0)
s_barrier
maav 0,64,64
cv 89,90,91
cv 90,92,93
maav 0,96,68
ex 122
maav 4,68,64
ex 123
maav 4,100,68
s_mov_b32 m0,s74
ex 124
maav 8,72,64
ds 255,8,78,0
maav 8,104,68
ex 125
maav 12,76,64
cv 91,94,95
cv 92,120,121
maav 12,108,68
ex 126
v_smfmac_f32_16x16x64_bf16 v[144:147],v[228:231],v[64:71],v248
cv 93,122,123
maav 16,80,64
ex 127
maav 16,112,68
cv 94,124,125
cv 95,126,127
maav 20,84,64
maav 20,116,68
buffer_load_dword v247,v245,s[36:39],s85 offen
maav 24,88,64
maav 24,120,68
ra 192,224,32768
maav 28,92,64
maav 28,124,68
ra 196,224,33792
maav 32,64,72
ds 255,8,78,1024
maav 32,96,76
s_add_u32 s85,s85,131072
maav 36,68,72
ra 200,224,34816
maav 36,100,76
maav 40,72,72
maav 40,104,76
ra 204,224,35840
maav 44,76,72
ds 255,8,78,2048
maav 44,108,76
v_smfmac_f32_16x16x64_bf16 v[148:151],v[228:231],v[72:79],v248
ra 208,224,36864
maav 48,80,72
maav 48,112,76
maav 52,84,72
ra 212,224,37888
maav 52,116,76
ex 0
maav 56,88,72
ds 255,8,78,3072
maav 56,120,76
ra 216,224,38912
maav 60,92,72
ex 1
maav 60,124,76
ex 2
mvav 160,64,80
ra 220,224,39936
cv 0,0,1
mvav 160,96,84
s_mov_b32 m0,s75
ex 3
mvav 164,68,80
ra 224,224,40960
cv 1,2,3
mvav 164,100,84
ds 255,8,79,0
mvav 168,72,80
ex 4
mvav 168,104,84
ra 228,224,41984
mvav 172,76,80
s_add_u32 s46,s46,16384
ex 5
mvav 172,108,84
s_add_u32 s47,s47,32768
ex 6
v_smfmac_f32_16x16x64_bf16 v[152:155],v[228:231],v[80:87],v248
ra 232,224,43008
mvav 176,80,80
ds 255,8,79,1024
mvav 176,112,84
s_add_u32 s95,s47,32768
ex 7
mvav 180,84,80
ra 236,224,44032
cv 2,4,5
mvav 180,116,84
ex 32
mvav 184,88,80
ex 33
mvav 184,120,84
ra 240,224,45056
cv 3,6,7
mvav 188,92,80
ds 255,8,79,2048
mvav 188,124,84
ex 34
mvav 192,64,88
ra 244,224,46080
cv 4,32,33
mvav 192,96,92
ex 35
mvav 196,68,88
ex 36
mvav 196,100,92
ra 248,224,47104
cv 5,34,35
mvav 200,72,88
ex 37
mvav 200,104,92
ds 255,8,79,3072
mvav 204,76,88
s_cmp_ge_i32 s47,s46
ex 38
mvav 204,108,92
s_cselect_b32 s78,s95,s46
ex 39
v_smfmac_f32_16x16x64_bf16 v[156:159],v[228:231],v[88:95],v248
ra 252,224,48128
mvav 208,80,88
s_cselect_b32 s79,s95,268435456
ex 8
mvav 208,112,92
s_add_u32 s79,s79,16384
ex 9
mvav 212,84,88
ex 10
mvav 212,116,92
ex 11
mvav 216,88,88
cv 6,36,37
cv 7,38,39
mvav 216,120,92
ex 12
mvav 220,92,88
ex 13
mvav 220,124,92
s_waitcnt vmcnt(16) lgkmcnt(0)
s_barrier
cvaa 64,192,128,232
s_bitcmp0_b32 s82,s81
s_cbranch_scc1 L_loop2
s_bitcmp0_b32 s15,s81
s_cbranch_scc1 L_eva_r2
L_short2:
v_add_f32_e32 v226,v152,v153
v_add_f32_e32 v227,v156,v157
v_subrev_u32_e32 v236,0x23800000,v226
v_subrev_u32_e32 v237,0x23800000,v227
v_max_u32_e32 v236,v236,v237
v_cmp_lt_u32_e64 s[40:41],s48,v236
v_rcp_f32_e32 v226,v226
v_rcp_f32_e32 v227,v227
s_add_u32 s86,s13,4096
)"
R"(v_mov_b32_e32 v236,0
v_mov_b32_e32 v237,0
v_mov_b32_e32 v238,0
v_mov_b32_e32 v239,0
v_mov_b64_e32 v[152:153],0
v_mov_b64_e32 v[156:157],0
v_mul_f32_e32 v226,v242,v226
v_mul_f32_e32 v227,v243,v227
v_add_u32_e32 v225,s94,v225
v_pk_mul_f32 v[160:161],v[160:161],v[226:227] op_sel:[0,0] op_sel_hi:[1,0]
v_pk_mul_f32 v[162:163],v[162:163],v[226:227] op_sel:[0,0] op_sel_hi:[1,0]
v_pk_mul_f32 v[164:165],v[164:165],v[226:227] op_sel:[0,0] op_sel_hi:[1,0]
v_pk_mul_f32 v[166:167],v[166:167],v[226:227] op_sel:[0,0] op_sel_hi:[1,0]
cv 160,160,161
cv 161,162,163
cv 162,164,165
cv 163,166,167
buffer_store_dwordx4 v[160:163],v244,s[8:11],s13 offen offset:0 sc0 sc1
v_pk_mul_f32 v[168:169],v[168:169],v[226:227] op_sel:[0,0] op_sel_hi:[1,0]
v_pk_mul_f32 v[170:171],v[170:171],v[226:227] op_sel:[0,0] op_sel_hi:[1,0]
v_pk_mul_f32 v[172:173],v[172:173],v[226:227] op_sel:[0,0] op_sel_hi:[1,0]
v_pk_mul_f32 v[174:175],v[174:175],v[226:227] op_sel:[0,0] op_sel_hi:[1,0]
cv 168,168,169
cv 169,170,171
cv 170,172,173
cv 171,174,175
buffer_store_dwordx4 v[168:171],v244,s[8:11],s13 offen offset:2048 sc0 sc1
v_pk_mul_f32 v[176:177],v[176:177],v[226:227] op_sel:[0,0] op_sel_hi:[1,0]
v_pk_mul_f32 v[178:179],v[178:179],v[226:227] op_sel:[0,0] op_sel_hi:[1,0]
v_pk_mul_f32 v[180:181],v[180:181],v[226:227] op_sel:[0,0] op_sel_hi:[1,0]
v_pk_mul_f32 v[182:183],v[182:183],v[226:227] op_sel:[0,0] op_sel_hi:[1,0]
cv 176,176,177
cv 177,178,179
cv 178,180,181
v_mfma_f32_32x32x16_bf16 v[160:175],v[236:239],v[236:239],0
cv 179,182,183
buffer_store_dwordx4 v[176:179],v244,s[8:11],s86 offen offset:0 sc0 sc1
v_pk_mul_f32 v[184:185],v[184:185],v[226:227] op_sel:[0,0] op_sel_hi:[1,0]
v_pk_mul_f32 v[186:187],v[186:187],v[226:227] op_sel:[0,0] op_sel_hi:[1,0]
v_pk_mul_f32 v[188:189],v[188:189],v[226:227] op_sel:[0,0] op_sel_hi:[1,0]
v_pk_mul_f32 v[190:191],v[190:191],v[226:227] op_sel:[0,0] op_sel_hi:[1,0]
cv 184,184,185
cv 185,186,187
cv 186,188,189
cv 187,190,191
buffer_store_dwordx4 v[184:187],v244,s[8:11],s86 offen offset:2048 sc0 sc1
v_pk_mul_f32 v[192:193],v[192:193],v[226:227] op_sel:[0,1] op_sel_hi:[1,1]
v_pk_mul_f32 v[194:195],v[194:195],v[226:227] op_sel:[0,1] op_sel_hi:[1,1]
v_pk_mul_f32 v[196:197],v[196:197],v[226:227] op_sel:[0,1] op_sel_hi:[1,1]
v_pk_mul_f32 v[198:199],v[198:199],v[226:227] op_sel:[0,1] op_sel_hi:[1,1]
cv 192,192,193
cv 193,194,195
cv 194,196,197
v_mfma_f32_32x32x16_bf16 v[176:191],v[236:239],v[236:239],0
cv 195,198,199
buffer_store_dwordx4 v[192:195],v244,s[8:11],s13 offen offset:1024 sc0 sc1
v_pk_mul_f32 v[200:201],v[200:201],v[226:227] op_sel:[0,1] op_sel_hi:[1,1]
v_pk_mul_f32 v[202:203],v[202:203],v[226:227] op_sel:[0,1] op_sel_hi:[1,1]
v_pk_mul_f32 v[204:205],v[204:205],v[226:227] op_sel:[0,1] op_sel_hi:[1,1]
v_pk_mul_f32 v[206:207],v[206:207],v[226:227] op_sel:[0,1] op_sel_hi:[1,1]
cv 200,200,201
cv 201,202,203
cv 202,204,205
cv 203,206,207
buffer_store_dwordx4 v[200:203],v244,s[8:11],s13 offen offset:3072 sc0 sc1
v_pk_mul_f32 v[208:209],v[208:209],v[226:227] op_sel:[0,1] op_sel_hi:[1,1]
v_pk_mul_f32 v[210:211],v[210:211],v[226:227] op_sel:[0,1] op_sel_hi:[1,1]
v_pk_mul_f32 v[212:213],v[212:213],v[226:227] op_sel:[0,1] op_sel_hi:[1,1]
v_pk_mul_f32 v[214:215],v[214:215],v[226:227] op_sel:[0,1] op_sel_hi:[1,1]
cv 208,208,209
cv 209,210,211
cv 210,212,213
v_mfma_f32_32x32x16_bf16 v[192:207],v[236:239],v[236:239],0
cv 211,214,215
buffer_store_dwordx4 v[208:211],v244,s[8:11],s86 offen offset:1024 sc0 sc1
v_pk_mul_f32 v[216:217],v[216:217],v[226:227] op_sel:[0,1] op_sel_hi:[1,1]
v_pk_mul_f32 v[218:219],v[218:219],v[226:227] op_sel:[0,1] op_sel_hi:[1,1]
v_pk_mul_f32 v[220:221],v[220:221],v[226:227] op_sel:[0,1] op_sel_hi:[1,1]
v_pk_mul_f32 v[222:223],v[222:223],v[226:227] op_sel:[0,1] op_sel_hi:[1,1]
cv 216,216,217
cv 217,218,219
cv 218,220,221
cv 219,222,223
buffer_store_dwordx4 v[216:219],v244,s[8:11],s86 offen offset:3072 sc0 sc1
s_nop 1
v_mfma_f32_32x32x16_bf16 v[208:223],v[236:239],v[236:239],0
L_eva_r2:
s_cmp_lg_u32 s81,14
s_cbranch_scc1 L_loop2
s_add_u32 s81,s81,1
s_bitcmp1_b32 170,s20
s_cbranch_scc1 L_odd
s_branch L_last
.fill 276,4,0xbf800000
L_cw3:
s_mov_b64 exec,-1
s_waitcnt lgkmcnt(0)
s_barrier
cvaa 0,192,128,232
mvaa 0,200,132
s_add_u32 s81,s81,1
ex 78
mvaa 0,208,136
s_add_u32 s44,s44,1024
ex 79
mvaa 0,216,140
cv 72,72,73
cv 73,74,75
cvaa 4,196,128,232
ex 104
mvaa 4,204,132
ex 105
mvaa 4,212,136
s_mov_b32 m0,s68
ex 106
mvaa 4,220,140
ra 64,225,32768
cv 74,76,77
cvaa 32,224,128,232
ds 249,8,76,0
mvaa 32,232,132
ex 107
mvaa 32,240,136
ra 68,225,33792
cv 75,78,79
mvaa 32,248,140
ex 108
cvaa 36,228,128,232
ex 109
mvaa 36,236,132
ra 72,225,34816
cv 76,104,105
mvaa 36,244,136
ex 110
mvaa 36,252,140
ds 249,8,76,1024
cvaa 8,192,144,232
ra 76,225,35840
cv 77,106,107
mvaa 8,200,148
ex 111
mvaa 8,208,152
ex 80
mvaa 8,216,156
ra 80,225,36864
cv 78,108,109
cvaa 12,196,144,232
ex 81
mvaa 12,204,148
ra 84,225,37888
cv 79,110,111
mvaa 12,212,152
ds 249,8,76,2048
mvaa 12,220,156
ex 82
cvaa 40,224,144,232
ex 83
mvaa 40,232,148
ra 88,225,38912
cv 80,80,81
mvaa 40,240,152
ex 84
mvaa 40,248,156
ra 92,225,39936
cv 81,82,83
cvaa 44,228,144,232
ex 85
mvaa 44,236,148
ds 249,8,76,3072
mvaa 44,244,152
ex 86
mvaa 44,252,156
ra 96,225,40960
cv 82,84,85
cvaa 16,192,160,232
ex 87
mvaa 16,200,164
ex 112
mvaa 16,208,168
ra 100,225,41984
cv 83,86,87
mvaa 16,216,172
s_mov_b32 m0,s69
ex 113
cvaa 20,196,160,232
ra 104,225,43008
cv 84,112,113
mvaa 20,204,164
ds 249,8,77,0
mvaa 20,212,168
ex 114
mvaa 20,220,172
ex 115
cvaa 48,224,160,232
ra 108,225,44032
cv 85,114,115
mvaa 48,232,164
ex 116
mvaa 48,240,168
ra 112,225,45056
mvaa 48,248,172
ex 117
cvaa 52,228,160,232
ds 249,8,77,1024
mvaa 52,236,164
ex 118
mvaa 52,244,168
ra 116,225,46080
cv 86,116,117
mvaa 52,252,172
ex 119
cvaa 24,192,176,232
ra 120,225,47104
cv 87,118,119
mvaa 24,200,180
ex 88
mvaa 24,208,184
ex 89
mvaa 24,216,188
ds 249,8,77,2048
cvaa 28,196,176,232
ra 124,225,48128
cv 88,88,89
mvaa 28,204,180
ex 90
mvaa 28,212,184
ex 91
mvaa 28,220,188
ex 92
cvaa 56,224,176,232
ex 93
mvaa 56,232,180
ex 94
mvaa 56,240,184
ds 249,8,77,3072
mvaa 56,248,188
ex 95
cvaa 60,228,176,232
ex 120
mvaa 60,236,180
ex 121
mvaa 60,244,184
mvaa 60,252,188
s_waitcnt vmcnt(8) lgkmcnt(0)
s_barrier
maav 0,64,64
cv 89,90,91
cv 90,92,93
maav 0,96,68
ex 122
maav 4,68,64
ex 123
maav 4,100,68
ex 124
maav 8,72,64
s_mov_b32 m0,s74
ex 125
maav 8,104,68
ds 255,8,78,0
maav 12,76,64
cv 91,94,95
cv 92,120,121
maav 12,108,68
ex 126
v_smfmac_f32_16x16x64_bf16 v[144:147],v[228:231],v[64:71],v248
cv 93,122,123
maav 16,80,64
ex 127
maav 16,112,68
cv 94,124,125
cv 95,126,127
maav 20,84,64
maav 20,116,68
maav 24,88,64
buffer_load_dword v247,v245,s[36:39],s85 offen
maav 24,120,68
ra 192,224,32768
maav 28,92,64
maav 28,124,68
ra 196,224,33792
maav 32,64,72
maav 32,96,76
ds 255,8,78,1024
maav 36,68,72
ra 200,224,34816
maav 36,100,76
s_add_u32 s85,s85,131072
maav 40,72,72
maav 40,104,76
ra 204,224,35840
maav 44,76,72
maav 44,108,76
ds 255,8,78,2048
v_smfmac_f32_16x16x64_bf16 v[148:151],v[228:231],v[72:79],v248
ra 208,224,36864
maav 48,80,72
maav 48,112,76
maav 52,84,72
ra 212,224,37888
maav 52,116,76
ex 0
maav 56,88,72
ra 216,224,38912
maav 56,120,76
ds 255,8,78,3072
maav 60,92,72
ex 1
maav 60,124,76
ex 2
zvav 160,64,80
ra 220,224,39936
cv 0,0,1
mvav 160,96,84
ex 3
zvav 164,68,80
ra 224,224,40960
cv 1,2,3
mvav 164,100,84
s_mov_b32 m0,s75
ex 4
zvav 168,72,80
ds 255,8,79,0
mvav 168,104,84
ra 228,224,41984
zvav 172,76,80
s_add_u32 s46,s46,16384
ex 5
mvav 172,108,84
s_add_u32 s47,s47,32768
ex 6
v_smfmac_f32_16x16x64_bf16 v[152:155],v[228:231],v[80:87],v248
ra 232,224,43008
zvav 176,80,80
ds 255,8,79,1024
mvav 176,112,84
s_add_u32 s95,s47,32768
ex 7
zvav 180,84,80
ra 236,224,44032
cv 2,4,5
mvav 180,116,84
ex 32
zvav 184,88,80
ex 33
mvav 184,120,84
ra 240,224,45056
cv 3,6,7
zvav 188,92,80
ex 34
mvav 188,124,84
ds 255,8,79,2048
mvav 192,64,88
ra 244,224,46080
cv 4,32,33
mvav 192,96,92
ex 35
mvav 196,68,88
ex 36
mvav 196,100,92
ra 248,224,47104
cv 5,34,35
mvav 200,72,88
ex 37
mvav 200,104,92
ex 38
mvav 204,76,88
ds 255,8,79,3072
mvav 204,108,92
s_cmp_ge_i32 s47,s46
ex 39
v_smfmac_f32_16x16x64_bf16 v[156:159],v[228:231],v[88:95],v248
ra 252,224,48128
mvav 208,80,88
s_cselect_b32 s78,s95,s46
ex 8
mvav 208,112,92
s_cselect_b32 s79,s95,268435456
ex 9
mvav 212,84,88
s_add_u32 s79,s79,16384
ex 10
mvav 212,116,92
ex 11
mvav 216,88,88
cv 6,36,37
cv 7,38,39
mvav 216,120,92
ex 12
mvav 220,92,88
ex 13
mvav 220,124,92
s_waitcnt vmcnt(16) lgkmcnt(0)
s_barrier
cvaa 64,192,128,232
s_bitcmp0_b32 s82,s81
s_cbranch_scc1 L_loop3
s_bitcmp1_b32 s15,s81
s_cbranch_scc1 L_short3
.p2align 6
L_loop3:
mvaa 64,200,132
s_add_u32 s81,s81,1
ex 14
mvaa 64,208,136
s_add_u32 s44,s44,1024
ex 15
mvaa 64,216,140
cv 8,8,9
cv 9,10,11
cvaa 68,196,128,232
ex 40
mvaa 68,204,132
ex 41
mvaa 68,212,136
s_mov_b32 m0,s70
ex 42
mvaa 68,220,140
ra 64,225,0
cv 10,12,13
cvaa 96,224,128,232
ds 249,8,78,0
mvaa 96,232,132
ex 43
mvaa 96,240,136
ra 68,225,1024
cv 11,14,15
mvaa 96,248,140
ex 44
cvaa 100,228,128,232
ex 45
mvaa 100,236,132
ra 72,225,2048
cv 12,40,41
mvaa 100,244,136
ex 46
mvaa 100,252,140
ds 249,8,78,1024
cvaa 72,192,144,232
ra 76,225,3072
cv 13,42,43
mvaa 72,200,148
ex 47
mvaa 72,208,152
ex 16
mvaa 72,216,156
ra 80,225,4096
cv 14,44,45
cvaa 76,196,144,232
ex 17
mvaa 76,204,148
ra 84,225,5120
cv 15,46,47
mvaa 76,212,152
ds 249,8,78,2048
mvaa 76,220,156
ex 18
cvaa 104,224,144,232
ex 19
mvaa 104,232,148
ra 88,225,6144
cv 16,16,17
mvaa 104,240,152
ex 20
mvaa 104,248,156
ra 92,225,7168
cv 17,18,19
cvaa 108,228,144,232
ex 21
mvaa 108,236,148
ds 249,8,78,3072
mvaa 108,244,152
ex 22
mvaa 108,252,156
ra 96,225,8192
cv 18,20,21
cvaa 80,192,160,232
ex 23
mvaa 80,200,164
ex 48
mvaa 80,208,168
ra 100,225,9216
cv 19,22,23
mvaa 80,216,172
s_mov_b32 m0,s71
ex 49
cvaa 84,196,160,232
ra 104,225,10240
cv 20,48,49
mvaa 84,204,164
ds 249,8,79,0
mvaa 84,212,168
ex 50
mvaa 84,220,172
ex 51
cvaa 112,224,160,232
ra 108,225,11264
cv 21,50,51
mvaa 112,232,164
ex 52
mvaa 112,240,168
ra 112,225,12288
mvaa 112,248,172
ex 53
cvaa 116,228,160,232
ds 249,8,79,1024
mvaa 116,236,164
ex 54
mvaa 116,244,168
ra 116,225,13312
cv 22,52,53
mvaa 116,252,172
ex 55
cvaa 88,192,176,232
ra 120,225,14336
cv 23,54,55
mvaa 88,200,180
ex 24
mvaa 88,208,184
ex 25
mvaa 88,216,188
ds 249,8,79,2048
cvaa 92,196,176,232
ra 124,225,15360
cv 24,24,25
mvaa 92,204,180
ex 26
mvaa 92,212,184
ex 27
mvaa 92,220,188
ex 28
cvaa 120,224,176,232
ex 29
mvaa 120,232,180
ex 30
mvaa 120,240,184
ds 249,8,79,3072
mvaa 120,248,188
ex 31
cvaa 124,228,176,232
ex 56
mvaa 124,236,180
ex 57
mvaa 124,244,184
buffer_store_dwordx4 v[250:253],v244,s[8:11],s44 offen sc0 sc1
mvaa 124,252,188
s_waitcnt vmcnt(16) lgkmcnt(0)
s_barrier
maav 0,64,0
cv 25,26,27
cv 26,28,29
maav 0,96,4
ex 58
maav 4,68,0
ex 59
maav 4,100,4
ex 60
maav 8,72,0
s_mov_b32 m0,s72
ex 61
maav 8,104,4
ds 255,8,76,0
maav 12,76,0
s_cmp_lg_u32 s80,s81
cv 27,30,31
cv 28,56,57
maav 12,108,4
ex 62
v_smfmac_f32_16x16x64_bf16 v[144:147],v[228:231],v[0:7],v248
cv 29,58,59
maav 16,80,0
ex 63
maav 16,112,4
cv 30,60,61
cv 31,62,63
maav 20,84,0
maav 20,116,4
s_cbranch_scc1 L_evb_r3
v_add_u32_e32 v224,s94,v224
s_lshl_b32 s86,s12,12
s_add_u32 s86,s86,131072
v_add_u32_e32 v236,s86,v244
ra 160,236,0
ra 164,236,1024
ra 168,236,2048
ra 172,236,3072
ra 176,236,16384
ra 180,236,17408
ra 184,236,18432
ra 188,236,19456
L_evb_r3:
maav 24,88,0
buffer_load_dword v247,v246,s[24:27],s49 offen
maav 24,120,4
ra 192,224,0
maav 28,92,0
maav 28,124,4
ra 196,224,1024
maav 32,64,8
maav 32,96,12
ds 255,8,76,1024
maav 36,68,8
ra 200,224,2048
maav 36,100,12
s_add_u32 s49,s49,32768
maav 40,72,8
maav 40,104,12
ra 204,224,3072
maav 44,76,8
maav 44,108,12
ds 255,8,76,2048
v_smfmac_f32_16x16x64_bf16 v[148:151],v[228:231],v[8:15],v248
ra 208,224,4096
maav 48,80,8
maav 48,112,12
maav 52,84,8
ra 212,224,5120
maav 52,116,12
ex 64
maav 56,88,8
ra 216,224,6144
maav 56,120,12
ds 255,8,76,3072
maav 60,92,8
ex 65
maav 60,124,12
ex 66
mvav 160,64,16
ra 220,224,7168
cv 64,64,65
mvav 160,96,20
ex 67
mvav 164,68,16
ra 224,224,8192
cv 65,66,67
mvav 164,100,20
s_mov_b32 m0,s73
ex 68
mvav 168,72,16
ds 255,8,77,0
mvav 168,104,20
ra 228,224,9216
mvav 172,76,16
s_add_u32 s46,s46,16384
ex 69
mvav 172,108,20
s_add_u32 s47,s47,32768
ex 70
v_smfmac_f32_16x16x64_bf16 v[152:155],v[228:231],v[16:23],v248
ra 232,224,10240
mvav 176,80,16
ds 255,8,77,1024
mvav 176,112,20
s_add_u32 s95,s47,32768
ex 71
mvav 180,84,16
ra 236,224,11264
cv 66,68,69
mvav 180,116,20
ex 96
mvav 184,88,16
ex 97
mvav 184,120,20
ra 240,224,12288
cv 67,70,71
mvav 188,92,16
ex 98
mvav 188,124,20
ds 255,8,77,2048
mvav 192,64,24
ra 244,224,13312
cv 68,96,97
mvav 192,96,28
ex 99
mvav 196,68,24
ex 100
mvav 196,100,28
ra 248,224,14336
cv 69,98,99
mvav 200,72,24
ex 101
mvav 200,104,28
ex 102
mvav 204,76,24
ds 255,8,77,3072
mvav 204,108,28
s_cmp_ge_i32 s47,s46
ex 103
v_smfmac_f32_16x16x64_bf16 v[156:159],v[228:231],v[24:31],v248
ra 252,224,15360
mvav 208,80,24
s_cselect_b32 s76,s95,s46
ex 72
mvav 208,112,28
s_cselect_b32 s77,s95,268435456
ex 73
mvav 212,84,24
s_add_u32 s77,s77,16384
ex 74
mvav 212,116,28
ex 75
mvav 216,88,24
cv 70,100,101
cv 71,102,103
mvav 216,120,28
ex 76
mvav 220,92,24
ex 77
mvav 220,124,28
s_waitcnt vmcnt(16) lgkmcnt(0)
s_barrier
cvaa 0,192,128,232
mvaa 0,200,132
s_add_u32 s81,s81,1
ex 78
mvaa 0,208,136
s_add_u32 s44,s44,1024
ex 79
mvaa 0,216,140
cv 72,72,73
cv 73,74,75
cvaa 4,196,128,232
ex 104
mvaa 4,204,132
ex 105
mvaa 4,212,136
s_mov_b32 m0,s68
ex 106
mvaa 4,220,140
ra 64,225,32768
cv 74,76,77
cvaa 32,224,128,232
ds 249,8,76,0
mvaa 32,232,132
ex 107
mvaa 32,240,136
ra 68,225,33792
cv 75,78,79
mvaa 32,248,140
ex 108
cvaa 36,228,128,232
ex 109
mvaa 36,236,132
ra 72,225,34816
cv 76,104,105
mvaa 36,244,136
ex 110
mvaa 36,252,140
ds 249,8,76,1024
cvaa 8,192,144,232
ra 76,225,35840
cv 77,106,107
mvaa 8,200,148
ex 111
mvaa 8,208,152
ex 80
mvaa 8,216,156
ra 80,225,36864
cv 78,108,109
cvaa 12,196,144,232
ex 81
mvaa 12,204,148
ra 84,225,37888
cv 79,110,111
mvaa 12,212,152
ds 249,8,76,2048
mvaa 12,220,156
ex 82
cvaa 40,224,144,232
ex 83
mvaa 40,232,148
ra 88,225,38912
cv 80,80,81
mvaa 40,240,152
ex 84
mvaa 40,248,156
ra 92,225,39936
cv 81,82,83
cvaa 44,228,144,232
ex 85
mvaa 44,236,148
ds 249,8,76,3072
mvaa 44,244,152
ex 86
mvaa 44,252,156
ra 96,225,40960
cv 82,84,85
cvaa 16,192,160,232
ex 87
mvaa 16,200,164
ex 112
mvaa 16,208,168
ra 100,225,41984
cv 83,86,87
mvaa 16,216,172
s_mov_b32 m0,s69
ex 113
cvaa 20,196,160,232
ra 104,225,43008
cv 84,112,113
mvaa 20,204,164
ds 249,8,77,0
mvaa 20,212,168
ex 114
mvaa 20,220,172
ex 115
cvaa 48,224,160,232
ra 108,225,44032
cv 85,114,115
mvaa 48,232,164
ex 116
mvaa 48,240,168
ra 112,225,45056
mvaa 48,248,172
ex 117
cvaa 52,228,160,232
ds 249,8,77,1024
mvaa 52,236,164
ex 118
mvaa 52,244,168
ra 116,225,46080
cv 86,116,117
mvaa 52,252,172
ex 119
cvaa 24,192,176,232
ra 120,225,47104
cv 87,118,119
mvaa 24,200,180
ex 88
mvaa 24,208,184
ex 89
mvaa 24,216,188
ds 249,8,77,2048
cvaa 28,196,176,232
ra 124,225,48128
cv 88,88,89
mvaa 28,204,180
ex 90
mvaa 28,212,184
ex 91
mvaa 28,220,188
ex 92
cvaa 56,224,176,232
ex 93
mvaa 56,232,180
ex 94
mvaa 56,240,184
ds 249,8,77,3072
mvaa 56,248,188
ex 95
cvaa 60,228,176,232
ex 120
mvaa 60,236,180
ex 121
mvaa 60,244,184
buffer_store_dwordx4 v[250:253],v244,s[8:11],s44 offen sc0 sc1
mvaa 60,252,188
s_waitcnt vmcnt(16) lgkmcnt(0)
s_barrier
maav 0,64,64
cv 89,90,91
cv 90,92,93
maav 0,96,68
ex 122
maav 4,68,64
ex 123
maav 4,100,68
ex 124
maav 8,72,64
s_mov_b32 m0,s74
ex 125
maav 8,104,68
ds 255,8,78,0
maav 12,76,64
cv 91,94,95
cv 92,120,121
maav 12,108,68
ex 126
v_smfmac_f32_16x16x64_bf16 v[144:147],v[228:231],v[64:71],v248
cv 93,122,123
maav 16,80,64
ex 127
maav 16,112,68
cv 94,124,125
cv 95,126,127
maav 20,84,64
maav 20,116,68
maav 24,88,64
buffer_load_dword v247,v245,s[36:39],s85 offen
maav 24,120,68
ra 192,224,32768
maav 28,92,64
maav 28,124,68
ra 196,224,33792
maav 32,64,72
maav 32,96,76
ds 255,8,78,1024
maav 36,68,72
ra 200,224,34816
maav 36,100,76
s_add_u32 s85,s85,131072
maav 40,72,72
maav 40,104,76
ra 204,224,35840
maav 44,76,72
maav 44,108,76
ds 255,8,78,2048
v_smfmac_f32_16x16x64_bf16 v[148:151],v[228:231],v[72:79],v248
ra 208,224,36864
maav 48,80,72
maav 48,112,76
maav 52,84,72
ra 212,224,37888
maav 52,116,76
ex 0
maav 56,88,72
ra 216,224,38912
maav 56,120,76
ds 255,8,78,3072
maav 60,92,72
ex 1
maav 60,124,76
ex 2
mvav 160,64,80
ra 220,224,39936
cv 0,0,1
mvav 160,96,84
ex 3
mvav 164,68,80
ra 224,224,40960
cv 1,2,3
mvav 164,100,84
s_mov_b32 m0,s75
ex 4
mvav 168,72,80
ds 255,8,79,0
mvav 168,104,84
ra 228,224,41984
mvav 172,76,80
s_add_u32 s46,s46,16384
ex 5
mvav 172,108,84
s_add_u32 s47,s47,32768
ex 6
v_smfmac_f32_16x16x64_bf16 v[152:155],v[228:231],v[80:87],v248
ra 232,224,43008
mvav 176,80,80
ds 255,8,79,1024
mvav 176,112,84
s_add_u32 s95,s47,32768
ex 7
mvav 180,84,80
ra 236,224,44032
cv 2,4,5
mvav 180,116,84
ex 32
mvav 184,88,80
ex 33
mvav 184,120,84
ra 240,224,45056
cv 3,6,7
mvav 188,92,80
ex 34
mvav 188,124,84
ds 255,8,79,2048
mvav 192,64,88
ra 244,224,46080
cv 4,32,33
mvav 192,96,92
ex 35
mvav 196,68,88
ex 36
mvav 196,100,92
ra 248,224,47104
cv 5,34,35
mvav 200,72,88
ex 37
mvav 200,104,92
ex 38
mvav 204,76,88
ds 255,8,79,3072
mvav 204,108,92
s_cmp_ge_i32 s47,s46
ex 39
v_smfmac_f32_16x16x64_bf16 v[156:159],v[228:231],v[88:95],v248
ra 252,224,48128
mvav 208,80,88
s_cselect_b32 s78,s95,s46
ex 8
mvav 208,112,92
s_cselect_b32 s79,s95,268435456
ex 9
mvav 212,84,88
s_add_u32 s79,s79,16384
ex 10
mvav 212,116,92
ex 11
mvav 216,88,88
cv 6,36,37
cv 7,38,39
mvav 216,120,92
)"
R"(ex 12
mvav 220,92,88
ex 13
mvav 220,124,92
s_waitcnt vmcnt(16) lgkmcnt(0)
s_barrier
cvaa 64,192,128,232
s_bitcmp0_b32 s82,s81
s_cbranch_scc1 L_loop3
s_bitcmp0_b32 s15,s81
s_cbranch_scc1 L_eva_r3
L_short3:
v_add_f32_e32 v226,v152,v153
v_add_f32_e32 v227,v156,v157
v_subrev_u32_e32 v236,0x23800000,v226
v_subrev_u32_e32 v237,0x23800000,v227
v_max_u32_e32 v236,v236,v237
v_cmp_lt_u32_e64 s[40:41],s48,v236
v_rcp_f32_e32 v226,v226
v_rcp_f32_e32 v227,v227
s_add_u32 s86,s13,4096
v_mov_b32_e32 v236,0
v_mov_b32_e32 v237,0
v_mov_b32_e32 v238,0
v_mov_b32_e32 v239,0
v_mov_b64_e32 v[152:153],0
v_mov_b64_e32 v[156:157],0
v_mul_f32_e32 v226,v242,v226
v_mul_f32_e32 v227,v243,v227
v_add_u32_e32 v225,s94,v225
v_pk_mul_f32 v[160:161],v[160:161],v[226:227] op_sel:[0,0] op_sel_hi:[1,0]
v_pk_mul_f32 v[162:163],v[162:163],v[226:227] op_sel:[0,0] op_sel_hi:[1,0]
v_pk_mul_f32 v[164:165],v[164:165],v[226:227] op_sel:[0,0] op_sel_hi:[1,0]
v_pk_mul_f32 v[166:167],v[166:167],v[226:227] op_sel:[0,0] op_sel_hi:[1,0]
cv 160,160,161
cv 161,162,163
cv 162,164,165
cv 163,166,167
buffer_store_dwordx4 v[160:163],v244,s[8:11],s13 offen offset:0 sc0 sc1
v_pk_mul_f32 v[168:169],v[168:169],v[226:227] op_sel:[0,0] op_sel_hi:[1,0]
v_pk_mul_f32 v[170:171],v[170:171],v[226:227] op_sel:[0,0] op_sel_hi:[1,0]
v_pk_mul_f32 v[172:173],v[172:173],v[226:227] op_sel:[0,0] op_sel_hi:[1,0]
v_pk_mul_f32 v[174:175],v[174:175],v[226:227] op_sel:[0,0] op_sel_hi:[1,0]
cv 168,168,169
cv 169,170,171
cv 170,172,173
cv 171,174,175
buffer_store_dwordx4 v[168:171],v244,s[8:11],s13 offen offset:2048 sc0 sc1
v_pk_mul_f32 v[176:177],v[176:177],v[226:227] op_sel:[0,0] op_sel_hi:[1,0]
v_pk_mul_f32 v[178:179],v[178:179],v[226:227] op_sel:[0,0] op_sel_hi:[1,0]
v_pk_mul_f32 v[180:181],v[180:181],v[226:227] op_sel:[0,0] op_sel_hi:[1,0]
v_pk_mul_f32 v[182:183],v[182:183],v[226:227] op_sel:[0,0] op_sel_hi:[1,0]
cv 176,176,177
cv 177,178,179
cv 178,180,181
v_mfma_f32_32x32x16_bf16 v[160:175],v[236:239],v[236:239],0
cv 179,182,183
buffer_store_dwordx4 v[176:179],v244,s[8:11],s86 offen offset:0 sc0 sc1
v_pk_mul_f32 v[184:185],v[184:185],v[226:227] op_sel:[0,0] op_sel_hi:[1,0]
v_pk_mul_f32 v[186:187],v[186:187],v[226:227] op_sel:[0,0] op_sel_hi:[1,0]
v_pk_mul_f32 v[188:189],v[188:189],v[226:227] op_sel:[0,0] op_sel_hi:[1,0]
v_pk_mul_f32 v[190:191],v[190:191],v[226:227] op_sel:[0,0] op_sel_hi:[1,0]
cv 184,184,185
cv 185,186,187
cv 186,188,189
cv 187,190,191
buffer_store_dwordx4 v[184:187],v244,s[8:11],s86 offen offset:2048 sc0 sc1
v_pk_mul_f32 v[192:193],v[192:193],v[226:227] op_sel:[0,1] op_sel_hi:[1,1]
v_pk_mul_f32 v[194:195],v[194:195],v[226:227] op_sel:[0,1] op_sel_hi:[1,1]
v_pk_mul_f32 v[196:197],v[196:197],v[226:227] op_sel:[0,1] op_sel_hi:[1,1]
v_pk_mul_f32 v[198:199],v[198:199],v[226:227] op_sel:[0,1] op_sel_hi:[1,1]
cv 192,192,193
cv 193,194,195
cv 194,196,197
v_mfma_f32_32x32x16_bf16 v[176:191],v[236:239],v[236:239],0
cv 195,198,199
buffer_store_dwordx4 v[192:195],v244,s[8:11],s13 offen offset:1024 sc0 sc1
v_pk_mul_f32 v[200:201],v[200:201],v[226:227] op_sel:[0,1] op_sel_hi:[1,1]
v_pk_mul_f32 v[202:203],v[202:203],v[226:227] op_sel:[0,1] op_sel_hi:[1,1]
v_pk_mul_f32 v[204:205],v[204:205],v[226:227] op_sel:[0,1] op_sel_hi:[1,1]
v_pk_mul_f32 v[206:207],v[206:207],v[226:227] op_sel:[0,1] op_sel_hi:[1,1]
cv 200,200,201
cv 201,202,203
cv 202,204,205
cv 203,206,207
buffer_store_dwordx4 v[200:203],v244,s[8:11],s13 offen offset:3072 sc0 sc1
v_pk_mul_f32 v[208:209],v[208:209],v[226:227] op_sel:[0,1] op_sel_hi:[1,1]
v_pk_mul_f32 v[210:211],v[210:211],v[226:227] op_sel:[0,1] op_sel_hi:[1,1]
v_pk_mul_f32 v[212:213],v[212:213],v[226:227] op_sel:[0,1] op_sel_hi:[1,1]
v_pk_mul_f32 v[214:215],v[214:215],v[226:227] op_sel:[0,1] op_sel_hi:[1,1]
cv 208,208,209
cv 209,210,211
cv 210,212,213
v_mfma_f32_32x32x16_bf16 v[192:207],v[236:239],v[236:239],0
cv 211,214,215
buffer_store_dwordx4 v[208:211],v244,s[8:11],s86 offen offset:1024 sc0 sc1
v_pk_mul_f32 v[216:217],v[216:217],v[226:227] op_sel:[0,1] op_sel_hi:[1,1]
v_pk_mul_f32 v[218:219],v[218:219],v[226:227] op_sel:[0,1] op_sel_hi:[1,1]
v_pk_mul_f32 v[220:221],v[220:221],v[226:227] op_sel:[0,1] op_sel_hi:[1,1]
v_pk_mul_f32 v[222:223],v[222:223],v[226:227] op_sel:[0,1] op_sel_hi:[1,1]
cv 216,216,217
cv 217,218,219
cv 218,220,221
cv 219,222,223
buffer_store_dwordx4 v[216:219],v244,s[8:11],s86 offen offset:3072 sc0 sc1
s_nop 1
v_mfma_f32_32x32x16_bf16 v[208:223],v[236:239],v[236:239],0
L_eva_r3:
s_cmp_lg_u32 s81,14
s_cbranch_scc1 L_loop3
s_add_u32 s81,s81,1
s_bitcmp1_b32 170,s20
s_cbranch_scc1 L_odd
L_last:
L_vs1:
s_cbranch_execz L_vs5
s_lshl_b32 s86,s21,2
s_sub_u32 s87,s13,s67
s_add_u32 s86,s86,s12
mvaa 64,200,132
s_lshl_b32 s86,s86,8
v_add_u32_e32 v252,s87,v244
s_add_u32 s86,s86,L_last-L_att_pc+1024
v_lshl_add_u32 v226,v244,3,s86
s_lshl_b32 s86,s12,1
s_bfe_u32 s87,s21,65539
mvaa 64,208,136
s_add_u32 s86,s86,s87
v_min_u32_e32 v226,L_att_end-L_att_pc-4,v226
s_lshl_b32 s86,s86,7
s_add_u32 s86,s86,L_pend2-L_att_pc
v_writelane_b32 v226,s86,2
mvaa 64,216,140
s_mov_b64 exec,7
global_load_dword v253,v226,s[96:97]
s_mov_b64 exec,-1
cvaa 68,196,128,232
s_add_u32 m0,s52,32768
v_and_b32_e32 v224,16,v244
v_lshlrev_b32_e32 v224,5,v224
mvaa 68,204,132
s_xor_b32 s51,s62,0
v_lshrrev_b32_e32 v239,4,v244
v_lshrrev_b32_e32 v236,3,v239
mvaa 68,212,136
s_lshl_b32 s87,s12,13
v_and_b32_e32 v237,112,v244
v_and_b32_e32 v238,48,v239
mvaa 68,220,140
s_xor_b32 s87,s87,8192
ra 64,225,0
v_lshlrev_b32_e32 v238,1,v238
cvaa 96,224,128,232
buffer_load_dword v253,v224,s[8:11],s53 offen sc1
mvaa 96,232,132
s_mov_b32 s84,6
ra 68,225,1024
v_xor_b32_e32 v237,v237,v238
mvaa 96,240,136
s_add_u32 s81,s81,1
ra 72,225,2048
v_lshl_add_u32 v245,v236,11,v237
mvaa 96,248,140
s_mov_b32 s84,4
v_add_u32_e32 v246,15360,v245
v_add_u32_e32 v249,30720,v245
cvaa 100,228,128,232
s_andn2_b32 s69,64,s54
ra 76,225,3072
s_waitcnt vmcnt(9)
v_add_u32_e32 v247,46080,v245
mvaa 100,236,132
dl 245,24,51,0
mvaa 100,244,136
s_add_u32 s88,s14,4096
ra 80,225,4096
cv 8,8,9
mvaa 100,252,140
s_lshl_b32 s35,s19,1
ra 84,225,5120
cv 9,10,11
cvaa 72,192,144,232
s_lshr_b32 s33,40569,s35
ra 88,225,6144
cv 10,12,13
mvaa 72,200,148
s_and_b32 s33,s33,3
ra 92,225,7168
v_add_u32_e32 v250,4096,v252
mvaa 72,208,152
L_vs2:
s_cbranch_execz L_vs6
dl 246,24,51,1024
mvaa 72,216,156
s_lshl_b32 s33,s33,13
ra 96,225,8192
v_mov_b32_e32 v251,4
cvaa 76,196,144,232
s_lshr_b32 s34,59294,s35
ra 100,225,9216
s_and_b32 s34,s34,3
mvaa 76,204,148
s_lshl_b32 s34,s34,13
ra 104,225,10240
s_xor_b32 s35,s33,s34
mvaa 76,212,152
s_and_b32 s98,s19,3
ra 108,225,11264
s_lshl_b32 s98,s98,13
mvaa 76,220,156
dl 249,24,51,2048
cvaa 104,224,144,232
s_mov_b32 s74,0x300003
ra 112,225,12288
s_mov_b32 s75,0x30000300
mvaa 104,232,148
s_lshl_b32 s71,s12,15
ra 116,225,13312
s_add_u32 s71,s71,s54
mvaa 104,240,152
s_mov_b64 s[44:45],s[30:31]
ra 120,225,14336
s_mov_b32 s46,4194304
mvaa 104,248,156
s_mov_b32 s47,131072
ex 14
cvaa 108,228,144,232
dl 247,24,51,3072
mvaa 108,236,148
s_add_u32 m0,s52,49152
s_xor_b32 s51,s63,0
ra 124,225,15360
mvaa 108,244,152
s_lshl_b32 s70,s23,11
ex 15
mvaa 108,252,156
s_add_u32 s70,s70,s71
ex 40
cvaa 80,192,160,232
ex 41
mvaa 80,200,164
dl 245,24,51,0
mvaa 80,208,168
ex 42
mvaa 80,216,172
ex 43
cvaa 84,196,160,232
ex 44
mvaa 84,204,164
ex 45
mvaa 84,212,168
dl 246,24,51,1024
mvaa 84,220,172
ex 46
cvaa 112,224,160,232
ex 47
mvaa 112,232,164
ex 16
mvaa 112,240,168
ex 17
mvaa 112,248,172
dl 249,24,51,2048
cvaa 116,228,160,232
ex 18
mvaa 116,236,164
ex 19
mvaa 116,244,168
ex 20
mvaa 116,252,172
ex 21
cvaa 88,192,176,232
dl 247,24,51,3072
s_waitcnt vmcnt(8)
v_cmp_ne_u32_e32 vcc,4,v253
mvaa 88,200,180
s_cbranch_vccnz L_spbe
L_sprbe:
ex 22
mvaa 88,208,184
s_xor_b32 s50,s66,262144
ex 23
mvaa 88,216,188
ex 48
cvaa 92,196,176,232
ex 49
mvaa 92,204,180
la 128,252,50,0
mvaa 92,212,184
s_waitcnt lgkmcnt(0)
s_barrier
mvaa 92,220,188
s_mov_b32 m0,s72
ex 50
cvaa 120,224,176,232
s_lshl_b32 s72,s22,11
ex 51
mvaa 120,232,180
s_add_u32 s72,s72,s71
ex 52
mvaa 120,240,184
ds 255,8,76,0
mvaa 120,248,188
s_add_u32 s71,s70,131072
ex 53
cvaa 124,228,176,232
ex 54
mvaa 124,236,180
ex 55
mvaa 124,244,184
ex 24
mvaa 124,252,188
ds 255,8,76,1024
maav 0,64,0
s_cmp_lg_u32 s19,7
s_cbranch_scc1 L_j7e
s_nop 1
s_lshl_b32 s86,s12,12
s_add_u32 s86,s86,131072
v_add_u32_e32 v236,s86,v244
ra 160,236,0
ra 164,236,1024
ra 168,236,2048
ra 172,236,3072
ra 176,236,16384
ra 180,236,17408
ra 184,236,18432
ra 188,236,19456
v_add_u32_e32 v224,s94,v224
s_waitcnt lgkmcnt(0)
L_j7e:
ex 25
maav 0,96,4
ex 26
maav 4,68,0
ex 27
maav 4,100,4
cv 11,14,15
cv 12,40,41
maav 8,72,0
ds 255,8,76,2048
maav 8,104,4
cv 13,42,43
cv 14,44,45
maav 12,76,0
cv 15,46,47
cv 16,16,17
maav 12,108,4
ex 28
v_smfmac_f32_16x16x64_bf16 v[144:147],v[228:231],v[0:7],v248
cv 17,18,19
maav 16,80,0
ds 255,8,76,3072
maav 16,112,4
s_mov_b32 m0,s73
ex 29
maav 20,84,0
s_add_u32 s73,s72,131072
ex 30
maav 20,116,4
ex 31
maav 24,88,0
ex 56
maav 24,120,4
ds 255,8,77,0
maav 28,92,0
ex 57
maav 28,124,4
ex 58
maav 32,64,8
ex 59
maav 32,96,12
ex 60
maav 36,68,8
ds 255,8,77,1024
maav 36,100,12
cv 18,20,21
cv 19,22,23
maav 40,72,8
cv 20,48,49
cv 21,50,51
maav 40,104,12
cv 22,52,53
cv 23,54,55
maav 44,76,8
ex 61
maav 44,108,12
ds 255,8,77,2048
v_smfmac_f32_16x16x64_bf16 v[148:151],v[228:231],v[8:15],v248
cv 24,24,25
maav 48,80,8
ex 62
maav 48,112,12
ex 63
maav 52,84,8
ra 192,244,0
cv 25,26,27
maav 52,116,12
ds 255,8,77,3072
maav 56,88,8
cv 26,28,29
cv 27,30,31
maav 56,120,12
cv 28,56,57
cv 29,58,59
maav 60,92,8
cv 30,60,61
cv 31,62,63
maav 60,124,12
ex 64
mvav 160,64,16
la 132,252,50,2048
mvav 160,96,20
ex 65
mvav 164,68,16
ex 66
mvav 164,100,20
ex 67
mvav 168,72,16
ex 68
mvav 168,104,20
la 136,250,50,0
mvav 172,76,16
ex 69
mvav 172,108,20
ex 70
v_smfmac_f32_16x16x64_bf16 v[152:155],v[228:231],v[16:23],v248
ra 200,244,2048
mvav 176,80,16
ra 208,244,4096
cv 64,64,65
mvav 176,112,20
la 140,250,50,2048
mvav 180,84,16
ra 216,244,6144
cv 65,66,67
mvav 180,116,20
ex 71
mvav 184,88,16
ex 96
mvav 184,120,20
s_barrier
ex 97
mvav 188,92,16
la 156,250,50,3072
mvav 188,124,20
s_add_u32 m0,s52,131072
ra 196,244,1024
s_xor_b32 s51,s62,1048576
mvav 192,64,24
ra 204,244,3072
cv 66,68,69
mvav 192,96,28
ra 212,244,5120
cv 67,70,71
mvav 196,68,24
ra 220,244,7168
cv 68,96,97
mvav 196,100,28
la 152,250,50,1024
mvav 200,72,24
ex 98
mvav 200,104,28
ex 99
mvav 204,76,24
ra 224,244,8192
cv 69,98,99
mvav 204,108,28
ra 232,244,10240
v_add_u32_e32 v6,s87,v244
v_smfmac_f32_16x16x64_bf16 v[156:159],v[228:231],v[24:31],v248
ra 240,244,12288
mvav 208,80,24
la 148,252,50,3072
mvav 208,112,28
s_xor_b32 s87,s52,4096
ra 248,244,14336
v_add_u32_e32 v7,98304,v6
mvav 212,84,24
s_add_u32 s87,s87,150528
ra 228,244,9216
v_add_u32_e32 v5,65536,v244
mvav 212,116,28
ra 236,244,11264
v_add_u32_e32 v32,s87,v244
mvav 216,88,24
ra 244,244,13312
v_and_b32_e32 v224,960,v244
mvav 216,120,28
la 144,252,50,1024
mvav 220,92,24
s_add_u32 s50,s50,s55
ra 252,244,15360
mvav 220,124,28
ra 64,225,32768
s_waitcnt lgkmcnt(8)
cvaa 24,192,176,232
dl 245,24,51,0
mvaa 24,200,180
ra 96,225,40960
mvaa 24,208,184
ra 68,225,33792
mvaa 24,216,188
dl 246,24,51,1024
cvaa 16,192,160,128
ra 100,225,41984
mvaa 16,200,164
ra 72,225,34816
mvaa 16,208,168
dl 249,24,51,2048
mvaa 16,216,172
ra 104,225,43008
cvaa 28,196,176,232
ra 76,225,35840
mvaa 28,204,180
dl 247,24,51,3072
mvaa 28,212,184
s_add_u32 m0,s52,147456
s_xor_b32 s51,s63,1048576
ra 108,225,44032
mvaa 28,220,188
ra 80,225,36864
cvaa 20,196,160,132
dl 245,24,51,0
mvaa 20,204,164
ra 112,225,45056
mvaa 20,212,168
ra 192,244,16384
mvaa 20,220,172
dl 246,24,51,1024
cvaa 56,224,176,232
ra 200,244,18432
s_waitcnt lgkmcnt(7)
mvaa 56,232,180
ra 208,244,20480
mvaa 56,240,184
dl 249,24,51,2048
mvaa 56,248,188
ra 216,244,22528
cvaa 48,224,160,136
ra 196,244,17408
mvaa 48,232,164
ra 204,244,19456
mvaa 48,240,168
ra 212,244,21504
mvaa 48,248,172
ra 220,244,23552
cvaa 60,228,176,232
ra 224,244,24576
mvaa 60,236,180
ra 232,244,26624
mvaa 60,244,184
ra 240,244,28672
mvaa 60,252,188
ra 248,244,30720
cvaa 52,228,160,140
ra 84,225,37888
mvaa 52,236,164
ra 116,225,46080
mvaa 52,244,168
la 160,252,50,0
ra 228,244,25600
mvaa 52,252,172
ra 236,244,27648
s_waitcnt lgkmcnt(10)
cvaa 8,192,176,128
ra 244,244,29696
mvaa 8,200,180
la 164,252,50,2048
ra 252,244,31744
mvaa 8,208,184
ra 88,225,38912
mvaa 8,216,188
ra 120,225,47104
cvaa 12,196,176,132
la 168,250,50,0
mvaa 12,204,180
ra 92,225,39936
ra 124,225,48128
s_waitcnt lgkmcnt(9)
mvaa 12,212,184
ex 100
mvaa 12,220,188
la 172,250,50,2048
cvaa 40,224,176,136
ex 101
mvaa 40,232,180
ex 102
mvaa 40,240,184
ex 103
mvaa 40,248,188
cv 70,100,101
cv 71,102,103
s_waitcnt lgkmcnt(4)
cvaa 44,228,176,140
ex 72
mvaa 44,236,180
s_waitcnt vmcnt(18) lgkmcnt(0)
s_barrier
ex 73
mvaa 44,244,184
la 176,252,50,1024
mvaa 44,252,188
ex 74
maav 0,64,64
ex 75
maav 0,96,68
la 188,250,50,3072
maav 4,68,64
ra 192,5,0
cv 72,72,73
maav 4,100,68
ra 196,5,1024
cv 73,74,75
maav 8,72,64
la 184,250,50,1024
ds_write_b128 v6,a[0:3] offset:0
maav 8,104,68
ex 76
maav 12,76,64
ex 77
maav 12,108,68
la 180,252,50,3072
ds_write_b128 v6,a[4:7] offset:1024
v_smfmac_f32_16x16x64_bf16 v[144:147],v[228:231],v[64:71],v248
cv 74,76,77
maav 16,80,64
ex 78
maav 16,112,68
ds_write_b128 v6,a[8:11] offset:2048
ex 79
maav 20,84,64
ex 104
maav 20,116,68
ds_write_b128 v6,a[12:15] offset:3072
ex 105
maav 24,88,64
ex 106
ex 107
ex 108
maav 24,120,68
ds_write_b128 v6,a[16:19] offset:4096
cv 75,78,79
maav 28,92,64
ex 109
ex 110
ex 111
maav 28,124,68
cv 76,104,105
cv 77,106,107
cv 78,108,109
cv 79,110,111
maav 32,64,72
ds_write_b128 v6,a[20:23] offset:5120
maav 32,96,76
ex 80
maav 36,68,72
ex 81
maav 36,100,76
ds_write_b128 v6,a[24:27] offset:6144
maav 40,72,72
ex 82
maav 40,104,76
ex 83
maav 44,76,72
ds_write_b128 v6,a[28:31] offset:7168
maav 44,108,76
cv 80,80,81
cv 81,82,83
v_smfmac_f32_16x16x64_bf16 v[148:151],v[228:231],v[72:79],v248
ex 84
maav 48,80,72
ds_write_b128 v7,a[32:35] offset:0
maav 48,112,76
ex 85
ex 86
ex 87
maav 52,84,72
ex 112
ex 113
maav 52,116,76
ds_write_b128 v7,a[36:39] offset:1024
cv 82,84,85
maav 56,88,72
ex 114
ex 115
ex 116
maav 56,120,76
ex 117
ex 118
ex 119
maav 60,92,72
ds_write_b128 v7,a[40:43] offset:2048
cv 83,86,87
maav 60,124,76
cv 84,112,113
cv 85,114,115
cv 86,116,117
cv 87,118,119
mvav 160,64,80
ex 88
mvav 160,96,84
ds_write_b128 v7,a[44:47] offset:3072
mvav 164,68,80
v_add_f32_e32 v130,v144,v145
v_add_f32_e32 v131,v148,v149
mvav 164,100,84
ex 89
mvav 168,72,80
ds_write_b128 v7,a[48:51] offset:4096
mvav 168,104,84
ex 90
mvav 172,76,80
ex 91
mvav 172,108,84
ds_write_b128 v32,v[128:131]
v_smfmac_f32_16x16x64_bf16 v[152:155],v[228:231],v[80:87],v248
ex 92
ex 93
mvav 176,80,80
ds_write_b128 v7,a[52:55] offset:5120
ex 94
mvav 176,112,84
ra 200,5,2048
ra 204,5,3072
ex 95
cv 88,88,89
mvav 180,84,80
ra 208,5,4096
ra 212,5,5120
cv 89,90,91
cv 90,92,93
ex 120
mvav 180,116,84
ds_write_b128 v7,a[56:59] offset:6144
ex 121
mvav 184,88,80
ex 122
ex 123
cv 91,94,95
mvav 184,120,84
ex 124
ex 125
mvav 188,92,80
ds_write_b128 v7,a[60:63] offset:7168
ex 126
mvav 188,124,84
ra 216,5,6144
ra 220,5,7168
ra 224,5,8192
ra 232,5,10240
ex 127
cv 92,120,121
mvav 192,64,88
ra 240,5,12288
ra 248,5,14336
ra 228,5,9216
ra 236,5,11264
ra 244,5,13312
ra 252,5,15360
cv 93,122,123
cv 94,124,125
cv 95,126,127
v_xor_b32_e32 v6,8192,v6
s_nop 0
mvav 192,96,92
ex 16
mvav 196,68,88
s_waitcnt lgkmcnt(10)
s_xor_b32 s87,s87,0
mvav 196,100,92
s_xor_b32 s50,s66,294912
s_barrier
v_xor_b32_e32 v7,8192,v7
mvav 200,72,88
la 4,252,50,0
ra 64,5,16384
ra 96,5,24576
rv 64,6,0
rv 68,6,1024
rv 72,6,2048
mvav 200,104,92
L_vs3:
s_cbranch_execz L_vs7
rv 76,6,3072
rv 80,6,4096
rv 84,6,5120
ex 17
ex 18
mvav 204,76,88
rv 96,7,0
rv 100,7,1024
ex 19
mvav 204,108,92
la 12,252,50,2048
rv 104,7,2048
rv 108,7,3072
cv 16,16,17
v_smfmac_f32_16x16x64_bf16 v[156:159],v[228:231],v[88:95],v248
rv 112,7,4096
rv 116,7,5120
ex 20
ex 21
mvav 208,80,88
rv 120,7,6144
rv 124,7,7168
ex 22
ex 23
mvav 208,112,92
la 20,250,50,0
ra 68,5,17408
ra 100,5,25600
cv 17,18,19
cv 18,20,21
mvav 212,84,88
ra 72,5,18432
ra 104,5,26624
ex 48
ex 49
mvav 212,116,92
ra 76,5,19456
ra 108,5,27648
ex 50
ex 51
mvav 216,88,88
la 28,250,50,2048
ra 80,5,20480
ra 112,5,28672
ra 84,5,21504
mvav 216,120,92
ra 116,5,29696
cv 19,22,23
ex 52
ex 53
mvav 220,92,88
ra 88,5,22528
cv 20,48,49
ex 54
ex 55
mvav 220,124,92
la 60,250,50,3072
ra 120,5,30720
cv 21,50,51
cv 22,52,53
cv 23,54,55
s_bitcmp1_b32 s82,s81
s_cbranch_scc1 L_pend2
L_ret_eb4:
mvav 160,192,16
ra 92,5,23552
rv 88,6,6144
ra 124,5,31744
rv 92,6,7168
ex 24
s_waitcnt lgkmcnt(10)
mvav 160,224,20
ex 25
ex 26
v_smfmac_f32_16x16x64_bf16 v[152:155],v[228:231],v[16:23],v248
ex 27
v_xor_b32_e32 v32,4096,v32
mvav 164,196,16
la 52,250,50,1024
ds_read_b128 v[236:239],v32
cv 24,24,25
ex 28
mvav 164,228,20
ex 29
cv 25,26,27
ex 30
ex 31
mvav 168,200,16
ex 56
ex 57
mvav 168,232,20
la 44,252,50,3072
cv 26,28,29
ex 58
ex 59
mvav 172,204,16
ex 60
ex 61
ex 62
mvav 172,236,20
s_waitcnt lgkmcnt(0)
s_barrier
cv 27,30,31
ex 63
mvav 176,208,16
la 36,252,50,1024
cv 28,56,57
cv 29,58,59
cv 30,60,61
cv 31,62,63
mvav 176,240,20
s_add_u32 m0,s52,147456
ex 8
s_xor_b32 s51,s63,1048576
ex 9
ex 10
mvav 180,212,16
s_add_u32 s50,s50,s55
ex 11
cv 8,8,9
cv 9,10,11
ex 12
mvav 180,244,20
dl 247,24,51,3072
ex 13
ex 14
mvav 184,216,16
s_add_u32 m0,s52,0
ex 15
ex 40
ex 41
mvav 184,248,20
s_xor_b32 s51,s64,0
ex 42
ex 43
ex 44
mvav 188,220,16
la 0,252,50,0
ex 45
ex 46
mvav 188,252,20
ex 47
cv 10,12,13
cv 11,14,15
cv 12,40,41
cv 13,42,43
cv 14,44,45
v_smfmac_f32_16x16x64_bf16 v[156:159],v[228:231],v[24:31],v248
cv 15,46,47
v_bfe_u32 v225,v244,1,5
s_nop 0
v_smfmac_f32_16x16x64_bf16 v[156:159],v[228:231],v[8:15],v248
v_lshl_add_u32 v224,v224,1,v225
v_bfe_u32 v225,v244,7,2
v_lshl_add_u32 v224,v225,5,v224
mvav 192,192,24
la 8,252,50,2048
v_xor_b32_e32 v224,s69,v224
v_add_f32_e32 v152,v152,v153
v_add_f32_e32 v137,v152,v238
v_subrev_u32_e32 v236,0x23800000,v137
mvav 192,224,28
v_rcp_f32_e32 v137,v137
mvav 192,64,8
tv 128,224,32768
v_mul_f32_e32 v232,v240,v137
v_mul_f32_e32 v64,v232,v64
v_mul_f32_e32 v65,v232,v65
v_mul_f32_e32 v66,v232,v66
mvav 192,96,12
la 16,250,50,0
v_mul_f32_e32 v67,v232,v67
v_mul_f32_e32 v68,v232,v68
v_mul_f32_e32 v69,v232,v69
v_mul_f32_e32 v70,v232,v70
mvav 196,196,24
v_fmac_f32_e32 v64,v232,v160
v_fmac_f32_e32 v65,v232,v161
v_mul_f32_e32 v71,v232,v71
mvav 196,228,28
v_fmac_f32_e32 v66,v232,v162
v_fmac_f32_e32 v67,v232,v163
v_fmac_f32_e32 v68,v232,v164
v_fmac_f32_e32 v69,v232,v165
v_fmac_f32_e32 v70,v232,v166
mvav 196,68,8
la 24,250,50,2048
v_fmac_f32_e32 v71,v232,v167
cv 160,64,65
cv 161,66,67
cv 162,68,69
cv 163,70,71
mvav 196,100,12
tv 130,224,34816
tv 132,224,36864
mvav 200,200,24
tv 134,224,38912
tv 144,224,40960
v_mul_f32_e32 v72,v232,v72
v_mul_f32_e32 v73,v232,v73
v_mul_f32_e32 v74,v232,v74
mvav 200,232,28
buffer_store_dwordx4 v[160:163],v244,s[8:11],s14 offen offset:0 sc0 sc1
v_mul_f32_e32 v75,v232,v75
v_mul_f32_e32 v76,v232,v76
v_mul_f32_e32 v77,v232,v77
v_mul_f32_e32 v78,v232,v78
mvav 200,72,8
v_fmac_f32_e32 v72,v232,v168
v_fmac_f32_e32 v73,v232,v169
v_mul_f32_e32 v79,v232,v79
mvav 200,104,12
v_fmac_f32_e32 v74,v232,v170
v_fmac_f32_e32 v75,v232,v171
v_fmac_f32_e32 v76,v232,v172
v_fmac_f32_e32 v77,v232,v173
v_fmac_f32_e32 v78,v232,v174
)"
R"(mvav 204,204,24
la 56,250,50,3072
v_fmac_f32_e32 v79,v232,v175
cv 168,72,73
cv 169,74,75
cv 170,76,77
cv 171,78,79
mvav 204,236,28
tv 146,224,43008
tv 148,224,45056
mvav 204,76,8
tv 150,224,47104
v_mul_f32_e32 v80,v232,v80
v_mul_f32_e32 v81,v232,v81
v_mul_f32_e32 v82,v232,v82
mvav 204,108,12
buffer_store_dwordx4 v[168:171],v244,s[8:11],s14 offen offset:2048 sc0 sc1
v_mul_f32_e32 v83,v232,v83
v_mul_f32_e32 v84,v232,v84
v_mul_f32_e32 v85,v232,v85
v_mul_f32_e32 v86,v232,v86
v_mul_f32_e32 v87,v232,v87
mvav 208,208,24
v_fmac_f32_e32 v80,v232,v176
v_fmac_f32_e32 v81,v232,v177
v_fmac_f32_e32 v82,v232,v178
mvav 208,240,28
v_fmac_f32_e32 v83,v232,v179
v_fmac_f32_e32 v84,v232,v180
v_fmac_f32_e32 v85,v232,v181
cv 176,80,81
cv 177,82,83
mvav 208,80,8
la 48,250,50,1024
v_fmac_f32_e32 v86,v232,v182
v_fmac_f32_e32 v87,v232,v183
cv 178,84,85
cv 179,86,87
mvav 208,112,12
v_mul_f32_e32 v88,v232,v88
v_mul_f32_e32 v89,v232,v89
v_mul_f32_e32 v90,v232,v90
mvav 212,212,24
v_mul_f32_e32 v91,v232,v91
v_mul_f32_e32 v92,v232,v92
v_mul_f32_e32 v93,v232,v93
v_mul_f32_e32 v94,v232,v94
v_mul_f32_e32 v95,v232,v95
mvav 212,244,28
buffer_store_dwordx4 v[176:179],v244,s[8:11],s88 offen offset:0 sc0 sc1
v_fmac_f32_e32 v88,v232,v184
v_fmac_f32_e32 v89,v232,v185
v_fmac_f32_e32 v90,v232,v186
v_fmac_f32_e32 v91,v232,v187
cv 184,88,89
mvav 212,84,8
v_fmac_f32_e32 v92,v232,v188
cv 185,90,91
mvav 212,116,12
v_fmac_f32_e32 v93,v232,v189
v_fmac_f32_e32 v94,v232,v190
v_fmac_f32_e32 v95,v232,v191
cv 186,92,93
cv 187,94,95
mvav 216,216,24
la 40,252,50,3072
v_add_f32_e32 v156,v156,v157
v_add_f32_e32 v141,v156,v239
v_subrev_u32_e32 v237,0x23800000,v141
v_rcp_f32_e32 v141,v141
mvav 216,248,28
v_mul_f32_e32 v233,v241,v141
v_mul_f32_e32 v96,v233,v96
mvav 216,88,8
v_mul_f32_e32 v97,v233,v97
v_mul_f32_e32 v98,v233,v98
v_mul_f32_e32 v99,v233,v99
v_mul_f32_e32 v100,v233,v100
mvav 216,120,12
buffer_store_dwordx4 v[184:187],v244,s[8:11],s88 offen offset:2048 sc0 sc1
v_fmac_f32_e32 v96,v233,v192
v_fmac_f32_e32 v97,v233,v193
v_mul_f32_e32 v101,v233,v101
v_mul_f32_e32 v102,v233,v102
v_mul_f32_e32 v103,v233,v103
mvav 220,220,24
v_fmac_f32_e32 v98,v233,v194
v_fmac_f32_e32 v99,v233,v195
mvav 220,252,28
v_fmac_f32_e32 v100,v233,v196
v_fmac_f32_e32 v101,v233,v197
cv 192,96,97
cv 193,98,99
mvav 220,92,8
la 32,252,50,1024
v_fmac_f32_e32 v102,v233,v198
v_fmac_f32_e32 v103,v233,v199
cv 194,100,101
cv 195,102,103
mvav 220,124,12
v_xor_b32_e32 v225,32,v224
v_xor_b32_e32 v226,64,v224
s_waitcnt lgkmcnt(0)
zvva 0,128,128
tv 138,225,34816
tv 142,225,38912
tv 152,225,40960
s_waitcnt vmcnt(36)
mvva 0,132,132
tv 136,225,32768
tv 154,225,43008
mvva 0,144,136
tv 140,225,36864
tv 158,225,47104
mvva 0,148,140
tv 156,225,45056
v_mul_f32_e32 v104,v233,v104
v_mul_f32_e32 v105,v233,v105
v_mul_f32_e32 v106,v233,v106
zvva 4,148,156
buffer_store_dwordx4 v[192:195],v244,s[8:11],s14 offen offset:1024 sc0 sc1
v_mul_f32_e32 v107,v233,v107
v_mul_f32_e32 v108,v233,v108
v_mul_f32_e32 v109,v233,v109
v_mul_f32_e32 v110,v233,v110
mvva 4,144,152
v_fmac_f32_e32 v104,v233,v200
v_fmac_f32_e32 v105,v233,v201
v_fmac_f32_e32 v106,v233,v202
v_mul_f32_e32 v111,v233,v111
mvva 4,132,148
v_fmac_f32_e32 v107,v233,v203
v_fmac_f32_e32 v108,v233,v204
v_fmac_f32_e32 v109,v233,v205
cv 200,104,105
cv 201,106,107
mvva 4,128,144
v_fmac_f32_e32 v110,v233,v206
v_fmac_f32_e32 v111,v233,v207
cv 202,108,109
cv 203,110,111
s_waitcnt vmcnt(16)
zvva 8,128,160
v_cmp_lt_u32_e64 s[42:43],s48,v236
v_mul_f32_e32 v112,v233,v112
s_or_b64 s[40:41],s[40:41],s[42:43]
v_cmp_lt_u32_e64 s[42:43],s48,v237
mvva 8,132,164
s_or_b64 s[40:41],s[40:41],s[42:43]
v_mul_f32_e32 v113,v233,v113
v_mul_f32_e32 v114,v233,v114
v_mul_f32_e32 v115,v233,v115
mvva 8,144,168
dl 245,36,51,0
v_mul_f32_e32 v116,v233,v116
v_mul_f32_e32 v117,v233,v117
v_mul_f32_e32 v118,v233,v118
v_mul_f32_e32 v119,v233,v119
v_mul_f32_e32 v120,v233,v120
mvva 8,148,172
v_fmac_f32_e32 v112,v233,v208
v_fmac_f32_e32 v113,v233,v209
v_fmac_f32_e32 v114,v233,v210
v_fmac_f32_e32 v115,v233,v211
zvva 12,148,188
v_fmac_f32_e32 v116,v233,v212
v_fmac_f32_e32 v117,v233,v213
cv 208,112,113
cv 209,114,115
mvva 12,144,184
v_fmac_f32_e32 v118,v233,v214
v_fmac_f32_e32 v119,v233,v215
cv 210,116,117
cv 211,118,119
mvva 12,132,180
v_mul_f32_e32 v121,v233,v121
v_mul_f32_e32 v122,v233,v122
v_mul_f32_e32 v123,v233,v123
mvva 12,128,176
v_mul_f32_e32 v124,v233,v124
v_mul_f32_e32 v125,v233,v125
v_mul_f32_e32 v126,v233,v126
v_mul_f32_e32 v127,v233,v127
s_waitcnt lgkmcnt(0)
zvva 28,136,176
buffer_store_dwordx4 v[200:203],v244,s[8:11],s14 offen offset:3072 sc0 sc1
tv 144,226,40960
v_fmac_f32_e32 v120,v233,v216
v_fmac_f32_e32 v121,v233,v217
v_fmac_f32_e32 v122,v233,v218
v_fmac_f32_e32 v123,v233,v219
mvva 28,140,180
tv 128,226,32768
tv 130,226,34816
cv 216,120,121
cv 217,122,123
mvva 28,152,184
tv 132,226,36864
tv 134,226,38912
v_fmac_f32_e32 v124,v233,v220
v_fmac_f32_e32 v125,v233,v221
v_fmac_f32_e32 v126,v233,v222
mvva 28,156,188
tv 146,226,43008
v_fmac_f32_e32 v127,v233,v223
cv 218,124,125
cv 219,126,127
zvva 24,156,172
tv 148,226,45056
tv 150,226,47104
mvva 24,152,168
v_xor_b32_e32 v227,96,v224
v_add_u32_e32 v232,131072,v224
mvva 24,140,164
buffer_store_dwordx4 v[208:211],v244,s[8:11],s88 offen offset:1024 sc0 sc1
mvva 24,136,160
v_add_u32_e32 v233,131072,v225
v_add_u32_e32 v234,131072,v226
zvva 20,136,144
v_add_u32_e32 v235,131072,v227
v_mov_b32_e32 v242,83914752
mvva 20,140,148
v_add_u32_e32 v228,65536,v224
v_add_u32_e32 v229,65536,v225
mvva 20,152,152
v_add_u32_e32 v230,65536,v226
v_add_u32_e32 v231,65536,v227
mvva 20,156,156
v_and_b32_e32 v243,240,v244
v_lshlrev_b32_e32 v243,7,v243
zvva 16,156,140
buffer_store_dwordx4 v[216:219],v244,s[8:11],s88 offen offset:3072 sc0 sc1
s_cmp_lg_u64 s[40:41],0
s_cbranch_scc1 L_redo
L_redo_r4:
mvva 16,152,136
tv 156,227,45056
v_mov_b32_e32 v236,16256
mvva 16,140,132
tv 158,227,47104
v_and_b32_e32 v248,256,v244
mvva 16,136,128
tv 152,227,40960
v_lshrrev_b32_e32 v248,3,v248
s_waitcnt lgkmcnt(3)
zvva 32,128,128
tv 140,227,36864
v_add_u32_e32 v243,v243,v248
mvva 32,132,132
tv 136,227,32768
tv 138,227,34816
mvva 32,144,136
dl 246,36,51,1024
tv 142,227,38912
mvva 32,148,140
tv 154,227,43008
v_and_b32_e32 v248,512,v244
zvva 36,148,156
v_lshrrev_b32_e32 v248,5,v248
v_add_u32_e32 v243,v243,v248
mvva 36,144,152
v_and_b32_e32 v248,16,v244
v_lshlrev_b32_e32 v248,v248,v236
mvva 36,132,148
v_cndmask_b32_e64 v238,0,v248,s[74:75]
s_mov_b32 s74,0xc0000c
mvva 36,128,144
s_mov_b32 s75,0xc0000c00
v_mov_b32_e32 v240,0
v_cndmask_b32_e64 v239,0,v248,s[74:75]
zvva 40,128,160
dl 249,36,51,2048
mvva 40,132,164
v_mov_b32_e32 v241,0
v_mov_b32_e32 v236,0
mvva 40,144,168
v_mov_b32_e32 v237,0
mvva 40,148,172
zvva 44,148,188
mvva 44,144,184
mvva 44,132,180
dl 247,36,51,3072
s_add_u32 m0,s52,16384
s_xor_b32 s51,s65,0
mvva 44,128,176
tv 144,224,57344
s_waitcnt lgkmcnt(1)
zvva 60,136,176
tv 132,224,53248
tv 134,224,55296
mvva 60,140,180
tv 128,224,49152
tv 130,224,51200
mvva 60,152,184
mvva 60,156,188
tv 146,224,59392
zvva 56,156,172
dl 245,36,51,0
tv 148,224,61440
tv 150,224,63488
mvva 56,152,168
mvva 56,140,164
mvva 56,136,160
zvva 52,136,144
mvva 52,140,148
mvva 52,152,152
dl 246,36,51,1024
mvva 52,156,156
zvva 48,156,140
mvva 48,152,136
mvva 48,140,132
tv 156,225,61440
mvva 48,136,128
tv 152,225,57344
s_waitcnt lgkmcnt(2)
zvva 64,128,128
dl 249,36,51,2048
tv 140,225,53248
mvva 64,132,132
tv 136,225,49152
tv 138,225,51200
mvva 64,144,136
tv 142,225,55296
mvva 64,148,140
tv 154,225,59392
zvva 68,148,156
tv 158,225,63488
mvva 68,144,152
mvva 68,132,148
dl 247,36,51,3072
s_lshl_b32 s51,s33,5
s_add_u32 m0,s52,98304
s_xor_b32 s51,s64,s51
mvva 68,128,144
zvva 72,128,160
mvva 72,132,164
s_waitcnt vmcnt(18)
s_mov_b32 s90,0
mvva 72,144,168
s_xor_b32 s50,s67,0
mvva 72,148,172
zvva 76,148,188
dl 245,36,51,0
mvva 76,144,184
mvva 76,132,180
tv 148,226,61440
mvva 76,128,176
tv 144,226,57344
s_waitcnt lgkmcnt(2)
zvva 92,136,176
tv 132,226,53248
mvva 92,140,180
tv 128,226,49152
tv 130,226,51200
mvva 92,152,184
dl 246,36,51,1024
tv 134,226,55296
mvva 92,156,188
tv 146,226,59392
zvva 88,156,172
tv 150,226,63488
mvva 88,152,168
mvva 88,140,164
mvva 88,136,160
zvva 84,136,144
dl 249,36,51,2048
mvva 84,140,148
mvva 84,152,152
mvva 84,156,156
zvva 80,156,140
mvva 80,152,136
mvva 80,140,132
dl 247,36,51,3072
mvva 80,136,128
s_lshl_b32 s51,s33,5
tv 152,227,57344
s_add_u32 m0,s52,114688
s_waitcnt lgkmcnt(1)
zvva 96,128,128
s_xor_b32 s51,s65,s51
tv 140,227,53248
tv 142,227,55296
mvva 96,132,132
tv 136,227,49152
tv 138,227,51200
mvva 96,144,136
mvva 96,148,140
tv 154,227,59392
zvva 100,148,156
lv 164,252,50,0
tv 156,227,61440
tv 158,227,63488
mvva 100,144,152
mvva 100,132,148
mvva 100,128,144
zvva 104,128,160
mvva 104,132,164
mvva 104,144,168
lv 172,252,50,2048
mvva 104,148,172
zvva 108,148,188
mvva 108,144,184
mvva 108,132,180
mvva 108,128,176
s_barrier
s_waitcnt lgkmcnt(0)
zvva 124,136,176
lv 180,250,50,0
tv 132,232,4096
tv 144,232,8192
tv 146,232,10240
mvva 124,140,180
L_vs4:
s_cbranch_execz L_vs8
tv 128,232,0
tv 130,232,2048
mvva 124,152,184
tv 134,232,6144
mvva 124,156,188
tv 148,232,12288
tv 150,232,14336
zvva 120,156,172
mvva 120,152,168
mvva 120,140,164
lv 188,250,50,2048
mvva 120,136,160
zvva 116,136,144
mvva 116,140,148
mvva 116,152,152
mvva 116,156,156
zvva 112,156,140
lv 220,250,50,3072
mvva 112,152,136
mvva 112,140,132
tv 156,233,12288
mvva 112,136,128
tv 152,233,8192
s_waitcnt lgkmcnt(2)
mvva 0,128,4
tv 140,233,4096
mvva 0,132,12
tv 136,233,0
tv 138,233,2048
mvva 0,144,20
lv 212,250,50,1024
tv 142,233,6144
mvva 0,148,28
tv 154,233,10240
mvva 4,148,60
tv 158,233,14336
mvva 4,144,52
mvva 4,132,44
mvva 4,128,36
mvva 8,128,0
lv 204,252,50,3072
mvva 8,132,8
mvva 8,144,16
mvva 8,148,24
mvva 12,148,56
s_waitcnt vmcnt(27)
mvva 12,144,48
s_waitcnt vmcnt(25)
mvva 12,132,40
lv 196,252,50,1024
s_waitcnt vmcnt(24)
mvva 12,128,32
tv 144,234,8192
s_waitcnt lgkmcnt(1)
mvva 28,136,32
tv 132,234,4096
tv 134,234,6144
mvva 28,140,40
tv 128,234,0
tv 130,234,2048
mvva 28,152,48
mvva 28,156,56
tv 146,234,10240
mvva 24,156,24
dl 245,36,51,0
s_waitcnt vmcnt(20)
s_barrier
tv 148,234,12288
tv 150,234,14336
mvva 24,152,16
mvva 24,140,8
mvva 24,136,0
mvva 20,136,36
mvva 20,140,44
mvva 20,152,52
s_cmp_eq_u32 s12,0
s_cselect_b64 exec,1,0
buffer_store_dword v251,off,s[8:11],s61 sc0 sc1
s_mov_b64 exec,-1
mvva 20,156,60
mvva 16,156,28
mvva 16,152,20
mvva 16,140,12
tv 156,235,12288
mvva 16,136,4
tv 152,235,8192
s_waitcnt lgkmcnt(2)
mvva 32,128,4
dl 246,36,51,1024
tv 140,235,4096
mvva 32,132,12
tv 136,235,0
tv 138,235,2048
mvva 32,144,20
tv 142,235,6144
mvva 32,148,28
tv 154,235,10240
mvva 36,148,60
tv 158,235,14336
mvva 36,144,52
mvva 36,132,44
dl 249,36,51,2048
mvva 36,128,36
mvva 40,128,0
mvva 40,132,8
mvva 40,144,16
mvva 40,148,24
mvva 44,148,56
dl 247,36,51,3072
s_lshl_b32 s51,s34,5
s_add_u32 m0,s52,65536
s_xor_b32 s51,s64,s51
mvva 44,144,48
tv 148,232,28672
mvva 44,132,40
s_xor_b32 s53,s60,s33
tv 150,232,30720
mvva 44,128,32
tv 144,232,24576
s_waitcnt lgkmcnt(3)
mvva 60,136,32
tv 132,232,20480
mvva 60,140,40
tv 128,232,16384
tv 130,232,18432
mvva 60,152,48
dl 245,36,51,0
tv 134,232,22528
mvva 60,156,56
tv 146,232,26624
mvva 56,156,24
mvva 56,152,16
mvva 56,140,8
mvva 56,136,0
mvva 52,136,36
buffer_load_dword v253,v242,s[8:11],s53 offen sc1
mvva 52,140,44
mvva 52,152,52
mvva 52,156,60
mvva 48,156,28
mvva 48,152,20
mvva 48,140,12
dl 246,36,51,1024
mvva 48,136,4
tv 152,233,24576
s_waitcnt lgkmcnt(1)
mvva 64,128,4
tv 140,233,20480
tv 142,233,22528
mvva 64,132,12
tv 136,233,16384
tv 138,233,18432
mvva 64,144,20
mvva 64,148,28
tv 154,233,26624
mvva 68,148,60
dl 249,36,51,2048
tv 156,233,28672
tv 158,233,30720
mvva 68,144,52
mvva 68,132,44
mvva 68,128,36
mvva 72,128,0
mvva 72,132,8
mvva 72,144,16
dl 247,36,51,3072
s_lshl_b32 s51,s34,5
s_add_u32 m0,s52,81920
s_xor_b32 s51,s65,s51
mvva 72,148,24
mvva 76,148,56
mvva 76,144,48
mvva 76,132,40
tv 148,234,28672
mvva 76,128,32
tv 144,234,24576
s_waitcnt lgkmcnt(2)
mvva 92,136,32
dl 245,36,51,0
tv 132,234,20480
mvva 92,140,40
tv 128,234,16384
tv 130,234,18432
mvva 92,152,48
tv 134,234,22528
mvva 92,156,56
tv 146,234,26624
mvva 88,156,24
tv 150,234,30720
mvva 88,152,16
mvva 88,140,8
dl 246,36,51,1024
mvva 88,136,0
mvva 84,136,36
mvva 84,140,44
mvva 84,152,52
mvva 84,156,60
mvva 80,156,28
dl 249,36,51,2048
mvva 80,152,20
mvva 80,140,12
tv 156,235,28672
mvva 80,136,4
tv 152,235,24576
s_waitcnt lgkmcnt(2)
mvva 96,128,4
tv 140,235,20480
mvva 96,132,12
tv 136,235,16384
tv 138,235,18432
mvva 96,144,20
dl 247,36,51,3072
s_lshl_b32 s51,s35,5
s_add_u32 m0,s52,32768
s_xor_b32 s51,s64,s51
tv 142,235,22528
mvva 96,148,28
tv 154,235,26624
mvva 100,148,60
tv 158,235,30720
mvva 100,144,52
mvva 100,132,44
mvva 100,128,36
mvva 104,128,0
dl 245,36,51,0
mvva 104,132,8
mvva 104,144,16
mvva 104,148,24
mvva 108,148,56
mvva 108,144,48
mvva 108,132,40
dl 246,36,51,1024
mvva 108,128,32
s_waitcnt vmcnt(27)
s_barrier
s_waitcnt lgkmcnt(0)
mvva 124,136,32
tv 132,224,4096
tv 134,224,6144
tv 144,224,8192
mvva 124,140,40
tv 128,224,0
tv 130,224,2048
mvva 124,152,48
mvva 124,156,56
tv 146,224,10240
mvva 120,156,24
dl 249,36,51,2048
tv 148,224,12288
tv 150,224,14336
mvva 120,152,16
mvva 120,140,8
mvva 120,136,0
mvva 116,136,36
mvva 116,140,44
mvva 116,152,52
dl 247,36,51,3072
s_lshl_b32 s51,s35,5
s_add_u32 m0,s52,49152
s_xor_b32 s51,s65,s51
mvva 116,156,60
mvva 112,156,28
mvva 112,152,20
s_waitcnt vmcnt(11)
v_cmp_ne_u32_e32 vcc,4,v253
mvva 112,140,12
dl 245,36,51,0
s_cbranch_vccnz L_spa3e
L_spra3e:
mvva 112,136,4
s_xor_b32 s50,s67,s33
tv 152,225,8192
s_waitcnt lgkmcnt(1)
mvvv 0,128,164
tv 140,225,4096
tv 142,225,6144
mvvv 0,132,172
tv 136,225,0
tv 138,225,2048
mvvv 0,144,180
dl 246,36,51,1024
mvvv 0,148,188
tv 154,225,10240
mvvv 4,148,220
tv 156,225,12288
tv 158,225,14336
mvvv 4,144,212
mvvv 4,132,204
la 196,252,50,0
mvvv 4,128,196
mvvv 8,128,160
s_xor_b32 s53,s60,s34
mvvv 8,132,168
mvvv 8,144,176
la 204,252,50,2048
mvvv 8,148,184
mvvv 12,148,216
mvvv 12,144,208
mvvv 12,132,200
buffer_load_dword v253,v242,s[8:11],s53 offen sc1
mvvv 12,128,192
tv 144,226,8192
s_waitcnt lgkmcnt(1)
mvvv 28,136,192
tv 132,226,4096
tv 134,226,6144
mvvv 28,140,200
tv 128,226,0
tv 130,226,2048
mvvv 28,152,208
la 212,250,50,0
mvvv 28,156,216
tv 146,226,10240
mvvv 24,156,184
tv 148,226,12288
tv 150,226,14336
mvvv 24,152,176
mvvv 24,140,168
la 220,250,50,2048
mvvv 24,136,160
mvvv 20,136,196
mvvv 20,140,204
mvvv 20,152,212
la 252,250,50,3072
mvvv 20,156,220
mvvv 16,156,188
mvvv 16,152,180
mvvv 16,140,172
la 244,250,50,1024
mvvv 16,136,164
tv 152,227,8192
s_waitcnt lgkmcnt(1)
mvvv 32,128,164
tv 140,227,4096
tv 142,227,6144
mvvv 32,132,172
tv 136,227,0
tv 138,227,2048
mvvv 32,144,180
la 236,252,50,3072
mvvv 32,148,188
tv 154,227,10240
mvvv 36,148,220
tv 156,227,12288
tv 158,227,14336
mvvv 36,144,212
mvvv 36,132,204
la 228,252,50,1024
s_add_u32 s50,s50,s55
mvvv 36,128,196
mvvv 40,128,160
mvvv 40,132,168
mvvv 40,144,176
la 192,252,50,0
mvvv 40,148,184
mvvv 44,148,216
mvvv 44,144,208
mvvv 44,132,200
la 200,252,50,2048
mvvv 44,128,192
tv 144,224,24576
s_waitcnt lgkmcnt(1)
mvvv 60,136,192
tv 132,224,20480
tv 134,224,22528
mvvv 60,140,200
tv 128,224,16384
tv 130,224,18432
mvvv 60,152,208
la 208,250,50,0
mvvv 60,156,216
tv 146,224,26624
mvvv 56,156,184
tv 148,224,28672
tv 150,224,30720
mvvv 56,152,176
mvvv 56,140,168
la 216,250,50,2048
mvvv 56,136,160
mvvv 52,136,196
mvvv 52,140,204
mvvv 52,152,212
la 248,250,50,3072
mvvv 52,156,220
mvvv 48,156,188
mvvv 48,152,180
mvvv 48,140,172
la 240,250,50,1024
mvvv 48,136,164
tv 152,225,24576
s_waitcnt lgkmcnt(1)
mvvv 64,128,164
tv 140,225,20480
tv 142,225,22528
mvvv 64,132,172
tv 136,225,16384
tv 138,225,18432
mvvv 64,144,180
la 232,252,50,3072
mvvv 64,148,188
tv 154,225,26624
mvvv 68,148,220
tv 156,225,28672
tv 158,225,30720
mvvv 68,144,212
mvvv 68,132,204
la 224,252,50,1024
mvvv 68,128,196
mvvv 72,128,160
mvvv 72,132,168
mvvv 72,144,176
dl 249,36,51,2048
mvvv 72,148,184
mvvv 76,148,216
mvvv 76,144,208
mvvv 76,132,200
dl 247,36,51,3072
s_lshl_b32 s51,s98,5
s_xor_b32 s51,s64,s51
s_add_u32 m0,s52,131072
s_xor_b32 s51,s51,4194304
mvvv 76,128,192
tv 144,226,24576
s_waitcnt lgkmcnt(1)
mvvv 92,136,192
tv 132,226,20480
tv 134,226,22528
mvvv 92,140,200
tv 128,226,16384
tv 130,226,18432
mvvv 92,152,208
dl 245,36,51,0
mvvv 92,156,216
tv 146,226,26624
mvvv 88,156,184
tv 148,226,28672
tv 150,226,30720
mvvv 88,152,176
mvvv 88,140,168
dl 246,36,51,1024
mvvv 88,136,160
s_waitcnt vmcnt(18)
v_cmp_ne_u32_e32 vcc,4,v253
mvvv 84,136,196
s_cbranch_vccnz L_spa4e
L_spra4e:
mvvv 84,140,204
s_xor_b32 s50,s67,s34
mvvv 84,152,212
dl 249,36,51,2048
mvvv 84,156,220
mvvv 80,156,188
mvvv 80,152,180
mvvv 80,140,172
la 68,252,50,0
mvvv 80,136,164
tv 152,227,24576
s_waitcnt lgkmcnt(1)
mvvv 96,128,164
tv 140,227,20480
tv 142,227,22528
mvvv 96,132,172
tv 136,227,16384
tv 138,227,18432
mvvv 96,144,180
la 76,252,50,2048
s_xor_b32 s53,s60,s35
mvvv 96,148,188
tv 154,227,26624
mvvv 100,148,220
tv 156,227,28672
tv 158,227,30720
mvvv 100,144,212
mvvv 100,132,204
la 84,250,50,0
mvvv 100,128,196
mvvv 104,128,160
mvvv 104,132,168
mvvv 104,144,176
buffer_load_dword v253,v242,s[8:11],s53 offen sc1
mvvv 104,148,184
mvvv 108,148,216
mvvv 108,144,208
mvvv 108,132,200
la 92,250,50,2048
mvvv 108,128,192
s_barrier
s_waitcnt lgkmcnt(0)
mvvv 124,136,192
tv 132,228,36864
tv 134,228,38912
tv 144,228,40960
mvvv 124,140,200
tv 128,228,32768
tv 130,228,34816
mvvv 124,152,208
la 124,250,50,3072
mvvv 124,156,216
tv 146,228,43008
mvvv 120,156,184
tv 148,228,45056
tv 150,228,47104
mvvv 120,152,176
mvvv 120,140,168
la 116,250,50,1024
mvvv 120,136,160
mvvv 116,136,196
mvvv 116,140,204
mvvv 116,152,212
la 108,252,50,3072
mvvv 116,156,220
mvvv 112,156,188
mvvv 112,152,180
mvvv 112,140,172
la 100,252,50,1024
s_add_u32 s50,s50,s55
mvvv 112,136,164
tv 152,229,40960
s_waitcnt lgkmcnt(1)
mvva 0,128,196
tv 140,229,36864
tv 142,229,38912
mvva 0,132,204
tv 136,229,32768
tv 138,229,34816
s_waitcnt vmcnt(23)
mvva 0,144,212
la 64,252,50,0
mvva 0,148,220
tv 154,229,43008
mvva 4,148,252
tv 156,229,45056
tv 158,229,47104
mvva 4,144,244
mvva 4,132,236
la 72,252,50,2048
s_waitcnt vmcnt(23)
mvva 4,128,228
L_vs5:
s_cbranch_execz L_cw0
mvva 8,128,192
s_waitcnt vmcnt(22)
mvva 8,132,200
s_waitcnt vmcnt(21)
mvva 8,144,208
la 80,250,50,0
s_waitcnt vmcnt(21)
mvva 8,148,216
s_waitcnt vmcnt(20)
mvva 12,148,248
s_waitcnt vmcnt(19)
mvva 12,144,240
s_waitcnt vmcnt(18)
mvva 12,132,232
la 88,250,50,2048
s_waitcnt vmcnt(18)
mvva 12,128,224
tv 144,230,40960
s_waitcnt lgkmcnt(1)
mvva 28,136,224
tv 132,230,36864
tv 134,230,38912
mvva 28,140,232
tv 128,230,32768
tv 130,230,34816
mvva 28,152,240
la 120,250,50,3072
mvva 28,156,248
tv 146,230,43008
mvva 24,156,216
tv 148,230,45056
tv 150,230,47104
mvva 24,152,208
mvva 24,140,200
la 112,250,50,1024
mvva 24,136,192
mvva 20,136,228
mvva 20,140,236
mvva 20,152,244
la 104,252,50,3072
mvva 20,156,252
mvva 16,156,220
mvva 16,152,212
mvva 16,140,204
la 96,252,50,1024
mvva 16,136,196
)"
R"(tv 152,231,40960
s_waitcnt lgkmcnt(1)
mvva 32,128,196
tv 140,231,36864
tv 142,231,38912
mvva 32,132,204
tv 136,231,32768
tv 138,231,34816
mvva 32,144,212
dl 247,36,51,3072
s_lshl_b32 s51,s98,5
s_xor_b32 s51,s65,s51
s_add_u32 m0,s52,147456
s_xor_b32 s51,s51,4194304
mvva 32,148,220
tv 154,231,43008
mvva 36,148,252
tv 156,231,45056
tv 158,231,47104
mvva 36,144,244
mvva 36,132,236
dl 245,36,51,0
mvva 36,128,228
mvva 40,128,192
mvva 40,132,200
mvva 40,144,208
dl 246,36,51,1024
mvva 40,148,216
mvva 44,148,248
mvva 44,144,240
mvva 44,132,232
dl 249,36,51,2048
mvva 44,128,224
tv 144,228,57344
s_waitcnt lgkmcnt(1)
mvva 60,136,224
tv 132,228,53248
tv 134,228,55296
mvva 60,140,232
tv 128,228,49152
tv 130,228,51200
s_waitcnt vmcnt(17)
v_cmp_ne_u32_e32 vcc,4,v253
mvva 60,152,240
dl 247,36,51,3072
s_lshl_b32 s51,s98,5
s_xor_b32 s51,s64,s51
s_add_u32 m0,s52,0
s_xor_b32 s51,s51,4456448
s_cbranch_vccnz L_spa5e
L_spra5e:
mvva 60,156,248
s_xor_b32 s50,s67,s35
tv 146,228,59392
mvva 56,156,216
tv 148,228,61440
tv 150,228,63488
mvva 56,152,208
mvva 56,140,200
dl 245,36,51,0
mvva 56,136,192
mvva 52,136,228
mvva 52,140,236
mvva 52,152,244
la 132,252,50,0
mvva 52,156,252
mvva 48,156,220
s_xor_b32 s53,s60,s98
mvva 48,152,212
s_xor_b32 s53,s53,512
mvva 48,140,204
la 140,252,50,2048
mvva 48,136,196
tv 152,229,57344
s_waitcnt lgkmcnt(1)
mvva 64,128,196
tv 140,229,53248
tv 142,229,55296
mvva 64,132,204
tv 136,229,49152
tv 138,229,51200
mvva 64,144,212
buffer_load_dword v253,v242,s[8:11],s53 offen sc1
mvva 64,148,220
tv 154,229,59392
mvva 68,148,252
tv 156,229,61440
tv 158,229,63488
mvva 68,144,244
mvva 68,132,236
la 148,250,50,0
mvva 68,128,228
mvva 72,128,192
mvva 72,132,200
mvva 72,144,208
la 156,250,50,2048
mvva 72,148,216
mvva 76,148,248
mvva 76,144,240
mvva 76,132,232
la 188,250,50,3072
mvva 76,128,224
tv 144,230,57344
s_waitcnt lgkmcnt(1)
mvva 92,136,224
tv 132,230,53248
tv 134,230,55296
mvva 92,140,232
tv 128,230,49152
tv 130,230,51200
mvva 92,152,240
la 180,250,50,1024
mvva 92,156,248
tv 146,230,59392
mvva 88,156,216
tv 148,230,61440
tv 150,230,63488
mvva 88,152,208
mvva 88,140,200
la 172,252,50,3072
mvva 88,136,192
mvva 84,136,228
mvva 84,140,236
mvva 84,152,244
la 164,252,50,1024
s_add_u32 s50,s50,s55
mvva 84,156,252
mvva 80,156,220
mvva 80,152,212
mvva 80,140,204
la 128,252,50,0
mvva 80,136,196
tv 152,231,57344
s_waitcnt lgkmcnt(1)
mvva 96,128,196
tv 140,231,53248
tv 142,231,55296
mvva 96,132,204
tv 136,231,49152
tv 138,231,51200
mvva 96,144,212
la 136,252,50,2048
mvva 96,148,220
tv 154,231,59392
mvva 100,148,252
tv 156,231,61440
tv 158,231,63488
mvva 100,144,244
mvva 100,132,236
la 144,250,50,0
mvva 100,128,228
mvva 104,128,192
mvva 104,132,200
mvva 104,144,208
la 152,250,50,2048
mvva 104,148,216
mvva 108,148,248
mvva 108,144,240
mvva 108,132,232
la 184,250,50,3072
mvva 108,128,224
s_barrier
s_waitcnt lgkmcnt(0)
mvva 124,136,224
tv 132,228,4096
tv 134,228,6144
tv 144,228,8192
mvva 124,140,232
tv 128,228,0
tv 130,228,2048
mvva 124,152,240
la 176,250,50,1024
mvva 124,156,248
tv 146,228,10240
mvva 120,156,216
tv 148,228,12288
tv 150,228,14336
mvva 120,152,208
mvva 120,140,200
la 168,252,50,3072
mvva 120,136,192
mvva 116,136,228
mvva 116,140,236
mvva 116,152,244
la 160,252,50,1024
mvva 116,156,252
mvva 112,156,220
mvva 112,152,212
mvva 112,140,204
dl 246,36,51,1024
mvva 112,136,196
tv 152,229,8192
s_waitcnt lgkmcnt(1)
mvva 0,128,68
tv 140,229,4096
tv 142,229,6144
mvva 0,132,76
tv 136,229,0
tv 138,229,2048
mvva 0,144,84
dl 249,36,51,2048
s_waitcnt vmcnt(25)
mvva 0,148,92
tv 154,229,10240
mvva 4,148,124
tv 156,229,12288
tv 158,229,14336
mvva 4,144,116
mvva 4,132,108
dl 247,36,51,3072
s_lshl_b32 s51,s98,5
s_xor_b32 s51,s65,s51
s_add_u32 m0,s52,16384
s_xor_b32 s51,s51,4456448
mvva 4,128,100
mvva 8,128,64
mvva 8,132,72
mvva 8,144,80
dl 245,36,51,0
mvva 8,148,88
s_waitcnt vmcnt(18)
v_cmp_ne_u32_e32 vcc,4,v253
mvva 12,148,120
s_cbranch_vccnz L_spa6e
L_spra6e:
mvva 12,144,112
s_xor_b32 s50,s67,s98
mvva 12,132,104
dl 246,36,51,1024
s_xor_b32 s50,s50,131072
mvva 12,128,96
tv 144,230,8192
s_waitcnt lgkmcnt(1)
mvva 28,136,96
tv 132,230,4096
tv 134,230,6144
mvva 28,140,104
tv 128,230,0
tv 130,230,2048
mvva 28,152,112
la 4,252,50,0
mvva 28,156,120
tv 146,230,10240
mvva 24,156,88
tv 148,230,12288
tv 150,230,14336
mvva 24,152,80
mvva 24,140,72
la 12,252,50,2048
s_xor_b32 s53,s60,s98
s_xor_b32 s53,s53,8704
mvva 24,136,64
mvva 20,136,100
mvva 20,140,108
mvva 20,152,116
la 20,250,50,0
mvva 20,156,124
mvva 16,156,92
mvva 16,152,84
mvva 16,140,76
buffer_load_dword v253,v242,s[8:11],s53 offen sc1
mvva 16,136,68
tv 152,231,8192
s_waitcnt lgkmcnt(1)
mvva 32,128,68
tv 140,231,4096
tv 142,231,6144
mvva 32,132,76
tv 136,231,0
tv 138,231,2048
mvva 32,144,84
la 28,250,50,2048
mvva 32,148,92
tv 154,231,10240
mvva 36,148,124
tv 156,231,12288
tv 158,231,14336
mvva 36,144,116
mvva 36,132,108
la 60,250,50,3072
mvva 36,128,100
mvva 40,128,64
mvva 40,132,72
mvva 40,144,80
la 52,250,50,1024
mvva 40,148,88
mvva 44,148,120
mvva 44,144,112
mvva 44,132,104
la 44,252,50,3072
mvva 44,128,96
tv 144,228,24576
s_waitcnt lgkmcnt(1)
mvva 60,136,96
tv 132,228,20480
tv 134,228,22528
mvva 60,140,104
tv 128,228,16384
tv 130,228,18432
mvva 60,152,112
la 36,252,50,1024
s_add_u32 s50,s50,s55
mvva 60,156,120
tv 146,228,26624
mvva 56,156,88
tv 148,228,28672
tv 150,228,30720
mvva 56,152,80
mvva 56,140,72
la 0,252,50,0
mvva 56,136,64
mvva 52,136,100
mvva 52,140,108
mvva 52,152,116
la 8,252,50,2048
mvva 52,156,124
mvva 48,156,92
mvva 48,152,84
mvva 48,140,76
la 16,250,50,0
mvva 48,136,68
tv 152,229,24576
s_waitcnt lgkmcnt(1)
mvva 64,128,68
tv 140,229,20480
tv 142,229,22528
mvva 64,132,76
tv 136,229,16384
tv 138,229,18432
mvva 64,144,84
la 24,250,50,2048
mvva 64,148,92
tv 154,229,26624
mvva 68,148,124
tv 156,229,28672
tv 158,229,30720
mvva 68,144,116
mvva 68,132,108
la 56,250,50,3072
mvva 68,128,100
mvva 72,128,64
mvva 72,132,72
mvva 72,144,80
la 48,250,50,1024
mvva 72,148,88
mvva 76,148,120
mvva 76,144,112
mvva 76,132,104
la 40,252,50,3072
mvva 76,128,96
tv 144,230,24576
s_waitcnt lgkmcnt(1)
mvva 92,136,96
tv 132,230,20480
tv 134,230,22528
mvva 92,140,104
tv 128,230,16384
tv 130,230,18432
mvva 92,152,112
la 32,252,50,1024
mvva 92,156,120
tv 146,230,26624
mvva 88,156,88
tv 148,230,28672
tv 150,230,30720
mvva 88,152,80
mvva 88,140,72
dl 249,36,51,2048
mvva 88,136,64
mvva 84,136,100
mvva 84,140,108
mvva 84,152,116
dl 247,36,51,3072
s_lshl_b32 s51,s98,5
s_xor_b32 s51,s64,s51
s_add_u32 m0,s52,98304
s_xor_b32 s51,s51,4718592
mvva 84,156,124
mvva 80,156,92
mvva 80,152,84
mvva 80,140,76
dl 245,36,51,0
mvva 80,136,68
tv 152,231,24576
s_waitcnt lgkmcnt(1)
mvva 96,128,68
tv 140,231,20480
tv 142,231,22528
mvva 96,132,76
tv 136,231,16384
tv 138,231,18432
mvva 96,144,84
dl 246,36,51,1024
mvva 96,148,92
tv 154,231,26624
mvva 100,148,124
tv 156,231,28672
tv 158,231,30720
mvva 100,144,116
s_waitcnt vmcnt(17)
v_cmp_ne_u32_e32 vcc,4,v253
mvva 100,132,108
dl 249,36,51,2048
s_cbranch_vccnz L_spa7e
L_spra7e:
mvva 100,128,100
s_xor_b32 s50,s67,s98
mvva 104,128,64
s_xor_b32 s50,s50,139264
mvva 104,132,72
mvva 104,144,80
dl 247,36,51,3072
mvva 104,148,88
mvva 108,148,120
mvva 108,144,112
mvva 108,132,104
lv 164,252,50,0
mvva 108,128,96
s_barrier
s_waitcnt lgkmcnt(0)
mvva 124,136,96
s_xor_b32 s53,s60,s98
tv 132,224,36864
tv 134,224,38912
tv 144,224,40960
s_xor_b32 s53,s53,16896
mvva 124,140,104
s_lshl_b32 s51,s98,5
tv 128,224,32768
tv 130,224,34816
mvva 124,152,112
lv 172,252,50,2048
mvva 124,156,120
s_xor_b32 s51,s65,s51
tv 146,224,43008
s_add_u32 m0,s52,114688
mvva 120,156,88
s_xor_b32 s51,s51,4718592
tv 148,224,45056
tv 150,224,47104
mvva 120,152,80
mvva 120,140,72
buffer_load_dword v253,v242,s[8:11],s53 offen sc1
mvva 120,136,64
mvva 116,136,100
mvva 116,140,108
mvva 116,152,116
lv 180,250,50,0
mvva 116,156,124
mvva 112,156,92
mvva 112,152,84
mvva 112,140,76
lv 188,250,50,2048
mvva 112,136,68
tv 152,225,40960
s_waitcnt lgkmcnt(1)
mvva 0,128,132
tv 140,225,36864
tv 142,225,38912
mvva 0,132,140
tv 136,225,32768
tv 138,225,34816
mvva 0,144,148
lv 220,250,50,3072
mvva 0,148,156
tv 154,225,43008
mvva 4,148,188
tv 156,225,45056
tv 158,225,47104
mvva 4,144,180
mvva 4,132,172
lv 212,250,50,1024
mvva 4,128,164
mvva 8,128,128
mvva 8,132,136
mvva 8,144,144
lv 204,252,50,3072
mvva 8,148,152
mvva 12,148,184
mvva 12,144,176
mvva 12,132,168
lv 196,252,50,1024
s_add_u32 s50,s50,s55
mvva 12,128,160
tv 144,226,40960
s_waitcnt lgkmcnt(1)
mvva 28,136,160
tv 132,226,36864
tv 134,226,38912
mvva 28,140,168
tv 128,226,32768
tv 130,226,34816
mvva 28,152,176
lv 160,252,50,0
mvva 28,156,184
tv 146,226,43008
mvva 24,156,152
L_vs6:
s_cbranch_execz L_cw1
tv 148,226,45056
tv 150,226,47104
mvva 24,152,144
mvva 24,140,136
lv 168,252,50,2048
mvva 24,136,128
mvva 20,136,164
mvva 20,140,172
mvva 20,152,180
lv 176,250,50,0
mvva 20,156,188
mvva 16,156,156
mvva 16,152,148
mvva 16,140,140
lv 184,250,50,2048
mvva 16,136,132
tv 152,227,40960
s_waitcnt lgkmcnt(1)
mvva 32,128,132
tv 140,227,36864
tv 142,227,38912
mvva 32,132,140
tv 136,227,32768
tv 138,227,34816
mvva 32,144,148
lv 216,250,50,3072
mvva 32,148,156
tv 154,227,43008
mvva 36,148,188
tv 156,227,45056
tv 158,227,47104
mvva 36,144,180
mvva 36,132,172
lv 208,250,50,1024
mvva 36,128,164
mvva 40,128,128
mvva 40,132,136
mvva 40,144,144
lv 200,252,50,3072
mvva 40,148,152
mvva 44,148,184
mvva 44,144,176
mvva 44,132,168
lv 192,252,50,1024
mvva 44,128,160
tv 144,224,57344
s_waitcnt lgkmcnt(1)
mvva 60,136,160
tv 132,224,53248
tv 134,224,55296
mvva 60,140,168
tv 128,224,49152
tv 130,224,51200
mvva 60,152,176
dl 245,36,51,0
mvva 60,156,184
tv 146,224,59392
mvva 56,156,152
tv 148,224,61440
tv 150,224,63488
mvva 56,152,144
mvva 56,140,136
dl 246,36,51,1024
mvva 56,136,128
mvva 52,136,164
mvva 52,140,172
mvva 52,152,180
dl 249,36,51,2048
mvva 52,156,188
mvva 48,156,156
mvva 48,152,148
mvva 48,140,140
dl 247,36,51,3072
s_lshl_b32 s51,s98,5
s_xor_b32 s51,s64,s51
s_add_u32 m0,s52,65536
s_xor_b32 s51,s51,4980736
mvva 48,136,132
tv 152,225,57344
s_waitcnt vmcnt(18)
v_cmp_ne_u32_e32 vcc,4,v253
s_waitcnt lgkmcnt(1)
mvva 64,128,132
s_cbranch_vccnz L_spa8e
L_spra8e:
tv 140,225,53248
tv 142,225,55296
mvva 64,132,140
s_xor_b32 s50,s67,s98
tv 136,225,49152
s_xor_b32 s50,s50,147456
tv 138,225,51200
mvva 64,144,148
dl 245,36,51,0
mvva 64,148,156
tv 154,225,59392
mvva 68,148,188
tv 156,225,61440
tv 158,225,63488
mvva 68,144,180
mvva 68,132,172
la 196,252,50,0
mvva 68,128,164
mvva 72,128,128
mvva 72,132,136
mvva 72,144,144
la 204,252,50,2048
s_xor_b32 s53,s60,s98
s_xor_b32 s53,s53,25088
mvva 72,148,152
mvva 76,148,184
mvva 76,144,176
mvva 76,132,168
la 212,250,50,0
mvva 76,128,160
tv 144,226,57344
s_waitcnt lgkmcnt(1)
mvva 92,136,160
tv 132,226,53248
tv 134,226,55296
mvva 92,140,168
tv 128,226,49152
tv 130,226,51200
mvva 92,152,176
buffer_load_dword v253,v242,s[8:11],s53 offen sc1
mvva 92,156,184
tv 146,226,59392
mvva 88,156,152
tv 148,226,61440
tv 150,226,63488
mvva 88,152,144
mvva 88,140,136
la 220,250,50,2048
mvva 88,136,128
mvva 84,136,164
mvva 84,140,172
mvva 84,152,180
la 252,250,50,3072
mvva 84,156,188
mvva 80,156,156
mvva 80,152,148
mvva 80,140,140
la 244,250,50,1024
mvva 80,136,132
tv 152,227,57344
s_waitcnt lgkmcnt(1)
mvva 96,128,132
tv 140,227,53248
tv 142,227,55296
mvva 96,132,140
tv 136,227,49152
tv 138,227,51200
mvva 96,144,148
la 236,252,50,3072
mvva 96,148,156
tv 154,227,59392
mvva 100,148,188
tv 156,227,61440
tv 158,227,63488
mvva 100,144,180
mvva 100,132,172
la 228,252,50,1024
s_add_u32 s50,s50,s55
mvva 100,128,164
mvva 104,128,128
mvva 104,132,136
mvva 104,144,144
la 192,252,50,0
mvva 104,148,152
mvva 108,148,184
mvva 108,144,176
mvva 108,132,168
la 200,252,50,2048
mvva 108,128,160
s_barrier
s_waitcnt lgkmcnt(0)
mvva 124,136,160
tv 132,232,4096
tv 134,232,6144
tv 144,232,8192
mvva 124,140,168
tv 128,232,0
tv 130,232,2048
mvva 124,152,176
la 208,250,50,0
mvva 124,156,184
tv 146,232,10240
mvva 120,156,152
tv 148,232,12288
tv 150,232,14336
mvva 120,152,144
mvva 120,140,136
la 216,250,50,2048
mvva 120,136,128
mvva 116,136,164
mvva 116,140,172
mvva 116,152,180
la 248,250,50,3072
mvva 116,156,188
mvva 112,156,156
mvva 112,152,148
mvva 112,140,140
la 240,250,50,1024
mvva 112,136,132
tv 152,233,8192
s_waitcnt lgkmcnt(1)
mvva 0,128,4
tv 140,233,4096
tv 142,233,6144
mvva 0,132,12
tv 136,233,0
tv 138,233,2048
mvva 0,144,20
la 232,252,50,3072
mvva 0,148,28
tv 154,233,10240
mvva 4,148,60
tv 156,233,12288
tv 158,233,14336
mvva 4,144,52
mvva 4,132,44
la 224,252,50,1024
mvva 4,128,36
mvva 8,128,0
mvva 8,132,8
mvva 8,144,16
dl 246,36,51,1024
mvva 8,148,24
mvva 12,148,56
mvva 12,144,48
mvva 12,132,40
dl 249,36,51,2048
mvva 12,128,32
tv 144,234,8192
s_waitcnt lgkmcnt(1)
mvva 28,136,32
tv 132,234,4096
tv 134,234,6144
mvva 28,140,40
tv 128,234,0
tv 130,234,2048
mvva 28,152,48
dl 247,36,51,3072
s_lshl_b32 s51,s98,5
s_xor_b32 s51,s64,s51
s_add_u32 m0,s52,81920
s_xor_b32 s51,s51,6029312
mvva 28,156,56
tv 146,234,10240
mvva 24,156,24
tv 148,234,12288
tv 150,234,14336
mvva 24,152,16
mvva 24,140,8
dl 245,36,51,0
mvva 24,136,0
mvva 20,136,36
mvva 20,140,44
s_waitcnt vmcnt(17)
v_cmp_ne_u32_e32 vcc,4,v253
mvva 20,152,52
dl 246,36,51,1024
s_cbranch_vccnz L_spa9e
L_spra9e:
mvva 20,156,60
s_xor_b32 s50,s67,s98
mvva 16,156,28
s_xor_b32 s50,s50,155648
mvva 16,152,20
mvva 16,140,12
dl 249,36,51,2048
mvva 16,136,4
tv 152,235,8192
s_waitcnt lgkmcnt(1)
mvva 32,128,4
tv 140,235,4096
tv 142,235,6144
mvva 32,132,12
tv 136,235,0
tv 138,235,2048
mvva 32,144,20
dl 247,36,51,3072
mvva 32,148,28
s_lshl_b32 s51,s98,5
tv 154,235,10240
s_xor_b32 s51,s65,s51
mvva 36,148,60
s_xor_b32 s53,s60,s98
tv 156,235,12288
tv 158,235,14336
s_xor_b32 s53,s53,25216
mvva 36,144,52
s_add_u32 m0,s52,32768
s_xor_b32 s51,s51,4980736
mvva 36,132,44
la 68,252,50,0
mvva 36,128,36
mvva 40,128,0
mvva 40,132,8
mvva 40,144,16
buffer_load_dword v253,v242,s[8:11],s53 offen sc1
mvva 40,148,24
mvva 44,148,56
mvva 44,144,48
mvva 44,132,40
la 76,252,50,2048
mvva 44,128,32
tv 144,232,24576
s_waitcnt lgkmcnt(1)
mvva 60,136,32
tv 132,232,20480
tv 134,232,22528
mvva 60,140,40
tv 128,232,16384
tv 130,232,18432
mvva 60,152,48
la 84,250,50,0
mvva 60,156,56
tv 146,232,26624
mvva 56,156,24
tv 148,232,28672
tv 150,232,30720
mvva 56,152,16
mvva 56,140,8
la 92,250,50,2048
mvva 56,136,0
mvva 52,136,36
mvva 52,140,44
mvva 52,152,52
la 124,250,50,3072
mvva 52,156,60
mvva 48,156,28
mvva 48,152,20
mvva 48,140,12
la 116,250,50,1024
mvva 48,136,4
tv 152,233,24576
s_waitcnt lgkmcnt(1)
mvva 64,128,4
tv 140,233,20480
tv 142,233,22528
mvva 64,132,12
tv 136,233,16384
tv 138,233,18432
mvva 64,144,20
la 108,252,50,3072
mvva 64,148,28
tv 154,233,26624
mvva 68,148,60
tv 156,233,28672
tv 158,233,30720
mvva 68,144,52
mvva 68,132,44
la 100,252,50,1024
s_add_u32 s50,s50,s55
mvva 68,128,36
mvva 72,128,0
mvva 72,132,8
mvva 72,144,16
la 64,252,50,0
mvva 72,148,24
mvva 76,148,56
mvva 76,144,48
mvva 76,132,40
la 72,252,50,2048
mvva 76,128,32
tv 144,224,8192
s_waitcnt lgkmcnt(1)
mvva 92,136,32
tv 132,224,4096
tv 134,224,6144
mvva 92,140,40
tv 128,224,0
tv 130,224,2048
mvva 92,152,48
la 80,250,50,0
mvva 92,156,56
tv 146,224,10240
mvva 88,156,24
tv 148,224,12288
tv 150,224,14336
mvva 88,152,16
mvva 88,140,8
la 88,250,50,2048
mvva 88,136,0
mvva 84,136,36
mvva 84,140,44
mvva 84,152,52
la 120,250,50,3072
mvva 84,156,60
mvva 80,156,28
mvva 80,152,20
mvva 80,140,12
la 112,250,50,1024
mvva 80,136,4
tv 152,225,8192
s_waitcnt lgkmcnt(1)
mvvv 0,128,164
tv 140,225,4096
tv 142,225,6144
mvvv 0,132,172
tv 136,225,0
tv 138,225,2048
mvvv 0,144,180
la 104,252,50,3072
mvvv 0,148,188
tv 154,225,10240
mvvv 4,148,220
tv 156,225,12288
tv 158,225,14336
mvvv 4,144,212
mvvv 4,132,204
la 96,252,50,1024
mvvv 4,128,196
mvvv 8,128,160
mvvv 8,132,168
mvvv 8,144,176
dl 245,36,51,0
mvvv 8,148,184
mvvv 12,148,216
mvvv 12,144,208
mvvv 12,132,200
dl 246,36,51,1024
mvvv 12,128,192
s_barrier
tv 144,226,8192
s_waitcnt lgkmcnt(1)
mvvv 28,136,192
tv 132,226,4096
tv 134,226,6144
mvvv 28,140,200
tv 128,226,0
tv 130,226,2048
mvvv 28,152,208
dl 249,36,51,2048
mvvv 28,156,216
tv 146,226,10240
s_waitcnt vmcnt(18)
v_cmp_ne_u32_e32 vcc,4,v253
mvvv 24,156,184
s_cbranch_vccnz L_spa10e
L_spra10e:
tv 148,226,12288
tv 150,226,14336
mvvv 24,152,176
s_xor_b32 s50,s67,s98
v_and_b32_e32 v242,48,v244
s_xor_b32 s50,s50,188416
mvvv 24,140,168
dl 247,36,51,3072
mvvv 24,136,160
s_lshl_b32 s51,s98,5
v_lshlrev_b32_e32 v242,3,v242
s_xor_b32 s51,s65,s51
mvvv 20,136,196
s_add_u32 m0,s52,49152
s_xor_b32 s51,s51,6029312
mvvv 20,140,204
mvvv 20,152,212
la 132,252,50,0
mvvv 20,156,220
mvvv 16,156,188
s_andn2_b32 s53,s61,384
mvvv 16,152,180
mvvv 16,140,172
la 140,252,50,2048
mvvv 16,136,164
tv 152,227,8192
s_waitcnt lgkmcnt(1)
mvvv 32,128,164
tv 140,227,4096
tv 142,227,6144
mvvv 32,132,172
tv 136,227,0
tv 138,227,2048
mvvv 32,144,180
buffer_load_dword v253,v242,s[8:11],s53 offen sc1
mvvv 32,148,188
tv 154,227,10240
mvvv 36,148,220
tv 156,227,12288
tv 158,227,14336
mvvv 36,144,212
mvvv 36,132,204
la 148,250,50,0
mvvv 36,128,196
mvvv 40,128,160
mvvv 40,132,168
mvvv 40,144,176
la 156,250,50,2048
mvvv 40,148,184
mvvv 44,148,216
mvvv 44,144,208
mvvv 44,132,200
la 188,250,50,3072
mvvv 44,128,192
tv 144,224,24576
s_waitcnt lgkmcnt(1)
mvvv 60,136,192
tv 132,224,20480
tv 134,224,22528
)"
R"(mvvv 60,140,200
tv 128,224,16384
tv 130,224,18432
mvvv 60,152,208
la 180,250,50,1024
mvvv 60,156,216
tv 146,224,26624
mvvv 56,156,184
tv 148,224,28672
tv 150,224,30720
mvvv 56,152,176
mvvv 56,140,168
la 172,252,50,3072
mvvv 56,136,160
mvvv 52,136,196
mvvv 52,140,204
mvvv 52,152,212
la 164,252,50,1024
s_add_u32 s50,s50,s55
mvvv 52,156,220
mvvv 48,156,188
mvvv 48,152,180
mvvv 48,140,172
la 128,252,50,0
mvvv 48,136,164
tv 152,225,24576
s_waitcnt lgkmcnt(1)
mvvv 64,128,164
tv 140,225,20480
tv 142,225,22528
mvvv 64,132,172
tv 136,225,16384
tv 138,225,18432
mvvv 64,144,180
la 136,252,50,2048
mvvv 64,148,188
L_vs7:
s_cbranch_execz L_cw2
tv 154,225,26624
mvvv 68,148,220
tv 156,225,28672
tv 158,225,30720
mvvv 68,144,212
mvvv 68,132,204
la 144,250,50,0
mvvv 68,128,196
mvvv 72,128,160
mvvv 72,132,168
mvvv 72,144,176
la 152,250,50,2048
mvvv 72,148,184
mvvv 76,148,216
mvvv 76,144,208
mvvv 76,132,200
la 184,250,50,3072
mvvv 76,128,192
tv 144,228,40960
s_waitcnt lgkmcnt(1)
mvvv 92,136,192
tv 132,228,36864
tv 134,228,38912
mvvv 92,140,200
tv 128,228,32768
tv 130,228,34816
mvvv 92,152,208
la 176,250,50,1024
mvvv 92,156,216
tv 146,228,43008
mvvv 88,156,184
tv 148,228,45056
tv 150,228,47104
mvvv 88,152,176
mvvv 88,140,168
la 168,252,50,3072
mvvv 88,136,160
mvvv 84,136,196
mvvv 84,140,204
mvvv 84,152,212
la 160,252,50,1024
mvvv 84,156,220
mvvv 80,156,188
mvvv 80,152,180
mvvv 80,140,172
dl 245,36,51,0
mvvv 80,136,164
tv 152,229,40960
s_waitcnt lgkmcnt(1)
mvva 0,128,196
tv 140,229,36864
tv 142,229,38912
mvva 0,132,204
tv 136,229,32768
tv 138,229,34816
mvva 0,144,212
dl 246,36,51,1024
mvva 0,148,220
tv 154,229,43008
mvva 4,148,252
tv 156,229,45056
tv 158,229,47104
mvva 4,144,244
mvva 4,132,236
dl 249,36,51,2048
mvva 4,128,228
mvva 8,128,192
mvva 8,132,200
mvva 8,144,208
dl 247,36,51,3072
mvva 8,148,216
s_waitcnt vmcnt(18)
v_cmp_ne_u32_e32 vcc,4,v253
mvva 12,148,248
s_cbranch_vccnz L_sppe
L_sprpe:
mvva 12,144,240
mvva 12,132,232
tv 148,230,45056
mvva 12,128,224
tv 144,230,40960
s_waitcnt lgkmcnt(2)
mvva 28,136,224
tv 132,230,36864
mvva 28,140,232
tv 128,230,32768
tv 130,230,34816
mvva 28,152,240
tv 134,230,38912
mvva 28,156,248
tv 146,230,43008
mvva 24,156,216
tv 150,230,47104
mvva 24,152,208
mvva 24,140,200
mvva 24,136,192
mvva 20,136,228
mvva 20,140,236
mvva 20,152,244
mvva 20,156,252
mvva 16,156,220
mvva 16,152,212
mvva 16,140,204
tv 156,231,45056
mvva 16,136,196
tv 152,231,40960
s_waitcnt lgkmcnt(2)
mvva 32,128,196
tv 140,231,36864
mvva 32,132,204
tv 136,231,32768
tv 138,231,34816
mvva 32,144,212
tv 142,231,38912
mvva 32,148,220
tv 154,231,43008
mvva 36,148,252
tv 158,231,47104
mvva 36,144,244
mvva 36,132,236
mvva 36,128,228
mvva 40,128,192
mvva 40,132,200
mvva 40,144,208
mvva 40,148,216
mvva 44,148,248
mvva 44,144,240
mvva 44,132,232
mvva 44,128,224
s_barrier
s_waitcnt lgkmcnt(0)
mvva 60,136,224
tv 132,228,53248
tv 144,228,57344
mvva 60,140,232
tv 128,228,49152
tv 130,228,51200
mvva 60,152,240
tv 134,228,55296
mvva 60,156,248
tv 146,228,59392
mvva 56,156,216
tv 148,228,61440
tv 150,228,63488
mvva 56,152,208
mvva 56,140,200
mvva 56,136,192
mvva 52,136,228
mvva 52,140,236
mvva 52,152,244
mvva 52,156,252
mvva 48,156,220
mvva 48,152,212
mvva 48,140,204
tv 156,229,61440
mvva 48,136,196
tv 152,229,57344
s_waitcnt lgkmcnt(2)
mvva 64,128,196
tv 140,229,53248
mvva 64,132,204
tv 136,229,49152
tv 138,229,51200
mvva 64,144,212
tv 142,229,55296
mvva 64,148,220
tv 154,229,59392
mvva 68,148,252
tv 158,229,63488
mvva 68,144,244
mvva 68,132,236
mvva 68,128,228
mvva 72,128,192
mvva 72,132,200
mvva 72,144,208
mvva 72,148,216
mvva 76,148,248
mvva 76,144,240
mvva 76,132,232
tv 148,228,12288
mvva 76,128,224
tv 144,228,8192
s_waitcnt lgkmcnt(2)
mvva 92,136,224
tv 132,228,4096
mvva 92,140,232
tv 128,228,0
tv 130,228,2048
mvva 92,152,240
tv 134,228,6144
mvva 92,156,248
tv 146,228,10240
mvva 88,156,216
tv 150,228,14336
mvva 88,152,208
mvva 88,140,200
mvva 88,136,192
mvva 84,136,228
mvva 84,140,236
mvva 84,152,244
mvva 84,156,252
mvva 80,156,220
mvva 80,152,212
mvva 80,140,204
tv 156,229,12288
mvva 80,136,196
tv 152,229,8192
s_waitcnt lgkmcnt(2)
mvva 0,128,68
tv 140,229,4096
mvva 0,132,76
tv 136,229,0
tv 138,229,2048
mvva 0,144,84
tv 142,229,6144
mvva 0,148,92
tv 154,229,10240
mvva 4,148,124
tv 158,229,14336
mvva 4,144,116
mvva 4,132,108
mvva 4,128,100
mvva 8,128,64
mvva 8,132,72
mvva 8,144,80
mvva 8,148,88
mvva 12,148,120
mvva 12,144,112
mvva 12,132,104
tv 148,228,28672
mvva 12,128,96
tv 144,228,24576
s_waitcnt lgkmcnt(2)
mvva 28,136,96
tv 132,228,20480
mvva 28,140,104
tv 128,228,16384
tv 130,228,18432
mvva 28,152,112
tv 134,228,22528
mvva 28,156,120
tv 146,228,26624
mvva 24,156,88
tv 150,228,30720
mvva 24,152,80
mvva 24,140,72
mvva 24,136,64
mvva 20,136,100
mvva 20,140,108
mvva 20,152,116
mvva 20,156,124
mvva 16,156,92
mvva 16,152,84
mvva 16,140,76
tv 156,229,28672
mvva 16,136,68
tv 152,229,24576
s_waitcnt lgkmcnt(2)
mvva 0,128,132
tv 140,229,20480
mvva 0,132,140
tv 136,229,16384
tv 138,229,18432
s_waitcnt vmcnt(4)
mvva 0,144,148
tv 142,229,22528
mvva 0,148,156
tv 154,229,26624
mvva 4,148,188
tv 158,229,30720
mvva 4,144,180
mvva 4,132,172
mvva 4,128,164
mvva 8,128,128
mvva 8,132,136
cv 0,0,1
cv 1,2,3
mvva 8,144,144
mvva 8,148,152
mvva 12,148,184
mvva 12,144,176
cv 4,4,5
cv 5,6,7
mvva 12,132,168
tv 148,230,12288
mvva 12,128,160
tv 144,230,8192
cv 8,8,9
s_waitcnt lgkmcnt(2)
mvva 28,136,160
tv 132,230,4096
cv 9,10,11
mvva 28,140,168
tv 128,230,0
tv 130,230,2048
mvva 28,152,176
tv 134,230,6144
cv 12,12,13
mvva 28,156,184
tv 146,230,10240
cv 13,14,15
mvva 24,156,152
tv 150,230,14336
mvva 24,152,144
mvva 24,140,136
mvva 24,136,128
mvva 20,136,164
cv 14,28,29
cv 15,30,31
mvva 20,140,172
buffer_store_dwordx4 v[12:15],v244,s[8:11],s56 offen offset:3072 sc0 sc1
mvva 20,152,180
mvva 20,156,188
mvva 16,156,156
cv 10,24,25
cv 11,26,27
mvva 16,152,148
buffer_store_dwordx4 v[8:11],v244,s[8:11],s56 offen offset:2048 sc0 sc1
mvva 16,140,140
tv 156,231,12288
mvva 16,136,132
cv 6,20,21
cv 7,22,23
s_waitcnt lgkmcnt(1)
mvva 32,128,68
tv 140,231,4096
mvva 32,132,76
buffer_store_dwordx4 v[4:7],v244,s[8:11],s56 offen offset:1024 sc0 sc1
tv 136,231,0
tv 138,231,2048
mvva 32,144,84
tv 142,231,6144
cv 2,16,17
mvva 32,148,92
tv 152,231,8192
tv 154,231,10240
mvva 36,148,124
tv 158,231,14336
cv 3,18,19
mvva 36,144,116
buffer_store_dwordx4 v[0:3],v244,s[8:11],s56 offen offset:0 sc0 sc1
mvva 36,132,108
mvva 36,128,100
mvva 40,128,64
mvva 40,132,72
mvva 40,144,80
mvva 40,148,88
mvva 44,148,120
mvva 44,144,112
mvva 44,132,104
tv 148,230,28672
mvva 44,128,96
s_waitcnt vmcnt(4)
s_barrier
tv 144,230,24576
s_waitcnt lgkmcnt(2)
mvva 60,136,96
tv 132,230,20480
mvva 60,140,104
tv 128,230,16384
tv 130,230,18432
mvva 60,152,112
tv 134,230,22528
mvva 60,156,120
tv 146,230,26624
mvva 56,156,88
tv 150,230,30720
mvva 56,152,80
mvva 56,140,72
mvva 56,136,64
mvva 52,136,100
mvva 52,140,108
mvva 52,152,116
mvva 52,156,124
mvva 48,156,92
mvva 48,152,84
mvva 48,140,76
tv 156,231,28672
mvva 48,136,68
tv 152,231,24576
s_waitcnt lgkmcnt(2)
mvva 32,128,132
tv 140,231,20480
mvva 32,132,140
tv 136,231,16384
tv 138,231,18432
mvva 32,144,148
tv 142,231,22528
mvva 32,148,156
tv 154,231,26624
mvva 36,148,188
tv 158,231,30720
mvva 36,144,180
mvva 36,132,172
mvva 36,128,164
mvva 40,128,128
mvva 40,132,136
cv 32,32,33
cv 33,34,35
mvva 40,144,144
mvva 40,148,152
mvva 44,148,184
mvva 44,144,176
cv 36,36,37
cv 37,38,39
mvva 44,132,168
tv 148,224,45056
mvva 44,128,160
tv 144,224,40960
cv 40,40,41
s_waitcnt lgkmcnt(2)
mvva 60,136,160
tv 132,224,36864
cv 41,42,43
mvva 60,140,168
tv 128,224,32768
tv 130,224,34816
mvva 60,152,176
tv 134,224,38912
cv 44,44,45
mvva 60,156,184
tv 146,224,43008
cv 45,46,47
mvva 56,156,152
tv 150,224,47104
mvva 56,152,144
mvva 56,140,136
mvva 56,136,128
mvva 52,136,164
cv 46,60,61
cv 47,62,63
mvva 52,140,172
buffer_store_dwordx4 v[44:47],v244,s[8:11],s57 offen offset:3072 sc0 sc1
mvva 52,152,180
mvva 52,156,188
mvva 48,156,156
cv 42,56,57
cv 43,58,59
mvva 48,152,148
buffer_store_dwordx4 v[40:43],v244,s[8:11],s57 offen offset:2048 sc0 sc1
mvva 48,140,140
tv 156,225,45056
mvva 48,136,132
cv 38,52,53
cv 39,54,55
s_waitcnt lgkmcnt(1)
mvva 64,128,68
tv 140,225,36864
mvva 64,132,76
buffer_store_dwordx4 v[36:39],v244,s[8:11],s57 offen offset:1024 sc0 sc1
tv 136,225,32768
tv 138,225,34816
mvva 64,144,84
tv 142,225,38912
cv 34,48,49
mvva 64,148,92
tv 152,225,40960
tv 154,225,43008
mvva 68,148,124
tv 158,225,47104
cv 35,50,51
mvva 68,144,116
buffer_store_dwordx4 v[32:35],v244,s[8:11],s57 offen offset:0 sc0 sc1
mvva 68,132,108
mvva 68,128,100
mvva 72,128,64
mvva 72,132,72
mvva 72,144,80
mvva 72,148,88
mvva 76,148,120
mvva 76,144,112
mvva 76,132,104
tv 148,224,61440
mvva 76,128,96
tv 144,224,57344
s_waitcnt lgkmcnt(2)
mvva 92,136,96
tv 132,224,53248
mvva 92,140,104
tv 128,224,49152
tv 130,224,51200
mvva 92,152,112
tv 134,224,55296
mvva 92,156,120
tv 146,224,59392
mvva 88,156,88
tv 150,224,63488
mvva 88,152,80
mvva 88,140,72
mvva 88,136,64
mvva 84,136,100
mvva 84,140,108
mvva 84,152,116
mvva 84,156,124
mvva 80,156,92
mvva 80,152,84
mvva 80,140,76
tv 156,225,61440
mvva 80,136,68
tv 152,225,57344
s_waitcnt lgkmcnt(2)
mvva 64,128,132
tv 140,225,53248
mvva 64,132,140
tv 136,225,49152
tv 138,225,51200
mvva 64,144,148
tv 142,225,55296
mvva 64,148,156
tv 154,225,59392
mvva 68,148,188
tv 158,225,63488
mvva 68,144,180
mvva 68,132,172
mvva 68,128,164
s_xor_b32 s82,s59,12
mvva 72,128,128
s_mul_i32 s82,s82,12288
cv 64,64,65
s_add_u32 s82,s82,0
mvva 72,132,136
lv 16,244,82,0
mvva 72,144,144
cv 65,66,67
cv 68,68,69
mvva 72,148,152
L_vs8:
s_cbranch_execz L_cw3
cv 69,70,71
mvva 76,148,184
mvva 76,144,176
lv 20,244,82,1024
mvva 76,132,168
tv 148,234,28672
tv 150,234,30720
mvva 76,128,160
cv 72,72,73
cv 73,74,75
s_waitcnt lgkmcnt(2)
mvva 92,136,160
tv 132,234,20480
mvva 92,140,168
lv 24,244,82,2048
tv 128,234,16384
tv 130,234,18432
mvva 92,152,176
tv 134,234,22528
cv 76,76,77
mvva 92,156,184
tv 144,234,24576
tv 146,234,26624
mvva 88,156,152
cv 77,78,79
mvva 88,152,144
lv 28,244,82,3072
mvva 88,140,136
mvva 88,136,128
cv 78,92,93
cv 79,94,95
mvva 84,136,164
mvva 84,140,172
buffer_store_dwordx4 v[76:79],v244,s[8:11],s58 offen offset:3072 sc0 sc1
mvva 84,152,180
mvva 84,156,188
mvva 80,156,156
cv 74,88,89
cv 75,90,91
mvva 80,152,148
buffer_store_dwordx4 v[72:75],v244,s[8:11],s58 offen offset:2048 sc0 sc1
mvva 80,140,140
tv 156,235,28672
mvva 80,136,132
cv 70,84,85
cv 71,86,87
s_waitcnt lgkmcnt(1)
mvva 96,128,4
tv 140,235,20480
mvva 96,132,12
buffer_store_dwordx4 v[68:71],v244,s[8:11],s58 offen offset:1024 sc0 sc1
tv 136,235,16384
tv 138,235,18432
mvva 96,144,20
tv 142,235,22528
cv 66,80,81
mvva 96,148,28
tv 152,235,24576
tv 154,235,26624
mvva 100,148,60
tv 158,235,30720
cv 67,82,83
mvva 100,144,52
buffer_store_dwordx4 v[64:67],v244,s[8:11],s58 offen offset:0 sc0 sc1
mvva 100,132,44
mvva 100,128,36
mvva 104,128,0
mvva 104,132,8
mvva 104,144,16
mvva 104,148,24
mvva 108,148,56
mvva 108,144,48
mvva 108,132,40
tv 148,226,28672
mvva 108,128,32
tv 144,226,24576
s_waitcnt lgkmcnt(2)
mvva 124,136,32
tv 132,226,20480
mvva 124,140,40
tv 128,226,16384
tv 130,226,18432
mvva 124,152,48
tv 134,226,22528
mvva 124,156,56
tv 146,226,26624
mvva 120,156,24
tv 150,226,30720
mvva 120,152,16
mvva 120,140,8
mvva 120,136,0
mvva 116,136,36
mvva 116,140,44
s_waitcnt vmcnt(7)
v_max_u32_e32 v248,v16,v17
mvva 116,152,52
v_max3_u32 v248,v248,v18,v19
s_waitcnt vmcnt(6)
v_max3_u32 v248,v248,v20,v21
v_max3_u32 v248,v248,v22,v23
mvva 116,156,60
s_waitcnt vmcnt(5)
v_max3_u32 v248,v248,v24,v25
v_max3_u32 v248,v248,v26,v27
s_waitcnt vmcnt(4)
v_max3_u32 v248,v248,v28,v29
mvva 112,156,28
v_max3_u32 v248,v248,v30,v31
v_cmp_eq_u32_e32 vcc,-1,v248
mvva 112,152,20
s_cbranch_vccnz L_spx0e
L_sprx0e:
mvva 112,140,12
tv 156,227,28672
tv 158,227,30720
v_lshlrev_b32_e32 v251,16,v18
v_and_b32_e32 v255,4294901760,v18
mvva 112,136,4
tv 152,227,24576
tv 154,227,26624
s_waitcnt lgkmcnt(4)
mvvv 96,128,164
v_lshlrev_b32_e32 v253,16,v19
v_and_b32_e32 v242,4294901760,v19
mvvv 96,132,172
tv 136,227,16384
tv 138,227,18432
mvvv 96,144,180
tv 140,227,20480
tv 142,227,22528
v_add_f32_e32 v112,v112,v251
mvvv 96,148,188
v_add_f32_e32 v113,v113,v255
v_add_f32_e32 v114,v114,v253
mvvv 100,148,220
v_add_f32_e32 v115,v115,v242
v_lshlrev_b32_e32 v251,16,v22
mvvv 100,144,212
v_and_b32_e32 v255,4294901760,v22
v_lshlrev_b32_e32 v253,16,v23
mvvv 100,132,204
v_and_b32_e32 v242,4294901760,v23
v_add_f32_e32 v116,v116,v251
v_add_f32_e32 v117,v117,v255
mvvv 100,128,196
v_add_f32_e32 v118,v118,v253
v_add_f32_e32 v119,v119,v242
mvvv 104,128,160
v_lshlrev_b32_e32 v251,16,v26
v_and_b32_e32 v255,4294901760,v26
mvvv 104,132,168
v_lshlrev_b32_e32 v253,16,v27
v_and_b32_e32 v242,4294901760,v27
mvvv 104,144,176
v_add_f32_e32 v120,v120,v251
v_add_f32_e32 v121,v121,v255
v_add_f32_e32 v122,v122,v253
mvvv 104,148,184
v_add_f32_e32 v123,v123,v242
v_lshlrev_b32_e32 v251,16,v30
mvvv 108,148,216
v_and_b32_e32 v255,4294901760,v30
v_lshlrev_b32_e32 v253,16,v31
mvvv 108,144,208
v_and_b32_e32 v242,4294901760,v31
v_add_f32_e32 v124,v124,v251
mvvv 108,132,200
v_add_f32_e32 v125,v125,v255
v_add_f32_e32 v126,v126,v253
v_add_f32_e32 v127,v127,v242
mvvv 108,128,192
tv 144,230,57344
v_lshlrev_b32_e32 v251,16,v16
s_waitcnt lgkmcnt(1)
mvvv 124,136,192
tv 132,230,53248
v_and_b32_e32 v255,4294901760,v16
mvvv 124,140,200
tv 128,230,49152
tv 130,230,51200
mvvv 124,152,208
tv 134,230,55296
v_lshlrev_b32_e32 v253,16,v17
mvvv 124,156,216
tv 146,230,59392
v_and_b32_e32 v242,4294901760,v17
mvvv 120,156,184
tv 148,230,61440
tv 150,230,63488
mvvv 120,152,176
v_add_f32_e32 v96,v96,v251
v_add_f32_e32 v97,v97,v255
mvvv 120,140,168
v_add_f32_e32 v98,v98,v253
v_add_f32_e32 v99,v99,v242
mvvv 120,136,160
v_lshlrev_b32_e32 v251,16,v20
v_and_b32_e32 v255,4294901760,v20
mvvv 116,136,196
v_lshlrev_b32_e32 v253,16,v21
v_and_b32_e32 v242,4294901760,v21
mvvv 116,140,204
v_add_f32_e32 v100,v100,v251
v_add_f32_e32 v101,v101,v255
mvvv 116,152,212
v_add_f32_e32 v102,v102,v253
v_add_f32_e32 v103,v103,v242
mvvv 116,156,220
v_lshlrev_b32_e32 v251,16,v24
v_and_b32_e32 v255,4294901760,v24
mvvv 112,156,188
v_lshlrev_b32_e32 v253,16,v25
v_and_b32_e32 v242,4294901760,v25
mvvv 112,152,180
v_add_f32_e32 v104,v104,v251
v_add_f32_e32 v105,v105,v255
mvvv 112,140,172
tv 156,231,61440
v_add_f32_e32 v106,v106,v253
mvvv 112,136,164
tv 152,231,57344
v_add_f32_e32 v107,v107,v242
s_waitcnt lgkmcnt(2)
mvva 96,128,196
tv 140,231,53248
v_lshlrev_b32_e32 v251,16,v28
mvva 96,132,204
tv 136,231,49152
tv 138,231,51200
mvva 96,144,212
tv 142,231,55296
v_and_b32_e32 v255,4294901760,v28
mvva 96,148,220
tv 154,231,59392
v_lshlrev_b32_e32 v253,16,v29
mvva 100,148,252
tv 158,231,63488
v_and_b32_e32 v242,4294901760,v29
mvva 100,144,244
v_add_f32_e32 v108,v108,v251
v_add_f32_e32 v109,v109,v255
mvva 100,132,236
v_add_f32_e32 v110,v110,v253
v_add_f32_e32 v111,v111,v242
mvva 100,128,228
mvva 104,128,192
mvva 104,132,200
mvva 104,144,208
mvva 104,148,216
mvva 108,148,248
mvva 108,144,240
mvva 108,132,232
tv 148,226,45056
mvva 108,128,224
tv 144,226,40960
s_waitcnt lgkmcnt(2)
mvva 124,136,224
s_xor_b32 s83,s59,8
tv 132,226,36864
s_mul_i32 s83,s83,12288
mvva 124,140,232
s_add_u32 s83,s83,4096
tv 128,226,32768
tv 130,226,34816
mvva 124,152,240
lv 48,244,83,0
tv 134,226,38912
mvva 124,156,248
tv 146,226,43008
mvva 120,156,216
lv 52,244,83,1024
tv 150,226,47104
mvva 120,152,208
mvva 120,140,200
lv 56,244,83,2048
mvva 120,136,192
mvva 116,136,228
lv 60,244,83,3072
mvva 116,140,236
mvva 116,152,244
s_xor_b32 s84,s59,4
mvva 116,156,252
s_mul_i32 s84,s84,12288
mvva 112,156,220
s_add_u32 s84,s84,8192
mvva 112,152,212
lv 80,244,84,0
mvva 112,140,204
tv 156,227,45056
mvva 112,136,196
lv 84,244,84,1024
tv 152,227,40960
s_waitcnt lgkmcnt(2)
mvva 96,128,68
tv 140,227,36864
mvva 96,132,76
lv 88,244,84,2048
tv 136,227,32768
tv 138,227,34816
mvva 96,144,84
tv 142,227,38912
mvva 96,148,92
lv 92,244,84,3072
tv 154,227,43008
mvva 100,148,124
tv 158,227,47104
mvva 100,144,116
mvva 100,132,108
mvva 100,128,100
mvva 104,128,64
mvva 104,132,72
mvva 104,144,80
mvva 104,148,88
mvva 108,148,120
mvva 108,144,112
mvva 108,132,104
tv 148,226,61440
mvva 108,128,96
tv 144,226,57344
s_waitcnt lgkmcnt(2)
mvva 124,136,96
tv 132,226,53248
mvva 124,140,104
tv 128,226,49152
tv 130,226,51200
mvva 124,152,112
tv 134,226,55296
mvva 124,156,120
tv 146,226,59392
mvva 120,156,88
tv 150,226,63488
mvva 120,152,80
mvva 120,140,72
mvva 120,136,64
mvva 116,136,100
mvva 116,140,108
mvva 116,152,116
mvva 116,156,124
mvva 112,156,92
mvva 112,152,84
mvva 112,140,76
tv 156,227,61440
mvva 112,136,68
tv 152,227,57344
s_waitcnt lgkmcnt(2)
mvva 96,128,132
tv 140,227,53248
mvva 96,132,140
tv 136,227,49152
tv 138,227,51200
mvva 96,144,148
tv 142,227,55296
mvva 96,148,156
tv 154,227,59392
mvva 100,148,188
tv 158,227,63488
mvva 100,144,180
mvva 100,132,172
mvva 100,128,164
mvva 104,128,128
mvva 104,132,136
mvva 104,144,144
mvva 104,148,152
s_waitcnt vmcnt(7)
v_max_u32_e32 v248,v48,v49
mvva 108,148,184
v_max3_u32 v248,v248,v50,v51
s_waitcnt vmcnt(6)
v_max3_u32 v248,v248,v52,v53
mvva 108,144,176
v_max3_u32 v248,v248,v54,v55
s_waitcnt vmcnt(5)
v_max3_u32 v248,v248,v56,v57
mvva 108,132,168
v_max3_u32 v248,v248,v58,v59
s_waitcnt vmcnt(4)
v_max3_u32 v248,v248,v60,v61
mvva 108,128,160
v_max3_u32 v248,v248,v62,v63
v_cmp_eq_u32_e32 vcc,-1,v248
s_waitcnt lgkmcnt(0)
mvva 124,136,160
s_cbranch_vccnz L_spx1e
L_sprx1e:
mvva 124,140,168
mvva 124,152,176
mvva 124,156,184
s_waitcnt vmcnt(3)
v_max_u32_e32 v248,v80,v81
mvva 120,156,152
v_max3_u32 v248,v248,v82,v83
s_waitcnt vmcnt(2)
v_max3_u32 v248,v248,v84,v85
mvva 120,152,144
v_max3_u32 v248,v248,v86,v87
s_waitcnt vmcnt(1)
v_max3_u32 v248,v248,v88,v89
mvva 120,140,136
v_max3_u32 v248,v248,v90,v91
s_waitcnt vmcnt(0)
v_max3_u32 v248,v248,v92,v93
mvva 120,136,128
v_max3_u32 v248,v248,v94,v95
v_cmp_eq_u32_e32 vcc,-1,v248
mvva 116,136,164
s_cbranch_vccnz L_spx2e
L_sprx2e:
mvva 116,140,172
mvva 116,152,180
mvva 116,156,188
mvva 112,156,156
mvva 112,152,148
mvva 112,140,140
mvva 112,136,132
mvvv 108,238,60
mvvv 108,238,92
mvvv 124,236,60
mvvv 124,236,92
mvvv 104,238,56
mvvv 104,238,88
mvvv 120,236,56
mvvv 120,236,88
mvvv 100,238,52
mvvv 100,238,84
cv 108,108,109
cv 109,110,111
cv 110,124,125
cv 111,126,127
cv 104,104,105
v_permlane16_swap_b32_e32 v108,v110
mvvv 116,236,52
v_permlane16_swap_b32_e32 v109,v111
cv 105,106,107
cv 106,120,121
)"
R"(cv 107,122,123
mvvv 116,236,84
buffer_store_dwordx4 v[108:111],v243,s[44:47],s73 offen
v_permlane16_swap_b32_e32 v104,v106
v_permlane16_swap_b32_e32 v105,v107
mvvv 96,238,48
cv 100,100,101
cv 101,102,103
mvvv 96,238,80
buffer_store_dwordx4 v[104:107],v243,s[44:47],s72 offen
cv 102,116,117
cv 103,118,119
mvvv 112,236,48
v_permlane16_swap_b32_e32 v100,v102
v_permlane16_swap_b32_e32 v101,v103
mvvv 112,236,80
buffer_store_dwordx4 v[100:103],v243,s[44:47],s71 offen
cv 96,96,97
cv 97,98,99
s_nop 4
cv 98,112,113
cv 99,114,115
s_nop 0
v_permlane16_swap_b32_e32 v96,v98
v_permlane16_swap_b32_e32 v97,v99
buffer_store_dwordx4 v[96:99],v243,s[44:47],s70 offen
s_endpgm
L_odd:
L_vs9:
s_cbranch_execz L_vs13
s_lshl_b32 s86,s21,2
s_sub_u32 s87,s13,s67
s_add_u32 s86,s86,s12
mvaa 64,200,132
s_lshl_b32 s86,s86,8
v_add_u32_e32 v252,s87,v244
s_add_u32 s86,s86,L_odd-L_att_pc+1024
v_lshl_add_u32 v226,v244,3,s86
s_lshl_b32 s86,s12,1
s_bfe_u32 s87,s21,65539
mvaa 64,208,136
s_add_u32 s86,s86,s87
v_min_u32_e32 v226,L_att_end-L_att_pc-4,v226
s_lshl_b32 s86,s86,7
s_add_u32 s86,s86,L_pend2-L_att_pc
v_writelane_b32 v226,s86,2
mvaa 64,216,140
s_mov_b64 exec,7
global_load_dword v253,v226,s[96:97]
s_mov_b64 exec,-1
cvaa 68,196,128,232
s_add_u32 m0,s52,32768
v_and_b32_e32 v224,16,v244
v_lshlrev_b32_e32 v224,5,v224
mvaa 68,204,132
s_xor_b32 s51,s62,0
v_lshrrev_b32_e32 v239,4,v244
v_lshrrev_b32_e32 v236,3,v239
mvaa 68,212,136
s_lshl_b32 s87,s12,13
v_and_b32_e32 v237,112,v244
v_and_b32_e32 v238,48,v239
mvaa 68,220,140
s_xor_b32 s87,s87,8192
ra 64,225,0
v_lshlrev_b32_e32 v238,1,v238
cvaa 96,224,128,232
buffer_load_dword v253,v224,s[8:11],s53 offen sc1
mvaa 96,232,132
s_mov_b32 s84,7
ra 68,225,1024
v_xor_b32_e32 v237,v237,v238
mvaa 96,240,136
s_add_u32 s81,s81,1
ra 72,225,2048
v_lshl_add_u32 v245,v236,11,v237
mvaa 96,248,140
s_mov_b32 s84,5
v_add_u32_e32 v246,15360,v245
v_add_u32_e32 v249,30720,v245
cvaa 100,228,128,232
s_andn2_b32 s69,64,s54
ra 76,225,3072
s_waitcnt vmcnt(9)
v_add_u32_e32 v247,46080,v245
mvaa 100,236,132
dl 245,24,51,0
mvaa 100,244,136
s_add_u32 s88,s14,4096
ra 80,225,4096
cv 8,8,9
mvaa 100,252,140
s_lshl_b32 s35,s19,1
ra 84,225,5120
cv 9,10,11
cvaa 72,192,144,232
s_lshr_b32 s33,40569,s35
ra 88,225,6144
cv 10,12,13
mvaa 72,200,148
s_and_b32 s33,s33,3
ra 92,225,7168
v_add_u32_e32 v250,4096,v252
mvaa 72,208,152
dl 246,24,51,1024
mvaa 72,216,156
s_lshl_b32 s33,s33,13
ra 96,225,8192
v_mov_b32_e32 v251,4
cvaa 76,196,144,232
s_lshr_b32 s34,59294,s35
ra 100,225,9216
s_and_b32 s34,s34,3
mvaa 76,204,148
s_lshl_b32 s34,s34,13
ra 104,225,10240
s_xor_b32 s35,s33,s34
mvaa 76,212,152
s_and_b32 s98,s19,3
ra 108,225,11264
s_lshl_b32 s98,s98,13
mvaa 76,220,156
dl 249,24,51,2048
cvaa 104,224,144,232
s_mov_b32 s74,0x300003
ra 112,225,12288
s_mov_b32 s75,0x30000300
mvaa 104,232,148
s_lshl_b32 s71,s12,15
ra 116,225,13312
s_add_u32 s71,s71,s54
mvaa 104,240,152
s_mov_b64 s[44:45],s[30:31]
ra 120,225,14336
s_mov_b32 s46,4194304
mvaa 104,248,156
s_mov_b32 s47,131072
ex 14
cvaa 108,228,144,232
dl 247,24,51,3072
mvaa 108,236,148
s_add_u32 m0,s52,49152
s_xor_b32 s51,s63,0
ra 124,225,15360
mvaa 108,244,152
s_lshl_b32 s70,s23,11
ex 15
mvaa 108,252,156
s_add_u32 s70,s70,s71
ex 40
cvaa 80,192,160,232
L_vs10:
s_cbranch_execz L_vs14
ex 41
mvaa 80,200,164
dl 245,24,51,0
mvaa 80,208,168
ex 42
mvaa 80,216,172
ex 43
cvaa 84,196,160,232
ex 44
mvaa 84,204,164
ex 45
mvaa 84,212,168
dl 246,24,51,1024
mvaa 84,220,172
ex 46
cvaa 112,224,160,232
ex 47
mvaa 112,232,164
ex 16
mvaa 112,240,168
ex 17
mvaa 112,248,172
dl 249,24,51,2048
cvaa 116,228,160,232
ex 18
mvaa 116,236,164
ex 19
mvaa 116,244,168
ex 20
mvaa 116,252,172
ex 21
cvaa 88,192,176,232
dl 247,24,51,3072
s_waitcnt vmcnt(8)
v_cmp_ne_u32_e32 vcc,4,v253
mvaa 88,200,180
s_cbranch_vccnz L_spbo
L_sprbo:
ex 22
mvaa 88,208,184
s_xor_b32 s50,s66,262144
ex 23
mvaa 88,216,188
ex 48
cvaa 92,196,176,232
ex 49
mvaa 92,204,180
la 128,252,50,0
mvaa 92,212,184
s_waitcnt lgkmcnt(0)
s_barrier
mvaa 92,220,188
s_mov_b32 m0,s72
ex 50
cvaa 120,224,176,232
s_lshl_b32 s72,s22,11
ex 51
mvaa 120,232,180
s_add_u32 s72,s72,s71
ex 52
mvaa 120,240,184
ds 255,8,76,0
mvaa 120,248,188
s_add_u32 s71,s70,131072
ex 53
cvaa 124,228,176,232
ex 54
mvaa 124,236,180
ex 55
mvaa 124,244,184
ex 24
mvaa 124,252,188
ds 255,8,76,1024
maav 0,64,0
s_cmp_lg_u32 s19,7
s_cbranch_scc1 L_j7o
s_nop 1
s_lshl_b32 s86,s12,12
s_add_u32 s86,s86,131072
v_add_u32_e32 v236,s86,v244
ra 160,236,0
ra 164,236,1024
ra 168,236,2048
ra 172,236,3072
ra 176,236,16384
ra 180,236,17408
ra 184,236,18432
ra 188,236,19456
v_add_u32_e32 v224,s94,v224
s_waitcnt lgkmcnt(0)
L_j7o:
ex 25
maav 0,96,4
ex 26
maav 4,68,0
ex 27
maav 4,100,4
cv 11,14,15
cv 12,40,41
maav 8,72,0
ds 255,8,76,2048
maav 8,104,4
cv 13,42,43
cv 14,44,45
maav 12,76,0
cv 15,46,47
cv 16,16,17
maav 12,108,4
ex 28
v_smfmac_f32_16x16x64_bf16 v[144:147],v[228:231],v[0:7],v248
cv 17,18,19
maav 16,80,0
ds 255,8,76,3072
maav 16,112,4
s_mov_b32 m0,s73
ex 29
maav 20,84,0
s_add_u32 s73,s72,131072
ex 30
maav 20,116,4
ex 31
maav 24,88,0
ex 56
maav 24,120,4
ds 255,8,77,0
maav 28,92,0
ex 57
maav 28,124,4
ex 58
maav 32,64,8
ex 59
maav 32,96,12
ex 60
maav 36,68,8
ds 255,8,77,1024
maav 36,100,12
cv 18,20,21
cv 19,22,23
maav 40,72,8
cv 20,48,49
cv 21,50,51
maav 40,104,12
cv 22,52,53
cv 23,54,55
maav 44,76,8
ex 61
maav 44,108,12
ds 255,8,77,2048
v_smfmac_f32_16x16x64_bf16 v[148:151],v[228:231],v[8:15],v248
cv 24,24,25
maav 48,80,8
ex 62
maav 48,112,12
ex 63
maav 52,84,8
ra 192,244,0
cv 25,26,27
maav 52,116,12
ds 255,8,77,3072
maav 56,88,8
cv 26,28,29
cv 27,30,31
maav 56,120,12
cv 28,56,57
cv 29,58,59
maav 60,92,8
cv 30,60,61
cv 31,62,63
maav 60,124,12
ex 64
mvav 160,64,16
la 132,252,50,2048
mvav 160,96,20
ex 65
mvav 164,68,16
ex 66
mvav 164,100,20
ex 67
mvav 168,72,16
ex 68
mvav 168,104,20
la 136,250,50,0
mvav 172,76,16
ex 69
mvav 172,108,20
ex 70
v_smfmac_f32_16x16x64_bf16 v[152:155],v[228:231],v[16:23],v248
ra 200,244,2048
mvav 176,80,16
ra 208,244,4096
cv 64,64,65
mvav 176,112,20
la 140,250,50,2048
mvav 180,84,16
ra 216,244,6144
cv 65,66,67
mvav 180,116,20
ex 71
mvav 184,88,16
ex 96
mvav 184,120,20
s_barrier
ex 97
mvav 188,92,16
la 156,250,50,3072
mvav 188,124,20
s_add_u32 m0,s52,131072
ra 196,244,1024
s_xor_b32 s51,s62,1048576
mvav 192,64,24
ra 204,244,3072
cv 66,68,69
mvav 192,96,28
ra 212,244,5120
cv 67,70,71
mvav 196,68,24
ra 220,244,7168
cv 68,96,97
mvav 196,100,28
la 152,250,50,1024
mvav 200,72,24
ex 98
mvav 200,104,28
ex 99
mvav 204,76,24
ra 224,244,8192
cv 69,98,99
mvav 204,108,28
ra 232,244,10240
v_add_u32_e32 v6,s87,v244
v_smfmac_f32_16x16x64_bf16 v[156:159],v[228:231],v[24:31],v248
ra 240,244,12288
mvav 208,80,24
la 148,252,50,3072
mvav 208,112,28
s_xor_b32 s87,s52,4096
ra 248,244,14336
v_add_u32_e32 v7,98304,v6
mvav 212,84,24
s_add_u32 s87,s87,150528
ra 228,244,9216
v_add_u32_e32 v5,65536,v244
mvav 212,116,28
ra 236,244,11264
v_add_u32_e32 v32,s87,v244
mvav 216,88,24
ra 244,244,13312
v_and_b32_e32 v224,960,v244
mvav 216,120,28
la 144,252,50,1024
mvav 220,92,24
s_add_u32 s50,s50,s55
ra 252,244,15360
mvav 220,124,28
ra 64,225,32768
s_waitcnt lgkmcnt(8)
cvaa 24,192,176,232
dl 245,24,51,0
mvaa 24,200,180
ra 96,225,40960
mvaa 24,208,184
ra 68,225,33792
mvaa 24,216,188
dl 246,24,51,1024
cvaa 16,192,160,128
ra 100,225,41984
mvaa 16,200,164
ra 72,225,34816
mvaa 16,208,168
dl 249,24,51,2048
mvaa 16,216,172
ra 104,225,43008
cvaa 28,196,176,232
ra 76,225,35840
mvaa 28,204,180
dl 247,24,51,3072
mvaa 28,212,184
s_add_u32 m0,s52,147456
s_xor_b32 s51,s63,1048576
ra 108,225,44032
mvaa 28,220,188
ra 80,225,36864
cvaa 20,196,160,132
dl 245,24,51,0
mvaa 20,204,164
ra 112,225,45056
mvaa 20,212,168
ra 192,244,16384
mvaa 20,220,172
dl 246,24,51,1024
cvaa 56,224,176,232
ra 200,244,18432
s_waitcnt lgkmcnt(7)
mvaa 56,232,180
ra 208,244,20480
mvaa 56,240,184
dl 249,24,51,2048
mvaa 56,248,188
ra 216,244,22528
cvaa 48,224,160,136
ra 196,244,17408
mvaa 48,232,164
ra 204,244,19456
mvaa 48,240,168
ra 212,244,21504
mvaa 48,248,172
ra 220,244,23552
cvaa 60,228,176,232
ra 224,244,24576
mvaa 60,236,180
ra 232,244,26624
mvaa 60,244,184
ra 240,244,28672
mvaa 60,252,188
ra 248,244,30720
cvaa 52,228,160,140
ra 84,225,37888
mvaa 52,236,164
ra 116,225,46080
mvaa 52,244,168
la 160,252,50,0
ra 228,244,25600
mvaa 52,252,172
ra 236,244,27648
s_waitcnt lgkmcnt(10)
cvaa 8,192,176,128
ra 244,244,29696
mvaa 8,200,180
la 164,252,50,2048
ra 252,244,31744
mvaa 8,208,184
ra 88,225,38912
mvaa 8,216,188
ra 120,225,47104
cvaa 12,196,176,132
la 168,250,50,0
mvaa 12,204,180
ra 92,225,39936
ra 124,225,48128
s_waitcnt lgkmcnt(9)
mvaa 12,212,184
ex 100
mvaa 12,220,188
la 172,250,50,2048
cvaa 40,224,176,136
ex 101
mvaa 40,232,180
ex 102
mvaa 40,240,184
ex 103
mvaa 40,248,188
cv 70,100,101
cv 71,102,103
s_waitcnt lgkmcnt(4)
cvaa 44,228,176,140
ex 72
mvaa 44,236,180
s_waitcnt vmcnt(18) lgkmcnt(0)
s_barrier
ex 73
mvaa 44,244,184
la 176,252,50,1024
mvaa 44,252,188
ex 74
maav 0,64,64
ex 75
maav 0,96,68
la 188,250,50,3072
maav 4,68,64
ra 192,5,0
cv 72,72,73
maav 4,100,68
ra 196,5,1024
cv 73,74,75
maav 8,72,64
la 184,250,50,1024
ds_write_b128 v6,a[0:3] offset:0
maav 8,104,68
ex 76
maav 12,76,64
ex 77
maav 12,108,68
la 180,252,50,3072
ds_write_b128 v6,a[4:7] offset:1024
v_smfmac_f32_16x16x64_bf16 v[144:147],v[228:231],v[64:71],v248
cv 74,76,77
maav 16,80,64
ex 78
maav 16,112,68
ds_write_b128 v6,a[8:11] offset:2048
ex 79
maav 20,84,64
ex 104
maav 20,116,68
ds_write_b128 v6,a[12:15] offset:3072
ex 105
maav 24,88,64
ex 106
ex 107
ex 108
maav 24,120,68
ds_write_b128 v6,a[16:19] offset:4096
cv 75,78,79
maav 28,92,64
ex 109
ex 110
ex 111
maav 28,124,68
cv 76,104,105
cv 77,106,107
cv 78,108,109
cv 79,110,111
maav 32,64,72
ds_write_b128 v6,a[20:23] offset:5120
maav 32,96,76
ex 80
maav 36,68,72
ex 81
maav 36,100,76
ds_write_b128 v6,a[24:27] offset:6144
maav 40,72,72
ex 82
maav 40,104,76
ex 83
maav 44,76,72
ds_write_b128 v6,a[28:31] offset:7168
maav 44,108,76
cv 80,80,81
cv 81,82,83
v_smfmac_f32_16x16x64_bf16 v[148:151],v[228:231],v[72:79],v248
ex 84
maav 48,80,72
ds_write_b128 v7,a[32:35] offset:0
maav 48,112,76
ex 85
ex 86
ex 87
maav 52,84,72
ex 112
ex 113
maav 52,116,76
ds_write_b128 v7,a[36:39] offset:1024
cv 82,84,85
maav 56,88,72
ex 114
ex 115
ex 116
maav 56,120,76
ex 117
ex 118
ex 119
maav 60,92,72
ds_write_b128 v7,a[40:43] offset:2048
cv 83,86,87
maav 60,124,76
cv 84,112,113
cv 85,114,115
cv 86,116,117
cv 87,118,119
mvav 160,64,80
ex 88
mvav 160,96,84
ds_write_b128 v7,a[44:47] offset:3072
mvav 164,68,80
v_add_f32_e32 v130,v144,v145
v_add_f32_e32 v131,v148,v149
mvav 164,100,84
ex 89
mvav 168,72,80
ds_write_b128 v7,a[48:51] offset:4096
mvav 168,104,84
ex 90
mvav 172,76,80
ex 91
mvav 172,108,84
ds_write_b128 v32,v[128:131]
v_smfmac_f32_16x16x64_bf16 v[152:155],v[228:231],v[80:87],v248
ex 92
ex 93
mvav 176,80,80
ds_write_b128 v7,a[52:55] offset:5120
ex 94
mvav 176,112,84
ra 200,5,2048
ra 204,5,3072
ex 95
cv 88,88,89
mvav 180,84,80
ra 208,5,4096
ra 212,5,5120
cv 89,90,91
cv 90,92,93
ex 120
mvav 180,116,84
ds_write_b128 v7,a[56:59] offset:6144
ex 121
mvav 184,88,80
ex 122
ex 123
cv 91,94,95
mvav 184,120,84
ex 124
ex 125
mvav 188,92,80
ds_write_b128 v7,a[60:63] offset:7168
ex 126
mvav 188,124,84
ra 216,5,6144
ra 220,5,7168
ra 224,5,8192
ra 232,5,10240
ex 127
cv 92,120,121
mvav 192,64,88
ra 240,5,12288
ra 248,5,14336
ra 228,5,9216
ra 236,5,11264
ra 244,5,13312
ra 252,5,15360
cv 93,122,123
cv 94,124,125
cv 95,126,127
v_xor_b32_e32 v6,8192,v6
s_nop 0
mvav 192,96,92
ex 16
mvav 196,68,88
s_waitcnt lgkmcnt(10)
s_xor_b32 s87,s87,0
mvav 196,100,92
s_xor_b32 s50,s66,294912
s_barrier
v_xor_b32_e32 v7,8192,v7
mvav 200,72,88
la 4,252,50,0
ra 64,5,16384
ra 96,5,24576
rv 64,6,0
rv 68,6,1024
rv 72,6,2048
mvav 200,104,92
rv 76,6,3072
rv 80,6,4096
rv 84,6,5120
ex 17
ex 18
mvav 204,76,88
rv 96,7,0
rv 100,7,1024
ex 19
mvav 204,108,92
la 12,252,50,2048
rv 104,7,2048
rv 108,7,3072
cv 16,16,17
v_smfmac_f32_16x16x64_bf16 v[156:159],v[228:231],v[88:95],v248
rv 112,7,4096
rv 116,7,5120
ex 20
ex 21
mvav 208,80,88
rv 120,7,6144
rv 124,7,7168
ex 22
ex 23
mvav 208,112,92
la 20,250,50,0
ra 68,5,17408
ra 100,5,25600
cv 17,18,19
cv 18,20,21
mvav 212,84,88
ra 72,5,18432
ra 104,5,26624
ex 48
ex 49
mvav 212,116,92
ra 76,5,19456
ra 108,5,27648
ex 50
ex 51
mvav 216,88,88
la 28,250,50,2048
ra 80,5,20480
ra 112,5,28672
ra 84,5,21504
mvav 216,120,92
L_vs11:
s_cbranch_execz L_vs15
ra 116,5,29696
cv 19,22,23
ex 52
ex 53
mvav 220,92,88
ra 88,5,22528
cv 20,48,49
ex 54
ex 55
mvav 220,124,92
la 60,250,50,3072
ra 120,5,30720
cv 21,50,51
cv 22,52,53
cv 23,54,55
s_bitcmp1_b32 s82,s81
s_cbranch_scc1 L_pend2
L_ret_eb5:
mvav 160,192,16
ra 92,5,23552
rv 88,6,6144
ra 124,5,31744
rv 92,6,7168
ex 24
s_waitcnt lgkmcnt(10)
mvav 160,224,20
ex 25
ex 26
v_smfmac_f32_16x16x64_bf16 v[152:155],v[228:231],v[16:23],v248
ex 27
v_xor_b32_e32 v32,4096,v32
mvav 164,196,16
la 52,250,50,1024
ds_read_b128 v[236:239],v32
cv 24,24,25
ex 28
mvav 164,228,20
ex 29
cv 25,26,27
ex 30
ex 31
mvav 168,200,16
ex 56
ex 57
mvav 168,232,20
la 44,252,50,3072
cv 26,28,29
ex 58
ex 59
mvav 172,204,16
ex 60
ex 61
ex 62
mvav 172,236,20
s_waitcnt lgkmcnt(0)
s_barrier
cv 27,30,31
ex 63
mvav 176,208,16
la 36,252,50,1024
cv 28,56,57
cv 29,58,59
cv 30,60,61
cv 31,62,63
mvav 176,240,20
s_add_u32 m0,s52,147456
ex 8
s_xor_b32 s51,s63,1048576
ex 9
ex 10
mvav 180,212,16
s_add_u32 s50,s50,s55
ex 11
cv 8,8,9
cv 9,10,11
ex 12
mvav 180,244,20
dl 247,24,51,3072
ex 13
ex 14
mvav 184,216,16
s_add_u32 m0,s52,0
ex 15
ex 40
ex 41
mvav 184,248,20
s_xor_b32 s51,s64,0
ex 42
ex 43
ex 44
mvav 188,220,16
la 0,252,50,0
ex 45
ex 46
mvav 188,252,20
ex 47
cv 10,12,13
cv 11,14,15
cv 12,40,41
cv 13,42,43
cv 14,44,45
v_smfmac_f32_16x16x64_bf16 v[156:159],v[228:231],v[24:31],v248
cv 15,46,47
v_bfe_u32 v225,v244,1,5
s_nop 0
v_smfmac_f32_16x16x64_bf16 v[156:159],v[228:231],v[8:15],v248
v_lshl_add_u32 v224,v224,1,v225
v_bfe_u32 v225,v244,7,2
v_lshl_add_u32 v224,v225,5,v224
mvav 192,192,24
la 8,252,50,2048
v_xor_b32_e32 v224,s69,v224
v_add_f32_e32 v152,v152,v153
v_add_f32_e32 v137,v152,v238
v_subrev_u32_e32 v236,0x23800000,v137
mvav 192,224,28
v_rcp_f32_e32 v137,v137
mvav 192,64,8
tv 128,224,32768
v_mul_f32_e32 v232,v240,v137
v_mul_f32_e32 v64,v232,v64
v_mul_f32_e32 v65,v232,v65
v_mul_f32_e32 v66,v232,v66
mvav 192,96,12
la 16,250,50,0
v_mul_f32_e32 v67,v232,v67
v_mul_f32_e32 v68,v232,v68
v_mul_f32_e32 v69,v232,v69
v_mul_f32_e32 v70,v232,v70
mvav 196,196,24
v_fmac_f32_e32 v64,v232,v160
v_fmac_f32_e32 v65,v232,v161
v_mul_f32_e32 v71,v232,v71
mvav 196,228,28
v_fmac_f32_e32 v66,v232,v162
v_fmac_f32_e32 v67,v232,v163
v_fmac_f32_e32 v68,v232,v164
v_fmac_f32_e32 v69,v232,v165
v_fmac_f32_e32 v70,v232,v166
mvav 196,68,8
la 24,250,50,2048
v_fmac_f32_e32 v71,v232,v167
cv 160,64,65
cv 161,66,67
cv 162,68,69
cv 163,70,71
mvav 196,100,12
tv 130,224,34816
tv 132,224,36864
mvav 200,200,24
tv 134,224,38912
tv 144,224,40960
v_mul_f32_e32 v72,v232,v72
v_mul_f32_e32 v73,v232,v73
v_mul_f32_e32 v74,v232,v74
mvav 200,232,28
buffer_store_dwordx4 v[160:163],v244,s[8:11],s14 offen offset:0 sc0 sc1
v_mul_f32_e32 v75,v232,v75
v_mul_f32_e32 v76,v232,v76
v_mul_f32_e32 v77,v232,v77
v_mul_f32_e32 v78,v232,v78
mvav 200,72,8
v_fmac_f32_e32 v72,v232,v168
v_fmac_f32_e32 v73,v232,v169
v_mul_f32_e32 v79,v232,v79
mvav 200,104,12
v_fmac_f32_e32 v74,v232,v170
v_fmac_f32_e32 v75,v232,v171
v_fmac_f32_e32 v76,v232,v172
v_fmac_f32_e32 v77,v232,v173
v_fmac_f32_e32 v78,v232,v174
mvav 204,204,24
la 56,250,50,3072
v_fmac_f32_e32 v79,v232,v175
cv 168,72,73
cv 169,74,75
cv 170,76,77
cv 171,78,79
mvav 204,236,28
tv 146,224,43008
tv 148,224,45056
mvav 204,76,8
tv 150,224,47104
v_mul_f32_e32 v80,v232,v80
v_mul_f32_e32 v81,v232,v81
v_mul_f32_e32 v82,v232,v82
mvav 204,108,12
buffer_store_dwordx4 v[168:171],v244,s[8:11],s14 offen offset:2048 sc0 sc1
v_mul_f32_e32 v83,v232,v83
v_mul_f32_e32 v84,v232,v84
v_mul_f32_e32 v85,v232,v85
v_mul_f32_e32 v86,v232,v86
v_mul_f32_e32 v87,v232,v87
mvav 208,208,24
v_fmac_f32_e32 v80,v232,v176
v_fmac_f32_e32 v81,v232,v177
v_fmac_f32_e32 v82,v232,v178
mvav 208,240,28
v_fmac_f32_e32 v83,v232,v179
v_fmac_f32_e32 v84,v232,v180
v_fmac_f32_e32 v85,v232,v181
cv 176,80,81
cv 177,82,83
mvav 208,80,8
la 48,250,50,1024
v_fmac_f32_e32 v86,v232,v182
v_fmac_f32_e32 v87,v232,v183
cv 178,84,85
cv 179,86,87
mvav 208,112,12
v_mul_f32_e32 v88,v232,v88
v_mul_f32_e32 v89,v232,v89
v_mul_f32_e32 v90,v232,v90
mvav 212,212,24
v_mul_f32_e32 v91,v232,v91
v_mul_f32_e32 v92,v232,v92
v_mul_f32_e32 v93,v232,v93
v_mul_f32_e32 v94,v232,v94
v_mul_f32_e32 v95,v232,v95
mvav 212,244,28
buffer_store_dwordx4 v[176:179],v244,s[8:11],s88 offen offset:0 sc0 sc1
v_fmac_f32_e32 v88,v232,v184
v_fmac_f32_e32 v89,v232,v185
v_fmac_f32_e32 v90,v232,v186
v_fmac_f32_e32 v91,v232,v187
cv 184,88,89
mvav 212,84,8
v_fmac_f32_e32 v92,v232,v188
cv 185,90,91
mvav 212,116,12
v_fmac_f32_e32 v93,v232,v189
v_fmac_f32_e32 v94,v232,v190
v_fmac_f32_e32 v95,v232,v191
cv 186,92,93
cv 187,94,95
mvav 216,216,24
la 40,252,50,3072
v_add_f32_e32 v156,v156,v157
v_add_f32_e32 v141,v156,v239
v_subrev_u32_e32 v237,0x23800000,v141
v_rcp_f32_e32 v141,v141
mvav 216,248,28
v_mul_f32_e32 v233,v241,v141
v_mul_f32_e32 v96,v233,v96
mvav 216,88,8
v_mul_f32_e32 v97,v233,v97
v_mul_f32_e32 v98,v233,v98
v_mul_f32_e32 v99,v233,v99
v_mul_f32_e32 v100,v233,v100
mvav 216,120,12
buffer_store_dwordx4 v[184:187],v244,s[8:11],s88 offen offset:2048 sc0 sc1
v_fmac_f32_e32 v96,v233,v192
v_fmac_f32_e32 v97,v233,v193
v_mul_f32_e32 v101,v233,v101
v_mul_f32_e32 v102,v233,v102
v_mul_f32_e32 v103,v233,v103
mvav 220,220,24
)"
R"(v_fmac_f32_e32 v98,v233,v194
v_fmac_f32_e32 v99,v233,v195
mvav 220,252,28
v_fmac_f32_e32 v100,v233,v196
v_fmac_f32_e32 v101,v233,v197
cv 192,96,97
cv 193,98,99
mvav 220,92,8
la 32,252,50,1024
v_fmac_f32_e32 v102,v233,v198
v_fmac_f32_e32 v103,v233,v199
cv 194,100,101
cv 195,102,103
mvav 220,124,12
v_xor_b32_e32 v225,32,v224
v_xor_b32_e32 v226,64,v224
s_waitcnt lgkmcnt(0)
zvva 0,128,128
tv 138,225,34816
tv 142,225,38912
tv 152,225,40960
s_waitcnt vmcnt(36)
mvva 0,132,132
tv 136,225,32768
tv 154,225,43008
mvva 0,144,136
tv 140,225,36864
tv 158,225,47104
mvva 0,148,140
tv 156,225,45056
v_mul_f32_e32 v104,v233,v104
v_mul_f32_e32 v105,v233,v105
v_mul_f32_e32 v106,v233,v106
zvva 4,148,156
buffer_store_dwordx4 v[192:195],v244,s[8:11],s14 offen offset:1024 sc0 sc1
v_mul_f32_e32 v107,v233,v107
v_mul_f32_e32 v108,v233,v108
v_mul_f32_e32 v109,v233,v109
v_mul_f32_e32 v110,v233,v110
mvva 4,144,152
v_fmac_f32_e32 v104,v233,v200
v_fmac_f32_e32 v105,v233,v201
v_fmac_f32_e32 v106,v233,v202
v_mul_f32_e32 v111,v233,v111
mvva 4,132,148
v_fmac_f32_e32 v107,v233,v203
v_fmac_f32_e32 v108,v233,v204
v_fmac_f32_e32 v109,v233,v205
cv 200,104,105
cv 201,106,107
mvva 4,128,144
v_fmac_f32_e32 v110,v233,v206
v_fmac_f32_e32 v111,v233,v207
cv 202,108,109
cv 203,110,111
s_waitcnt vmcnt(16)
zvva 8,128,160
v_cmp_lt_u32_e64 s[42:43],s48,v236
v_mul_f32_e32 v112,v233,v112
s_or_b64 s[40:41],s[40:41],s[42:43]
v_cmp_lt_u32_e64 s[42:43],s48,v237
mvva 8,132,164
s_or_b64 s[40:41],s[40:41],s[42:43]
v_mul_f32_e32 v113,v233,v113
v_mul_f32_e32 v114,v233,v114
v_mul_f32_e32 v115,v233,v115
mvva 8,144,168
dl 245,36,51,0
v_mul_f32_e32 v116,v233,v116
v_mul_f32_e32 v117,v233,v117
v_mul_f32_e32 v118,v233,v118
v_mul_f32_e32 v119,v233,v119
v_mul_f32_e32 v120,v233,v120
mvva 8,148,172
v_fmac_f32_e32 v112,v233,v208
v_fmac_f32_e32 v113,v233,v209
v_fmac_f32_e32 v114,v233,v210
v_fmac_f32_e32 v115,v233,v211
zvva 12,148,188
v_fmac_f32_e32 v116,v233,v212
v_fmac_f32_e32 v117,v233,v213
cv 208,112,113
cv 209,114,115
mvva 12,144,184
v_fmac_f32_e32 v118,v233,v214
v_fmac_f32_e32 v119,v233,v215
cv 210,116,117
cv 211,118,119
mvva 12,132,180
v_mul_f32_e32 v121,v233,v121
v_mul_f32_e32 v122,v233,v122
v_mul_f32_e32 v123,v233,v123
mvva 12,128,176
v_mul_f32_e32 v124,v233,v124
v_mul_f32_e32 v125,v233,v125
v_mul_f32_e32 v126,v233,v126
v_mul_f32_e32 v127,v233,v127
s_waitcnt lgkmcnt(0)
zvva 28,136,176
buffer_store_dwordx4 v[200:203],v244,s[8:11],s14 offen offset:3072 sc0 sc1
tv 144,226,40960
v_fmac_f32_e32 v120,v233,v216
v_fmac_f32_e32 v121,v233,v217
v_fmac_f32_e32 v122,v233,v218
v_fmac_f32_e32 v123,v233,v219
mvva 28,140,180
tv 128,226,32768
tv 130,226,34816
cv 216,120,121
cv 217,122,123
mvva 28,152,184
tv 132,226,36864
tv 134,226,38912
v_fmac_f32_e32 v124,v233,v220
v_fmac_f32_e32 v125,v233,v221
v_fmac_f32_e32 v126,v233,v222
mvva 28,156,188
tv 146,226,43008
v_fmac_f32_e32 v127,v233,v223
cv 218,124,125
cv 219,126,127
zvva 24,156,172
tv 148,226,45056
tv 150,226,47104
mvva 24,152,168
v_xor_b32_e32 v227,96,v224
v_add_u32_e32 v232,131072,v224
mvva 24,140,164
buffer_store_dwordx4 v[208:211],v244,s[8:11],s88 offen offset:1024 sc0 sc1
mvva 24,136,160
v_add_u32_e32 v233,131072,v225
v_add_u32_e32 v234,131072,v226
zvva 20,136,144
v_add_u32_e32 v235,131072,v227
v_mov_b32_e32 v242,83914752
mvva 20,140,148
v_add_u32_e32 v228,65536,v224
v_add_u32_e32 v229,65536,v225
mvva 20,152,152
v_add_u32_e32 v230,65536,v226
v_add_u32_e32 v231,65536,v227
mvva 20,156,156
v_and_b32_e32 v243,240,v244
v_lshlrev_b32_e32 v243,7,v243
zvva 16,156,140
buffer_store_dwordx4 v[216:219],v244,s[8:11],s88 offen offset:3072 sc0 sc1
s_cmp_lg_u64 s[40:41],0
s_cbranch_scc1 L_redo
L_redo_r5:
mvva 16,152,136
tv 156,227,45056
v_mov_b32_e32 v236,16256
mvva 16,140,132
tv 158,227,47104
v_and_b32_e32 v248,256,v244
mvva 16,136,128
tv 152,227,40960
v_lshrrev_b32_e32 v248,3,v248
s_waitcnt lgkmcnt(3)
zvva 32,128,128
tv 140,227,36864
v_add_u32_e32 v243,v243,v248
mvva 32,132,132
tv 136,227,32768
tv 138,227,34816
mvva 32,144,136
dl 246,36,51,1024
tv 142,227,38912
mvva 32,148,140
tv 154,227,43008
v_and_b32_e32 v248,512,v244
zvva 36,148,156
v_lshrrev_b32_e32 v248,5,v248
v_add_u32_e32 v243,v243,v248
mvva 36,144,152
v_and_b32_e32 v248,16,v244
v_lshlrev_b32_e32 v248,v248,v236
mvva 36,132,148
v_cndmask_b32_e64 v238,0,v248,s[74:75]
s_mov_b32 s74,0xc0000c
mvva 36,128,144
s_mov_b32 s75,0xc0000c00
v_mov_b32_e32 v240,0
v_cndmask_b32_e64 v239,0,v248,s[74:75]
zvva 40,128,160
dl 249,36,51,2048
mvva 40,132,164
v_mov_b32_e32 v241,0
v_mov_b32_e32 v236,0
mvva 40,144,168
v_mov_b32_e32 v237,0
mvva 40,148,172
zvva 44,148,188
mvva 44,144,184
mvva 44,132,180
dl 247,36,51,3072
s_add_u32 m0,s52,16384
s_xor_b32 s51,s65,0
mvva 44,128,176
tv 144,224,57344
s_waitcnt lgkmcnt(1)
zvva 60,136,176
tv 132,224,53248
tv 134,224,55296
mvva 60,140,180
tv 128,224,49152
tv 130,224,51200
mvva 60,152,184
mvva 60,156,188
tv 146,224,59392
zvva 56,156,172
dl 245,36,51,0
tv 148,224,61440
tv 150,224,63488
mvva 56,152,168
mvva 56,140,164
mvva 56,136,160
zvva 52,136,144
mvva 52,140,148
mvva 52,152,152
dl 246,36,51,1024
mvva 52,156,156
zvva 48,156,140
mvva 48,152,136
mvva 48,140,132
tv 156,225,61440
mvva 48,136,128
tv 152,225,57344
s_waitcnt lgkmcnt(2)
zvva 64,128,128
dl 249,36,51,2048
tv 140,225,53248
mvva 64,132,132
tv 136,225,49152
tv 138,225,51200
mvva 64,144,136
tv 142,225,55296
mvva 64,148,140
tv 154,225,59392
zvva 68,148,156
tv 158,225,63488
mvva 68,144,152
mvva 68,132,148
dl 247,36,51,3072
s_lshl_b32 s51,s33,5
s_add_u32 m0,s52,98304
s_xor_b32 s51,s64,s51
mvva 68,128,144
zvva 72,128,160
mvva 72,132,164
s_waitcnt vmcnt(18)
s_mov_b32 s90,0
mvva 72,144,168
s_xor_b32 s50,s67,0
mvva 72,148,172
zvva 76,148,188
dl 245,36,51,0
mvva 76,144,184
mvva 76,132,180
tv 148,226,61440
mvva 76,128,176
tv 144,226,57344
s_waitcnt lgkmcnt(2)
zvva 92,136,176
tv 132,226,53248
mvva 92,140,180
tv 128,226,49152
tv 130,226,51200
mvva 92,152,184
dl 246,36,51,1024
tv 134,226,55296
mvva 92,156,188
tv 146,226,59392
zvva 88,156,172
tv 150,226,63488
mvva 88,152,168
mvva 88,140,164
mvva 88,136,160
zvva 84,136,144
dl 249,36,51,2048
mvva 84,140,148
mvva 84,152,152
mvva 84,156,156
zvva 80,156,140
mvva 80,152,136
mvva 80,140,132
dl 247,36,51,3072
mvva 80,136,128
s_lshl_b32 s51,s33,5
tv 152,227,57344
s_add_u32 m0,s52,114688
s_waitcnt lgkmcnt(1)
zvva 96,128,128
s_xor_b32 s51,s65,s51
tv 140,227,53248
tv 142,227,55296
mvva 96,132,132
tv 136,227,49152
tv 138,227,51200
mvva 96,144,136
mvva 96,148,140
tv 154,227,59392
zvva 100,148,156
lv 164,252,50,0
tv 156,227,61440
tv 158,227,63488
mvva 100,144,152
mvva 100,132,148
mvva 100,128,144
zvva 104,128,160
mvva 104,132,164
mvva 104,144,168
lv 172,252,50,2048
mvva 104,148,172
zvva 108,148,188
mvva 108,144,184
mvva 108,132,180
mvva 108,128,176
s_barrier
s_waitcnt lgkmcnt(0)
zvva 124,136,176
lv 180,250,50,0
tv 132,232,4096
tv 144,232,8192
tv 146,232,10240
mvva 124,140,180
tv 128,232,0
tv 130,232,2048
mvva 124,152,184
tv 134,232,6144
mvva 124,156,188
tv 148,232,12288
tv 150,232,14336
zvva 120,156,172
mvva 120,152,168
mvva 120,140,164
lv 188,250,50,2048
mvva 120,136,160
zvva 116,136,144
mvva 116,140,148
mvva 116,152,152
mvva 116,156,156
zvva 112,156,140
lv 220,250,50,3072
mvva 112,152,136
mvva 112,140,132
tv 156,233,12288
mvva 112,136,128
tv 152,233,8192
s_waitcnt lgkmcnt(2)
mvva 0,128,4
tv 140,233,4096
mvva 0,132,12
tv 136,233,0
tv 138,233,2048
mvva 0,144,20
lv 212,250,50,1024
tv 142,233,6144
mvva 0,148,28
tv 154,233,10240
mvva 4,148,60
tv 158,233,14336
mvva 4,144,52
mvva 4,132,44
mvva 4,128,36
L_vs12:
s_cbranch_execz L_cw3
mvva 8,128,0
lv 204,252,50,3072
mvva 8,132,8
mvva 8,144,16
mvva 8,148,24
mvva 12,148,56
s_waitcnt vmcnt(27)
mvva 12,144,48
s_waitcnt vmcnt(25)
mvva 12,132,40
lv 196,252,50,1024
s_waitcnt vmcnt(24)
mvva 12,128,32
tv 144,234,8192
s_waitcnt lgkmcnt(1)
mvva 28,136,32
tv 132,234,4096
tv 134,234,6144
mvva 28,140,40
tv 128,234,0
tv 130,234,2048
mvva 28,152,48
mvva 28,156,56
tv 146,234,10240
mvva 24,156,24
dl 245,36,51,0
s_waitcnt vmcnt(20)
s_barrier
tv 148,234,12288
tv 150,234,14336
mvva 24,152,16
mvva 24,140,8
mvva 24,136,0
mvva 20,136,36
mvva 20,140,44
mvva 20,152,52
s_cmp_eq_u32 s12,0
s_cselect_b64 exec,1,0
buffer_store_dword v251,off,s[8:11],s61 sc0 sc1
s_mov_b64 exec,-1
mvva 20,156,60
mvva 16,156,28
mvva 16,152,20
mvva 16,140,12
tv 156,235,12288
mvva 16,136,4
tv 152,235,8192
s_waitcnt lgkmcnt(2)
mvva 32,128,4
dl 246,36,51,1024
tv 140,235,4096
mvva 32,132,12
tv 136,235,0
tv 138,235,2048
mvva 32,144,20
tv 142,235,6144
mvva 32,148,28
tv 154,235,10240
mvva 36,148,60
tv 158,235,14336
mvva 36,144,52
mvva 36,132,44
dl 249,36,51,2048
mvva 36,128,36
mvva 40,128,0
mvva 40,132,8
mvva 40,144,16
mvva 40,148,24
mvva 44,148,56
dl 247,36,51,3072
s_lshl_b32 s51,s34,5
s_add_u32 m0,s52,65536
s_xor_b32 s51,s64,s51
mvva 44,144,48
tv 148,232,28672
mvva 44,132,40
s_xor_b32 s53,s60,s33
tv 150,232,30720
mvva 44,128,32
tv 144,232,24576
s_waitcnt lgkmcnt(3)
mvva 60,136,32
tv 132,232,20480
mvva 60,140,40
tv 128,232,16384
tv 130,232,18432
mvva 60,152,48
dl 245,36,51,0
tv 134,232,22528
mvva 60,156,56
tv 146,232,26624
mvva 56,156,24
mvva 56,152,16
mvva 56,140,8
mvva 56,136,0
mvva 52,136,36
buffer_load_dword v253,v242,s[8:11],s53 offen sc1
mvva 52,140,44
mvva 52,152,52
mvva 52,156,60
mvva 48,156,28
mvva 48,152,20
mvva 48,140,12
dl 246,36,51,1024
mvva 48,136,4
tv 152,233,24576
s_waitcnt lgkmcnt(1)
mvva 64,128,4
tv 140,233,20480
tv 142,233,22528
mvva 64,132,12
tv 136,233,16384
tv 138,233,18432
mvva 64,144,20
mvva 64,148,28
tv 154,233,26624
mvva 68,148,60
dl 249,36,51,2048
tv 156,233,28672
tv 158,233,30720
mvva 68,144,52
mvva 68,132,44
mvva 68,128,36
mvva 72,128,0
mvva 72,132,8
mvva 72,144,16
dl 247,36,51,3072
s_lshl_b32 s51,s34,5
s_add_u32 m0,s52,81920
s_xor_b32 s51,s65,s51
mvva 72,148,24
mvva 76,148,56
mvva 76,144,48
mvva 76,132,40
tv 148,234,28672
mvva 76,128,32
tv 144,234,24576
s_waitcnt lgkmcnt(2)
mvva 92,136,32
dl 245,36,51,0
tv 132,234,20480
mvva 92,140,40
tv 128,234,16384
tv 130,234,18432
mvva 92,152,48
tv 134,234,22528
mvva 92,156,56
tv 146,234,26624
mvva 88,156,24
tv 150,234,30720
mvva 88,152,16
mvva 88,140,8
dl 246,36,51,1024
mvva 88,136,0
mvva 84,136,36
mvva 84,140,44
mvva 84,152,52
mvva 84,156,60
mvva 80,156,28
dl 249,36,51,2048
mvva 80,152,20
mvva 80,140,12
tv 156,235,28672
mvva 80,136,4
tv 152,235,24576
s_waitcnt lgkmcnt(2)
mvva 96,128,4
tv 140,235,20480
mvva 96,132,12
tv 136,235,16384
tv 138,235,18432
mvva 96,144,20
dl 247,36,51,3072
s_lshl_b32 s51,s35,5
s_add_u32 m0,s52,32768
s_xor_b32 s51,s64,s51
tv 142,235,22528
mvva 96,148,28
tv 154,235,26624
mvva 100,148,60
tv 158,235,30720
mvva 100,144,52
mvva 100,132,44
mvva 100,128,36
mvva 104,128,0
dl 245,36,51,0
mvva 104,132,8
mvva 104,144,16
mvva 104,148,24
mvva 108,148,56
mvva 108,144,48
mvva 108,132,40
dl 246,36,51,1024
mvva 108,128,32
s_waitcnt vmcnt(27)
s_barrier
s_waitcnt lgkmcnt(0)
mvva 124,136,32
tv 132,224,4096
tv 134,224,6144
tv 144,224,8192
mvva 124,140,40
tv 128,224,0
tv 130,224,2048
mvva 124,152,48
mvva 124,156,56
tv 146,224,10240
mvva 120,156,24
dl 249,36,51,2048
tv 148,224,12288
tv 150,224,14336
mvva 120,152,16
mvva 120,140,8
mvva 120,136,0
mvva 116,136,36
mvva 116,140,44
mvva 116,152,52
dl 247,36,51,3072
s_lshl_b32 s51,s35,5
s_add_u32 m0,s52,49152
s_xor_b32 s51,s65,s51
mvva 116,156,60
mvva 112,156,28
mvva 112,152,20
s_waitcnt vmcnt(11)
v_cmp_ne_u32_e32 vcc,4,v253
mvva 112,140,12
dl 245,36,51,0
s_cbranch_vccnz L_spa3o
L_spra3o:
mvva 112,136,4
s_xor_b32 s50,s67,s33
tv 152,225,8192
s_waitcnt lgkmcnt(1)
mvvv 0,128,164
tv 140,225,4096
tv 142,225,6144
mvvv 0,132,172
tv 136,225,0
tv 138,225,2048
mvvv 0,144,180
dl 246,36,51,1024
mvvv 0,148,188
tv 154,225,10240
mvvv 4,148,220
tv 156,225,12288
tv 158,225,14336
mvvv 4,144,212
mvvv 4,132,204
la 196,252,50,0
mvvv 4,128,196
mvvv 8,128,160
s_xor_b32 s53,s60,s34
mvvv 8,132,168
mvvv 8,144,176
la 204,252,50,2048
mvvv 8,148,184
mvvv 12,148,216
mvvv 12,144,208
mvvv 12,132,200
buffer_load_dword v253,v242,s[8:11],s53 offen sc1
mvvv 12,128,192
tv 144,226,8192
s_waitcnt lgkmcnt(1)
mvvv 28,136,192
tv 132,226,4096
tv 134,226,6144
mvvv 28,140,200
tv 128,226,0
tv 130,226,2048
mvvv 28,152,208
la 212,250,50,0
mvvv 28,156,216
tv 146,226,10240
mvvv 24,156,184
tv 148,226,12288
tv 150,226,14336
mvvv 24,152,176
mvvv 24,140,168
la 220,250,50,2048
mvvv 24,136,160
mvvv 20,136,196
mvvv 20,140,204
mvvv 20,152,212
la 252,250,50,3072
mvvv 20,156,220
mvvv 16,156,188
mvvv 16,152,180
mvvv 16,140,172
la 244,250,50,1024
mvvv 16,136,164
tv 152,227,8192
s_waitcnt lgkmcnt(1)
mvvv 32,128,164
tv 140,227,4096
tv 142,227,6144
mvvv 32,132,172
tv 136,227,0
tv 138,227,2048
mvvv 32,144,180
la 236,252,50,3072
mvvv 32,148,188
tv 154,227,10240
mvvv 36,148,220
tv 156,227,12288
tv 158,227,14336
mvvv 36,144,212
mvvv 36,132,204
la 228,252,50,1024
s_add_u32 s50,s50,s55
mvvv 36,128,196
mvvv 40,128,160
mvvv 40,132,168
mvvv 40,144,176
la 192,252,50,0
mvvv 40,148,184
mvvv 44,148,216
mvvv 44,144,208
mvvv 44,132,200
la 200,252,50,2048
mvvv 44,128,192
tv 144,224,24576
s_waitcnt lgkmcnt(1)
mvvv 60,136,192
tv 132,224,20480
tv 134,224,22528
mvvv 60,140,200
tv 128,224,16384
tv 130,224,18432
mvvv 60,152,208
la 208,250,50,0
mvvv 60,156,216
tv 146,224,26624
mvvv 56,156,184
tv 148,224,28672
tv 150,224,30720
mvvv 56,152,176
mvvv 56,140,168
la 216,250,50,2048
mvvv 56,136,160
mvvv 52,136,196
mvvv 52,140,204
mvvv 52,152,212
la 248,250,50,3072
mvvv 52,156,220
mvvv 48,156,188
mvvv 48,152,180
mvvv 48,140,172
la 240,250,50,1024
mvvv 48,136,164
tv 152,225,24576
s_waitcnt lgkmcnt(1)
mvvv 64,128,164
tv 140,225,20480
tv 142,225,22528
mvvv 64,132,172
tv 136,225,16384
tv 138,225,18432
mvvv 64,144,180
la 232,252,50,3072
mvvv 64,148,188
tv 154,225,26624
mvvv 68,148,220
tv 156,225,28672
tv 158,225,30720
mvvv 68,144,212
mvvv 68,132,204
la 224,252,50,1024
mvvv 68,128,196
mvvv 72,128,160
mvvv 72,132,168
mvvv 72,144,176
dl 249,36,51,2048
mvvv 72,148,184
mvvv 76,148,216
mvvv 76,144,208
mvvv 76,132,200
dl 247,36,51,3072
s_lshl_b32 s51,s98,5
s_xor_b32 s51,s64,s51
s_add_u32 m0,s52,131072
s_xor_b32 s51,s51,4194304
mvvv 76,128,192
tv 144,226,24576
s_waitcnt lgkmcnt(1)
mvvv 92,136,192
tv 132,226,20480
tv 134,226,22528
mvvv 92,140,200
tv 128,226,16384
tv 130,226,18432
mvvv 92,152,208
dl 245,36,51,0
mvvv 92,156,216
tv 146,226,26624
mvvv 88,156,184
tv 148,226,28672
tv 150,226,30720
mvvv 88,152,176
mvvv 88,140,168
dl 246,36,51,1024
mvvv 88,136,160
s_waitcnt vmcnt(18)
v_cmp_ne_u32_e32 vcc,4,v253
mvvv 84,136,196
s_cbranch_vccnz L_spa4o
L_spra4o:
mvvv 84,140,204
s_xor_b32 s50,s67,s34
mvvv 84,152,212
dl 249,36,51,2048
mvvv 84,156,220
mvvv 80,156,188
mvvv 80,152,180
mvvv 80,140,172
la 68,252,50,0
mvvv 80,136,164
tv 152,227,24576
s_waitcnt lgkmcnt(1)
mvvv 96,128,164
tv 140,227,20480
tv 142,227,22528
mvvv 96,132,172
tv 136,227,16384
tv 138,227,18432
mvvv 96,144,180
la 76,252,50,2048
s_xor_b32 s53,s60,s35
mvvv 96,148,188
tv 154,227,26624
mvvv 100,148,220
tv 156,227,28672
tv 158,227,30720
mvvv 100,144,212
mvvv 100,132,204
la 84,250,50,0
mvvv 100,128,196
mvvv 104,128,160
mvvv 104,132,168
mvvv 104,144,176
buffer_load_dword v253,v242,s[8:11],s53 offen sc1
mvvv 104,148,184
mvvv 108,148,216
mvvv 108,144,208
mvvv 108,132,200
la 92,250,50,2048
mvvv 108,128,192
s_barrier
s_waitcnt lgkmcnt(0)
mvvv 124,136,192
tv 132,228,36864
tv 134,228,38912
tv 144,228,40960
mvvv 124,140,200
tv 128,228,32768
tv 130,228,34816
mvvv 124,152,208
la 124,250,50,3072
mvvv 124,156,216
tv 146,228,43008
mvvv 120,156,184
tv 148,228,45056
tv 150,228,47104
mvvv 120,152,176
mvvv 120,140,168
la 116,250,50,1024
mvvv 120,136,160
mvvv 116,136,196
mvvv 116,140,204
mvvv 116,152,212
la 108,252,50,3072
mvvv 116,156,220
mvvv 112,156,188
mvvv 112,152,180
mvvv 112,140,172
la 100,252,50,1024
s_add_u32 s50,s50,s55
mvvv 112,136,164
tv 152,229,40960
s_waitcnt lgkmcnt(1)
mvva 0,128,196
tv 140,229,36864
tv 142,229,38912
mvva 0,132,204
tv 136,229,32768
tv 138,229,34816
s_waitcnt vmcnt(23)
mvva 0,144,212
la 64,252,50,0
mvva 0,148,220
tv 154,229,43008
mvva 4,148,252
tv 156,229,45056
tv 158,229,47104
mvva 4,144,244
mvva 4,132,236
la 72,252,50,2048
s_waitcnt vmcnt(23)
mvva 4,128,228
mvva 8,128,192
s_waitcnt vmcnt(22)
mvva 8,132,200
s_waitcnt vmcnt(21)
mvva 8,144,208
la 80,250,50,0
s_waitcnt vmcnt(21)
mvva 8,148,216
s_waitcnt vmcnt(20)
mvva 12,148,248
s_waitcnt vmcnt(19)
mvva 12,144,240
s_waitcnt vmcnt(18)
mvva 12,132,232
la 88,250,50,2048
s_waitcnt vmcnt(18)
mvva 12,128,224
tv 144,230,40960
s_waitcnt lgkmcnt(1)
mvva 28,136,224
tv 132,230,36864
tv 134,230,38912
mvva 28,140,232
tv 128,230,32768
tv 130,230,34816
mvva 28,152,240
la 120,250,50,3072
mvva 28,156,248
tv 146,230,43008
mvva 24,156,216
tv 148,230,45056
tv 150,230,47104
mvva 24,152,208
mvva 24,140,200
la 112,250,50,1024
mvva 24,136,192
mvva 20,136,228
mvva 20,140,236
mvva 20,152,244
la 104,252,50,3072
mvva 20,156,252
mvva 16,156,220
mvva 16,152,212
mvva 16,140,204
L_vs13:
s_cbranch_execz L_cw0
la 96,252,50,1024
mvva 16,136,196
tv 152,231,40960
s_waitcnt lgkmcnt(1)
mvva 32,128,196
tv 140,231,36864
tv 142,231,38912
mvva 32,132,204
tv 136,231,32768
tv 138,231,34816
mvva 32,144,212
dl 247,36,51,3072
s_lshl_b32 s51,s98,5
s_xor_b32 s51,s65,s51
s_add_u32 m0,s52,147456
s_xor_b32 s51,s51,4194304
mvva 32,148,220
tv 154,231,43008
mvva 36,148,252
tv 156,231,45056
tv 158,231,47104
mvva 36,144,244
mvva 36,132,236
dl 245,36,51,0
mvva 36,128,228
mvva 40,128,192
mvva 40,132,200
mvva 40,144,208
dl 246,36,51,1024
mvva 40,148,216
mvva 44,148,248
mvva 44,144,240
mvva 44,132,232
dl 249,36,51,2048
mvva 44,128,224
tv 144,228,57344
s_waitcnt lgkmcnt(1)
mvva 60,136,224
tv 132,228,53248
tv 134,228,55296
mvva 60,140,232
tv 128,228,49152
tv 130,228,51200
s_waitcnt vmcnt(17)
v_cmp_ne_u32_e32 vcc,4,v253
mvva 60,152,240
dl 247,36,51,3072
s_lshl_b32 s51,s98,5
s_xor_b32 s51,s64,s51
s_add_u32 m0,s52,0
s_xor_b32 s51,s51,4456448
s_cbranch_vccnz L_spa5o
L_spra5o:
mvva 60,156,248
s_xor_b32 s50,s67,s35
tv 146,228,59392
mvva 56,156,216
tv 148,228,61440
tv 150,228,63488
mvva 56,152,208
mvva 56,140,200
dl 245,36,51,0
mvva 56,136,192
mvva 52,136,228
mvva 52,140,236
mvva 52,152,244
la 132,252,50,0
mvva 52,156,252
mvva 48,156,220
s_xor_b32 s53,s60,s98
mvva 48,152,212
s_xor_b32 s53,s53,512
mvva 48,140,204
la 140,252,50,2048
mvva 48,136,196
tv 152,229,57344
s_waitcnt lgkmcnt(1)
mvva 64,128,196
tv 140,229,53248
tv 142,229,55296
mvva 64,132,204
tv 136,229,49152
tv 138,229,51200
mvva 64,144,212
buffer_load_dword v253,v242,s[8:11],s53 offen sc1
mvva 64,148,220
tv 154,229,59392
mvva 68,148,252
)"
R"(tv 156,229,61440
tv 158,229,63488
mvva 68,144,244
mvva 68,132,236
la 148,250,50,0
mvva 68,128,228
mvva 72,128,192
mvva 72,132,200
mvva 72,144,208
la 156,250,50,2048
mvva 72,148,216
mvva 76,148,248
mvva 76,144,240
mvva 76,132,232
la 188,250,50,3072
mvva 76,128,224
tv 144,230,57344
s_waitcnt lgkmcnt(1)
mvva 92,136,224
tv 132,230,53248
tv 134,230,55296
mvva 92,140,232
tv 128,230,49152
tv 130,230,51200
mvva 92,152,240
la 180,250,50,1024
mvva 92,156,248
tv 146,230,59392
mvva 88,156,216
tv 148,230,61440
tv 150,230,63488
mvva 88,152,208
mvva 88,140,200
la 172,252,50,3072
mvva 88,136,192
mvva 84,136,228
mvva 84,140,236
mvva 84,152,244
la 164,252,50,1024
s_add_u32 s50,s50,s55
mvva 84,156,252
mvva 80,156,220
mvva 80,152,212
mvva 80,140,204
la 128,252,50,0
mvva 80,136,196
tv 152,231,57344
s_waitcnt lgkmcnt(1)
mvva 96,128,196
tv 140,231,53248
tv 142,231,55296
mvva 96,132,204
tv 136,231,49152
tv 138,231,51200
mvva 96,144,212
la 136,252,50,2048
mvva 96,148,220
tv 154,231,59392
mvva 100,148,252
tv 156,231,61440
tv 158,231,63488
mvva 100,144,244
mvva 100,132,236
la 144,250,50,0
mvva 100,128,228
mvva 104,128,192
mvva 104,132,200
mvva 104,144,208
la 152,250,50,2048
mvva 104,148,216
mvva 108,148,248
mvva 108,144,240
mvva 108,132,232
la 184,250,50,3072
mvva 108,128,224
s_barrier
s_waitcnt lgkmcnt(0)
mvva 124,136,224
tv 132,228,4096
tv 134,228,6144
tv 144,228,8192
mvva 124,140,232
tv 128,228,0
tv 130,228,2048
mvva 124,152,240
la 176,250,50,1024
mvva 124,156,248
tv 146,228,10240
mvva 120,156,216
tv 148,228,12288
tv 150,228,14336
mvva 120,152,208
mvva 120,140,200
la 168,252,50,3072
mvva 120,136,192
mvva 116,136,228
mvva 116,140,236
mvva 116,152,244
la 160,252,50,1024
mvva 116,156,252
mvva 112,156,220
mvva 112,152,212
mvva 112,140,204
dl 246,36,51,1024
mvva 112,136,196
tv 152,229,8192
s_waitcnt lgkmcnt(1)
mvva 0,128,68
tv 140,229,4096
tv 142,229,6144
mvva 0,132,76
tv 136,229,0
tv 138,229,2048
mvva 0,144,84
dl 249,36,51,2048
s_waitcnt vmcnt(25)
mvva 0,148,92
tv 154,229,10240
mvva 4,148,124
tv 156,229,12288
tv 158,229,14336
mvva 4,144,116
mvva 4,132,108
dl 247,36,51,3072
s_lshl_b32 s51,s98,5
s_xor_b32 s51,s64,s51
s_add_u32 m0,s52,16384
s_xor_b32 s51,s51,4718592
mvva 4,128,100
mvva 8,128,64
mvva 8,132,72
mvva 8,144,80
dl 245,36,51,0
mvva 8,148,88
s_waitcnt vmcnt(18)
v_cmp_ne_u32_e32 vcc,4,v253
mvva 12,148,120
s_cbranch_vccnz L_spa6o
L_spra6o:
mvva 12,144,112
s_xor_b32 s50,s67,s98
mvva 12,132,104
dl 246,36,51,1024
s_xor_b32 s50,s50,131072
mvva 12,128,96
tv 144,230,8192
s_waitcnt lgkmcnt(1)
mvva 28,136,96
tv 132,230,4096
tv 134,230,6144
mvva 28,140,104
tv 128,230,0
tv 130,230,2048
mvva 28,152,112
la 4,252,50,0
mvva 28,156,120
tv 146,230,10240
mvva 24,156,88
tv 148,230,12288
tv 150,230,14336
mvva 24,152,80
mvva 24,140,72
la 12,252,50,2048
s_xor_b32 s53,s60,s98
s_xor_b32 s53,s53,8704
mvva 24,136,64
mvva 20,136,100
mvva 20,140,108
mvva 20,152,116
la 20,250,50,0
mvva 20,156,124
mvva 16,156,92
mvva 16,152,84
mvva 16,140,76
buffer_load_dword v253,v242,s[8:11],s53 offen sc1
mvva 16,136,68
tv 152,231,8192
s_waitcnt lgkmcnt(1)
mvva 32,128,68
tv 140,231,4096
tv 142,231,6144
mvva 32,132,76
tv 136,231,0
tv 138,231,2048
mvva 32,144,84
la 28,250,50,2048
mvva 32,148,92
tv 154,231,10240
mvva 36,148,124
tv 156,231,12288
tv 158,231,14336
mvva 36,144,116
mvva 36,132,108
la 60,250,50,3072
mvva 36,128,100
mvva 40,128,64
mvva 40,132,72
mvva 40,144,80
la 52,250,50,1024
mvva 40,148,88
mvva 44,148,120
mvva 44,144,112
mvva 44,132,104
la 44,252,50,3072
mvva 44,128,96
tv 144,228,24576
s_waitcnt lgkmcnt(1)
mvva 60,136,96
tv 132,228,20480
tv 134,228,22528
mvva 60,140,104
tv 128,228,16384
tv 130,228,18432
mvva 60,152,112
la 36,252,50,1024
s_add_u32 s50,s50,s55
mvva 60,156,120
tv 146,228,26624
mvva 56,156,88
tv 148,228,28672
tv 150,228,30720
mvva 56,152,80
mvva 56,140,72
la 0,252,50,0
mvva 56,136,64
mvva 52,136,100
mvva 52,140,108
mvva 52,152,116
la 8,252,50,2048
mvva 52,156,124
mvva 48,156,92
mvva 48,152,84
mvva 48,140,76
la 16,250,50,0
mvva 48,136,68
tv 152,229,24576
s_waitcnt lgkmcnt(1)
mvva 64,128,68
tv 140,229,20480
tv 142,229,22528
mvva 64,132,76
tv 136,229,16384
tv 138,229,18432
mvva 64,144,84
la 24,250,50,2048
mvva 64,148,92
tv 154,229,26624
mvva 68,148,124
tv 156,229,28672
tv 158,229,30720
mvva 68,144,116
mvva 68,132,108
la 56,250,50,3072
mvva 68,128,100
mvva 72,128,64
mvva 72,132,72
mvva 72,144,80
la 48,250,50,1024
mvva 72,148,88
mvva 76,148,120
mvva 76,144,112
mvva 76,132,104
la 40,252,50,3072
mvva 76,128,96
tv 144,224,40960
s_waitcnt lgkmcnt(1)
mvva 92,136,96
tv 132,224,36864
tv 134,224,38912
mvva 92,140,104
tv 128,224,32768
tv 130,224,34816
mvva 92,152,112
la 32,252,50,1024
mvva 92,156,120
tv 146,224,43008
mvva 88,156,88
tv 148,224,45056
tv 150,224,47104
mvva 88,152,80
mvva 88,140,72
dl 249,36,51,2048
mvva 88,136,64
mvva 84,136,100
mvva 84,140,108
mvva 84,152,116
dl 247,36,51,3072
s_lshl_b32 s51,s98,5
s_xor_b32 s51,s65,s51
s_add_u32 m0,s52,98304
s_xor_b32 s51,s51,4456448
mvva 84,156,124
mvva 80,156,92
mvva 80,152,84
mvva 80,140,76
dl 245,36,51,0
mvva 80,136,68
tv 152,225,40960
s_waitcnt lgkmcnt(1)
mvva 0,128,132
tv 140,225,36864
tv 142,225,38912
mvva 0,132,140
tv 136,225,32768
tv 138,225,34816
s_waitcnt vmcnt(25)
mvva 0,144,148
dl 246,36,51,1024
mvva 0,148,156
tv 154,225,43008
mvva 4,148,188
tv 156,225,45056
tv 158,225,47104
mvva 4,144,180
s_waitcnt vmcnt(17)
v_cmp_ne_u32_e32 vcc,4,v253
mvva 4,132,172
dl 249,36,51,2048
s_cbranch_vccnz L_spa7o
L_spra7o:
mvva 4,128,164
s_xor_b32 s50,s67,s98
mvva 8,128,128
s_xor_b32 s50,s50,139264
mvva 8,132,136
mvva 8,144,144
dl 247,36,51,3072
mvva 8,148,152
mvva 12,148,184
mvva 12,144,176
mvva 12,132,168
lv 164,252,50,0
mvva 12,128,160
s_barrier
tv 144,226,40960
s_lshl_b32 s51,s98,5
s_waitcnt lgkmcnt(1)
mvva 28,136,160
s_xor_b32 s53,s60,s98
tv 132,226,36864
tv 134,226,38912
s_xor_b32 s53,s53,16896
mvva 28,140,168
s_xor_b32 s51,s65,s51
tv 128,226,32768
tv 130,226,34816
mvva 28,152,176
lv 172,252,50,2048
mvva 28,156,184
s_add_u32 m0,s52,114688
tv 146,226,43008
s_xor_b32 s51,s51,4718592
mvva 24,156,152
tv 148,226,45056
tv 150,226,47104
mvva 24,152,144
mvva 24,140,136
buffer_load_dword v253,v242,s[8:11],s53 offen sc1
mvva 24,136,128
mvva 20,136,164
mvva 20,140,172
mvva 20,152,180
lv 180,250,50,0
mvva 20,156,188
mvva 16,156,156
mvva 16,152,148
mvva 16,140,140
lv 188,250,50,2048
mvva 16,136,132
tv 152,227,40960
s_waitcnt lgkmcnt(1)
mvva 32,128,132
tv 140,227,36864
tv 142,227,38912
mvva 32,132,140
tv 136,227,32768
tv 138,227,34816
mvva 32,144,148
lv 220,250,50,3072
mvva 32,148,156
tv 154,227,43008
mvva 36,148,188
tv 156,227,45056
tv 158,227,47104
mvva 36,144,180
mvva 36,132,172
lv 212,250,50,1024
mvva 36,128,164
mvva 40,128,128
mvva 40,132,136
mvva 40,144,144
lv 204,252,50,3072
mvva 40,148,152
mvva 44,148,184
mvva 44,144,176
mvva 44,132,168
lv 196,252,50,1024
s_add_u32 s50,s50,s55
mvva 44,128,160
tv 144,224,57344
s_waitcnt lgkmcnt(1)
mvva 60,136,160
tv 132,224,53248
tv 134,224,55296
mvva 60,140,168
tv 128,224,49152
tv 130,224,51200
mvva 60,152,176
lv 160,252,50,0
mvva 60,156,184
tv 146,224,59392
mvva 56,156,152
tv 148,224,61440
tv 150,224,63488
mvva 56,152,144
mvva 56,140,136
lv 168,252,50,2048
mvva 56,136,128
mvva 52,136,164
mvva 52,140,172
mvva 52,152,180
lv 176,250,50,0
mvva 52,156,188
mvva 48,156,156
mvva 48,152,148
mvva 48,140,140
lv 184,250,50,2048
mvva 48,136,132
tv 152,225,57344
s_waitcnt lgkmcnt(1)
mvva 64,128,132
tv 140,225,53248
tv 142,225,55296
mvva 64,132,140
tv 136,225,49152
tv 138,225,51200
mvva 64,144,148
lv 216,250,50,3072
mvva 64,148,156
tv 154,225,59392
mvva 68,148,188
tv 156,225,61440
tv 158,225,63488
mvva 68,144,180
mvva 68,132,172
lv 208,250,50,1024
mvva 68,128,164
mvva 72,128,128
mvva 72,132,136
mvva 72,144,144
lv 200,252,50,3072
mvva 72,148,152
L_vs14:
s_cbranch_execz L_cw1
mvva 76,148,184
mvva 76,144,176
mvva 76,132,168
lv 192,252,50,1024
mvva 76,128,160
tv 144,232,8192
s_waitcnt lgkmcnt(1)
mvva 92,136,160
tv 132,232,4096
tv 134,232,6144
mvva 92,140,168
tv 128,232,0
tv 130,232,2048
mvva 92,152,176
dl 245,36,51,0
mvva 92,156,184
tv 146,232,10240
mvva 88,156,152
tv 148,232,12288
tv 150,232,14336
mvva 88,152,144
mvva 88,140,136
dl 246,36,51,1024
mvva 88,136,128
mvva 84,136,164
mvva 84,140,172
mvva 84,152,180
dl 249,36,51,2048
mvva 84,156,188
mvva 80,156,156
mvva 80,152,148
mvva 80,140,140
dl 247,36,51,3072
mvva 80,136,132
tv 152,233,8192
s_waitcnt vmcnt(18)
v_cmp_ne_u32_e32 vcc,4,v253
s_waitcnt lgkmcnt(1)
mvva 0,128,4
s_cbranch_vccnz L_spa8o
L_spra8o:
tv 140,233,4096
s_xor_b32 s50,s67,s98
mvva 0,132,12
s_xor_b32 s50,s50,147456
tv 136,233,0
tv 138,233,2048
mvva 0,144,20
la 196,252,50,0
tv 142,233,6144
mvva 0,148,28
tv 154,233,10240
v_and_b32_e32 v242,48,v244
mvva 4,148,60
tv 156,233,12288
tv 158,233,14336
mvva 4,144,52
v_lshlrev_b32_e32 v242,3,v242
mvva 4,132,44
la 204,252,50,2048
mvva 4,128,36
mvva 8,128,0
s_andn2_b32 s53,s61,384
mvva 8,132,8
mvva 8,144,16
buffer_load_dword v253,v242,s[8:11],s53 offen sc1
mvva 8,148,24
mvva 12,148,56
mvva 12,144,48
mvva 12,132,40
la 212,250,50,0
mvva 12,128,32
tv 144,234,8192
s_waitcnt lgkmcnt(1)
mvva 28,136,32
tv 132,234,4096
mvva 28,140,40
tv 128,234,0
tv 130,234,2048
mvva 28,152,48
la 220,250,50,2048
tv 134,234,6144
mvva 28,156,56
tv 146,234,10240
mvva 24,156,24
tv 148,234,12288
tv 150,234,14336
mvva 24,152,16
mvva 24,140,8
la 252,250,50,3072
mvva 24,136,0
mvva 20,136,36
mvva 20,140,44
mvva 20,152,52
la 244,250,50,1024
mvva 20,156,60
mvva 16,156,28
mvva 16,152,20
mvva 16,140,12
la 236,252,50,3072
mvva 16,136,4
tv 152,235,8192
s_waitcnt lgkmcnt(1)
mvva 32,128,4
tv 140,235,4096
mvva 32,132,12
tv 136,235,0
tv 138,235,2048
mvva 32,144,20
la 228,252,50,1024
tv 142,235,6144
mvva 32,148,28
s_add_u32 s50,s50,s55
tv 154,235,10240
mvva 36,148,60
tv 156,235,12288
tv 158,235,14336
mvva 36,144,52
mvva 36,132,44
la 192,252,50,0
mvva 36,128,36
mvva 40,128,0
mvva 40,132,8
mvva 40,144,16
la 200,252,50,2048
mvva 40,148,24
mvva 44,148,56
mvva 44,144,48
mvva 44,132,40
la 208,250,50,0
mvva 44,128,32
s_barrier
s_waitcnt lgkmcnt(0)
mvva 60,136,32
tv 132,232,20480
tv 144,232,24576
mvva 60,140,40
tv 128,232,16384
tv 130,232,18432
mvva 60,152,48
la 216,250,50,2048
tv 134,232,22528
mvva 60,156,56
tv 146,232,26624
mvva 56,156,24
tv 148,232,28672
tv 150,232,30720
mvva 56,152,16
mvva 56,140,8
la 248,250,50,3072
mvva 56,136,0
mvva 52,136,36
mvva 52,140,44
mvva 52,152,52
la 240,250,50,1024
mvva 52,156,60
mvva 48,156,28
mvva 48,152,20
mvva 48,140,12
la 232,252,50,3072
mvva 48,136,4
tv 152,233,24576
s_waitcnt lgkmcnt(1)
mvva 64,128,4
tv 140,233,20480
mvva 64,132,12
tv 136,233,16384
tv 138,233,18432
mvva 64,144,20
la 224,252,50,1024
tv 142,233,22528
mvva 64,148,28
tv 154,233,26624
mvva 68,148,60
tv 156,233,28672
tv 158,233,30720
mvva 68,144,52
mvva 68,132,44
mvva 68,128,36
mvva 72,128,0
mvva 72,132,8
mvva 72,144,16
mvva 72,148,24
mvva 76,148,56
mvva 76,144,48
mvva 76,132,40
tv 148,224,12288
mvva 76,128,32
tv 144,224,8192
s_waitcnt lgkmcnt(2)
mvva 92,136,32
tv 132,224,4096
mvva 92,140,40
tv 128,224,0
tv 130,224,2048
mvva 92,152,48
tv 134,224,6144
mvva 92,156,56
tv 146,224,10240
mvva 88,156,24
tv 150,224,14336
mvva 88,152,16
mvva 88,140,8
mvva 88,136,0
s_waitcnt vmcnt(14)
v_cmp_ne_u32_e32 vcc,4,v253
mvva 84,136,36
s_cbranch_vccnz L_sppo
L_sprpo:
mvva 84,140,44
mvva 84,152,52
mvva 84,156,60
mvva 80,156,28
mvva 80,152,20
mvva 80,140,12
tv 156,225,12288
mvva 80,136,4
tv 152,225,8192
s_waitcnt lgkmcnt(2)
mvvv 0,128,164
tv 140,225,4096
mvvv 0,132,172
tv 136,225,0
tv 138,225,2048
mvvv 0,144,180
tv 142,225,6144
mvvv 0,148,188
tv 154,225,10240
mvvv 4,148,220
tv 158,225,14336
mvvv 4,144,212
mvvv 4,132,204
mvvv 4,128,196
mvvv 8,128,160
mvvv 8,132,168
mvvv 8,144,176
mvvv 8,148,184
mvvv 12,148,216
mvvv 12,144,208
mvvv 12,132,200
tv 148,224,28672
mvvv 12,128,192
tv 144,224,24576
s_waitcnt lgkmcnt(2)
mvvv 28,136,192
tv 132,224,20480
mvvv 28,140,200
tv 128,224,16384
tv 130,224,18432
mvvv 28,152,208
tv 134,224,22528
mvvv 28,156,216
tv 146,224,26624
mvvv 24,156,184
tv 150,224,30720
mvvv 24,152,176
mvvv 24,140,168
mvvv 24,136,160
mvvv 20,136,196
mvvv 20,140,204
mvvv 20,152,212
mvvv 20,156,220
mvvv 16,156,188
mvvv 16,152,180
mvvv 16,140,172
tv 156,225,28672
mvvv 16,136,164
tv 152,225,24576
s_waitcnt lgkmcnt(2)
mvva 0,128,196
tv 140,225,20480
mvva 0,132,204
tv 136,225,16384
tv 138,225,18432
s_waitcnt vmcnt(8)
mvva 0,144,212
tv 142,225,22528
mvva 0,148,220
tv 154,225,26624
mvva 4,148,252
tv 158,225,30720
mvva 4,144,244
mvva 4,132,236
mvva 4,128,228
s_waitcnt vmcnt(6)
mvva 8,128,192
cv 0,0,1
cv 1,2,3
mvva 8,132,200
s_waitcnt vmcnt(5)
mvva 8,144,208
s_waitcnt vmcnt(4)
mvva 8,148,216
cv 4,4,5
cv 5,6,7
s_waitcnt vmcnt(3)
mvva 12,148,248
s_waitcnt vmcnt(2)
mvva 12,144,240
s_waitcnt vmcnt(1)
mvva 12,132,232
tv 148,226,12288
cv 8,8,9
s_waitcnt vmcnt(0)
mvva 12,128,224
tv 144,226,8192
cv 9,10,11
s_waitcnt lgkmcnt(2)
mvva 28,136,224
tv 132,226,4096
mvva 28,140,232
tv 128,226,0
tv 130,226,2048
mvva 28,152,240
tv 134,226,6144
cv 12,12,13
mvva 28,156,248
tv 146,226,10240
cv 13,14,15
mvva 24,156,216
tv 150,226,14336
mvva 24,152,208
mvva 24,140,200
mvva 24,136,192
mvva 20,136,228
cv 14,28,29
cv 15,30,31
mvva 20,140,236
buffer_store_dwordx4 v[12:15],v244,s[8:11],s56 offen offset:3072 sc0 sc1
mvva 20,152,244
mvva 20,156,252
mvva 16,156,220
cv 10,24,25
cv 11,26,27
mvva 16,152,212
buffer_store_dwordx4 v[8:11],v244,s[8:11],s56 offen offset:2048 sc0 sc1
mvva 16,140,204
tv 156,227,12288
mvva 16,136,196
cv 6,20,21
cv 7,22,23
s_waitcnt lgkmcnt(1)
mvvv 32,128,164
tv 140,227,4096
mvvv 32,132,172
buffer_store_dwordx4 v[4:7],v244,s[8:11],s56 offen offset:1024 sc0 sc1
tv 136,227,0
tv 138,227,2048
mvvv 32,144,180
tv 142,227,6144
cv 2,16,17
mvvv 32,148,188
tv 152,227,8192
tv 154,227,10240
mvvv 36,148,220
tv 158,227,14336
cv 3,18,19
mvvv 36,144,212
buffer_store_dwordx4 v[0:3],v244,s[8:11],s56 offen offset:0 sc0 sc1
mvvv 36,132,204
mvvv 36,128,196
mvvv 40,128,160
mvvv 40,132,168
mvvv 40,144,176
mvvv 40,148,184
mvvv 44,148,216
mvvv 44,144,208
mvvv 44,132,200
tv 148,226,28672
mvvv 44,128,192
s_barrier
tv 144,226,24576
s_waitcnt lgkmcnt(2)
mvvv 60,136,192
tv 132,226,20480
mvvv 60,140,200
tv 128,226,16384
tv 130,226,18432
mvvv 60,152,208
tv 134,226,22528
mvvv 60,156,216
tv 146,226,26624
mvvv 56,156,184
tv 150,226,30720
mvvv 56,152,176
mvvv 56,140,168
mvvv 56,136,160
mvvv 52,136,196
mvvv 52,140,204
mvvv 52,152,212
mvvv 52,156,220
mvvv 48,156,188
mvvv 48,152,180
mvvv 48,140,172
tv 156,227,28672
mvvv 48,136,164
tv 152,227,24576
s_waitcnt lgkmcnt(2)
mvva 32,128,196
tv 140,227,20480
mvva 32,132,204
tv 136,227,16384
tv 138,227,18432
mvva 32,144,212
tv 142,227,22528
mvva 32,148,220
tv 154,227,26624
mvva 36,148,252
tv 158,227,30720
mvva 36,144,244
mvva 36,132,236
mvva 36,128,228
mvva 40,128,192
mvva 40,132,200
cv 32,32,33
cv 33,34,35
mvva 40,144,208
mvva 40,148,216
mvva 44,148,248
mvva 44,144,240
cv 36,36,37
cv 37,38,39
mvva 44,132,232
tv 148,228,45056
mvva 44,128,224
tv 144,228,40960
cv 40,40,41
s_waitcnt lgkmcnt(2)
mvva 60,136,224
tv 132,228,36864
cv 41,42,43
mvva 60,140,232
tv 128,228,32768
tv 130,228,34816
mvva 60,152,240
tv 134,228,38912
cv 44,44,45
mvva 60,156,248
tv 146,228,43008
cv 45,46,47
mvva 56,156,216
tv 150,228,47104
mvva 56,152,208
mvva 56,140,200
mvva 56,136,192
mvva 52,136,228
cv 46,60,61
cv 47,62,63
mvva 52,140,236
buffer_store_dwordx4 v[44:47],v244,s[8:11],s57 offen offset:3072 sc0 sc1
mvva 52,152,244
mvva 52,156,252
mvva 48,156,220
cv 42,56,57
cv 43,58,59
mvva 48,152,212
buffer_store_dwordx4 v[40:43],v244,s[8:11],s57 offen offset:2048 sc0 sc1
mvva 48,140,204
tv 156,229,45056
mvva 48,136,196
cv 38,52,53
cv 39,54,55
s_waitcnt lgkmcnt(1)
mvvv 64,128,164
tv 140,229,36864
mvvv 64,132,172
buffer_store_dwordx4 v[36:39],v244,s[8:11],s57 offen offset:1024 sc0 sc1
tv 136,229,32768
tv 138,229,34816
mvvv 64,144,180
tv 142,229,38912
cv 34,48,49
mvvv 64,148,188
tv 152,229,40960
tv 154,229,43008
mvvv 68,148,220
tv 158,229,47104
cv 35,50,51
mvvv 68,144,212
buffer_store_dwordx4 v[32:35],v244,s[8:11],s57 offen offset:0 sc0 sc1
mvvv 68,132,204
mvvv 68,128,196
mvvv 72,128,160
mvvv 72,132,168
mvvv 72,144,176
mvvv 72,148,184
mvvv 76,148,216
mvvv 76,144,208
mvvv 76,132,200
tv 148,228,61440
mvvv 76,128,192
tv 144,228,57344
s_waitcnt lgkmcnt(2)
mvvv 92,136,192
tv 132,228,53248
mvvv 92,140,200
tv 128,228,49152
tv 130,228,51200
mvvv 92,152,208
tv 134,228,55296
mvvv 92,156,216
tv 146,228,59392
mvvv 88,156,184
tv 150,228,63488
mvvv 88,152,176
mvvv 88,140,168
mvvv 88,136,160
mvvv 84,136,196
mvvv 84,140,204
mvvv 84,152,212
mvvv 84,156,220
mvvv 80,156,188
mvvv 80,152,180
mvvv 80,140,172
tv 156,229,61440
mvvv 80,136,164
tv 152,229,57344
s_waitcnt lgkmcnt(2)
mvva 64,128,196
tv 140,229,53248
mvva 64,132,204
tv 136,229,49152
tv 138,229,51200
mvva 64,144,212
tv 142,229,55296
mvva 64,148,220
tv 154,229,59392
mvva 68,148,252
tv 158,229,63488
mvva 68,144,244
mvva 68,132,236
mvva 68,128,228
L_vs15:
s_cbranch_execz L_cw2
s_xor_b32 s82,s59,12
mvva 72,128,192
s_mul_i32 s82,s82,12288
cv 64,64,65
s_add_u32 s82,s82,0
mvva 72,132,200
lv 16,244,82,0
mvva 72,144,208
cv 65,66,67
cv 68,68,69
mvva 72,148,216
cv 69,70,71
)"
R"(mvva 76,148,248
mvva 76,144,240
lv 20,244,82,1024
mvva 76,132,232
tv 148,230,28672
tv 150,230,30720
mvva 76,128,224
cv 72,72,73
cv 73,74,75
s_waitcnt lgkmcnt(2)
mvva 92,136,224
tv 132,230,20480
mvva 92,140,232
lv 24,244,82,2048
tv 128,230,16384
tv 130,230,18432
mvva 92,152,240
tv 134,230,22528
cv 76,76,77
mvva 92,156,248
tv 144,230,24576
tv 146,230,26624
mvva 88,156,216
cv 77,78,79
mvva 88,152,208
lv 28,244,82,3072
mvva 88,140,200
mvva 88,136,192
cv 78,92,93
cv 79,94,95
mvva 84,136,228
mvva 84,140,236
buffer_store_dwordx4 v[76:79],v244,s[8:11],s58 offen offset:3072 sc0 sc1
mvva 84,152,244
mvva 84,156,252
mvva 80,156,220
cv 74,88,89
cv 75,90,91
mvva 80,152,212
buffer_store_dwordx4 v[72:75],v244,s[8:11],s58 offen offset:2048 sc0 sc1
mvva 80,140,204
tv 156,231,28672
mvva 80,136,196
cv 70,84,85
cv 71,86,87
s_waitcnt lgkmcnt(1)
mvva 96,128,68
tv 140,231,20480
mvva 96,132,76
buffer_store_dwordx4 v[68:71],v244,s[8:11],s58 offen offset:1024 sc0 sc1
tv 136,231,16384
tv 138,231,18432
mvva 96,144,84
tv 142,231,22528
cv 66,80,81
mvva 96,148,92
tv 152,231,24576
tv 154,231,26624
mvva 100,148,124
tv 158,231,30720
cv 67,82,83
mvva 100,144,116
buffer_store_dwordx4 v[64:67],v244,s[8:11],s58 offen offset:0 sc0 sc1
mvva 100,132,108
mvva 100,128,100
mvva 104,128,64
mvva 104,132,72
mvva 104,144,80
mvva 104,148,88
mvva 108,148,120
mvva 108,144,112
mvva 108,132,104
tv 148,226,61440
mvva 108,128,96
tv 144,226,57344
s_waitcnt lgkmcnt(2)
mvva 124,136,96
tv 132,226,53248
mvva 124,140,104
tv 128,226,49152
tv 130,226,51200
mvva 124,152,112
tv 134,226,55296
mvva 124,156,120
tv 146,226,59392
mvva 120,156,88
tv 150,226,63488
mvva 120,152,80
mvva 120,140,72
mvva 120,136,64
mvva 116,136,100
mvva 116,140,108
s_waitcnt vmcnt(7)
v_max_u32_e32 v248,v16,v17
mvva 116,152,116
v_max3_u32 v248,v248,v18,v19
s_waitcnt vmcnt(6)
v_max3_u32 v248,v248,v20,v21
v_max3_u32 v248,v248,v22,v23
mvva 116,156,124
s_waitcnt vmcnt(5)
v_max3_u32 v248,v248,v24,v25
v_max3_u32 v248,v248,v26,v27
s_waitcnt vmcnt(4)
v_max3_u32 v248,v248,v28,v29
mvva 112,156,92
v_max3_u32 v248,v248,v30,v31
v_cmp_eq_u32_e32 vcc,-1,v248
mvva 112,152,84
s_cbranch_vccnz L_spx0o
L_sprx0o:
mvva 112,140,76
tv 156,227,61440
tv 158,227,63488
v_lshlrev_b32_e32 v251,16,v18
v_and_b32_e32 v255,4294901760,v18
mvva 112,136,68
tv 152,227,57344
tv 154,227,59392
s_waitcnt lgkmcnt(4)
mvva 96,128,132
v_lshlrev_b32_e32 v253,16,v19
v_and_b32_e32 v242,4294901760,v19
mvva 96,132,140
tv 136,227,49152
tv 138,227,51200
mvva 96,144,148
tv 140,227,53248
tv 142,227,55296
v_add_f32_e32 v112,v112,v251
mvva 96,148,156
v_add_f32_e32 v113,v113,v255
v_add_f32_e32 v114,v114,v253
mvva 100,148,188
v_add_f32_e32 v115,v115,v242
v_lshlrev_b32_e32 v251,16,v22
mvva 100,144,180
v_and_b32_e32 v255,4294901760,v22
v_lshlrev_b32_e32 v253,16,v23
mvva 100,132,172
v_and_b32_e32 v242,4294901760,v23
v_add_f32_e32 v116,v116,v251
v_add_f32_e32 v117,v117,v255
mvva 100,128,164
v_add_f32_e32 v118,v118,v253
v_add_f32_e32 v119,v119,v242
mvva 104,128,128
v_lshlrev_b32_e32 v251,16,v26
v_and_b32_e32 v255,4294901760,v26
mvva 104,132,136
v_lshlrev_b32_e32 v253,16,v27
v_and_b32_e32 v242,4294901760,v27
mvva 104,144,144
v_add_f32_e32 v120,v120,v251
v_add_f32_e32 v121,v121,v255
v_add_f32_e32 v122,v122,v253
mvva 104,148,152
v_add_f32_e32 v123,v123,v242
v_lshlrev_b32_e32 v251,16,v30
mvva 108,148,184
v_and_b32_e32 v255,4294901760,v30
v_lshlrev_b32_e32 v253,16,v31
mvva 108,144,176
v_and_b32_e32 v242,4294901760,v31
v_add_f32_e32 v124,v124,v251
mvva 108,132,168
v_add_f32_e32 v125,v125,v255
v_add_f32_e32 v126,v126,v253
v_add_f32_e32 v127,v127,v242
mvva 108,128,160
tv 144,234,24576
v_lshlrev_b32_e32 v251,16,v16
s_waitcnt lgkmcnt(1)
mvva 124,136,160
tv 132,234,20480
v_and_b32_e32 v255,4294901760,v16
mvva 124,140,168
tv 128,234,16384
tv 130,234,18432
mvva 124,152,176
tv 134,234,22528
v_lshlrev_b32_e32 v253,16,v17
mvva 124,156,184
tv 146,234,26624
v_and_b32_e32 v242,4294901760,v17
mvva 120,156,152
tv 148,234,28672
tv 150,234,30720
mvva 120,152,144
v_add_f32_e32 v96,v96,v251
v_add_f32_e32 v97,v97,v255
mvva 120,140,136
v_add_f32_e32 v98,v98,v253
v_add_f32_e32 v99,v99,v242
mvva 120,136,128
v_lshlrev_b32_e32 v251,16,v20
v_and_b32_e32 v255,4294901760,v20
mvva 116,136,164
v_lshlrev_b32_e32 v253,16,v21
v_and_b32_e32 v242,4294901760,v21
mvva 116,140,172
v_add_f32_e32 v100,v100,v251
v_add_f32_e32 v101,v101,v255
mvva 116,152,180
v_add_f32_e32 v102,v102,v253
v_add_f32_e32 v103,v103,v242
mvva 116,156,188
v_lshlrev_b32_e32 v251,16,v24
v_and_b32_e32 v255,4294901760,v24
mvva 112,156,156
v_lshlrev_b32_e32 v253,16,v25
v_and_b32_e32 v242,4294901760,v25
mvva 112,152,148
v_add_f32_e32 v104,v104,v251
v_add_f32_e32 v105,v105,v255
mvva 112,140,140
tv 156,235,28672
v_add_f32_e32 v106,v106,v253
mvva 112,136,132
tv 152,235,24576
v_add_f32_e32 v107,v107,v242
s_waitcnt lgkmcnt(2)
mvva 96,128,4
tv 140,235,20480
v_lshlrev_b32_e32 v251,16,v28
mvva 96,132,12
tv 136,235,16384
tv 138,235,18432
mvva 96,144,20
tv 142,235,22528
v_and_b32_e32 v255,4294901760,v28
mvva 96,148,28
tv 154,235,26624
v_lshlrev_b32_e32 v253,16,v29
mvva 100,148,60
tv 158,235,30720
v_and_b32_e32 v242,4294901760,v29
mvva 100,144,52
v_add_f32_e32 v108,v108,v251
v_add_f32_e32 v109,v109,v255
mvva 100,132,44
v_add_f32_e32 v110,v110,v253
v_add_f32_e32 v111,v111,v242
mvva 100,128,36
mvva 104,128,0
mvva 104,132,8
mvva 104,144,16
mvva 104,148,24
mvva 108,148,56
mvva 108,144,48
mvva 108,132,40
tv 148,230,45056
mvva 108,128,32
tv 144,230,40960
s_waitcnt lgkmcnt(2)
mvva 124,136,32
s_xor_b32 s83,s59,8
tv 132,230,36864
s_mul_i32 s83,s83,12288
mvva 124,140,40
s_add_u32 s83,s83,4096
tv 128,230,32768
tv 130,230,34816
mvva 124,152,48
lv 48,244,83,0
tv 134,230,38912
mvva 124,156,56
tv 146,230,43008
mvva 120,156,24
lv 52,244,83,1024
tv 150,230,47104
mvva 120,152,16
mvva 120,140,8
lv 56,244,83,2048
mvva 120,136,0
mvva 116,136,36
lv 60,244,83,3072
mvva 116,140,44
mvva 116,152,52
s_xor_b32 s84,s59,4
mvva 116,156,60
s_mul_i32 s84,s84,12288
mvva 112,156,28
s_add_u32 s84,s84,8192
mvva 112,152,20
lv 80,244,84,0
mvva 112,140,12
tv 156,231,45056
mvva 112,136,4
lv 84,244,84,1024
tv 152,231,40960
s_waitcnt lgkmcnt(2)
mvvv 96,128,164
tv 140,231,36864
mvvv 96,132,172
lv 88,244,84,2048
tv 136,231,32768
tv 138,231,34816
mvvv 96,144,180
tv 142,231,38912
mvvv 96,148,188
lv 92,244,84,3072
tv 154,231,43008
mvvv 100,148,220
tv 158,231,47104
mvvv 100,144,212
mvvv 100,132,204
mvvv 100,128,196
mvvv 104,128,160
mvvv 104,132,168
mvvv 104,144,176
mvvv 104,148,184
mvvv 108,148,216
mvvv 108,144,208
mvvv 108,132,200
tv 148,230,61440
mvvv 108,128,192
tv 144,230,57344
s_waitcnt lgkmcnt(2)
mvvv 124,136,192
tv 132,230,53248
mvvv 124,140,200
tv 128,230,49152
tv 130,230,51200
mvvv 124,152,208
tv 134,230,55296
mvvv 124,156,216
tv 146,230,59392
mvvv 120,156,184
tv 150,230,63488
mvvv 120,152,176
mvvv 120,140,168
mvvv 120,136,160
mvvv 116,136,196
mvvv 116,140,204
mvvv 116,152,212
mvvv 116,156,220
mvvv 112,156,188
mvvv 112,152,180
mvvv 112,140,172
tv 156,231,61440
mvvv 112,136,164
tv 152,231,57344
s_waitcnt lgkmcnt(2)
mvva 96,128,196
tv 140,231,53248
mvva 96,132,204
tv 136,231,49152
tv 138,231,51200
mvva 96,144,212
tv 142,231,55296
mvva 96,148,220
tv 154,231,59392
mvva 100,148,252
tv 158,231,63488
mvva 100,144,244
mvva 100,132,236
mvva 100,128,228
mvva 104,128,192
mvva 104,132,200
mvva 104,144,208
mvva 104,148,216
s_waitcnt vmcnt(7)
v_max_u32_e32 v248,v48,v49
mvva 108,148,248
v_max3_u32 v248,v248,v50,v51
s_waitcnt vmcnt(6)
v_max3_u32 v248,v248,v52,v53
mvva 108,144,240
v_max3_u32 v248,v248,v54,v55
s_waitcnt vmcnt(5)
v_max3_u32 v248,v248,v56,v57
mvva 108,132,232
v_max3_u32 v248,v248,v58,v59
s_waitcnt vmcnt(4)
v_max3_u32 v248,v248,v60,v61
mvva 108,128,224
v_max3_u32 v248,v248,v62,v63
v_cmp_eq_u32_e32 vcc,-1,v248
s_waitcnt lgkmcnt(0)
mvva 124,136,224
s_cbranch_vccnz L_spx1o
L_sprx1o:
mvva 124,140,232
mvva 124,152,240
mvva 124,156,248
s_waitcnt vmcnt(3)
v_max_u32_e32 v248,v80,v81
mvva 120,156,216
v_max3_u32 v248,v248,v82,v83
s_waitcnt vmcnt(2)
v_max3_u32 v248,v248,v84,v85
mvva 120,152,208
v_max3_u32 v248,v248,v86,v87
s_waitcnt vmcnt(1)
v_max3_u32 v248,v248,v88,v89
mvva 120,140,200
v_max3_u32 v248,v248,v90,v91
s_waitcnt vmcnt(0)
v_max3_u32 v248,v248,v92,v93
mvva 120,136,192
v_max3_u32 v248,v248,v94,v95
v_cmp_eq_u32_e32 vcc,-1,v248
mvva 116,136,228
s_cbranch_vccnz L_spx2o
L_sprx2o:
mvva 116,140,236
mvva 116,152,244
mvva 116,156,252
mvva 112,156,220
mvva 112,152,212
mvva 112,140,204
mvva 112,136,196
mvvv 108,238,60
mvvv 108,238,92
mvvv 124,236,60
mvvv 124,236,92
mvvv 104,238,56
mvvv 104,238,88
mvvv 120,236,56
mvvv 120,236,88
mvvv 100,238,52
mvvv 100,238,84
cv 108,108,109
cv 109,110,111
cv 110,124,125
cv 111,126,127
cv 104,104,105
v_permlane16_swap_b32_e32 v108,v110
mvvv 116,236,52
v_permlane16_swap_b32_e32 v109,v111
cv 105,106,107
cv 106,120,121
cv 107,122,123
mvvv 116,236,84
buffer_store_dwordx4 v[108:111],v243,s[44:47],s73 offen
v_permlane16_swap_b32_e32 v104,v106
v_permlane16_swap_b32_e32 v105,v107
mvvv 96,238,48
cv 100,100,101
cv 101,102,103
mvvv 96,238,80
buffer_store_dwordx4 v[104:107],v243,s[44:47],s72 offen
cv 102,116,117
cv 103,118,119
mvvv 112,236,48
v_permlane16_swap_b32_e32 v100,v102
v_permlane16_swap_b32_e32 v101,v103
mvvv 112,236,80
buffer_store_dwordx4 v[100:103],v243,s[44:47],s71 offen
cv 96,96,97
cv 97,98,99
s_nop 4
cv 98,112,113
cv 99,114,115
s_nop 0
v_permlane16_swap_b32_e32 v96,v98
v_permlane16_swap_b32_e32 v97,v99
buffer_store_dwordx4 v[96:99],v243,s[44:47],s70 offen
L_oddx:
s_endpgm
L_pend2:
s_nop 7
v_add_f32_e32 v226,v152,v153
v_add_f32_e32 v227,v156,v157
v_subrev_u32_e32 v236,0x23800000,v226
v_subrev_u32_e32 v237,0x23800000,v227
v_max_u32_e32 v236,v236,v237
v_cmp_lt_u32_e64 s[40:41],s48,v236
v_rcp_f32_e32 v226,v226
v_rcp_f32_e32 v227,v227
s_add_u32 s86,s13,4096
v_mov_b32_e32 v236,0
v_mov_b32_e32 v237,0
v_mov_b32_e32 v238,0
v_mov_b32_e32 v239,0
v_mov_b64_e32 v[152:153],0
v_mov_b64_e32 v[156:157],0
v_mul_f32_e32 v226,v242,v226
v_mul_f32_e32 v227,v243,v227
v_add_u32_e32 v225,s94,v225
v_pk_mul_f32 v[160:161],v[160:161],v[226:227] op_sel:[0,0] op_sel_hi:[1,0]
v_pk_mul_f32 v[162:163],v[162:163],v[226:227] op_sel:[0,0] op_sel_hi:[1,0]
v_pk_mul_f32 v[164:165],v[164:165],v[226:227] op_sel:[0,0] op_sel_hi:[1,0]
v_pk_mul_f32 v[166:167],v[166:167],v[226:227] op_sel:[0,0] op_sel_hi:[1,0]
cv 160,160,161
cv 161,162,163
cv 162,164,165
cv 163,166,167
buffer_store_dwordx4 v[160:163],v244,s[8:11],s13 offen offset:0 sc0 sc1
v_pk_mul_f32 v[168:169],v[168:169],v[226:227] op_sel:[0,0] op_sel_hi:[1,0]
v_pk_mul_f32 v[170:171],v[170:171],v[226:227] op_sel:[0,0] op_sel_hi:[1,0]
v_pk_mul_f32 v[172:173],v[172:173],v[226:227] op_sel:[0,0] op_sel_hi:[1,0]
v_pk_mul_f32 v[174:175],v[174:175],v[226:227] op_sel:[0,0] op_sel_hi:[1,0]
cv 168,168,169
cv 169,170,171
cv 170,172,173
cv 171,174,175
buffer_store_dwordx4 v[168:171],v244,s[8:11],s13 offen offset:2048 sc0 sc1
v_pk_mul_f32 v[176:177],v[176:177],v[226:227] op_sel:[0,0] op_sel_hi:[1,0]
v_pk_mul_f32 v[178:179],v[178:179],v[226:227] op_sel:[0,0] op_sel_hi:[1,0]
v_pk_mul_f32 v[180:181],v[180:181],v[226:227] op_sel:[0,0] op_sel_hi:[1,0]
v_pk_mul_f32 v[182:183],v[182:183],v[226:227] op_sel:[0,0] op_sel_hi:[1,0]
cv 176,176,177
cv 177,178,179
cv 178,180,181
v_mfma_f32_32x32x16_bf16 v[160:175],v[236:239],v[236:239],0
cv 179,182,183
buffer_store_dwordx4 v[176:179],v244,s[8:11],s86 offen offset:0 sc0 sc1
v_pk_mul_f32 v[184:185],v[184:185],v[226:227] op_sel:[0,0] op_sel_hi:[1,0]
v_pk_mul_f32 v[186:187],v[186:187],v[226:227] op_sel:[0,0] op_sel_hi:[1,0]
v_pk_mul_f32 v[188:189],v[188:189],v[226:227] op_sel:[0,0] op_sel_hi:[1,0]
v_pk_mul_f32 v[190:191],v[190:191],v[226:227] op_sel:[0,0] op_sel_hi:[1,0]
cv 184,184,185
cv 185,186,187
cv 186,188,189
cv 187,190,191
buffer_store_dwordx4 v[184:187],v244,s[8:11],s86 offen offset:2048 sc0 sc1
v_pk_mul_f32 v[192:193],v[192:193],v[226:227] op_sel:[0,1] op_sel_hi:[1,1]
v_pk_mul_f32 v[194:195],v[194:195],v[226:227] op_sel:[0,1] op_sel_hi:[1,1]
v_pk_mul_f32 v[196:197],v[196:197],v[226:227] op_sel:[0,1] op_sel_hi:[1,1]
v_pk_mul_f32 v[198:199],v[198:199],v[226:227] op_sel:[0,1] op_sel_hi:[1,1]
cv 192,192,193
cv 193,194,195
cv 194,196,197
v_mfma_f32_32x32x16_bf16 v[176:191],v[236:239],v[236:239],0
cv 195,198,199
buffer_store_dwordx4 v[192:195],v244,s[8:11],s13 offen offset:1024 sc0 sc1
v_pk_mul_f32 v[200:201],v[200:201],v[226:227] op_sel:[0,1] op_sel_hi:[1,1]
v_pk_mul_f32 v[202:203],v[202:203],v[226:227] op_sel:[0,1] op_sel_hi:[1,1]
v_pk_mul_f32 v[204:205],v[204:205],v[226:227] op_sel:[0,1] op_sel_hi:[1,1]
v_pk_mul_f32 v[206:207],v[206:207],v[226:227] op_sel:[0,1] op_sel_hi:[1,1]
cv 200,200,201
cv 201,202,203
cv 202,204,205
cv 203,206,207
buffer_store_dwordx4 v[200:203],v244,s[8:11],s13 offen offset:3072 sc0 sc1
v_pk_mul_f32 v[208:209],v[208:209],v[226:227] op_sel:[0,1] op_sel_hi:[1,1]
v_pk_mul_f32 v[210:211],v[210:211],v[226:227] op_sel:[0,1] op_sel_hi:[1,1]
v_pk_mul_f32 v[212:213],v[212:213],v[226:227] op_sel:[0,1] op_sel_hi:[1,1]
v_pk_mul_f32 v[214:215],v[214:215],v[226:227] op_sel:[0,1] op_sel_hi:[1,1]
cv 208,208,209
cv 209,210,211
cv 210,212,213
v_mfma_f32_32x32x16_bf16 v[192:207],v[236:239],v[236:239],0
cv 211,214,215
buffer_store_dwordx4 v[208:211],v244,s[8:11],s86 offen offset:1024 sc0 sc1
v_pk_mul_f32 v[216:217],v[216:217],v[226:227] op_sel:[0,1] op_sel_hi:[1,1]
v_pk_mul_f32 v[218:219],v[218:219],v[226:227] op_sel:[0,1] op_sel_hi:[1,1]
v_pk_mul_f32 v[220:221],v[220:221],v[226:227] op_sel:[0,1] op_sel_hi:[1,1]
v_pk_mul_f32 v[222:223],v[222:223],v[226:227] op_sel:[0,1] op_sel_hi:[1,1]
cv 216,216,217
cv 217,218,219
cv 218,220,221
cv 219,222,223
buffer_store_dwordx4 v[216:219],v244,s[8:11],s86 offen offset:3072 sc0 sc1
s_nop 1
v_mfma_f32_32x32x16_bf16 v[208:223],v[236:239],v[236:239],0
s_nop 7
s_nop 1
s_cmp_eq_u32 s84,4
s_cbranch_scc1 L_ret_eb4
s_cmp_lg_u32 s80,s81
s_branch L_ret_eb5
L_redo:
s_waitcnt vmcnt(0) lgkmcnt(0)
s_nop 15
s_nop 3
s_load_dwordx2 s[76:77],%0,0
s_load_dwordx2 s[80:81],%0,16
s_mov_b32 s78,4194304
s_mov_b32 s79,131072
s_mov_b32 s82,8388608
s_mov_b32 s83,131072
s_mov_b32 s86,0
s_waitcnt lgkmcnt(0)
L_rd_t:
s_nop 15
s_nop 3
s_cmp_lt_u32 s86,2
s_cselect_b32 s88,s23,s22
s_cselect_b32 s95,s13,s14
s_and_b32 s89,s86,1
s_lshl_b32 s90,s89,10
s_add_u32 s95,s95,s90
s_lshl_b32 s89,s89,6
s_add_u32 s88,s88,s89
s_lshl_b32 s89,s12,4
s_add_u32 s88,s88,s89
v_bfe_u32 v213,v244,4,4
v_lshrrev_b32_e32 v214,8,v244
v_add_u32_e32 v210,s88,v213
v_lshlrev_b32_e32 v208,11,v210
v_lshl_add_u32 v208,v214,4,v208
v_lshlrev_b32_e32 v209,16,v214
v_lshl_add_u32 v209,v213,1,v209
s_lshl_b32 s89,s18,8
v_add_u32_e32 v209,s89,v209
v_lshlrev_b32_e32 v214,2,v214
v_sub_u32_e32 v210,v210,v214
v_accvgpr_write_b32 a192,0
v_accvgpr_write_b32 a193,0
v_accvgpr_write_b32 a194,0
v_accvgpr_write_b32 a195,0
v_accvgpr_write_b32 a196,0
v_accvgpr_write_b32 a197,0
v_accvgpr_write_b32 a198,0
v_accvgpr_write_b32 a199,0
v_accvgpr_write_b32 a200,0
v_accvgpr_write_b32 a201,0
v_accvgpr_write_b32 a202,0
v_accvgpr_write_b32 a203,0
v_accvgpr_write_b32 a204,0
v_accvgpr_write_b32 a205,0
v_accvgpr_write_b32 a206,0
v_accvgpr_write_b32 a207,0
v_accvgpr_write_b32 a208,0
v_accvgpr_write_b32 a209,0
v_accvgpr_write_b32 a210,0
v_accvgpr_write_b32 a211,0
v_accvgpr_write_b32 a212,0
v_accvgpr_write_b32 a213,0
v_accvgpr_write_b32 a214,0
v_accvgpr_write_b32 a215,0
v_accvgpr_write_b32 a216,0
v_accvgpr_write_b32 a217,0
v_accvgpr_write_b32 a218,0
v_accvgpr_write_b32 a219,0
v_accvgpr_write_b32 a220,0
v_accvgpr_write_b32 a221,0
v_accvgpr_write_b32 a222,0
v_accvgpr_write_b32 a223,0
s_mov_b32 s87,0
L_rd_q:
s_nop 15
s_nop 3
s_lshl_b32 s89,s87,6
s_lshl_b32 s90,s87,18
buffer_load_dwordx4 v[200:203],v208,s[76:79],s89 offen
buffer_load_ushort v160,v209,s[80:83],s90 offen offset:0
buffer_load_ushort v164,v209,s[80:83],s90 offen offset:32
buffer_load_ushort v168,v209,s[80:83],s90 offen offset:64
buffer_load_ushort v172,v209,s[80:83],s90 offen offset:96
buffer_load_ushort v176,v209,s[80:83],s90 offen offset:128
buffer_load_ushort v180,v209,s[80:83],s90 offen offset:160
buffer_load_ushort v184,v209,s[80:83],s90 offen offset:192
buffer_load_ushort v188,v209,s[80:83],s90 offen offset:224
s_add_u32 s90,s90,8192
buffer_load_ushort v192,v209,s[80:83],s90 offen offset:0
buffer_load_ushort v193,v209,s[80:83],s90 offen offset:32
buffer_load_ushort v194,v209,s[80:83],s90 offen offset:64
buffer_load_ushort v195,v209,s[80:83],s90 offen offset:96
buffer_load_ushort v196,v209,s[80:83],s90 offen offset:128
buffer_load_ushort v197,v209,s[80:83],s90 offen offset:160
buffer_load_ushort v198,v209,s[80:83],s90 offen offset:192
buffer_load_ushort v199,v209,s[80:83],s90 offen offset:224
s_add_u32 s90,s90,8192
s_waitcnt vmcnt(0)
v_lshl_or_b32 v160,v192,16,v160
v_lshl_or_b32 v164,v193,16,v164
v_lshl_or_b32 v168,v194,16,v168
v_lshl_or_b32 v172,v195,16,v172
v_lshl_or_b32 v176,v196,16,v176
v_lshl_or_b32 v180,v197,16,v180
v_lshl_or_b32 v184,v198,16,v184
v_lshl_or_b32 v188,v199,16,v188
buffer_load_ushort v161,v209,s[80:83],s90 offen offset:0
buffer_load_ushort v165,v209,s[80:83],s90 offen offset:32
buffer_load_ushort v169,v209,s[80:83],s90 offen offset:64
buffer_load_ushort v173,v209,s[80:83],s90 offen offset:96
buffer_load_ushort v177,v209,s[80:83],s90 offen offset:128
buffer_load_ushort v181,v209,s[80:83],s90 offen offset:160
buffer_load_ushort v185,v209,s[80:83],s90 offen offset:192
buffer_load_ushort v189,v209,s[80:83],s90 offen offset:224
s_add_u32 s90,s90,8192
buffer_load_ushort v192,v209,s[80:83],s90 offen offset:0
buffer_load_ushort v193,v209,s[80:83],s90 offen offset:32
buffer_load_ushort v194,v209,s[80:83],s90 offen offset:64
buffer_load_ushort v195,v209,s[80:83],s90 offen offset:96
buffer_load_ushort v196,v209,s[80:83],s90 offen offset:128
buffer_load_ushort v197,v209,s[80:83],s90 offen offset:160
buffer_load_ushort v198,v209,s[80:83],s90 offen offset:192
buffer_load_ushort v199,v209,s[80:83],s90 offen offset:224
s_add_u32 s90,s90,8192
s_waitcnt vmcnt(0)
v_lshl_or_b32 v161,v192,16,v161
v_lshl_or_b32 v165,v193,16,v165
v_lshl_or_b32 v169,v194,16,v169
v_lshl_or_b32 v173,v195,16,v173
v_lshl_or_b32 v177,v196,16,v177
v_lshl_or_b32 v181,v197,16,v181
v_lshl_or_b32 v185,v198,16,v185
v_lshl_or_b32 v189,v199,16,v189
buffer_load_ushort v162,v209,s[80:83],s90 offen offset:0
buffer_load_ushort v166,v209,s[80:83],s90 offen offset:32
buffer_load_ushort v170,v209,s[80:83],s90 offen offset:64
buffer_load_ushort v174,v209,s[80:83],s90 offen offset:96
buffer_load_ushort v178,v209,s[80:83],s90 offen offset:128
buffer_load_ushort v182,v209,s[80:83],s90 offen offset:160
buffer_load_ushort v186,v209,s[80:83],s90 offen offset:192
buffer_load_ushort v190,v209,s[80:83],s90 offen offset:224
s_add_u32 s90,s90,8192
buffer_load_ushort v192,v209,s[80:83],s90 offen offset:0
buffer_load_ushort v193,v209,s[80:83],s90 offen offset:32
buffer_load_ushort v194,v209,s[80:83],s90 offen offset:64
buffer_load_ushort v195,v209,s[80:83],s90 offen offset:96
buffer_load_ushort v196,v209,s[80:83],s90 offen offset:128
buffer_load_ushort v197,v209,s[80:83],s90 offen offset:160
buffer_load_ushort v198,v209,s[80:83],s90 offen offset:192
buffer_load_ushort v199,v209,s[80:83],s90 offen offset:224
s_add_u32 s90,s90,8192
s_waitcnt vmcnt(0)
v_lshl_or_b32 v162,v192,16,v162
v_lshl_or_b32 v166,v193,16,v166
v_lshl_or_b32 v170,v194,16,v170
v_lshl_or_b32 v174,v195,16,v174
v_lshl_or_b32 v178,v196,16,v178
v_lshl_or_b32 v182,v197,16,v182
v_lshl_or_b32 v186,v198,16,v186
v_lshl_or_b32 v190,v199,16,v190
buffer_load_ushort v163,v209,s[80:83],s90 offen offset:0
buffer_load_ushort v167,v209,s[80:83],s90 offen offset:32
buffer_load_ushort v171,v209,s[80:83],s90 offen offset:64
buffer_load_ushort v175,v209,s[80:83],s90 offen offset:96
buffer_load_ushort v179,v209,s[80:83],s90 offen offset:128
buffer_load_ushort v183,v209,s[80:83],s90 offen offset:160
buffer_load_ushort v187,v209,s[80:83],s90 offen offset:192
buffer_load_ushort v191,v209,s[80:83],s90 offen offset:224
s_add_u32 s90,s90,8192
buffer_load_ushort v192,v209,s[80:83],s90 offen offset:0
buffer_load_ushort v193,v209,s[80:83],s90 offen offset:32
buffer_load_ushort v194,v209,s[80:83],s90 offen offset:64
buffer_load_ushort v195,v209,s[80:83],s90 offen offset:96
buffer_load_ushort v196,v209,s[80:83],s90 offen offset:128
buffer_load_ushort v197,v209,s[80:83],s90 offen offset:160
buffer_load_ushort v198,v209,s[80:83],s90 offen offset:192
buffer_load_ushort v199,v209,s[80:83],s90 offen offset:224
s_add_u32 s90,s90,8192
s_waitcnt vmcnt(0)
v_lshl_or_b32 v163,v192,16,v163
v_lshl_or_b32 v167,v193,16,v167
v_lshl_or_b32 v171,v194,16,v171
v_lshl_or_b32 v175,v195,16,v175
v_lshl_or_b32 v179,v196,16,v179
v_lshl_or_b32 v183,v197,16,v183
v_lshl_or_b32 v187,v198,16,v187
v_lshl_or_b32 v191,v199,16,v191
mavv 192,160,200
mavv 196,164,200
mavv 200,168,200
mavv 204,172,200
mavv 208,176,200
mavv 212,180,200
mavv 216,184,200
mavv 220,188,200
s_add_u32 s87,s87,1
s_cmp_lt_u32 s87,32
s_cbranch_scc1 L_rd_q
s_nop 15
s_nop 3
v_accvgpr_read_b32 v192,a192
v_accvgpr_read_b32 v193,a193
v_accvgpr_read_b32 v194,a194
v_accvgpr_read_b32 v195,a195
v_accvgpr_read_b32 v196,a196
v_accvgpr_read_b32 v197,a197
v_accvgpr_read_b32 v198,a198
v_accvgpr_read_b32 v199,a199
v_mul_f32_e32 v192,0x3e0293ee,v192
v_mul_f32_e32 v193,0x3e0293ee,v193
v_mul_f32_e32 v194,0x3e0293ee,v194
v_mul_f32_e32 v195,0x3e0293ee,v195
v_mul_f32_e32 v196,0x3e0293ee,v196
v_mul_f32_e32 v197,0x3e0293ee,v197
v_mul_f32_e32 v198,0x3e0293ee,v198
v_mul_f32_e32 v199,0x3e0293ee,v199
cv 192,192,193
cv 193,194,195
cv 194,196,197
cv 195,198,199
v_accvgpr_write_b32 a224,v192
v_accvgpr_write_b32 a225,v193
v_accvgpr_write_b32 a226,v194
v_accvgpr_write_b32 a227,v195
v_accvgpr_read_b32 v192,a200
v_accvgpr_read_b32 v193,a201
v_accvgpr_read_b32 v194,a202
v_accvgpr_read_b32 v195,a203
v_accvgpr_read_b32 v196,a204
v_accvgpr_read_b32 v197,a205
v_accvgpr_read_b32 v198,a206
v_accvgpr_read_b32 v199,a207
v_mul_f32_e32 v192,0x3e0293ee,v192
v_mul_f32_e32 v193,0x3e0293ee,v193
v_mul_f32_e32 v194,0x3e0293ee,v194
v_mul_f32_e32 v195,0x3e0293ee,v195
v_mul_f32_e32 v196,0x3e0293ee,v196
v_mul_f32_e32 v197,0x3e0293ee,v197
v_mul_f32_e32 v198,0x3e0293ee,v198
v_mul_f32_e32 v199,0x3e0293ee,v199
cv 192,192,193
cv 193,194,195
cv 194,196,197
cv 195,198,199
v_accvgpr_write_b32 a228,v192
v_accvgpr_write_b32 a229,v193
v_accvgpr_write_b32 a230,v194
v_accvgpr_write_b32 a231,v195
v_accvgpr_read_b32 v192,a208
v_accvgpr_read_b32 v193,a209
v_accvgpr_read_b32 v194,a210
v_accvgpr_read_b32 v195,a211
v_accvgpr_read_b32 v196,a212
v_accvgpr_read_b32 v197,a213
v_accvgpr_read_b32 v198,a214
v_accvgpr_read_b32 v199,a215
v_mul_f32_e32 v192,0x3e0293ee,v192
v_mul_f32_e32 v193,0x3e0293ee,v193
v_mul_f32_e32 v194,0x3e0293ee,v194
v_mul_f32_e32 v195,0x3e0293ee,v195
v_mul_f32_e32 v196,0x3e0293ee,v196
v_mul_f32_e32 v197,0x3e0293ee,v197
v_mul_f32_e32 v198,0x3e0293ee,v198
v_mul_f32_e32 v199,0x3e0293ee,v199
cv 192,192,193
cv 193,194,195
cv 194,196,197
cv 195,198,199
v_accvgpr_write_b32 a232,v192
v_accvgpr_write_b32 a233,v193
v_accvgpr_write_b32 a234,v194
v_accvgpr_write_b32 a235,v195
v_accvgpr_read_b32 v192,a216
v_accvgpr_read_b32 v193,a217
v_accvgpr_read_b32 v194,a218
v_accvgpr_read_b32 v195,a219
v_accvgpr_read_b32 v196,a220
v_accvgpr_read_b32 v197,a221
v_accvgpr_read_b32 v198,a222
v_accvgpr_read_b32 v199,a223
v_mul_f32_e32 v192,0x3e0293ee,v192
v_mul_f32_e32 v193,0x3e0293ee,v193
v_mul_f32_e32 v194,0x3e0293ee,v194
v_mul_f32_e32 v195,0x3e0293ee,v195
v_mul_f32_e32 v196,0x3e0293ee,v196
v_mul_f32_e32 v197,0x3e0293ee,v197
v_mul_f32_e32 v198,0x3e0293ee,v198
v_mul_f32_e32 v199,0x3e0293ee,v199
cv 192,192,193
cv 193,194,195
cv 194,196,197
cv 195,198,199
v_accvgpr_write_b32 a236,v192
v_accvgpr_write_b32 a237,v193
v_accvgpr_write_b32 a238,v194
v_accvgpr_write_b32 a239,v195
v_mov_b32_e32 v160,0
v_mov_b32_e32 v161,0
v_mov_b32_e32 v162,0
v_mov_b32_e32 v163,0
v_mov_b32_e32 v164,0
v_mov_b32_e32 v165,0
v_mov_b32_e32 v166,0
v_mov_b32_e32 v167,0
v_mov_b32_e32 v168,0
v_mov_b32_e32 v169,0
v_mov_b32_e32 v170,0
v_mov_b32_e32 v171,0
v_mov_b32_e32 v172,0
v_mov_b32_e32 v173,0
v_mov_b32_e32 v174,0
v_mov_b32_e32 v175,0
v_mov_b32_e32 v176,0
v_mov_b32_e32 v177,0
v_mov_b32_e32 v178,0
v_mov_b32_e32 v179,0
v_mov_b32_e32 v180,0
v_mov_b32_e32 v181,0
v_mov_b32_e32 v182,0
v_mov_b32_e32 v183,0
v_mov_b32_e32 v184,0
v_mov_b32_e32 v185,0
v_mov_b32_e32 v186,0
v_mov_b32_e32 v187,0
v_mov_b32_e32 v188,0
v_mov_b32_e32 v189,0
v_mov_b32_e32 v190,0
v_mov_b32_e32 v191,0
v_mov_b32_e32 v216,0xf149f2ca
v_mov_b32_e32 v211,v216
v_mov_b32_e32 v212,0
s_lshr_b32 s91,s88,5
s_mov_b32 s87,0
L_rd_a:
s_nop 15
s_nop 3
s_mul_i32 s89,s18,524288
s_lshl_b32 s90,s87,13
s_add_u32 s89,s89,s90
s_add_u32 s90,s89,16777216
la 64,244,90,0
la 68,244,90,1024
la 72,244,90,2048
la 76,244,90,3072
s_add_u32 s90,s89,16781312
la 80,244,90,0
la 84,244,90,1024
la 88,244,90,2048
la 92,244,90,3072
s_add_u32 s90,s89,33554432
la 96,244,90,0
la 100,244,90,1024
la 104,244,90,2048
la 108,244,90,3072
s_add_u32 s90,s89,33558528
la 112,244,90,0
la 116,244,90,1024
la 120,244,90,2048
la 124,244,90,3072
s_waitcnt vmcnt(0)
zvaa 192,64,224
mvaa 192,72,228
mvaa 192,80,232
mvaa 192,88,236
zvaa 196,68,224
mvaa 196,76,228
mvaa 196,84,232
mvaa 196,92,236
v_cmp_gt_i32_e32 vcc,0,v210
s_nop 2
v_cndmask_b32_e32 v192,v192,v216,vcc
v_cmp_gt_i32_e32 vcc,1,v210
s_nop 1
v_cndmask_b32_e32 v193,v193,v216,vcc
v_cmp_gt_i32_e32 vcc,2,v210
s_nop 1
v_cndmask_b32_e32 v194,v194,v216,vcc
v_cmp_gt_i32_e32 vcc,3,v210
s_nop 1
v_cndmask_b32_e32 v195,v195,v216,vcc
v_cmp_gt_i32_e32 vcc,16,v210
s_nop 1
v_cndmask_b32_e32 v196,v196,v216,vcc
v_cmp_gt_i32_e32 vcc,17,v210
s_nop 1
v_cndmask_b32_e32 v197,v197,v216,vcc
v_cmp_gt_i32_e32 vcc,18,v210
s_nop 1
v_cndmask_b32_e32 v198,v198,v216,vcc
v_cmp_gt_i32_e32 vcc,19,v210
s_nop 1
v_cndmask_b32_e32 v199,v199,v216,vcc
v_max3_f32 v213,v192,v193,v194
v_max3_f32 v214,v195,v196,v197
v_max3_f32 v213,v213,v198,v199
v_max_f32_e32 v213,v213,v214
v_mov_b32_e32 v214,v213
s_nop 1
v_permlane16_swap_b32_e32 v213,v214
v_max_f32_e32 v213,v213,v214
v_mov_b32_e32 v214,v213
s_nop 1
v_permlane32_swap_b32_e32 v213,v214
v_max_f32_e32 v213,v213,v214
v_max_f32_e32 v213,v213,v211
v_sub_f32_e32 v215,v211,v213
ex 215
v_mov_b32_e32 v211,v213
v_sub_f32_e32 v192,v192,v211
v_sub_f32_e32 v193,v193,v211
v_sub_f32_e32 v194,v194,v211
v_sub_f32_e32 v195,v195,v211
v_sub_f32_e32 v196,v196,v211
v_sub_f32_e32 v197,v197,v211
v_sub_f32_e32 v198,v198,v211
v_sub_f32_e32 v199,v199,v211
ex 192
ex 193
ex 194
ex 195
ex 196
ex 197
ex 198
ex 199
v_mul_f32_e32 v212,v215,v212
v_add_f32_e32 v212,v212,v192
v_add_f32_e32 v212,v212,v193
v_add_f32_e32 v212,v212,v194
v_add_f32_e32 v212,v212,v195
v_add_f32_e32 v212,v212,v196
v_add_f32_e32 v212,v212,v197
v_add_f32_e32 v212,v212,v198
v_add_f32_e32 v212,v212,v199
cv 192,192,193
cv 193,194,195
cv 194,196,197
cv 195,198,199
v_mul_f32_e32 v160,v215,v160
v_mul_f32_e32 v161,v215,v161
v_mul_f32_e32 v162,v215,v162
)"
R"(v_mul_f32_e32 v163,v215,v163
v_mul_f32_e32 v164,v215,v164
v_mul_f32_e32 v165,v215,v165
v_mul_f32_e32 v166,v215,v166
v_mul_f32_e32 v167,v215,v167
v_mul_f32_e32 v168,v215,v168
v_mul_f32_e32 v169,v215,v169
v_mul_f32_e32 v170,v215,v170
v_mul_f32_e32 v171,v215,v171
v_mul_f32_e32 v172,v215,v172
v_mul_f32_e32 v173,v215,v173
v_mul_f32_e32 v174,v215,v174
v_mul_f32_e32 v175,v215,v175
v_mul_f32_e32 v176,v215,v176
v_mul_f32_e32 v177,v215,v177
v_mul_f32_e32 v178,v215,v178
v_mul_f32_e32 v179,v215,v179
v_mul_f32_e32 v180,v215,v180
v_mul_f32_e32 v181,v215,v181
v_mul_f32_e32 v182,v215,v182
v_mul_f32_e32 v183,v215,v183
v_mul_f32_e32 v184,v215,v184
v_mul_f32_e32 v185,v215,v185
v_mul_f32_e32 v186,v215,v186
v_mul_f32_e32 v187,v215,v187
v_mul_f32_e32 v188,v215,v188
v_mul_f32_e32 v189,v215,v189
v_mul_f32_e32 v190,v215,v190
v_mul_f32_e32 v191,v215,v191
mvav 160,96,192
mvav 164,100,192
mvav 168,104,192
mvav 172,108,192
mvav 176,112,192
mvav 180,116,192
mvav 184,120,192
mvav 188,124,192
v_subrev_u32_e32 v210,32,v210
s_add_u32 s87,s87,1
s_cmp_le_u32 s87,s91
s_cbranch_scc1 L_rd_a
s_nop 15
s_nop 3
s_lshl_b32 s89,s88,2
s_add_u32 s89,s89,83886080
v_bfe_u32 v213,v244,2,6
buffer_load_dword v215,v213,s[8:11],s89 offen sc1
v_mov_b32_e32 v214,v212
s_nop 1
v_permlane16_swap_b32_e32 v212,v214
v_add_f32_e32 v212,v212,v214
v_mov_b32_e32 v214,v212
s_nop 1
v_permlane32_swap_b32_e32 v212,v214
v_add_f32_e32 v212,v212,v214
v_rcp_f32_e32 v212,v212
s_waitcnt vmcnt(0)
v_mul_f32_e32 v215,v215,v212
v_mul_f32_e32 v160,v215,v160
v_mul_f32_e32 v161,v215,v161
v_mul_f32_e32 v162,v215,v162
v_mul_f32_e32 v163,v215,v163
v_mul_f32_e32 v164,v215,v164
v_mul_f32_e32 v165,v215,v165
v_mul_f32_e32 v166,v215,v166
v_mul_f32_e32 v167,v215,v167
v_mul_f32_e32 v168,v215,v168
v_mul_f32_e32 v169,v215,v169
v_mul_f32_e32 v170,v215,v170
v_mul_f32_e32 v171,v215,v171
v_mul_f32_e32 v172,v215,v172
v_mul_f32_e32 v173,v215,v173
v_mul_f32_e32 v174,v215,v174
v_mul_f32_e32 v175,v215,v175
v_mul_f32_e32 v176,v215,v176
v_mul_f32_e32 v177,v215,v177
v_mul_f32_e32 v178,v215,v178
v_mul_f32_e32 v179,v215,v179
v_mul_f32_e32 v180,v215,v180
v_mul_f32_e32 v181,v215,v181
v_mul_f32_e32 v182,v215,v182
v_mul_f32_e32 v183,v215,v183
v_mul_f32_e32 v184,v215,v184
v_mul_f32_e32 v185,v215,v185
v_mul_f32_e32 v186,v215,v186
v_mul_f32_e32 v187,v215,v187
v_mul_f32_e32 v188,v215,v188
v_mul_f32_e32 v189,v215,v189
v_mul_f32_e32 v190,v215,v190
v_mul_f32_e32 v191,v215,v191
s_add_u32 s89,s95,4096
cv 160,160,161
cv 161,162,163
cv 162,164,165
cv 163,166,167
buffer_store_dwordx4 v[160:163],v244,s[8:11],s95 offen offset:0 sc0 sc1
cv 168,168,169
cv 169,170,171
cv 170,172,173
cv 171,174,175
buffer_store_dwordx4 v[168:171],v244,s[8:11],s95 offen offset:2048 sc0 sc1
cv 176,176,177
cv 177,178,179
cv 178,180,181
cv 179,182,183
buffer_store_dwordx4 v[176:179],v244,s[8:11],s89 offen offset:0 sc0 sc1
cv 184,184,185
cv 185,186,187
cv 186,188,189
cv 187,190,191
buffer_store_dwordx4 v[184:187],v244,s[8:11],s89 offen offset:2048 sc0 sc1
s_waitcnt vmcnt(0)
v_mov_b32_e32 v192,v160
v_accvgpr_read_b32 v160,a240
v_accvgpr_write_b32 a240,v192
v_mov_b32_e32 v193,v161
v_accvgpr_read_b32 v161,a241
v_accvgpr_write_b32 a241,v193
v_mov_b32_e32 v194,v162
v_accvgpr_read_b32 v162,a242
v_accvgpr_write_b32 a242,v194
v_mov_b32_e32 v195,v163
v_accvgpr_read_b32 v163,a243
v_accvgpr_write_b32 a243,v195
v_mov_b32_e32 v200,v168
v_accvgpr_read_b32 v168,a244
v_accvgpr_write_b32 a244,v200
v_mov_b32_e32 v201,v169
v_accvgpr_read_b32 v169,a245
v_accvgpr_write_b32 a245,v201
v_mov_b32_e32 v202,v170
v_accvgpr_read_b32 v170,a246
v_accvgpr_write_b32 a246,v202
v_mov_b32_e32 v203,v171
v_accvgpr_read_b32 v171,a247
v_accvgpr_write_b32 a247,v203
v_mov_b32_e32 v208,v176
v_accvgpr_read_b32 v176,a248
v_accvgpr_write_b32 a248,v208
v_mov_b32_e32 v209,v177
v_accvgpr_read_b32 v177,a249
v_accvgpr_write_b32 a249,v209
v_mov_b32_e32 v210,v178
v_accvgpr_read_b32 v178,a250
v_accvgpr_write_b32 a250,v210
v_mov_b32_e32 v211,v179
v_accvgpr_read_b32 v179,a251
v_accvgpr_write_b32 a251,v211
v_mov_b32_e32 v216,v184
v_accvgpr_read_b32 v184,a252
v_accvgpr_write_b32 a252,v216
v_mov_b32_e32 v217,v185
v_accvgpr_read_b32 v185,a253
v_accvgpr_write_b32 a253,v217
v_mov_b32_e32 v218,v186
v_accvgpr_read_b32 v186,a254
v_accvgpr_write_b32 a254,v218
v_mov_b32_e32 v219,v187
v_accvgpr_read_b32 v187,a255
v_accvgpr_write_b32 a255,v219
s_add_u32 s86,s86,1
s_cmp_lt_u32 s86,4
s_cbranch_scc1 L_rd_t
s_nop 15
s_nop 3
s_nop 1
s_cmp_eq_u32 s84,4
s_cbranch_scc1 L_redo_r4
s_cmp_lg_u32 s80,s81
s_branch L_redo_r5
L_spbe:
s_mov_b32 s91,65536
L_splbe:
s_sleep 2
buffer_load_dword v253,v224,s[8:11],s53 offen sc1
s_waitcnt vmcnt(0)
v_cmp_ne_u32_e32 vcc,4,v253
s_nop 1
s_cbranch_vccz L_sprbe
s_sub_u32 s91,s91,1
s_cbranch_scc0 L_splbe
s_branch L_sprbe
L_spa3e:
s_mov_b32 s91,65536
L_spla3e:
s_sleep 2
buffer_load_dword v253,v242,s[8:11],s53 offen sc1
s_waitcnt vmcnt(0)
v_cmp_ne_u32_e32 vcc,4,v253
s_nop 1
s_cbranch_vccz L_spra3e
s_sub_u32 s91,s91,1
s_cbranch_scc0 L_spla3e
s_branch L_spra3e
L_spa4e:
s_mov_b32 s91,65536
L_spla4e:
s_sleep 2
buffer_load_dword v253,v242,s[8:11],s53 offen sc1
s_waitcnt vmcnt(0)
v_cmp_ne_u32_e32 vcc,4,v253
s_nop 1
s_cbranch_vccz L_spra4e
s_sub_u32 s91,s91,1
s_cbranch_scc0 L_spla4e
s_branch L_spra4e
L_spa5e:
s_mov_b32 s91,65536
L_spla5e:
s_sleep 2
buffer_load_dword v253,v242,s[8:11],s53 offen sc1
s_waitcnt vmcnt(0)
v_cmp_ne_u32_e32 vcc,4,v253
s_nop 1
s_cbranch_vccz L_spra5e
s_sub_u32 s91,s91,1
s_cbranch_scc0 L_spla5e
s_branch L_spra5e
L_spa6e:
s_mov_b32 s91,65536
L_spla6e:
s_sleep 2
buffer_load_dword v253,v242,s[8:11],s53 offen sc1
s_waitcnt vmcnt(0)
v_cmp_ne_u32_e32 vcc,4,v253
s_nop 1
s_cbranch_vccz L_spra6e
s_sub_u32 s91,s91,1
s_cbranch_scc0 L_spla6e
s_branch L_spra6e
L_spa7e:
s_mov_b32 s91,65536
L_spla7e:
s_sleep 2
buffer_load_dword v253,v242,s[8:11],s53 offen sc1
s_waitcnt vmcnt(0)
v_cmp_ne_u32_e32 vcc,4,v253
s_nop 1
s_cbranch_vccz L_spra7e
s_sub_u32 s91,s91,1
s_cbranch_scc0 L_spla7e
s_branch L_spra7e
L_spa8e:
s_mov_b32 s91,65536
L_spla8e:
s_sleep 2
buffer_load_dword v253,v242,s[8:11],s53 offen sc1
s_waitcnt vmcnt(0)
v_cmp_ne_u32_e32 vcc,4,v253
s_nop 1
s_cbranch_vccz L_spra8e
s_sub_u32 s91,s91,1
s_cbranch_scc0 L_spla8e
s_branch L_spra8e
L_spa9e:
s_mov_b32 s91,65536
L_spla9e:
s_sleep 2
buffer_load_dword v253,v242,s[8:11],s53 offen sc1
s_waitcnt vmcnt(0)
v_cmp_ne_u32_e32 vcc,4,v253
s_nop 1
s_cbranch_vccz L_spra9e
s_sub_u32 s91,s91,1
s_cbranch_scc0 L_spla9e
s_branch L_spra9e
L_spa10e:
s_mov_b32 s91,65536
L_spla10e:
s_sleep 2
buffer_load_dword v253,v242,s[8:11],s53 offen sc1
s_waitcnt vmcnt(0)
v_cmp_ne_u32_e32 vcc,4,v253
s_nop 1
s_cbranch_vccz L_spra10e
s_sub_u32 s91,s91,1
s_cbranch_scc0 L_spla10e
s_branch L_spra10e
L_sppe:
s_mov_b32 s91,65536
L_splpe:
s_sleep 2
buffer_load_dword v253,v242,s[8:11],s53 offen sc1
s_waitcnt vmcnt(0)
v_cmp_ne_u32_e32 vcc,4,v253
s_nop 1
s_cbranch_vccz L_sprpe
s_sub_u32 s91,s91,1
s_cbranch_scc0 L_splpe
s_branch L_sprpe
L_spx0e:
s_mov_b32 s91,65536
L_splx0e:
lv 16,244,82,0
lv 20,244,82,1024
lv 24,244,82,2048
lv 28,244,82,3072
s_waitcnt vmcnt(0)
v_max_u32_e32 v248,v16,v17
v_max3_u32 v248,v248,v18,v19
v_max3_u32 v248,v248,v20,v21
v_max3_u32 v248,v248,v22,v23
v_max3_u32 v248,v248,v24,v25
v_max3_u32 v248,v248,v26,v27
v_max3_u32 v248,v248,v28,v29
v_max3_u32 v248,v248,v30,v31
v_cmp_eq_u32_e32 vcc,-1,v248
s_nop 1
s_cbranch_vccz L_sprx0e
s_sub_u32 s91,s91,1
s_cbranch_scc0 L_splx0e
s_branch L_sprx0e
L_spx1e:
s_mov_b32 s91,65536
L_splx1e:
lv 48,244,83,0
lv 52,244,83,1024
lv 56,244,83,2048
lv 60,244,83,3072
s_waitcnt vmcnt(0)
v_max_u32_e32 v248,v48,v49
v_max3_u32 v248,v248,v50,v51
v_max3_u32 v248,v248,v52,v53
v_max3_u32 v248,v248,v54,v55
v_max3_u32 v248,v248,v56,v57
v_max3_u32 v248,v248,v58,v59
v_max3_u32 v248,v248,v60,v61
v_max3_u32 v248,v248,v62,v63
v_cmp_eq_u32_e32 vcc,-1,v248
s_nop 1
s_cbranch_vccz L_sprx1e
s_sub_u32 s91,s91,1
s_cbranch_scc0 L_splx1e
s_branch L_sprx1e
L_spx2e:
s_mov_b32 s91,65536
L_splx2e:
lv 80,244,84,0
lv 84,244,84,1024
lv 88,244,84,2048
lv 92,244,84,3072
s_waitcnt vmcnt(0)
v_max_u32_e32 v248,v80,v81
v_max3_u32 v248,v248,v82,v83
v_max3_u32 v248,v248,v84,v85
v_max3_u32 v248,v248,v86,v87
v_max3_u32 v248,v248,v88,v89
v_max3_u32 v248,v248,v90,v91
v_max3_u32 v248,v248,v92,v93
v_max3_u32 v248,v248,v94,v95
v_cmp_eq_u32_e32 vcc,-1,v248
s_nop 1
s_cbranch_vccz L_sprx2e
s_sub_u32 s91,s91,1
s_cbranch_scc0 L_splx2e
s_branch L_sprx2e
L_spbo:
s_mov_b32 s91,65536
L_splbo:
s_sleep 2
buffer_load_dword v253,v224,s[8:11],s53 offen sc1
s_waitcnt vmcnt(0)
v_cmp_ne_u32_e32 vcc,4,v253
s_nop 1
s_cbranch_vccz L_sprbo
s_sub_u32 s91,s91,1
s_cbranch_scc0 L_splbo
s_branch L_sprbo
L_spa3o:
s_mov_b32 s91,65536
L_spla3o:
s_sleep 2
buffer_load_dword v253,v242,s[8:11],s53 offen sc1
s_waitcnt vmcnt(0)
v_cmp_ne_u32_e32 vcc,4,v253
s_nop 1
s_cbranch_vccz L_spra3o
s_sub_u32 s91,s91,1
s_cbranch_scc0 L_spla3o
s_branch L_spra3o
L_spa4o:
s_mov_b32 s91,65536
L_spla4o:
s_sleep 2
buffer_load_dword v253,v242,s[8:11],s53 offen sc1
s_waitcnt vmcnt(0)
v_cmp_ne_u32_e32 vcc,4,v253
s_nop 1
s_cbranch_vccz L_spra4o
s_sub_u32 s91,s91,1
s_cbranch_scc0 L_spla4o
s_branch L_spra4o
L_spa5o:
s_mov_b32 s91,65536
L_spla5o:
s_sleep 2
buffer_load_dword v253,v242,s[8:11],s53 offen sc1
s_waitcnt vmcnt(0)
v_cmp_ne_u32_e32 vcc,4,v253
s_nop 1
s_cbranch_vccz L_spra5o
s_sub_u32 s91,s91,1
s_cbranch_scc0 L_spla5o
s_branch L_spra5o
L_spa6o:
s_mov_b32 s91,65536
L_spla6o:
s_sleep 2
buffer_load_dword v253,v242,s[8:11],s53 offen sc1
s_waitcnt vmcnt(0)
v_cmp_ne_u32_e32 vcc,4,v253
s_nop 1
s_cbranch_vccz L_spra6o
s_sub_u32 s91,s91,1
s_cbranch_scc0 L_spla6o
s_branch L_spra6o
L_spa7o:
s_mov_b32 s91,65536
L_spla7o:
s_sleep 2
buffer_load_dword v253,v242,s[8:11],s53 offen sc1
s_waitcnt vmcnt(0)
v_cmp_ne_u32_e32 vcc,4,v253
s_nop 1
s_cbranch_vccz L_spra7o
s_sub_u32 s91,s91,1
s_cbranch_scc0 L_spla7o
s_branch L_spra7o
L_spa8o:
s_mov_b32 s91,65536
L_spla8o:
s_sleep 2
buffer_load_dword v253,v242,s[8:11],s53 offen sc1
s_waitcnt vmcnt(0)
v_cmp_ne_u32_e32 vcc,4,v253
s_nop 1
s_cbranch_vccz L_spra8o
s_sub_u32 s91,s91,1
s_cbranch_scc0 L_spla8o
s_branch L_spra8o
L_sppo:
s_mov_b32 s91,65536
L_splpo:
s_sleep 2
buffer_load_dword v253,v242,s[8:11],s53 offen sc1
s_waitcnt vmcnt(0)
v_cmp_ne_u32_e32 vcc,4,v253
s_nop 1
s_cbranch_vccz L_sprpo
s_sub_u32 s91,s91,1
s_cbranch_scc0 L_splpo
s_branch L_sprpo
L_spx0o:
s_mov_b32 s91,65536
L_splx0o:
lv 16,244,82,0
lv 20,244,82,1024
lv 24,244,82,2048
lv 28,244,82,3072
s_waitcnt vmcnt(0)
v_max_u32_e32 v248,v16,v17
v_max3_u32 v248,v248,v18,v19
v_max3_u32 v248,v248,v20,v21
v_max3_u32 v248,v248,v22,v23
v_max3_u32 v248,v248,v24,v25
v_max3_u32 v248,v248,v26,v27
v_max3_u32 v248,v248,v28,v29
v_max3_u32 v248,v248,v30,v31
v_cmp_eq_u32_e32 vcc,-1,v248
s_nop 1
s_cbranch_vccz L_sprx0o
s_sub_u32 s91,s91,1
s_cbranch_scc0 L_splx0o
s_branch L_sprx0o
L_spx1o:
s_mov_b32 s91,65536
L_splx1o:
lv 48,244,83,0
lv 52,244,83,1024
lv 56,244,83,2048
lv 60,244,83,3072
s_waitcnt vmcnt(0)
v_max_u32_e32 v248,v48,v49
v_max3_u32 v248,v248,v50,v51
v_max3_u32 v248,v248,v52,v53
v_max3_u32 v248,v248,v54,v55
v_max3_u32 v248,v248,v56,v57
v_max3_u32 v248,v248,v58,v59
v_max3_u32 v248,v248,v60,v61
v_max3_u32 v248,v248,v62,v63
v_cmp_eq_u32_e32 vcc,-1,v248
s_nop 1
s_cbranch_vccz L_sprx1o
s_sub_u32 s91,s91,1
s_cbranch_scc0 L_splx1o
s_branch L_sprx1o
L_spx2o:
s_mov_b32 s91,65536
L_splx2o:
lv 80,244,84,0
lv 84,244,84,1024
lv 88,244,84,2048
lv 92,244,84,3072
s_waitcnt vmcnt(0)
v_max_u32_e32 v248,v80,v81
v_max3_u32 v248,v248,v82,v83
v_max3_u32 v248,v248,v84,v85
v_max3_u32 v248,v248,v86,v87
v_max3_u32 v248,v248,v88,v89
v_max3_u32 v248,v248,v90,v91
v_max3_u32 v248,v248,v92,v93
v_max3_u32 v248,v248,v94,v95
v_cmp_eq_u32_e32 vcc,-1,v248
s_nop 1
s_cbranch_vccz L_sprx2o
s_sub_u32 s91,s91,1
s_cbranch_scc0 L_splx2o
s_branch L_sprx2o
L_att_end:
s_endpgm
.p2align 12
.purgem cv
.purgem cvaa
.purgem cvvv
.purgem dl
.purgem ds
.purgem ex
.purgem la
.purgem lv
.purgem maaa
.purgem maav
.purgem mavv
.purgem mvaa
.purgem mvav
.purgem mvva
.purgem mvvv
.purgem ra
.purgem rv
.purgem ta
.purgem tv
.purgem zaaa
.purgem zaav
.purgem zvaa
.purgem zvav
.purgem zvva
.purgem zvvv
)"
        :: "s"(__builtin_amdgcn_kernarg_segment_ptr()), "s"(blockIdx.x), "v"(tid)
        : "v0", "v1", "v2", "v3", "v4", "v5", "v6", "v7", "v8", "v9", "v10", "v11", "v12", "v13", "v14", "v15", "v16", "v17", "v18", "v19", "v20", "v21", "v22", "v23", "v24", "v25", "v26", "v27", "v28", "v29", "v30", "v31", "v32", "v33", "v34", "v35", "v36", "v37", "v38", "v39", "v40", "v41", "v42", "v43", "v44", "v45", "v46", "v47", "v48", "v49", "v50", "v51", "v52", "v53", "v54", "v55", "v56", "v57", "v58", "v59", "v60", "v61", "v62", "v63", "v64", "v65", "v66", "v67", "v68", "v69", "v70", "v71", "v72", "v73", "v74", "v75", "v76", "v77", "v78", "v79", "v80", "v81", "v82", "v83", "v84", "v85", "v86", "v87", "v88", "v89", "v90", "v91", "v92", "v93", "v94", "v95", "v96", "v97", "v98", "v99", "v100", "v101", "v102", "v103", "v104", "v105", "v106", "v107", "v108", "v109", "v110", "v111", "v112", "v113", "v114", "v115", "v116", "v117", "v118", "v119", "v120", "v121", "v122", "v123", "v124", "v125", "v126", "v127", "v128", "v129", "v130", "v131", "v132", "v133", "v134", "v135", "v136", "v137", "v138", "v139", "v140", "v141", "v142", "v143", "v144", "v145", "v146", "v147", "v148", "v149", "v150", "v151", "v152", "v153", "v154", "v155", "v156", "v157", "v158", "v159", "v160", "v161", "v162", "v163", "v164", "v165", "v166", "v167", "v168", "v169", "v170", "v171", "v172", "v173", "v174", "v175", "v176", "v177", "v178", "v179", "v180", "v181", "v182", "v183", "v184", "v185", "v186", "v187", "v188", "v189", "v190", "v191", "v192", "v193", "v194", "v195", "v196", "v197", "v198", "v199", "v200", "v201", "v202", "v203", "v204", "v205", "v206", "v207", "v208", "v209", "v210", "v211", "v212", "v213", "v214", "v215", "v216", "v217", "v218", "v219", "v220", "v221", "v222", "v223", "v224", "v225", "v226", "v227", "v228", "v229", "v230", "v231", "v232", "v233", "v234", "v235", "v236", "v237", "v238", "v239", "v240", "v241", "v242", "v243", "v244", "v245", "v246", "v247", "v248", "v249", "v250", "v251", "v252", "v253", "v254", "a0", "a1", "a2", "a3", "a4", "a5", "a6", "a7", "a8", "a9", "a10", "a11", "a12", "a13", "a14", "a15", "a16", "a17", "a18", "a19", "a20", "a21", "a22", "a23", "a24", "a25", "a26", "a27", "a28", "a29", "a30", "a31", "a32", "a33", "a34", "a35", "a36", "a37", "a38", "a39", "a40", "a41", "a42", "a43", "a44", "a45", "a46", "a47", "a48", "a49", "a50", "a51", "a52", "a53", "a54", "a55", "a56", "a57", "a58", "a59", "a60", "a61", "a62", "a63", "a64", "a65", "a66", "a67", "a68", "a69", "a70", "a71", "a72", "a73", "a74", "a75", "a76", "a77", "a78", "a79", "a80", "a81", "a82", "a83", "a84", "a85", "a86", "a87", "a88", "a89", "a90", "a91", "a92", "a93", "a94", "a95", "a96", "a97", "a98", "a99", "a100", "a101", "a102", "a103", "a104", "a105", "a106", "a107", "a108", "a109", "a110", "a111", "a112", "a113", "a114", "a115", "a116", "a117", "a118", "a119", "a120", "a121", "a122", "a123", "a124", "a125", "a126", "a127", "a128", "a129", "a130", "a131", "a132", "a133", "a134", "a135", "a136", "a137", "a138", "a139", "a140", "a141", "a142", "a143", "a144", "a145", "a146", "a147", "a148", "a149", "a150", "a151", "a152", "a153", "a154", "a155", "a156", "a157", "a158", "a159", "a160", "a161", "a162", "a163", "a164", "a165", "a166", "a167", "a168", "a169", "a170", "a171", "a172", "a173", "a174", "a175", "a176", "a177", "a178", "a179", "a180", "a181", "a182", "a183", "a184", "a185", "a186", "a187", "a188", "a189", "a190", "a191", "a192", "a193", "a194", "a195", "a196", "a197", "a198", "a199", "a200", "a201", "a202", "a203", "a204", "a205", "a206", "a207", "a208", "a209", "a210", "a211", "a212", "a213", "a214", "a215", "a216", "a217", "a218", "a219", "a220", "a221", "a222", "a223", "a224", "a225", "a226", "a227", "a228", "a229", "a230", "a231", "a232", "a233", "a234", "a235", "a236", "a237", "a238", "a239", "a240", "a241", "a242", "a243", "a244", "a245", "a246", "a247", "a248", "a249", "a250", "a251", "a252", "a253", "a254", "a255", "s4", "s5", "s6", "s7", "s8", "s9", "s10", "s11", "s12", "s13", "s14", "s15", "s16", "s17", "s18", "s19", "s20", "s21", "s22", "s23", "s24", "s25", "s26", "s27", "s28", "s29", "s30", "s31", "s32", "s33", "s34", "s35", "s36", "s37", "s38", "s39", "s40", "s41", "s42", "s43", "s44", "s45", "s46", "s47", "s48", "s49", "s50", "s51", "s52", "s53", "s54", "s55", "s56", "s57", "s58", "s59", "s60", "s61", "s62", "s63", "s64", "s65", "s66", "s67", "s68", "s69", "s70", "s71", "s72", "s73", "s74", "s75", "s76", "s77", "s78", "s79", "s80", "s81", "s82", "s83", "s84", "s85", "s86", "s87", "s88", "s89", "s90", "s91", "s92", "s93", "s94", "s95", "s96", "s97", "s98", "s99", "s100", "s101", "vcc", "scc", "m0", "memory");
}

extern "C" void kernel_launch(void* const* d_in, const int* in_sizes, int n_in, void* d_out, int out_size, void* d_ws, size_t ws_size, hipStream_t stream) {
    (void)in_sizes; (void)n_in; (void)out_size; (void)ws_size;
    char* ws = (char*)d_ws + 0ull;
    Args a;
    a.p[0] = d_in[0]; a.p[1] = d_in[1]; a.p[2] = d_in[2]; a.p[3] = d_in[3]; a.p[4] = d_in[4]; a.p[5] = d_in[5]; a.p[6] = d_in[7]; a.p[7] = d_in[8]; a.p[8] = d_in[9]; a.p[9] = d_out; a.p[10] = ws;
    mega<<<256, 256, 0, stream>>>(a);
}
```

```cpp
#include <hip/hip_runtime.h>
#include <hip/hip_bf16.h>
#include <stdint.h>
#include <type_traits>

#define LAS __attribute__((address_space(3)))
struct Args { const void* p[11]; };
__global__ __launch_bounds__(256, 1)
void mega(Args args) {
    __shared__ __attribute__((aligned(1024))) char smem[163840];
    const unsigned tid = threadIdx.x + (unsigned)(uintptr_t)(LAS char*)smem;
    asm volatile(
R"(.macro cv a,b,c
v_cvt_pk_bf16_f32 v\a,v\b,v\c
.endm
.macro cvaa a,b,c,d
v_mfma_f32_16x16x32_bf16 v[\a:\a+3],a[\b:\b+3],a[\c:\c+3],v[\d:\d+3]
.endm
.macro cvvv a,b,c,d
v_mfma_f32_16x16x32_bf16 v[\a:\a+3],v[\b:\b+3],v[\c:\c+3],v[\d:\d+3]
.endm
.macro dl a,b,c,d
buffer_load_dwordx4 v\a,s[\b:\b+3],s\c offen offset:\d lds
.endm
.macro ds a,b,c,d
buffer_load_dwordx4 v\a,s[\b:\b+3],s\c offen offset:\d sc1 lds
.endm
.macro ex a
v_exp_f32_e32 v\a,v\a
.endm
.macro la a,b,c,d
buffer_load_dwordx4 a[\a:\a+3],v\b,s[8:11],s\c offen offset:\d sc1
.endm
.macro lv a,b,c,d
buffer_load_dwordx4 v[\a:\a+3],v\b,s[8:11],s\c offen offset:\d sc1
.endm
.macro maaa a,b,c
v_mfma_f32_16x16x32_bf16 a[\a:\a+3],a[\b:\b+3],a[\c:\c+3],a[\a:\a+3]
.endm
.macro maav a,b,c
v_mfma_f32_16x16x32_bf16 a[\a:\a+3],a[\b:\b+3],v[\c:\c+3],a[\a:\a+3]
.endm
.macro mavv a,b,c
v_mfma_f32_16x16x32_bf16 a[\a:\a+3],v[\b:\b+3],v[\c:\c+3],a[\a:\a+3]
.endm
.macro mvaa a,b,c
v_mfma_f32_16x16x32_bf16 v[\a:\a+3],a[\b:\b+3],a[\c:\c+3],v[\a:\a+3]
.endm
.macro mvav a,b,c
v_mfma_f32_16x16x32_bf16 v[\a:\a+3],a[\b:\b+3],v[\c:\c+3],v[\a:\a+3]
.endm
.macro mvva a,b,c
v_mfma_f32_16x16x32_bf16 v[\a:\a+3],v[\b:\b+3],a[\c:\c+3],v[\a:\a+3]
.endm
.macro mvvv a,b,c
v_mfma_f32_16x16x32_bf16 v[\a:\a+3],v[\b:\b+3],v[\c:\c+3],v[\a:\a+3]
.endm
.macro ra a,b,c
ds_read_b128 a[\a:\a+3],v\b offset:\c
.endm
.macro rv a,b,c
ds_read_b128 v[\a:\a+3],v\b offset:\c
.endm
.macro ta a,b,c
ds_read_b64_tr_b16 a[\a:\a+1],v\b offset:\c
.endm
.macro tv a,b,c
ds_read_b64_tr_b16 v[\a:\a+1],v\b offset:\c
.endm
.macro zaaa a,b,c
v_mfma_f32_16x16x32_bf16 a[\a:\a+3],a[\b:\b+3],a[\c:\c+3],0
.endm
.macro zaav a,b,c
v_mfma_f32_16x16x32_bf16 a[\a:\a+3],a[\b:\b+3],v[\c:\c+3],0
.endm
.macro zvaa a,b,c
v_mfma_f32_16x16x32_bf16 v[\a:\a+3],a[\b:\b+3],a[\c:\c+3],0
.endm
.macro zvav a,b,c
v_mfma_f32_16x16x32_bf16 v[\a:\a+3],a[\b:\b+3],v[\c:\c+3],0
.endm
.macro zvva a,b,c
v_mfma_f32_16x16x32_bf16 v[\a:\a+3],v[\b:\b+3],a[\c:\c+3],0
.endm
.macro zvvv a,b,c
v_mfma_f32_16x16x32_bf16 v[\a:\a+3],v[\b:\b+3],v[\c:\c+3],0
.endm
L_begin:
s_load_dwordx2 s[28:29],%0,72
s_load_dwordx16 s[68:83],%0,0
s_load_dwordx2 s[84:85],%0,64
s_load_dwordx2 s[8:9],%0,80
v_lshrrev_b32_e32 v246,6,%2
v_and_b32_e32 v247,63,%2
v_lshlrev_b32_e32 v244,4,v247
v_readfirstlane_b32 s12,v246
s_and_b32 s20,%1,7
s_lshr_b32 s21,%1,3
s_getpc_b64 s[96:97]
L_att_pc:
s_lshl_b32 s86,s21,2
s_add_u32 s86,s86,s12
s_mul_i32 s86,s86,256
v_lshl_add_u32 v246,v247,7,s86
v_min_u32_e32 v246,L_att_end-L_att_pc-4,v246
s_mov_b64 exec,3
global_load_dword a188,v246,s[96:97]
s_mov_b64 exec,-1
s_and_b32 s19,s21,7
s_lshr_b32 s86,s21,3
s_lshl_b32 s18,s20,2
s_add_u32 s18,s18,s86
v_lshrrev_b32_e32 v248,3,v247
v_and_b32_e32 v250,112,v244
v_and_b32_e32 v249,48,v247
v_xor_b32_e32 v249,v249,v250
v_and_b32_e32 v251,16,v247
v_lshlrev_b32_e32 v251,1,v251
v_xor_b32_e32 v250,v250,v251
v_lshl_add_u32 v224,v248,10,v249
v_xor_b32_e32 v225,64,v224
v_add_u32_e32 v227,21504,v225
v_add_u32_e32 v226,14336,v224
v_add_u32_e32 v225,7168,v225
v_lshl_add_u32 v228,v248,13,v250
v_xor_b32_e32 v229,64,v228
v_add_u32_e32 v231,193536,v229
v_add_u32_e32 v230,129024,v228
v_add_u32_e32 v229,64512,v229
s_lshl_b32 s86,s19,2
s_add_u32 s86,s86,s12
s_lshl_b32 s52,s86,16
s_bfe_u32 s87,s21,65539
s_lshl_b32 s88,s20,1
s_add_u32 s87,s87,s88
s_lshl_b32 s87,s87,2
s_add_u32 s87,s87,s12
s_lshl_b32 s53,s87,7
s_mov_b32 s54,s53
s_mov_b32 s39,131072
s_mov_b32 s43,131072
s_mov_b32 s47,131072
s_mov_b32 s51,131072
s_mov_b32 s38,2097152
s_mov_b32 s42,4194304
s_mov_b32 s46,4194304
s_mov_b32 s56,32768
s_mov_b32 s60,-32640
s_mov_b32 s57,262144
s_mov_b32 s58,262144
s_bfe_u32 s91,s21,65536
s_lshl_b32 s91,s91,18
s_add_u32 s53,s53,s91
s_mov_b32 s54,s53
s_lshl_b32 s91,s91,1
s_sub_u32 s57,s57,s91
s_add_u32 s58,s58,s91
s_lshl_b32 s98,s12,13
s_lshl_b32 s62,s12,12
s_add_u32 s62,s62,98304
s_lshl_b32 s35,%1,2
s_add_u32 s35,s35,83896320
v_lshrrev_b32_e32 v48,2,v247
v_and_b32_e32 v49,3,v247
v_lshlrev_b32_e32 v49,7,v49
v_lshl_add_u32 v48,v48,13,v49
s_lshl_b32 s89,s86,14
s_and_b32 s90,s53,7680
s_add_u32 s90,s90,s89
s_lshr_b32 s89,s21,3
s_lshl_b32 s89,s89,2
s_add_u32 s89,s89,s12
s_lshl_b32 s89,s89,14
s_lshl_b32 s91,s19,18
s_add_u32 s89,s89,s91
s_cmp_ge_u32 s21,16
s_waitcnt lgkmcnt(0)
s_mov_b64 s[36:37],s[70:71]
s_cselect_b64 s[40:41],s[76:77],s[74:75]
s_mov_b64 s[44:45],s[40:41]
s_bfm_b64 exec,8,0
buffer_load_dword a189,v48,s[40:43],s90 offen
s_mov_b64 exec,-1
s_and_b32 s9,s9,65535
s_mov_b32 s10,83951616
s_mov_b32 s11,131072
buffer_load_dword v44,off,s[8:11],s35 sc1
buffer_load_dword v45,off,s[8:11],s35 offset:1024 sc1
s_add_u32 m0,s62,0
s_nop 0
buffer_load_dwordx4 v228,s[40:43],s53 offen offset:0 nt lds
buffer_load_dwordx4 v229,s[40:43],s53 offen offset:1024 nt lds
s_add_u32 s53,s53,s57
buffer_load_dwordx4 v230,s[44:47],s54 offen offset:2048 nt lds
buffer_load_dwordx4 v231,s[44:47],s54 offen offset:3072 nt lds
s_add_u32 s54,s54,s57
s_add_u32 m0,s98,0
s_nop 0
dl 224,36,52,0
dl 225,36,52,1024
dl 226,36,52,2048
dl 227,36,52,3072
s_add_u32 s52,s52,s56
s_add_u32 m0,s98,4096
s_nop 0
dl 224,36,52,0
dl 225,36,52,1024
dl 226,36,52,2048
dl 227,36,52,3072
s_add_u32 s52,s52,s60
s_add_u32 s61,s98,65536
s_mov_b32 s63,0
s_lshl_b32 s88,%1,2
s_add_u32 s88,s88,s12
s_lshl_b32 s88,s88,12
s_add_u32 s30,s68,s88
s_addc_u32 s31,s69,0
v_mov_b32_e32 v40,v247
v_and_b32_e32 v246,15,v40
v_lshlrev_b32_e32 v248,3,v40
v_xor_b32_e32 v248,v248,v40
v_and_b32_e32 v248,112,v248
v_lshl_add_u32 v232,v246,7,v248
s_and_b32 s86,s98,16384
v_add_u32_e32 v232,s86,v232
s_bfe_u32 s86,s21,65536
s_lshl_b32 s86,s86,6
v_xor_b32_e32 v232,s86,v232
v_xor_b32_e32 v233,64,v232
v_and_b32_e32 v246,12,v40
v_lshlrev_b32_e32 v246,5,v246
v_and_b32_e32 v248,3,v40
v_lshl_add_u32 v246,v248,3,v246
v_and_b32_e32 v248,24,v40
v_lshl_add_u32 v246,v248,2,v246
s_and_b32 s87,s98,8192
s_add_u32 s87,s87,98304
v_add_u32_e32 v246,s87,v246
v_and_b32_e32 v248,48,v40
v_lshl_add_u32 v234,v248,6,v246
v_xor_b32_e32 v235,32,v234
v_xor_b32_e32 v236,64,v234
v_xor_b32_e32 v237,96,v234
s_lshl_b32 s86,s19,1
s_add_u32 s86,s86,2
s_lshl_b32 s15,1,s86
s_xor_b32 s87,s19,15
s_lshl_b32 s22,s87,7
s_lshl_b32 s23,s19,7
s_lshl_b32 s87,s87,2
s_lshl_b32 s88,s19,2
s_add_u32 s87,s87,s12
s_add_u32 s88,s88,s12
s_lshl_b32 s90,s18,13
s_add_u32 s90,s90,62914560
s_mul_i32 s14,s87,327680
s_add_u32 s14,s14,s90
s_mul_i32 s13,s88,327680
s_add_u32 s13,s13,s90
s_mul_i32 s90,s18,524288
s_lshl_b32 s91,s12,12
s_add_u32 s90,s90,s91
s_add_u32 s90,s90,16777216
v_add_u32_e32 v249,s90,v244
v_add_u32_e32 v255,16777216,v249
v_accvgpr_write_b32 a240,v249
v_accvgpr_write_b32 a241,v255
s_or_b32 s86,%1,s12
s_cselect_b32 s65,0,4
v_lshrrev_b32_e32 v241,3,v40
v_lshlrev_b32_e32 v241,27,v241
v_lshl_add_u32 v241,v40,7,v241
s_cmp_lt_u32 s12,2
s_cselect_b64 s[48:49],s[72:73],s[80:81]
s_cselect_b32 s50,8,6
s_lshl_b32 s50,s50,20
s_and_b32 s86,s12,1
s_lshl_b32 s87,%1,1
s_add_u32 s86,s86,s87
s_lshl_b32 s55,s86,10
s_mov_b32 s59,524288
s_cmp_lt_u32 s12,2
s_cselect_b32 s87,10,8
v_cmp_gt_u32_e32 vcc,s87,v40
v_lshlrev_b32_e32 v241,7,v40
v_mov_b32_e32 v41,134217728
s_lshl_b32 s87,s87,7
s_mul_i32 s55,s86,s87
s_lshl_b32 s59,s87,9
v_cndmask_b32_e32 v241,v41,v241,vcc
s_cmp_lt_u32 s12,2
s_cbranch_scc1 L_lx
s_cmp_eq_u32 s12,2
s_cselect_b32 s87,16,11
v_cmp_gt_u32_e32 vcc,s87,v40
v_lshlrev_b32_e32 v241,7,v40
s_mul_i32 s55,%1,2048
v_cndmask_b32_e32 v241,v41,v241,vcc
s_cmp_eq_u32 s12,2
s_cbranch_scc1 L_lx
s_lshl_b32 s55,%1,14
s_add_u32 s86,s55,16384
s_mov_b64 s[48:49],s[68:69]
s_mov_b32 s50,s86
s_mov_b32 s55,s55
s_mov_b32 s59,1408
L_lx:
s_mov_b32 s33,s50
s_mov_b32 s34,s59
s_mov_b32 s50,0
s_mov_b32 s59,0
v_and_b32_e32 v246,15,v40
v_and_b32_e32 v248,48,v40
v_lshlrev_b32_e32 v249,2,v40
v_and_b32_e32 v249,32,v249
v_xor_b32_e32 v248,v248,v249
v_lshl_add_u32 v240,v246,6,v248
s_lshr_b32 s86,s12,1
s_lshl_b32 s86,s86,10
s_add_u32 s86,s86,112640
v_add_u32_e32 v240,s86,v240
v_and_b32_e32 v246,12,v40
v_lshlrev_b32_e32 v246,5,v246
v_and_b32_e32 v248,3,v40
v_lshl_add_u32 v246,v248,3,v246
v_and_b32_e32 v248,24,v40
v_lshl_add_u32 v246,v248,2,v246
v_add_u32_e32 v246,98304,v246
v_and_b32_e32 v248,32,v40
v_lshl_add_u32 v238,v248,5,v246
v_xor_b32_e32 v239,32,v238
v_lshrrev_b32_e32 v248,3,v40
v_and_b32_e32 v250,112,v244
v_and_b32_e32 v249,48,v40
v_xor_b32_e32 v249,v249,v250
v_and_b32_e32 v251,16,v40
v_lshlrev_b32_e32 v251,1,v251
v_xor_b32_e32 v250,v250,v251
v_lshl_add_u32 v48,v248,11,v249
v_xor_b32_e32 v49,64,v48
v_add_u32_e32 v51,46080,v49
v_add_u32_e32 v50,30720,v48
v_add_u32_e32 v49,15360,v49
v_mov_b32_e32 v52,v228
v_mov_b32_e32 v53,v229
v_mov_b32_e32 v54,v230
v_mov_b32_e32 v55,v231
s_cmp_lt_u32 s12,2
s_cbranch_scc1 L_pqx
v_mul_u32_u24_e32 v246,6144,v248
v_and_b32_e32 v251,48,v250
v_add_u32_e32 v246,v246,v251
v_and_b32_e32 v251,64,v250
v_lshl_add_u32 v52,v251,5,v246
v_xor_b32_e32 v252,64,v251
v_lshl_add_u32 v53,v252,5,v246
v_add_u32_e32 v55,144384,v53
v_add_u32_e32 v54,96256,v52
v_add_u32_e32 v53,48128,v53
s_cmp_eq_u32 s12,2
s_cbranch_scc1 L_pqx
v_mul_u32_u24_e32 v251,768,v251
v_add_u32_e32 v246,v246,v251
v_add_u32_e32 v52,4096,v246
v_add_u32_e32 v53,101376,v246
v_lshrrev_b32_e32 v252,2,v40
v_and_b32_e32 v253,32,v40
v_and_b32_e32 v250,48,v244
v_xor_b32_e32 v253,v253,v250
v_lshl_add_u32 v246,v252,11,v253
s_lshl_b32 s86,s22,11
s_sub_u32 s86,s86,34816
v_add_u32_e32 v54,s86,v246
s_lshl_b32 s86,s23,11
s_sub_u32 s86,s86,35840
v_add_u32_e32 v55,s86,v246
v_max_i32_e32 v55,0,v55
L_pqx:
v_accvgpr_write_b32 a242,v48
v_accvgpr_write_b32 a243,v49
v_accvgpr_write_b32 a244,v50
v_accvgpr_write_b32 a245,v51
v_accvgpr_write_b32 a246,v52
v_accvgpr_write_b32 a247,v53
v_accvgpr_write_b32 a248,v54
v_accvgpr_write_b32 a249,v55
s_add_u32 m0,s62,16384
s_nop 0
buffer_load_dwordx4 v228,s[40:43],s53 offen offset:0 nt lds
buffer_load_dwordx4 v229,s[40:43],s53 offen offset:1024 nt lds
s_add_u32 s53,s53,s58
buffer_load_dwordx4 v230,s[44:47],s54 offen offset:2048 nt lds
buffer_load_dwordx4 v231,s[44:47],s54 offen offset:3072 nt lds
s_add_u32 s54,s54,s58
s_add_u32 m0,s98,32768
s_nop 0
dl 224,36,52,0
dl 225,36,52,1024
dl 226,36,52,2048
dl 227,36,52,3072
s_add_u32 s52,s52,s56
s_waitcnt vmcnt(20)
s_cmp_lg_u32 s12,0
s_cbranch_scc1 L_rqx
v_readfirstlane_b32 s32,v44
v_readfirstlane_b32 s86,v45
s_add_u32 s32,s32,1
s_cmp_eq_u32 s32,s86
s_addc_u32 s32,s32,0
v_mov_b32_e32 v44,s32
v_mov_b32_e32 v45,0
v_lshlrev_b32_e32 v46,3,v244
v_mov_b32_e32 v47,0
s_lshl_b32 s86,%1,7
s_add_u32 s87,s86,83914752
s_and_b32 s86,s86,16383
s_add_u32 s86,s86,83898368
s_mov_b64 exec,1
buffer_store_dword v44,off,s[8:11],s35 sc0 sc1
v_mov_b32_e32 v48,0
v_mov_b32_e32 v49,0
buffer_store_dwordx4 v[46:49],off,s[8:11],s86 sc0 sc1
buffer_store_dwordx4 v[46:49],off,s[8:11],s87 sc0 sc1
s_cmp_lg_u32 %1,0
s_cbranch_scc1 L_rqy
s_mov_b64 exec,65535
s_mov_b32 s86,83894272
buffer_store_dword v45,v46,s[8:11],s86 offen sc0 sc1
L_rqy:
s_mov_b64 exec,-1
L_rqx:
L_vr0:
s_cmp_ge_u32 s21,16
s_cbranch_scc1 L_vtile
s_waitcnt vmcnt(8)
s_barrier
ta 32,234,0
ta 34,234,512
ra 0,232,0
ta 36,235,0
ta 38,235,512
ra 4,232,2048
ra 8,232,4096
ra 12,232,6144
ra 16,232,8192
ra 20,232,10240
ra 24,232,12288
ra 28,232,14336
s_add_u32 m0,s62,32768
ta 40,236,0
buffer_load_dwordx4 v228,s[40:43],s53 offen offset:0 nt lds
ta 42,236,512
ta 44,237,0
ta 46,237,512
ta 48,234,4096
ta 50,234,4608
ta 52,235,4096
ta 54,235,4608
ta 56,236,4096
ta 58,236,4608
ta 60,237,4096
ta 62,237,4608
s_cmp_ge_u32 s63,65536
s_cselect_b32 s99,-2,1
s_lshl_b32 s99,s99,15
s_add_u32 s63,s63,s99
v_add_u32_e32 v232,s99,v232
s_mov_b32 s64,1
s_waitcnt lgkmcnt(15)
zaaa 128,32,0
zaaa 132,36,0
zaaa 140,36,4
zaaa 136,32,4
buffer_load_dwordx4 v229,s[40:43],s53 offen offset:1024 nt lds
zvaa 32,32,8
s_add_u32 s53,s53,s57
zvaa 36,36,8
zvaa 44,36,12
zvaa 40,32,12
buffer_load_dwordx4 v230,s[44:47],s54 offen offset:2048 nt lds
zvaa 96,32,16
zvaa 100,36,16
s_waitcnt lgkmcnt(0)
zvaa 108,36,20
zvaa 104,32,20
buffer_load_dwordx4 v231,s[44:47],s54 offen offset:3072 nt lds
zvaa 160,32,24
s_add_u32 m0,s98,36864
s_add_u32 s54,s54,s57
zvaa 164,36,24
zvaa 172,36,28
zvaa 168,32,28
dl 224,36,52,0
zvaa 184,40,28
zvaa 188,44,28
zvaa 180,44,24
zvaa 176,40,24
dl 225,36,52,1024
zvaa 120,40,20
zvaa 124,44,20
zvaa 116,44,16
zvaa 112,40,16
dl 226,36,52,2048
zvaa 56,40,12
s_waitcnt vmcnt(11)
s_barrier
zvaa 60,44,12
ta 96,234,16384
zvaa 52,44,8
ta 98,234,16896
zvaa 48,40,8
dl 227,36,52,3072
zaaa 152,40,4
s_add_u32 m0,s62,49152
ra 64,233,0
s_add_u32 s52,s52,s60
zaaa 156,44,4
ta 100,235,16384
zaaa 148,44,0
ta 102,235,16896
zaaa 144,40,0
buffer_load_dwordx4 v228,s[40:43],s53 offen offset:0 nt lds
zvaa 0,48,0
ra 68,233,2048
zvaa 4,52,0
ta 104,236,16384
zvaa 12,52,4
ta 106,236,16896
zvaa 8,48,4
buffer_load_dwordx4 v229,s[40:43],s53 offen offset:1024 nt lds
zvaa 64,48,8
s_add_u32 s53,s53,s58
ra 72,233,4096
zvaa 68,52,8
ta 108,237,16384
zvaa 76,52,12
ta 110,237,16896
zvaa 72,48,12
buffer_load_dwordx4 v230,s[44:47],s54 offen offset:2048 nt lds
zvaa 128,48,16
ra 76,233,6144
zvaa 132,52,16
ta 112,234,20480
zvaa 140,52,20
ta 114,234,20992
zvaa 136,48,20
buffer_load_dwordx4 v231,s[44:47],s54 offen offset:3072 nt lds
zvaa 192,48,24
s_add_u32 m0,s61,0
ra 80,233,8192
s_add_u32 s54,s54,s58
zvaa 196,52,24
ta 116,235,20480
zvaa 204,52,28
ta 118,235,20992
zvaa 200,48,28
dl 224,36,52,0
zvaa 216,56,28
ra 84,233,10240
zvaa 220,60,28
ta 120,236,20480
zvaa 212,60,24
ta 122,236,20992
zvaa 208,56,24
dl 225,36,52,1024
ra 88,233,12288
zvaa 152,56,20
ta 124,237,20480
zvaa 156,60,20
ta 126,237,20992
zvaa 148,60,16
ra 92,233,14336
zvaa 144,56,16
dl 226,36,52,2048
zvaa 88,56,12
zvaa 92,60,12
zvaa 84,60,8
zvaa 80,56,8
dl 227,36,52,3072
zvaa 24,56,4
s_add_u32 s52,s52,s56
zvaa 28,60,4
zvaa 20,60,0
zvaa 16,56,0
buffer_load_dword a184,v241,s[48:51],s55 offen
s_add_u32 s55,s55,s59
s_waitcnt vmcnt(9)
s_waitcnt lgkmcnt(0)
s_barrier
L_k_head:
maaa 128,96,64
ta 32,234,32768
maaa 132,100,64
s_add_u32 m0,s62,0
ta 34,234,33280
maaa 140,100,68
buffer_load_dwordx4 v228,s[40:43],s53 offen offset:0 nt lds
maaa 136,96,68
mvaa 32,96,72
ra 0,232,0
mvaa 36,100,72
mvaa 44,100,76
ta 36,235,32768
mvaa 40,96,76
buffer_load_dwordx4 v229,s[40:43],s53 offen offset:1024 nt lds
mvaa 96,96,80
s_add_u32 s53,s53,s57
mvaa 100,100,80
ta 38,235,33280
mvaa 108,100,84
mvaa 104,96,84
ra 4,232,2048
mvaa 160,96,88
buffer_load_dwordx4 v230,s[44:47],s54 offen offset:2048 nt lds
mvaa 164,100,88
mvaa 172,100,92
ta 40,236,32768
mvaa 168,96,92
mvaa 184,104,92
ta 42,236,33280
mvaa 188,108,92
buffer_load_dwordx4 v231,s[44:47],s54 offen offset:3072 nt lds
mvaa 180,108,88
s_add_u32 s54,s54,s57
mvaa 176,104,88
ra 8,232,4096
mvaa 120,104,84
mvaa 124,108,84
ta 44,237,32768
mvaa 116,108,80
s_add_u32 m0,s61,4096
mvaa 112,104,80
dl 224,36,52,0
mvaa 56,104,76
ta 46,237,33280
mvaa 60,108,76
mvaa 52,108,72
ra 12,232,6144
mvaa 48,104,72
maaa 152,104,68
ta 48,234,36864
maaa 156,108,68
dl 225,36,52,1024
maaa 148,108,64
ta 50,234,37376
maaa 144,104,64
mvaa 0,112,64
ra 16,232,8192
mvaa 4,116,64
mvaa 12,116,68
ta 52,235,36864
mvaa 8,112,68
dl 226,36,52,2048
mvaa 64,112,72
mvaa 68,116,72
ta 54,235,37376
mvaa 76,116,76
mvaa 72,112,76
ra 20,232,10240
mvaa 128,112,80
mvaa 132,116,80
dl 227,36,52,3072
mvaa 140,116,84
s_add_u32 s52,s52,s60
ta 56,236,36864
mvaa 136,112,84
v_add_u32_e32 v233,s99,v233
mvaa 192,112,88
ta 58,236,37376
s_sub_u32 s61,s63,s99
mvaa 196,116,88
s_add_u32 s61,s61,s98
mvaa 204,116,92
ra 24,232,12288
mvaa 200,112,92
buffer_load_dword a185,v241,s[48:51],s55 offen
s_add_u32 s55,s55,s59
mvaa 216,120,92
mvaa 220,124,92
ta 60,237,36864
mvaa 212,124,88
mvaa 208,120,88
ta 62,237,37376
mvaa 152,120,84
mvaa 156,124,84
ra 28,232,14336
mvaa 148,124,80
mvaa 144,120,80
mvaa 88,120,76
mvaa 92,124,76
mvaa 84,124,72
mvaa 80,120,72
mvaa 24,120,68
mvaa 28,124,68
mvaa 20,124,64
s_waitcnt vmcnt(10) lgkmcnt(0)
s_barrier
mvaa 16,120,64
maaa 128,32,0
ta 96,234,49152
maaa 132,36,0
s_add_u32 m0,s62,16384
ta 98,234,49664
maaa 140,36,4
buffer_load_dwordx4 v228,s[40:43],s53 offen offset:0 nt lds
maaa 136,32,4
mvaa 32,32,8
ra 64,233,0
mvaa 36,36,8
mvaa 44,36,12
ta 100,235,49152
mvaa 40,32,12
buffer_load_dwordx4 v229,s[40:43],s53 offen offset:1024 nt lds
mvaa 96,32,16
s_add_u32 s53,s53,s58
mvaa 100,36,16
ta 102,235,49664
mvaa 108,36,20
mvaa 104,32,20
ra 68,233,2048
mvaa 160,32,24
buffer_load_dwordx4 v230,s[44:47],s54 offen offset:2048 nt lds
mvaa 164,36,24
mvaa 172,36,28
ta 104,236,49152
mvaa 168,32,28
mvaa 184,40,28
ta 106,236,49664
mvaa 188,44,28
buffer_load_dwordx4 v231,s[44:47],s54 offen offset:3072 nt lds
mvaa 180,44,24
s_add_u32 s54,s54,s58
mvaa 176,40,24
ra 72,233,4096
mvaa 120,40,20
mvaa 124,44,20
ta 108,237,49152
mvaa 116,44,16
s_add_u32 m0,s61,0
mvaa 112,40,16
dl 224,36,52,0
mvaa 56,40,12
ta 110,237,49664
mvaa 60,44,12
mvaa 52,44,8
ra 76,233,6144
mvaa 48,40,8
maaa 152,40,4
ta 112,234,53248
maaa 156,44,4
dl 225,36,52,1024
maaa 148,44,0
ta 114,234,53760
maaa 144,40,0
mvaa 0,48,0
ra 80,233,8192
mvaa 4,52,0
mvaa 12,52,4
ta 116,235,53248
mvaa 8,48,4
dl 226,36,52,2048
mvaa 64,48,8
mvaa 68,52,8
ta 118,235,53760
mvaa 76,52,12
mvaa 72,48,12
ra 84,233,10240
mvaa 128,48,16
mvaa 132,52,16
dl 227,36,52,3072
mvaa 140,52,20
s_add_u32 s52,s52,s56
ta 120,236,53248
mvaa 136,48,20
s_cmp_ge_u32 s63,65536
s_cselect_b32 s99,-2,1
s_lshl_b32 s99,s99,15
mvaa 192,48,24
ta 122,236,53760
s_add_u32 s63,s63,s99
mvaa 196,52,24
v_add_u32_e32 v232,s99,v232
mvaa 204,52,28
ra 88,233,12288
mvaa 200,48,28
buffer_load_dword a186,v241,s[48:51],s55 offen
s_add_u32 s55,s55,s59
mvaa 216,56,28
mvaa 220,60,28
ta 124,237,53248
mvaa 212,60,24
mvaa 208,56,24
ta 126,237,53760
mvaa 152,56,20
mvaa 156,60,20
ra 92,233,14336
mvaa 148,60,16
mvaa 144,56,16
mvaa 88,56,12
mvaa 92,60,12
mvaa 84,60,8
mvaa 80,56,8
mvaa 24,56,4
mvaa 28,60,4
s_mov_b32 s50,s33
s_mov_b32 s59,s34
mvaa 20,60,0
s_waitcnt vmcnt(10) lgkmcnt(0)
s_barrier
mvaa 16,56,0
maaa 128,96,64
ta 32,234,0
maaa 132,100,64
s_add_u32 m0,s62,32768
ta 34,234,512
maaa 140,100,68
buffer_load_dwordx4 v228,s[40:43],s53 offen offset:0 nt lds
maaa 136,96,68
mvaa 32,96,72
ra 0,232,0
mvaa 36,100,72
mvaa 44,100,76
ta 36,235,0
mvaa 40,96,76
buffer_load_dwordx4 v229,s[40:43],s53 offen offset:1024 nt lds
mvaa 96,96,80
s_add_u32 s53,s53,s57
mvaa 100,100,80
ta 38,235,512
mvaa 108,100,84
mvaa 104,96,84
ra 4,232,2048
mvaa 160,96,88
buffer_load_dwordx4 v230,s[44:47],s54 offen offset:2048 nt lds
mvaa 164,100,88
mvaa 172,100,92
ta 40,236,0
mvaa 168,96,92
mvaa 184,104,92
ta 42,236,512
mvaa 188,108,92
buffer_load_dwordx4 v231,s[44:47],s54 offen offset:3072 nt lds
mvaa 180,108,88
s_add_u32 s54,s54,s57
mvaa 176,104,88
ra 8,232,4096
mvaa 120,104,84
mvaa 124,108,84
ta 44,237,0
mvaa 116,108,80
s_add_u32 m0,s61,4096
mvaa 112,104,80
dl 224,36,52,0
mvaa 56,104,76
ta 46,237,512
mvaa 60,108,76
mvaa 52,108,72
ra 12,232,6144
mvaa 48,104,72
maaa 152,104,68
ta 48,234,4096
maaa 156,108,68
dl 225,36,52,1024
maaa 148,108,64
ta 50,234,4608
maaa 144,104,64
mvaa 0,112,64
ra 16,232,8192
mvaa 4,116,64
mvaa 12,116,68
ta 52,235,4096
mvaa 8,112,68
dl 226,36,52,2048
mvaa 64,112,72
mvaa 68,116,72
ta 54,235,4608
mvaa 76,116,76
mvaa 72,112,76
ra 20,232,10240
mvaa 128,112,80
mvaa 132,116,80
dl 227,36,52,3072
mvaa 140,116,84
s_add_u32 s52,s52,s60
ta 56,236,4096
mvaa 136,112,84
v_add_u32_e32 v233,s99,v233
mvaa 192,112,88
ta 58,236,4608
s_sub_u32 s61,s63,s99
mvaa 196,116,88
s_add_u32 s61,s61,s98
mvaa 204,116,92
ra 24,232,12288
mvaa 200,112,92
buffer_load_dword a187,v241,s[48:51],s55 offen
s_add_u32 s55,s55,s59
mvaa 216,120,92
mvaa 220,124,92
ta 60,237,4096
mvaa 212,124,88
mvaa 208,120,88
ta 62,237,4608
mvaa 152,120,84
mvaa 156,124,84
ra 28,232,14336
mvaa 148,124,80
mvaa 144,120,80
mvaa 88,120,76
mvaa 92,124,76
mvaa 84,124,72
mvaa 80,120,72
mvaa 24,120,68
mvaa 28,124,68
s_cmp_eq_u32 s64,3
mvaa 20,124,64
s_waitcnt vmcnt(10) lgkmcnt(0)
s_barrier
mvaa 16,120,64
s_cbranch_scc1 L_k_exit
maaa 128,32,0
ta 96,234,16384
maaa 132,36,0
s_add_u32 m0,s62,49152
ta 98,234,16896
maaa 140,36,4
buffer_load_dwordx4 v228,s[40:43],s53 offen offset:0 nt lds
maaa 136,32,4
mvaa 32,32,8
ra 64,233,0
mvaa 36,36,8
mvaa 44,36,12
ta 100,235,16384
mvaa 40,32,12
buffer_load_dwordx4 v229,s[40:43],s53 offen offset:1024 nt lds
mvaa 96,32,16
s_add_u32 s53,s53,s58
mvaa 100,36,16
ta 102,235,16896
mvaa 108,36,20
mvaa 104,32,20
ra 68,233,2048
mvaa 160,32,24
buffer_load_dwordx4 v230,s[44:47],s54 offen offset:2048 nt lds
mvaa 164,36,24
mvaa 172,36,28
ta 104,236,16384
mvaa 168,32,28
mvaa 184,40,28
ta 106,236,16896
mvaa 188,44,28
buffer_load_dwordx4 v231,s[44:47],s54 offen offset:3072 nt lds
mvaa 180,44,24
s_add_u32 s54,s54,s58
mvaa 176,40,24
ra 72,233,4096
s_bitcmp1_b32 s65,s64
s_cbranch_scc1 L_k_ev
L_k_evr:
mvaa 120,40,20
mvaa 124,44,20
ta 108,237,16384
mvaa 116,44,16
s_add_u32 m0,s61,0
mvaa 112,40,16
dl 224,36,52,0
mvaa 56,40,12
ta 110,237,16896
mvaa 60,44,12
mvaa 52,44,8
ra 76,233,6144
mvaa 48,40,8
maaa 152,40,4
ta 112,234,20480
maaa 156,44,4
dl 225,36,52,1024
maaa 148,44,0
ta 114,234,20992
maaa 144,40,0
mvaa 0,48,0
ra 80,233,8192
mvaa 4,52,0
mvaa 12,52,4
ta 116,235,20480
mvaa 8,48,4
dl 226,36,52,2048
mvaa 64,48,8
mvaa 68,52,8
ta 118,235,20992
mvaa 76,52,12
mvaa 72,48,12
ra 84,233,10240
mvaa 128,48,16
mvaa 132,52,16
dl 227,36,52,3072
mvaa 140,52,20
s_add_u32 s52,s52,s56
ta 120,236,20480
mvaa 136,48,20
s_cmp_ge_u32 s63,65536
s_cselect_b32 s99,-2,1
s_lshl_b32 s99,s99,15
mvaa 192,48,24
ta 122,236,20992
s_add_u32 s63,s63,s99
mvaa 196,52,24
v_add_u32_e32 v232,s99,v232
mvaa 204,52,28
ra 88,233,12288
mvaa 200,48,28
buffer_load_dword a184,v241,s[48:51],s55 offen
s_add_u32 s55,s55,s59
)"
R"(mvaa 216,56,28
mvaa 220,60,28
ta 124,237,20480
mvaa 212,60,24
mvaa 208,56,24
ta 126,237,20992
mvaa 152,56,20
mvaa 156,60,20
ra 92,233,14336
mvaa 148,60,16
mvaa 144,56,16
mvaa 88,56,12
mvaa 92,60,12
mvaa 84,60,8
mvaa 80,56,8
mvaa 24,56,4
mvaa 28,60,4
s_lshl_b32 s86,s21,2
s_add_u32 s86,s86,s12
s_mul_i32 s86,s86,384
s_add_u32 s86,s86,4096
v_lshl_add_u32 v253,v244,3,s86
s_mov_b64 exec,7
global_load_dword a188,v253,s[96:97]
s_mov_b64 exec,-1
s_add_u32 s64,s64,1
mvaa 20,60,0
s_waitcnt vmcnt(10) lgkmcnt(0)
s_barrier
mvaa 16,56,0
s_branch L_k_head
L_k_exit:
global_load_dwordx4 a[160:163],v244,s[30:31] offset:0
s_add_u32 m0,s62,49152
maaa 128,32,0
s_cmp_lt_u32 s12,2
s_cselect_b32 s86,s22,s23
ta 96,234,16384
v_accvgpr_read_b32 v224,a242
maaa 132,36,0
s_and_b32 s87,s12,1
ta 98,234,16896
s_lshl_b32 s86,s86,11
maaa 140,36,4
s_lshl_b32 s87,s87,17
ra 64,233,0
s_add_u32 s86,s86,s87
maaa 136,32,4
s_mov_b64 s[36:37],s[68:69]
s_mov_b32 s38,4194304
s_mov_b32 s52,s86
mvaa 32,32,8
buffer_load_dwordx4 v228,s[40:43],s53 offen offset:0 nt lds
mvaa 36,36,8
s_mov_b32 s56,65536
ta 100,235,16384
v_accvgpr_read_b32 v225,a243
mvaa 44,36,12
s_lshl_b32 s88,s18,1
ta 102,235,16896
v_accvgpr_read_b32 v226,a244
mvaa 40,32,12
s_add_u32 s88,s88,s12
ra 68,233,2048
v_accvgpr_read_b32 v227,a245
mvaa 96,32,16
s_mov_b32 s91,196608
ra 72,233,4096
s_lshl_b32 s88,s88,7
mvaa 100,36,16
buffer_load_dwordx4 v229,s[40:43],s53 offen offset:1024 nt lds
mvaa 108,36,20
s_lshl_b32 s89,s18,6
ra 76,233,6144
s_mov_b32 s90,6291456
mvaa 104,32,20
s_cmp_lt_u32 s12,2
s_cselect_b32 s57,262144,s91
ra 80,233,8192
s_add_u32 s53,s53,s58
mvaa 160,32,24
s_cmp_lt_u32 s12,2
s_cselect_b32 s42,8388608,s90
ra 84,233,10240
v_accvgpr_read_b32 v228,a246
mvaa 164,36,24
s_cmp_lt_u32 s12,2
s_cselect_b32 s53,s88,s89
ra 88,233,12288
v_accvgpr_read_b32 v229,a247
mvaa 172,36,28
buffer_load_dwordx4 v230,s[44:47],s54 offen offset:2048 nt lds
mvaa 168,32,28
s_bitcmp1_b32 s21,0
s_cselect_b32 s90,s57,0
ra 92,233,14336
s_add_u32 s53,s53,s90
mvaa 184,40,28
s_cmp_lt_u32 s12,2
s_cselect_b64 s[40:41],s[72:73],s[80:81]
ta 104,236,16384
v_accvgpr_read_b32 v230,a248
mvaa 188,44,28
s_lshl_b32 s90,s90,1
ta 106,236,16896
s_mov_b32 s60,-65408
mvaa 180,44,24
s_and_b32 s89,s98,8192
ta 108,237,16384
s_add_u32 s91,s89,65536
mvaa 176,40,24
buffer_load_dwordx4 v231,s[44:47],s54 offen offset:3072 nt lds
mvaa 120,40,20
s_add_u32 m0,s98,65536
ta 110,237,16896
s_add_u32 s54,s54,s58
mvaa 124,44,20
s_cmp_eq_u32 s12,3
s_cselect_b64 s[44:45],s[68:69],s[40:41]
ta 112,234,20480
v_accvgpr_read_b32 v231,a249
mvaa 116,44,16
s_cmp_eq_u32 s12,3
s_cselect_b32 s46,0,s42
ta 114,234,20992
s_add_u32 s58,s57,s90
mvaa 112,40,16
s_cmp_eq_u32 s12,3
s_cselect_b32 s54,0,s53
ta 116,235,20480
s_sub_u32 s57,s57,s90
mvaa 56,40,12
global_load_dwordx4 a[168:171],v244,s[30:31] offset:2048
mvaa 60,44,12
s_add_u32 s90,s89,65536
ta 118,235,20992
s_bfe_u32 s88,s21,65539
mvaa 52,44,8
s_lshl_b32 s88,s88,1
ta 120,236,20480
s_lshl_b32 s87,s20,2
mvaa 48,40,8
s_add_u32 s88,s88,s87
ta 122,236,20992
s_and_b32 s87,s12,1
maaa 152,40,4
s_add_u32 s88,s88,s87
ta 124,237,20480
s_mul_i32 s88,s88,524288
maaa 156,44,4
global_load_dwordx4 a[176:179],v244,s[84:85] offset:0
maaa 148,44,0
s_and_b32 s87,s21,7
ta 126,237,20992
s_add_u32 s86,s88,16777216
maaa 144,40,0
s_lshl_b32 s87,s87,16
s_mov_b32 s26,32768
s_add_u32 s86,s86,s87
mvaa 0,48,0
s_lshr_b32 s87,s12,1
s_mov_b32 s27,131072
s_lshl_b32 s87,s87,15
mvaa 4,52,0
s_add_u32 s86,s86,s87
s_lshl_b32 s87,s12,3
s_add_u32 s61,s98,32768
mvaa 12,52,4
global_load_dwordx4 a[164:167],v244,s[30:31] offset:1024
mvaa 8,48,4
s_add_u32 s24,s8,s86
s_addc_u32 s25,s9,0
s_lshl_b32 s86,%1,5
s_add_u32 s86,s86,s87
mvaa 64,48,8
s_add_u32 s86,s86,83886080
s_mov_b32 s63,65536
s_mov_b32 s67,2048
mvaa 68,52,8
s_cmp_ge_u32 s63,65536
s_cselect_b32 s99,-2,1
s_lshl_b32 s99,s99,15
s_mov_b32 s66,8
mvaa 76,52,12
s_cmp_lg_u32 s65,0
s_cbranch_scc1 L_k_ec1
L_k_ecr1:
s_cmp_eq_u32 s12,0
s_cselect_b32 s65,96,0
mvaa 72,48,12
global_load_dwordx4 a[172:175],v244,s[30:31] offset:3072
mvaa 128,48,16
s_mov_b32 s30,1048577
s_mov_b32 s31,268435712
s_mov_b32 s88,4096
mvaa 132,52,16
s_mov_b32 s64,1
s_or_b32 s65,s65,128
mvaa 140,52,20
s_bitcmp1_b32 s12,0
s_cselect_b64 vcc,-1,0
mvaa 136,48,20
mvaa 192,48,24
global_load_dwordx4 a[180:183],v244,s[84:85] offset:1024
mvaa 196,52,24
mvaa 204,52,28
mvaa 200,48,28
mvaa 216,56,28
mvaa 220,60,28
dl 224,36,52,0
mvaa 212,60,24
mvaa 208,56,24
mvaa 152,56,20
mvaa 156,60,20
mvaa 148,60,16
dl 225,36,52,1024
mvaa 144,56,16
mvaa 88,56,12
s_waitcnt vmcnt(13) lgkmcnt(0)
s_barrier
mvaa 92,60,12
ta 32,234,32768
mvaa 84,60,8
ta 34,234,33280
mvaa 80,56,8
dl 226,36,52,2048
mvaa 24,56,4
ta 36,235,32768
mvaa 28,60,4
ta 38,235,33280
mvaa 20,60,0
ta 40,236,32768
mvaa 16,56,0
ta 42,236,33280
maaa 128,96,64
dl 227,36,52,3072
maaa 132,100,64
s_add_u32 s52,s52,s56
s_add_u32 m0,s98,69632
ta 44,237,32768
maaa 140,100,68
ta 46,237,33280
maaa 136,96,68
ta 48,234,36864
mvaa 32,96,72
ta 50,234,37376
mvaa 36,100,72
dl 224,36,52,0
mvaa 44,100,76
ta 52,235,36864
mvaa 40,96,76
ta 54,235,37376
mvaa 96,96,80
ra 20,232,43008
mvaa 100,100,80
ra 24,232,45056
mvaa 108,100,84
dl 225,36,52,1024
mvaa 104,96,84
ra 28,232,47104
mvaa 160,96,88
ra 16,232,40960
mvaa 164,100,88
ra 0,232,32768
mvaa 172,100,92
ra 4,232,34816
mvaa 168,96,92
dl 226,36,52,2048
mvaa 184,104,92
ra 12,232,38912
mvaa 188,108,92
ra 8,232,36864
v_add_u32_e32 v232,s90,v232
mvaa 180,108,88
ta 60,237,36864
mvaa 176,104,88
ta 62,237,37376
mvaa 120,104,84
dl 227,36,52,3072
mvaa 124,108,84
s_add_u32 m0,s62,0
ta 56,236,36864
s_add_u32 s52,s52,s60
mvaa 116,108,80
ta 58,236,37376
mvaa 112,104,80
mvaa 56,104,76
mvaa 60,108,76
dl 228,40,53,0
mvaa 52,108,72
mvaa 48,104,72
maaa 152,104,68
maaa 156,108,68
maaa 148,108,64
dl 229,40,53,1024
maaa 144,104,64
s_add_u32 s53,s53,s57
mvaa 0,112,64
mvaa 4,116,64
mvaa 12,116,68
mvaa 8,112,68
dl 230,44,54,2048
mvaa 64,112,72
mvaa 68,116,72
mvaa 76,116,76
mvaa 72,112,76
mvaa 128,112,80
dl 231,44,54,3072
mvaa 132,116,80
s_add_u32 m0,s62,16384
s_add_u32 s54,s54,s57
mvaa 140,116,84
mvaa 136,112,84
mvaa 192,112,88
mvaa 196,116,88
dl 228,40,53,0
mvaa 204,116,92
mvaa 200,112,92
mvaa 216,120,92
mvaa 220,124,92
mvaa 212,124,88
dl 229,40,53,1024
mvaa 208,120,88
s_waitcnt vmcnt(19) lgkmcnt(0)
s_barrier
mvaa 152,120,84
s_add_u32 s53,s53,s58
ta 96,234,49152
mvaa 156,124,84
ra 92,233,47104
mvaa 148,124,80
ta 98,234,49664
mvaa 144,120,80
dl 230,44,54,2048
mvaa 88,120,76
ra 88,233,45056
mvaa 92,124,76
ta 100,235,49152
mvaa 84,124,72
ta 112,234,53248
mvaa 80,120,72
ta 116,235,53248
mvaa 24,120,68
dl 231,44,54,3072
mvaa 28,124,68
s_add_u32 m0,s98,0
ta 118,235,53760
s_add_u32 s54,s54,s58
mvaa 20,124,64
ta 114,234,53760
v_subrev_u32_e32 v234,s89,v234
mvaa 16,120,64
ta 102,235,49664
v_subrev_u32_e32 v235,s89,v235
maaa 128,32,0
ta 124,237,53248
maaa 132,36,0
dl 224,36,52,0
maaa 140,36,4
ta 126,237,53760
maaa 136,32,4
ta 120,236,53248
mvaa 32,32,8
ta 122,236,53760
mvaa 36,36,8
ta 104,236,49152
mvaa 44,36,12
dl 225,36,52,1024
mvaa 40,32,12
ta 106,236,49664
v_subrev_u32_e32 v236,s89,v236
mvaa 56,40,12
ta 108,237,49152
mvaa 60,44,12
ta 110,237,49664
v_subrev_u32_e32 v237,s89,v237
mvaa 52,44,8
ra 84,233,43008
mvaa 48,40,8
dl 226,36,52,2048
maaa 152,40,4
ra 76,233,38912
maaa 156,44,4
ra 80,233,40960
maaa 148,44,0
ra 68,233,34816
maaa 144,40,0
ra 72,233,36864
mvaa 0,48,0
dl 227,36,52,3072
mvaa 4,52,0
s_add_u32 s52,s52,s56
s_add_u32 m0,s98,4096
ra 64,233,32768
mvaa 12,52,4
v_add_u32_e32 v233,s91,v233
mvaa 8,48,4
mvaa 24,56,4
mvaa 28,60,4
dl 224,36,52,0
mvaa 20,60,0
mvaa 16,56,0
mvaa 220,60,28
s_waitcnt lgkmcnt(2)
mvaa 220,124,92
mvaa 216,120,92
dl 225,36,52,1024
mvaa 216,56,28
mvaa 212,60,24
mvaa 212,124,88
mvaa 208,120,88
mvaa 208,56,24
dl 226,36,52,2048
s_waitcnt vmcnt(25)
zvaa 246,160,176
mvaa 204,52,28
cv 223,222,223
cv 222,220,221
mvaa 204,116,92
cv 221,218,219
zvaa 250,168,176
mvaa 200,112,92
cv 220,216,217
cv 219,214,215
mvaa 200,48,28
cv 218,212,213
cv 217,210,211
mvaa 196,52,24
dl 227,36,52,3072
mvaa 196,116,88
s_add_u32 m0,s62,32768
cv 216,208,209
cv 215,206,207
mvaa 192,112,88
s_add_u32 s52,s52,s60
cv 214,204,205
cv 213,202,203
mvaa 192,48,24
cv 212,200,201
s_waitcnt vmcnt(24)
mvaa 246,164,180
mvaa 250,172,180
mvaa 188,44,28
cv 211,198,199
cv 210,196,197
mvaa 188,108,92
dl 228,40,53,0
mvaa 184,104,92
cv 209,194,195
v_cndmask_b32_e64 v246,0,v246,s[30:31]
v_cndmask_b32_e64 v250,0,v250,s[30:31]
s_lshl_b64 s[30:31],s[30:31],1
v_cndmask_b32_e64 v246,v246,v247,s[30:31]
v_cndmask_b32_e64 v250,v250,v251,s[30:31]
s_lshl_b64 s[30:31],s[30:31],1
mvaa 184,40,28
v_cndmask_b32_e64 v246,v246,v248,s[30:31]
v_cndmask_b32_e64 v250,v250,v252,s[30:31]
s_lshl_b64 s[30:31],s[30:31],1
v_cndmask_b32_e64 v246,v246,v249,s[30:31]
v_cndmask_b32_e64 v250,v250,v253,s[30:31]
mvaa 180,44,24
v_add_f32_dpp v246,v246,v246 quad_perm:[1,0,3,2] row_mask:0xf bank_mask:0xf
v_add_f32_dpp v250,v250,v250 quad_perm:[1,0,3,2] row_mask:0xf bank_mask:0xf
mvaa 180,108,88
v_add_f32_dpp v246,v246,v246 quad_perm:[2,3,0,1] row_mask:0xf bank_mask:0xf
v_add_f32_dpp v250,v250,v250 quad_perm:[2,3,0,1] row_mask:0xf bank_mask:0xf
mvaa 176,104,88
dl 229,40,53,1024
mvaa 176,40,24
s_add_u32 s53,s53,s57
v_add_f32_dpp v246,v246,v246 row_half_mirror row_mask:0xf bank_mask:0xf
v_add_f32_dpp v250,v250,v250 row_half_mirror row_mask:0xf bank_mask:0xf
mvaa 172,36,28
v_add_f32_dpp v246,v246,v246 row_mirror row_mask:0xf bank_mask:0xf
v_add_f32_dpp v250,v250,v250 row_mirror row_mask:0xf bank_mask:0xf
mvaa 172,100,92
cv 208,192,193
cv 207,190,191
mvaa 168,96,92
v_permlane16_swap_b32_e32 v246,v250
mvaa 168,32,28
dl 230,44,54,2048
v_add_f32_e32 v246,v246,v250
v_mov_b32_e32 v250,v246
mvaa 164,36,24
cv 206,188,189
cv 205,186,187
mvaa 164,100,88
v_permlane32_swap_b32_e32 v246,v250
mvaa 160,96,88
v_add_f32_e32 v246,v246,v250
v_lshrrev_b32_e32 v250,6,v244
mvaa 160,32,24
v_mul_f32_e32 v246,0xbfb8aa3b,v246
cv 204,184,185
mvaa 156,60,20
dl 231,44,54,3072
ex 246
mvaa 156,124,84
s_add_u32 s54,s54,s57
v_add_f32_e32 v246,1.0,v246
cv 203,182,183
mvaa 152,120,84
v_rcp_f32_e32 v246,v246
mvaa 152,56,20
cv 202,180,181
cv 201,178,179
mvaa 148,60,16
cv 200,176,177
cv 199,174,175
mvaa 148,124,80
buffer_load_dword a242,v241,s[48:51],s55 offen
mvaa 144,120,80
s_add_u32 s55,s55,s59
cv 198,172,173
cv 197,170,171
mvaa 144,56,16
s_waitcnt vmcnt(17) lgkmcnt(0)
s_barrier
s_add_u32 m0,s62,49152
mvaa 140,52,20
cv 196,168,169
cv 195,166,167
mvaa 140,116,84
cv 194,164,165
cv 193,162,163
mvaa 136,112,84
dl 228,40,53,0
mvaa 136,48,20
cv 192,160,161
cv 191,158,159
mvaa 132,52,16
ra 4,232,0
cv 190,156,157
mvaa 132,116,80
ta 88,238,12288
cv 189,154,155
mvaa 128,112,80
ta 90,238,12800
cv 188,152,153
mvaa 128,48,16
dl 229,40,53,1024
mvaa 124,44,20
s_add_u32 s53,s53,s58
ta 92,239,12288
cv 187,150,151
mvaa 124,108,84
ta 94,239,12800
cv 186,148,149
mvaa 120,104,84
cv 185,146,147
cv 184,144,145
mvaa 120,40,20
cv 183,142,143
cv 182,140,141
mvaa 116,44,16
dl 230,44,54,2048
mvaa 116,108,80
cv 181,138,139
cv 180,136,137
mvaa 112,104,80
cv 179,134,135
cv 178,132,133
mvaa 112,40,16
cv 177,130,131
cv 176,128,129
mvaa 108,36,20
cv 175,126,127
cv 174,124,125
mvaa 108,100,84
dl 231,44,54,3072
mvaa 104,96,84
s_add_u32 m0,s98,32768
cv 173,122,123
cv 172,120,121
mvaa 104,32,20
s_add_u32 s54,s54,s58
ta 44,235,0
cv 171,118,119
mvaa 100,36,16
ta 46,235,512
cv 170,116,117
mvaa 100,100,80
ta 40,234,0
cv 169,114,115
mvaa 96,96,80
dl 224,36,52,0
mvaa 96,32,16
ta 42,234,512
cv 168,112,113
mvaa 92,60,12
cv 167,110,111
cv 166,108,109
mvaa 92,124,76
ta 84,237,8192
cv 165,106,107
mvaa 88,120,76
ta 86,237,8704
cv 164,104,105
mvaa 88,56,12
dl 225,36,52,1024
mvaa 84,60,8
cv 163,102,103
cv 162,100,101
mvaa 84,124,72
ta 80,236,8192
cv 161,98,99
mvaa 80,120,72
ra 16,232,6144
cv 160,96,97
mvaa 80,56,8
ta 82,236,8704
cv 159,94,95
mvaa 76,52,12
dl 226,36,52,2048
mvaa 76,116,76
ta 60,235,4096
cv 158,92,93
mvaa 72,112,76
ta 62,235,4608
cv 157,90,91
mvaa 72,48,12
ta 56,234,4096
cv 156,88,89
mvaa 68,52,8
ta 58,234,4608
cv 151,78,79
mvaa 68,116,72
dl 227,36,52,3072
mvaa 64,112,72
s_add_u32 m0,s62,0
ra 12,232,4096
s_add_u32 s52,s52,s56
mvaa 64,48,8
ta 52,237,0
cv 150,76,77
mvaa 60,108,76
ta 54,237,512
cv 147,70,71
mvaa 56,104,76
ra 8,232,2048
cv 146,68,69
mvaa 52,108,72
buffer_load_dword a243,v241,s[48:51],s55 offen
mvaa 48,104,72
s_add_u32 s55,s55,s59
ta 48,236,0
cv 145,66,67
mvaa 44,100,76
ta 50,236,512
cv 144,64,65
mvaa 40,96,76
cv 143,62,63
cv 142,60,61
mvaa 36,100,72
cv 141,58,59
cv 140,56,57
mvaa 32,96,72
buffer_load_dword a244,v241,s[48:51],s55 offen
mvaa 28,124,68
s_add_u32 s55,s55,s59
cv 139,54,55
cv 138,52,53
mvaa 24,120,68
cv 137,50,51
cv 136,48,49
mvaa 20,124,64
cv 135,46,47
cv 134,44,45
mvaa 16,120,64
ta 76,235,8192
cv 133,42,43
mvaa 12,116,68
buffer_load_dword a245,v241,s[48:51],s55 offen
mvaa 8,112,68
s_add_u32 s55,s55,s59
ta 78,235,8704
cv 132,40,41
mvaa 4,116,64
cv 131,38,39
cv 130,36,37
mvaa 0,112,64
ta 72,234,8192
cv 129,34,35
maaa 156,108,68
ta 74,234,8704
cv 128,32,33
maaa 152,104,68
s_mov_b64 exec,65537
buffer_store_dword v246,v250,s[8:11],s86 offen sc0 sc1
s_mov_b64 exec,-1
maaa 148,108,64
cv 127,30,31
v_accvgpr_read_b32 v63,a159
maaa 144,104,64
v_accvgpr_read_b32 v62,a158
v_accvgpr_read_b32 v61,a157
maaa 140,100,68
v_accvgpr_read_b32 v60,a156
v_accvgpr_read_b32 v59,a155
maaa 136,96,68
v_accvgpr_read_b32 v58,a154
v_accvgpr_read_b32 v57,a153
maaa 132,100,64
v_accvgpr_read_b32 v56,a152
cv 56,56,57
maaa 128,96,64
ta 68,237,4096
ta 70,237,4608
s_waitcnt lgkmcnt(4)
zaaa 236,40,4
cv 57,58,59
cv 58,60,61
zaaa 220,44,4
ta 64,236,4096
ta 66,236,4608
zaaa 216,44,8
cv 59,62,63
v_accvgpr_read_b32 v55,a151
zaaa 232,40,8
buffer_store_dwordx4 v[56:59],v244,s[24:27],0 offen offset:3072 sc0 sc1
zaaa 228,40,12
v_accvgpr_read_b32 v54,a150
v_accvgpr_read_b32 v53,a149
zaaa 212,44,12
v_accvgpr_read_b32 v52,a148
v_accvgpr_read_b32 v51,a147
zaaa 208,44,16
v_accvgpr_read_b32 v50,a146
v_accvgpr_read_b32 v49,a145
zaaa 224,40,16
v_accvgpr_read_b32 v48,a144
cv 48,48,49
zaaa 192,48,16
cv 49,50,51
cv 50,52,53
zaaa 176,52,16
cv 51,54,55
s_waitcnt vmcnt(26) lgkmcnt(0)
s_barrier
zaaa 180,52,12
dl 228,40,53,0
zaaa 196,48,12
ra 24,233,0
v_accvgpr_read_b32 v47,a143
zaaa 200,48,8
ra 28,233,2048
v_accvgpr_read_b32 v46,a142
v_accvgpr_read_b32 v45,a141
zaaa 184,52,8
ra 32,233,4096
ra 36,233,6144
zaaa 188,52,4
v_accvgpr_read_b32 v43,a139
v_accvgpr_read_b32 v42,a138
zaaa 204,48,4
dl 229,40,53,1024
ta 96,236,24576
zaaa 172,56,4
s_add_u32 s53,s53,s57
ta 98,236,25088
v_accvgpr_read_b32 v41,a137
zaaa 156,60,4
ta 100,237,24576
v_accvgpr_read_b32 v39,a135
zaaa 152,60,8
ta 102,237,25088
ta 104,238,28672
zaaa 168,56,8
v_accvgpr_read_b32 v38,a134
v_accvgpr_read_b32 v35,a131
zaaa 164,56,12
buffer_store_dwordx4 v[48:51],v244,s[24:27],0 offen offset:2048 sc0 sc1
ta 106,238,29184
zaaa 148,60,12
ta 108,239,28672
v_accvgpr_read_b32 v34,a130
zaaa 144,60,16
ta 110,239,29184
v_accvgpr_read_b32 v33,a129
zaaa 160,56,16
ta 44,235,16384
ta 46,235,16896
ta 40,234,16384
ta 42,234,16896
ta 52,237,16384
ta 54,237,16896
ta 48,236,16384
ta 50,236,16896
ta 60,235,20480
ta 62,235,20992
ta 56,234,20480
ta 58,234,20992
v_accvgpr_read_b32 v32,a128
zaaa 128,64,16
v_accvgpr_read_b32 v37,a133
v_accvgpr_read_b32 v36,a132
zaaa 112,68,16
dl 230,44,54,2048
zaaa 116,68,12
v_accvgpr_read_b32 v40,a136
v_accvgpr_read_b32 v44,a140
zaaa 132,64,12
cv 251,42,43
cv 250,40,41
zaaa 136,64,8
cv 253,46,47
cv 252,44,45
zaaa 120,68,8
cv 126,28,29
cv 125,26,27
zaaa 124,68,4
dl 231,44,54,3072
zaaa 140,64,4
s_add_u32 m0,s61,4096
cv 249,38,39
cv 247,34,35
zvaa 40,72,4
s_add_u32 s54,s54,s57
ta 68,237,20480
cv 246,32,33
zvaa 44,76,4
s_add_u32 s61,s63,s98
ta 70,237,20992
cv 248,36,37
zvaa 52,76,8
s_add_u32 s63,s63,s99
ta 64,236,20480
cv 124,24,25
zvaa 48,72,8
dl 224,36,52,0
zvaa 56,72,12
ta 66,236,20992
cv 123,22,23
zvaa 60,76,12
cv 121,18,19
cv 120,16,17
zvaa 68,76,16
cv 122,20,21
cv 119,14,15
zvaa 64,72,16
cv 118,12,13
cv 117,10,11
zvaa 32,80,16
dl 225,36,52,1024
ta 76,235,24576
ta 78,235,25088
zvaa 36,84,16
ta 72,234,24576
ta 74,234,25088
zvaa 28,84,12
cv 116,8,9
cv 153,82,83
zvaa 24,80,12
cv 152,80,81
cv 155,86,87
zvaa 16,80,8
cv 154,84,85
cv 149,74,75
zvaa 20,84,8
dl 226,36,52,2048
zvaa 12,84,4
cv 148,72,73
v_add_u32_e32 v232,s99,v232
zvaa 8,80,4
cv 115,6,7
cv 114,4,5
zvaa 76,4,88
cv 113,2,3
cv 112,0,1
zvaa 96,4,92
zvaa 100,8,92
dl 227,36,52,3072
zvaa 80,8,88
s_add_u32 s52,s52,s60
zvaa 84,12,88
zvaa 104,12,92
zvaa 108,16,92
s_waitcnt vmcnt(23) lgkmcnt(0)
s_barrier
zvaa 88,16,88
buffer_store_dwordx4 v[250:253],v244,s[24:27],0 offen offset:1024 sc0 sc1
buffer_store_dwordx4 v[246:249],v244,s[24:27],0 offen offset:0 sc0 sc1
s_branch L_kvx
L_vtile:
s_waitcnt vmcnt(8)
s_barrier
ta 32,234,0
ta 34,234,512
ra 0,232,0
ta 36,235,0
ta 38,235,512
ra 4,232,2048
ra 8,232,4096
ra 12,232,6144
ra 16,232,8192
ra 20,232,10240
ra 24,232,12288
ra 28,232,14336
s_add_u32 m0,s62,32768
ta 40,236,0
buffer_load_dwordx4 v228,s[40:43],s53 offen offset:0 nt lds
ta 42,236,512
ta 44,237,0
ta 46,237,512
ta 48,234,4096
ta 50,234,4608
ta 52,235,4096
ta 54,235,4608
ta 56,236,4096
ta 58,236,4608
ta 60,237,4096
ta 62,237,4608
s_cmp_ge_u32 s63,65536
s_cselect_b32 s99,-2,1
s_lshl_b32 s99,s99,15
s_add_u32 s63,s63,s99
v_add_u32_e32 v232,s99,v232
s_mov_b32 s64,1
s_waitcnt lgkmcnt(15)
zaaa 128,0,32
zaaa 136,0,36
zaaa 140,4,36
zaaa 132,4,32
buffer_load_dwordx4 v229,s[40:43],s53 offen offset:1024 nt lds
zvaa 32,8,32
s_add_u32 s53,s53,s57
zvaa 40,8,36
zvaa 44,12,36
zvaa 36,12,32
buffer_load_dwordx4 v230,s[44:47],s54 offen offset:2048 nt lds
zvaa 96,16,32
zvaa 104,16,36
s_waitcnt lgkmcnt(0)
zvaa 108,20,36
zvaa 100,20,32
buffer_load_dwordx4 v231,s[44:47],s54 offen offset:3072 nt lds
zvaa 160,24,32
s_add_u32 m0,s98,36864
s_add_u32 s54,s54,s57
zvaa 168,24,36
zvaa 172,28,36
zvaa 164,28,32
dl 224,36,52,0
zvaa 180,28,40
zvaa 188,28,44
zvaa 184,24,44
zvaa 176,24,40
dl 225,36,52,1024
zvaa 116,20,40
zvaa 124,20,44
zvaa 120,16,44
zvaa 112,16,40
dl 226,36,52,2048
zvaa 52,12,40
s_waitcnt vmcnt(11)
s_barrier
zvaa 60,12,44
ta 96,234,16384
zvaa 56,8,44
ta 98,234,16896
zvaa 48,8,40
dl 227,36,52,3072
zaaa 148,4,40
s_add_u32 m0,s62,49152
ra 64,233,0
s_add_u32 s52,s52,s60
zaaa 156,4,44
ta 100,235,16384
zaaa 152,0,44
ta 102,235,16896
zaaa 144,0,40
buffer_load_dwordx4 v228,s[40:43],s53 offen offset:0 nt lds
zvaa 0,0,48
ra 68,233,2048
zvaa 8,0,52
ta 104,236,16384
zvaa 12,4,52
ta 106,236,16896
zvaa 4,4,48
buffer_load_dwordx4 v229,s[40:43],s53 offen offset:1024 nt lds
zvaa 64,8,48
s_add_u32 s53,s53,s58
ra 72,233,4096
zvaa 72,8,52
ta 108,237,16384
zvaa 76,12,52
ta 110,237,16896
zvaa 68,12,48
buffer_load_dwordx4 v230,s[44:47],s54 offen offset:2048 nt lds
zvaa 128,16,48
ra 76,233,6144
zvaa 136,16,52
ta 112,234,20480
zvaa 140,20,52
ta 114,234,20992
zvaa 132,20,48
buffer_load_dwordx4 v231,s[44:47],s54 offen offset:3072 nt lds
zvaa 192,24,48
)"
R"(s_add_u32 m0,s61,0
ra 80,233,8192
s_add_u32 s54,s54,s58
zvaa 200,24,52
ta 116,235,20480
zvaa 204,28,52
ta 118,235,20992
zvaa 196,28,48
dl 224,36,52,0
zvaa 212,28,56
ra 84,233,10240
zvaa 220,28,60
ta 120,236,20480
zvaa 216,24,60
ta 122,236,20992
zvaa 208,24,56
dl 225,36,52,1024
ra 88,233,12288
zvaa 148,20,56
ta 124,237,20480
zvaa 156,20,60
ta 126,237,20992
zvaa 152,16,60
ra 92,233,14336
zvaa 144,16,56
dl 226,36,52,2048
zvaa 84,12,56
zvaa 92,12,60
zvaa 88,8,60
zvaa 80,8,56
dl 227,36,52,3072
zvaa 20,4,56
s_add_u32 s52,s52,s56
zvaa 28,4,60
zvaa 24,0,60
zvaa 16,0,56
buffer_load_dword a184,v241,s[48:51],s55 offen
s_add_u32 s55,s55,s59
s_waitcnt vmcnt(9)
s_waitcnt lgkmcnt(0)
s_barrier
L_v_head:
maaa 128,64,96
ta 32,234,32768
maaa 136,64,100
s_add_u32 m0,s62,0
ta 34,234,33280
maaa 140,68,100
buffer_load_dwordx4 v228,s[40:43],s53 offen offset:0 nt lds
maaa 132,68,96
mvaa 32,72,96
ra 0,232,0
mvaa 40,72,100
mvaa 44,76,100
ta 36,235,32768
mvaa 36,76,96
buffer_load_dwordx4 v229,s[40:43],s53 offen offset:1024 nt lds
mvaa 96,80,96
s_add_u32 s53,s53,s57
mvaa 104,80,100
ta 38,235,33280
mvaa 108,84,100
mvaa 100,84,96
ra 4,232,2048
mvaa 160,88,96
buffer_load_dwordx4 v230,s[44:47],s54 offen offset:2048 nt lds
mvaa 168,88,100
mvaa 172,92,100
ta 40,236,32768
mvaa 164,92,96
mvaa 180,92,104
ta 42,236,33280
mvaa 188,92,108
buffer_load_dwordx4 v231,s[44:47],s54 offen offset:3072 nt lds
mvaa 184,88,108
s_add_u32 s54,s54,s57
mvaa 176,88,104
ra 8,232,4096
mvaa 116,84,104
mvaa 124,84,108
ta 44,237,32768
mvaa 120,80,108
s_add_u32 m0,s61,4096
mvaa 112,80,104
dl 224,36,52,0
mvaa 52,76,104
ta 46,237,33280
mvaa 60,76,108
mvaa 56,72,108
ra 12,232,6144
mvaa 48,72,104
maaa 148,68,104
ta 48,234,36864
maaa 156,68,108
dl 225,36,52,1024
maaa 152,64,108
ta 50,234,37376
maaa 144,64,104
mvaa 0,64,112
ra 16,232,8192
mvaa 8,64,116
mvaa 12,68,116
ta 52,235,36864
mvaa 4,68,112
dl 226,36,52,2048
mvaa 64,72,112
mvaa 72,72,116
ta 54,235,37376
mvaa 76,76,116
mvaa 68,76,112
ra 20,232,10240
mvaa 128,80,112
mvaa 136,80,116
dl 227,36,52,3072
mvaa 140,84,116
s_add_u32 s52,s52,s60
ta 56,236,36864
mvaa 132,84,112
v_add_u32_e32 v233,s99,v233
mvaa 192,88,112
ta 58,236,37376
s_sub_u32 s61,s63,s99
mvaa 200,88,116
s_add_u32 s61,s61,s98
mvaa 204,92,116
ra 24,232,12288
mvaa 196,92,112
buffer_load_dword a185,v241,s[48:51],s55 offen
s_add_u32 s55,s55,s59
mvaa 212,92,120
mvaa 220,92,124
ta 60,237,36864
mvaa 216,88,124
mvaa 208,88,120
ta 62,237,37376
mvaa 148,84,120
mvaa 156,84,124
ra 28,232,14336
mvaa 152,80,124
mvaa 144,80,120
mvaa 84,76,120
mvaa 92,76,124
mvaa 88,72,124
mvaa 80,72,120
mvaa 20,68,120
mvaa 28,68,124
mvaa 24,64,124
s_waitcnt vmcnt(10) lgkmcnt(0)
s_barrier
mvaa 16,64,120
maaa 128,0,32
ta 96,234,49152
maaa 136,0,36
s_add_u32 m0,s62,16384
ta 98,234,49664
maaa 140,4,36
buffer_load_dwordx4 v228,s[40:43],s53 offen offset:0 nt lds
maaa 132,4,32
mvaa 32,8,32
ra 64,233,0
mvaa 40,8,36
mvaa 44,12,36
ta 100,235,49152
mvaa 36,12,32
buffer_load_dwordx4 v229,s[40:43],s53 offen offset:1024 nt lds
mvaa 96,16,32
s_add_u32 s53,s53,s58
mvaa 104,16,36
ta 102,235,49664
mvaa 108,20,36
mvaa 100,20,32
ra 68,233,2048
mvaa 160,24,32
buffer_load_dwordx4 v230,s[44:47],s54 offen offset:2048 nt lds
mvaa 168,24,36
mvaa 172,28,36
ta 104,236,49152
mvaa 164,28,32
mvaa 180,28,40
ta 106,236,49664
mvaa 188,28,44
buffer_load_dwordx4 v231,s[44:47],s54 offen offset:3072 nt lds
mvaa 184,24,44
s_add_u32 s54,s54,s58
mvaa 176,24,40
ra 72,233,4096
mvaa 116,20,40
mvaa 124,20,44
ta 108,237,49152
mvaa 120,16,44
s_add_u32 m0,s61,0
mvaa 112,16,40
dl 224,36,52,0
mvaa 52,12,40
ta 110,237,49664
mvaa 60,12,44
mvaa 56,8,44
ra 76,233,6144
mvaa 48,8,40
maaa 148,4,40
ta 112,234,53248
maaa 156,4,44
dl 225,36,52,1024
maaa 152,0,44
ta 114,234,53760
maaa 144,0,40
mvaa 0,0,48
ra 80,233,8192
mvaa 8,0,52
mvaa 12,4,52
ta 116,235,53248
mvaa 4,4,48
dl 226,36,52,2048
mvaa 64,8,48
mvaa 72,8,52
ta 118,235,53760
mvaa 76,12,52
mvaa 68,12,48
ra 84,233,10240
mvaa 128,16,48
mvaa 136,16,52
dl 227,36,52,3072
mvaa 140,20,52
s_add_u32 s52,s52,s56
ta 120,236,53248
mvaa 132,20,48
s_cmp_ge_u32 s63,65536
s_cselect_b32 s99,-2,1
s_lshl_b32 s99,s99,15
mvaa 192,24,48
ta 122,236,53760
s_add_u32 s63,s63,s99
mvaa 200,24,52
v_add_u32_e32 v232,s99,v232
mvaa 204,28,52
ra 88,233,12288
mvaa 196,28,48
buffer_load_dword a186,v241,s[48:51],s55 offen
s_add_u32 s55,s55,s59
mvaa 212,28,56
mvaa 220,28,60
ta 124,237,53248
mvaa 216,24,60
mvaa 208,24,56
ta 126,237,53760
mvaa 148,20,56
mvaa 156,20,60
ra 92,233,14336
mvaa 152,16,60
mvaa 144,16,56
mvaa 84,12,56
mvaa 92,12,60
mvaa 88,8,60
mvaa 80,8,56
mvaa 20,4,56
mvaa 28,4,60
s_mov_b32 s50,s33
s_mov_b32 s59,s34
mvaa 24,0,60
s_waitcnt vmcnt(10) lgkmcnt(0)
s_barrier
mvaa 16,0,56
maaa 128,64,96
ta 32,234,0
maaa 136,64,100
s_add_u32 m0,s62,32768
ta 34,234,512
maaa 140,68,100
buffer_load_dwordx4 v228,s[40:43],s53 offen offset:0 nt lds
maaa 132,68,96
mvaa 32,72,96
ra 0,232,0
mvaa 40,72,100
mvaa 44,76,100
ta 36,235,0
mvaa 36,76,96
buffer_load_dwordx4 v229,s[40:43],s53 offen offset:1024 nt lds
mvaa 96,80,96
s_add_u32 s53,s53,s57
mvaa 104,80,100
ta 38,235,512
mvaa 108,84,100
mvaa 100,84,96
ra 4,232,2048
mvaa 160,88,96
buffer_load_dwordx4 v230,s[44:47],s54 offen offset:2048 nt lds
mvaa 168,88,100
mvaa 172,92,100
ta 40,236,0
mvaa 164,92,96
mvaa 180,92,104
ta 42,236,512
mvaa 188,92,108
buffer_load_dwordx4 v231,s[44:47],s54 offen offset:3072 nt lds
mvaa 184,88,108
s_add_u32 s54,s54,s57
mvaa 176,88,104
ra 8,232,4096
mvaa 116,84,104
mvaa 124,84,108
ta 44,237,0
mvaa 120,80,108
s_add_u32 m0,s61,4096
mvaa 112,80,104
dl 224,36,52,0
mvaa 52,76,104
ta 46,237,512
mvaa 60,76,108
mvaa 56,72,108
ra 12,232,6144
mvaa 48,72,104
maaa 148,68,104
ta 48,234,4096
maaa 156,68,108
dl 225,36,52,1024
maaa 152,64,108
ta 50,234,4608
maaa 144,64,104
mvaa 0,64,112
ra 16,232,8192
mvaa 8,64,116
mvaa 12,68,116
ta 52,235,4096
mvaa 4,68,112
dl 226,36,52,2048
mvaa 64,72,112
mvaa 72,72,116
ta 54,235,4608
mvaa 76,76,116
mvaa 68,76,112
ra 20,232,10240
mvaa 128,80,112
mvaa 136,80,116
dl 227,36,52,3072
mvaa 140,84,116
s_add_u32 s52,s52,s60
ta 56,236,4096
mvaa 132,84,112
v_add_u32_e32 v233,s99,v233
mvaa 192,88,112
ta 58,236,4608
s_sub_u32 s61,s63,s99
mvaa 200,88,116
s_add_u32 s61,s61,s98
mvaa 204,92,116
ra 24,232,12288
mvaa 196,92,112
buffer_load_dword a187,v241,s[48:51],s55 offen
s_add_u32 s55,s55,s59
mvaa 212,92,120
mvaa 220,92,124
ta 60,237,4096
mvaa 216,88,124
mvaa 208,88,120
ta 62,237,4608
mvaa 148,84,120
mvaa 156,84,124
ra 28,232,14336
mvaa 152,80,124
mvaa 144,80,120
mvaa 84,76,120
mvaa 92,76,124
mvaa 88,72,124
mvaa 80,72,120
mvaa 20,68,120
mvaa 28,68,124
s_cmp_eq_u32 s64,3
mvaa 24,64,124
s_waitcnt vmcnt(10) lgkmcnt(0)
s_barrier
mvaa 16,64,120
s_cbranch_scc1 L_v_exit
maaa 128,0,32
ta 96,234,16384
maaa 136,0,36
s_add_u32 m0,s62,49152
ta 98,234,16896
maaa 140,4,36
buffer_load_dwordx4 v228,s[40:43],s53 offen offset:0 nt lds
maaa 132,4,32
mvaa 32,8,32
ra 64,233,0
mvaa 40,8,36
mvaa 44,12,36
ta 100,235,16384
mvaa 36,12,32
buffer_load_dwordx4 v229,s[40:43],s53 offen offset:1024 nt lds
mvaa 96,16,32
s_add_u32 s53,s53,s58
mvaa 104,16,36
ta 102,235,16896
mvaa 108,20,36
mvaa 100,20,32
ra 68,233,2048
mvaa 160,24,32
buffer_load_dwordx4 v230,s[44:47],s54 offen offset:2048 nt lds
mvaa 168,24,36
mvaa 172,28,36
ta 104,236,16384
mvaa 164,28,32
mvaa 180,28,40
ta 106,236,16896
mvaa 188,28,44
buffer_load_dwordx4 v231,s[44:47],s54 offen offset:3072 nt lds
mvaa 184,24,44
s_add_u32 s54,s54,s58
mvaa 176,24,40
ra 72,233,4096
s_bitcmp1_b32 s65,s64
s_cbranch_scc1 L_v_ev
L_v_evr:
mvaa 116,20,40
mvaa 124,20,44
ta 108,237,16384
mvaa 120,16,44
s_add_u32 m0,s61,0
mvaa 112,16,40
dl 224,36,52,0
mvaa 52,12,40
ta 110,237,16896
mvaa 60,12,44
mvaa 56,8,44
ra 76,233,6144
mvaa 48,8,40
maaa 148,4,40
ta 112,234,20480
maaa 156,4,44
dl 225,36,52,1024
maaa 152,0,44
ta 114,234,20992
maaa 144,0,40
mvaa 0,0,48
ra 80,233,8192
mvaa 8,0,52
mvaa 12,4,52
ta 116,235,20480
mvaa 4,4,48
dl 226,36,52,2048
mvaa 64,8,48
mvaa 72,8,52
ta 118,235,20992
mvaa 76,12,52
mvaa 68,12,48
ra 84,233,10240
mvaa 128,16,48
mvaa 136,16,52
dl 227,36,52,3072
mvaa 140,20,52
s_add_u32 s52,s52,s56
ta 120,236,20480
mvaa 132,20,48
s_cmp_ge_u32 s63,65536
s_cselect_b32 s99,-2,1
s_lshl_b32 s99,s99,15
mvaa 192,24,48
ta 122,236,20992
s_add_u32 s63,s63,s99
mvaa 200,24,52
v_add_u32_e32 v232,s99,v232
mvaa 204,28,52
ra 88,233,12288
mvaa 196,28,48
buffer_load_dword a184,v241,s[48:51],s55 offen
s_add_u32 s55,s55,s59
mvaa 212,28,56
mvaa 220,28,60
ta 124,237,20480
mvaa 216,24,60
mvaa 208,24,56
ta 126,237,20992
mvaa 148,20,56
mvaa 156,20,60
ra 92,233,14336
mvaa 152,16,60
mvaa 144,16,56
mvaa 84,12,56
mvaa 92,12,60
mvaa 88,8,60
mvaa 80,8,56
mvaa 20,4,56
mvaa 28,4,60
s_lshl_b32 s86,s21,2
s_add_u32 s86,s86,s12
s_mul_i32 s86,s86,384
s_add_u32 s86,s86,4096
v_lshl_add_u32 v253,v244,3,s86
s_mov_b64 exec,7
global_load_dword a188,v253,s[96:97]
s_mov_b64 exec,-1
s_add_u32 s64,s64,1
mvaa 24,0,60
s_waitcnt vmcnt(10) lgkmcnt(0)
s_barrier
mvaa 16,0,56
s_branch L_v_head
L_v_exit:
global_load_dwordx4 a[160:163],v244,s[30:31] offset:0
s_add_u32 m0,s62,49152
maaa 128,0,32
s_cmp_lt_u32 s12,2
s_cselect_b32 s86,s22,s23
ta 96,234,16384
v_accvgpr_read_b32 v224,a242
maaa 136,0,36
s_and_b32 s87,s12,1
ta 98,234,16896
s_lshl_b32 s86,s86,11
maaa 140,4,36
s_lshl_b32 s87,s87,17
ra 64,233,0
s_add_u32 s86,s86,s87
maaa 132,4,32
s_mov_b64 s[36:37],s[68:69]
s_mov_b32 s38,4194304
s_mov_b32 s52,s86
mvaa 32,8,32
buffer_load_dwordx4 v228,s[40:43],s53 offen offset:0 nt lds
mvaa 40,8,36
s_mov_b32 s56,65536
ta 100,235,16384
v_accvgpr_read_b32 v225,a243
mvaa 44,12,36
s_lshl_b32 s88,s18,1
ta 102,235,16896
v_accvgpr_read_b32 v226,a244
mvaa 36,12,32
s_add_u32 s88,s88,s12
ra 68,233,2048
v_accvgpr_read_b32 v227,a245
mvaa 96,16,32
s_mov_b32 s91,196608
ra 72,233,4096
s_lshl_b32 s88,s88,7
mvaa 104,16,36
buffer_load_dwordx4 v229,s[40:43],s53 offen offset:1024 nt lds
mvaa 108,20,36
s_lshl_b32 s89,s18,6
ra 76,233,6144
s_mov_b32 s90,6291456
mvaa 100,20,32
s_cmp_lt_u32 s12,2
s_cselect_b32 s57,262144,s91
ra 80,233,8192
s_add_u32 s53,s53,s58
mvaa 160,24,32
s_cmp_lt_u32 s12,2
s_cselect_b32 s42,8388608,s90
ra 84,233,10240
v_accvgpr_read_b32 v228,a246
mvaa 168,24,36
s_cmp_lt_u32 s12,2
s_cselect_b32 s53,s88,s89
ra 88,233,12288
v_accvgpr_read_b32 v229,a247
mvaa 172,28,36
buffer_load_dwordx4 v230,s[44:47],s54 offen offset:2048 nt lds
mvaa 164,28,32
s_bitcmp1_b32 s21,0
s_cselect_b32 s90,s57,0
ra 92,233,14336
s_add_u32 s53,s53,s90
mvaa 180,28,40
s_cmp_lt_u32 s12,2
s_cselect_b64 s[40:41],s[72:73],s[80:81]
ta 104,236,16384
v_accvgpr_read_b32 v230,a248
mvaa 188,28,44
s_lshl_b32 s90,s90,1
ta 106,236,16896
s_mov_b32 s60,-65408
mvaa 184,24,44
s_and_b32 s89,s98,8192
ta 108,237,16384
s_add_u32 s91,s89,65536
mvaa 176,24,40
buffer_load_dwordx4 v231,s[44:47],s54 offen offset:3072 nt lds
mvaa 116,20,40
s_add_u32 m0,s98,65536
ta 110,237,16896
s_add_u32 s54,s54,s58
mvaa 124,20,44
s_cmp_eq_u32 s12,3
s_cselect_b64 s[44:45],s[68:69],s[40:41]
ta 112,234,20480
v_accvgpr_read_b32 v231,a249
mvaa 120,16,44
s_cmp_eq_u32 s12,3
s_cselect_b32 s46,0,s42
ta 114,234,20992
s_add_u32 s58,s57,s90
mvaa 112,16,40
s_cmp_eq_u32 s12,3
s_cselect_b32 s54,0,s53
ta 116,235,20480
s_sub_u32 s57,s57,s90
mvaa 52,12,40
global_load_dwordx4 a[168:171],v244,s[30:31] offset:2048
mvaa 60,12,44
s_add_u32 s90,s89,65536
ta 118,235,20992
s_bfe_u32 s88,s21,65539
mvaa 56,8,44
s_lshl_b32 s88,s88,1
ta 120,236,20480
s_lshl_b32 s87,s20,2
mvaa 48,8,40
s_add_u32 s88,s88,s87
ta 122,236,20992
s_and_b32 s87,s12,1
maaa 148,4,40
s_add_u32 s88,s88,s87
ta 124,237,20480
s_mul_i32 s88,s88,524288
maaa 156,4,44
global_load_dwordx4 a[176:179],v244,s[84:85] offset:0
maaa 152,0,44
s_and_b32 s87,s21,7
ta 126,237,20992
s_add_u32 s86,s88,33554432
maaa 144,0,40
s_lshl_b32 s87,s87,16
s_mov_b32 s26,32768
s_add_u32 s86,s86,s87
mvaa 0,0,48
s_lshr_b32 s87,s12,1
s_mov_b32 s27,131072
s_lshl_b32 s87,s87,15
mvaa 8,0,52
s_add_u32 s86,s86,s87
s_lshl_b32 s87,s12,3
s_add_u32 s61,s98,32768
mvaa 12,4,52
global_load_dwordx4 a[164:167],v244,s[30:31] offset:1024
mvaa 4,4,48
s_add_u32 s24,s8,s86
s_addc_u32 s25,s9,0
s_lshl_b32 s86,%1,5
s_add_u32 s86,s86,s87
mvaa 64,8,48
s_add_u32 s86,s86,83886080
s_mov_b32 s63,65536
s_mov_b32 s67,2048
mvaa 72,8,52
s_cmp_ge_u32 s63,65536
s_cselect_b32 s99,-2,1
s_lshl_b32 s99,s99,15
s_mov_b32 s66,8
mvaa 76,12,52
s_cmp_eq_u32 s12,0
s_cselect_b32 s65,96,0
s_mov_b32 s88,4096
s_mov_b32 s64,1
mvaa 68,12,48
global_load_dwordx4 a[172:175],v244,s[30:31] offset:3072
mvaa 128,16,48
s_mov_b32 s30,1048577
s_mov_b32 s31,268435712
s_or_b32 s65,s65,128
mvaa 136,16,52
s_bitcmp1_b32 s12,0
s_cselect_b64 vcc,-1,0
mvaa 140,20,52
mvaa 132,20,48
mvaa 192,24,48
global_load_dwordx4 a[180:183],v244,s[84:85] offset:1024
mvaa 200,24,52
mvaa 204,28,52
mvaa 196,28,48
mvaa 212,28,56
mvaa 220,28,60
dl 224,36,52,0
mvaa 216,24,60
mvaa 208,24,56
mvaa 148,20,56
mvaa 156,20,60
mvaa 152,16,60
dl 225,36,52,1024
mvaa 144,16,56
mvaa 84,12,56
s_waitcnt vmcnt(13) lgkmcnt(0)
s_barrier
mvaa 92,12,60
ta 32,234,32768
mvaa 88,8,60
ta 34,234,33280
mvaa 80,8,56
dl 226,36,52,2048
mvaa 20,4,56
ta 36,235,32768
mvaa 28,4,60
ta 38,235,33280
mvaa 24,0,60
ta 40,236,32768
mvaa 16,0,56
ta 42,236,33280
maaa 128,64,96
dl 227,36,52,3072
maaa 136,64,100
s_add_u32 s52,s52,s56
s_add_u32 m0,s98,69632
ta 44,237,32768
maaa 140,68,100
ta 46,237,33280
maaa 132,68,96
ta 48,234,36864
mvaa 32,72,96
ta 50,234,37376
mvaa 40,72,100
dl 224,36,52,0
mvaa 44,76,100
ta 52,235,36864
mvaa 36,76,96
ta 54,235,37376
mvaa 96,80,96
ra 20,232,43008
mvaa 104,80,100
ra 24,232,45056
mvaa 108,84,100
dl 225,36,52,1024
mvaa 100,84,96
ra 28,232,47104
mvaa 160,88,96
ra 16,232,40960
mvaa 168,88,100
ra 0,232,32768
mvaa 172,92,100
ra 4,232,34816
mvaa 164,92,96
dl 226,36,52,2048
mvaa 180,92,104
ra 12,232,38912
mvaa 188,92,108
ra 8,232,36864
v_add_u32_e32 v232,s90,v232
mvaa 184,88,108
ta 60,237,36864
mvaa 176,88,104
ta 62,237,37376
mvaa 116,84,104
dl 227,36,52,3072
mvaa 124,84,108
s_add_u32 m0,s62,0
ta 56,236,36864
s_add_u32 s52,s52,s60
mvaa 120,80,108
ta 58,236,37376
mvaa 112,80,104
mvaa 52,76,104
mvaa 60,76,108
dl 228,40,53,0
mvaa 56,72,108
mvaa 48,72,104
maaa 148,68,104
maaa 156,68,108
maaa 152,64,108
dl 229,40,53,1024
maaa 144,64,104
s_add_u32 s53,s53,s57
mvaa 0,64,112
mvaa 8,64,116
mvaa 12,68,116
mvaa 4,68,112
dl 230,44,54,2048
mvaa 64,72,112
mvaa 72,72,116
mvaa 76,76,116
mvaa 68,76,112
mvaa 128,80,112
dl 231,44,54,3072
mvaa 136,80,116
s_add_u32 m0,s62,16384
s_add_u32 s54,s54,s57
mvaa 140,84,116
mvaa 132,84,112
mvaa 192,88,112
mvaa 200,88,116
dl 228,40,53,0
mvaa 204,92,116
mvaa 196,92,112
mvaa 212,92,120
mvaa 220,92,124
mvaa 216,88,124
dl 229,40,53,1024
mvaa 208,88,120
s_waitcnt vmcnt(19) lgkmcnt(0)
s_barrier
mvaa 148,84,120
s_add_u32 s53,s53,s58
ta 96,234,49152
mvaa 156,84,124
ra 92,233,47104
mvaa 152,80,124
ta 98,234,49664
mvaa 144,80,120
dl 230,44,54,2048
mvaa 84,76,120
ra 88,233,45056
mvaa 92,76,124
ta 100,235,49152
mvaa 88,72,124
ta 102,235,49664
mvaa 80,72,120
ta 116,235,53248
mvaa 20,68,120
dl 231,44,54,3072
mvaa 28,68,124
s_add_u32 m0,s98,0
ta 118,235,53760
v_subrev_u32_e32 v235,s89,v235
mvaa 24,64,124
s_add_u32 s54,s54,s58
ta 104,236,49152
mvaa 16,64,120
ta 106,236,49664
maaa 128,0,32
ta 124,237,53248
maaa 136,0,36
dl 224,36,52,0
maaa 140,4,36
ta 126,237,53760
maaa 132,4,32
ta 120,236,53248
mvaa 32,8,32
ta 122,236,53760
v_subrev_u32_e32 v236,s89,v236
mvaa 40,8,36
ta 112,234,53248
mvaa 44,12,36
dl 225,36,52,1024
mvaa 36,12,32
ta 114,234,53760
v_subrev_u32_e32 v234,s89,v234
mvaa 52,12,40
ta 108,237,49152
mvaa 60,12,44
ta 110,237,49664
v_subrev_u32_e32 v237,s89,v237
mvaa 56,8,44
ra 84,233,43008
mvaa 48,8,40
dl 226,36,52,2048
maaa 148,4,40
ra 76,233,38912
maaa 156,4,44
ra 80,233,40960
maaa 152,0,44
ra 68,233,34816
maaa 144,0,40
ra 72,233,36864
mvaa 0,0,48
dl 227,36,52,3072
mvaa 8,0,52
s_add_u32 s52,s52,s56
s_add_u32 m0,s98,4096
ra 64,233,32768
mvaa 12,4,52
v_add_u32_e32 v233,s91,v233
mvaa 4,4,48
mvaa 20,4,56
mvaa 28,4,60
dl 224,36,52,0
mvaa 24,0,60
mvaa 16,0,56
mvaa 220,28,60
s_waitcnt lgkmcnt(2)
mvaa 220,92,124
mvaa 216,88,124
dl 225,36,52,1024
mvaa 216,24,60
mvaa 212,28,56
mvaa 212,92,120
mvaa 208,88,120
mvaa 208,24,56
dl 226,36,52,2048
s_waitcnt vmcnt(25)
zvaa 246,160,176
mvaa 204,28,52
cv 223,222,223
cv 222,220,221
mvaa 204,92,116
cv 221,218,219
zvaa 250,168,176
mvaa 200,88,116
cv 220,216,217
cv 219,214,215
mvaa 200,24,52
cv 218,212,213
cv 217,210,211
mvaa 196,28,48
dl 227,36,52,3072
mvaa 196,92,112
s_add_u32 m0,s62,32768
cv 216,208,209
cv 215,206,207
mvaa 192,88,112
s_add_u32 s52,s52,s60
cv 214,204,205
cv 213,202,203
mvaa 192,24,48
cv 212,200,201
s_waitcnt vmcnt(24)
mvaa 246,164,180
mvaa 250,172,180
mvaa 188,28,44
cv 211,198,199
cv 210,196,197
mvaa 188,92,108
dl 228,40,53,0
mvaa 184,88,108
cv 209,194,195
v_cndmask_b32_e64 v246,0,v246,s[30:31]
v_cndmask_b32_e64 v250,0,v250,s[30:31]
s_lshl_b64 s[30:31],s[30:31],1
v_cndmask_b32_e64 v246,v246,v247,s[30:31]
v_cndmask_b32_e64 v250,v250,v251,s[30:31]
s_lshl_b64 s[30:31],s[30:31],1
mvaa 184,24,44
v_cndmask_b32_e64 v246,v246,v248,s[30:31]
v_cndmask_b32_e64 v250,v250,v252,s[30:31]
s_lshl_b64 s[30:31],s[30:31],1
v_cndmask_b32_e64 v246,v246,v249,s[30:31]
v_cndmask_b32_e64 v250,v250,v253,s[30:31]
mvaa 180,28,40
v_add_f32_dpp v246,v246,v246 quad_perm:[1,0,3,2] row_mask:0xf bank_mask:0xf
v_add_f32_dpp v250,v250,v250 quad_perm:[1,0,3,2] row_mask:0xf bank_mask:0xf
mvaa 180,92,104
v_add_f32_dpp v246,v246,v246 quad_perm:[2,3,0,1] row_mask:0xf bank_mask:0xf
v_add_f32_dpp v250,v250,v250 quad_perm:[2,3,0,1] row_mask:0xf bank_mask:0xf
mvaa 176,88,104
dl 229,40,53,1024
mvaa 176,24,40
s_add_u32 s53,s53,s57
v_add_f32_dpp v246,v246,v246 row_half_mirror row_mask:0xf bank_mask:0xf
v_add_f32_dpp v250,v250,v250 row_half_mirror row_mask:0xf bank_mask:0xf
mvaa 172,28,36
v_add_f32_dpp v246,v246,v246 row_mirror row_mask:0xf bank_mask:0xf
v_add_f32_dpp v250,v250,v250 row_mirror row_mask:0xf bank_mask:0xf
mvaa 172,92,100
cv 208,192,193
cv 207,190,191
mvaa 168,88,100
v_permlane16_swap_b32_e32 v246,v250
mvaa 168,24,36
dl 230,44,54,2048
v_add_f32_e32 v246,v246,v250
v_mov_b32_e32 v250,v246
mvaa 164,28,32
cv 206,188,189
cv 205,186,187
mvaa 164,92,96
v_permlane32_swap_b32_e32 v246,v250
mvaa 160,88,96
v_add_f32_e32 v246,v246,v250
v_lshrrev_b32_e32 v250,6,v244
mvaa 160,24,32
v_mul_f32_e32 v246,0xbfb8aa3b,v246
cv 204,184,185
mvaa 156,20,60
dl 231,44,54,3072
ex 246
mvaa 156,84,124
s_add_u32 s54,s54,s57
v_add_f32_e32 v246,1.0,v246
cv 203,182,183
mvaa 152,80,124
v_rcp_f32_e32 v246,v246
mvaa 152,16,60
cv 202,180,181
cv 201,178,179
mvaa 148,20,56
cv 200,176,177
cv 199,174,175
mvaa 148,84,120
buffer_load_dword a242,v241,s[48:51],s55 offen
mvaa 144,80,120
s_add_u32 s55,s55,s59
cv 198,172,173
cv 197,170,171
mvaa 144,16,56
s_waitcnt vmcnt(17) lgkmcnt(0)
s_barrier
s_add_u32 m0,s62,49152
mvaa 140,20,52
cv 196,168,169
cv 195,166,167
mvaa 140,84,116
cv 194,164,165
cv 193,162,163
mvaa 136,80,116
dl 228,40,53,0
mvaa 136,16,52
cv 192,160,161
cv 191,158,159
mvaa 132,20,48
ra 4,232,0
cv 190,156,157
mvaa 132,84,112
ta 88,238,12288
cv 189,154,155
mvaa 128,80,112
)"
R"(ta 90,238,12800
cv 188,152,153
mvaa 128,16,48
dl 229,40,53,1024
mvaa 124,20,44
s_add_u32 s53,s53,s58
ta 92,239,12288
cv 187,150,151
mvaa 124,84,108
ta 94,239,12800
cv 186,148,149
mvaa 120,80,108
cv 185,146,147
cv 184,144,145
mvaa 120,16,44
cv 183,142,143
cv 182,140,141
mvaa 116,20,40
dl 230,44,54,2048
mvaa 116,84,104
cv 181,138,139
cv 180,136,137
mvaa 112,80,104
cv 179,134,135
cv 178,132,133
mvaa 112,16,40
cv 177,130,131
cv 176,128,129
mvaa 108,20,36
cv 175,126,127
cv 174,124,125
mvaa 108,84,100
dl 231,44,54,3072
mvaa 104,80,100
s_add_u32 m0,s98,32768
ta 44,235,0
cv 173,122,123
mvaa 104,16,36
s_add_u32 s54,s54,s58
ta 46,235,512
cv 172,120,121
mvaa 100,20,32
cv 171,118,119
cv 170,116,117
mvaa 100,84,96
ta 40,234,0
cv 169,114,115
mvaa 96,80,96
dl 224,36,52,0
mvaa 96,16,32
ta 42,234,512
cv 168,112,113
mvaa 92,12,60
cv 167,110,111
cv 166,108,109
mvaa 92,76,124
cv 165,106,107
cv 164,104,105
mvaa 88,72,124
ta 84,237,8192
cv 163,102,103
mvaa 88,8,60
dl 225,36,52,1024
mvaa 84,12,56
ta 86,237,8704
cv 162,100,101
mvaa 84,76,120
ta 80,236,8192
cv 161,98,99
mvaa 80,72,120
ra 16,232,6144
cv 160,96,97
mvaa 80,8,56
ta 82,236,8704
cv 159,94,95
mvaa 76,12,52
dl 226,36,52,2048
mvaa 76,76,116
ta 60,235,4096
cv 158,92,93
mvaa 72,72,116
ta 62,235,4608
cv 157,90,91
mvaa 72,8,52
ta 56,234,4096
cv 156,88,89
mvaa 68,12,48
ta 58,234,4608
cv 151,78,79
mvaa 68,76,112
dl 227,36,52,3072
mvaa 64,72,112
s_add_u32 m0,s62,0
ta 52,237,0
s_add_u32 s52,s52,s56
mvaa 64,8,48
ra 12,232,4096
cv 150,76,77
mvaa 60,76,108
ta 54,237,512
cv 147,70,71
mvaa 56,72,108
ra 8,232,2048
cv 146,68,69
mvaa 52,76,104
buffer_load_dword a243,v241,s[48:51],s55 offen
mvaa 48,72,104
s_add_u32 s55,s55,s59
ta 48,236,0
cv 145,66,67
mvaa 44,76,100
ta 50,236,512
cv 144,64,65
mvaa 40,72,100
cv 143,62,63
cv 142,60,61
mvaa 36,76,96
cv 141,58,59
cv 140,56,57
mvaa 32,72,96
buffer_load_dword a244,v241,s[48:51],s55 offen
mvaa 28,68,124
s_add_u32 s55,s55,s59
cv 139,54,55
cv 138,52,53
mvaa 24,64,124
cv 137,50,51
cv 136,48,49
mvaa 20,68,120
cv 135,46,47
cv 134,44,45
mvaa 16,64,120
cv 133,42,43
cv 132,40,41
mvaa 12,68,116
buffer_load_dword a245,v241,s[48:51],s55 offen
mvaa 8,64,116
s_add_u32 s55,s55,s59
ta 76,235,8192
cv 131,38,39
mvaa 4,68,112
ta 78,235,8704
cv 130,36,37
mvaa 0,64,112
ta 72,234,8192
cv 129,34,35
maaa 156,68,108
ta 74,234,8704
cv 128,32,33
maaa 152,64,108
s_mov_b64 exec,65537
buffer_store_dword v246,v250,s[8:11],s86 offen sc0 sc1
s_mov_b64 exec,-1
maaa 148,68,104
cv 127,30,31
v_accvgpr_read_b32 v63,a159
maaa 144,64,104
v_accvgpr_read_b32 v62,a158
v_accvgpr_read_b32 v61,a157
maaa 140,68,100
v_accvgpr_read_b32 v60,a156
v_accvgpr_read_b32 v59,a155
maaa 136,64,100
v_accvgpr_read_b32 v58,a154
v_accvgpr_read_b32 v57,a153
maaa 132,68,96
v_accvgpr_read_b32 v56,a152
cv 56,56,57
maaa 128,64,96
cv 57,58,59
cv 58,60,61
s_waitcnt lgkmcnt(2)
zaaa 236,40,4
ta 68,237,4096
ta 70,237,4608
zaaa 220,44,4
ta 64,236,4096
ta 66,236,4608
zaaa 216,44,8
cv 59,62,63
v_accvgpr_read_b32 v55,a151
zaaa 232,40,8
buffer_store_dwordx4 v[56:59],v244,s[24:27],0 offen offset:3072 sc0 sc1
zaaa 228,40,12
v_accvgpr_read_b32 v54,a150
v_accvgpr_read_b32 v53,a149
zaaa 212,44,12
v_accvgpr_read_b32 v52,a148
v_accvgpr_read_b32 v51,a147
zaaa 208,44,16
v_accvgpr_read_b32 v50,a146
v_accvgpr_read_b32 v49,a145
zaaa 224,40,16
v_accvgpr_read_b32 v48,a144
cv 48,48,49
zaaa 192,48,16
cv 49,50,51
cv 50,52,53
zaaa 176,52,16
cv 51,54,55
s_waitcnt vmcnt(26) lgkmcnt(0)
s_barrier
zaaa 180,52,12
dl 228,40,53,0
zaaa 196,48,12
ra 24,233,0
v_accvgpr_read_b32 v47,a143
zaaa 200,48,8
ra 28,233,2048
v_accvgpr_read_b32 v46,a142
v_accvgpr_read_b32 v45,a141
zaaa 184,52,8
ra 32,233,4096
ra 36,233,6144
zaaa 188,52,4
v_accvgpr_read_b32 v43,a139
v_accvgpr_read_b32 v42,a138
zaaa 204,48,4
dl 229,40,53,1024
ta 96,236,24576
zaaa 172,56,4
s_add_u32 s53,s53,s57
ta 98,236,25088
v_accvgpr_read_b32 v41,a137
zaaa 156,60,4
ta 100,237,24576
v_accvgpr_read_b32 v39,a135
zaaa 152,60,8
ta 102,237,25088
ta 104,238,28672
zaaa 168,56,8
v_accvgpr_read_b32 v38,a134
v_accvgpr_read_b32 v35,a131
zaaa 164,56,12
buffer_store_dwordx4 v[48:51],v244,s[24:27],0 offen offset:2048 sc0 sc1
ta 106,238,29184
zaaa 148,60,12
ta 108,239,28672
v_accvgpr_read_b32 v34,a130
zaaa 144,60,16
ta 110,239,29184
v_accvgpr_read_b32 v33,a129
zaaa 160,56,16
ta 44,235,16384
ta 46,235,16896
ta 40,234,16384
ta 42,234,16896
ta 52,237,16384
ta 54,237,16896
ta 48,236,16384
ta 50,236,16896
ta 60,235,20480
ta 62,235,20992
ta 56,234,20480
ta 58,234,20992
v_accvgpr_read_b32 v32,a128
zaaa 128,64,16
v_accvgpr_read_b32 v37,a133
v_accvgpr_read_b32 v36,a132
zaaa 112,68,16
dl 230,44,54,2048
zaaa 116,68,12
v_accvgpr_read_b32 v40,a136
v_accvgpr_read_b32 v44,a140
zaaa 132,64,12
cv 251,42,43
cv 250,40,41
zaaa 136,64,8
cv 253,46,47
cv 252,44,45
zaaa 120,68,8
cv 126,28,29
cv 125,26,27
zaaa 124,68,4
dl 231,44,54,3072
zaaa 140,64,4
s_add_u32 m0,s61,4096
cv 249,38,39
cv 247,34,35
zvaa 40,72,4
s_add_u32 s54,s54,s57
ta 68,237,20480
cv 246,32,33
zvaa 44,76,4
s_add_u32 s61,s63,s98
ta 70,237,20992
cv 248,36,37
zvaa 52,76,8
s_add_u32 s63,s63,s99
ta 64,236,20480
cv 124,24,25
zvaa 48,72,8
dl 224,36,52,0
zvaa 56,72,12
ta 66,236,20992
cv 123,22,23
zvaa 60,76,12
cv 121,18,19
cv 120,16,17
zvaa 68,76,16
cv 122,20,21
cv 119,14,15
zvaa 64,72,16
cv 118,12,13
cv 117,10,11
zvaa 32,80,16
dl 225,36,52,1024
ta 76,235,24576
ta 78,235,25088
zvaa 36,84,16
ta 72,234,24576
ta 74,234,25088
zvaa 28,84,12
cv 116,8,9
cv 153,82,83
zvaa 24,80,12
cv 152,80,81
cv 155,86,87
zvaa 16,80,8
cv 154,84,85
cv 149,74,75
zvaa 20,84,8
dl 226,36,52,2048
zvaa 12,84,4
cv 148,72,73
v_add_u32_e32 v232,s99,v232
zvaa 8,80,4
cv 115,6,7
cv 114,4,5
zvaa 76,4,88
cv 113,2,3
cv 112,0,1
zvaa 96,4,92
zvaa 100,8,92
dl 227,36,52,3072
zvaa 80,8,88
s_add_u32 s52,s52,s60
zvaa 84,12,88
zvaa 104,12,92
zvaa 108,16,92
s_waitcnt vmcnt(23) lgkmcnt(0)
s_barrier
zvaa 88,16,88
buffer_store_dwordx4 v[250:253],v244,s[24:27],0 offen offset:1024 sc0 sc1
buffer_store_dwordx4 v[246:249],v244,s[24:27],0 offen offset:0 sc0 sc1
L_kvx:
L_p_exit:
s_add_u32 m0,s62,16384
ra 4,232,0
s_lshl_b32 s86,s21,2
maaa 236,40,24
dl 228,40,53,0
maaa 220,44,24
s_add_u32 s86,s86,s12
ra 8,232,2048
s_lshl_b32 s86,s86,8
maaa 216,44,28
s_add_u32 s86,s86,L_p_exit-L_att_pc+2048
ra 12,232,4096
v_lshl_add_u32 v253,v244,3,s86
maaa 232,40,28
s_mov_b32 s67,4096
ra 16,232,6144
s_and_b32 s87,s12,1
maaa 228,40,32
s_cmp_lt_u32 s12,2
s_cselect_b32 s94,s22,s23
ta 80,236,40960
s_lshr_b32 s94,s94,6
maaa 212,44,32
s_add_u32 s94,s94,s87
ta 82,236,41472
v_mov_b32_e32 v248,-1
maaa 208,44,36
dl 229,40,53,1024
maaa 224,40,36
s_add_u32 s53,s53,s58
ta 84,237,40960
v_mov_b32_e32 v249,-1
maaa 192,48,36
s_lshl_b32 s87,s18,5
ta 86,237,41472
s_add_u32 s94,s94,s87
maaa 176,52,36
s_lshl_b32 s94,s94,11
ta 88,238,45056
s_add_u32 s94,s94,12582912
maaa 180,52,32
s_bfe_u32 s88,s21,65539
ta 90,238,45568
v_mov_b32_e32 v250,-1
maaa 196,48,32
s_lshl_b32 s87,s20,1
ta 92,239,45056
v_mov_b32_e32 v251,-1
maaa 200,48,28
dl 230,44,54,2048
maaa 184,52,28
s_add_u32 s88,s88,s87
ta 94,239,45568
v_add_u32_e32 v233,s99,v233
maaa 188,52,24
s_cmp_ge_u32 s63,65536
s_cselect_b32 s99,-2,1
ta 44,235,32768
s_lshl_b32 s99,s99,15
maaa 204,48,24
s_lshl_b32 s88,s88,7
ta 46,235,33280
v_add_u32_e32 v232,s99,v232
maaa 172,56,24
s_add_u32 s85,s88,83894272
ta 40,234,32768
v_mov_b32_e32 v247,1
maaa 156,60,24
s_lshl_b32 s91,s12,4
ta 42,234,33280
v_bfe_u32 v252,v244,4,4
maaa 152,60,28
dl 231,44,54,3072
maaa 168,56,28
s_add_u32 m0,s61,0
ta 52,237,32768
s_add_u32 s54,s54,s58
maaa 164,56,32
s_mov_b32 s64,0x55505555
ta 54,237,33280
v_bfe_u32 v241,v244,6,4
maaa 148,60,32
s_mov_b32 s65,0x50005500
ta 48,236,32768
v_and_b32_e32 v241,12,v241
maaa 144,60,36
s_mov_b32 s66,0x50000
ta 50,236,33280
v_sub_u32_e32 v252,v252,v241
maaa 160,56,36
s_cmp_lt_u32 s12,2
s_cselect_b32 s90,s22,s23
v_add_u32_e32 v252,s91,v252
v_mov_b32_e32 v241,0xf149f2ca
maaa 128,64,36
s_mov_b64 exec,3
global_load_dword a250,v253,s[96:97]
s_mov_b64 exec,-1
maaa 112,68,36
s_mul_i32 s33,s90,10240
ta 60,235,36864
v_add_u32_e32 v253,10240,v253
maaa 116,68,32
s_cmp_lt_u32 s12,2
s_cselect_b32 s91,32768,0
ta 62,235,37376
s_add_u32 s91,s91,98304
maaa 132,64,32
s_lshl_b32 s95,s12,12
ta 56,234,36864
v_accvgpr_read_b32 v255,a241
maaa 136,64,28
s_add_u32 s70,s95,32768
ta 58,234,37376
s_add_u32 s72,s95,65536
maaa 120,68,28
s_mov_b64 s[30:31],s[28:29]
s_mov_b64 s[28:29],s[78:79]
v_cvt_f32_i32_e32 v252,v252
maaa 124,68,24
buffer_store_dwordx4 v[112:115],v244,s[24:27],s67 offen offset:0 sc0 sc1
maaa 140,64,24
s_add_u32 s68,s95,0
v_cndmask_b32_e64 v112,v241,0,s[64:65]
s_mov_b32 s64,0xaaa0aaaa
mvaa 40,72,24
s_mov_b32 s65,0xa000aa00
ta 68,237,36864
v_cndmask_b32_e64 v113,v241,0,s[64:65]
mvaa 44,76,24
s_mov_b32 s64,0x55405554
ta 70,237,37376
s_mov_b32 s65,0x40005400
mvaa 52,76,28
s_add_u32 s69,s95,16384
ta 64,236,36864
v_cndmask_b32_e64 v114,v241,0,s[64:65]
mvaa 48,72,28
s_mov_b32 s64,0xaa80aaa8
ta 66,236,37376
s_mov_b32 s65,0x8000a800
mvaa 56,72,32
dl 224,36,52,0
mvaa 60,76,32
s_add_u32 s74,s95,98304
v_cndmask_b32_e64 v115,v241,0,s[64:65]
v_mul_f32_e32 v252,0x7149f2ca,v252
mvaa 68,76,36
s_add_u32 s71,s95,49152
s_add_u32 s73,s95,81920
v_and_b32_e32 v245,48,v244
mvaa 64,72,36
s_lshl_b32 s59,%1,2
v_lshlrev_b32_e32 v245,3,v245
s_lshl_b32 s84,%1,2
mvaa 32,96,36
s_add_u32 s59,s59,s12
ta 76,235,40960
s_add_u32 s75,s95,114688
mvaa 36,100,36
s_add_u32 s84,s84,s12
ta 78,235,41472
s_mul_i32 s84,s84,12288
mvaa 28,100,32
dl 225,36,52,1024
mvaa 24,96,32
s_sub_u32 s55,s14,s13
ta 72,234,40960
s_mov_b32 s48,0x38000000
mvaa 16,96,28
s_mov_b32 s81,1
ta 74,234,41472
s_mov_b32 s84,0
mvaa 20,100,28
mvaa 12,100,24
mvaa 8,96,24
mvaa 76,24,104
dl 226,36,52,2048
mvaa 96,24,108
mvaa 100,28,108
mvaa 80,28,104
mvaa 84,32,104
mvaa 104,32,108
mvaa 108,36,108
dl 227,36,52,3072
mvaa 88,36,104
s_waitcnt vmcnt(20) lgkmcnt(0)
s_barrier
maaa 236,40,4
s_add_u32 m0,s62,32768
ra 24,233,0
s_add_u32 s52,s52,s56
maaa 220,44,4
ra 28,233,2048
maaa 216,44,8
ta 96,236,57344
maaa 232,40,8
ta 98,236,57856
maaa 228,40,12
dl 228,40,53,0
maaa 212,44,12
ta 100,237,57344
maaa 208,44,16
ta 102,237,57856
maaa 224,40,16
ra 32,233,4096
maaa 192,48,16
ta 108,239,61440
maaa 176,52,16
ta 110,239,61952
maaa 180,52,12
buffer_store_dwordx4 v[248:251],v244,s[8:11],s94 offen offset:0 sc0 sc1
maaa 196,48,12
ra 36,233,6144
v_add_u32_e32 v233,s99,v233
maaa 200,48,8
ta 104,238,61440
maaa 184,52,8
ta 106,238,61952
maaa 188,52,4
ta 44,235,49152
maaa 204,48,4
ta 46,235,49664
maaa 172,56,4
buffer_store_dwordx4 v[116:119],v244,s[24:27],s67 offen offset:1024 sc0 sc1
maaa 156,60,4
ta 40,234,49152
maaa 152,60,8
ta 42,234,49664
maaa 168,56,8
ta 52,237,49152
maaa 164,56,12
ta 54,237,49664
maaa 148,60,12
ta 48,236,49152
maaa 144,60,16
dl 229,40,53,1024
maaa 160,56,16
s_add_u32 s53,s53,s57
ta 50,236,49664
maaa 128,64,16
ta 60,235,53248
maaa 112,68,16
ta 62,235,53760
maaa 116,68,12
ta 56,234,53248
maaa 132,64,12
ta 58,234,53760
maaa 136,64,8
dl 230,44,54,2048
maaa 120,68,8
maaa 124,68,4
maaa 140,64,4
mvaa 40,72,4
ta 68,237,53248
mvaa 44,76,4
ta 70,237,53760
mvaa 52,76,8
dl 231,44,54,3072
mvaa 48,72,8
s_add_u32 m0,s61,4096
ta 64,236,53248
s_add_u32 s54,s54,s57
mvaa 56,72,12
s_add_u32 s61,s63,s98
ta 66,236,53760
s_add_u32 s63,s63,s99
mvaa 60,76,12
s_cmp_ge_u32 s63,65536
s_cselect_b32 s99,-2,1
s_lshl_b32 s99,s99,15
mvaa 68,76,16
mvaa 64,72,16
mvaa 32,80,16
dl 224,36,52,0
mvaa 36,84,16
ta 76,235,57344
mvaa 28,84,12
ta 78,235,57856
mvaa 24,80,12
ta 72,234,57344
mvaa 16,80,8
ta 74,234,57856
mvaa 20,84,8
mvaa 12,84,4
dl 225,36,52,1024
mvaa 8,80,4
mvaa 76,4,88
mvaa 96,4,92
mvaa 100,8,92
mvaa 80,8,88
mvaa 84,12,88
dl 226,36,52,2048
mvaa 104,12,92
mvaa 108,16,92
s_waitcnt vmcnt(21) lgkmcnt(0)
s_barrier
mvaa 88,16,88
ra 4,232,0
maaa 236,40,24
ra 8,232,2048
maaa 220,44,24
ta 80,236,8192
maaa 216,44,28
buffer_store_dwordx4 v[120:123],v244,s[24:27],s67 offen offset:2048 sc0 sc1
maaa 232,40,28
ta 82,236,8704
maaa 228,40,32
ta 84,237,8192
maaa 212,44,32
ta 86,237,8704
maaa 208,44,36
ra 12,232,4096
maaa 224,40,36
ta 92,239,12288
maaa 192,48,36
buffer_store_dwordx4 v[248:251],v244,s[8:11],s94 offen offset:1024 sc0 sc1
maaa 176,52,36
ta 94,239,12800
v_and_b32_e32 v250,240,v244
maaa 180,52,32
ra 16,232,6144
v_add_u32_e32 v232,s99,v232
maaa 196,48,32
ta 88,238,12288
v_accvgpr_read_b32 v249,a240
maaa 200,48,28
ta 90,238,12800
maaa 184,52,28
ta 44,235,0
maaa 188,52,24
dl 227,36,52,3072
maaa 204,48,24
s_add_u32 m0,s62,49152
ta 46,235,512
s_add_u32 s52,s52,s60
maaa 172,56,24
ta 40,234,0
maaa 156,60,24
ta 42,234,512
maaa 152,60,28
ta 52,237,0
maaa 168,56,28
ta 54,237,512
maaa 164,56,32
dl 228,40,53,0
maaa 148,60,32
ta 48,236,0
maaa 144,60,36
ta 50,236,512
maaa 160,56,36
maaa 128,64,36
ta 60,235,4096
maaa 112,68,36
ta 62,235,4608
maaa 116,68,32
dl 229,40,53,1024
maaa 132,64,32
s_add_u32 s53,s53,s58
ta 56,234,4096
maaa 136,64,28
ta 58,234,4608
maaa 120,68,28
maaa 124,68,24
maaa 140,64,24
mvaa 40,72,24
dl 230,44,54,2048
mvaa 44,76,24
ta 68,237,4096
mvaa 52,76,28
ta 70,237,4608
mvaa 48,72,28
ta 64,236,4096
mvaa 56,72,32
ta 66,236,4608
mvaa 60,76,32
mvaa 68,76,36
buffer_store_dwordx4 v[124:127],v244,s[24:27],s67 offen offset:3072 sc0 sc1
mvaa 64,72,36
s_mov_b32 s67,8192
v_mov_b32_e32 v124,-1
v_mov_b32_e32 v125,-1
mvaa 32,96,36
ta 76,235,8192
v_mov_b32_e32 v126,-1
mvaa 36,100,36
ta 78,235,8704
v_mov_b32_e32 v127,-1
mvaa 28,100,32
ta 72,234,8192
mvaa 24,96,32
ta 74,234,8704
mvaa 16,96,28
dl 231,44,54,3072
mvaa 20,100,28
s_add_u32 m0,s61,0
s_add_u32 s54,s54,s58
mvaa 12,100,24
mvaa 8,96,24
mvaa 76,24,104
mvaa 96,24,108
mvaa 100,28,108
dl 224,36,52,0
mvaa 80,28,104
mvaa 84,32,104
mvaa 104,32,108
mvaa 108,36,108
s_waitcnt vmcnt(18) lgkmcnt(0)
s_barrier
mvaa 88,36,104
ra 24,233,0
maaa 236,40,4
dl 225,36,52,1024
maaa 220,44,4
ra 28,233,2048
maaa 216,44,8
ta 96,236,24576
maaa 232,40,8
ta 98,236,25088
maaa 228,40,12
ta 100,237,24576
maaa 212,44,12
ta 102,237,25088
maaa 208,44,16
dl 226,36,52,2048
maaa 224,40,16
ra 32,233,4096
maaa 192,48,16
ta 108,239,28672
maaa 176,52,16
ta 110,239,29184
maaa 180,52,12
ra 36,233,6144
v_add_u32_e32 v233,s99,v233
maaa 196,48,12
ta 104,238,28672
maaa 200,48,8
dl 227,36,52,3072
maaa 184,52,8
s_add_u32 m0,s62,0
ta 106,238,29184
s_add_u32 s52,s52,s56
maaa 188,52,4
ta 44,235,16384
maaa 204,48,4
ta 46,235,16896
maaa 172,56,4
ta 40,234,16384
maaa 156,60,4
ta 42,234,16896
maaa 152,60,8
dl 228,40,53,0
maaa 168,56,8
ta 52,237,16384
maaa 164,56,12
ta 54,237,16896
maaa 148,60,12
ta 48,236,16384
maaa 144,60,16
ta 50,236,16896
maaa 160,56,16
maaa 128,64,16
buffer_store_dwordx4 v[128:131],v244,s[24:27],s67 offen offset:0 sc0 sc1
maaa 112,68,16
ta 60,235,20480
v_add_f32_e32 v128,0x70c9f2ca,v252
maaa 116,68,12
ta 62,235,20992
v_add_f32_e32 v129,0xf0c9f2ca,v252
maaa 132,64,12
ta 56,234,20480
v_add_f32_e32 v130,0xf1977617,v252
maaa 136,64,8
ta 58,234,20992
v_add_f32_e32 v131,0xf1fc6f7c,v252
maaa 120,68,8
v_min_f32_e32 v128,0xc1a00000,v128
v_min_f32_e32 v129,0xc1a00000,v129
maaa 124,68,4
dl 229,40,53,1024
maaa 140,64,4
s_add_u32 s53,s53,s57
v_min_f32_e32 v130,0xc1a00000,v130
v_min_f32_e32 v131,0xc1a00000,v131
mvaa 40,72,4
ta 68,237,20480
mvaa 44,76,4
ta 70,237,20992
mvaa 52,76,8
ta 64,236,20480
mvaa 48,72,8
ta 66,236,20992
mvaa 56,72,12
dl 230,44,54,2048
mvaa 60,76,12
mvaa 68,76,16
mvaa 64,72,16
mvaa 32,80,16
ta 76,235,24576
mvaa 36,84,16
ta 78,235,25088
mvaa 28,84,12
dl 231,44,54,3072
mvaa 24,80,12
s_add_u32 m0,s61,4096
ta 72,234,24576
s_add_u32 s54,s54,s57
mvaa 16,80,8
s_add_u32 s61,s63,s98
ta 74,234,25088
s_add_u32 s63,s63,s99
mvaa 20,84,8
s_cmp_ge_u32 s63,65536
s_cselect_b32 s99,-2,1
s_lshl_b32 s99,s99,15
mvaa 12,84,4
mvaa 8,80,4
mvaa 76,4,88
dl 224,36,52,0
mvaa 96,4,92
mvaa 100,8,92
mvaa 80,8,88
mvaa 84,12,88
mvaa 104,12,92
mvaa 108,16,92
dl 225,36,52,1024
mvaa 88,16,88
s_waitcnt vmcnt(16) lgkmcnt(0)
s_barrier
maaa 236,40,24
ra 4,232,0
maaa 220,44,24
ra 8,232,2048
maaa 216,44,28
ta 80,236,40960
maaa 232,40,28
ta 82,236,41472
maaa 228,40,32
buffer_load_dword v246,off,s[8:11],s35 offset:1024 sc1
maaa 212,44,32
ta 84,237,40960
maaa 208,44,36
ta 86,237,41472
maaa 224,40,36
ra 12,232,4096
maaa 192,48,36
ta 92,239,45056
maaa 176,52,36
ta 94,239,45568
maaa 180,52,32
buffer_store_dwordx4 v[132:135],v244,s[24:27],s67 offen offset:1024 sc0 sc1
maaa 196,48,32
ra 16,232,6144
v_add_u32_e32 v232,s99,v232
maaa 200,48,28
ta 88,238,45056
v_add_f32_e32 v132,0xf343a333,v252
maaa 184,52,28
ta 90,238,45568
v_add_f32_e32 v133,0xf3504260,v252
maaa 188,52,24
ta 44,235,32768
v_add_f32_e32 v134,0xf35ce18d,v252
maaa 204,48,24
ta 46,235,33280
v_min_f32_e32 v132,0xc1a00000,v132
maaa 172,56,24
dl 226,36,52,2048
maaa 156,60,24
ta 40,234,32768
v_add_f32_e32 v135,0xf36980b9,v252
maaa 152,60,28
ta 42,234,33280
v_min_f32_e32 v133,0xc1a00000,v133
maaa 168,56,28
ta 52,237,32768
v_min_f32_e32 v134,0xc1a00000,v134
maaa 164,56,32
ta 54,237,33280
v_min_f32_e32 v135,0xc1a00000,v135
maaa 148,60,32
ta 48,236,32768
maaa 144,60,36
dl 227,36,52,3072
maaa 160,56,36
s_add_u32 m0,s62,16384
ta 50,236,33280
s_add_u32 s52,s52,s60
maaa 128,64,36
ta 60,235,36864
maaa 112,68,36
ta 62,235,37376
maaa 116,68,32
ta 56,234,36864
maaa 132,64,32
ta 58,234,37376
maaa 136,64,28
dl 228,40,53,0
maaa 120,68,28
maaa 124,68,24
maaa 140,64,24
mvaa 40,72,24
ta 68,237,36864
mvaa 44,76,24
ta 70,237,37376
mvaa 52,76,28
dl 229,40,53,1024
mvaa 48,72,28
s_add_u32 s53,s53,s58
ta 64,236,36864
mvaa 56,72,32
ta 66,236,37376
mvaa 60,76,32
mvaa 68,76,36
mvaa 64,72,36
mvaa 32,96,36
dl 230,44,54,2048
mvaa 36,100,36
ta 76,235,40960
mvaa 28,100,32
ta 78,235,41472
mvaa 24,96,32
ta 72,234,40960
mvaa 16,96,28
ta 74,234,41472
mvaa 20,100,28
mvaa 12,100,24
buffer_store_dwordx4 v[136:139],v244,s[24:27],s67 offen offset:2048 sc0 sc1
mvaa 8,96,24
mvaa 76,24,104
v_add_f32_e32 v136,0xf3c6caff,v252
v_add_f32_e32 v137,0xf3cd1a95,v252
mvaa 96,24,108
v_add_f32_e32 v138,0xf3d36a2b,v252
v_add_f32_e32 v139,0xf3d9b9c2,v252
mvaa 100,28,108
v_min_f32_e32 v136,0xc1a00000,v136
v_min_f32_e32 v137,0xc1a00000,v137
mvaa 80,28,104
v_min_f32_e32 v138,0xc1a00000,v138
v_min_f32_e32 v139,0xc1a00000,v139
mvaa 84,32,104
dl 231,44,54,3072
mvaa 104,32,108
s_add_u32 m0,s61,0
s_add_u32 s54,s54,s58
mvaa 108,36,108
s_waitcnt vmcnt(16) lgkmcnt(0)
s_barrier
mvaa 88,36,104
ra 24,233,0
maaa 236,40,4
ra 28,233,2048
)"
R"(maaa 220,44,4
ta 96,236,57344
maaa 216,44,8
dl 224,36,52,0
maaa 232,40,8
ta 98,236,57856
maaa 228,40,12
ta 100,237,57344
maaa 212,44,12
ta 102,237,57856
maaa 208,44,16
ra 32,233,4096
maaa 224,40,16
ta 108,239,61440
maaa 192,48,16
dl 225,36,52,1024
maaa 176,52,16
ta 110,239,61952
maaa 180,52,12
ra 36,233,6144
v_add_u32_e32 v233,s99,v233
maaa 196,48,12
ta 104,238,61440
maaa 200,48,8
ta 106,238,61952
maaa 184,52,8
ta 44,235,49152
maaa 188,52,4
dl 226,36,52,2048
maaa 204,48,4
ta 46,235,49664
maaa 172,56,4
ta 40,234,49152
maaa 156,60,4
ta 42,234,49664
maaa 152,60,8
ta 52,237,49152
maaa 168,56,8
ta 54,237,49664
maaa 164,56,12
dl 227,36,52,3072
maaa 148,60,12
s_add_u32 m0,s62,32768
ta 48,236,49152
s_add_u32 s52,s52,s56
maaa 144,60,16
ta 50,236,49664
maaa 160,56,16
s_waitcnt vmcnt(12)
v_readfirstlane_b32 s87,v246
maaa 128,64,16
s_cmp_eq_u32 s12,0
s_cselect_b32 s89,s32,s87
ta 60,235,53248
maaa 112,68,16
s_cmp_lg_u32 s87,s89
s_cbranch_scc1 L_q_ack
L_q_acked:
ta 62,235,53760
s_and_b32 s87,s12,1
maaa 116,68,12
dl 228,40,53,0
maaa 132,64,12
s_lshl_b32 s88,s87,6
ta 56,234,53248
s_add_u32 s90,s90,s88
maaa 136,64,8
s_cmp_eq_u32 s90,0
s_cselect_b64 s[64:65],-1,0
ta 58,234,53760
s_lshl_b32 s88,s87,10
maaa 120,68,8
s_lshl_b32 s87,s87,14
s_add_u32 s33,s33,s88
s_add_u32 s91,s91,s87
maaa 124,68,4
s_lshr_b32 s87,s18,1
v_add_u32_e32 v251,s91,v244
s_lshl_b32 s87,s87,4
maaa 140,64,4
s_lshl_b32 s88,s18,11
s_lshr_b32 s91,s21,3
s_add_u32 s88,s33,s88
mvaa 40,72,4
buffer_store_dwordx4 v[140:143],v244,s[24:27],s67 offen offset:3072 sc0 sc1
mvaa 44,76,4
s_mov_b32 s67,12288
ta 68,237,53248
s_add_u32 s33,s88,63176704
mvaa 52,76,8
s_add_u32 s34,s88,63504384
ta 70,237,53760
s_add_u32 s50,s88,63832064
mvaa 48,72,8
s_add_u32 s51,s88,64159744
ta 64,236,53248
s_and_b32 s88,s20,4
mvaa 56,72,12
s_and_b32 s89,s20,3
ta 66,236,53760
s_add_u32 s91,s91,s88
mvaa 60,76,12
s_lshl_b32 s88,s89,6
v_add_f32_e32 v140,0xf415e232,v252
v_add_f32_e32 v141,0xf41909fd,v252
mvaa 68,76,16
dl 229,40,53,1024
mvaa 64,72,16
s_add_u32 s53,s53,s57
v_add_f32_e32 v142,0xf41c31c8,v252
v_min_f32_e32 v140,0xc1a00000,v140
mvaa 32,80,16
ta 76,235,57344
v_add_f32_e32 v143,0xf41f5993,v252
mvaa 36,84,16
ta 78,235,57856
v_bfe_i32 v252,v244,4,1
mvaa 28,84,12
ta 72,234,57344
v_min_f32_e32 v141,0xc1a00000,v141
mvaa 24,80,12
ta 74,234,57856
v_min_f32_e32 v142,0xc1a00000,v142
mvaa 16,80,8
dl 230,44,54,2048
mvaa 20,84,8
v_min_f32_e32 v143,0xc1a00000,v143
v_xor_b32_e32 v252,65535,v252
mvaa 12,84,4
v_lshrrev_b32_e32 v246,6,v244
v_lshl_add_u32 v245,v246,22,v245
mvaa 8,80,4
v_lshlrev_b32_e32 v246,16,v244
mvaa 76,4,88
mvaa 96,4,92
mvaa 100,8,92
dl 231,44,54,3072
mvaa 80,8,88
s_add_u32 m0,s61,4096
s_add_u32 s54,s54,s57
s_add_u32 s61,s63,s98
mvaa 84,12,88
s_add_u32 s63,s63,s99
mvaa 104,12,92
s_cmp_ge_u32 s63,65536
s_cselect_b32 s99,-2,1
s_lshl_b32 s99,s99,15
mvaa 108,16,92
mvaa 88,16,88
s_waitcnt lgkmcnt(0)
maaa 236,40,24
dl 224,36,52,0
maaa 220,44,24
maaa 216,44,28
maaa 232,40,28
s_waitcnt vmcnt(15)
s_barrier
maaa 228,40,32
ra 4,232,0
maaa 212,44,32
ra 8,232,2048
maaa 208,44,36
dl 225,36,52,1024
maaa 224,40,36
ta 80,236,8192
maaa 192,48,36
ta 82,236,8704
maaa 176,52,36
ta 84,237,8192
maaa 180,52,32
ta 86,237,8704
maaa 196,48,32
ra 12,232,4096
maaa 200,48,28
dl 226,36,52,2048
maaa 184,52,28
ta 92,239,12288
maaa 188,52,24
ra 16,232,6144
v_add_u32_e32 v232,s99,v232
maaa 204,48,24
ta 94,239,12800
maaa 172,56,24
ta 88,238,12288
maaa 156,60,24
ta 90,238,12800
maaa 152,60,28
buffer_store_dwordx4 v[144:147],v244,s[24:27],s67 offen offset:0 sc0 sc1
maaa 168,56,28
ta 44,235,0
maaa 164,56,32
ta 46,235,512
maaa 148,60,32
ta 40,234,0
maaa 144,60,36
ta 42,234,512
maaa 160,56,36
ta 52,237,0
maaa 128,64,36
dl 227,36,52,3072
maaa 112,68,36
s_add_u32 m0,s62,49152
ta 54,237,512
s_add_u32 s52,s52,s60
maaa 116,68,32
ta 48,236,0
maaa 132,64,32
ta 50,236,512
maaa 136,64,28
ta 60,235,4096
maaa 120,68,28
ta 62,235,4608
maaa 124,68,24
dl 228,40,53,0
maaa 140,64,24
ta 56,234,4096
mvaa 40,72,24
ta 58,234,4608
mvaa 44,76,24
ta 68,237,4096
mvaa 52,76,28
ta 70,237,4608
mvaa 48,72,28
ta 64,236,4096
mvaa 56,72,32
dl 229,40,53,1024
mvaa 60,76,32
s_add_u32 s53,s53,s58
ta 66,236,4608
mvaa 68,76,36
mvaa 64,72,36
mvaa 32,96,36
ta 76,235,8192
mvaa 36,100,36
ta 78,235,8704
mvaa 28,100,32
dl 230,44,54,2048
mvaa 24,96,32
ta 72,234,8192
mvaa 16,96,28
ta 74,234,8704
mvaa 20,100,28
mvaa 12,100,24
mvaa 8,96,24
mvaa 76,24,104
dl 231,44,54,3072
mvaa 96,24,108
s_add_u32 m0,s61,0
s_add_u32 s54,s54,s58
mvaa 100,28,108
mvaa 80,28,104
mvaa 84,32,104
mvaa 104,32,108
mvaa 108,36,108
buffer_store_dwordx4 v[148:151],v244,s[24:27],s67 offen offset:1024 sc0 sc1
mvaa 88,36,104
s_waitcnt lgkmcnt(0)
maaa 236,40,4
maaa 220,44,4
s_waitcnt vmcnt(15)
s_barrier
maaa 216,44,8
ra 24,233,0
maaa 232,40,8
ra 28,233,2048
maaa 228,40,12
s_cmp_eq_u32 s12,0
s_cselect_b64 exec,1,0
buffer_atomic_add v247,off,s[8:11],s85 sc1
s_mov_b64 exec,-1
maaa 212,44,12
ta 96,236,24576
v_mov_b32_e32 v247,32
maaa 208,44,16
ta 98,236,25088
maaa 224,40,16
ta 100,237,24576
maaa 192,48,16
ta 102,237,25088
maaa 176,52,16
ra 32,233,4096
maaa 180,52,12
dl 224,36,52,0
maaa 196,48,12
ta 108,239,28672
maaa 200,48,8
ta 110,239,29184
maaa 184,52,8
ra 36,233,6144
v_add_u32_e32 v233,s99,v233
maaa 188,52,4
ta 104,238,28672
maaa 204,48,4
ta 106,238,29184
maaa 172,56,4
dl 225,36,52,1024
maaa 156,60,4
ta 44,235,16384
maaa 152,60,8
ta 46,235,16896
maaa 168,56,8
ta 40,234,16384
maaa 164,56,12
ta 42,234,16896
maaa 148,60,12
ta 52,237,16384
maaa 144,60,16
dl 226,36,52,2048
maaa 160,56,16
ta 54,237,16896
maaa 128,64,16
ta 48,236,16384
maaa 112,68,16
ta 50,236,16896
maaa 116,68,12
ta 60,235,20480
maaa 132,64,12
ta 62,235,20992
maaa 136,64,8
dl 227,36,52,3072
maaa 120,68,8
s_add_u32 m0,s62,0
ta 56,234,20480
s_add_u32 s52,s52,s56
maaa 124,68,4
ta 58,234,20992
maaa 140,64,4
mvaa 40,72,4
ta 68,237,20480
mvaa 44,76,4
ta 70,237,20992
mvaa 52,76,8
dl 228,40,53,0
mvaa 48,72,8
ta 64,236,20480
mvaa 56,72,12
ta 66,236,20992
mvaa 60,76,12
mvaa 68,76,16
mvaa 64,72,16
mvaa 32,80,16
buffer_store_dwordx4 v[152:155],v244,s[24:27],s67 offen offset:2048 sc0 sc1
mvaa 36,84,16
ta 76,235,24576
mvaa 28,84,12
ta 78,235,25088
mvaa 24,80,12
ta 72,234,24576
mvaa 16,80,8
ta 74,234,25088
mvaa 20,84,8
mvaa 12,84,4
dl 229,40,53,1024
mvaa 8,80,4
s_add_u32 s53,s53,s57
mvaa 76,4,88
mvaa 96,4,92
mvaa 100,8,92
mvaa 80,8,88
mvaa 84,12,88
dl 230,44,54,2048
mvaa 104,12,92
mvaa 108,16,92
mvaa 88,16,88
s_waitcnt lgkmcnt(0)
maaa 236,40,24
maaa 220,44,24
maaa 216,44,28
dl 231,44,54,3072
maaa 232,40,28
s_add_u32 m0,s61,4096
s_add_u32 s54,s54,s57
s_add_u32 s61,s63,s98
maaa 228,40,32
s_add_u32 s63,s63,s99
maaa 212,44,32
s_waitcnt vmcnt(14)
s_barrier
maaa 208,44,36
s_cmp_ge_u32 s63,65536
s_cselect_b32 s99,-2,1
ra 4,232,0
s_lshl_b32 s99,s99,15
maaa 224,40,36
ra 8,232,2048
maaa 192,48,36
dl 224,36,52,0
maaa 176,52,36
ta 80,236,40960
maaa 180,52,32
ta 82,236,41472
maaa 196,48,32
ta 84,237,40960
maaa 200,48,28
ta 86,237,41472
maaa 184,52,28
ra 12,232,4096
maaa 188,52,24
dl 225,36,52,1024
maaa 204,48,24
ta 92,239,45056
maaa 172,56,24
ta 94,239,45568
maaa 156,60,24
ra 16,232,6144
v_add_u32_e32 v232,s99,v232
maaa 152,60,28
ta 88,238,45056
maaa 168,56,28
ta 90,238,45568
maaa 164,56,32
dl 226,36,52,2048
maaa 148,60,32
ta 44,235,32768
maaa 144,60,36
ta 46,235,33280
maaa 160,56,36
ta 40,234,32768
maaa 128,64,36
ta 42,234,33280
maaa 112,68,36
ta 52,237,32768
maaa 116,68,32
buffer_store_dwordx4 v[156:159],v244,s[24:27],s67 offen offset:3072 sc0 sc1
maaa 132,64,32
s_mov_b32 s67,16384
ta 54,237,33280
maaa 136,64,28
ta 48,236,32768
maaa 120,68,28
ta 50,236,33280
maaa 124,68,24
ta 60,235,36864
maaa 140,64,24
ta 62,235,37376
mvaa 40,72,24
dl 227,36,52,3072
mvaa 44,76,24
s_add_u32 m0,s62,16384
ta 56,234,36864
s_add_u32 s52,s52,s60
mvaa 52,76,28
ta 58,234,37376
mvaa 48,72,28
ta 68,237,36864
mvaa 56,72,32
ta 70,237,37376
mvaa 60,76,32
ta 64,236,36864
mvaa 68,76,36
dl 228,40,53,0
mvaa 64,72,36
ta 66,236,37376
mvaa 32,96,36
ta 76,235,40960
mvaa 36,100,36
ta 78,235,41472
mvaa 28,100,32
ta 72,234,40960
mvaa 24,96,32
ta 74,234,41472
mvaa 16,96,28
dl 229,40,53,1024
mvaa 20,100,28
s_add_u32 s53,s53,s58
mvaa 12,100,24
mvaa 8,96,24
mvaa 76,24,104
mvaa 96,24,108
mvaa 100,28,108
dl 230,44,54,2048
mvaa 80,28,104
mvaa 84,32,104
mvaa 104,32,108
mvaa 108,36,108
mvaa 88,36,104
s_waitcnt lgkmcnt(0)
maaa 236,40,4
dl 231,44,54,3072
maaa 220,44,4
s_add_u32 m0,s61,0
s_add_u32 s54,s54,s58
maaa 216,44,8
maaa 232,40,8
maaa 228,40,12
maaa 212,44,12
maaa 208,44,16
buffer_store_dwordx4 v[160:163],v244,s[24:27],s67 offen offset:0 sc0 sc1
maaa 224,40,16
maaa 192,48,16
maaa 176,52,16
maaa 180,52,12
s_waitcnt vmcnt(15)
s_barrier
maaa 196,48,12
dl 224,36,52,0
maaa 200,48,8
ra 24,233,0
maaa 184,52,8
ra 28,233,2048
maaa 188,52,4
ra 32,233,4096
maaa 204,48,4
ta 96,236,57344
maaa 172,56,4
dl 225,36,52,1024
maaa 156,60,4
ta 98,236,57856
maaa 152,60,8
ta 100,237,57344
maaa 168,56,8
ta 102,237,57856
maaa 164,56,12
ta 108,239,61440
maaa 148,60,12
ta 110,239,61952
maaa 144,60,16
dl 226,36,52,2048
maaa 160,56,16
ra 36,233,6144
v_add_u32_e32 v233,s99,v233
maaa 128,64,16
ta 104,238,61440
maaa 112,68,16
ta 106,238,61952
maaa 116,68,12
ta 44,235,49152
maaa 132,64,12
dl 227,36,52,3072
maaa 136,64,8
s_add_u32 m0,s62,32768
ta 46,235,49664
s_add_u32 s52,s52,s56
maaa 120,68,8
ta 40,234,49152
maaa 124,68,4
ta 42,234,49664
maaa 140,64,4
ta 52,237,49152
mvaa 40,72,4
dl 228,40,53,0
mvaa 44,76,4
ta 54,237,49664
mvaa 52,76,8
ta 48,236,49152
mvaa 48,72,8
ta 50,236,49664
mvaa 56,72,12
ta 60,235,53248
ta 62,235,53760
mvaa 60,76,12
dl 229,40,53,1024
mvaa 68,76,16
s_add_u32 s53,s53,s57
ta 56,234,53248
mvaa 64,72,16
ta 58,234,53760
mvaa 32,80,16
ta 68,237,53248
mvaa 36,84,16
ta 70,237,53760
ta 64,236,53248
mvaa 28,84,12
mvaa 24,80,12
dl 230,44,54,2048
ta 66,236,53760
mvaa 16,80,8
ta 76,235,57344
mvaa 20,84,8
ta 78,235,57856
mvaa 12,84,4
ta 72,234,57344
ta 74,234,57856
mvaa 8,80,4
mvaa 76,4,88
buffer_store_dwordx4 v[164:167],v244,s[24:27],s67 offen offset:1024 sc0 sc1
mvaa 96,4,92
mvaa 100,8,92
mvaa 80,8,88
mvaa 84,12,88
mvaa 104,12,92
dl 231,44,54,3072
mvaa 108,16,92
s_add_u32 m0,s61,4096
s_add_u32 s54,s54,s57
s_add_u32 s61,s63,s98
mvaa 88,16,88
s_add_u32 s63,s63,s99
s_waitcnt lgkmcnt(2)
maaa 236,40,24
s_cmp_ge_u32 s63,65536
s_cselect_b32 s99,-2,1
s_lshl_b32 s99,s99,15
maaa 220,44,24
maaa 216,44,28
dl 224,36,52,0
maaa 232,40,28
maaa 228,40,32
maaa 212,44,32
maaa 208,44,36
maaa 224,40,36
maaa 192,48,36
dl 225,36,52,1024
maaa 176,52,36
maaa 180,52,32
maaa 196,48,32
maaa 200,48,28
maaa 184,52,28
dl 226,36,52,2048
maaa 188,52,24
s_waitcnt vmcnt(17) lgkmcnt(0)
s_barrier
maaa 204,48,24
ra 4,232,0
maaa 172,56,24
ra 8,232,2048
maaa 156,60,24
ra 12,232,4096
maaa 152,60,28
dl 227,36,52,3072
maaa 168,56,28
s_add_u32 m0,s62,49152
ra 16,232,6144
s_add_u32 s52,s52,s60
maaa 164,56,32
ta 80,236,8192
v_add_u32_e32 v232,s99,v232
maaa 148,60,32
ta 82,236,8704
maaa 144,60,36
ta 84,237,8192
ta 86,237,8704
maaa 160,56,36
dl 228,40,53,0
maaa 128,64,36
ta 88,238,12288
maaa 112,68,36
ta 90,238,12800
maaa 116,68,32
ta 92,239,12288
maaa 132,64,32
ta 94,239,12800
ta 44,235,0
maaa 136,64,28
maaa 120,68,28
dl 229,40,53,1024
ta 46,235,512
maaa 124,68,24
s_add_u32 s53,s53,s58
ta 40,234,0
maaa 140,64,24
ta 42,234,512
mvaa 40,72,24
ta 52,237,0
mvaa 44,76,24
ta 54,237,512
ta 48,236,0
mvaa 52,76,28
buffer_store_dwordx4 v[168:171],v244,s[24:27],s67 offen offset:2048 sc0 sc1
mvaa 48,72,28
ta 50,236,512
mvaa 56,72,32
ta 60,235,4096
mvaa 60,76,32
ta 62,235,4608
mvaa 68,76,36
ta 56,234,4096
ta 58,234,4608
mvaa 64,72,36
dl 230,44,54,2048
mvaa 32,96,36
ta 68,237,4096
mvaa 36,100,36
ta 70,237,4608
mvaa 28,100,32
ta 64,236,4096
mvaa 24,96,32
ta 66,236,4608
ta 76,235,8192
mvaa 16,96,28
dl 231,44,54,3072
mvaa 20,100,28
s_add_u32 m0,s61,0
ta 78,235,8704
s_add_u32 s54,s54,s58
mvaa 12,100,24
ta 72,234,8192
ta 74,234,8704
mvaa 8,96,24
mvaa 76,24,104
mvaa 96,24,108
dl 224,36,52,0
mvaa 100,28,108
mvaa 80,28,104
mvaa 84,32,104
mvaa 104,32,108
mvaa 108,36,108
mvaa 88,36,104
dl 225,36,52,1024
s_waitcnt lgkmcnt(2)
maaa 236,40,4
maaa 220,44,4
maaa 216,44,8
maaa 232,40,8
maaa 228,40,12
dl 226,36,52,2048
maaa 212,44,12
maaa 208,44,16
maaa 224,40,16
maaa 192,48,16
s_waitcnt vmcnt(17) lgkmcnt(0)
s_barrier
maaa 176,52,16
dl 227,36,52,3072
maaa 180,52,12
s_add_u32 m0,s62,0
ra 24,233,0
s_add_u32 s52,s52,s56
maaa 196,48,12
ra 28,233,2048
maaa 200,48,8
ta 96,236,24576
maaa 184,52,8
ta 98,236,25088
maaa 188,52,4
buffer_store_dwordx4 v[172:175],v244,s[24:27],s67 offen offset:3072 sc0 sc1
maaa 204,48,4
s_mov_b32 s67,20480
ta 100,237,24576
maaa 172,56,4
ta 102,237,25088
maaa 156,60,4
ra 32,233,4096
maaa 152,60,8
ta 108,239,28672
maaa 168,56,8
ta 110,239,29184
maaa 164,56,12
dl 228,40,53,0
maaa 148,60,12
ra 36,233,6144
v_add_u32_e32 v233,s99,v233
maaa 144,60,16
ta 104,238,28672
maaa 160,56,16
ta 106,238,29184
maaa 128,64,16
ta 44,235,16384
maaa 112,68,16
s_mov_b64 exec,3
global_load_dword a250,v253,s[96:97]
s_mov_b64 exec,-1
maaa 116,68,12
ta 46,235,16896
maaa 132,64,12
ta 40,234,16384
maaa 136,64,8
ta 42,234,16896
maaa 120,68,8
ta 52,237,16384
maaa 124,68,4
dl 229,40,53,1024
maaa 140,64,4
s_add_u32 s53,s53,s57
ta 54,237,16896
mvaa 40,72,4
ta 48,236,16384
mvaa 44,76,4
ta 50,236,16896
mvaa 52,76,8
ta 60,235,20480
mvaa 48,72,8
dl 230,44,54,2048
mvaa 56,72,12
ta 62,235,20992
mvaa 60,76,12
ta 56,234,20480
mvaa 68,76,16
ta 58,234,20992
mvaa 64,72,16
ta 68,237,20480
ta 70,237,20992
mvaa 32,80,16
mvaa 36,84,16
dl 231,44,54,3072
ta 64,236,20480
mvaa 28,84,12
s_add_u32 m0,s61,4096
ta 66,236,20992
s_add_u32 s54,s54,s57
mvaa 24,80,12
s_add_u32 s61,s63,s98
ta 76,235,24576
s_add_u32 s63,s63,s99
mvaa 16,80,8
s_cmp_ge_u32 s63,65536
s_cselect_b32 s99,-2,1
ta 78,235,25088
s_lshl_b32 s99,s99,15
mvaa 20,84,8
ta 72,234,24576
ta 74,234,25088
mvaa 12,84,4
dl 224,36,52,0
mvaa 8,80,4
mvaa 76,4,88
mvaa 96,4,92
mvaa 100,8,92
mvaa 80,8,88
dl 225,36,52,1024
mvaa 84,12,88
mvaa 104,12,92
mvaa 108,16,92
mvaa 88,16,88
s_waitcnt vmcnt(17) lgkmcnt(0)
s_barrier
maaa 236,40,24
buffer_store_dwordx4 v[176:179],v244,s[24:27],s67 offen offset:0 sc0 sc1
maaa 220,44,24
ra 4,232,0
maaa 216,44,28
ra 8,232,2048
maaa 232,40,28
ta 80,236,40960
maaa 228,40,32
ta 82,236,41472
maaa 212,44,32
ta 84,237,40960
maaa 208,44,36
dl 226,36,52,2048
maaa 224,40,36
ta 86,237,41472
maaa 192,48,36
ra 12,232,4096
maaa 176,52,36
ta 92,239,45056
maaa 180,52,32
ta 94,239,45568
maaa 196,48,32
dl 227,36,52,3072
maaa 200,48,28
s_add_u32 m0,s62,16384
ra 16,232,6144
s_add_u32 s52,s52,s60
maaa 184,52,28
ta 88,238,45056
v_add_u32_e32 v232,s99,v232
maaa 188,52,24
ta 90,238,45568
maaa 204,48,24
ta 44,235,32768
maaa 172,56,24
dl 228,40,53,0
maaa 156,60,24
ta 46,235,33280
maaa 152,60,28
ta 40,234,32768
maaa 168,56,28
ta 42,234,33280
maaa 164,56,32
ta 52,237,32768
maaa 148,60,32
dl 229,40,53,1024
maaa 144,60,36
s_add_u32 s53,s53,s58
ta 54,237,33280
maaa 160,56,36
ta 48,236,32768
maaa 128,64,36
ta 50,236,33280
maaa 112,68,36
ta 60,235,36864
maaa 116,68,32
dl 230,44,54,2048
maaa 132,64,32
ta 62,235,37376
maaa 136,64,28
ta 56,234,36864
maaa 120,68,28
ta 58,234,37376
maaa 124,68,24
maaa 140,64,24
mvaa 40,72,24
dl 231,44,54,3072
mvaa 44,76,24
s_add_u32 m0,s61,0
ta 68,237,36864
s_add_u32 s54,s54,s58
mvaa 52,76,28
ta 70,237,37376
mvaa 48,72,28
ta 64,236,36864
mvaa 56,72,32
ta 66,236,37376
mvaa 60,76,32
dl 224,36,52,0
mvaa 68,76,36
mvaa 64,72,36
mvaa 32,96,36
ta 76,235,40960
mvaa 36,100,36
ta 78,235,41472
mvaa 28,100,32
buffer_store_dwordx4 v[180:183],v244,s[24:27],s67 offen offset:1024 sc0 sc1
mvaa 24,96,32
ta 72,234,40960
mvaa 16,96,28
ta 74,234,41472
mvaa 20,100,28
mvaa 12,100,24
mvaa 8,96,24
dl 225,36,52,1024
mvaa 76,24,104
mvaa 96,24,108
mvaa 100,28,108
mvaa 80,28,104
mvaa 84,32,104
mvaa 104,32,108
dl 226,36,52,2048
mvaa 108,36,108
s_waitcnt vmcnt(19) lgkmcnt(0)
s_barrier
mvaa 88,36,104
ra 24,233,0
maaa 236,40,4
ra 28,233,2048
maaa 220,44,4
ta 96,236,57344
maaa 216,44,8
dl 227,36,52,3072
maaa 232,40,8
s_add_u32 m0,s62,32768
ta 98,236,57856
s_add_u32 s52,s52,s56
maaa 228,40,12
ta 100,237,57344
maaa 212,44,12
ra 32,233,4096
maaa 208,44,16
ta 102,237,57856
maaa 224,40,16
dl 228,40,53,0
maaa 192,48,16
ta 108,239,61440
maaa 176,52,16
ta 110,239,61952
maaa 180,52,12
ra 36,233,6144
v_add_u32_e32 v233,s99,v233
maaa 196,48,12
ta 104,238,61440
maaa 200,48,8
dl 229,40,53,1024
maaa 184,52,8
s_add_u32 s53,s53,s57
ta 106,238,61952
maaa 188,52,4
ta 44,235,49152
maaa 204,48,4
ta 46,235,49664
maaa 172,56,4
ta 40,234,49152
maaa 156,60,4
ta 42,234,49664
maaa 152,60,8
dl 230,44,54,2048
maaa 168,56,8
ta 52,237,49152
maaa 164,56,12
ta 54,237,49664
maaa 148,60,12
ta 48,236,49152
maaa 144,60,16
ta 50,236,49664
maaa 160,56,16
buffer_store_dwordx4 v[184:187],v244,s[24:27],s67 offen offset:2048 sc0 sc1
maaa 128,64,16
ta 60,235,53248
maaa 112,68,16
ta 62,235,53760
maaa 116,68,12
ta 56,234,53248
maaa 132,64,12
ta 58,234,53760
maaa 136,64,8
dl 231,44,54,3072
maaa 120,68,8
s_add_u32 m0,s61,4096
s_add_u32 s54,s54,s57
s_add_u32 s61,s63,s98
maaa 124,68,4
s_add_u32 s63,s63,s99
maaa 140,64,4
s_cmp_ge_u32 s63,65536
s_cselect_b32 s99,-2,1
s_lshl_b32 s99,s99,15
ta 68,237,53248
mvaa 40,72,4
ta 70,237,53760
mvaa 44,76,4
)"
R"(dl 224,36,52,0
mvaa 52,76,8
ta 64,236,53248
mvaa 48,72,8
ta 66,236,53760
mvaa 56,72,12
mvaa 60,76,12
mvaa 68,76,16
mvaa 64,72,16
dl 225,36,52,1024
mvaa 32,80,16
ta 76,235,57344
ta 78,235,57856
mvaa 36,84,16
ta 72,234,57344
ta 74,234,57856
mvaa 28,84,12
mvaa 24,80,12
mvaa 16,80,8
dl 226,36,52,2048
mvaa 20,84,8
mvaa 12,84,4
mvaa 8,80,4
mvaa 76,4,88
mvaa 96,4,92
dl 227,36,52,3072
mvaa 100,8,92
s_add_u32 m0,s62,49152
s_add_u32 s52,s52,s60
mvaa 80,8,88
mvaa 84,12,88
mvaa 104,12,92
mvaa 108,16,92
s_waitcnt vmcnt(18) lgkmcnt(0)
s_barrier
mvaa 88,16,88
dl 228,40,53,0
maaa 236,40,24
ra 4,232,0
maaa 220,44,24
ra 8,232,2048
maaa 216,44,28
ta 80,236,8192
maaa 232,40,28
ta 82,236,8704
maaa 228,40,32
dl 229,40,53,1024
maaa 212,44,32
s_add_u32 s53,s53,s58
ta 84,237,8192
maaa 208,44,36
ta 86,237,8704
maaa 224,40,36
ra 12,232,4096
maaa 192,48,36
ta 92,239,12288
maaa 176,52,36
buffer_store_dwordx4 v[188:191],v244,s[24:27],s67 offen offset:3072 sc0 sc1
maaa 180,52,32
s_mov_b32 s67,24576
ta 94,239,12800
maaa 196,48,32
ra 16,232,6144
v_add_u32_e32 v232,s99,v232
maaa 200,48,28
ta 88,238,12288
maaa 184,52,28
ta 90,238,12800
maaa 188,52,24
dl 230,44,54,2048
maaa 204,48,24
ta 44,235,0
maaa 172,56,24
ta 46,235,512
maaa 156,60,24
ta 40,234,0
maaa 152,60,28
ta 42,234,512
maaa 168,56,28
dl 231,44,54,3072
maaa 164,56,32
s_add_u32 m0,s61,0
ta 52,237,0
s_add_u32 s54,s54,s58
maaa 148,60,32
ta 54,237,512
maaa 144,60,36
ta 48,236,0
maaa 160,56,36
ta 50,236,512
maaa 128,64,36
dl 224,36,52,0
maaa 112,68,36
ta 60,235,4096
maaa 116,68,32
ta 62,235,4608
maaa 132,64,32
ta 56,234,4096
maaa 136,64,28
ta 58,234,4608
maaa 120,68,28
maaa 124,68,24
dl 225,36,52,1024
maaa 140,64,24
mvaa 40,72,24
ta 68,237,4096
mvaa 44,76,24
ta 70,237,4608
mvaa 52,76,28
ta 64,236,4096
mvaa 48,72,28
dl 226,36,52,2048
mvaa 56,72,32
ta 66,236,4608
mvaa 60,76,32
mvaa 68,76,36
mvaa 64,72,36
mvaa 32,96,36
dl 227,36,52,3072
ta 76,235,8192
ta 78,235,8704
mvaa 36,100,36
s_add_u32 m0,s62,0
ta 72,234,8192
ta 74,234,8704
mvaa 28,100,32
s_add_u32 s52,s52,s56
mvaa 24,96,32
mvaa 16,96,28
mvaa 20,100,28
mvaa 12,100,24
buffer_store_dwordx4 v[192:195],v244,s[24:27],s67 offen offset:0 sc0 sc1
mvaa 8,96,24
mvaa 76,24,104
mvaa 96,24,108
mvaa 100,28,108
mvaa 80,28,104
mvaa 84,32,104
mvaa 104,32,108
mvaa 108,36,108
s_waitcnt vmcnt(19) lgkmcnt(0)
s_barrier
mvaa 88,36,104
dl 228,40,53,0
maaa 236,40,4
ra 24,233,0
maaa 220,44,4
ra 28,233,2048
maaa 216,44,8
ta 96,236,24576
maaa 232,40,8
ta 98,236,25088
maaa 228,40,12
ta 100,237,24576
v_accvgpr_read_b32 v0,a236
maaa 212,44,12
dl 229,40,53,1024
maaa 208,44,16
s_add_u32 s53,s53,s57
ta 102,237,25088
v_accvgpr_read_b32 v1,a237
maaa 224,40,16
ra 32,233,4096
v_accvgpr_read_b32 v2,a238
maaa 192,48,16
ta 108,239,28672
v_accvgpr_read_b32 v3,a239
maaa 176,52,16
ta 110,239,29184
v_accvgpr_read_b32 v4,a220
maaa 180,52,12
dl 230,44,54,2048
maaa 196,48,12
ra 36,233,6144
v_accvgpr_read_b32 v5,a221
maaa 200,48,8
ta 104,238,28672
v_accvgpr_read_b32 v6,a222
maaa 184,52,8
ta 106,238,29184
v_accvgpr_read_b32 v7,a223
maaa 188,52,4
ta 44,235,16384
v_add_u32_e32 v233,s99,v233
maaa 204,48,4
ta 46,235,16896
maaa 172,56,4
dl 231,44,54,3072
maaa 156,60,4
s_add_u32 m0,s61,4096
ta 40,234,16384
s_add_u32 s54,s54,s57
maaa 152,60,8
s_add_u32 s61,s63,s98
ta 42,234,16896
s_add_u32 s63,s63,s99
maaa 168,56,8
s_cmp_ge_u32 s63,65536
s_cselect_b32 s99,-2,1
ta 52,237,16384
s_lshl_b32 s99,s99,15
maaa 164,56,12
ta 54,237,16896
maaa 148,60,12
dl 224,36,52,0
maaa 144,60,16
ta 48,236,16384
maaa 160,56,16
ta 50,236,16896
maaa 128,64,16
ta 60,235,20480
maaa 112,68,16
ta 62,235,20992
maaa 116,68,12
dl 225,36,52,1024
maaa 132,64,12
ta 56,234,20480
maaa 136,64,8
ta 58,234,20992
maaa 120,68,8
maaa 124,68,4
maaa 140,64,4
buffer_store_dwordx4 v[196:199],v244,s[24:27],s67 offen offset:1024 sc0 sc1
mvaa 40,72,4
ta 68,237,20480
mvaa 44,76,4
ta 70,237,20992
mvaa 52,76,8
ta 64,236,20480
mvaa 48,72,8
ta 66,236,20992
mvaa 56,72,12
dl 226,36,52,2048
mvaa 60,76,12
mvaa 68,76,16
mvaa 64,72,16
mvaa 32,80,16
ta 76,235,24576
ta 78,235,25088
mvaa 36,84,16
ta 72,234,24576
ta 74,234,25088
mvaa 28,84,12
dl 227,36,52,3072
mvaa 24,80,12
s_add_u32 m0,s62,16384
s_add_u32 s52,s52,s60
mvaa 16,80,8
mvaa 20,84,8
mvaa 12,84,4
mvaa 8,80,4
mvaa 76,4,88
mvaa 96,4,92
mvaa 100,8,92
mvaa 80,8,88
mvaa 84,12,88
mvaa 104,12,92
mvaa 108,16,92
s_waitcnt vmcnt(19) lgkmcnt(0)
s_barrier
mvaa 88,16,88
dl 228,40,53,0
mvaa 0,40,24
ra 4,232,0
mvaa 4,44,24
ra 8,232,2048
maaa 216,44,28
ta 80,236,40960
maaa 232,40,28
ta 82,236,41472
maaa 228,40,32
dl 229,40,53,1024
maaa 212,44,32
s_add_u32 s53,s53,s58
ta 84,237,40960
v_accvgpr_read_b32 v92,a216
maaa 208,44,36
ra 12,232,4096
v_accvgpr_read_b32 v93,a217
maaa 224,40,36
ta 86,237,41472
v_accvgpr_read_b32 v94,a218
maaa 192,48,36
ta 92,239,45056
v_accvgpr_read_b32 v95,a219
maaa 176,52,36
dl 230,44,54,2048
maaa 180,52,32
ta 94,239,45568
v_accvgpr_read_b32 v72,a232
maaa 196,48,32
ra 16,232,6144
v_accvgpr_read_b32 v73,a233
maaa 200,48,28
ta 88,238,45056
v_accvgpr_read_b32 v74,a234
maaa 184,52,28
ta 90,238,45568
v_accvgpr_read_b32 v75,a235
maaa 188,52,24
ta 44,235,32768
v_add_u32_e32 v232,s99,v232
maaa 204,48,24
buffer_store_dwordx4 v[200:203],v244,s[24:27],s67 offen offset:2048 sc0 sc1
maaa 172,56,24
ta 46,235,33280
maaa 156,60,24
ta 40,234,32768
maaa 152,60,28
ta 42,234,33280
maaa 168,56,28
ta 52,237,32768
maaa 164,56,32
dl 231,44,54,3072
maaa 148,60,32
s_add_u32 m0,s61,0
ta 54,237,33280
s_add_u32 s54,s54,s58
maaa 144,60,36
ta 48,236,32768
maaa 160,56,36
ta 50,236,33280
maaa 128,64,36
ta 60,235,36864
maaa 112,68,36
dl 224,36,52,0
maaa 116,68,32
ta 62,235,37376
maaa 132,64,32
ta 56,234,36864
maaa 136,64,28
ta 58,234,37376
maaa 120,68,28
maaa 124,68,24
dl 225,36,52,1024
maaa 140,64,24
mvaa 40,72,24
ta 68,237,36864
mvaa 44,76,24
ta 70,237,37376
mvaa 52,76,28
ta 64,236,36864
mvaa 48,72,28
dl 226,36,52,2048
mvaa 56,72,32
ta 66,236,37376
mvaa 60,76,32
mvaa 68,76,36
mvaa 64,72,36
mvaa 32,96,36
ta 76,235,40960
mvaa 36,100,36
dl 227,36,52,3072
mvaa 28,100,32
s_add_u32 m0,s62,32768
ta 78,235,41472
s_add_u32 s52,s52,s56
mvaa 24,96,32
ta 72,234,40960
mvaa 16,96,28
ta 74,234,41472
mvaa 20,100,28
mvaa 12,100,24
mvaa 8,96,24
mvaa 76,24,104
mvaa 96,24,108
mvaa 100,28,108
mvaa 80,28,104
mvaa 84,32,104
mvaa 104,32,108
mvaa 108,36,108
buffer_store_dwordx4 v[204:207],v244,s[24:27],s67 offen offset:3072 sc0 sc1
mvaa 88,36,104
s_waitcnt vmcnt(20) lgkmcnt(0)
s_barrier
mvaa 0,40,4
s_mov_b32 s67,28672
ra 24,233,0
mvaa 4,44,4
ra 28,233,2048
mvaa 92,44,8
ta 96,236,57344
mvaa 72,40,8
dl 228,40,53,0
maaa 228,40,12
ta 98,236,57856
maaa 212,44,12
ta 100,237,57344
maaa 208,44,16
ta 102,237,57856
maaa 224,40,16
ra 32,233,4096
maaa 192,48,16
dl 229,40,53,1024
maaa 176,52,16
s_add_u32 s53,s53,s57
ta 108,239,61440
v_accvgpr_read_b32 v144,a228
maaa 180,52,12
ta 110,239,61952
v_accvgpr_read_b32 v145,a229
maaa 196,48,12
ra 36,233,6144
v_accvgpr_read_b32 v146,a230
maaa 200,48,8
ta 104,238,61440
v_accvgpr_read_b32 v147,a231
maaa 184,52,8
ta 106,238,61952
v_accvgpr_read_b32 v148,a212
maaa 188,52,4
dl 230,44,54,2048
maaa 204,48,4
ta 44,235,49152
v_accvgpr_read_b32 v149,a213
maaa 172,56,4
ta 46,235,49664
v_accvgpr_read_b32 v150,a214
maaa 156,60,4
ta 40,234,49152
v_accvgpr_read_b32 v151,a215
maaa 152,60,8
ta 42,234,49664
v_add_u32_e32 v233,s99,v233
maaa 168,56,8
dl 231,44,54,3072
maaa 164,56,12
s_add_u32 m0,s61,4096
ta 52,237,49152
s_add_u32 s54,s54,s57
maaa 148,60,12
s_add_u32 s61,s63,s98
ta 54,237,49664
s_add_u32 s63,s63,s99
maaa 144,60,16
s_cmp_ge_u32 s63,65536
s_cselect_b32 s99,-2,1
ta 48,236,49152
s_lshl_b32 s99,s99,15
maaa 160,56,16
ta 50,236,49664
maaa 128,64,16
dl 224,36,52,0
maaa 112,68,16
ta 60,235,53248
maaa 116,68,12
ta 62,235,53760
maaa 132,64,12
ta 56,234,53248
maaa 136,64,8
ta 58,234,53760
maaa 120,68,8
dl 225,36,52,1024
maaa 124,68,4
maaa 140,64,4
mvaa 40,72,4
ta 68,237,53248
mvaa 44,76,4
ta 70,237,53760
mvaa 52,76,8
dl 226,36,52,2048
mvaa 48,72,8
ta 64,236,53248
mvaa 56,72,12
ta 66,236,53760
mvaa 60,76,12
mvaa 68,76,16
mvaa 64,72,16
mvaa 32,80,16
buffer_store_dwordx4 v[208:211],v244,s[24:27],s67 offen offset:0 sc0 sc1
ta 76,235,57344
ta 78,235,57856
mvaa 36,84,16
ta 72,234,57344
ta 74,234,57856
mvaa 28,84,12
mvaa 24,80,12
mvaa 16,80,8
mvaa 20,84,8
dl 227,36,52,3072
mvaa 12,84,4
s_add_u32 m0,s62,49152
s_add_u32 s52,s52,s60
mvaa 8,80,4
mvaa 76,4,88
mvaa 96,4,92
mvaa 100,8,92
mvaa 80,8,88
mvaa 84,12,88
mvaa 104,12,92
mvaa 108,16,92
s_waitcnt vmcnt(19) lgkmcnt(0)
s_barrier
mvaa 88,16,88
dl 228,40,53,0
mvaa 0,40,24
ra 4,232,0
mvaa 4,44,24
ra 8,232,2048
mvaa 92,44,28
ta 80,236,8192
mvaa 72,40,28
ta 82,236,8704
mvaa 144,40,32
ta 84,237,8192
mvaa 148,44,32
dl 229,40,53,1024
maaa 208,44,36
s_add_u32 s53,s53,s58
ta 86,237,8704
maaa 224,40,36
ra 12,232,4096
maaa 192,48,36
ta 92,239,12288
maaa 176,52,36
ra 16,232,6144
v_accvgpr_read_b32 v156,a208
maaa 180,52,32
dl 230,44,54,2048
maaa 196,48,32
ta 94,239,12800
v_accvgpr_read_b32 v157,a209
maaa 200,48,28
ta 88,238,12288
v_accvgpr_read_b32 v158,a210
maaa 184,52,28
ta 90,238,12800
v_accvgpr_read_b32 v159,a211
maaa 188,52,24
ta 44,235,0
v_accvgpr_read_b32 v152,a224
maaa 204,48,24
dl 231,44,54,3072
maaa 172,56,24
s_add_u32 m0,s61,0
ta 46,235,512
v_accvgpr_read_b32 v153,a225
maaa 156,60,24
s_add_u32 s54,s54,s58
ta 40,234,0
v_accvgpr_read_b32 v154,a226
maaa 152,60,28
ta 42,234,512
v_accvgpr_read_b32 v155,a227
maaa 168,56,28
ta 52,237,0
v_add_u32_e32 v232,s99,v232
maaa 164,56,32
dl 224,36,52,0
maaa 148,60,32
ta 54,237,512
maaa 144,60,36
ta 48,236,0
maaa 160,56,36
ta 50,236,512
maaa 128,64,36
ta 60,235,4096
maaa 112,68,36
ta 62,235,4608
maaa 116,68,32
buffer_store_dwordx4 v[212:215],v244,s[24:27],s67 offen offset:1024 sc0 sc1
maaa 132,64,32
ta 56,234,4096
maaa 136,64,28
ta 58,234,4608
maaa 120,68,28
maaa 124,68,24
maaa 140,64,24
dl 225,36,52,1024
mvaa 40,72,24
ta 68,237,4096
mvaa 44,76,24
ta 70,237,4608
mvaa 52,76,28
ta 64,236,4096
mvaa 48,72,28
ta 66,236,4608
mvaa 56,72,32
dl 226,36,52,2048
mvaa 60,76,32
mvaa 68,76,36
mvaa 64,72,36
mvaa 32,96,36
ta 76,235,8192
ta 78,235,8704
mvaa 36,100,36
dl 227,36,52,3072
ta 72,234,8192
ta 74,234,8704
mvaa 28,100,32
s_add_u32 m0,s62,0
s_add_u32 s52,s52,s56
mvaa 24,96,32
mvaa 16,96,28
mvaa 20,100,28
mvaa 12,100,24
mvaa 8,96,24
mvaa 76,24,104
mvaa 96,24,108
mvaa 100,28,108
mvaa 80,28,104
mvaa 84,32,104
mvaa 104,32,108
mvaa 108,36,108
s_waitcnt vmcnt(19) lgkmcnt(0)
s_barrier
mvaa 88,36,104
dl 228,40,53,0
mvaa 0,40,4
ra 24,233,0
mvaa 4,44,4
ra 28,233,2048
mvaa 92,44,8
ta 96,236,24576
mvaa 72,40,8
ta 98,236,25088
mvaa 144,40,12
dl 229,40,53,1024
mvaa 148,44,12
s_add_u32 s53,s53,s57
ta 100,237,24576
mvaa 156,44,16
ta 102,237,25088
mvaa 152,40,16
ra 32,233,4096
maaa 192,48,16
ta 108,239,28672
maaa 176,52,16
buffer_store_dwordx4 v[216:219],v244,s[24:27],s67 offen offset:2048 sc0 sc1
maaa 180,52,12
ta 110,239,29184
maaa 196,48,12
ra 36,233,6144
v_add_u32_e32 v233,s99,v233
maaa 200,48,8
ta 104,238,28672
v_accvgpr_read_b32 v160,a192
maaa 184,52,8
ta 106,238,29184
v_accvgpr_read_b32 v161,a193
maaa 188,52,4
dl 230,44,54,2048
maaa 204,48,4
ta 44,235,16384
v_accvgpr_read_b32 v162,a194
maaa 172,56,4
ta 46,235,16896
v_accvgpr_read_b32 v163,a195
maaa 156,60,4
ta 40,234,16384
v_accvgpr_read_b32 v164,a176
maaa 152,60,8
ta 42,234,16896
v_accvgpr_read_b32 v165,a177
maaa 168,56,8
dl 231,44,54,3072
maaa 164,56,12
s_add_u32 m0,s61,4096
ta 52,237,16384
v_accvgpr_read_b32 v166,a178
maaa 148,60,12
s_add_u32 s54,s54,s57
ta 54,237,16896
v_accvgpr_read_b32 v167,a179
maaa 144,60,16
s_add_u32 s61,s63,s98
ta 48,236,16384
s_add_u32 s63,s63,s99
maaa 160,56,16
s_cmp_ge_u32 s63,65536
s_cselect_b32 s99,-2,1
ta 50,236,16896
s_lshl_b32 s99,s99,15
maaa 128,64,16
ta 60,235,20480
maaa 112,68,16
dl 224,36,52,0
maaa 116,68,12
ta 62,235,20992
maaa 132,64,12
ta 56,234,20480
maaa 136,64,8
ta 58,234,20992
maaa 120,68,8
maaa 124,68,4
dl 225,36,52,1024
maaa 140,64,4
mvaa 40,72,4
ta 68,237,20480
mvaa 44,76,4
ta 70,237,20992
mvaa 52,76,8
ta 64,236,20480
mvaa 48,72,8
dl 226,36,52,2048
mvaa 56,72,12
ta 66,236,20992
mvaa 60,76,12
mvaa 68,76,16
mvaa 64,72,16
mvaa 32,80,16
dl 227,36,52,3072
ta 76,235,24576
ta 78,235,25088
mvaa 36,84,16
s_add_u32 m0,s62,16384
ta 72,234,24576
ta 74,234,25088
mvaa 28,84,12
s_add_u32 s52,s52,s60
mvaa 24,80,12
mvaa 16,80,8
mvaa 20,84,8
mvaa 12,84,4
buffer_store_dwordx4 v[220:223],v244,s[24:27],s67 offen offset:3072 sc0 sc1
mvaa 8,80,4
s_mov_b32 s67,0x5550055
s_mov_b32 s26,2097152
v_cndmask_b32_e64 v116,v241,0,s[66:67]
mvaa 76,4,88
s_mov_b32 s66,0xa0000
s_mov_b32 s67,0xaaa00aa
v_cndmask_b32_e64 v117,v241,0,s[66:67]
mvaa 96,4,92
s_mov_b32 s66,0x150001
v_cndmask_b32_e64 v120,v116,v241,s[64:65]
s_mov_b32 s67,0x15550155
mvaa 100,8,92
s_mov_b32 s27,131072
v_cndmask_b32_e64 v118,v241,0,s[66:67]
v_cndmask_b32_e64 v121,v117,v241,s[64:65]
mvaa 80,8,88
s_mov_b32 s66,0x2a0002
s_mov_b32 s67,0x2aaa02aa
v_cndmask_b32_e64 v119,v241,0,s[66:67]
mvaa 84,12,88
s_lshr_b32 s66,s18,2
v_cndmask_b32_e64 v122,v118,v241,s[64:65]
v_cndmask_b32_e64 v123,v119,v241,s[64:65]
mvaa 104,12,92
s_lshl_b32 s66,s66,13
v_cmp_eq_u32_e64 s[64:65],s87,v250
v_lshlrev_b32_e32 v250,3,v250
mvaa 108,16,92
s_waitcnt vmcnt(19) lgkmcnt(0)
s_barrier
mvaa 88,16,88
dl 228,40,53,0
mvaa 0,40,24
s_lshl_b32 s67,s18,13
ra 4,232,0
v_add_u32_e32 v250,83894272,v250
mvaa 4,44,24
s_mov_b64 s[24:25],s[82:83]
ra 8,232,2048
v_cndmask_b32_e64 v242,31,-1,s[64:65]
mvaa 92,44,28
s_cmp_eq_u32 s12,0
s_cselect_b32 s82,0,0
ta 80,236,40960
v_and_b32_e32 v243,528,v242
mvaa 72,40,28
s_or_b32 s82,s82,s15
ta 82,236,41472
s_mov_b32 s83,0
mvaa 144,40,32
s_or_b32 s82,s82,16384
ta 84,237,40960
mvaa 148,44,32
dl 229,40,53,1024
mvaa 156,44,36
s_add_u32 s53,s53,s58
ta 86,237,41472
mvaa 152,40,36
ra 12,232,4096
mvaa 160,48,36
ta 92,239,45056
mvaa 164,52,36
ta 94,239,45568
maaa 180,52,32
dl 230,44,54,2048
maaa 196,48,32
ra 16,232,6144
v_add_u32_e32 v232,s99,v232
maaa 200,48,28
ta 88,238,45056
maaa 184,52,28
ta 90,238,45568
v_accvgpr_read_b32 v172,a180
maaa 188,52,24
ta 44,235,32768
v_accvgpr_read_b32 v173,a181
maaa 204,48,24
ta 46,235,33280
v_accvgpr_read_b32 v174,a182
maaa 172,56,24
dl 231,44,54,3072
maaa 156,60,24
s_add_u32 m0,s61,0
ta 40,234,32768
v_accvgpr_read_b32 v175,a183
maaa 152,60,28
s_add_u32 s54,s54,s58
ta 42,234,33280
v_accvgpr_read_b32 v168,a196
maaa 168,56,28
ta 52,237,32768
v_accvgpr_read_b32 v169,a197
maaa 164,56,32
ta 54,237,33280
v_accvgpr_read_b32 v170,a198
maaa 148,60,32
dl 224,36,52,0
maaa 144,60,36
ta 48,236,32768
v_accvgpr_read_b32 v171,a199
maaa 160,56,36
ta 50,236,33280
maaa 128,64,36
ta 60,235,36864
maaa 112,68,36
ta 62,235,37376
maaa 116,68,32
dl 225,36,52,1024
maaa 132,64,32
ta 56,234,36864
maaa 136,64,28
ta 58,234,37376
maaa 120,68,28
maaa 124,68,24
maaa 140,64,24
dl 226,36,52,2048
mvaa 40,72,24
ta 68,237,36864
mvaa 44,76,24
ta 70,237,37376
mvaa 52,76,28
ta 64,236,36864
mvaa 48,72,28
ta 66,236,37376
mvaa 56,72,32
mvaa 60,76,32
dl 227,36,52,3072
mvaa 68,76,36
s_add_u32 m0,s62,32768
s_add_u32 s52,s52,s56
mvaa 64,72,36
mvaa 32,96,36
ta 76,235,40960
ta 78,235,41472
mvaa 36,100,36
ta 72,234,40960
ta 74,234,41472
mvaa 28,100,32
mvaa 24,96,32
mvaa 16,96,28
mvaa 20,100,28
mvaa 12,100,24
mvaa 8,96,24
mvaa 76,24,104
mvaa 96,24,108
mvaa 100,28,108
mvaa 80,28,104
mvaa 84,32,104
mvaa 104,32,108
mvaa 108,36,108
s_waitcnt vmcnt(18) lgkmcnt(0)
s_barrier
mvaa 88,36,104
dl 228,40,53,0
mvaa 0,40,4
ra 24,233,0
mvaa 4,44,4
ra 28,233,2048
mvaa 92,44,8
ta 96,236,57344
mvaa 72,40,8
ta 98,236,57856
mvaa 144,40,12
dl 229,40,53,1024
mvaa 148,44,12
s_add_u32 s53,s53,s57
ta 100,237,57344
mvaa 156,44,16
ta 102,237,57856
mvaa 152,40,16
ra 32,233,4096
mvaa 160,48,16
ta 108,239,61440
mvaa 164,52,16
dl 230,44,54,2048
mvaa 172,52,12
ra 36,233,6144
v_add_u32_e32 v233,s99,v233
mvaa 168,48,12
ta 110,239,61952
maaa 200,48,8
ta 104,238,61440
maaa 184,52,8
ta 106,238,61952
maaa 188,52,4
ta 44,235,49152
maaa 204,48,4
dl 231,44,54,3072
maaa 172,56,4
s_add_u32 m0,s61,4096
ta 46,235,49664
v_accvgpr_read_b32 v176,a200
maaa 156,60,4
s_add_u32 s54,s54,s57
ta 40,234,49152
v_accvgpr_read_b32 v177,a201
maaa 152,60,8
s_add_u32 s61,s63,s98
ta 42,234,49664
v_accvgpr_read_b32 v178,a202
maaa 168,56,8
s_add_u32 s63,s63,s99
ta 52,237,49152
v_accvgpr_read_b32 v179,a203
maaa 164,56,12
s_mov_b64 exec,3
global_load_dword a250,v253,s[96:97]
s_mov_b64 exec,-1
maaa 148,60,12
s_cmp_ge_u32 s63,65536
s_cselect_b32 s99,-2,1
ta 54,237,49664
v_accvgpr_read_b32 v180,a184
maaa 144,60,16
s_lshl_b32 s99,s99,15
ta 48,236,49152
v_accvgpr_read_b32 v181,a185
maaa 160,56,16
ta 50,236,49664
v_accvgpr_read_b32 v182,a186
maaa 128,64,16
ta 60,235,53248
v_accvgpr_read_b32 v183,a187
maaa 112,68,16
dl 224,36,52,0
maaa 116,68,12
ta 62,235,53760
v_add_u32_e32 v253,24576,v253
maaa 132,64,12
ta 56,234,53248
maaa 136,64,8
ta 58,234,53760
maaa 120,68,8
maaa 124,68,4
dl 225,36,52,1024
maaa 140,64,4
mvaa 40,72,4
ta 68,237,53248
mvaa 44,76,4
ta 70,237,53760
mvaa 52,76,8
ta 64,236,53248
mvaa 48,72,8
ta 66,236,53760
mvaa 56,72,12
dl 226,36,52,2048
mvaa 60,76,12
mvaa 68,76,16
mvaa 64,72,16
mvaa 32,80,16
ta 76,235,57344
ta 78,235,57856
mvaa 36,84,16
dl 227,36,52,3072
ta 72,234,57344
ta 74,234,57856
mvaa 28,84,12
s_add_u32 m0,s62,49152
s_add_u32 s52,s52,s60
mvaa 24,80,12
mvaa 16,80,8
mvaa 20,84,8
mvaa 12,84,4
mvaa 8,80,4
mvaa 76,4,88
mvaa 96,4,92
mvaa 100,8,92
mvaa 80,8,88
mvaa 84,12,88
mvaa 104,12,92
mvaa 108,16,92
s_waitcnt vmcnt(17) lgkmcnt(0)
s_barrier
mvaa 88,16,88
s_cmp_eq_u32 s12,0
s_cselect_b64 exec,1,0
buffer_atomic_add v247,off,s[8:11],s85 sc1
s_mov_b64 exec,-1
mvaa 0,40,24
ra 4,232,0
v_bfe_u32 v247,v244,2,6
mvaa 4,44,24
ra 8,232,2048
mvaa 92,44,28
)"
R"(ta 80,236,8192
mvaa 72,40,28
ta 82,236,8704
mvaa 144,40,32
dl 228,40,53,0
mvaa 148,44,32
ta 84,237,8192
mvaa 156,44,36
ta 86,237,8704
mvaa 152,40,36
ra 12,232,4096
mvaa 160,48,36
ta 92,239,12288
mvaa 164,52,36
dl 229,40,53,1024
mvaa 172,52,32
s_add_u32 s53,s53,s58
ra 16,232,6144
v_add_u32_e32 v232,s99,v232
mvaa 168,48,32
ta 94,239,12800
mvaa 176,48,28
ta 88,238,12288
mvaa 180,52,28
ta 90,238,12800
maaa 188,52,24
dl 230,44,54,2048
maaa 204,48,24
ta 44,235,0
maaa 172,56,24
ta 46,235,512
maaa 156,60,24
ta 40,234,0
maaa 152,60,28
ta 42,234,512
v_accvgpr_read_b32 v188,a188
maaa 168,56,28
dl 231,44,54,3072
maaa 164,56,32
s_add_u32 m0,s61,0
ta 52,237,0
v_accvgpr_read_b32 v189,a189
maaa 148,60,32
s_add_u32 s54,s54,s58
ta 54,237,512
v_accvgpr_read_b32 v190,a190
maaa 144,60,36
ta 48,236,0
v_accvgpr_read_b32 v191,a191
maaa 160,56,36
ta 50,236,512
v_accvgpr_read_b32 v184,a204
maaa 128,64,36
dl 224,36,52,0
maaa 112,68,36
ta 60,235,4096
v_accvgpr_read_b32 v185,a205
maaa 116,68,32
ta 62,235,4608
v_accvgpr_read_b32 v186,a206
maaa 132,64,32
ta 56,234,4096
v_accvgpr_read_b32 v187,a207
maaa 136,64,28
ta 58,234,4608
maaa 120,68,28
dl 225,36,52,1024
maaa 124,68,24
maaa 140,64,24
mvaa 40,72,24
ta 68,237,4096
mvaa 44,76,24
ta 70,237,4608
mvaa 52,76,28
dl 226,36,52,2048
mvaa 48,72,28
ta 64,236,4096
mvaa 56,72,32
ta 66,236,4608
mvaa 60,76,32
mvaa 68,76,36
mvaa 64,72,36
dl 227,36,52,3072
mvaa 32,96,36
s_add_u32 m0,s62,0
ta 76,235,8192
ta 78,235,8704
mvaa 36,100,36
s_add_u32 s52,s52,s56
ta 72,234,8192
ta 74,234,8704
mvaa 28,100,32
mvaa 24,96,32
mvaa 16,96,28
s_waitcnt vmcnt(12)
s_mov_b64 exec,3
global_load_dword a250,v253,s[96:97]
s_mov_b64 exec,-1
mvaa 20,100,28
v_lshrrev_b32_e32 v253,1,v244
mvaa 12,100,24
mvaa 8,96,24
mvaa 76,24,104
mvaa 96,24,108
mvaa 100,28,108
mvaa 80,28,104
mvaa 84,32,104
mvaa 104,32,108
mvaa 108,36,108
s_waitcnt lgkmcnt(0)
s_barrier
mvaa 88,36,104
dl 228,40,53,0
mvaa 0,40,4
ra 24,233,0
mvaa 4,44,4
ra 28,233,2048
mvaa 92,44,8
ta 96,236,24576
mvaa 72,40,8
ta 98,236,25088
mvaa 144,40,12
dl 229,40,53,1024
mvaa 148,44,12
s_add_u32 s53,s53,s57
ta 100,237,24576
mvaa 156,44,16
ta 102,237,25088
mvaa 152,40,16
ra 32,233,4096
mvaa 160,48,16
ta 108,239,28672
mvaa 164,52,16
dl 230,44,54,2048
mvaa 172,52,12
ta 110,239,29184
mvaa 168,48,12
ra 36,233,6144
v_add_u32_e32 v233,s99,v233
mvaa 176,48,8
ta 104,238,28672
mvaa 180,52,8
ta 106,238,29184
mvaa 188,52,4
dl 231,44,54,3072
mvaa 184,48,4
s_add_u32 m0,s61,4096
ta 44,235,16384
s_add_u32 s54,s54,s57
maaa 172,56,4
s_add_u32 s61,s63,s98
ta 46,235,16896
s_add_u32 s63,s63,s99
maaa 156,60,4
s_cmp_ge_u32 s63,65536
s_cselect_b32 s99,-2,1
ta 40,234,16384
s_lshl_b32 s99,s99,15
maaa 152,60,8
ta 42,234,16896
v_accvgpr_read_b32 v192,a172
maaa 168,56,8
dl 224,36,52,0
maaa 164,56,12
ta 52,237,16384
v_accvgpr_read_b32 v193,a173
maaa 148,60,12
ta 54,237,16896
v_accvgpr_read_b32 v194,a174
maaa 144,60,16
ta 48,236,16384
v_accvgpr_read_b32 v195,a175
maaa 160,56,16
ta 50,236,16896
v_accvgpr_read_b32 v196,a156
maaa 128,64,16
dl 225,36,52,1024
maaa 112,68,16
ta 60,235,20480
v_accvgpr_read_b32 v197,a157
maaa 116,68,12
ta 62,235,20992
v_accvgpr_read_b32 v198,a158
maaa 132,64,12
ta 56,234,20480
v_accvgpr_read_b32 v199,a159
maaa 136,64,8
ta 58,234,20992
maaa 120,68,8
dl 226,36,52,2048
maaa 124,68,4
maaa 140,64,4
mvaa 40,72,4
ta 68,237,20480
mvaa 44,76,4
ta 70,237,20992
mvaa 52,76,8
dl 227,36,52,3072
mvaa 48,72,8
s_add_u32 m0,s62,16384
ta 64,236,20480
s_add_u32 s52,s52,s60
mvaa 56,72,12
ta 66,236,20992
mvaa 60,76,12
mvaa 68,76,16
mvaa 64,72,16
mvaa 32,80,16
ta 76,235,24576
ta 78,235,25088
mvaa 36,84,16
ta 72,234,24576
ta 74,234,25088
mvaa 28,84,12
mvaa 24,80,12
mvaa 16,80,8
mvaa 20,84,8
mvaa 12,84,4
mvaa 8,80,4
mvaa 76,4,88
mvaa 96,4,92
mvaa 100,8,92
mvaa 80,8,88
mvaa 84,12,88
mvaa 104,12,92
mvaa 108,16,92
s_waitcnt vmcnt(17) lgkmcnt(0)
s_barrier
mvaa 88,16,88
dl 228,40,53,0
mvaa 0,40,24
ra 4,232,0
mvaa 4,44,24
ra 8,232,2048
mvaa 92,44,28
ta 80,236,40960
mvaa 72,40,28
ta 82,236,41472
mvaa 144,40,32
dl 229,40,53,1024
mvaa 148,44,32
s_add_u32 s53,s53,s58
ta 84,237,40960
mvaa 156,44,36
ta 86,237,41472
mvaa 152,40,36
ra 12,232,4096
mvaa 160,48,36
ta 92,239,45056
mvaa 164,52,36
dl 230,44,54,2048
mvaa 172,52,32
ta 94,239,45568
mvaa 168,48,32
ra 16,232,6144
v_add_u32_e32 v232,s99,v232
mvaa 176,48,28
ta 88,238,45056
mvaa 180,52,28
ta 90,238,45568
mvaa 188,52,24
dl 231,44,54,3072
mvaa 184,48,24
s_add_u32 m0,s61,0
ta 44,235,32768
s_add_u32 s54,s54,s58
mvaa 192,56,24
ta 46,235,33280
mvaa 196,60,24
ta 40,234,32768
maaa 152,60,28
ta 42,234,33280
maaa 168,56,28
dl 224,36,52,0
maaa 164,56,32
ta 52,237,32768
maaa 148,60,32
ta 54,237,33280
maaa 144,60,36
ta 48,236,32768
v_accvgpr_read_b32 v204,a152
maaa 160,56,36
ta 50,236,33280
v_accvgpr_read_b32 v205,a153
maaa 128,64,36
dl 225,36,52,1024
maaa 112,68,36
ta 60,235,36864
v_accvgpr_read_b32 v206,a154
maaa 116,68,32
ta 62,235,37376
v_accvgpr_read_b32 v207,a155
maaa 132,64,32
ta 56,234,36864
v_accvgpr_read_b32 v200,a168
maaa 136,64,28
ta 58,234,37376
v_accvgpr_read_b32 v201,a169
maaa 120,68,28
dl 226,36,52,2048
maaa 124,68,24
v_accvgpr_read_b32 v202,a170
v_accvgpr_read_b32 v203,a171
maaa 140,64,24
mvaa 40,72,24
ta 68,237,36864
mvaa 44,76,24
ta 70,237,37376
mvaa 52,76,28
dl 227,36,52,3072
mvaa 48,72,28
s_add_u32 m0,s62,32768
ta 64,236,36864
s_add_u32 s52,s52,s56
mvaa 56,72,32
s_mul_i32 s56,s59,12288
ta 66,236,37376
mvaa 60,76,32
mvaa 68,76,36
mvaa 64,72,36
mvaa 32,96,36
ta 76,235,40960
ta 78,235,41472
mvaa 36,100,36
ta 72,234,40960
ta 74,234,41472
mvaa 28,100,32
mvaa 24,96,32
mvaa 16,96,28
mvaa 20,100,28
mvaa 12,100,24
mvaa 8,96,24
mvaa 76,24,104
mvaa 96,24,108
mvaa 100,28,108
mvaa 80,28,104
mvaa 84,32,104
mvaa 104,32,108
mvaa 108,36,108
s_waitcnt vmcnt(16) lgkmcnt(0)
s_barrier
mvaa 88,36,104
dl 228,40,53,0
mvaa 0,40,4
ra 24,233,0
mvaa 4,44,4
ra 28,233,2048
mvaa 92,44,8
ta 96,236,57344
mvaa 72,40,8
ta 98,236,57856
mvaa 144,40,12
dl 229,40,53,1024
mvaa 148,44,12
s_add_u32 s53,s53,s57
ta 100,237,57344
mvaa 156,44,16
ta 102,237,57856
mvaa 152,40,16
ra 32,233,4096
mvaa 160,48,16
ta 108,239,61440
mvaa 164,52,16
dl 230,44,54,2048
mvaa 172,52,12
ta 110,239,61952
mvaa 168,48,12
ra 36,233,6144
v_add_u32_e32 v233,s99,v233
mvaa 176,48,8
ta 104,238,61440
mvaa 180,52,8
ta 106,238,61952
mvaa 188,52,4
dl 231,44,54,3072
mvaa 184,48,4
s_add_u32 m0,s61,4096
ta 44,235,49152
s_add_u32 s54,s54,s57
mvaa 192,56,4
s_add_u32 s61,s63,s98
ta 46,235,49664
s_add_u32 s63,s63,s99
mvaa 196,60,4
s_mov_b32 s98,s94
ta 40,234,49152
s_mov_b32 s99,s98
mvaa 204,60,8
s_and_b32 s94,s12,1
ta 42,234,49664
s_lshl_b32 s94,s94,14
mvaa 200,56,8
dl 224,36,52,0
maaa 164,56,12
s_add_u32 s57,s56,4096
ta 52,237,49152
v_mov_b32_e32 v224,v244
maaa 148,60,12
ta 54,237,49664
maaa 144,60,16
ta 48,236,49152
maaa 160,56,16
ta 50,236,49664
v_accvgpr_read_b32 v208,a164
maaa 128,64,16
dl 225,36,52,1024
maaa 112,68,16
ta 60,235,53248
v_accvgpr_read_b32 v209,a165
maaa 116,68,12
ta 62,235,53760
v_accvgpr_read_b32 v210,a166
maaa 132,64,12
ta 56,234,53248
v_accvgpr_read_b32 v211,a167
maaa 136,64,8
ta 58,234,53760
v_accvgpr_read_b32 v212,a148
maaa 120,68,8
dl 226,36,52,2048
maaa 124,68,4
v_accvgpr_read_b32 v213,a149
v_accvgpr_read_b32 v214,a150
maaa 140,64,4
v_accvgpr_read_b32 v215,a151
v_add_u32_e32 v225,65536,v224
mvaa 40,72,4
ta 68,237,53248
v_mov_b32_e32 v226,1065369472
mvaa 44,76,4
ta 70,237,53760
mvaa 52,76,8
dl 227,36,52,3072
mvaa 48,72,8
s_add_u32 m0,s62,49152
ta 64,236,53248
s_add_u32 s52,s52,s60
mvaa 56,72,12
s_lshl_b32 s60,%1,7
ta 66,236,53760
s_mov_b64 s[36:37],s[28:29]
mvaa 60,76,12
s_mov_b32 s38,8388608
s_mov_b32 s39,131072
s_add_u32 s61,s60,83914752
mvaa 68,76,16
s_lshl_b32 s52,s12,12
mvaa 64,72,16
mvaa 32,80,16
ta 76,235,57344
ta 78,235,57856
mvaa 36,84,16
ta 72,234,57344
ta 74,234,57856
mvaa 28,84,12
mvaa 24,80,12
mvaa 16,80,8
mvaa 20,84,8
mvaa 12,84,4
mvaa 8,80,4
mvaa 76,4,88
mvaa 96,4,92
mvaa 100,8,92
mvaa 80,8,88
mvaa 84,12,88
mvaa 104,12,92
mvaa 108,16,92
s_waitcnt vmcnt(16) lgkmcnt(0)
s_barrier
mvaa 88,16,88
dl 228,40,53,0
mvaa 0,40,24
ra 4,232,0
mvaa 4,44,24
ra 8,232,2048
mvaa 92,44,28
ta 80,236,8192
mvaa 72,40,28
ta 82,236,8704
mvaa 144,40,32
dl 229,40,53,1024
mvaa 148,44,32
s_lshl_b32 s43,s19,15
ta 84,237,8192
s_add_u32 s42,s43,16384
mvaa 156,44,36
s_add_u32 s53,s53,s58
ta 86,237,8704
s_mov_b32 s40,858993459
mvaa 152,40,36
s_mov_b32 s41,858993459
ra 12,232,4096
v_cndmask_b32_e64 v228,0,v226,s[40:41]
mvaa 160,48,36
ta 92,239,12288
v_cndmask_b32_e64 v229,0,v226,s[40:41]
mvaa 164,52,36
buffer_load_dword v248,v250,s[8:11],0 offen sc1
mvaa 172,52,32
ta 94,239,12800
mvaa 168,48,32
ra 16,232,6144
mvaa 176,48,28
ta 88,238,12288
mvaa 180,52,28
ta 90,238,12800
mvaa 188,52,24
dl 230,44,54,2048
mvaa 184,48,24
ta 44,235,0
v_cndmask_b32_e64 v230,0,v226,s[40:41]
mvaa 192,56,24
ta 46,235,512
mvaa 196,60,24
ta 40,234,0
mvaa 204,60,28
ta 42,234,512
mvaa 200,56,28
dl 231,44,54,3072
mvaa 208,56,32
s_add_u32 s54,s54,s58
ta 52,237,0
s_mov_b32 s46,16384
mvaa 212,60,32
s_sub_u32 s47,32768,s43
ta 54,237,512
v_cndmask_b32_e64 v231,0,v226,s[40:41]
maaa 144,60,36
s_mov_b32 s40,0xf0000
ta 48,236,0
s_mov_b32 s41,0xfff00ff
maaa 160,56,36
s_add_u32 s47,s47,32768
ta 50,236,512
v_cndmask_b32_e64 v226,0,v252,s[40:41]
maaa 128,64,36
s_mov_b32 s40,0x3f0003
ta 60,235,4096
v_accvgpr_read_b32 v220,a144
maaa 112,68,36
s_mov_b32 s41,0x3fff03ff
ta 62,235,4608
v_accvgpr_read_b32 v221,a145
maaa 116,68,32
s_lshr_b32 s45,s15,1
ta 56,234,4096
v_accvgpr_read_b32 v222,a146
maaa 132,64,32
s_add_u32 s58,s56,8192
ta 58,234,4608
v_accvgpr_read_b32 v223,a147
maaa 136,64,28
s_ff1_i32_b32 s80,s45
v_accvgpr_read_b32 v216,a160
v_accvgpr_read_b32 v217,a161
maaa 120,68,28
s_sub_u32 s44,s56,2048
v_accvgpr_read_b32 v218,a162
v_accvgpr_read_b32 v219,a163
maaa 124,68,24
v_cndmask_b32_e64 v227,0,v252,s[40:41]
s_mov_b32 s40,0xfff0ffff
maaa 140,64,24
s_mov_b32 s41,0xf000ff00
mvaa 40,72,24
ta 68,237,4096
mvaa 44,76,24
ta 70,237,4608
mvaa 52,76,28
ta 64,236,4096
mvaa 48,72,28
ta 66,236,4608
mvaa 56,72,32
mvaa 60,76,32
mvaa 68,76,36
mvaa 64,72,36
mvaa 32,96,36
ta 76,235,8192
ta 78,235,8704
mvaa 36,100,36
ta 72,234,8192
ta 74,234,8704
mvaa 28,100,32
mvaa 24,96,32
mvaa 16,96,28
mvaa 20,100,28
mvaa 12,100,24
mvaa 8,96,24
mvaa 76,24,104
mvaa 96,24,108
mvaa 100,28,108
mvaa 80,28,104
mvaa 84,32,104
mvaa 104,32,108
mvaa 108,36,108
s_waitcnt vmcnt(13) lgkmcnt(0)
s_barrier
mvaa 88,36,104
ra 0,233,0
ta 144,234,16384
ta 146,234,16896
ta 164,235,16384
mvaa 0,40,4
ta 166,235,16896
mvaa 4,44,4
ra 28,233,2048
mvaa 92,44,8
ra 96,233,4096
mvaa 72,40,8
ta 168,236,16384
ta 170,236,16896
mvaa 144,40,12
ta 188,237,16384
mvaa 148,44,12
ta 190,237,16896
mvaa 156,44,16
ta 192,234,20480
mvaa 152,40,16
ta 194,234,20992
ta 212,235,20480
mvaa 160,48,16
ta 214,235,20992
mvaa 164,52,16
ta 216,236,20480
mvaa 172,52,12
ta 218,236,20992
mvaa 168,48,12
ta 236,237,20480
ta 238,237,20992
mvaa 176,48,8
ta 244,235,24576
mvaa 180,52,8
ta 246,235,25088
mvaa 188,52,4
ra 108,233,6144
mvaa 184,48,4
mvaa 192,56,4
ta 52,239,28672
ta 54,239,29184
mvaa 196,60,4
mvaa 204,60,8
mvaa 200,56,8
mvaa 208,56,12
mvaa 212,60,12
mvaa 220,60,16
mvaa 216,56,16
maaa 128,64,16
maaa 112,68,16
s_waitcnt vmcnt(0)
s_barrier
ra 20,232,32768
ta 148,234,32768
ta 150,234,33280
ra 24,233,32768
ta 152,234,49152
maaa 116,68,12
ta 154,234,49664
ta 156,235,49152
ta 158,235,49664
ta 160,235,32768
ta 162,235,33280
ra 32,232,34816
ra 36,233,34816
maaa 132,64,12
ra 100,232,36864
ra 104,233,36864
ta 172,236,32768
ta 174,236,33280
ta 176,236,49152
maaa 136,64,8
ta 178,236,49664
ta 180,237,49152
v_and_b32_e32 v248,v242,v248
v_cmp_ne_u32_e64 s[64:65],v243,v248
maaa 120,68,8
ta 182,237,49664
ta 184,237,32768
maaa 124,68,4
s_cmp_lg_u64 s[64:65],0
s_cbranch_scc1 L_q_spin
L_q_ok:
ta 186,237,33280
ta 196,234,36864
s_mov_b32 m0,s95
maaa 140,64,4
ds 249,8,42,0
ta 198,234,37376
ta 200,234,53248
mvaa 40,72,4
s_and_b32 s87,s60,7552
ta 202,234,53760
ta 204,235,53248
mvaa 44,76,4
s_add_u32 s53,s87,83898368
ta 206,235,53760
ta 208,235,36864
mvaa 52,76,8
s_lshl_b32 s87,s91,8
ta 210,235,37376
ta 220,236,36864
mvaa 48,72,8
s_add_u32 s54,s87,s88
ta 222,236,37376
ta 224,236,53248
mvaa 56,72,12
ds 249,8,42,1024
ta 226,236,53760
ta 228,237,53248
mvaa 60,76,12
s_lshl_b32 s87,s12,16
ta 230,237,53760
ta 232,237,36864
mvaa 68,76,16
s_add_u32 s87,s87,s54
ta 234,237,37376
ta 240,235,40960
mvaa 64,72,16
s_andn2_b32 s87,s87,64
ta 242,235,41472
ra 44,232,38912
mvaa 32,80,16
s_lshl_b32 s88,s66,5
ra 40,233,38912
ta 48,239,45056
mvaa 36,84,16
ds 249,8,42,2048
ta 50,239,45568
ta 60,239,61440
mvaa 28,84,12
s_lshl_b32 s86,s21,2
ta 62,239,61952
ta 56,238,61440
mvaa 24,80,12
s_add_u32 s63,s87,s88
ta 58,238,61952
ta 68,238,45056
mvaa 16,80,8
s_lshl_b32 s88,s67,5
ta 70,238,45568
ta 64,238,28672
mvaa 20,84,8
s_add_u32 s86,s86,s12
ta 66,238,29184
ta 76,237,24576
mvaa 12,84,4
ds 249,8,42,3072
ta 78,237,25088
ta 72,237,40960
mvaa 8,80,4
s_add_u32 s65,s87,s88
ta 74,237,41472
s_and_b32 s87,s20,4
mvaa 76,4,88
s_lshr_b32 s85,s86,1
ta 84,237,57344
ta 86,237,57856
s_lshl_b32 s87,s87,8
s_lshl_b32 s85,s85,11
s_and_b32 s89,s86,1
s_and_b32 s88,s20,3
s_add_u32 s85,s85,s87
mvaa 96,4,92
s_lshl_b32 s89,s89,9
s_lshr_b32 s49,s86,3
s_add_u32 s85,s85,s89
s_lshl_b32 s89,s88,20
s_add_u32 s85,s85,s89
s_lshl_b32 s49,s49,11
s_and_b32 s89,s86,7
s_add_u32 s49,s49,s87
mvaa 100,8,92
s_lshl_b32 s89,s89,7
ta 80,236,57344
ta 82,236,57856
s_add_u32 s49,s49,s89
s_lshl_b32 s89,s88,18
s_add_u32 s49,s49,s89
s_lshl_b32 s89,s12,6
s_lshl_b32 s35,s90,2
mvaa 80,8,88
s_add_u32 s89,s89,83886080
s_lshl_b32 s90,s22,2
s_add_u32 s90,s90,s89
ta 4,236,40960
ta 6,236,41472
mvaa 84,12,88
buffer_load_dword v240,v247,s[8:11],s90 offen offset:0 sc1
mvaa 104,12,92
s_lshl_b32 s91,s23,2
ta 8,236,24576
ta 10,236,25088
mvaa 108,16,92
s_add_u32 s91,s91,s89
s_mov_b32 s86,268435456
v_mov_b32_e32 v232,0xc1a00000
mvaa 88,16,88
s_add_u32 s35,s35,83886080
ta 12,234,24576
ta 14,234,25088
s_waitcnt lgkmcnt(8)
mvaa 0,144,0
s_bfe_u32 s87,s98,327691
s_cselect_b32 s87,2048,0
ta 92,234,40960
ta 94,234,41472
mvaa 0,148,20
buffer_load_dword v241,v247,s[8:11],s90 offen offset:256 sc1
ta 16,234,57344
ta 18,234,57856
ta 88,235,57344
ta 90,235,57856
mvaa 0,152,24
s_sub_u32 s98,s98,s87
v_mov_b32_e32 v233,0xc1a00000
v_mov_b32_e32 v234,0xc1a00000
mvaa 4,156,24
s_xor_b32 s62,s63,128
v_mov_b32_e32 v235,0xc1a00000
v_cndmask_b32_e64 v250,0,v252,s[40:41]
mvaa 4,160,20
s_mov_b32 s40,0xffc0fffc
s_mov_b32 s41,0xc000fc00
v_mul_f32_e32 v0,0x3e0293ee,v0
mvaa 4,164,0
s_xor_b32 s64,s65,128
v_mul_f32_e32 v1,0x3e0293ee,v1
v_mul_f32_e32 v2,0x3e0293ee,v2
mvaa 92,164,28
buffer_load_dword v242,v247,s[8:11],s91 offen offset:0 sc1
mvaa 92,160,32
v_mul_f32_e32 v3,0x3e0293ee,v3
cv 0,0,1
mvaa 92,156,36
v_mul_f32_e32 v4,0x3e0293ee,v4
cv 1,2,3
mvaa 72,152,36
v_mul_f32_e32 v5,0x3e0293ee,v5
s_waitcnt lgkmcnt(0)
s_barrier
s_mov_b32 m0,s70
mvaa 72,148,32
v_mul_f32_e32 v6,0x3e0293ee,v6
v_mul_f32_e32 v7,0x3e0293ee,v7
mvaa 72,144,28
ds 249,8,43,0
mvaa 144,144,96
cv 2,4,5
cv 3,6,7
mvaa 144,148,100
v_mul_f32_e32 v92,0x3e0293ee,v92
v_mul_f32_e32 v93,0x3e0293ee,v93
mvaa 144,152,104
v_mul_f32_e32 v94,0x3e0293ee,v94
v_mul_f32_e32 v95,0x3e0293ee,v95
mvaa 148,156,104
v_mul_f32_e32 v72,0x3e0293ee,v72
v_mul_f32_e32 v73,0x3e0293ee,v73
mvaa 148,160,100
ds 249,8,43,1024
mvaa 148,164,96
v_mul_f32_e32 v74,0x3e0293ee,v74
v_mul_f32_e32 v75,0x3e0293ee,v75
mvaa 156,164,108
cv 72,72,73
cv 73,74,75
mvaa 156,160,44
cv 74,92,93
cv 75,94,95
mvaa 156,156,40
v_mul_f32_e32 v144,0x3e0293ee,v144
v_mul_f32_e32 v145,0x3e0293ee,v145
mvaa 152,152,40
ds 249,8,43,2048
mvaa 152,148,44
v_mul_f32_e32 v146,0x3e0293ee,v146
v_mul_f32_e32 v147,0x3e0293ee,v147
mvaa 152,144,108
cv 144,144,145
v_mul_f32_e32 v148,0x3e0293ee,v148
mvaa 160,168,108
cv 145,146,147
v_mul_f32_e32 v149,0x3e0293ee,v149
mvaa 160,172,44
v_mul_f32_e32 v150,0x3e0293ee,v150
v_mul_f32_e32 v151,0x3e0293ee,v151
mvaa 160,176,40
ds 249,8,43,3072
mvaa 164,180,40
s_mov_b32 m0,s72
cv 146,148,149
cv 147,150,151
mvaa 164,184,44
v_mul_f32_e32 v156,0x3e0293ee,v156
v_mul_f32_e32 v157,0x3e0293ee,v157
mvaa 164,188,108
v_mul_f32_e32 v158,0x3e0293ee,v158
v_mul_f32_e32 v152,0x3e0293ee,v152
mvaa 172,188,96
v_mul_f32_e32 v159,0x3e0293ee,v159
v_mul_f32_e32 v153,0x3e0293ee,v153
mvaa 172,184,100
ds 255,8,42,0
mvaa 172,180,104
v_mul_f32_e32 v154,0x3e0293ee,v154
v_mul_f32_e32 v155,0x3e0293ee,v155
mvaa 168,176,104
cv 152,152,153
cv 153,154,155
mvaa 168,172,100
ds_write_b128 v251,v[0:3] offset:0
mvaa 168,168,96
cv 154,156,157
cv 155,158,159
mvaa 176,168,28
ds 255,8,42,1024
mvaa 176,172,32
v_mul_f32_e32 v160,0x3e0293ee,v160
v_mul_f32_e32 v161,0x3e0293ee,v161
mvaa 176,176,36
v_mul_f32_e32 v162,0x3e0293ee,v162
v_mul_f32_e32 v163,0x3e0293ee,v163
mvaa 180,180,36
cv 160,160,161
cv 161,162,163
mvaa 180,184,32
v_mul_f32_e32 v164,0x3e0293ee,v164
v_mul_f32_e32 v165,0x3e0293ee,v165
mvaa 180,188,28
ds 255,8,42,2048
ds_write_b128 v251,v[72:75] offset:4096
mvaa 188,188,0
v_mul_f32_e32 v166,0x3e0293ee,v166
v_mul_f32_e32 v167,0x3e0293ee,v167
mvaa 188,184,20
cv 162,164,165
cv 163,166,167
mvaa 188,180,24
v_mul_f32_e32 v172,0x3e0293ee,v172
v_mul_f32_e32 v173,0x3e0293ee,v173
mvaa 184,176,24
v_mul_f32_e32 v174,0x3e0293ee,v174
v_mul_f32_e32 v175,0x3e0293ee,v175
mvaa 184,172,20
ds 255,8,42,3072
mvaa 184,168,0
v_mul_f32_e32 v168,0x3e0293ee,v168
v_mul_f32_e32 v169,0x3e0293ee,v169
mvaa 192,192,0
ds_write_b128 v251,v[144:147] offset:8192
mvaa 192,196,20
v_mul_f32_e32 v170,0x3e0293ee,v170
v_mul_f32_e32 v171,0x3e0293ee,v171
mvaa 192,200,24
cv 168,168,169
cv 169,170,171
mvaa 196,204,24
buffer_load_dword v243,v247,s[8:11],s91 offen offset:256 sc1
mvaa 196,208,20
cv 170,172,173
cv 171,174,175
mvaa 196,212,0
v_mul_f32_e32 v176,0x3e0293ee,v176
v_mul_f32_e32 v177,0x3e0293ee,v177
mvaa 204,212,28
v_mul_f32_e32 v178,0x3e0293ee,v178
v_mul_f32_e32 v179,0x3e0293ee,v179
mvaa 204,208,32
ds_write_b128 v251,v[152:155] offset:12288
mvaa 204,204,36
cv 176,176,177
cv 177,178,179
mvaa 200,200,36
v_mul_f32_e32 v180,0x3e0293ee,v180
v_mul_f32_e32 v181,0x3e0293ee,v181
mvaa 200,196,32
v_mul_f32_e32 v182,0x3e0293ee,v182
v_mul_f32_e32 v183,0x3e0293ee,v183
mvaa 200,192,28
cv 178,180,181
cv 179,182,183
mvaa 208,192,96
v_mul_f32_e32 v188,0x3e0293ee,v188
v_mul_f32_e32 v189,0x3e0293ee,v189
mvaa 208,196,100
v_mul_f32_e32 v190,0x3e0293ee,v190
v_mul_f32_e32 v191,0x3e0293ee,v191
mvaa 208,200,104
ds_write_b128 v251,v[160:163] offset:13312
mvaa 212,204,104
v_mul_f32_e32 v184,0x3e0293ee,v184
v_mul_f32_e32 v185,0x3e0293ee,v185
mvaa 212,208,100
v_mul_f32_e32 v186,0x3e0293ee,v186
v_mul_f32_e32 v187,0x3e0293ee,v187
mvaa 212,212,96
cv 184,184,185
cv 185,186,187
mvaa 220,212,108
cv 186,188,189
cv 187,190,191
)"
R"(mvaa 220,208,44
v_mul_f32_e32 v192,0x3e0293ee,v192
v_mul_f32_e32 v193,0x3e0293ee,v193
mvaa 220,204,40
v_mul_f32_e32 v194,0x3e0293ee,v194
v_mul_f32_e32 v195,0x3e0293ee,v195
mvaa 216,200,40
ds_write_b128 v251,v[168:171] offset:9216
mvaa 216,196,44
cv 192,192,193
cv 193,194,195
mvaa 216,192,108
v_mul_f32_e32 v196,0x3e0293ee,v196
v_mul_f32_e32 v197,0x3e0293ee,v197
maaa 128,216,108
s_waitcnt vmcnt(12)
s_barrier
maaa 128,220,44
v_mul_f32_e32 v198,0x3e0293ee,v198
v_mul_f32_e32 v199,0x3e0293ee,v199
maaa 128,224,40
cv 194,196,197
cv 195,198,199
maaa 112,228,40
v_mul_f32_e32 v204,0x3e0293ee,v204
v_mul_f32_e32 v205,0x3e0293ee,v205
maaa 112,232,44
ds_write_b128 v251,v[176:179] offset:5120
maaa 112,236,108
v_mul_f32_e32 v206,0x3e0293ee,v206
v_mul_f32_e32 v207,0x3e0293ee,v207
maaa 116,236,96
v_mul_f32_e32 v200,0x3e0293ee,v200
v_mul_f32_e32 v201,0x3e0293ee,v201
maaa 116,232,100
v_mul_f32_e32 v202,0x3e0293ee,v202
v_mul_f32_e32 v203,0x3e0293ee,v203
maaa 116,228,104
cv 200,200,201
cv 201,202,203
maaa 132,224,104
cv 202,204,205
cv 203,206,207
maaa 132,220,100
v_mul_f32_e32 v208,0x3e0293ee,v208
v_mul_f32_e32 v209,0x3e0293ee,v209
maaa 132,216,96
ds_write_b128 v251,v[184:187] offset:1024
maaa 136,216,28
v_mul_f32_e32 v210,0x3e0293ee,v210
v_mul_f32_e32 v211,0x3e0293ee,v211
maaa 136,220,32
cv 208,208,209
cv 209,210,211
maaa 136,224,36
v_mul_f32_e32 v212,0x3e0293ee,v212
v_mul_f32_e32 v213,0x3e0293ee,v213
maaa 120,228,36
v_mul_f32_e32 v214,0x3e0293ee,v214
v_mul_f32_e32 v215,0x3e0293ee,v215
maaa 120,232,32
cv 210,212,213
cv 211,214,215
maaa 120,236,28
v_mul_f32_e32 v220,0x3e0293ee,v220
v_mul_f32_e32 v221,0x3e0293ee,v221
maaa 124,236,0
ds_write_b128 v251,v[192:195] offset:2048
maaa 124,232,20
v_mul_f32_e32 v216,0x3e0293ee,v216
v_mul_f32_e32 v217,0x3e0293ee,v217
maaa 124,228,24
v_mul_f32_e32 v222,0x3e0293ee,v222
v_mul_f32_e32 v223,0x3e0293ee,v223
maaa 140,224,24
v_mul_f32_e32 v218,0x3e0293ee,v218
v_mul_f32_e32 v219,0x3e0293ee,v219
maaa 140,220,20
cv 216,216,217
cv 217,218,219
maaa 140,216,0
cv 218,220,221
cv 219,222,223
mvaa 88,108,64
v_accvgpr_read_b32 v0,a128
v_accvgpr_read_b32 v1,a129
mvaa 88,44,68
ds_write_b128 v251,v[200:203] offset:6144
mvaa 88,40,56
v_mul_f32_e32 v0,0x3e0293ee,v0
v_mul_f32_e32 v1,0x3e0293ee,v1
mvaa 108,40,60
v_accvgpr_read_b32 v2,a130
v_accvgpr_read_b32 v3,a131
mvaa 108,44,48
v_mul_f32_e32 v2,0x3e0293ee,v2
v_mul_f32_e32 v3,0x3e0293ee,v3
cv 88,88,89
cv 89,90,91
mvaa 108,108,52
buffer_store_dwordx2 v[88:89],v253,s[8:11],s99 offen offset:1024 sc0 sc1
mvaa 36,76,108
cv 0,0,1
v_accvgpr_read_b32 v4,a112
mvaa 36,72,44
v_accvgpr_read_b32 v5,a113
v_accvgpr_read_b32 v6,a114
mvaa 36,84,40
ds_write_b128 v251,v[208:211] offset:10240
mvaa 32,80,40
cv 108,108,109
cv 109,110,111
mvaa 32,4,44
buffer_store_dwordx2 v[108:109],v253,s[8:11],s99 offen offset:1536 sc0 sc1
mvaa 32,8,108
cv 1,2,3
v_mul_f32_e32 v4,0x3e0293ee,v4
mvaa 64,12,108
v_mul_f32_e32 v5,0x3e0293ee,v5
v_accvgpr_read_b32 v7,a115
mvaa 64,92,44
v_mul_f32_e32 v6,0x3e0293ee,v6
v_mul_f32_e32 v7,0x3e0293ee,v7
mvaa 64,16,40
cv 32,32,33
cv 33,34,35
cv 34,36,37
cv 35,38,39
mvaa 68,88,40
buffer_store_dwordx4 v[32:35],v244,s[8:11],s99 offen sc0 sc1
ds_write_b128 v251,v[216:219] offset:14336
mvaa 68,240,44
cv 2,4,5
cv 3,6,7
mvaa 68,244,108
v_accvgpr_read_b32 v92,a116
v_accvgpr_read_b32 v93,a117
mvaa 60,244,96
ra 192,224,0
v_accvgpr_read_b32 v94,a118
mvaa 60,240,100
ra 196,224,1024
v_mul_f32_e32 v92,0x3e0293ee,v92
mvaa 60,88,104
ra 200,224,2048
v_mul_f32_e32 v93,0x3e0293ee,v93
mvaa 56,16,104
v_mul_f32_e32 v94,0x3e0293ee,v94
v_accvgpr_read_b32 v95,a119
mvaa 56,92,100
ds_write_b128 v251,v[0:3] offset:15360
mvaa 56,12,96
v_accvgpr_read_b32 v72,a132
v_accvgpr_read_b32 v73,a133
mvaa 24,8,96
v_accvgpr_read_b32 v74,a134
v_mul_f32_e32 v95,0x3e0293ee,v95
mvaa 24,4,100
v_mul_f32_e32 v72,0x3e0293ee,v72
v_mul_f32_e32 v73,0x3e0293ee,v73
mvaa 24,80,104
v_mul_f32_e32 v74,0x3e0293ee,v74
v_accvgpr_read_b32 v75,a135
mvaa 28,84,104
v_mul_f32_e32 v75,0x3e0293ee,v75
cv 72,72,73
mvaa 28,72,100
ra 204,224,3072
cv 73,74,75
cv 74,92,93
mvaa 28,76,96
ra 208,224,4096
cv 75,94,95
mvaa 20,76,28
ds_write_b128 v251,v[72:75] offset:11264
mvaa 20,72,32
v_accvgpr_read_b32 v144,a136
v_accvgpr_read_b32 v145,a137
mvaa 20,84,36
v_accvgpr_read_b32 v146,a138
v_accvgpr_read_b32 v147,a139
mvaa 16,80,36
ra 212,224,5120
ra 248,224,14336
ra 252,224,15360
v_mul_f32_e32 v144,0x3e0293ee,v144
v_mul_f32_e32 v145,0x3e0293ee,v145
v_mul_f32_e32 v146,0x3e0293ee,v146
v_mul_f32_e32 v147,0x3e0293ee,v147
mvaa 16,4,32
ra 236,224,11264
cv 144,144,145
v_accvgpr_read_b32 v148,a120
v_accvgpr_read_b32 v149,a121
v_accvgpr_read_b32 v150,a122
cv 145,146,147
mvaa 16,8,28
v_mul_f32_e32 v148,0x3e0293ee,v148
v_mul_f32_e32 v149,0x3e0293ee,v149
v_mul_f32_e32 v150,0x3e0293ee,v150
v_accvgpr_read_b32 v151,a123
v_mul_f32_e32 v151,0x3e0293ee,v151
cv 146,148,149
mvaa 48,12,28
ra 228,224,9216
cv 147,150,151
mvaa 48,92,32
ds_write_b128 v251,v[144:147] offset:7168
mvaa 48,16,36
v_accvgpr_read_b32 v156,a124
v_accvgpr_read_b32 v157,a125
mvaa 52,88,36
ra 232,224,10240
ra 216,224,6144
ra 220,224,7168
ra 224,224,8192
v_accvgpr_read_b32 v158,a126
v_accvgpr_read_b32 v159,a127
mvaa 52,240,32
v_accvgpr_read_b32 v152,a140
v_mul_f32_e32 v156,0x3e0293ee,v156
v_mul_f32_e32 v157,0x3e0293ee,v157
v_mul_f32_e32 v158,0x3e0293ee,v158
v_mul_f32_e32 v159,0x3e0293ee,v159
mvaa 52,244,28
v_mul_f32_e32 v152,0x3e0293ee,v152
v_accvgpr_read_b32 v153,a141
v_mul_f32_e32 v153,0x3e0293ee,v153
v_accvgpr_read_b32 v154,a142
v_accvgpr_read_b32 v155,a143
mvaa 44,244,0
v_mul_f32_e32 v154,0x3e0293ee,v154
v_mul_f32_e32 v155,0x3e0293ee,v155
cv 152,152,153
cv 153,154,155
cv 154,156,157
mvaa 44,240,20
ra 244,224,13312
cv 155,158,159
mvaa 44,88,24
ds_write_b128 v251,v[152:155] offset:3072
mvaa 40,16,24
cv 64,64,65
cv 56,56,57
mvaa 40,92,20
cv 57,58,59
cv 58,60,61
mvaa 40,12,0
ra 240,224,12288
cv 59,62,63
mvaa 8,8,0
cv 16,16,17
cv 17,18,19
mvaa 8,4,20
cv 48,48,49
cv 194,44,45
mvaa 8,80,24
cv 195,46,47
cv 192,40,41
mvaa 12,84,24
cv 193,42,43
cv 49,50,51
mvaa 12,72,20
s_cmp_eq_u32 s19,0
s_cselect_b32 s76,32768,0
s_cselect_b32 s77,49152,s86
s_cselect_b32 s78,65536,s46
s_cselect_b32 s79,81920,s86
mvaa 12,76,0
s_add_u32 s86,s95,98304
cv 8,8,9
v_add_u32_e32 v236,s86,v244
mvaa 76,0,64
s_add_u32 s46,s46,16384
v_xor_b32_e32 v237,4096,v236
s_waitcnt lgkmcnt(0)
s_barrier
s_mov_b32 m0,s68
mvaa 76,20,68
ds 249,8,76,0
ra 128,237,32768
ra 132,237,33792
mvaa 76,24,56
s_add_u32 s95,s47,32768
ra 136,237,34816
cv 9,10,11
mvaa 96,24,60
ra 140,237,35840
ra 144,237,49152
mvaa 96,20,48
cv 10,12,13
cv 11,14,15
mvaa 96,0,52
ra 148,237,50176
cv 50,52,53
mvaa 100,28,52
ds 249,8,76,1024
ra 152,237,51200
ra 156,237,52224
mvaa 100,32,48
ra 176,236,16384
cv 51,54,55
mvaa 100,36,60
ra 180,236,17408
cv 18,20,21
mvaa 80,36,56
ra 184,236,18432
cv 19,22,23
mvaa 80,32,68
ra 188,236,19456
cv 24,24,25
mvaa 80,28,64
ds 249,8,76,2048
ra 160,236,0
mvaa 84,96,64
ra 164,236,1024
cv 25,26,27
mvaa 84,100,68
ra 168,236,2048
ra 172,236,3072
mvaa 84,104,56
cv 26,28,29
cv 27,30,31
mvaa 104,104,60
cv 65,66,67
cv 66,68,69
mvaa 104,100,48
ds 249,8,76,3072
mvaa 104,96,52
s_mov_b32 m0,s69
cv 67,70,71
v_cndmask_b32_e64 v252,0,v252,s[40:41]
cvvv 164,8,192,112
cv 70,76,77
cv 71,78,79
cvvv 168,8,48,116
ds 249,8,77,0
cvvv 172,16,48,112
cv 94,96,97
v_mov_b32_e32 v76,v88
cvvv 176,16,56,116
cv 95,98,99
s_waitcnt vmcnt(8) lgkmcnt(0)
s_barrier
cvvv 180,24,56,112
ds 249,8,77,1024
cvvv 184,24,64,116
ra 0,224,32768
cv 96,100,101
cvvv 188,32,64,112
ra 4,224,33792
cv 97,102,103
cvaa 0,192,128,232
buffer_load_dword v17,v247,s[8:11],s35 offen offset:64 sc1
mvaa 0,200,132
ra 8,224,34816
cv 72,80,81
mvaa 0,208,136
ra 12,224,35840
cv 73,82,83
mvaa 0,216,140
ds 249,8,77,2048
cvaa 4,196,128,232
ra 16,224,36864
cv 74,84,85
mvaa 4,204,132
ra 20,224,37888
v_max3_f32 v172,v168,v169,v172
mvaa 4,212,136
ds 249,8,77,3072
mvaa 4,220,140
s_mov_b32 m0,s74
ra 24,224,38912
v_max_f32_e32 v172,v172,v173
cvaa 32,224,128,232
ra 28,224,39936
v_max3_f32 v174,v170,v171,v174
mvaa 32,232,132
buffer_load_dword v19,v247,s[8:11],s35 offen offset:192 sc1
mvaa 32,240,136
ra 32,224,40960
v_max_f32_e32 v174,v174,v175
mvaa 32,248,140
ra 36,224,41984
ra 40,224,43008
cvaa 36,228,128,232
buffer_load_dword v18,v247,s[8:11],s35 offen offset:128 sc1
ra 44,224,44032
mvaa 36,236,132
ra 48,224,45056
v_max_f32_e32 v148,v172,v174
mvaa 36,244,136
ra 56,224,47104
ra 60,224,48128
mvaa 36,252,140
buffer_load_dword v16,v247,s[8:11],s35 offen offset:0 sc1
ra 52,224,46080
cvaa 8,192,144,232
v_mov_b32_e32 v149,v148
cv 75,86,87
mvaa 8,200,148
v_permlane16_swap_b32_e32 v148,v149
mvaa 8,208,152
ds 255,8,43,0
mvaa 8,216,156
v_max_f32_e32 v148,v148,v149
v_mov_b32_e32 v149,v148
cvaa 12,196,144,232
cv 98,104,105
v_max3_f32 v180,v176,v177,v180
mvaa 12,204,148
ds 255,8,43,1024
mvaa 12,212,152
v_permlane32_swap_b32_e32 v148,v149
mvaa 12,220,156
v_max_f32_e32 v148,v148,v149
s_waitcnt lgkmcnt(0)
s_barrier
cvaa 40,224,144,232
ds 255,8,43,2048
mvaa 40,232,148
ra 64,225,0
v_mul_f32_e32 v148,0xbe8293ee,v148
mvaa 40,240,152
ra 68,225,1024
v_fmamk_f32 v172,v172,0x3e8293ee,v148
mvaa 40,248,156
ds 255,8,43,3072
cvaa 44,228,144,232
s_mov_b32 m0,s70
ra 72,225,2048
v_fmamk_f32 v174,v174,0x3e8293ee,v148
mvaa 44,236,148
ra 76,225,3072
v_max3_f32 v182,v178,v179,v182
mvaa 44,244,152
ds 249,8,78,0
mvaa 44,252,156
ra 80,225,4096
v_max_f32_e32 v180,v180,v181
cvaa 24,192,176,128
ra 84,225,5120
v_max_f32_e32 v182,v182,v183
mvaa 24,200,180
ds 249,8,78,1024
mvaa 24,208,184
ra 88,225,6144
v_max_f32_e32 v152,v180,v182
mvaa 24,216,188
ra 92,225,7168
v_mov_b32_e32 v153,v152
cvaa 28,196,176,132
ds 249,8,78,2048
mvaa 28,204,180
ra 96,225,8192
v_max3_f32 v188,v184,v185,v188
mvaa 28,212,184
ra 100,225,9216
ra 104,225,10240
mvaa 28,220,188
ds 249,8,78,3072
ra 108,225,11264
cvaa 56,224,176,136
s_mov_b32 m0,s71
ra 112,225,12288
v_max3_f32 v190,v186,v187,v190
mvaa 56,232,180
ra 116,225,13312
ra 120,225,14336
mvaa 56,240,184
ds 249,8,79,0
ra 124,225,15360
mvaa 56,248,188
ex 172
cvaa 60,228,176,140
ex 174
mvaa 60,236,180
ds 249,8,79,1024
mvaa 60,244,184
v_permlane16_swap_b32_e32 v152,v153
mvaa 60,252,188
cv 172,172,172
cv 174,174,174
cvaa 80,0,160,128
ds 249,8,79,2048
mvaa 80,8,164
v_and_b32_e32 v168,v226,v172
v_max_f32_e32 v152,v152,v153
mvaa 80,16,168
v_and_b32_e32 v169,v227,v174
v_and_b32_e32 v170,v250,v172
v_and_b32_e32 v171,v252,v174
mvaa 80,24,172
ds 249,8,79,3072
v_mov_b32_e32 v153,v152
cvaa 84,4,160,132
v_max_f32_e32 v188,v188,v189
v_permlane32_swap_b32_e32 v152,v153
v_max_f32_e32 v152,v152,v153
mvaa 84,12,164
v_mul_f32_e32 v152,0xbe8293ee,v152
v_fmamk_f32 v180,v180,0x3e8293ee,v152
mvaa 84,20,168
v_fmamk_f32 v182,v182,0x3e8293ee,v152
v_max_f32_e32 v190,v190,v191
mvaa 84,28,172
ex 180
cvaa 112,32,160,136
ex 182
v_max_f32_e32 v156,v188,v190
mvaa 112,40,164
v_mov_b32_e32 v157,v156
cv 180,180,180
mvaa 112,48,168
v_permlane16_swap_b32_e32 v156,v157
mvaa 112,56,172
cv 182,182,182
v_and_b32_e32 v176,v226,v180
v_max_f32_e32 v156,v156,v157
s_waitcnt vmcnt(15) lgkmcnt(0)
s_barrier
s_mov_b32 m0,s72
cvaa 116,36,160,140
ds 255,8,76,0
v_and_b32_e32 v177,v227,v182
mvaa 116,44,164
v_and_b32_e32 v178,v250,v180
v_and_b32_e32 v179,v252,v182
v_mov_b32_e32 v157,v156
mvaa 116,52,168
cv 99,106,107
v_permlane32_swap_b32_e32 v156,v157
v_max_f32_e32 v156,v156,v157
mvaa 116,60,172
ds 255,8,76,1024
v_mul_f32_e32 v156,0xbe8293ee,v156
zvvv 148,228,168
v_fmamk_f32 v188,v188,0x3e8293ee,v156
ex 188
zvvv 200,70,168
v_fmamk_f32 v190,v190,0x3e8293ee,v156
ex 190
zvvv 204,94,168
ds 255,8,76,2048
zvvv 152,228,176
cv 188,188,188
cv 190,190,190
v_and_b32_e32 v184,v226,v188
zvvv 208,72,176
v_and_b32_e32 v185,v227,v190
v_and_b32_e32 v186,v250,v188
v_and_b32_e32 v187,v252,v190
zvvv 212,96,176
ds 255,8,76,3072
s_mov_b32 m0,s73
v_mov_b32_e32 v77,v89
zvvv 156,228,184
v_mov_b32_e32 v100,v108
v_mov_b32_e32 v101,v109
zvvv 216,74,184
ex 0
zvvv 220,98,184
ds 255,8,77,0
cvaa 64,0,128,232
ex 1
mvaa 64,8,132
v_mov_b32_e32 v170,v70
v_mov_b32_e32 v171,v71
mvaa 64,16,136
ds 255,8,77,1024
mvaa 64,24,140
ex 2
cvaa 68,4,128,232
ex 3
mvaa 68,12,132
ds 255,8,77,2048
mvaa 68,20,136
ex 4
mvaa 68,28,140
ex 5
cvaa 96,32,128,232
ds 255,8,77,3072
mvaa 96,40,132
ex 6
mvaa 96,48,136
ex 7
mvaa 96,56,140
ex 32
cvaa 100,36,128,232
ex 33
mvaa 100,44,132
ex 34
mvaa 100,52,136
v_mov_b32_e32 v160,v120
ex 35
mvaa 100,60,140
v_mov_b32_e32 v161,v121
ex 36
cvaa 72,0,144,232
v_mov_b32_e32 v162,v122
ex 37
mvaa 72,8,148
v_mov_b32_e32 v163,v123
ex 38
mvaa 72,16,152
v_mov_b32_e32 v175,v95
ex 39
mvaa 72,24,156
v_mov_b32_e32 v174,v94
ex 8
cvaa 76,4,144,232
cv 0,0,1
ex 9
mvaa 76,12,148
cv 1,2,3
ex 10
mvaa 76,20,152
cv 2,4,5
ex 11
mvaa 76,28,156
cv 3,6,7
ex 12
cvaa 104,32,144,232
cv 4,32,33
ex 13
mvaa 104,40,148
v_sub_f32_e32 v17,1.0,v17
ex 14
mvaa 104,48,152
v_rcp_f32_e32 v148,v148
s_waitcnt vmcnt(21)
v_sub_f32_e32 v19,1.0,v19
mvaa 104,56,156
v_mul_f32_e32 v148,v148,v17
v_mul_f32_e32 v200,v148,v200
v_sub_f32_e32 v18,1.0,v18
cvaa 108,36,144,232
v_mul_f32_e32 v201,v148,v201
v_mul_f32_e32 v202,v148,v202
v_mul_f32_e32 v203,v148,v203
mvaa 108,44,148
v_mul_f32_e32 v204,v148,v204
cv 200,200,201
cv 201,202,203
mvaa 108,52,152
buffer_load_dwordx4 v[176:179],v244,s[8:11],s98 offen sc1
v_mul_f32_e32 v205,v148,v205
mvaa 108,60,156
v_mul_f32_e32 v206,v148,v206
v_mul_f32_e32 v207,v148,v207
cv 202,204,205
cvaa 88,0,176,232
cv 203,206,207
v_rcp_f32_e32 v152,v152
mvaa 88,8,180
buffer_load_dwordx2 v[168:169],v253,s[8:11],s98 offen offset:1024 sc1
v_mul_f32_e32 v152,v152,v18
mvaa 88,16,184
v_mul_f32_e32 v208,v152,v208
v_mul_f32_e32 v209,v152,v209
v_mul_f32_e32 v210,v152,v210
mvaa 88,24,188
v_mul_f32_e32 v211,v152,v211
cv 208,208,209
cv 209,210,211
cvaa 92,4,176,232
buffer_load_dwordx2 v[172:173],v253,s[8:11],s98 offen offset:1536 sc1
v_mul_f32_e32 v212,v152,v212
mvaa 92,12,180
v_mul_f32_e32 v213,v152,v213
v_mul_f32_e32 v214,v152,v214
v_mul_f32_e32 v215,v152,v215
mvaa 92,20,184
cv 210,212,213
v_rcp_f32_e32 v156,v156
mvaa 92,28,188
buffer_store_dwordx4 v[200:203],v244,s[8:11],s34 offen sc0 sc1
cv 211,214,215
cvaa 120,32,176,232
v_mul_f32_e32 v156,v156,v19
v_mul_f32_e32 v216,v156,v216
v_mul_f32_e32 v217,v156,v217
mvaa 120,40,180
v_mul_f32_e32 v218,v156,v218
v_mul_f32_e32 v219,v156,v219
cv 216,216,217
mvaa 120,48,184
buffer_store_dwordx4 v[208:211],v244,s[8:11],s50 offen sc0 sc1
cv 217,218,219
mvaa 120,56,188
v_mul_f32_e32 v220,v156,v220
v_mul_f32_e32 v221,v156,v221
v_mul_f32_e32 v222,v156,v222
cvaa 124,36,176,232
v_mul_f32_e32 v223,v156,v223
cv 218,220,221
cv 219,222,223
mvaa 124,44,180
buffer_store_dwordx4 v[216:219],v244,s[8:11],s51 offen sc0 sc1
cv 5,34,35
mvaa 124,52,184
cv 6,36,37
ex 15
mvaa 124,60,188
s_cmp_lg_u32 s19,0
s_cbranch_scc1 L_j0
s_nop 1
s_lshl_b32 s86,s12,12
s_add_u32 s86,s86,131072
v_add_u32_e32 v236,s86,v244
ra 160,236,0
ra 164,236,1024
ra 168,236,2048
ra 172,236,3072
ra 176,236,16384
ra 180,236,17408
ra 184,236,18432
ra 188,236,19456
v_add_u32_e32 v224,s94,v224
s_waitcnt lgkmcnt(0)
L_j0:
cv 7,38,39
ex 40
zaav 0,64,0
buffer_load_dword v247,v246,s[24:27],s49 offen
maav 0,96,4
s_add_u32 s49,s49,32768
ex 41
zaav 4,68,0
s_lshl_b32 s86,s21,2
ex 42
v_bfe_i32 v248,v244,4,1
maav 4,100,4
buffer_load_dword v247,v245,s[36:39],s85 offen
v_and_b32_e32 v248,2863311530,v248
zaav 8,72,0
s_add_u32 s86,s86,s12
ex 43
v_or_b32_e32 v248,1145324612,v248
maav 8,104,4
s_lshl_b32 s86,s86,8
ex 44
v_mov_b64_e32 v[144:145],0
zaav 12,76,0
buffer_load_dword v247,v246,s[24:27],s49 offen
cv 8,8,9
maav 12,108,4
s_add_u32 s85,s85,131072
cv 9,10,11
ex 45
v_smfmac_f32_16x16x64_bf16 v[144:147],v[228:231],v[0:7],v248
cv 10,12,13
ex 46
zaav 16,80,0
s_add_u32 s49,s49,32768
cv 11,14,15
ex 47
maav 16,112,4
s_waitcnt vmcnt(8)
v_max3_u32 v180,v176,v177,v178
cv 12,40,41
zaav 20,84,0
s_waitcnt vmcnt(7)
v_max3_u32 v180,v180,v179,v168
s_waitcnt vmcnt(6)
v_max3_u32 v180,v180,v169,v172
v_max_u32_e32 v180,v180,v173
v_cmp_eq_u32_e64 s[40:41],-1,v180
maav 20,116,4
s_cmp_lg_u64 s[40:41],0
s_cbranch_scc1 L_bx
L_bxr:
s_bitcmp1_b32 170,s20
s_cselect_b32 s87,L_odd-L_last,0
s_add_u32 s86,s86,s87
s_add_u32 s86,s86,L_last-L_att_pc
mvvv 160,176,192
cv 13,42,43
zaav 24,88,0
cv 14,44,45
v_lshl_add_u32 v238,v244,3,s86
v_min_u32_e32 v238,L_att_end-L_att_pc-4,v238
cv 15,46,47
maav 24,120,4
s_mov_b64 exec,3
global_load_dword v239,v238,s[96:97]
s_mov_b64 exec,-1
zaav 28,92,0
v_max3_f32 v164,v160,v161,v164
v_max_f32_e32 v164,v164,v165
v_max3_f32 v166,v162,v163,v166
v_max_f32_e32 v166,v166,v167
v_max_f32_e32 v180,v164,v166
v_mov_b32_e32 v181,v180
maav 28,124,4
ex 24
v_permlane16_swap_b32_e32 v180,v181
v_max_f32_e32 v180,v180,v181
v_mov_b32_e32 v181,v180
zaav 32,64,8
ex 25
v_permlane32_swap_b32_e32 v180,v181
v_max_f32_e32 v180,v180,v181
v_mul_f32_e32 v180,0xbe8293ee,v180
v_fmamk_f32 v164,v164,0x3e8293ee,v180
maav 32,96,12
ex 164
v_fmamk_f32 v166,v166,0x3e8293ee,v180
ex 166
cv 164,164,164
cv 166,166,166
zaav 36,68,8
v_and_b32_e32 v160,v226,v164
v_and_b32_e32 v161,v227,v166
v_and_b32_e32 v162,v250,v164
v_and_b32_e32 v163,v252,v166
maav 36,100,12
v_sub_f32_e32 v16,1.0,v16
zvvv 180,228,160
zaav 40,72,8
ex 26
zvvv 184,168,160
maav 40,104,12
ex 27
zvvv 188,172,160
zaav 44,76,8
v_mov_b64_e32 v[148:149],0
v_rcp_f32_e32 v180,v180
maav 44,108,12
v_mul_f32_e32 v180,v180,v16
v_mul_f32_e32 v184,v180,v184
v_mul_f32_e32 v185,v180,v185
v_mul_f32_e32 v186,v180,v186
v_mul_f32_e32 v187,v180,v187
v_mul_f32_e32 v188,v180,v188
v_mul_f32_e32 v189,v180,v189
v_smfmac_f32_16x16x64_bf16 v[148:151],v[228:231],v[8:15],v248
v_mul_f32_e32 v190,v180,v190
v_mul_f32_e32 v191,v180,v191
cv 184,184,185
cv 185,186,187
cv 186,188,189
cv 187,190,191
zaav 48,80,8
buffer_store_dwordx4 v[184:187],v244,s[8:11],s33 offen sc0 sc1
cv 24,24,25
cv 25,26,27
ex 28
maav 48,112,12
ex 29
ex 30
ex 31
cv 26,28,29
zaav 52,84,8
cv 27,30,31
ex 56
ex 57
ex 58
maav 52,116,12
ex 59
ex 60
zaav 56,88,8
ra 192,224,0
ra 196,224,1024
ex 61
maav 56,120,12
ra 200,224,2048
cv 28,56,57
ex 62
zaav 60,92,8
ra 204,224,3072
ra 208,224,4096
ex 63
maav 60,124,12
ra 212,224,5120
cv 29,58,59
cv 30,60,61
cv 31,62,63
zvav 192,64,24
ra 216,224,6144
ra 220,224,7168
ex 64
mvav 192,96,28
ra 224,224,8192
v_mov_b32_e32 v251,-1
ex 65
zvav 196,68,24
ra 228,224,9216
ra 232,224,10240
ex 66
mvav 196,100,28
ra 236,224,11264
v_mov_b64_e32 v[152:153],0
ex 67
zvav 200,72,24
ra 240,224,12288
ra 244,224,13312
v_mov_b64_e32 v[156:157],0
cv 64,64,65
mvav 200,104,28
ra 248,224,14336
ra 252,224,15360
ex 68
zvav 204,76,24
ex 69
ex 70
mvav 204,108,28
cv 65,66,67
ex 71
cv 66,68,69
v_smfmac_f32_16x16x64_bf16 v[156:159],v[228:231],v[24:31],v248
ex 96
ex 97
zvav 208,80,24
ex 98
ex 99
mvav 208,112,28
cv 67,70,71
cv 68,96,97
ex 100
zvav 212,84,24
ex 101
ex 102
mvav 212,116,28
cv 69,98,99
ex 103
cv 70,100,101
zvav 216,88,24
ex 72
ex 73
mvav 216,120,28
ex 74
ex 75
zvav 220,92,24
ex 76
ex 77
mvav 220,124,28
s_cmp_ge_i32 s47,s46
s_cselect_b32 s76,s95,s46
s_cselect_b32 s77,s95,268435456
s_add_u32 s77,s77,16384
v_mov_b32_e32 v250,-1
v_mov_b32_e32 v252,-1
v_mov_b32_e32 v253,-1
cv 71,102,103
s_mov_b64 exec,0
s_bitcmp1_b32 170,s20
s_cbranch_scc1 L_vbO
s_cmp_eq_u32 s12,0
s_cbranch_scc1 L_vs1
s_cmp_eq_u32 s12,1
s_cbranch_scc1 L_vs2
s_cmp_eq_u32 s12,2
s_cbranch_scc1 L_vs3
s_branch L_vs4
L_vbO:
s_cmp_eq_u32 s12,0
s_cbranch_scc1 L_vs9
s_cmp_eq_u32 s12,1
s_cbranch_scc1 L_vs10
s_cmp_eq_u32 s12,2
s_cbranch_scc1 L_vs11
s_branch L_vs12
L_cw0:
s_mov_b64 exec,-1
s_waitcnt lgkmcnt(0)
s_barrier
cvaa 0,192,128,232
mvaa 0,200,132
s_add_u32 s81,s81,1
ex 78
mvaa 0,208,136
s_add_u32 s44,s44,1024
ex 79
mvaa 0,216,140
cv 72,72,73
cv 73,74,75
cvaa 4,196,128,232
s_mov_b32 m0,s68
ex 104
mvaa 4,204,132
ds 249,8,76,0
mvaa 4,212,136
ex 105
mvaa 4,220,140
ex 106
cvaa 32,224,128,232
ra 64,225,32768
cv 74,76,77
mvaa 32,232,132
ex 107
mvaa 32,240,136
ra 68,225,33792
cv 75,78,79
mvaa 32,248,140
ex 108
cvaa 36,228,128,232
ds 249,8,76,1024
mvaa 36,236,132
ex 109
)"
R"(mvaa 36,244,136
ra 72,225,34816
cv 76,104,105
mvaa 36,252,140
ex 110
cvaa 8,192,144,232
ra 76,225,35840
cv 77,106,107
mvaa 8,200,148
ex 111
mvaa 8,208,152
ex 80
mvaa 8,216,156
ds 249,8,76,2048
cvaa 12,196,144,232
ra 80,225,36864
cv 78,108,109
mvaa 12,204,148
ex 81
mvaa 12,212,152
ra 84,225,37888
cv 79,110,111
mvaa 12,220,156
ex 82
cvaa 40,224,144,232
ex 83
mvaa 40,232,148
ra 88,225,38912
cv 80,80,81
mvaa 40,240,152
ds 249,8,76,3072
mvaa 40,248,156
ex 84
cvaa 44,228,144,232
ra 92,225,39936
cv 81,82,83
mvaa 44,236,148
ex 85
mvaa 44,244,152
ex 86
mvaa 44,252,156
ra 96,225,40960
cv 82,84,85
cvaa 16,192,160,232
ex 87
mvaa 16,200,164
s_mov_b32 m0,s69
ex 112
mvaa 16,208,168
ds 249,8,77,0
mvaa 16,216,172
ra 100,225,41984
cv 83,86,87
cvaa 20,196,160,232
ex 113
mvaa 20,204,164
ra 104,225,43008
cv 84,112,113
mvaa 20,212,168
ex 114
mvaa 20,220,172
ex 115
cvaa 48,224,160,232
ra 108,225,44032
cv 85,114,115
mvaa 48,232,164
ds 249,8,77,1024
mvaa 48,240,168
ex 116
mvaa 48,248,172
ra 112,225,45056
cvaa 52,228,160,232
ex 117
mvaa 52,236,164
ex 118
mvaa 52,244,168
ra 116,225,46080
cv 86,116,117
mvaa 52,252,172
ex 119
cvaa 24,192,176,232
ds 249,8,77,2048
mvaa 24,200,180
ra 120,225,47104
cv 87,118,119
mvaa 24,208,184
ex 88
mvaa 24,216,188
ex 89
cvaa 28,196,176,232
ra 124,225,48128
cv 88,88,89
mvaa 28,204,180
ex 90
mvaa 28,212,184
ex 91
mvaa 28,220,188
ds 249,8,77,3072
cvaa 56,224,176,232
ex 92
mvaa 56,232,180
ex 93
mvaa 56,240,184
ex 94
mvaa 56,248,188
cvaa 60,228,176,232
ex 95
mvaa 60,236,180
ex 120
mvaa 60,244,184
ex 121
mvaa 60,252,188
s_waitcnt vmcnt(8) lgkmcnt(0)
s_barrier
maav 0,64,64
cv 89,90,91
cv 90,92,93
maav 0,96,68
s_mov_b32 m0,s74
ex 122
maav 4,68,64
ds 255,8,78,0
maav 4,100,68
ex 123
maav 8,72,64
ex 124
maav 8,104,68
ex 125
maav 12,76,64
cv 91,94,95
cv 92,120,121
maav 12,108,68
ex 126
v_smfmac_f32_16x16x64_bf16 v[144:147],v[228:231],v[64:71],v248
cv 93,122,123
maav 16,80,64
ex 127
maav 16,112,68
buffer_load_dword v247,v245,s[36:39],s85 offen
maav 20,84,64
cv 94,124,125
cv 95,126,127
maav 20,116,68
s_lshl_b32 s86,s19,3
s_lshr_b32 s87,s18,2
s_add_u32 s86,s86,s87
maav 24,88,64
s_lshl_b32 s86,s86,7
s_add_u32 s86,s86,83898368
v_mov_b32_e32 v238,1
maav 24,120,68
ra 192,224,32768
maav 28,92,64
ds 255,8,78,1024
maav 28,124,68
s_mov_b64 exec,1
buffer_atomic_add v238,off,s[8:11],s86 sc1
s_mov_b64 exec,-1
maav 32,64,72
ra 196,224,33792
maav 32,96,76
s_add_u32 s85,s85,131072
maav 36,68,72
ra 200,224,34816
maav 36,100,76
maav 40,72,72
ds 255,8,78,2048
maav 40,104,76
maav 44,76,72
ra 204,224,35840
maav 44,108,76
v_smfmac_f32_16x16x64_bf16 v[148:151],v[228:231],v[72:79],v248
ra 208,224,36864
maav 48,80,72
maav 48,112,76
maav 52,84,72
ds 255,8,78,3072
maav 52,116,76
ra 212,224,37888
maav 56,88,72
ex 0
maav 56,120,76
ra 216,224,38912
maav 60,92,72
ex 1
maav 60,124,76
s_mov_b32 m0,s75
ex 2
zvav 160,64,80
ra 220,224,39936
cv 0,0,1
mvav 160,96,84
ds 255,8,79,0
zvav 164,68,80
ex 3
mvav 164,100,84
ra 224,224,40960
cv 1,2,3
zvav 168,72,80
ex 4
mvav 168,104,84
ra 228,224,41984
zvav 172,76,80
ds 255,8,79,1024
mvav 172,108,84
s_add_u32 s46,s46,16384
ex 5
v_smfmac_f32_16x16x64_bf16 v[152:155],v[228:231],v[80:87],v248
ra 232,224,43008
zvav 176,80,80
s_add_u32 s47,s47,32768
ex 6
mvav 176,112,84
s_add_u32 s95,s47,32768
ex 7
zvav 180,84,80
ra 236,224,44032
cv 2,4,5
mvav 180,116,84
ex 32
zvav 184,88,80
ds 255,8,79,2048
mvav 184,120,84
ex 33
zvav 188,92,80
ra 240,224,45056
cv 3,6,7
mvav 188,124,84
ex 34
mvav 192,64,88
ra 244,224,46080
cv 4,32,33
mvav 192,96,92
ex 35
mvav 196,68,88
ex 36
mvav 196,100,92
ds 255,8,79,3072
mvav 200,72,88
ra 248,224,47104
cv 5,34,35
mvav 200,104,92
s_cmp_ge_i32 s47,s46
ex 37
mvav 204,76,88
s_cselect_b32 s78,s95,s46
ex 38
mvav 204,108,92
s_cselect_b32 s79,s95,268435456
ex 39
v_smfmac_f32_16x16x64_bf16 v[156:159],v[228:231],v[88:95],v248
ra 252,224,48128
mvav 208,80,88
s_add_u32 s79,s79,16384
ex 8
mvav 208,112,92
ex 9
mvav 212,84,88
ex 10
mvav 212,116,92
ex 11
mvav 216,88,88
cv 6,36,37
cv 7,38,39
mvav 216,120,92
ex 12
mvav 220,92,88
ex 13
mvav 220,124,92
s_waitcnt vmcnt(16) lgkmcnt(0)
s_barrier
cvaa 64,192,128,232
s_bitcmp0_b32 s82,s81
s_cbranch_scc1 L_loop0
s_bitcmp1_b32 s15,s81
s_cbranch_scc1 L_short0
.p2align 6
L_loop0:
mvaa 64,200,132
s_add_u32 s81,s81,1
ex 14
mvaa 64,208,136
s_add_u32 s44,s44,1024
ex 15
mvaa 64,216,140
cv 8,8,9
cv 9,10,11
cvaa 68,196,128,232
s_mov_b32 m0,s70
ex 40
mvaa 68,204,132
ds 249,8,78,0
mvaa 68,212,136
ex 41
mvaa 68,220,140
ex 42
cvaa 96,224,128,232
ra 64,225,0
cv 10,12,13
mvaa 96,232,132
ex 43
mvaa 96,240,136
ra 68,225,1024
cv 11,14,15
mvaa 96,248,140
ex 44
cvaa 100,228,128,232
ds 249,8,78,1024
mvaa 100,236,132
ex 45
mvaa 100,244,136
ra 72,225,2048
cv 12,40,41
mvaa 100,252,140
ex 46
cvaa 72,192,144,232
ra 76,225,3072
cv 13,42,43
mvaa 72,200,148
ex 47
mvaa 72,208,152
ex 16
mvaa 72,216,156
ds 249,8,78,2048
cvaa 76,196,144,232
ra 80,225,4096
cv 14,44,45
mvaa 76,204,148
ex 17
mvaa 76,212,152
ra 84,225,5120
cv 15,46,47
mvaa 76,220,156
ex 18
cvaa 104,224,144,232
ex 19
mvaa 104,232,148
ra 88,225,6144
cv 16,16,17
mvaa 104,240,152
ds 249,8,78,3072
mvaa 104,248,156
ex 20
cvaa 108,228,144,232
ra 92,225,7168
cv 17,18,19
mvaa 108,236,148
ex 21
mvaa 108,244,152
ex 22
mvaa 108,252,156
ra 96,225,8192
cv 18,20,21
cvaa 80,192,160,232
ex 23
mvaa 80,200,164
s_mov_b32 m0,s71
ex 48
mvaa 80,208,168
ds 249,8,79,0
mvaa 80,216,172
ra 100,225,9216
cv 19,22,23
cvaa 84,196,160,232
ex 49
mvaa 84,204,164
ra 104,225,10240
cv 20,48,49
mvaa 84,212,168
ex 50
mvaa 84,220,172
ex 51
cvaa 112,224,160,232
ra 108,225,11264
cv 21,50,51
mvaa 112,232,164
ds 249,8,79,1024
mvaa 112,240,168
ex 52
mvaa 112,248,172
ra 112,225,12288
cvaa 116,228,160,232
ex 53
mvaa 116,236,164
ex 54
mvaa 116,244,168
ra 116,225,13312
cv 22,52,53
mvaa 116,252,172
ex 55
cvaa 88,192,176,232
ds 249,8,79,2048
mvaa 88,200,180
ra 120,225,14336
cv 23,54,55
mvaa 88,208,184
ex 24
mvaa 88,216,188
ex 25
cvaa 92,196,176,232
ra 124,225,15360
cv 24,24,25
mvaa 92,204,180
ex 26
mvaa 92,212,184
ex 27
mvaa 92,220,188
ds 249,8,79,3072
cvaa 120,224,176,232
ex 28
mvaa 120,232,180
ex 29
mvaa 120,240,184
ex 30
mvaa 120,248,188
buffer_store_dwordx4 v[250:253],v244,s[8:11],s44 offen sc0 sc1
cvaa 124,228,176,232
ex 31
mvaa 124,236,180
ex 56
mvaa 124,244,184
ex 57
mvaa 124,252,188
s_waitcnt vmcnt(16) lgkmcnt(0)
s_barrier
maav 0,64,0
cv 25,26,27
cv 26,28,29
maav 0,96,4
s_mov_b32 m0,s72
ex 58
maav 4,68,0
ds 255,8,76,0
maav 4,100,4
ex 59
maav 8,72,0
ex 60
maav 8,104,4
ex 61
maav 12,76,0
s_cmp_lg_u32 s80,s81
cv 27,30,31
cv 28,56,57
maav 12,108,4
ex 62
v_smfmac_f32_16x16x64_bf16 v[144:147],v[228:231],v[0:7],v248
cv 29,58,59
maav 16,80,0
ex 63
maav 16,112,4
buffer_load_dword v247,v246,s[24:27],s49 offen
maav 20,84,0
cv 30,60,61
cv 31,62,63
maav 20,116,4
maav 24,88,0
s_cbranch_scc1 L_evb_r0
v_add_u32_e32 v224,s94,v224
s_lshl_b32 s86,s12,12
s_add_u32 s86,s86,131072
v_add_u32_e32 v236,s86,v244
ra 160,236,0
ra 164,236,1024
ra 168,236,2048
ra 172,236,3072
ra 176,236,16384
ra 180,236,17408
ra 184,236,18432
ra 188,236,19456
L_evb_r0:
maav 24,120,4
ra 192,224,0
maav 28,92,0
ds 255,8,76,1024
maav 28,124,4
maav 32,64,8
ra 196,224,1024
maav 32,96,12
s_add_u32 s49,s49,32768
maav 36,68,8
ra 200,224,2048
maav 36,100,12
maav 40,72,8
ds 255,8,76,2048
maav 40,104,12
maav 44,76,8
ra 204,224,3072
maav 44,108,12
v_smfmac_f32_16x16x64_bf16 v[148:151],v[228:231],v[8:15],v248
ra 208,224,4096
maav 48,80,8
maav 48,112,12
maav 52,84,8
ds 255,8,76,3072
maav 52,116,12
ra 212,224,5120
maav 56,88,8
ex 64
maav 56,120,12
ra 216,224,6144
maav 60,92,8
ex 65
maav 60,124,12
s_mov_b32 m0,s73
ex 66
mvav 160,64,16
ra 220,224,7168
cv 64,64,65
mvav 160,96,20
ds 255,8,77,0
mvav 164,68,16
ex 67
mvav 164,100,20
ra 224,224,8192
cv 65,66,67
mvav 168,72,16
ex 68
mvav 168,104,20
ra 228,224,9216
mvav 172,76,16
ds 255,8,77,1024
mvav 172,108,20
s_add_u32 s46,s46,16384
ex 69
v_smfmac_f32_16x16x64_bf16 v[152:155],v[228:231],v[16:23],v248
ra 232,224,10240
mvav 176,80,16
s_add_u32 s47,s47,32768
ex 70
mvav 176,112,20
s_add_u32 s95,s47,32768
ex 71
mvav 180,84,16
ra 236,224,11264
cv 66,68,69
mvav 180,116,20
ex 96
mvav 184,88,16
ds 255,8,77,2048
mvav 184,120,20
ex 97
mvav 188,92,16
ra 240,224,12288
cv 67,70,71
mvav 188,124,20
ex 98
mvav 192,64,24
ra 244,224,13312
cv 68,96,97
mvav 192,96,28
ex 99
mvav 196,68,24
ex 100
mvav 196,100,28
ds 255,8,77,3072
mvav 200,72,24
ra 248,224,14336
cv 69,98,99
mvav 200,104,28
s_cmp_ge_i32 s47,s46
ex 101
mvav 204,76,24
s_cselect_b32 s76,s95,s46
ex 102
mvav 204,108,28
s_cselect_b32 s77,s95,268435456
ex 103
v_smfmac_f32_16x16x64_bf16 v[156:159],v[228:231],v[24:31],v248
ra 252,224,15360
mvav 208,80,24
s_add_u32 s77,s77,16384
ex 72
mvav 208,112,28
ex 73
mvav 212,84,24
ex 74
mvav 212,116,28
ex 75
mvav 216,88,24
cv 70,100,101
cv 71,102,103
mvav 216,120,28
ex 76
mvav 220,92,24
ex 77
mvav 220,124,28
s_waitcnt vmcnt(16) lgkmcnt(0)
s_barrier
cvaa 0,192,128,232
mvaa 0,200,132
s_add_u32 s81,s81,1
ex 78
mvaa 0,208,136
s_add_u32 s44,s44,1024
ex 79
mvaa 0,216,140
cv 72,72,73
cv 73,74,75
cvaa 4,196,128,232
s_mov_b32 m0,s68
ex 104
mvaa 4,204,132
ds 249,8,76,0
mvaa 4,212,136
ex 105
mvaa 4,220,140
ex 106
cvaa 32,224,128,232
ra 64,225,32768
cv 74,76,77
mvaa 32,232,132
ex 107
mvaa 32,240,136
ra 68,225,33792
cv 75,78,79
mvaa 32,248,140
ex 108
cvaa 36,228,128,232
ds 249,8,76,1024
mvaa 36,236,132
ex 109
mvaa 36,244,136
ra 72,225,34816
cv 76,104,105
mvaa 36,252,140
ex 110
cvaa 8,192,144,232
ra 76,225,35840
cv 77,106,107
mvaa 8,200,148
ex 111
mvaa 8,208,152
ex 80
mvaa 8,216,156
ds 249,8,76,2048
cvaa 12,196,144,232
ra 80,225,36864
cv 78,108,109
mvaa 12,204,148
ex 81
mvaa 12,212,152
ra 84,225,37888
cv 79,110,111
mvaa 12,220,156
ex 82
cvaa 40,224,144,232
ex 83
mvaa 40,232,148
ra 88,225,38912
cv 80,80,81
mvaa 40,240,152
ds 249,8,76,3072
mvaa 40,248,156
ex 84
cvaa 44,228,144,232
ra 92,225,39936
cv 81,82,83
mvaa 44,236,148
ex 85
mvaa 44,244,152
ex 86
mvaa 44,252,156
ra 96,225,40960
cv 82,84,85
cvaa 16,192,160,232
ex 87
mvaa 16,200,164
s_mov_b32 m0,s69
ex 112
mvaa 16,208,168
ds 249,8,77,0
mvaa 16,216,172
ra 100,225,41984
cv 83,86,87
cvaa 20,196,160,232
ex 113
mvaa 20,204,164
ra 104,225,43008
cv 84,112,113
mvaa 20,212,168
ex 114
mvaa 20,220,172
ex 115
cvaa 48,224,160,232
ra 108,225,44032
cv 85,114,115
mvaa 48,232,164
ds 249,8,77,1024
mvaa 48,240,168
ex 116
mvaa 48,248,172
ra 112,225,45056
cvaa 52,228,160,232
ex 117
mvaa 52,236,164
ex 118
mvaa 52,244,168
ra 116,225,46080
cv 86,116,117
mvaa 52,252,172
ex 119
cvaa 24,192,176,232
ds 249,8,77,2048
mvaa 24,200,180
ra 120,225,47104
cv 87,118,119
mvaa 24,208,184
ex 88
mvaa 24,216,188
ex 89
cvaa 28,196,176,232
ra 124,225,48128
cv 88,88,89
mvaa 28,204,180
ex 90
mvaa 28,212,184
ex 91
mvaa 28,220,188
ds 249,8,77,3072
cvaa 56,224,176,232
ex 92
mvaa 56,232,180
ex 93
mvaa 56,240,184
ex 94
mvaa 56,248,188
buffer_store_dwordx4 v[250:253],v244,s[8:11],s44 offen sc0 sc1
cvaa 60,228,176,232
ex 95
mvaa 60,236,180
ex 120
mvaa 60,244,184
ex 121
mvaa 60,252,188
s_waitcnt vmcnt(16) lgkmcnt(0)
s_barrier
maav 0,64,64
cv 89,90,91
cv 90,92,93
maav 0,96,68
s_mov_b32 m0,s74
ex 122
maav 4,68,64
ds 255,8,78,0
maav 4,100,68
ex 123
maav 8,72,64
ex 124
maav 8,104,68
ex 125
maav 12,76,64
cv 91,94,95
cv 92,120,121
maav 12,108,68
ex 126
v_smfmac_f32_16x16x64_bf16 v[144:147],v[228:231],v[64:71],v248
cv 93,122,123
maav 16,80,64
ex 127
maav 16,112,68
buffer_load_dword v247,v245,s[36:39],s85 offen
maav 20,84,64
cv 94,124,125
cv 95,126,127
maav 20,116,68
maav 24,88,64
maav 24,120,68
ra 192,224,32768
maav 28,92,64
ds 255,8,78,1024
maav 28,124,68
maav 32,64,72
ra 196,224,33792
maav 32,96,76
s_add_u32 s85,s85,131072
maav 36,68,72
ra 200,224,34816
maav 36,100,76
maav 40,72,72
ds 255,8,78,2048
maav 40,104,76
maav 44,76,72
ra 204,224,35840
maav 44,108,76
v_smfmac_f32_16x16x64_bf16 v[148:151],v[228:231],v[72:79],v248
ra 208,224,36864
maav 48,80,72
maav 48,112,76
maav 52,84,72
ds 255,8,78,3072
maav 52,116,76
ra 212,224,37888
maav 56,88,72
ex 0
maav 56,120,76
ra 216,224,38912
maav 60,92,72
ex 1
maav 60,124,76
s_mov_b32 m0,s75
ex 2
mvav 160,64,80
ra 220,224,39936
cv 0,0,1
mvav 160,96,84
ds 255,8,79,0
mvav 164,68,80
ex 3
mvav 164,100,84
ra 224,224,40960
cv 1,2,3
mvav 168,72,80
ex 4
mvav 168,104,84
ra 228,224,41984
mvav 172,76,80
ds 255,8,79,1024
mvav 172,108,84
s_add_u32 s46,s46,16384
ex 5
v_smfmac_f32_16x16x64_bf16 v[152:155],v[228:231],v[80:87],v248
ra 232,224,43008
mvav 176,80,80
s_add_u32 s47,s47,32768
ex 6
mvav 176,112,84
s_add_u32 s95,s47,32768
ex 7
mvav 180,84,80
ra 236,224,44032
cv 2,4,5
mvav 180,116,84
ex 32
mvav 184,88,80
ds 255,8,79,2048
mvav 184,120,84
ex 33
mvav 188,92,80
ra 240,224,45056
cv 3,6,7
mvav 188,124,84
ex 34
mvav 192,64,88
ra 244,224,46080
cv 4,32,33
mvav 192,96,92
ex 35
mvav 196,68,88
ex 36
mvav 196,100,92
ds 255,8,79,3072
mvav 200,72,88
ra 248,224,47104
cv 5,34,35
mvav 200,104,92
s_cmp_ge_i32 s47,s46
ex 37
mvav 204,76,88
s_cselect_b32 s78,s95,s46
ex 38
mvav 204,108,92
s_cselect_b32 s79,s95,268435456
ex 39
v_smfmac_f32_16x16x64_bf16 v[156:159],v[228:231],v[88:95],v248
ra 252,224,48128
mvav 208,80,88
s_add_u32 s79,s79,16384
ex 8
mvav 208,112,92
ex 9
mvav 212,84,88
ex 10
mvav 212,116,92
ex 11
mvav 216,88,88
cv 6,36,37
cv 7,38,39
mvav 216,120,92
ex 12
mvav 220,92,88
ex 13
mvav 220,124,92
s_waitcnt vmcnt(16) lgkmcnt(0)
s_barrier
cvaa 64,192,128,232
s_bitcmp0_b32 s82,s81
s_cbranch_scc1 L_loop0
s_bitcmp0_b32 s15,s81
s_cbranch_scc1 L_eva_r0
L_short0:
v_add_f32_e32 v226,v152,v153
v_add_f32_e32 v227,v156,v157
v_subrev_u32_e32 v236,0x23800000,v226
v_subrev_u32_e32 v237,0x23800000,v227
v_max_u32_e32 v236,v236,v237
v_cmp_lt_u32_e64 s[40:41],s48,v236
v_rcp_f32_e32 v226,v226
v_rcp_f32_e32 v227,v227
s_add_u32 s86,s13,4096
v_mov_b32_e32 v236,0
v_mov_b32_e32 v237,0
v_mov_b32_e32 v238,0
v_mov_b32_e32 v239,0
v_mov_b64_e32 v[152:153],0
v_mov_b64_e32 v[156:157],0
v_mul_f32_e32 v226,v242,v226
v_mul_f32_e32 v227,v243,v227
v_add_u32_e32 v225,s94,v225
v_pk_mul_f32 v[160:161],v[160:161],v[226:227] op_sel:[0,0] op_sel_hi:[1,0]
v_pk_mul_f32 v[162:163],v[162:163],v[226:227] op_sel:[0,0] op_sel_hi:[1,0]
v_pk_mul_f32 v[164:165],v[164:165],v[226:227] op_sel:[0,0] op_sel_hi:[1,0]
v_pk_mul_f32 v[166:167],v[166:167],v[226:227] op_sel:[0,0] op_sel_hi:[1,0]
cv 160,160,161
cv 161,162,163
cv 162,164,165
cv 163,166,167
buffer_store_dwordx4 v[160:163],v244,s[8:11],s13 offen offset:0 sc0 sc1
v_pk_mul_f32 v[168:169],v[168:169],v[226:227] op_sel:[0,0] op_sel_hi:[1,0]
v_pk_mul_f32 v[170:171],v[170:171],v[226:227] op_sel:[0,0] op_sel_hi:[1,0]
v_pk_mul_f32 v[172:173],v[172:173],v[226:227] op_sel:[0,0] op_sel_hi:[1,0]
v_pk_mul_f32 v[174:175],v[174:175],v[226:227] op_sel:[0,0] op_sel_hi:[1,0]
cv 168,168,169
cv 169,170,171
cv 170,172,173
cv 171,174,175
buffer_store_dwordx4 v[168:171],v244,s[8:11],s13 offen offset:2048 sc0 sc1
v_pk_mul_f32 v[176:177],v[176:177],v[226:227] op_sel:[0,0] op_sel_hi:[1,0]
v_pk_mul_f32 v[178:179],v[178:179],v[226:227] op_sel:[0,0] op_sel_hi:[1,0]
v_pk_mul_f32 v[180:181],v[180:181],v[226:227] op_sel:[0,0] op_sel_hi:[1,0]
v_pk_mul_f32 v[182:183],v[182:183],v[226:227] op_sel:[0,0] op_sel_hi:[1,0]
cv 176,176,177
cv 177,178,179
cv 178,180,181
v_mfma_f32_32x32x16_bf16 v[160:175],v[236:239],v[236:239],0
cv 179,182,183
buffer_store_dwordx4 v[176:179],v244,s[8:11],s86 offen offset:0 sc0 sc1
v_pk_mul_f32 v[184:185],v[184:185],v[226:227] op_sel:[0,0] op_sel_hi:[1,0]
v_pk_mul_f32 v[186:187],v[186:187],v[226:227] op_sel:[0,0] op_sel_hi:[1,0]
v_pk_mul_f32 v[188:189],v[188:189],v[226:227] op_sel:[0,0] op_sel_hi:[1,0]
v_pk_mul_f32 v[190:191],v[190:191],v[226:227] op_sel:[0,0] op_sel_hi:[1,0]
cv 184,184,185
cv 185,186,187
cv 186,188,189
cv 187,190,191
buffer_store_dwordx4 v[184:187],v244,s[8:11],s86 offen offset:2048 sc0 sc1
v_pk_mul_f32 v[192:193],v[192:193],v[226:227] op_sel:[0,1] op_sel_hi:[1,1]
v_pk_mul_f32 v[194:195],v[194:195],v[226:227] op_sel:[0,1] op_sel_hi:[1,1]
v_pk_mul_f32 v[196:197],v[196:197],v[226:227] op_sel:[0,1] op_sel_hi:[1,1]
v_pk_mul_f32 v[198:199],v[198:199],v[226:227] op_sel:[0,1] op_sel_hi:[1,1]
cv 192,192,193
cv 193,194,195
cv 194,196,197
v_mfma_f32_32x32x16_bf16 v[176:191],v[236:239],v[236:239],0
cv 195,198,199
buffer_store_dwordx4 v[192:195],v244,s[8:11],s13 offen offset:1024 sc0 sc1
v_pk_mul_f32 v[200:201],v[200:201],v[226:227] op_sel:[0,1] op_sel_hi:[1,1]
v_pk_mul_f32 v[202:203],v[202:203],v[226:227] op_sel:[0,1] op_sel_hi:[1,1]
v_pk_mul_f32 v[204:205],v[204:205],v[226:227] op_sel:[0,1] op_sel_hi:[1,1]
v_pk_mul_f32 v[206:207],v[206:207],v[226:227] op_sel:[0,1] op_sel_hi:[1,1]
cv 200,200,201
cv 201,202,203
cv 202,204,205
cv 203,206,207
buffer_store_dwordx4 v[200:203],v244,s[8:11],s13 offen offset:3072 sc0 sc1
v_pk_mul_f32 v[208:209],v[208:209],v[226:227] op_sel:[0,1] op_sel_hi:[1,1]
v_pk_mul_f32 v[210:211],v[210:211],v[226:227] op_sel:[0,1] op_sel_hi:[1,1]
v_pk_mul_f32 v[212:213],v[212:213],v[226:227] op_sel:[0,1] op_sel_hi:[1,1]
v_pk_mul_f32 v[214:215],v[214:215],v[226:227] op_sel:[0,1] op_sel_hi:[1,1]
cv 208,208,209
cv 209,210,211
cv 210,212,213
v_mfma_f32_32x32x16_bf16 v[192:207],v[236:239],v[236:239],0
cv 211,214,215
buffer_store_dwordx4 v[208:211],v244,s[8:11],s86 offen offset:1024 sc0 sc1
v_pk_mul_f32 v[216:217],v[216:217],v[226:227] op_sel:[0,1] op_sel_hi:[1,1]
v_pk_mul_f32 v[218:219],v[218:219],v[226:227] op_sel:[0,1] op_sel_hi:[1,1]
v_pk_mul_f32 v[220:221],v[220:221],v[226:227] op_sel:[0,1] op_sel_hi:[1,1]
v_pk_mul_f32 v[222:223],v[222:223],v[226:227] op_sel:[0,1] op_sel_hi:[1,1]
cv 216,216,217
cv 217,218,219
cv 218,220,221
cv 219,222,223
buffer_store_dwordx4 v[216:219],v244,s[8:11],s86 offen offset:3072 sc0 sc1
s_nop 1
v_mfma_f32_32x32x16_bf16 v[208:223],v[236:239],v[236:239],0
L_eva_r0:
s_cmp_lg_u32 s81,14
s_cbranch_scc1 L_loop0
s_add_u32 s81,s81,1
s_bitcmp1_b32 170,s20
s_cbranch_scc1 L_odd
s_branch L_last
L_k_ec1:
buffer_store_dwordx4 a[252:255],v244,s[8:11],s35 offen offset:1024 sc0 sc1
s_branch L_k_ecr1
L_q_ack:
s_mov_b32 s86,65536
L_q_ack2:
s_sleep 8
buffer_load_dword v246,off,s[8:11],s35 offset:1024 sc1
)"
R"(s_waitcnt vmcnt(0)
v_readfirstlane_b32 s87,v246
s_nop 0
s_cmp_eq_u32 s87,s32
s_cbranch_scc1 L_q_acked
s_sub_u32 s86,s86,1
s_cbranch_scc0 L_q_ack2
s_branch L_q_acked
L_q_spin:
s_mov_b32 s86,65536
L_q_spin1:
s_sleep 8
buffer_load_dword v248,v250,s[8:11],0 offen sc1
s_or_b32 s87,%1,s12
s_cbranch_scc1 L_q_spin2
buffer_load_dwordx4 a[252:255],v244,s[8:11],s35 offen sc1
s_waitcnt vmcnt(0)
buffer_store_dwordx4 a[252:255],v244,s[8:11],s35 offen offset:1024 sc0 sc1
L_q_spin2:
s_waitcnt vmcnt(0)
v_and_b32_e32 v248,v242,v248
v_cmp_ne_u32_e64 s[64:65],v243,v248
s_nop 1
s_cmp_eq_u64 s[64:65],0
s_cbranch_scc1 L_q_ok
s_sub_u32 s86,s86,1
s_cbranch_scc0 L_q_spin1
s_branch L_q_ok
L_bx:
s_mov_b32 s87,65536
L_bx1:
s_sleep 2
buffer_load_dwordx4 v[176:179],v244,s[8:11],s98 offen sc1
buffer_load_dwordx2 v[168:169],v253,s[8:11],s98 offen offset:1024 sc1
buffer_load_dwordx2 v[172:173],v253,s[8:11],s98 offen offset:1536 sc1
s_waitcnt vmcnt(0)
v_max3_u32 v180,v176,v177,v178
v_max3_u32 v180,v180,v179,v168
v_max3_u32 v180,v180,v169,v172
v_max_u32_e32 v180,v180,v173
v_cmp_eq_u32_e64 s[40:41],-1,v180
s_nop 1
s_cmp_eq_u64 s[40:41],0
s_cbranch_scc1 L_bxr
s_sub_u32 s87,s87,1
s_cbranch_scc0 L_bx1
s_branch L_bxr
L_k_ev:
L_v_ev:
buffer_load_dwordx4 a[252:255],v244,s[8:11],s35 offen sc1
s_branch L_k_evr
.fill 194,4,0xbf800000
L_cw1:
s_mov_b64 exec,-1
s_waitcnt lgkmcnt(0)
s_barrier
cvaa 0,192,128,232
mvaa 0,200,132
s_add_u32 s81,s81,1
ex 78
mvaa 0,208,136
s_add_u32 s44,s44,1024
ex 79
mvaa 0,216,140
cv 72,72,73
cv 73,74,75
cvaa 4,196,128,232
ex 104
mvaa 4,204,132
s_mov_b32 m0,s68
ex 105
mvaa 4,212,136
ds 249,8,76,0
mvaa 4,220,140
ex 106
cvaa 32,224,128,232
ra 64,225,32768
cv 74,76,77
mvaa 32,232,132
ex 107
mvaa 32,240,136
ra 68,225,33792
cv 75,78,79
mvaa 32,248,140
ex 108
cvaa 36,228,128,232
ex 109
mvaa 36,236,132
ds 249,8,76,1024
mvaa 36,244,136
ra 72,225,34816
cv 76,104,105
mvaa 36,252,140
ex 110
cvaa 8,192,144,232
ra 76,225,35840
cv 77,106,107
mvaa 8,200,148
ex 111
mvaa 8,208,152
ex 80
mvaa 8,216,156
ra 80,225,36864
cv 78,108,109
cvaa 12,196,144,232
ds 249,8,76,2048
mvaa 12,204,148
ex 81
mvaa 12,212,152
ra 84,225,37888
cv 79,110,111
mvaa 12,220,156
ex 82
cvaa 40,224,144,232
ex 83
mvaa 40,232,148
ra 88,225,38912
cv 80,80,81
mvaa 40,240,152
ex 84
mvaa 40,248,156
ds 249,8,76,3072
cvaa 44,228,144,232
ra 92,225,39936
cv 81,82,83
mvaa 44,236,148
ex 85
mvaa 44,244,152
ex 86
mvaa 44,252,156
ra 96,225,40960
cv 82,84,85
cvaa 16,192,160,232
ex 87
mvaa 16,200,164
s_mov_b32 m0,s69
ex 112
mvaa 16,208,168
ra 100,225,41984
cv 83,86,87
mvaa 16,216,172
ds 249,8,77,0
cvaa 20,196,160,232
ex 113
mvaa 20,204,164
ra 104,225,43008
cv 84,112,113
mvaa 20,212,168
ex 114
mvaa 20,220,172
ex 115
cvaa 48,224,160,232
ra 108,225,44032
cv 85,114,115
mvaa 48,232,164
ex 116
mvaa 48,240,168
ds 249,8,77,1024
mvaa 48,248,172
ra 112,225,45056
cvaa 52,228,160,232
ex 117
mvaa 52,236,164
ex 118
mvaa 52,244,168
ra 116,225,46080
cv 86,116,117
mvaa 52,252,172
ex 119
cvaa 24,192,176,232
ra 120,225,47104
cv 87,118,119
mvaa 24,200,180
ds 249,8,77,2048
mvaa 24,208,184
ex 88
mvaa 24,216,188
ex 89
cvaa 28,196,176,232
ra 124,225,48128
cv 88,88,89
mvaa 28,204,180
ex 90
mvaa 28,212,184
ex 91
mvaa 28,220,188
ex 92
cvaa 56,224,176,232
ds 249,8,77,3072
mvaa 56,232,180
ex 93
mvaa 56,240,184
ex 94
mvaa 56,248,188
ex 95
cvaa 60,228,176,232
mvaa 60,236,180
ex 120
mvaa 60,244,184
ex 121
mvaa 60,252,188
s_waitcnt vmcnt(8) lgkmcnt(0)
s_barrier
maav 0,64,64
cv 89,90,91
cv 90,92,93
maav 0,96,68
ex 122
maav 4,68,64
s_mov_b32 m0,s74
ex 123
maav 4,100,68
ds 255,8,78,0
maav 8,72,64
ex 124
maav 8,104,68
ex 125
maav 12,76,64
cv 91,94,95
cv 92,120,121
maav 12,108,68
ex 126
v_smfmac_f32_16x16x64_bf16 v[144:147],v[228:231],v[64:71],v248
cv 93,122,123
maav 16,80,64
ex 127
maav 16,112,68
cv 94,124,125
cv 95,126,127
maav 20,84,64
buffer_load_dword v247,v245,s[36:39],s85 offen
maav 20,116,68
maav 24,88,64
maav 24,120,68
ra 192,224,32768
maav 28,92,64
maav 28,124,68
ds 255,8,78,1024
maav 32,64,72
ra 196,224,33792
maav 32,96,76
s_add_u32 s85,s85,131072
maav 36,68,72
ra 200,224,34816
maav 36,100,76
maav 40,72,72
maav 40,104,76
ds 255,8,78,2048
maav 44,76,72
ra 204,224,35840
maav 44,108,76
v_smfmac_f32_16x16x64_bf16 v[148:151],v[228:231],v[72:79],v248
ra 208,224,36864
maav 48,80,72
maav 48,112,76
maav 52,84,72
ra 212,224,37888
maav 52,116,76
ds 255,8,78,3072
maav 56,88,72
ex 0
maav 56,120,76
ra 216,224,38912
maav 60,92,72
ex 1
maav 60,124,76
ex 2
zvav 160,64,80
ra 220,224,39936
cv 0,0,1
mvav 160,96,84
s_mov_b32 m0,s75
ex 3
zvav 164,68,80
ds 255,8,79,0
mvav 164,100,84
ra 224,224,40960
cv 1,2,3
zvav 168,72,80
ex 4
mvav 168,104,84
ra 228,224,41984
zvav 172,76,80
s_add_u32 s46,s46,16384
ex 5
mvav 172,108,84
ds 255,8,79,1024
v_smfmac_f32_16x16x64_bf16 v[152:155],v[228:231],v[80:87],v248
ra 232,224,43008
zvav 176,80,80
s_add_u32 s47,s47,32768
ex 6
mvav 176,112,84
s_add_u32 s95,s47,32768
ex 7
zvav 180,84,80
ra 236,224,44032
cv 2,4,5
mvav 180,116,84
ex 32
zvav 184,88,80
ex 33
mvav 184,120,84
ds 255,8,79,2048
zvav 188,92,80
ra 240,224,45056
cv 3,6,7
mvav 188,124,84
ex 34
mvav 192,64,88
ra 244,224,46080
cv 4,32,33
mvav 192,96,92
ex 35
mvav 196,68,88
ex 36
mvav 196,100,92
ra 248,224,47104
cv 5,34,35
mvav 200,72,88
ds 255,8,79,3072
mvav 200,104,92
s_cmp_ge_i32 s47,s46
ex 37
mvav 204,76,88
s_cselect_b32 s78,s95,s46
ex 38
mvav 204,108,92
s_cselect_b32 s79,s95,268435456
ex 39
v_smfmac_f32_16x16x64_bf16 v[156:159],v[228:231],v[88:95],v248
ra 252,224,48128
mvav 208,80,88
s_add_u32 s79,s79,16384
ex 8
mvav 208,112,92
ex 9
mvav 212,84,88
ex 10
mvav 212,116,92
ex 11
mvav 216,88,88
cv 6,36,37
cv 7,38,39
mvav 216,120,92
ex 12
mvav 220,92,88
ex 13
mvav 220,124,92
s_waitcnt vmcnt(16) lgkmcnt(0)
s_barrier
cvaa 64,192,128,232
s_bitcmp0_b32 s82,s81
s_cbranch_scc1 L_loop1
s_bitcmp1_b32 s15,s81
s_cbranch_scc1 L_short1
.p2align 6
L_loop1:
mvaa 64,200,132
s_add_u32 s81,s81,1
ex 14
mvaa 64,208,136
s_add_u32 s44,s44,1024
ex 15
mvaa 64,216,140
cv 8,8,9
cv 9,10,11
cvaa 68,196,128,232
ex 40
mvaa 68,204,132
s_mov_b32 m0,s70
ex 41
mvaa 68,212,136
ds 249,8,78,0
mvaa 68,220,140
ex 42
cvaa 96,224,128,232
ra 64,225,0
cv 10,12,13
mvaa 96,232,132
ex 43
mvaa 96,240,136
ra 68,225,1024
cv 11,14,15
mvaa 96,248,140
ex 44
cvaa 100,228,128,232
ex 45
mvaa 100,236,132
ds 249,8,78,1024
mvaa 100,244,136
ra 72,225,2048
cv 12,40,41
mvaa 100,252,140
ex 46
cvaa 72,192,144,232
ra 76,225,3072
cv 13,42,43
mvaa 72,200,148
ex 47
mvaa 72,208,152
ex 16
mvaa 72,216,156
ra 80,225,4096
cv 14,44,45
cvaa 76,196,144,232
ds 249,8,78,2048
mvaa 76,204,148
ex 17
mvaa 76,212,152
ra 84,225,5120
cv 15,46,47
mvaa 76,220,156
ex 18
cvaa 104,224,144,232
ex 19
mvaa 104,232,148
ra 88,225,6144
cv 16,16,17
mvaa 104,240,152
ex 20
mvaa 104,248,156
ds 249,8,78,3072
cvaa 108,228,144,232
ra 92,225,7168
cv 17,18,19
mvaa 108,236,148
ex 21
mvaa 108,244,152
ex 22
mvaa 108,252,156
ra 96,225,8192
cv 18,20,21
cvaa 80,192,160,232
ex 23
mvaa 80,200,164
s_mov_b32 m0,s71
ex 48
mvaa 80,208,168
ra 100,225,9216
cv 19,22,23
mvaa 80,216,172
ds 249,8,79,0
cvaa 84,196,160,232
ex 49
mvaa 84,204,164
ra 104,225,10240
cv 20,48,49
mvaa 84,212,168
ex 50
mvaa 84,220,172
ex 51
cvaa 112,224,160,232
ra 108,225,11264
cv 21,50,51
mvaa 112,232,164
ex 52
mvaa 112,240,168
ds 249,8,79,1024
mvaa 112,248,172
ra 112,225,12288
cvaa 116,228,160,232
ex 53
mvaa 116,236,164
ex 54
mvaa 116,244,168
ra 116,225,13312
cv 22,52,53
mvaa 116,252,172
ex 55
cvaa 88,192,176,232
ra 120,225,14336
cv 23,54,55
mvaa 88,200,180
ds 249,8,79,2048
mvaa 88,208,184
ex 24
mvaa 88,216,188
ex 25
cvaa 92,196,176,232
ra 124,225,15360
cv 24,24,25
mvaa 92,204,180
ex 26
mvaa 92,212,184
ex 27
mvaa 92,220,188
ex 28
cvaa 120,224,176,232
ds 249,8,79,3072
mvaa 120,232,180
ex 29
mvaa 120,240,184
ex 30
mvaa 120,248,188
ex 31
cvaa 124,228,176,232
buffer_store_dwordx4 v[250:253],v244,s[8:11],s44 offen sc0 sc1
mvaa 124,236,180
ex 56
mvaa 124,244,184
ex 57
mvaa 124,252,188
s_waitcnt vmcnt(16) lgkmcnt(0)
s_barrier
maav 0,64,0
cv 25,26,27
cv 26,28,29
maav 0,96,4
ex 58
maav 4,68,0
s_mov_b32 m0,s72
ex 59
maav 4,100,4
ds 255,8,76,0
maav 8,72,0
ex 60
maav 8,104,4
ex 61
maav 12,76,0
s_cmp_lg_u32 s80,s81
cv 27,30,31
cv 28,56,57
maav 12,108,4
ex 62
v_smfmac_f32_16x16x64_bf16 v[144:147],v[228:231],v[0:7],v248
cv 29,58,59
maav 16,80,0
ex 63
maav 16,112,4
cv 30,60,61
cv 31,62,63
maav 20,84,0
buffer_load_dword v247,v246,s[24:27],s49 offen
maav 20,116,4
maav 24,88,0
s_cbranch_scc1 L_evb_r1
v_add_u32_e32 v224,s94,v224
s_lshl_b32 s86,s12,12
s_add_u32 s86,s86,131072
v_add_u32_e32 v236,s86,v244
ra 160,236,0
ra 164,236,1024
ra 168,236,2048
ra 172,236,3072
ra 176,236,16384
ra 180,236,17408
ra 184,236,18432
ra 188,236,19456
L_evb_r1:
maav 24,120,4
ra 192,224,0
maav 28,92,0
maav 28,124,4
ds 255,8,76,1024
maav 32,64,8
ra 196,224,1024
maav 32,96,12
s_add_u32 s49,s49,32768
maav 36,68,8
ra 200,224,2048
maav 36,100,12
maav 40,72,8
maav 40,104,12
ds 255,8,76,2048
maav 44,76,8
ra 204,224,3072
maav 44,108,12
v_smfmac_f32_16x16x64_bf16 v[148:151],v[228:231],v[8:15],v248
ra 208,224,4096
maav 48,80,8
maav 48,112,12
maav 52,84,8
ra 212,224,5120
maav 52,116,12
ds 255,8,76,3072
maav 56,88,8
ex 64
maav 56,120,12
ra 216,224,6144
maav 60,92,8
ex 65
maav 60,124,12
ex 66
mvav 160,64,16
ra 220,224,7168
cv 64,64,65
mvav 160,96,20
s_mov_b32 m0,s73
ex 67
mvav 164,68,16
ds 255,8,77,0
mvav 164,100,20
ra 224,224,8192
cv 65,66,67
mvav 168,72,16
ex 68
mvav 168,104,20
ra 228,224,9216
mvav 172,76,16
s_add_u32 s46,s46,16384
ex 69
mvav 172,108,20
ds 255,8,77,1024
v_smfmac_f32_16x16x64_bf16 v[152:155],v[228:231],v[16:23],v248
ra 232,224,10240
mvav 176,80,16
s_add_u32 s47,s47,32768
ex 70
mvav 176,112,20
s_add_u32 s95,s47,32768
ex 71
mvav 180,84,16
ra 236,224,11264
cv 66,68,69
mvav 180,116,20
ex 96
mvav 184,88,16
ex 97
mvav 184,120,20
ds 255,8,77,2048
mvav 188,92,16
ra 240,224,12288
cv 67,70,71
mvav 188,124,20
ex 98
mvav 192,64,24
ra 244,224,13312
cv 68,96,97
mvav 192,96,28
ex 99
mvav 196,68,24
ex 100
mvav 196,100,28
ra 248,224,14336
cv 69,98,99
mvav 200,72,24
ds 255,8,77,3072
mvav 200,104,28
s_cmp_ge_i32 s47,s46
ex 101
mvav 204,76,24
s_cselect_b32 s76,s95,s46
ex 102
mvav 204,108,28
s_cselect_b32 s77,s95,268435456
ex 103
v_smfmac_f32_16x16x64_bf16 v[156:159],v[228:231],v[24:31],v248
ra 252,224,15360
mvav 208,80,24
s_add_u32 s77,s77,16384
ex 72
mvav 208,112,28
ex 73
mvav 212,84,24
ex 74
mvav 212,116,28
ex 75
mvav 216,88,24
cv 70,100,101
cv 71,102,103
mvav 216,120,28
ex 76
mvav 220,92,24
ex 77
mvav 220,124,28
s_waitcnt vmcnt(16) lgkmcnt(0)
s_barrier
cvaa 0,192,128,232
mvaa 0,200,132
s_add_u32 s81,s81,1
ex 78
mvaa 0,208,136
s_add_u32 s44,s44,1024
ex 79
mvaa 0,216,140
cv 72,72,73
cv 73,74,75
cvaa 4,196,128,232
ex 104
mvaa 4,204,132
s_mov_b32 m0,s68
ex 105
mvaa 4,212,136
ds 249,8,76,0
mvaa 4,220,140
ex 106
cvaa 32,224,128,232
ra 64,225,32768
cv 74,76,77
mvaa 32,232,132
ex 107
mvaa 32,240,136
ra 68,225,33792
cv 75,78,79
mvaa 32,248,140
ex 108
cvaa 36,228,128,232
ex 109
mvaa 36,236,132
ds 249,8,76,1024
mvaa 36,244,136
ra 72,225,34816
cv 76,104,105
mvaa 36,252,140
ex 110
cvaa 8,192,144,232
ra 76,225,35840
cv 77,106,107
mvaa 8,200,148
ex 111
mvaa 8,208,152
ex 80
mvaa 8,216,156
ra 80,225,36864
cv 78,108,109
cvaa 12,196,144,232
ds 249,8,76,2048
mvaa 12,204,148
ex 81
mvaa 12,212,152
ra 84,225,37888
cv 79,110,111
mvaa 12,220,156
ex 82
cvaa 40,224,144,232
ex 83
mvaa 40,232,148
ra 88,225,38912
cv 80,80,81
mvaa 40,240,152
ex 84
mvaa 40,248,156
ds 249,8,76,3072
cvaa 44,228,144,232
ra 92,225,39936
cv 81,82,83
mvaa 44,236,148
ex 85
mvaa 44,244,152
ex 86
mvaa 44,252,156
ra 96,225,40960
cv 82,84,85
cvaa 16,192,160,232
ex 87
mvaa 16,200,164
s_mov_b32 m0,s69
ex 112
mvaa 16,208,168
ra 100,225,41984
cv 83,86,87
mvaa 16,216,172
ds 249,8,77,0
cvaa 20,196,160,232
ex 113
mvaa 20,204,164
ra 104,225,43008
cv 84,112,113
mvaa 20,212,168
ex 114
mvaa 20,220,172
ex 115
cvaa 48,224,160,232
ra 108,225,44032
cv 85,114,115
mvaa 48,232,164
ex 116
mvaa 48,240,168
ds 249,8,77,1024
mvaa 48,248,172
ra 112,225,45056
cvaa 52,228,160,232
ex 117
mvaa 52,236,164
ex 118
mvaa 52,244,168
ra 116,225,46080
cv 86,116,117
mvaa 52,252,172
ex 119
cvaa 24,192,176,232
ra 120,225,47104
cv 87,118,119
mvaa 24,200,180
ds 249,8,77,2048
mvaa 24,208,184
ex 88
mvaa 24,216,188
ex 89
cvaa 28,196,176,232
ra 124,225,48128
cv 88,88,89
mvaa 28,204,180
ex 90
mvaa 28,212,184
ex 91
mvaa 28,220,188
ex 92
cvaa 56,224,176,232
ds 249,8,77,3072
mvaa 56,232,180
ex 93
mvaa 56,240,184
ex 94
mvaa 56,248,188
ex 95
cvaa 60,228,176,232
buffer_store_dwordx4 v[250:253],v244,s[8:11],s44 offen sc0 sc1
mvaa 60,236,180
ex 120
mvaa 60,244,184
ex 121
mvaa 60,252,188
s_waitcnt vmcnt(16) lgkmcnt(0)
s_barrier
maav 0,64,64
cv 89,90,91
cv 90,92,93
maav 0,96,68
ex 122
maav 4,68,64
s_mov_b32 m0,s74
ex 123
maav 4,100,68
ds 255,8,78,0
maav 8,72,64
ex 124
maav 8,104,68
ex 125
maav 12,76,64
cv 91,94,95
cv 92,120,121
maav 12,108,68
ex 126
v_smfmac_f32_16x16x64_bf16 v[144:147],v[228:231],v[64:71],v248
cv 93,122,123
maav 16,80,64
ex 127
maav 16,112,68
cv 94,124,125
cv 95,126,127
maav 20,84,64
buffer_load_dword v247,v245,s[36:39],s85 offen
maav 20,116,68
maav 24,88,64
maav 24,120,68
ra 192,224,32768
maav 28,92,64
maav 28,124,68
ds 255,8,78,1024
maav 32,64,72
ra 196,224,33792
maav 32,96,76
s_add_u32 s85,s85,131072
maav 36,68,72
ra 200,224,34816
maav 36,100,76
maav 40,72,72
maav 40,104,76
ds 255,8,78,2048
maav 44,76,72
ra 204,224,35840
maav 44,108,76
v_smfmac_f32_16x16x64_bf16 v[148:151],v[228:231],v[72:79],v248
ra 208,224,36864
maav 48,80,72
maav 48,112,76
maav 52,84,72
ra 212,224,37888
maav 52,116,76
ds 255,8,78,3072
maav 56,88,72
ex 0
maav 56,120,76
ra 216,224,38912
maav 60,92,72
ex 1
maav 60,124,76
ex 2
mvav 160,64,80
ra 220,224,39936
cv 0,0,1
mvav 160,96,84
s_mov_b32 m0,s75
ex 3
mvav 164,68,80
ds 255,8,79,0
mvav 164,100,84
ra 224,224,40960
cv 1,2,3
mvav 168,72,80
ex 4
mvav 168,104,84
ra 228,224,41984
mvav 172,76,80
s_add_u32 s46,s46,16384
ex 5
mvav 172,108,84
ds 255,8,79,1024
v_smfmac_f32_16x16x64_bf16 v[152:155],v[228:231],v[80:87],v248
ra 232,224,43008
mvav 176,80,80
s_add_u32 s47,s47,32768
ex 6
mvav 176,112,84
s_add_u32 s95,s47,32768
ex 7
mvav 180,84,80
ra 236,224,44032
cv 2,4,5
mvav 180,116,84
ex 32
mvav 184,88,80
ex 33
mvav 184,120,84
ds 255,8,79,2048
mvav 188,92,80
ra 240,224,45056
cv 3,6,7
mvav 188,124,84
ex 34
mvav 192,64,88
ra 244,224,46080
cv 4,32,33
mvav 192,96,92
ex 35
mvav 196,68,88
ex 36
mvav 196,100,92
ra 248,224,47104
cv 5,34,35
mvav 200,72,88
ds 255,8,79,3072
mvav 200,104,92
s_cmp_ge_i32 s47,s46
ex 37
mvav 204,76,88
s_cselect_b32 s78,s95,s46
ex 38
mvav 204,108,92
s_cselect_b32 s79,s95,268435456
ex 39
v_smfmac_f32_16x16x64_bf16 v[156:159],v[228:231],v[88:95],v248
ra 252,224,48128
mvav 208,80,88
s_add_u32 s79,s79,16384
ex 8
mvav 208,112,92
ex 9
mvav 212,84,88
ex 10
mvav 212,116,92
ex 11
mvav 216,88,88
cv 6,36,37
cv 7,38,39
mvav 216,120,92
ex 12
mvav 220,92,88
ex 13
mvav 220,124,92
s_waitcnt vmcnt(16) lgkmcnt(0)
s_barrier
cvaa 64,192,128,232
s_bitcmp0_b32 s82,s81
s_cbranch_scc1 L_loop1
s_bitcmp0_b32 s15,s81
s_cbranch_scc1 L_eva_r1
L_short1:
v_add_f32_e32 v226,v152,v153
v_add_f32_e32 v227,v156,v157
v_subrev_u32_e32 v236,0x23800000,v226
v_subrev_u32_e32 v237,0x23800000,v227
v_max_u32_e32 v236,v236,v237
v_cmp_lt_u32_e64 s[40:41],s48,v236
v_rcp_f32_e32 v226,v226
v_rcp_f32_e32 v227,v227
s_add_u32 s86,s13,4096
v_mov_b32_e32 v236,0
v_mov_b32_e32 v237,0
v_mov_b32_e32 v238,0
v_mov_b32_e32 v239,0
v_mov_b64_e32 v[152:153],0
v_mov_b64_e32 v[156:157],0
v_mul_f32_e32 v226,v242,v226
v_mul_f32_e32 v227,v243,v227
v_add_u32_e32 v225,s94,v225
v_pk_mul_f32 v[160:161],v[160:161],v[226:227] op_sel:[0,0] op_sel_hi:[1,0]
v_pk_mul_f32 v[162:163],v[162:163],v[226:227] op_sel:[0,0] op_sel_hi:[1,0]
v_pk_mul_f32 v[164:165],v[164:165],v[226:227] op_sel:[0,0] op_sel_hi:[1,0]
v_pk_mul_f32 v[166:167],v[166:167],v[226:227] op_sel:[0,0] op_sel_hi:[1,0]
cv 160,160,161
cv 161,162,163
cv 162,164,165
cv 163,166,167
buffer_store_dwordx4 v[160:163],v244,s[8:11],s13 offen offset:0 sc0 sc1
v_pk_mul_f32 v[168:169],v[168:169],v[226:227] op_sel:[0,0] op_sel_hi:[1,0]
v_pk_mul_f32 v[170:171],v[170:171],v[226:227] op_sel:[0,0] op_sel_hi:[1,0]
v_pk_mul_f32 v[172:173],v[172:173],v[226:227] op_sel:[0,0] op_sel_hi:[1,0]
)"
R"(v_pk_mul_f32 v[174:175],v[174:175],v[226:227] op_sel:[0,0] op_sel_hi:[1,0]
cv 168,168,169
cv 169,170,171
cv 170,172,173
cv 171,174,175
buffer_store_dwordx4 v[168:171],v244,s[8:11],s13 offen offset:2048 sc0 sc1
v_pk_mul_f32 v[176:177],v[176:177],v[226:227] op_sel:[0,0] op_sel_hi:[1,0]
v_pk_mul_f32 v[178:179],v[178:179],v[226:227] op_sel:[0,0] op_sel_hi:[1,0]
v_pk_mul_f32 v[180:181],v[180:181],v[226:227] op_sel:[0,0] op_sel_hi:[1,0]
v_pk_mul_f32 v[182:183],v[182:183],v[226:227] op_sel:[0,0] op_sel_hi:[1,0]
cv 176,176,177
cv 177,178,179
cv 178,180,181
v_mfma_f32_32x32x16_bf16 v[160:175],v[236:239],v[236:239],0
cv 179,182,183
buffer_store_dwordx4 v[176:179],v244,s[8:11],s86 offen offset:0 sc0 sc1
v_pk_mul_f32 v[184:185],v[184:185],v[226:227] op_sel:[0,0] op_sel_hi:[1,0]
v_pk_mul_f32 v[186:187],v[186:187],v[226:227] op_sel:[0,0] op_sel_hi:[1,0]
v_pk_mul_f32 v[188:189],v[188:189],v[226:227] op_sel:[0,0] op_sel_hi:[1,0]
v_pk_mul_f32 v[190:191],v[190:191],v[226:227] op_sel:[0,0] op_sel_hi:[1,0]
cv 184,184,185
cv 185,186,187
cv 186,188,189
cv 187,190,191
buffer_store_dwordx4 v[184:187],v244,s[8:11],s86 offen offset:2048 sc0 sc1
v_pk_mul_f32 v[192:193],v[192:193],v[226:227] op_sel:[0,1] op_sel_hi:[1,1]
v_pk_mul_f32 v[194:195],v[194:195],v[226:227] op_sel:[0,1] op_sel_hi:[1,1]
v_pk_mul_f32 v[196:197],v[196:197],v[226:227] op_sel:[0,1] op_sel_hi:[1,1]
v_pk_mul_f32 v[198:199],v[198:199],v[226:227] op_sel:[0,1] op_sel_hi:[1,1]
cv 192,192,193
cv 193,194,195
cv 194,196,197
v_mfma_f32_32x32x16_bf16 v[176:191],v[236:239],v[236:239],0
cv 195,198,199
buffer_store_dwordx4 v[192:195],v244,s[8:11],s13 offen offset:1024 sc0 sc1
v_pk_mul_f32 v[200:201],v[200:201],v[226:227] op_sel:[0,1] op_sel_hi:[1,1]
v_pk_mul_f32 v[202:203],v[202:203],v[226:227] op_sel:[0,1] op_sel_hi:[1,1]
v_pk_mul_f32 v[204:205],v[204:205],v[226:227] op_sel:[0,1] op_sel_hi:[1,1]
v_pk_mul_f32 v[206:207],v[206:207],v[226:227] op_sel:[0,1] op_sel_hi:[1,1]
cv 200,200,201
cv 201,202,203
cv 202,204,205
cv 203,206,207
buffer_store_dwordx4 v[200:203],v244,s[8:11],s13 offen offset:3072 sc0 sc1
v_pk_mul_f32 v[208:209],v[208:209],v[226:227] op_sel:[0,1] op_sel_hi:[1,1]
v_pk_mul_f32 v[210:211],v[210:211],v[226:227] op_sel:[0,1] op_sel_hi:[1,1]
v_pk_mul_f32 v[212:213],v[212:213],v[226:227] op_sel:[0,1] op_sel_hi:[1,1]
v_pk_mul_f32 v[214:215],v[214:215],v[226:227] op_sel:[0,1] op_sel_hi:[1,1]
cv 208,208,209
cv 209,210,211
cv 210,212,213
v_mfma_f32_32x32x16_bf16 v[192:207],v[236:239],v[236:239],0
cv 211,214,215
buffer_store_dwordx4 v[208:211],v244,s[8:11],s86 offen offset:1024 sc0 sc1
v_pk_mul_f32 v[216:217],v[216:217],v[226:227] op_sel:[0,1] op_sel_hi:[1,1]
v_pk_mul_f32 v[218:219],v[218:219],v[226:227] op_sel:[0,1] op_sel_hi:[1,1]
v_pk_mul_f32 v[220:221],v[220:221],v[226:227] op_sel:[0,1] op_sel_hi:[1,1]
v_pk_mul_f32 v[222:223],v[222:223],v[226:227] op_sel:[0,1] op_sel_hi:[1,1]
cv 216,216,217
cv 217,218,219
cv 218,220,221
cv 219,222,223
buffer_store_dwordx4 v[216:219],v244,s[8:11],s86 offen offset:3072 sc0 sc1
s_nop 1
v_mfma_f32_32x32x16_bf16 v[208:223],v[236:239],v[236:239],0
L_eva_r1:
s_cmp_lg_u32 s81,14
s_cbranch_scc1 L_loop1
s_add_u32 s81,s81,1
s_bitcmp1_b32 170,s20
s_cbranch_scc1 L_odd
s_branch L_last
.fill 276,4,0xbf800000
L_cw2:
s_mov_b64 exec,-1
s_waitcnt lgkmcnt(0)
s_barrier
cvaa 0,192,128,232
mvaa 0,200,132
s_add_u32 s81,s81,1
ex 78
mvaa 0,208,136
s_add_u32 s44,s44,1024
ex 79
mvaa 0,216,140
cv 72,72,73
cv 73,74,75
cvaa 4,196,128,232
ex 104
mvaa 4,204,132
ex 105
mvaa 4,212,136
s_mov_b32 m0,s68
ex 106
mvaa 4,220,140
ds 249,8,76,0
cvaa 32,224,128,232
ra 64,225,32768
cv 74,76,77
mvaa 32,232,132
ex 107
mvaa 32,240,136
ra 68,225,33792
cv 75,78,79
mvaa 32,248,140
ex 108
cvaa 36,228,128,232
ex 109
mvaa 36,236,132
ra 72,225,34816
cv 76,104,105
mvaa 36,244,136
ds 249,8,76,1024
mvaa 36,252,140
ex 110
cvaa 8,192,144,232
ra 76,225,35840
cv 77,106,107
mvaa 8,200,148
ex 111
mvaa 8,208,152
ex 80
mvaa 8,216,156
ra 80,225,36864
cv 78,108,109
cvaa 12,196,144,232
ex 81
mvaa 12,204,148
ds 249,8,76,2048
mvaa 12,212,152
ra 84,225,37888
cv 79,110,111
mvaa 12,220,156
ex 82
cvaa 40,224,144,232
ex 83
mvaa 40,232,148
ra 88,225,38912
cv 80,80,81
mvaa 40,240,152
ex 84
mvaa 40,248,156
ra 92,225,39936
cv 81,82,83
cvaa 44,228,144,232
ds 249,8,76,3072
mvaa 44,236,148
ex 85
mvaa 44,244,152
ex 86
mvaa 44,252,156
ra 96,225,40960
cv 82,84,85
cvaa 16,192,160,232
ex 87
mvaa 16,200,164
ex 112
mvaa 16,208,168
ra 100,225,41984
cv 83,86,87
mvaa 16,216,172
s_mov_b32 m0,s69
ex 113
cvaa 20,196,160,232
ds 249,8,77,0
mvaa 20,204,164
ra 104,225,43008
cv 84,112,113
mvaa 20,212,168
ex 114
mvaa 20,220,172
ex 115
cvaa 48,224,160,232
ra 108,225,44032
cv 85,114,115
mvaa 48,232,164
ex 116
mvaa 48,240,168
ra 112,225,45056
mvaa 48,248,172
ds 249,8,77,1024
cvaa 52,228,160,232
ex 117
mvaa 52,236,164
ex 118
mvaa 52,244,168
ra 116,225,46080
cv 86,116,117
mvaa 52,252,172
ex 119
cvaa 24,192,176,232
ra 120,225,47104
cv 87,118,119
mvaa 24,200,180
ex 88
mvaa 24,208,184
ds 249,8,77,2048
mvaa 24,216,188
ex 89
cvaa 28,196,176,232
ra 124,225,48128
cv 88,88,89
mvaa 28,204,180
ex 90
mvaa 28,212,184
ex 91
mvaa 28,220,188
ex 92
cvaa 56,224,176,232
ex 93
mvaa 56,232,180
ds 249,8,77,3072
mvaa 56,240,184
ex 94
mvaa 56,248,188
ex 95
cvaa 60,228,176,232
ex 120
mvaa 60,236,180
mvaa 60,244,184
ex 121
mvaa 60,252,188
s_waitcnt vmcnt(8) lgkmcnt(0)
s_barrier
maav 0,64,64
cv 89,90,91
cv 90,92,93
maav 0,96,68
ex 122
maav 4,68,64
ex 123
maav 4,100,68
s_mov_b32 m0,s74
ex 124
maav 8,72,64
ds 255,8,78,0
maav 8,104,68
ex 125
maav 12,76,64
cv 91,94,95
cv 92,120,121
maav 12,108,68
ex 126
v_smfmac_f32_16x16x64_bf16 v[144:147],v[228:231],v[64:71],v248
cv 93,122,123
maav 16,80,64
ex 127
maav 16,112,68
cv 94,124,125
cv 95,126,127
maav 20,84,64
maav 20,116,68
buffer_load_dword v247,v245,s[36:39],s85 offen
maav 24,88,64
maav 24,120,68
ra 192,224,32768
maav 28,92,64
maav 28,124,68
ra 196,224,33792
maav 32,64,72
ds 255,8,78,1024
maav 32,96,76
s_add_u32 s85,s85,131072
maav 36,68,72
ra 200,224,34816
maav 36,100,76
maav 40,72,72
maav 40,104,76
ra 204,224,35840
maav 44,76,72
ds 255,8,78,2048
maav 44,108,76
v_smfmac_f32_16x16x64_bf16 v[148:151],v[228:231],v[72:79],v248
ra 208,224,36864
maav 48,80,72
maav 48,112,76
maav 52,84,72
ra 212,224,37888
maav 52,116,76
ex 0
maav 56,88,72
ds 255,8,78,3072
maav 56,120,76
ra 216,224,38912
maav 60,92,72
ex 1
maav 60,124,76
ex 2
zvav 160,64,80
ra 220,224,39936
cv 0,0,1
mvav 160,96,84
s_mov_b32 m0,s75
ex 3
zvav 164,68,80
ra 224,224,40960
cv 1,2,3
mvav 164,100,84
ds 255,8,79,0
zvav 168,72,80
ex 4
mvav 168,104,84
ra 228,224,41984
zvav 172,76,80
s_add_u32 s46,s46,16384
ex 5
mvav 172,108,84
s_add_u32 s47,s47,32768
ex 6
v_smfmac_f32_16x16x64_bf16 v[152:155],v[228:231],v[80:87],v248
ra 232,224,43008
zvav 176,80,80
ds 255,8,79,1024
mvav 176,112,84
s_add_u32 s95,s47,32768
ex 7
zvav 180,84,80
ra 236,224,44032
cv 2,4,5
mvav 180,116,84
ex 32
zvav 184,88,80
ex 33
mvav 184,120,84
ra 240,224,45056
cv 3,6,7
zvav 188,92,80
ds 255,8,79,2048
mvav 188,124,84
ex 34
mvav 192,64,88
ra 244,224,46080
cv 4,32,33
mvav 192,96,92
ex 35
mvav 196,68,88
ex 36
mvav 196,100,92
ra 248,224,47104
cv 5,34,35
mvav 200,72,88
ex 37
mvav 200,104,92
ds 255,8,79,3072
mvav 204,76,88
s_cmp_ge_i32 s47,s46
ex 38
mvav 204,108,92
s_cselect_b32 s78,s95,s46
ex 39
v_smfmac_f32_16x16x64_bf16 v[156:159],v[228:231],v[88:95],v248
ra 252,224,48128
mvav 208,80,88
s_cselect_b32 s79,s95,268435456
ex 8
mvav 208,112,92
s_add_u32 s79,s79,16384
ex 9
mvav 212,84,88
ex 10
mvav 212,116,92
ex 11
mvav 216,88,88
cv 6,36,37
cv 7,38,39
mvav 216,120,92
ex 12
mvav 220,92,88
ex 13
mvav 220,124,92
s_waitcnt vmcnt(16) lgkmcnt(0)
s_barrier
cvaa 64,192,128,232
s_bitcmp0_b32 s82,s81
s_cbranch_scc1 L_loop2
s_bitcmp1_b32 s15,s81
s_cbranch_scc1 L_short2
.p2align 6
L_loop2:
mvaa 64,200,132
s_add_u32 s81,s81,1
ex 14
mvaa 64,208,136
s_add_u32 s44,s44,1024
ex 15
mvaa 64,216,140
cv 8,8,9
cv 9,10,11
cvaa 68,196,128,232
ex 40
mvaa 68,204,132
ex 41
mvaa 68,212,136
s_mov_b32 m0,s70
ex 42
mvaa 68,220,140
ds 249,8,78,0
cvaa 96,224,128,232
ra 64,225,0
cv 10,12,13
mvaa 96,232,132
ex 43
mvaa 96,240,136
ra 68,225,1024
cv 11,14,15
mvaa 96,248,140
ex 44
cvaa 100,228,128,232
ex 45
mvaa 100,236,132
ra 72,225,2048
cv 12,40,41
mvaa 100,244,136
ds 249,8,78,1024
mvaa 100,252,140
ex 46
cvaa 72,192,144,232
ra 76,225,3072
cv 13,42,43
mvaa 72,200,148
ex 47
mvaa 72,208,152
ex 16
mvaa 72,216,156
ra 80,225,4096
cv 14,44,45
cvaa 76,196,144,232
ex 17
mvaa 76,204,148
ds 249,8,78,2048
mvaa 76,212,152
ra 84,225,5120
cv 15,46,47
mvaa 76,220,156
ex 18
cvaa 104,224,144,232
ex 19
mvaa 104,232,148
ra 88,225,6144
cv 16,16,17
mvaa 104,240,152
ex 20
mvaa 104,248,156
ra 92,225,7168
cv 17,18,19
cvaa 108,228,144,232
ds 249,8,78,3072
mvaa 108,236,148
ex 21
mvaa 108,244,152
ex 22
mvaa 108,252,156
ra 96,225,8192
cv 18,20,21
cvaa 80,192,160,232
ex 23
mvaa 80,200,164
ex 48
mvaa 80,208,168
ra 100,225,9216
cv 19,22,23
mvaa 80,216,172
s_mov_b32 m0,s71
ex 49
cvaa 84,196,160,232
ds 249,8,79,0
mvaa 84,204,164
ra 104,225,10240
cv 20,48,49
mvaa 84,212,168
ex 50
mvaa 84,220,172
ex 51
cvaa 112,224,160,232
ra 108,225,11264
cv 21,50,51
mvaa 112,232,164
ex 52
mvaa 112,240,168
ra 112,225,12288
mvaa 112,248,172
ds 249,8,79,1024
cvaa 116,228,160,232
ex 53
mvaa 116,236,164
ex 54
mvaa 116,244,168
ra 116,225,13312
cv 22,52,53
mvaa 116,252,172
ex 55
cvaa 88,192,176,232
ra 120,225,14336
cv 23,54,55
mvaa 88,200,180
ex 24
mvaa 88,208,184
ds 249,8,79,2048
mvaa 88,216,188
ex 25
cvaa 92,196,176,232
ra 124,225,15360
cv 24,24,25
mvaa 92,204,180
ex 26
mvaa 92,212,184
ex 27
mvaa 92,220,188
ex 28
cvaa 120,224,176,232
ex 29
mvaa 120,232,180
ds 249,8,79,3072
mvaa 120,240,184
ex 30
mvaa 120,248,188
ex 31
cvaa 124,228,176,232
ex 56
mvaa 124,236,180
buffer_store_dwordx4 v[250:253],v244,s[8:11],s44 offen sc0 sc1
mvaa 124,244,184
ex 57
mvaa 124,252,188
s_waitcnt vmcnt(16) lgkmcnt(0)
s_barrier
maav 0,64,0
cv 25,26,27
cv 26,28,29
maav 0,96,4
ex 58
maav 4,68,0
ex 59
maav 4,100,4
s_mov_b32 m0,s72
ex 60
maav 8,72,0
ds 255,8,76,0
maav 8,104,4
ex 61
maav 12,76,0
s_cmp_lg_u32 s80,s81
cv 27,30,31
cv 28,56,57
maav 12,108,4
ex 62
v_smfmac_f32_16x16x64_bf16 v[144:147],v[228:231],v[0:7],v248
cv 29,58,59
maav 16,80,0
ex 63
maav 16,112,4
cv 30,60,61
cv 31,62,63
maav 20,84,0
maav 20,116,4
buffer_load_dword v247,v246,s[24:27],s49 offen
maav 24,88,0
s_cbranch_scc1 L_evb_r2
v_add_u32_e32 v224,s94,v224
s_lshl_b32 s86,s12,12
s_add_u32 s86,s86,131072
v_add_u32_e32 v236,s86,v244
ra 160,236,0
ra 164,236,1024
ra 168,236,2048
ra 172,236,3072
ra 176,236,16384
ra 180,236,17408
ra 184,236,18432
ra 188,236,19456
L_evb_r2:
maav 24,120,4
ra 192,224,0
maav 28,92,0
maav 28,124,4
ra 196,224,1024
maav 32,64,8
ds 255,8,76,1024
maav 32,96,12
s_add_u32 s49,s49,32768
maav 36,68,8
ra 200,224,2048
maav 36,100,12
maav 40,72,8
maav 40,104,12
ra 204,224,3072
maav 44,76,8
ds 255,8,76,2048
maav 44,108,12
v_smfmac_f32_16x16x64_bf16 v[148:151],v[228:231],v[8:15],v248
ra 208,224,4096
maav 48,80,8
maav 48,112,12
maav 52,84,8
ra 212,224,5120
maav 52,116,12
ex 64
maav 56,88,8
ds 255,8,76,3072
maav 56,120,12
ra 216,224,6144
maav 60,92,8
ex 65
maav 60,124,12
ex 66
mvav 160,64,16
ra 220,224,7168
cv 64,64,65
mvav 160,96,20
s_mov_b32 m0,s73
ex 67
mvav 164,68,16
ra 224,224,8192
cv 65,66,67
mvav 164,100,20
ds 255,8,77,0
mvav 168,72,16
ex 68
mvav 168,104,20
ra 228,224,9216
mvav 172,76,16
s_add_u32 s46,s46,16384
ex 69
mvav 172,108,20
s_add_u32 s47,s47,32768
ex 70
v_smfmac_f32_16x16x64_bf16 v[152:155],v[228:231],v[16:23],v248
ra 232,224,10240
mvav 176,80,16
ds 255,8,77,1024
mvav 176,112,20
s_add_u32 s95,s47,32768
ex 71
mvav 180,84,16
ra 236,224,11264
cv 66,68,69
mvav 180,116,20
ex 96
mvav 184,88,16
ex 97
mvav 184,120,20
ra 240,224,12288
cv 67,70,71
mvav 188,92,16
ds 255,8,77,2048
mvav 188,124,20
ex 98
mvav 192,64,24
ra 244,224,13312
cv 68,96,97
mvav 192,96,28
ex 99
mvav 196,68,24
ex 100
mvav 196,100,28
ra 248,224,14336
cv 69,98,99
mvav 200,72,24
ex 101
mvav 200,104,28
ds 255,8,77,3072
mvav 204,76,24
s_cmp_ge_i32 s47,s46
ex 102
mvav 204,108,28
s_cselect_b32 s76,s95,s46
ex 103
v_smfmac_f32_16x16x64_bf16 v[156:159],v[228:231],v[24:31],v248
ra 252,224,15360
mvav 208,80,24
s_cselect_b32 s77,s95,268435456
ex 72
mvav 208,112,28
s_add_u32 s77,s77,16384
ex 73
mvav 212,84,24
ex 74
mvav 212,116,28
ex 75
mvav 216,88,24
cv 70,100,101
cv 71,102,103
mvav 216,120,28
ex 76
mvav 220,92,24
ex 77
mvav 220,124,28
s_waitcnt vmcnt(16) lgkmcnt(0)
s_barrier
cvaa 0,192,128,232
mvaa 0,200,132
s_add_u32 s81,s81,1
ex 78
mvaa 0,208,136
s_add_u32 s44,s44,1024
ex 79
mvaa 0,216,140
cv 72,72,73
cv 73,74,75
cvaa 4,196,128,232
ex 104
mvaa 4,204,132
ex 105
mvaa 4,212,136
s_mov_b32 m0,s68
ex 106
mvaa 4,220,140
ds 249,8,76,0
cvaa 32,224,128,232
ra 64,225,32768
cv 74,76,77
mvaa 32,232,132
ex 107
mvaa 32,240,136
ra 68,225,33792
cv 75,78,79
mvaa 32,248,140
ex 108
cvaa 36,228,128,232
ex 109
mvaa 36,236,132
ra 72,225,34816
cv 76,104,105
mvaa 36,244,136
ds 249,8,76,1024
mvaa 36,252,140
ex 110
cvaa 8,192,144,232
ra 76,225,35840
cv 77,106,107
mvaa 8,200,148
ex 111
mvaa 8,208,152
ex 80
mvaa 8,216,156
ra 80,225,36864
cv 78,108,109
cvaa 12,196,144,232
ex 81
mvaa 12,204,148
ds 249,8,76,2048
mvaa 12,212,152
ra 84,225,37888
cv 79,110,111
mvaa 12,220,156
ex 82
cvaa 40,224,144,232
ex 83
mvaa 40,232,148
ra 88,225,38912
cv 80,80,81
mvaa 40,240,152
ex 84
mvaa 40,248,156
ra 92,225,39936
cv 81,82,83
cvaa 44,228,144,232
ds 249,8,76,3072
mvaa 44,236,148
ex 85
mvaa 44,244,152
ex 86
mvaa 44,252,156
ra 96,225,40960
cv 82,84,85
cvaa 16,192,160,232
ex 87
mvaa 16,200,164
ex 112
mvaa 16,208,168
ra 100,225,41984
cv 83,86,87
mvaa 16,216,172
s_mov_b32 m0,s69
ex 113
cvaa 20,196,160,232
ds 249,8,77,0
mvaa 20,204,164
ra 104,225,43008
cv 84,112,113
mvaa 20,212,168
ex 114
mvaa 20,220,172
ex 115
cvaa 48,224,160,232
ra 108,225,44032
cv 85,114,115
mvaa 48,232,164
ex 116
mvaa 48,240,168
ra 112,225,45056
mvaa 48,248,172
ds 249,8,77,1024
cvaa 52,228,160,232
ex 117
mvaa 52,236,164
ex 118
mvaa 52,244,168
ra 116,225,46080
cv 86,116,117
mvaa 52,252,172
ex 119
cvaa 24,192,176,232
ra 120,225,47104
cv 87,118,119
mvaa 24,200,180
ex 88
mvaa 24,208,184
ds 249,8,77,2048
mvaa 24,216,188
ex 89
cvaa 28,196,176,232
ra 124,225,48128
cv 88,88,89
mvaa 28,204,180
ex 90
mvaa 28,212,184
ex 91
mvaa 28,220,188
ex 92
cvaa 56,224,176,232
ex 93
mvaa 56,232,180
ds 249,8,77,3072
mvaa 56,240,184
ex 94
mvaa 56,248,188
ex 95
cvaa 60,228,176,232
ex 120
mvaa 60,236,180
buffer_store_dwordx4 v[250:253],v244,s[8:11],s44 offen sc0 sc1
mvaa 60,244,184
ex 121
mvaa 60,252,188
s_waitcnt vmcnt(16) lgkmcnt(0)
s_barrier
maav 0,64,64
cv 89,90,91
cv 90,92,93
maav 0,96,68
ex 122
maav 4,68,64
ex 123
maav 4,100,68
s_mov_b32 m0,s74
ex 124
maav 8,72,64
ds 255,8,78,0
maav 8,104,68
ex 125
maav 12,76,64
cv 91,94,95
cv 92,120,121
maav 12,108,68
ex 126
v_smfmac_f32_16x16x64_bf16 v[144:147],v[228:231],v[64:71],v248
cv 93,122,123
maav 16,80,64
ex 127
maav 16,112,68
cv 94,124,125
cv 95,126,127
maav 20,84,64
maav 20,116,68
buffer_load_dword v247,v245,s[36:39],s85 offen
maav 24,88,64
maav 24,120,68
ra 192,224,32768
maav 28,92,64
maav 28,124,68
ra 196,224,33792
maav 32,64,72
ds 255,8,78,1024
maav 32,96,76
s_add_u32 s85,s85,131072
maav 36,68,72
ra 200,224,34816
maav 36,100,76
maav 40,72,72
maav 40,104,76
ra 204,224,35840
maav 44,76,72
ds 255,8,78,2048
maav 44,108,76
v_smfmac_f32_16x16x64_bf16 v[148:151],v[228:231],v[72:79],v248
ra 208,224,36864
maav 48,80,72
maav 48,112,76
maav 52,84,72
ra 212,224,37888
maav 52,116,76
ex 0
maav 56,88,72
ds 255,8,78,3072
maav 56,120,76
ra 216,224,38912
maav 60,92,72
ex 1
maav 60,124,76
ex 2
mvav 160,64,80
ra 220,224,39936
cv 0,0,1
mvav 160,96,84
s_mov_b32 m0,s75
ex 3
mvav 164,68,80
ra 224,224,40960
cv 1,2,3
mvav 164,100,84
ds 255,8,79,0
mvav 168,72,80
ex 4
mvav 168,104,84
ra 228,224,41984
mvav 172,76,80
s_add_u32 s46,s46,16384
ex 5
mvav 172,108,84
s_add_u32 s47,s47,32768
ex 6
v_smfmac_f32_16x16x64_bf16 v[152:155],v[228:231],v[80:87],v248
ra 232,224,43008
mvav 176,80,80
ds 255,8,79,1024
mvav 176,112,84
s_add_u32 s95,s47,32768
ex 7
mvav 180,84,80
ra 236,224,44032
cv 2,4,5
mvav 180,116,84
ex 32
mvav 184,88,80
ex 33
mvav 184,120,84
ra 240,224,45056
cv 3,6,7
mvav 188,92,80
ds 255,8,79,2048
mvav 188,124,84
ex 34
mvav 192,64,88
ra 244,224,46080
cv 4,32,33
mvav 192,96,92
ex 35
mvav 196,68,88
ex 36
mvav 196,100,92
ra 248,224,47104
cv 5,34,35
mvav 200,72,88
ex 37
mvav 200,104,92
ds 255,8,79,3072
mvav 204,76,88
s_cmp_ge_i32 s47,s46
ex 38
mvav 204,108,92
s_cselect_b32 s78,s95,s46
ex 39
v_smfmac_f32_16x16x64_bf16 v[156:159],v[228:231],v[88:95],v248
ra 252,224,48128
mvav 208,80,88
s_cselect_b32 s79,s95,268435456
ex 8
mvav 208,112,92
s_add_u32 s79,s79,16384
ex 9
mvav 212,84,88
ex 10
mvav 212,116,92
ex 11
mvav 216,88,88
cv 6,36,37
cv 7,38,39
mvav 216,120,92
ex 12
mvav 220,92,88
ex 13
mvav 220,124,92
s_waitcnt vmcnt(16) lgkmcnt(0)
s_barrier
cvaa 64,192,128,232
s_bitcmp0_b32 s82,s81
s_cbranch_scc1 L_loop2
s_bitcmp0_b32 s15,s81
s_cbranch_scc1 L_eva_r2
L_short2:
v_add_f32_e32 v226,v152,v153
v_add_f32_e32 v227,v156,v157
v_subrev_u32_e32 v236,0x23800000,v226
v_subrev_u32_e32 v237,0x23800000,v227
v_max_u32_e32 v236,v236,v237
v_cmp_lt_u32_e64 s[40:41],s48,v236
v_rcp_f32_e32 v226,v226
v_rcp_f32_e32 v227,v227
s_add_u32 s86,s13,4096
)"
R"(v_mov_b32_e32 v236,0
v_mov_b32_e32 v237,0
v_mov_b32_e32 v238,0
v_mov_b32_e32 v239,0
v_mov_b64_e32 v[152:153],0
v_mov_b64_e32 v[156:157],0
v_mul_f32_e32 v226,v242,v226
v_mul_f32_e32 v227,v243,v227
v_add_u32_e32 v225,s94,v225
v_pk_mul_f32 v[160:161],v[160:161],v[226:227] op_sel:[0,0] op_sel_hi:[1,0]
v_pk_mul_f32 v[162:163],v[162:163],v[226:227] op_sel:[0,0] op_sel_hi:[1,0]
v_pk_mul_f32 v[164:165],v[164:165],v[226:227] op_sel:[0,0] op_sel_hi:[1,0]
v_pk_mul_f32 v[166:167],v[166:167],v[226:227] op_sel:[0,0] op_sel_hi:[1,0]
cv 160,160,161
cv 161,162,163
cv 162,164,165
cv 163,166,167
buffer_store_dwordx4 v[160:163],v244,s[8:11],s13 offen offset:0 sc0 sc1
v_pk_mul_f32 v[168:169],v[168:169],v[226:227] op_sel:[0,0] op_sel_hi:[1,0]
v_pk_mul_f32 v[170:171],v[170:171],v[226:227] op_sel:[0,0] op_sel_hi:[1,0]
v_pk_mul_f32 v[172:173],v[172:173],v[226:227] op_sel:[0,0] op_sel_hi:[1,0]
v_pk_mul_f32 v[174:175],v[174:175],v[226:227] op_sel:[0,0] op_sel_hi:[1,0]
cv 168,168,169
cv 169,170,171
cv 170,172,173
cv 171,174,175
buffer_store_dwordx4 v[168:171],v244,s[8:11],s13 offen offset:2048 sc0 sc1
v_pk_mul_f32 v[176:177],v[176:177],v[226:227] op_sel:[0,0] op_sel_hi:[1,0]
v_pk_mul_f32 v[178:179],v[178:179],v[226:227] op_sel:[0,0] op_sel_hi:[1,0]
v_pk_mul_f32 v[180:181],v[180:181],v[226:227] op_sel:[0,0] op_sel_hi:[1,0]
v_pk_mul_f32 v[182:183],v[182:183],v[226:227] op_sel:[0,0] op_sel_hi:[1,0]
cv 176,176,177
cv 177,178,179
cv 178,180,181
v_mfma_f32_32x32x16_bf16 v[160:175],v[236:239],v[236:239],0
cv 179,182,183
buffer_store_dwordx4 v[176:179],v244,s[8:11],s86 offen offset:0 sc0 sc1
v_pk_mul_f32 v[184:185],v[184:185],v[226:227] op_sel:[0,0] op_sel_hi:[1,0]
v_pk_mul_f32 v[186:187],v[186:187],v[226:227] op_sel:[0,0] op_sel_hi:[1,0]
v_pk_mul_f32 v[188:189],v[188:189],v[226:227] op_sel:[0,0] op_sel_hi:[1,0]
v_pk_mul_f32 v[190:191],v[190:191],v[226:227] op_sel:[0,0] op_sel_hi:[1,0]
cv 184,184,185
cv 185,186,187
cv 186,188,189
cv 187,190,191
buffer_store_dwordx4 v[184:187],v244,s[8:11],s86 offen offset:2048 sc0 sc1
v_pk_mul_f32 v[192:193],v[192:193],v[226:227] op_sel:[0,1] op_sel_hi:[1,1]
v_pk_mul_f32 v[194:195],v[194:195],v[226:227] op_sel:[0,1] op_sel_hi:[1,1]
v_pk_mul_f32 v[196:197],v[196:197],v[226:227] op_sel:[0,1] op_sel_hi:[1,1]
v_pk_mul_f32 v[198:199],v[198:199],v[226:227] op_sel:[0,1] op_sel_hi:[1,1]
cv 192,192,193
cv 193,194,195
cv 194,196,197
v_mfma_f32_32x32x16_bf16 v[176:191],v[236:239],v[236:239],0
cv 195,198,199
buffer_store_dwordx4 v[192:195],v244,s[8:11],s13 offen offset:1024 sc0 sc1
v_pk_mul_f32 v[200:201],v[200:201],v[226:227] op_sel:[0,1] op_sel_hi:[1,1]
v_pk_mul_f32 v[202:203],v[202:203],v[226:227] op_sel:[0,1] op_sel_hi:[1,1]
v_pk_mul_f32 v[204:205],v[204:205],v[226:227] op_sel:[0,1] op_sel_hi:[1,1]
v_pk_mul_f32 v[206:207],v[206:207],v[226:227] op_sel:[0,1] op_sel_hi:[1,1]
cv 200,200,201
cv 201,202,203
cv 202,204,205
cv 203,206,207
buffer_store_dwordx4 v[200:203],v244,s[8:11],s13 offen offset:3072 sc0 sc1
v_pk_mul_f32 v[208:209],v[208:209],v[226:227] op_sel:[0,1] op_sel_hi:[1,1]
v_pk_mul_f32 v[210:211],v[210:211],v[226:227] op_sel:[0,1] op_sel_hi:[1,1]
v_pk_mul_f32 v[212:213],v[212:213],v[226:227] op_sel:[0,1] op_sel_hi:[1,1]
v_pk_mul_f32 v[214:215],v[214:215],v[226:227] op_sel:[0,1] op_sel_hi:[1,1]
cv 208,208,209
cv 209,210,211
cv 210,212,213
v_mfma_f32_32x32x16_bf16 v[192:207],v[236:239],v[236:239],0
cv 211,214,215
buffer_store_dwordx4 v[208:211],v244,s[8:11],s86 offen offset:1024 sc0 sc1
v_pk_mul_f32 v[216:217],v[216:217],v[226:227] op_sel:[0,1] op_sel_hi:[1,1]
v_pk_mul_f32 v[218:219],v[218:219],v[226:227] op_sel:[0,1] op_sel_hi:[1,1]
v_pk_mul_f32 v[220:221],v[220:221],v[226:227] op_sel:[0,1] op_sel_hi:[1,1]
v_pk_mul_f32 v[222:223],v[222:223],v[226:227] op_sel:[0,1] op_sel_hi:[1,1]
cv 216,216,217
cv 217,218,219
cv 218,220,221
cv 219,222,223
buffer_store_dwordx4 v[216:219],v244,s[8:11],s86 offen offset:3072 sc0 sc1
s_nop 1
v_mfma_f32_32x32x16_bf16 v[208:223],v[236:239],v[236:239],0
L_eva_r2:
s_cmp_lg_u32 s81,14
s_cbranch_scc1 L_loop2
s_add_u32 s81,s81,1
s_bitcmp1_b32 170,s20
s_cbranch_scc1 L_odd
s_branch L_last
.fill 276,4,0xbf800000
L_cw3:
s_mov_b64 exec,-1
s_waitcnt lgkmcnt(0)
s_barrier
cvaa 0,192,128,232
mvaa 0,200,132
s_add_u32 s81,s81,1
ex 78
mvaa 0,208,136
s_add_u32 s44,s44,1024
ex 79
mvaa 0,216,140
cv 72,72,73
cv 73,74,75
cvaa 4,196,128,232
ex 104
mvaa 4,204,132
ex 105
mvaa 4,212,136
s_mov_b32 m0,s68
ex 106
mvaa 4,220,140
ra 64,225,32768
cv 74,76,77
cvaa 32,224,128,232
ds 249,8,76,0
mvaa 32,232,132
ex 107
mvaa 32,240,136
ra 68,225,33792
cv 75,78,79
mvaa 32,248,140
ex 108
cvaa 36,228,128,232
ex 109
mvaa 36,236,132
ra 72,225,34816
cv 76,104,105
mvaa 36,244,136
ex 110
mvaa 36,252,140
ds 249,8,76,1024
cvaa 8,192,144,232
ra 76,225,35840
cv 77,106,107
mvaa 8,200,148
ex 111
mvaa 8,208,152
ex 80
mvaa 8,216,156
ra 80,225,36864
cv 78,108,109
cvaa 12,196,144,232
ex 81
mvaa 12,204,148
ra 84,225,37888
cv 79,110,111
mvaa 12,212,152
ds 249,8,76,2048
mvaa 12,220,156
ex 82
cvaa 40,224,144,232
ex 83
mvaa 40,232,148
ra 88,225,38912
cv 80,80,81
mvaa 40,240,152
ex 84
mvaa 40,248,156
ra 92,225,39936
cv 81,82,83
cvaa 44,228,144,232
ex 85
mvaa 44,236,148
ds 249,8,76,3072
mvaa 44,244,152
ex 86
mvaa 44,252,156
ra 96,225,40960
cv 82,84,85
cvaa 16,192,160,232
ex 87
mvaa 16,200,164
ex 112
mvaa 16,208,168
ra 100,225,41984
cv 83,86,87
mvaa 16,216,172
s_mov_b32 m0,s69
ex 113
cvaa 20,196,160,232
ra 104,225,43008
cv 84,112,113
mvaa 20,204,164
ds 249,8,77,0
mvaa 20,212,168
ex 114
mvaa 20,220,172
ex 115
cvaa 48,224,160,232
ra 108,225,44032
cv 85,114,115
mvaa 48,232,164
ex 116
mvaa 48,240,168
ra 112,225,45056
mvaa 48,248,172
ex 117
cvaa 52,228,160,232
ds 249,8,77,1024
mvaa 52,236,164
ex 118
mvaa 52,244,168
ra 116,225,46080
cv 86,116,117
mvaa 52,252,172
ex 119
cvaa 24,192,176,232
ra 120,225,47104
cv 87,118,119
mvaa 24,200,180
ex 88
mvaa 24,208,184
ex 89
mvaa 24,216,188
ds 249,8,77,2048
cvaa 28,196,176,232
ra 124,225,48128
cv 88,88,89
mvaa 28,204,180
ex 90
mvaa 28,212,184
ex 91
mvaa 28,220,188
ex 92
cvaa 56,224,176,232
ex 93
mvaa 56,232,180
ex 94
mvaa 56,240,184
ds 249,8,77,3072
mvaa 56,248,188
ex 95
cvaa 60,228,176,232
ex 120
mvaa 60,236,180
ex 121
mvaa 60,244,184
mvaa 60,252,188
s_waitcnt vmcnt(8) lgkmcnt(0)
s_barrier
maav 0,64,64
cv 89,90,91
cv 90,92,93
maav 0,96,68
ex 122
maav 4,68,64
ex 123
maav 4,100,68
ex 124
maav 8,72,64
s_mov_b32 m0,s74
ex 125
maav 8,104,68
ds 255,8,78,0
maav 12,76,64
cv 91,94,95
cv 92,120,121
maav 12,108,68
ex 126
v_smfmac_f32_16x16x64_bf16 v[144:147],v[228:231],v[64:71],v248
cv 93,122,123
maav 16,80,64
ex 127
maav 16,112,68
cv 94,124,125
cv 95,126,127
maav 20,84,64
maav 20,116,68
maav 24,88,64
buffer_load_dword v247,v245,s[36:39],s85 offen
maav 24,120,68
ra 192,224,32768
maav 28,92,64
maav 28,124,68
ra 196,224,33792
maav 32,64,72
maav 32,96,76
ds 255,8,78,1024
maav 36,68,72
ra 200,224,34816
maav 36,100,76
s_add_u32 s85,s85,131072
maav 40,72,72
maav 40,104,76
ra 204,224,35840
maav 44,76,72
maav 44,108,76
ds 255,8,78,2048
v_smfmac_f32_16x16x64_bf16 v[148:151],v[228:231],v[72:79],v248
ra 208,224,36864
maav 48,80,72
maav 48,112,76
maav 52,84,72
ra 212,224,37888
maav 52,116,76
ex 0
maav 56,88,72
ra 216,224,38912
maav 56,120,76
ds 255,8,78,3072
maav 60,92,72
ex 1
maav 60,124,76
ex 2
zvav 160,64,80
ra 220,224,39936
cv 0,0,1
mvav 160,96,84
ex 3
zvav 164,68,80
ra 224,224,40960
cv 1,2,3
mvav 164,100,84
s_mov_b32 m0,s75
ex 4
zvav 168,72,80
ds 255,8,79,0
mvav 168,104,84
ra 228,224,41984
zvav 172,76,80
s_add_u32 s46,s46,16384
ex 5
mvav 172,108,84
s_add_u32 s47,s47,32768
ex 6
v_smfmac_f32_16x16x64_bf16 v[152:155],v[228:231],v[80:87],v248
ra 232,224,43008
zvav 176,80,80
ds 255,8,79,1024
mvav 176,112,84
s_add_u32 s95,s47,32768
ex 7
zvav 180,84,80
ra 236,224,44032
cv 2,4,5
mvav 180,116,84
ex 32
zvav 184,88,80
ex 33
mvav 184,120,84
ra 240,224,45056
cv 3,6,7
zvav 188,92,80
ex 34
mvav 188,124,84
ds 255,8,79,2048
mvav 192,64,88
ra 244,224,46080
cv 4,32,33
mvav 192,96,92
ex 35
mvav 196,68,88
ex 36
mvav 196,100,92
ra 248,224,47104
cv 5,34,35
mvav 200,72,88
ex 37
mvav 200,104,92
ex 38
mvav 204,76,88
ds 255,8,79,3072
mvav 204,108,92
s_cmp_ge_i32 s47,s46
ex 39
v_smfmac_f32_16x16x64_bf16 v[156:159],v[228:231],v[88:95],v248
ra 252,224,48128
mvav 208,80,88
s_cselect_b32 s78,s95,s46
ex 8
mvav 208,112,92
s_cselect_b32 s79,s95,268435456
ex 9
mvav 212,84,88
s_add_u32 s79,s79,16384
ex 10
mvav 212,116,92
ex 11
mvav 216,88,88
cv 6,36,37
cv 7,38,39
mvav 216,120,92
ex 12
mvav 220,92,88
ex 13
mvav 220,124,92
s_waitcnt vmcnt(16) lgkmcnt(0)
s_barrier
cvaa 64,192,128,232
s_bitcmp0_b32 s82,s81
s_cbranch_scc1 L_loop3
s_bitcmp1_b32 s15,s81
s_cbranch_scc1 L_short3
.p2align 6
L_loop3:
mvaa 64,200,132
s_add_u32 s81,s81,1
ex 14
mvaa 64,208,136
s_add_u32 s44,s44,1024
ex 15
mvaa 64,216,140
cv 8,8,9
cv 9,10,11
cvaa 68,196,128,232
ex 40
mvaa 68,204,132
ex 41
mvaa 68,212,136
s_mov_b32 m0,s70
ex 42
mvaa 68,220,140
ra 64,225,0
cv 10,12,13
cvaa 96,224,128,232
ds 249,8,78,0
mvaa 96,232,132
ex 43
mvaa 96,240,136
ra 68,225,1024
cv 11,14,15
mvaa 96,248,140
ex 44
cvaa 100,228,128,232
ex 45
mvaa 100,236,132
ra 72,225,2048
cv 12,40,41
mvaa 100,244,136
ex 46
mvaa 100,252,140
ds 249,8,78,1024
cvaa 72,192,144,232
ra 76,225,3072
cv 13,42,43
mvaa 72,200,148
ex 47
mvaa 72,208,152
ex 16
mvaa 72,216,156
ra 80,225,4096
cv 14,44,45
cvaa 76,196,144,232
ex 17
mvaa 76,204,148
ra 84,225,5120
cv 15,46,47
mvaa 76,212,152
ds 249,8,78,2048
mvaa 76,220,156
ex 18
cvaa 104,224,144,232
ex 19
mvaa 104,232,148
ra 88,225,6144
cv 16,16,17
mvaa 104,240,152
ex 20
mvaa 104,248,156
ra 92,225,7168
cv 17,18,19
cvaa 108,228,144,232
ex 21
mvaa 108,236,148
ds 249,8,78,3072
mvaa 108,244,152
ex 22
mvaa 108,252,156
ra 96,225,8192
cv 18,20,21
cvaa 80,192,160,232
ex 23
mvaa 80,200,164
ex 48
mvaa 80,208,168
ra 100,225,9216
cv 19,22,23
mvaa 80,216,172
s_mov_b32 m0,s71
ex 49
cvaa 84,196,160,232
ra 104,225,10240
cv 20,48,49
mvaa 84,204,164
ds 249,8,79,0
mvaa 84,212,168
ex 50
mvaa 84,220,172
ex 51
cvaa 112,224,160,232
ra 108,225,11264
cv 21,50,51
mvaa 112,232,164
ex 52
mvaa 112,240,168
ra 112,225,12288
mvaa 112,248,172
ex 53
cvaa 116,228,160,232
ds 249,8,79,1024
mvaa 116,236,164
ex 54
mvaa 116,244,168
ra 116,225,13312
cv 22,52,53
mvaa 116,252,172
ex 55
cvaa 88,192,176,232
ra 120,225,14336
cv 23,54,55
mvaa 88,200,180
ex 24
mvaa 88,208,184
ex 25
mvaa 88,216,188
ds 249,8,79,2048
cvaa 92,196,176,232
ra 124,225,15360
cv 24,24,25
mvaa 92,204,180
ex 26
mvaa 92,212,184
ex 27
mvaa 92,220,188
ex 28
cvaa 120,224,176,232
ex 29
mvaa 120,232,180
ex 30
mvaa 120,240,184
ds 249,8,79,3072
mvaa 120,248,188
ex 31
cvaa 124,228,176,232
ex 56
mvaa 124,236,180
ex 57
mvaa 124,244,184
buffer_store_dwordx4 v[250:253],v244,s[8:11],s44 offen sc0 sc1
mvaa 124,252,188
s_waitcnt vmcnt(16) lgkmcnt(0)
s_barrier
maav 0,64,0
cv 25,26,27
cv 26,28,29
maav 0,96,4
ex 58
maav 4,68,0
ex 59
maav 4,100,4
ex 60
maav 8,72,0
s_mov_b32 m0,s72
ex 61
maav 8,104,4
ds 255,8,76,0
maav 12,76,0
s_cmp_lg_u32 s80,s81
cv 27,30,31
cv 28,56,57
maav 12,108,4
ex 62
v_smfmac_f32_16x16x64_bf16 v[144:147],v[228:231],v[0:7],v248
cv 29,58,59
maav 16,80,0
ex 63
maav 16,112,4
cv 30,60,61
cv 31,62,63
maav 20,84,0
maav 20,116,4
s_cbranch_scc1 L_evb_r3
v_add_u32_e32 v224,s94,v224
s_lshl_b32 s86,s12,12
s_add_u32 s86,s86,131072
v_add_u32_e32 v236,s86,v244
ra 160,236,0
ra 164,236,1024
ra 168,236,2048
ra 172,236,3072
ra 176,236,16384
ra 180,236,17408
ra 184,236,18432
ra 188,236,19456
L_evb_r3:
maav 24,88,0
buffer_load_dword v247,v246,s[24:27],s49 offen
maav 24,120,4
ra 192,224,0
maav 28,92,0
maav 28,124,4
ra 196,224,1024
maav 32,64,8
maav 32,96,12
ds 255,8,76,1024
maav 36,68,8
ra 200,224,2048
maav 36,100,12
s_add_u32 s49,s49,32768
maav 40,72,8
maav 40,104,12
ra 204,224,3072
maav 44,76,8
maav 44,108,12
ds 255,8,76,2048
v_smfmac_f32_16x16x64_bf16 v[148:151],v[228:231],v[8:15],v248
ra 208,224,4096
maav 48,80,8
maav 48,112,12
maav 52,84,8
ra 212,224,5120
maav 52,116,12
ex 64
maav 56,88,8
ra 216,224,6144
maav 56,120,12
ds 255,8,76,3072
maav 60,92,8
ex 65
maav 60,124,12
ex 66
mvav 160,64,16
ra 220,224,7168
cv 64,64,65
mvav 160,96,20
ex 67
mvav 164,68,16
ra 224,224,8192
cv 65,66,67
mvav 164,100,20
s_mov_b32 m0,s73
ex 68
mvav 168,72,16
ds 255,8,77,0
mvav 168,104,20
ra 228,224,9216
mvav 172,76,16
s_add_u32 s46,s46,16384
ex 69
mvav 172,108,20
s_add_u32 s47,s47,32768
ex 70
v_smfmac_f32_16x16x64_bf16 v[152:155],v[228:231],v[16:23],v248
ra 232,224,10240
mvav 176,80,16
ds 255,8,77,1024
mvav 176,112,20
s_add_u32 s95,s47,32768
ex 71
mvav 180,84,16
ra 236,224,11264
cv 66,68,69
mvav 180,116,20
ex 96
mvav 184,88,16
ex 97
mvav 184,120,20
ra 240,224,12288
cv 67,70,71
mvav 188,92,16
ex 98
mvav 188,124,20
ds 255,8,77,2048
mvav 192,64,24
ra 244,224,13312
cv 68,96,97
mvav 192,96,28
ex 99
mvav 196,68,24
ex 100
mvav 196,100,28
ra 248,224,14336
cv 69,98,99
mvav 200,72,24
ex 101
mvav 200,104,28
ex 102
mvav 204,76,24
ds 255,8,77,3072
mvav 204,108,28
s_cmp_ge_i32 s47,s46
ex 103
v_smfmac_f32_16x16x64_bf16 v[156:159],v[228:231],v[24:31],v248
ra 252,224,15360
mvav 208,80,24
s_cselect_b32 s76,s95,s46
ex 72
mvav 208,112,28
s_cselect_b32 s77,s95,268435456
ex 73
mvav 212,84,24
s_add_u32 s77,s77,16384
ex 74
mvav 212,116,28
ex 75
mvav 216,88,24
cv 70,100,101
cv 71,102,103
mvav 216,120,28
ex 76
mvav 220,92,24
ex 77
mvav 220,124,28
s_waitcnt vmcnt(16) lgkmcnt(0)
s_barrier
cvaa 0,192,128,232
mvaa 0,200,132
s_add_u32 s81,s81,1
ex 78
mvaa 0,208,136
s_add_u32 s44,s44,1024
ex 79
mvaa 0,216,140
cv 72,72,73
cv 73,74,75
cvaa 4,196,128,232
ex 104
mvaa 4,204,132
ex 105
mvaa 4,212,136
s_mov_b32 m0,s68
ex 106
mvaa 4,220,140
ra 64,225,32768
cv 74,76,77
cvaa 32,224,128,232
ds 249,8,76,0
mvaa 32,232,132
ex 107
mvaa 32,240,136
ra 68,225,33792
cv 75,78,79
mvaa 32,248,140
ex 108
cvaa 36,228,128,232
ex 109
mvaa 36,236,132
ra 72,225,34816
cv 76,104,105
mvaa 36,244,136
ex 110
mvaa 36,252,140
ds 249,8,76,1024
cvaa 8,192,144,232
ra 76,225,35840
cv 77,106,107
mvaa 8,200,148
ex 111
mvaa 8,208,152
ex 80
mvaa 8,216,156
ra 80,225,36864
cv 78,108,109
cvaa 12,196,144,232
ex 81
mvaa 12,204,148
ra 84,225,37888
cv 79,110,111
mvaa 12,212,152
ds 249,8,76,2048
mvaa 12,220,156
ex 82
cvaa 40,224,144,232
ex 83
mvaa 40,232,148
ra 88,225,38912
cv 80,80,81
mvaa 40,240,152
ex 84
mvaa 40,248,156
ra 92,225,39936
cv 81,82,83
cvaa 44,228,144,232
ex 85
mvaa 44,236,148
ds 249,8,76,3072
mvaa 44,244,152
ex 86
mvaa 44,252,156
ra 96,225,40960
cv 82,84,85
cvaa 16,192,160,232
ex 87
mvaa 16,200,164
ex 112
mvaa 16,208,168
ra 100,225,41984
cv 83,86,87
mvaa 16,216,172
s_mov_b32 m0,s69
ex 113
cvaa 20,196,160,232
ra 104,225,43008
cv 84,112,113
mvaa 20,204,164
ds 249,8,77,0
mvaa 20,212,168
ex 114
mvaa 20,220,172
ex 115
cvaa 48,224,160,232
ra 108,225,44032
cv 85,114,115
mvaa 48,232,164
ex 116
mvaa 48,240,168
ra 112,225,45056
mvaa 48,248,172
ex 117
cvaa 52,228,160,232
ds 249,8,77,1024
mvaa 52,236,164
ex 118
mvaa 52,244,168
ra 116,225,46080
cv 86,116,117
mvaa 52,252,172
ex 119
cvaa 24,192,176,232
ra 120,225,47104
cv 87,118,119
mvaa 24,200,180
ex 88
mvaa 24,208,184
ex 89
mvaa 24,216,188
ds 249,8,77,2048
cvaa 28,196,176,232
ra 124,225,48128
cv 88,88,89
mvaa 28,204,180
ex 90
mvaa 28,212,184
ex 91
mvaa 28,220,188
ex 92
cvaa 56,224,176,232
ex 93
mvaa 56,232,180
ex 94
mvaa 56,240,184
ds 249,8,77,3072
mvaa 56,248,188
ex 95
cvaa 60,228,176,232
ex 120
mvaa 60,236,180
ex 121
mvaa 60,244,184
buffer_store_dwordx4 v[250:253],v244,s[8:11],s44 offen sc0 sc1
mvaa 60,252,188
s_waitcnt vmcnt(16) lgkmcnt(0)
s_barrier
maav 0,64,64
cv 89,90,91
cv 90,92,93
maav 0,96,68
ex 122
maav 4,68,64
ex 123
maav 4,100,68
ex 124
maav 8,72,64
s_mov_b32 m0,s74
ex 125
maav 8,104,68
ds 255,8,78,0
maav 12,76,64
cv 91,94,95
cv 92,120,121
maav 12,108,68
ex 126
v_smfmac_f32_16x16x64_bf16 v[144:147],v[228:231],v[64:71],v248
cv 93,122,123
maav 16,80,64
ex 127
maav 16,112,68
cv 94,124,125
cv 95,126,127
maav 20,84,64
maav 20,116,68
maav 24,88,64
buffer_load_dword v247,v245,s[36:39],s85 offen
maav 24,120,68
ra 192,224,32768
maav 28,92,64
maav 28,124,68
ra 196,224,33792
maav 32,64,72
maav 32,96,76
ds 255,8,78,1024
maav 36,68,72
ra 200,224,34816
maav 36,100,76
s_add_u32 s85,s85,131072
maav 40,72,72
maav 40,104,76
ra 204,224,35840
maav 44,76,72
maav 44,108,76
ds 255,8,78,2048
v_smfmac_f32_16x16x64_bf16 v[148:151],v[228:231],v[72:79],v248
ra 208,224,36864
maav 48,80,72
maav 48,112,76
maav 52,84,72
ra 212,224,37888
maav 52,116,76
ex 0
maav 56,88,72
ra 216,224,38912
maav 56,120,76
ds 255,8,78,3072
maav 60,92,72
ex 1
maav 60,124,76
ex 2
mvav 160,64,80
ra 220,224,39936
cv 0,0,1
mvav 160,96,84
ex 3
mvav 164,68,80
ra 224,224,40960
cv 1,2,3
mvav 164,100,84
s_mov_b32 m0,s75
ex 4
mvav 168,72,80
ds 255,8,79,0
mvav 168,104,84
ra 228,224,41984
mvav 172,76,80
s_add_u32 s46,s46,16384
ex 5
mvav 172,108,84
s_add_u32 s47,s47,32768
ex 6
v_smfmac_f32_16x16x64_bf16 v[152:155],v[228:231],v[80:87],v248
ra 232,224,43008
mvav 176,80,80
ds 255,8,79,1024
mvav 176,112,84
s_add_u32 s95,s47,32768
ex 7
mvav 180,84,80
ra 236,224,44032
cv 2,4,5
mvav 180,116,84
ex 32
mvav 184,88,80
ex 33
mvav 184,120,84
ra 240,224,45056
cv 3,6,7
mvav 188,92,80
ex 34
mvav 188,124,84
ds 255,8,79,2048
mvav 192,64,88
ra 244,224,46080
cv 4,32,33
mvav 192,96,92
ex 35
mvav 196,68,88
ex 36
mvav 196,100,92
ra 248,224,47104
cv 5,34,35
mvav 200,72,88
ex 37
mvav 200,104,92
ex 38
mvav 204,76,88
ds 255,8,79,3072
mvav 204,108,92
s_cmp_ge_i32 s47,s46
ex 39
v_smfmac_f32_16x16x64_bf16 v[156:159],v[228:231],v[88:95],v248
ra 252,224,48128
mvav 208,80,88
s_cselect_b32 s78,s95,s46
ex 8
mvav 208,112,92
s_cselect_b32 s79,s95,268435456
ex 9
mvav 212,84,88
s_add_u32 s79,s79,16384
ex 10
mvav 212,116,92
ex 11
mvav 216,88,88
cv 6,36,37
cv 7,38,39
mvav 216,120,92
)"
R"(ex 12
mvav 220,92,88
ex 13
mvav 220,124,92
s_waitcnt vmcnt(16) lgkmcnt(0)
s_barrier
cvaa 64,192,128,232
s_bitcmp0_b32 s82,s81
s_cbranch_scc1 L_loop3
s_bitcmp0_b32 s15,s81
s_cbranch_scc1 L_eva_r3
L_short3:
v_add_f32_e32 v226,v152,v153
v_add_f32_e32 v227,v156,v157
v_subrev_u32_e32 v236,0x23800000,v226
v_subrev_u32_e32 v237,0x23800000,v227
v_max_u32_e32 v236,v236,v237
v_cmp_lt_u32_e64 s[40:41],s48,v236
v_rcp_f32_e32 v226,v226
v_rcp_f32_e32 v227,v227
s_add_u32 s86,s13,4096
v_mov_b32_e32 v236,0
v_mov_b32_e32 v237,0
v_mov_b32_e32 v238,0
v_mov_b32_e32 v239,0
v_mov_b64_e32 v[152:153],0
v_mov_b64_e32 v[156:157],0
v_mul_f32_e32 v226,v242,v226
v_mul_f32_e32 v227,v243,v227
v_add_u32_e32 v225,s94,v225
v_pk_mul_f32 v[160:161],v[160:161],v[226:227] op_sel:[0,0] op_sel_hi:[1,0]
v_pk_mul_f32 v[162:163],v[162:163],v[226:227] op_sel:[0,0] op_sel_hi:[1,0]
v_pk_mul_f32 v[164:165],v[164:165],v[226:227] op_sel:[0,0] op_sel_hi:[1,0]
v_pk_mul_f32 v[166:167],v[166:167],v[226:227] op_sel:[0,0] op_sel_hi:[1,0]
cv 160,160,161
cv 161,162,163
cv 162,164,165
cv 163,166,167
buffer_store_dwordx4 v[160:163],v244,s[8:11],s13 offen offset:0 sc0 sc1
v_pk_mul_f32 v[168:169],v[168:169],v[226:227] op_sel:[0,0] op_sel_hi:[1,0]
v_pk_mul_f32 v[170:171],v[170:171],v[226:227] op_sel:[0,0] op_sel_hi:[1,0]
v_pk_mul_f32 v[172:173],v[172:173],v[226:227] op_sel:[0,0] op_sel_hi:[1,0]
v_pk_mul_f32 v[174:175],v[174:175],v[226:227] op_sel:[0,0] op_sel_hi:[1,0]
cv 168,168,169
cv 169,170,171
cv 170,172,173
cv 171,174,175
buffer_store_dwordx4 v[168:171],v244,s[8:11],s13 offen offset:2048 sc0 sc1
v_pk_mul_f32 v[176:177],v[176:177],v[226:227] op_sel:[0,0] op_sel_hi:[1,0]
v_pk_mul_f32 v[178:179],v[178:179],v[226:227] op_sel:[0,0] op_sel_hi:[1,0]
v_pk_mul_f32 v[180:181],v[180:181],v[226:227] op_sel:[0,0] op_sel_hi:[1,0]
v_pk_mul_f32 v[182:183],v[182:183],v[226:227] op_sel:[0,0] op_sel_hi:[1,0]
cv 176,176,177
cv 177,178,179
cv 178,180,181
v_mfma_f32_32x32x16_bf16 v[160:175],v[236:239],v[236:239],0
cv 179,182,183
buffer_store_dwordx4 v[176:179],v244,s[8:11],s86 offen offset:0 sc0 sc1
v_pk_mul_f32 v[184:185],v[184:185],v[226:227] op_sel:[0,0] op_sel_hi:[1,0]
v_pk_mul_f32 v[186:187],v[186:187],v[226:227] op_sel:[0,0] op_sel_hi:[1,0]
v_pk_mul_f32 v[188:189],v[188:189],v[226:227] op_sel:[0,0] op_sel_hi:[1,0]
v_pk_mul_f32 v[190:191],v[190:191],v[226:227] op_sel:[0,0] op_sel_hi:[1,0]
cv 184,184,185
cv 185,186,187
cv 186,188,189
cv 187,190,191
buffer_store_dwordx4 v[184:187],v244,s[8:11],s86 offen offset:2048 sc0 sc1
v_pk_mul_f32 v[192:193],v[192:193],v[226:227] op_sel:[0,1] op_sel_hi:[1,1]
v_pk_mul_f32 v[194:195],v[194:195],v[226:227] op_sel:[0,1] op_sel_hi:[1,1]
v_pk_mul_f32 v[196:197],v[196:197],v[226:227] op_sel:[0,1] op_sel_hi:[1,1]
v_pk_mul_f32 v[198:199],v[198:199],v[226:227] op_sel:[0,1] op_sel_hi:[1,1]
cv 192,192,193
cv 193,194,195
cv 194,196,197
v_mfma_f32_32x32x16_bf16 v[176:191],v[236:239],v[236:239],0
cv 195,198,199
buffer_store_dwordx4 v[192:195],v244,s[8:11],s13 offen offset:1024 sc0 sc1
v_pk_mul_f32 v[200:201],v[200:201],v[226:227] op_sel:[0,1] op_sel_hi:[1,1]
v_pk_mul_f32 v[202:203],v[202:203],v[226:227] op_sel:[0,1] op_sel_hi:[1,1]
v_pk_mul_f32 v[204:205],v[204:205],v[226:227] op_sel:[0,1] op_sel_hi:[1,1]
v_pk_mul_f32 v[206:207],v[206:207],v[226:227] op_sel:[0,1] op_sel_hi:[1,1]
cv 200,200,201
cv 201,202,203
cv 202,204,205
cv 203,206,207
buffer_store_dwordx4 v[200:203],v244,s[8:11],s13 offen offset:3072 sc0 sc1
v_pk_mul_f32 v[208:209],v[208:209],v[226:227] op_sel:[0,1] op_sel_hi:[1,1]
v_pk_mul_f32 v[210:211],v[210:211],v[226:227] op_sel:[0,1] op_sel_hi:[1,1]
v_pk_mul_f32 v[212:213],v[212:213],v[226:227] op_sel:[0,1] op_sel_hi:[1,1]
v_pk_mul_f32 v[214:215],v[214:215],v[226:227] op_sel:[0,1] op_sel_hi:[1,1]
cv 208,208,209
cv 209,210,211
cv 210,212,213
v_mfma_f32_32x32x16_bf16 v[192:207],v[236:239],v[236:239],0
cv 211,214,215
buffer_store_dwordx4 v[208:211],v244,s[8:11],s86 offen offset:1024 sc0 sc1
v_pk_mul_f32 v[216:217],v[216:217],v[226:227] op_sel:[0,1] op_sel_hi:[1,1]
v_pk_mul_f32 v[218:219],v[218:219],v[226:227] op_sel:[0,1] op_sel_hi:[1,1]
v_pk_mul_f32 v[220:221],v[220:221],v[226:227] op_sel:[0,1] op_sel_hi:[1,1]
v_pk_mul_f32 v[222:223],v[222:223],v[226:227] op_sel:[0,1] op_sel_hi:[1,1]
cv 216,216,217
cv 217,218,219
cv 218,220,221
cv 219,222,223
buffer_store_dwordx4 v[216:219],v244,s[8:11],s86 offen offset:3072 sc0 sc1
s_nop 1
v_mfma_f32_32x32x16_bf16 v[208:223],v[236:239],v[236:239],0
L_eva_r3:
s_cmp_lg_u32 s81,14
s_cbranch_scc1 L_loop3
s_add_u32 s81,s81,1
s_bitcmp1_b32 170,s20
s_cbranch_scc1 L_odd
L_last:
L_vs1:
s_cbranch_execz L_vs5
s_lshl_b32 s86,s21,2
s_sub_u32 s87,s13,s67
s_add_u32 s86,s86,s12
mvaa 64,200,132
s_lshl_b32 s86,s86,8
v_add_u32_e32 v252,s87,v244
s_add_u32 s86,s86,L_last-L_att_pc+1024
v_lshl_add_u32 v226,v244,3,s86
s_lshl_b32 s86,s12,1
s_bfe_u32 s87,s21,65539
mvaa 64,208,136
s_add_u32 s86,s86,s87
v_min_u32_e32 v226,L_att_end-L_att_pc-4,v226
s_lshl_b32 s86,s86,7
s_add_u32 s86,s86,L_pend2-L_att_pc
v_writelane_b32 v226,s86,2
mvaa 64,216,140
s_mov_b64 exec,7
global_load_dword v253,v226,s[96:97]
s_mov_b64 exec,-1
cvaa 68,196,128,232
s_add_u32 m0,s52,32768
v_and_b32_e32 v224,16,v244
v_lshlrev_b32_e32 v224,5,v224
mvaa 68,204,132
s_xor_b32 s51,s62,0
v_lshrrev_b32_e32 v239,4,v244
v_lshrrev_b32_e32 v236,3,v239
mvaa 68,212,136
s_lshl_b32 s87,s12,13
v_and_b32_e32 v237,112,v244
v_and_b32_e32 v238,48,v239
mvaa 68,220,140
s_xor_b32 s87,s87,8192
ra 64,225,0
v_lshlrev_b32_e32 v238,1,v238
cvaa 96,224,128,232
buffer_load_dword v253,v224,s[8:11],s53 offen sc1
mvaa 96,232,132
s_mov_b32 s84,6
ra 68,225,1024
v_xor_b32_e32 v237,v237,v238
mvaa 96,240,136
s_add_u32 s81,s81,1
ra 72,225,2048
v_lshl_add_u32 v245,v236,11,v237
mvaa 96,248,140
s_mov_b32 s84,4
v_add_u32_e32 v246,15360,v245
v_add_u32_e32 v249,30720,v245
cvaa 100,228,128,232
s_andn2_b32 s69,64,s54
ra 76,225,3072
s_waitcnt vmcnt(9)
v_add_u32_e32 v247,46080,v245
mvaa 100,236,132
dl 245,24,51,0
mvaa 100,244,136
s_add_u32 s88,s14,4096
ra 80,225,4096
cv 8,8,9
mvaa 100,252,140
s_lshl_b32 s35,s19,1
ra 84,225,5120
cv 9,10,11
cvaa 72,192,144,232
s_lshr_b32 s33,40569,s35
ra 88,225,6144
cv 10,12,13
mvaa 72,200,148
s_and_b32 s33,s33,3
ra 92,225,7168
v_add_u32_e32 v250,4096,v252
mvaa 72,208,152
L_vs2:
s_cbranch_execz L_vs6
dl 246,24,51,1024
mvaa 72,216,156
s_lshl_b32 s33,s33,13
ra 96,225,8192
v_mov_b32_e32 v251,4
cvaa 76,196,144,232
s_lshr_b32 s34,59294,s35
ra 100,225,9216
s_and_b32 s34,s34,3
mvaa 76,204,148
s_lshl_b32 s34,s34,13
ra 104,225,10240
s_xor_b32 s35,s33,s34
mvaa 76,212,152
s_and_b32 s98,s19,3
ra 108,225,11264
s_lshl_b32 s98,s98,13
mvaa 76,220,156
dl 249,24,51,2048
cvaa 104,224,144,232
s_mov_b32 s74,0x300003
ra 112,225,12288
s_mov_b32 s75,0x30000300
mvaa 104,232,148
s_lshl_b32 s71,s12,15
ra 116,225,13312
s_add_u32 s71,s71,s54
mvaa 104,240,152
s_mov_b64 s[44:45],s[30:31]
ra 120,225,14336
s_mov_b32 s46,4194304
mvaa 104,248,156
s_mov_b32 s47,131072
ex 14
cvaa 108,228,144,232
dl 247,24,51,3072
mvaa 108,236,148
s_add_u32 m0,s52,49152
s_xor_b32 s51,s63,0
ra 124,225,15360
mvaa 108,244,152
s_lshl_b32 s70,s23,11
ex 15
mvaa 108,252,156
s_add_u32 s70,s70,s71
ex 40
cvaa 80,192,160,232
ex 41
mvaa 80,200,164
dl 245,24,51,0
mvaa 80,208,168
ex 42
mvaa 80,216,172
ex 43
cvaa 84,196,160,232
ex 44
mvaa 84,204,164
ex 45
mvaa 84,212,168
dl 246,24,51,1024
mvaa 84,220,172
ex 46
cvaa 112,224,160,232
ex 47
mvaa 112,232,164
ex 16
mvaa 112,240,168
ex 17
mvaa 112,248,172
dl 249,24,51,2048
cvaa 116,228,160,232
ex 18
mvaa 116,236,164
ex 19
mvaa 116,244,168
ex 20
mvaa 116,252,172
ex 21
cvaa 88,192,176,232
dl 247,24,51,3072
s_waitcnt vmcnt(8)
v_cmp_ne_u32_e32 vcc,4,v253
mvaa 88,200,180
s_cbranch_vccnz L_spbe
L_sprbe:
ex 22
mvaa 88,208,184
s_xor_b32 s50,s66,262144
ex 23
mvaa 88,216,188
ex 48
cvaa 92,196,176,232
ex 49
mvaa 92,204,180
la 128,252,50,0
mvaa 92,212,184
s_waitcnt lgkmcnt(0)
s_barrier
mvaa 92,220,188
s_mov_b32 m0,s72
ex 50
cvaa 120,224,176,232
s_lshl_b32 s72,s22,11
ex 51
mvaa 120,232,180
s_add_u32 s72,s72,s71
ex 52
mvaa 120,240,184
ds 255,8,76,0
mvaa 120,248,188
s_add_u32 s71,s70,131072
ex 53
cvaa 124,228,176,232
ex 54
mvaa 124,236,180
ex 55
mvaa 124,244,184
ex 24
mvaa 124,252,188
ds 255,8,76,1024
maav 0,64,0
s_cmp_lg_u32 s19,7
s_cbranch_scc1 L_j7e
s_nop 1
s_lshl_b32 s86,s12,12
s_add_u32 s86,s86,131072
v_add_u32_e32 v236,s86,v244
ra 160,236,0
ra 164,236,1024
ra 168,236,2048
ra 172,236,3072
ra 176,236,16384
ra 180,236,17408
ra 184,236,18432
ra 188,236,19456
v_add_u32_e32 v224,s94,v224
s_waitcnt lgkmcnt(0)
L_j7e:
ex 25
maav 0,96,4
ex 26
maav 4,68,0
ex 27
maav 4,100,4
cv 11,14,15
cv 12,40,41
maav 8,72,0
ds 255,8,76,2048
maav 8,104,4
cv 13,42,43
cv 14,44,45
maav 12,76,0
cv 15,46,47
cv 16,16,17
maav 12,108,4
ex 28
v_smfmac_f32_16x16x64_bf16 v[144:147],v[228:231],v[0:7],v248
cv 17,18,19
maav 16,80,0
ds 255,8,76,3072
maav 16,112,4
s_mov_b32 m0,s73
ex 29
maav 20,84,0
s_add_u32 s73,s72,131072
ex 30
maav 20,116,4
ex 31
maav 24,88,0
ex 56
maav 24,120,4
ds 255,8,77,0
maav 28,92,0
ex 57
maav 28,124,4
ex 58
maav 32,64,8
ex 59
maav 32,96,12
ex 60
maav 36,68,8
ds 255,8,77,1024
maav 36,100,12
cv 18,20,21
cv 19,22,23
maav 40,72,8
cv 20,48,49
cv 21,50,51
maav 40,104,12
cv 22,52,53
cv 23,54,55
maav 44,76,8
ex 61
maav 44,108,12
ds 255,8,77,2048
v_smfmac_f32_16x16x64_bf16 v[148:151],v[228:231],v[8:15],v248
cv 24,24,25
maav 48,80,8
ex 62
maav 48,112,12
ex 63
maav 52,84,8
ra 192,244,0
cv 25,26,27
maav 52,116,12
ds 255,8,77,3072
maav 56,88,8
cv 26,28,29
cv 27,30,31
maav 56,120,12
cv 28,56,57
cv 29,58,59
maav 60,92,8
cv 30,60,61
cv 31,62,63
maav 60,124,12
ex 64
mvav 160,64,16
la 132,252,50,2048
mvav 160,96,20
ex 65
mvav 164,68,16
ex 66
mvav 164,100,20
ex 67
mvav 168,72,16
ex 68
mvav 168,104,20
la 136,250,50,0
mvav 172,76,16
ex 69
mvav 172,108,20
ex 70
v_smfmac_f32_16x16x64_bf16 v[152:155],v[228:231],v[16:23],v248
ra 200,244,2048
mvav 176,80,16
ra 208,244,4096
cv 64,64,65
mvav 176,112,20
la 140,250,50,2048
mvav 180,84,16
ra 216,244,6144
cv 65,66,67
mvav 180,116,20
ex 71
mvav 184,88,16
ex 96
mvav 184,120,20
s_barrier
ex 97
mvav 188,92,16
la 156,250,50,3072
mvav 188,124,20
s_add_u32 m0,s52,131072
ra 196,244,1024
s_xor_b32 s51,s62,1048576
mvav 192,64,24
ra 204,244,3072
cv 66,68,69
mvav 192,96,28
ra 212,244,5120
cv 67,70,71
mvav 196,68,24
ra 220,244,7168
cv 68,96,97
mvav 196,100,28
la 152,250,50,1024
mvav 200,72,24
ex 98
mvav 200,104,28
ex 99
mvav 204,76,24
ra 224,244,8192
cv 69,98,99
mvav 204,108,28
ra 232,244,10240
v_add_u32_e32 v6,s87,v244
v_smfmac_f32_16x16x64_bf16 v[156:159],v[228:231],v[24:31],v248
ra 240,244,12288
mvav 208,80,24
la 148,252,50,3072
mvav 208,112,28
s_xor_b32 s87,s52,4096
ra 248,244,14336
v_add_u32_e32 v7,98304,v6
mvav 212,84,24
s_add_u32 s87,s87,150528
ra 228,244,9216
v_add_u32_e32 v5,65536,v244
mvav 212,116,28
ra 236,244,11264
v_add_u32_e32 v32,s87,v244
mvav 216,88,24
ra 244,244,13312
v_and_b32_e32 v224,960,v244
mvav 216,120,28
la 144,252,50,1024
mvav 220,92,24
s_add_u32 s50,s50,s55
ra 252,244,15360
mvav 220,124,28
ra 64,225,32768
s_waitcnt lgkmcnt(8)
cvaa 24,192,176,232
dl 245,24,51,0
mvaa 24,200,180
ra 96,225,40960
mvaa 24,208,184
ra 68,225,33792
mvaa 24,216,188
dl 246,24,51,1024
cvaa 16,192,160,128
ra 100,225,41984
mvaa 16,200,164
ra 72,225,34816
mvaa 16,208,168
dl 249,24,51,2048
mvaa 16,216,172
ra 104,225,43008
cvaa 28,196,176,232
ra 76,225,35840
mvaa 28,204,180
dl 247,24,51,3072
mvaa 28,212,184
s_add_u32 m0,s52,147456
s_xor_b32 s51,s63,1048576
ra 108,225,44032
mvaa 28,220,188
ra 80,225,36864
cvaa 20,196,160,132
dl 245,24,51,0
mvaa 20,204,164
ra 112,225,45056
mvaa 20,212,168
ra 192,244,16384
mvaa 20,220,172
dl 246,24,51,1024
cvaa 56,224,176,232
ra 200,244,18432
s_waitcnt lgkmcnt(7)
mvaa 56,232,180
ra 208,244,20480
mvaa 56,240,184
dl 249,24,51,2048
mvaa 56,248,188
ra 216,244,22528
cvaa 48,224,160,136
ra 196,244,17408
mvaa 48,232,164
ra 204,244,19456
mvaa 48,240,168
ra 212,244,21504
mvaa 48,248,172
ra 220,244,23552
cvaa 60,228,176,232
ra 224,244,24576
mvaa 60,236,180
ra 232,244,26624
mvaa 60,244,184
ra 240,244,28672
mvaa 60,252,188
ra 248,244,30720
cvaa 52,228,160,140
ra 84,225,37888
mvaa 52,236,164
ra 116,225,46080
mvaa 52,244,168
la 160,252,50,0
ra 228,244,25600
mvaa 52,252,172
ra 236,244,27648
s_waitcnt lgkmcnt(10)
cvaa 8,192,176,128
ra 244,244,29696
mvaa 8,200,180
la 164,252,50,2048
ra 252,244,31744
mvaa 8,208,184
ra 88,225,38912
mvaa 8,216,188
ra 120,225,47104
cvaa 12,196,176,132
la 168,250,50,0
mvaa 12,204,180
ra 92,225,39936
ra 124,225,48128
s_waitcnt lgkmcnt(9)
mvaa 12,212,184
ex 100
mvaa 12,220,188
la 172,250,50,2048
cvaa 40,224,176,136
ex 101
mvaa 40,232,180
ex 102
mvaa 40,240,184
ex 103
mvaa 40,248,188
cv 70,100,101
cv 71,102,103
s_waitcnt lgkmcnt(4)
cvaa 44,228,176,140
ex 72
mvaa 44,236,180
s_waitcnt vmcnt(18) lgkmcnt(0)
s_barrier
ex 73
mvaa 44,244,184
la 176,252,50,1024
mvaa 44,252,188
ex 74
maav 0,64,64
ex 75
maav 0,96,68
la 188,250,50,3072
maav 4,68,64
ra 192,5,0
cv 72,72,73
maav 4,100,68
ra 196,5,1024
cv 73,74,75
maav 8,72,64
la 184,250,50,1024
ds_write_b128 v6,a[0:3] offset:0
maav 8,104,68
ex 76
maav 12,76,64
ex 77
maav 12,108,68
la 180,252,50,3072
ds_write_b128 v6,a[4:7] offset:1024
v_smfmac_f32_16x16x64_bf16 v[144:147],v[228:231],v[64:71],v248
cv 74,76,77
maav 16,80,64
ex 78
maav 16,112,68
ds_write_b128 v6,a[8:11] offset:2048
ex 79
maav 20,84,64
ex 104
maav 20,116,68
ds_write_b128 v6,a[12:15] offset:3072
ex 105
maav 24,88,64
ex 106
ex 107
ex 108
maav 24,120,68
ds_write_b128 v6,a[16:19] offset:4096
cv 75,78,79
maav 28,92,64
ex 109
ex 110
ex 111
maav 28,124,68
cv 76,104,105
cv 77,106,107
cv 78,108,109
cv 79,110,111
maav 32,64,72
ds_write_b128 v6,a[20:23] offset:5120
maav 32,96,76
ex 80
maav 36,68,72
ex 81
maav 36,100,76
ds_write_b128 v6,a[24:27] offset:6144
maav 40,72,72
ex 82
maav 40,104,76
ex 83
maav 44,76,72
ds_write_b128 v6,a[28:31] offset:7168
maav 44,108,76
cv 80,80,81
cv 81,82,83
v_smfmac_f32_16x16x64_bf16 v[148:151],v[228:231],v[72:79],v248
ex 84
maav 48,80,72
ds_write_b128 v7,a[32:35] offset:0
maav 48,112,76
ex 85
ex 86
ex 87
maav 52,84,72
ex 112
ex 113
maav 52,116,76
ds_write_b128 v7,a[36:39] offset:1024
cv 82,84,85
maav 56,88,72
ex 114
ex 115
ex 116
maav 56,120,76
ex 117
ex 118
ex 119
maav 60,92,72
ds_write_b128 v7,a[40:43] offset:2048
cv 83,86,87
maav 60,124,76
cv 84,112,113
cv 85,114,115
cv 86,116,117
cv 87,118,119
mvav 160,64,80
ex 88
mvav 160,96,84
ds_write_b128 v7,a[44:47] offset:3072
mvav 164,68,80
v_add_f32_e32 v130,v144,v145
v_add_f32_e32 v131,v148,v149
mvav 164,100,84
ex 89
mvav 168,72,80
ds_write_b128 v7,a[48:51] offset:4096
mvav 168,104,84
ex 90
mvav 172,76,80
ex 91
mvav 172,108,84
ds_write_b128 v32,v[128:131]
v_smfmac_f32_16x16x64_bf16 v[152:155],v[228:231],v[80:87],v248
ex 92
ex 93
mvav 176,80,80
ds_write_b128 v7,a[52:55] offset:5120
ex 94
mvav 176,112,84
ra 200,5,2048
ra 204,5,3072
ex 95
cv 88,88,89
mvav 180,84,80
ra 208,5,4096
ra 212,5,5120
cv 89,90,91
cv 90,92,93
ex 120
mvav 180,116,84
ds_write_b128 v7,a[56:59] offset:6144
ex 121
mvav 184,88,80
ex 122
ex 123
cv 91,94,95
mvav 184,120,84
ex 124
ex 125
mvav 188,92,80
ds_write_b128 v7,a[60:63] offset:7168
ex 126
mvav 188,124,84
ra 216,5,6144
ra 220,5,7168
ra 224,5,8192
ra 232,5,10240
ex 127
cv 92,120,121
mvav 192,64,88
ra 240,5,12288
ra 248,5,14336
ra 228,5,9216
ra 236,5,11264
ra 244,5,13312
ra 252,5,15360
cv 93,122,123
cv 94,124,125
cv 95,126,127
v_xor_b32_e32 v6,8192,v6
s_nop 0
mvav 192,96,92
ex 16
mvav 196,68,88
s_waitcnt lgkmcnt(10)
s_xor_b32 s87,s87,0
mvav 196,100,92
s_xor_b32 s50,s66,294912
s_barrier
v_xor_b32_e32 v7,8192,v7
mvav 200,72,88
la 4,252,50,0
ra 64,5,16384
ra 96,5,24576
rv 64,6,0
rv 68,6,1024
rv 72,6,2048
mvav 200,104,92
L_vs3:
s_cbranch_execz L_vs7
rv 76,6,3072
rv 80,6,4096
rv 84,6,5120
ex 17
ex 18
mvav 204,76,88
rv 96,7,0
rv 100,7,1024
ex 19
mvav 204,108,92
la 12,252,50,2048
rv 104,7,2048
rv 108,7,3072
cv 16,16,17
v_smfmac_f32_16x16x64_bf16 v[156:159],v[228:231],v[88:95],v248
rv 112,7,4096
rv 116,7,5120
ex 20
ex 21
mvav 208,80,88
rv 120,7,6144
rv 124,7,7168
ex 22
ex 23
mvav 208,112,92
la 20,250,50,0
ra 68,5,17408
ra 100,5,25600
cv 17,18,19
cv 18,20,21
mvav 212,84,88
ra 72,5,18432
ra 104,5,26624
ex 48
ex 49
mvav 212,116,92
ra 76,5,19456
ra 108,5,27648
ex 50
ex 51
mvav 216,88,88
la 28,250,50,2048
ra 80,5,20480
ra 112,5,28672
ra 84,5,21504
mvav 216,120,92
ra 116,5,29696
cv 19,22,23
ex 52
ex 53
mvav 220,92,88
ra 88,5,22528
cv 20,48,49
ex 54
ex 55
mvav 220,124,92
la 60,250,50,3072
ra 120,5,30720
cv 21,50,51
cv 22,52,53
cv 23,54,55
s_bitcmp1_b32 s82,s81
s_cbranch_scc1 L_pend2
L_ret_eb4:
mvav 160,192,16
ra 92,5,23552
rv 88,6,6144
ra 124,5,31744
rv 92,6,7168
ex 24
s_waitcnt lgkmcnt(10)
mvav 160,224,20
ex 25
ex 26
v_smfmac_f32_16x16x64_bf16 v[152:155],v[228:231],v[16:23],v248
ex 27
v_xor_b32_e32 v32,4096,v32
mvav 164,196,16
la 52,250,50,1024
ds_read_b128 v[236:239],v32
cv 24,24,25
ex 28
mvav 164,228,20
ex 29
cv 25,26,27
ex 30
ex 31
mvav 168,200,16
ex 56
ex 57
mvav 168,232,20
la 44,252,50,3072
cv 26,28,29
ex 58
ex 59
mvav 172,204,16
ex 60
ex 61
ex 62
mvav 172,236,20
s_waitcnt lgkmcnt(0)
s_barrier
cv 27,30,31
ex 63
mvav 176,208,16
la 36,252,50,1024
cv 28,56,57
cv 29,58,59
cv 30,60,61
cv 31,62,63
mvav 176,240,20
s_add_u32 m0,s52,147456
ex 8
s_xor_b32 s51,s63,1048576
ex 9
ex 10
mvav 180,212,16
s_add_u32 s50,s50,s55
ex 11
cv 8,8,9
cv 9,10,11
ex 12
mvav 180,244,20
dl 247,24,51,3072
ex 13
ex 14
mvav 184,216,16
s_add_u32 m0,s52,0
ex 15
ex 40
ex 41
mvav 184,248,20
s_xor_b32 s51,s64,0
ex 42
ex 43
ex 44
mvav 188,220,16
la 0,252,50,0
ex 45
ex 46
mvav 188,252,20
ex 47
cv 10,12,13
cv 11,14,15
cv 12,40,41
cv 13,42,43
cv 14,44,45
v_smfmac_f32_16x16x64_bf16 v[156:159],v[228:231],v[24:31],v248
cv 15,46,47
v_bfe_u32 v225,v244,1,5
s_nop 0
v_smfmac_f32_16x16x64_bf16 v[156:159],v[228:231],v[8:15],v248
v_lshl_add_u32 v224,v224,1,v225
v_bfe_u32 v225,v244,7,2
v_lshl_add_u32 v224,v225,5,v224
mvav 192,192,24
la 8,252,50,2048
v_xor_b32_e32 v224,s69,v224
v_add_f32_e32 v152,v152,v153
v_add_f32_e32 v137,v152,v238
v_subrev_u32_e32 v236,0x23800000,v137
mvav 192,224,28
v_rcp_f32_e32 v137,v137
mvav 192,64,8
tv 128,224,32768
v_mul_f32_e32 v232,v240,v137
v_mul_f32_e32 v64,v232,v64
v_mul_f32_e32 v65,v232,v65
v_mul_f32_e32 v66,v232,v66
mvav 192,96,12
la 16,250,50,0
v_mul_f32_e32 v67,v232,v67
v_mul_f32_e32 v68,v232,v68
v_mul_f32_e32 v69,v232,v69
v_mul_f32_e32 v70,v232,v70
mvav 196,196,24
v_fmac_f32_e32 v64,v232,v160
v_fmac_f32_e32 v65,v232,v161
v_mul_f32_e32 v71,v232,v71
mvav 196,228,28
v_fmac_f32_e32 v66,v232,v162
v_fmac_f32_e32 v67,v232,v163
v_fmac_f32_e32 v68,v232,v164
v_fmac_f32_e32 v69,v232,v165
v_fmac_f32_e32 v70,v232,v166
mvav 196,68,8
la 24,250,50,2048
v_fmac_f32_e32 v71,v232,v167
cv 160,64,65
cv 161,66,67
cv 162,68,69
cv 163,70,71
mvav 196,100,12
tv 130,224,34816
tv 132,224,36864
mvav 200,200,24
tv 134,224,38912
tv 144,224,40960
v_mul_f32_e32 v72,v232,v72
v_mul_f32_e32 v73,v232,v73
v_mul_f32_e32 v74,v232,v74
mvav 200,232,28
buffer_store_dwordx4 v[160:163],v244,s[8:11],s14 offen offset:0 sc0 sc1
v_mul_f32_e32 v75,v232,v75
v_mul_f32_e32 v76,v232,v76
v_mul_f32_e32 v77,v232,v77
v_mul_f32_e32 v78,v232,v78
mvav 200,72,8
v_fmac_f32_e32 v72,v232,v168
v_fmac_f32_e32 v73,v232,v169
v_mul_f32_e32 v79,v232,v79
mvav 200,104,12
v_fmac_f32_e32 v74,v232,v170
v_fmac_f32_e32 v75,v232,v171
v_fmac_f32_e32 v76,v232,v172
v_fmac_f32_e32 v77,v232,v173
v_fmac_f32_e32 v78,v232,v174
)"
R"(mvav 204,204,24
la 56,250,50,3072
v_fmac_f32_e32 v79,v232,v175
cv 168,72,73
cv 169,74,75
cv 170,76,77
cv 171,78,79
mvav 204,236,28
tv 146,224,43008
tv 148,224,45056
mvav 204,76,8
tv 150,224,47104
v_mul_f32_e32 v80,v232,v80
v_mul_f32_e32 v81,v232,v81
v_mul_f32_e32 v82,v232,v82
mvav 204,108,12
buffer_store_dwordx4 v[168:171],v244,s[8:11],s14 offen offset:2048 sc0 sc1
v_mul_f32_e32 v83,v232,v83
v_mul_f32_e32 v84,v232,v84
v_mul_f32_e32 v85,v232,v85
v_mul_f32_e32 v86,v232,v86
v_mul_f32_e32 v87,v232,v87
mvav 208,208,24
v_fmac_f32_e32 v80,v232,v176
v_fmac_f32_e32 v81,v232,v177
v_fmac_f32_e32 v82,v232,v178
mvav 208,240,28
v_fmac_f32_e32 v83,v232,v179
v_fmac_f32_e32 v84,v232,v180
v_fmac_f32_e32 v85,v232,v181
cv 176,80,81
cv 177,82,83
mvav 208,80,8
la 48,250,50,1024
v_fmac_f32_e32 v86,v232,v182
v_fmac_f32_e32 v87,v232,v183
cv 178,84,85
cv 179,86,87
mvav 208,112,12
v_mul_f32_e32 v88,v232,v88
v_mul_f32_e32 v89,v232,v89
v_mul_f32_e32 v90,v232,v90
mvav 212,212,24
v_mul_f32_e32 v91,v232,v91
v_mul_f32_e32 v92,v232,v92
v_mul_f32_e32 v93,v232,v93
v_mul_f32_e32 v94,v232,v94
v_mul_f32_e32 v95,v232,v95
mvav 212,244,28
buffer_store_dwordx4 v[176:179],v244,s[8:11],s88 offen offset:0 sc0 sc1
v_fmac_f32_e32 v88,v232,v184
v_fmac_f32_e32 v89,v232,v185
v_fmac_f32_e32 v90,v232,v186
v_fmac_f32_e32 v91,v232,v187
cv 184,88,89
mvav 212,84,8
v_fmac_f32_e32 v92,v232,v188
cv 185,90,91
mvav 212,116,12
v_fmac_f32_e32 v93,v232,v189
v_fmac_f32_e32 v94,v232,v190
v_fmac_f32_e32 v95,v232,v191
cv 186,92,93
cv 187,94,95
mvav 216,216,24
la 40,252,50,3072
v_add_f32_e32 v156,v156,v157
v_add_f32_e32 v141,v156,v239
v_subrev_u32_e32 v237,0x23800000,v141
v_rcp_f32_e32 v141,v141
mvav 216,248,28
v_mul_f32_e32 v233,v241,v141
v_mul_f32_e32 v96,v233,v96
mvav 216,88,8
v_mul_f32_e32 v97,v233,v97
v_mul_f32_e32 v98,v233,v98
v_mul_f32_e32 v99,v233,v99
v_mul_f32_e32 v100,v233,v100
mvav 216,120,12
buffer_store_dwordx4 v[184:187],v244,s[8:11],s88 offen offset:2048 sc0 sc1
v_fmac_f32_e32 v96,v233,v192
v_fmac_f32_e32 v97,v233,v193
v_mul_f32_e32 v101,v233,v101
v_mul_f32_e32 v102,v233,v102
v_mul_f32_e32 v103,v233,v103
mvav 220,220,24
v_fmac_f32_e32 v98,v233,v194
v_fmac_f32_e32 v99,v233,v195
mvav 220,252,28
v_fmac_f32_e32 v100,v233,v196
v_fmac_f32_e32 v101,v233,v197
cv 192,96,97
cv 193,98,99
mvav 220,92,8
la 32,252,50,1024
v_fmac_f32_e32 v102,v233,v198
v_fmac_f32_e32 v103,v233,v199
cv 194,100,101
cv 195,102,103
mvav 220,124,12
v_xor_b32_e32 v225,32,v224
v_xor_b32_e32 v226,64,v224
s_waitcnt lgkmcnt(0)
zvva 0,128,128
tv 138,225,34816
tv 142,225,38912
tv 152,225,40960
s_waitcnt vmcnt(36)
mvva 0,132,132
tv 136,225,32768
tv 154,225,43008
mvva 0,144,136
tv 140,225,36864
tv 158,225,47104
mvva 0,148,140
tv 156,225,45056
v_mul_f32_e32 v104,v233,v104
v_mul_f32_e32 v105,v233,v105
v_mul_f32_e32 v106,v233,v106
zvva 4,148,156
buffer_store_dwordx4 v[192:195],v244,s[8:11],s14 offen offset:1024 sc0 sc1
v_mul_f32_e32 v107,v233,v107
v_mul_f32_e32 v108,v233,v108
v_mul_f32_e32 v109,v233,v109
v_mul_f32_e32 v110,v233,v110
mvva 4,144,152
v_fmac_f32_e32 v104,v233,v200
v_fmac_f32_e32 v105,v233,v201
v_fmac_f32_e32 v106,v233,v202
v_mul_f32_e32 v111,v233,v111
mvva 4,132,148
v_fmac_f32_e32 v107,v233,v203
v_fmac_f32_e32 v108,v233,v204
v_fmac_f32_e32 v109,v233,v205
cv 200,104,105
cv 201,106,107
mvva 4,128,144
v_fmac_f32_e32 v110,v233,v206
v_fmac_f32_e32 v111,v233,v207
cv 202,108,109
cv 203,110,111
s_waitcnt vmcnt(16)
zvva 8,128,160
v_cmp_lt_u32_e64 s[42:43],s48,v236
v_mul_f32_e32 v112,v233,v112
s_or_b64 s[40:41],s[40:41],s[42:43]
v_cmp_lt_u32_e64 s[42:43],s48,v237
mvva 8,132,164
s_or_b64 s[40:41],s[40:41],s[42:43]
v_mul_f32_e32 v113,v233,v113
v_mul_f32_e32 v114,v233,v114
v_mul_f32_e32 v115,v233,v115
mvva 8,144,168
dl 245,36,51,0
v_mul_f32_e32 v116,v233,v116
v_mul_f32_e32 v117,v233,v117
v_mul_f32_e32 v118,v233,v118
v_mul_f32_e32 v119,v233,v119
v_mul_f32_e32 v120,v233,v120
mvva 8,148,172
v_fmac_f32_e32 v112,v233,v208
v_fmac_f32_e32 v113,v233,v209
v_fmac_f32_e32 v114,v233,v210
v_fmac_f32_e32 v115,v233,v211
zvva 12,148,188
v_fmac_f32_e32 v116,v233,v212
v_fmac_f32_e32 v117,v233,v213
cv 208,112,113
cv 209,114,115
mvva 12,144,184
v_fmac_f32_e32 v118,v233,v214
v_fmac_f32_e32 v119,v233,v215
cv 210,116,117
cv 211,118,119
mvva 12,132,180
v_mul_f32_e32 v121,v233,v121
v_mul_f32_e32 v122,v233,v122
v_mul_f32_e32 v123,v233,v123
mvva 12,128,176
v_mul_f32_e32 v124,v233,v124
v_mul_f32_e32 v125,v233,v125
v_mul_f32_e32 v126,v233,v126
v_mul_f32_e32 v127,v233,v127
s_waitcnt lgkmcnt(0)
zvva 28,136,176
buffer_store_dwordx4 v[200:203],v244,s[8:11],s14 offen offset:3072 sc0 sc1
tv 144,226,40960
v_fmac_f32_e32 v120,v233,v216
v_fmac_f32_e32 v121,v233,v217
v_fmac_f32_e32 v122,v233,v218
v_fmac_f32_e32 v123,v233,v219
mvva 28,140,180
tv 128,226,32768
tv 130,226,34816
cv 216,120,121
cv 217,122,123
mvva 28,152,184
tv 132,226,36864
tv 134,226,38912
v_fmac_f32_e32 v124,v233,v220
v_fmac_f32_e32 v125,v233,v221
v_fmac_f32_e32 v126,v233,v222
mvva 28,156,188
tv 146,226,43008
v_fmac_f32_e32 v127,v233,v223
cv 218,124,125
cv 219,126,127
zvva 24,156,172
tv 148,226,45056
tv 150,226,47104
mvva 24,152,168
v_xor_b32_e32 v227,96,v224
v_add_u32_e32 v232,131072,v224
mvva 24,140,164
buffer_store_dwordx4 v[208:211],v244,s[8:11],s88 offen offset:1024 sc0 sc1
mvva 24,136,160
v_add_u32_e32 v233,131072,v225
v_add_u32_e32 v234,131072,v226
zvva 20,136,144
v_add_u32_e32 v235,131072,v227
v_mov_b32_e32 v242,83914752
mvva 20,140,148
v_add_u32_e32 v228,65536,v224
v_add_u32_e32 v229,65536,v225
mvva 20,152,152
v_add_u32_e32 v230,65536,v226
v_add_u32_e32 v231,65536,v227
mvva 20,156,156
v_and_b32_e32 v243,240,v244
v_lshlrev_b32_e32 v243,7,v243
zvva 16,156,140
buffer_store_dwordx4 v[216:219],v244,s[8:11],s88 offen offset:3072 sc0 sc1
s_cmp_lg_u64 s[40:41],0
s_cbranch_scc1 L_redo
L_redo_r4:
mvva 16,152,136
tv 156,227,45056
v_mov_b32_e32 v236,16256
mvva 16,140,132
tv 158,227,47104
v_and_b32_e32 v248,256,v244
mvva 16,136,128
tv 152,227,40960
v_lshrrev_b32_e32 v248,3,v248
s_waitcnt lgkmcnt(3)
zvva 32,128,128
tv 140,227,36864
v_add_u32_e32 v243,v243,v248
mvva 32,132,132
tv 136,227,32768
tv 138,227,34816
mvva 32,144,136
dl 246,36,51,1024
tv 142,227,38912
mvva 32,148,140
tv 154,227,43008
v_and_b32_e32 v248,512,v244
zvva 36,148,156
v_lshrrev_b32_e32 v248,5,v248
v_add_u32_e32 v243,v243,v248
mvva 36,144,152
v_and_b32_e32 v248,16,v244
v_lshlrev_b32_e32 v248,v248,v236
mvva 36,132,148
v_cndmask_b32_e64 v238,0,v248,s[74:75]
s_mov_b32 s74,0xc0000c
mvva 36,128,144
s_mov_b32 s75,0xc0000c00
v_mov_b32_e32 v240,0
v_cndmask_b32_e64 v239,0,v248,s[74:75]
zvva 40,128,160
dl 249,36,51,2048
mvva 40,132,164
v_mov_b32_e32 v241,0
v_mov_b32_e32 v236,0
mvva 40,144,168
v_mov_b32_e32 v237,0
mvva 40,148,172
zvva 44,148,188
mvva 44,144,184
mvva 44,132,180
dl 247,36,51,3072
s_add_u32 m0,s52,16384
s_xor_b32 s51,s65,0
mvva 44,128,176
tv 144,224,57344
s_waitcnt lgkmcnt(1)
zvva 60,136,176
tv 132,224,53248
tv 134,224,55296
mvva 60,140,180
tv 128,224,49152
tv 130,224,51200
mvva 60,152,184
mvva 60,156,188
tv 146,224,59392
zvva 56,156,172
dl 245,36,51,0
tv 148,224,61440
tv 150,224,63488
mvva 56,152,168
mvva 56,140,164
mvva 56,136,160
zvva 52,136,144
mvva 52,140,148
mvva 52,152,152
dl 246,36,51,1024
mvva 52,156,156
zvva 48,156,140
mvva 48,152,136
mvva 48,140,132
tv 156,225,61440
mvva 48,136,128
tv 152,225,57344
s_waitcnt lgkmcnt(2)
zvva 64,128,128
dl 249,36,51,2048
tv 140,225,53248
mvva 64,132,132
tv 136,225,49152
tv 138,225,51200
mvva 64,144,136
tv 142,225,55296
mvva 64,148,140
tv 154,225,59392
zvva 68,148,156
tv 158,225,63488
mvva 68,144,152
mvva 68,132,148
dl 247,36,51,3072
s_lshl_b32 s51,s33,5
s_add_u32 m0,s52,98304
s_xor_b32 s51,s64,s51
mvva 68,128,144
zvva 72,128,160
mvva 72,132,164
s_waitcnt vmcnt(18)
s_mov_b32 s90,0
mvva 72,144,168
s_xor_b32 s50,s67,0
mvva 72,148,172
zvva 76,148,188
dl 245,36,51,0
mvva 76,144,184
mvva 76,132,180
tv 148,226,61440
mvva 76,128,176
tv 144,226,57344
s_waitcnt lgkmcnt(2)
zvva 92,136,176
tv 132,226,53248
mvva 92,140,180
tv 128,226,49152
tv 130,226,51200
mvva 92,152,184
dl 246,36,51,1024
tv 134,226,55296
mvva 92,156,188
tv 146,226,59392
zvva 88,156,172
tv 150,226,63488
mvva 88,152,168
mvva 88,140,164
mvva 88,136,160
zvva 84,136,144
dl 249,36,51,2048
mvva 84,140,148
mvva 84,152,152
mvva 84,156,156
zvva 80,156,140
mvva 80,152,136
mvva 80,140,132
dl 247,36,51,3072
mvva 80,136,128
s_lshl_b32 s51,s33,5
tv 152,227,57344
s_add_u32 m0,s52,114688
s_waitcnt lgkmcnt(1)
zvva 96,128,128
s_xor_b32 s51,s65,s51
tv 140,227,53248
tv 142,227,55296
mvva 96,132,132
tv 136,227,49152
tv 138,227,51200
mvva 96,144,136
mvva 96,148,140
tv 154,227,59392
zvva 100,148,156
lv 164,252,50,0
tv 156,227,61440
tv 158,227,63488
mvva 100,144,152
mvva 100,132,148
mvva 100,128,144
zvva 104,128,160
mvva 104,132,164
mvva 104,144,168
lv 172,252,50,2048
mvva 104,148,172
zvva 108,148,188
mvva 108,144,184
mvva 108,132,180
mvva 108,128,176
s_barrier
s_waitcnt lgkmcnt(0)
zvva 124,136,176
lv 180,250,50,0
tv 132,232,4096
tv 144,232,8192
tv 146,232,10240
mvva 124,140,180
L_vs4:
s_cbranch_execz L_vs8
tv 128,232,0
tv 130,232,2048
mvva 124,152,184
tv 134,232,6144
mvva 124,156,188
tv 148,232,12288
tv 150,232,14336
zvva 120,156,172
mvva 120,152,168
mvva 120,140,164
lv 188,250,50,2048
mvva 120,136,160
zvva 116,136,144
mvva 116,140,148
mvva 116,152,152
mvva 116,156,156
zvva 112,156,140
lv 220,250,50,3072
mvva 112,152,136
mvva 112,140,132
tv 156,233,12288
mvva 112,136,128
tv 152,233,8192
s_waitcnt lgkmcnt(2)
mvva 0,128,4
tv 140,233,4096
mvva 0,132,12
tv 136,233,0
tv 138,233,2048
mvva 0,144,20
lv 212,250,50,1024
tv 142,233,6144
mvva 0,148,28
tv 154,233,10240
mvva 4,148,60
tv 158,233,14336
mvva 4,144,52
mvva 4,132,44
mvva 4,128,36
mvva 8,128,0
lv 204,252,50,3072
mvva 8,132,8
mvva 8,144,16
mvva 8,148,24
mvva 12,148,56
s_waitcnt vmcnt(27)
mvva 12,144,48
s_waitcnt vmcnt(25)
mvva 12,132,40
lv 196,252,50,1024
s_waitcnt vmcnt(24)
mvva 12,128,32
tv 144,234,8192
s_waitcnt lgkmcnt(1)
mvva 28,136,32
tv 132,234,4096
tv 134,234,6144
mvva 28,140,40
tv 128,234,0
tv 130,234,2048
mvva 28,152,48
mvva 28,156,56
tv 146,234,10240
mvva 24,156,24
dl 245,36,51,0
s_waitcnt vmcnt(20)
s_barrier
tv 148,234,12288
tv 150,234,14336
mvva 24,152,16
mvva 24,140,8
mvva 24,136,0
mvva 20,136,36
mvva 20,140,44
mvva 20,152,52
s_cmp_eq_u32 s12,0
s_cselect_b64 exec,1,0
buffer_store_dword v251,off,s[8:11],s61 sc0 sc1
s_mov_b64 exec,-1
mvva 20,156,60
mvva 16,156,28
mvva 16,152,20
mvva 16,140,12
tv 156,235,12288
mvva 16,136,4
tv 152,235,8192
s_waitcnt lgkmcnt(2)
mvva 32,128,4
dl 246,36,51,1024
tv 140,235,4096
mvva 32,132,12
tv 136,235,0
tv 138,235,2048
mvva 32,144,20
tv 142,235,6144
mvva 32,148,28
tv 154,235,10240
mvva 36,148,60
tv 158,235,14336
mvva 36,144,52
mvva 36,132,44
dl 249,36,51,2048
mvva 36,128,36
mvva 40,128,0
mvva 40,132,8
mvva 40,144,16
mvva 40,148,24
mvva 44,148,56
dl 247,36,51,3072
s_lshl_b32 s51,s34,5
s_add_u32 m0,s52,65536
s_xor_b32 s51,s64,s51
mvva 44,144,48
tv 148,232,28672
mvva 44,132,40
s_xor_b32 s53,s60,s33
tv 150,232,30720
mvva 44,128,32
tv 144,232,24576
s_waitcnt lgkmcnt(3)
mvva 60,136,32
tv 132,232,20480
mvva 60,140,40
tv 128,232,16384
tv 130,232,18432
mvva 60,152,48
dl 245,36,51,0
tv 134,232,22528
mvva 60,156,56
tv 146,232,26624
mvva 56,156,24
mvva 56,152,16
mvva 56,140,8
mvva 56,136,0
mvva 52,136,36
buffer_load_dword v253,v242,s[8:11],s53 offen sc1
mvva 52,140,44
mvva 52,152,52
mvva 52,156,60
mvva 48,156,28
mvva 48,152,20
mvva 48,140,12
dl 246,36,51,1024
mvva 48,136,4
tv 152,233,24576
s_waitcnt lgkmcnt(1)
mvva 64,128,4
tv 140,233,20480
tv 142,233,22528
mvva 64,132,12
tv 136,233,16384
tv 138,233,18432
mvva 64,144,20
mvva 64,148,28
tv 154,233,26624
mvva 68,148,60
dl 249,36,51,2048
tv 156,233,28672
tv 158,233,30720
mvva 68,144,52
mvva 68,132,44
mvva 68,128,36
mvva 72,128,0
mvva 72,132,8
mvva 72,144,16
dl 247,36,51,3072
s_lshl_b32 s51,s34,5
s_add_u32 m0,s52,81920
s_xor_b32 s51,s65,s51
mvva 72,148,24
mvva 76,148,56
mvva 76,144,48
mvva 76,132,40
tv 148,234,28672
mvva 76,128,32
tv 144,234,24576
s_waitcnt lgkmcnt(2)
mvva 92,136,32
dl 245,36,51,0
tv 132,234,20480
mvva 92,140,40
tv 128,234,16384
tv 130,234,18432
mvva 92,152,48
tv 134,234,22528
mvva 92,156,56
tv 146,234,26624
mvva 88,156,24
tv 150,234,30720
mvva 88,152,16
mvva 88,140,8
dl 246,36,51,1024
mvva 88,136,0
mvva 84,136,36
mvva 84,140,44
mvva 84,152,52
mvva 84,156,60
mvva 80,156,28
dl 249,36,51,2048
mvva 80,152,20
mvva 80,140,12
tv 156,235,28672
mvva 80,136,4
tv 152,235,24576
s_waitcnt lgkmcnt(2)
mvva 96,128,4
tv 140,235,20480
mvva 96,132,12
tv 136,235,16384
tv 138,235,18432
mvva 96,144,20
dl 247,36,51,3072
s_lshl_b32 s51,s35,5
s_add_u32 m0,s52,32768
s_xor_b32 s51,s64,s51
tv 142,235,22528
mvva 96,148,28
tv 154,235,26624
mvva 100,148,60
tv 158,235,30720
mvva 100,144,52
mvva 100,132,44
mvva 100,128,36
mvva 104,128,0
dl 245,36,51,0
mvva 104,132,8
mvva 104,144,16
mvva 104,148,24
mvva 108,148,56
mvva 108,144,48
mvva 108,132,40
dl 246,36,51,1024
mvva 108,128,32
s_waitcnt vmcnt(27)
s_barrier
s_waitcnt lgkmcnt(0)
mvva 124,136,32
tv 132,224,4096
tv 134,224,6144
tv 144,224,8192
mvva 124,140,40
tv 128,224,0
tv 130,224,2048
mvva 124,152,48
mvva 124,156,56
tv 146,224,10240
mvva 120,156,24
dl 249,36,51,2048
tv 148,224,12288
tv 150,224,14336
mvva 120,152,16
mvva 120,140,8
mvva 120,136,0
mvva 116,136,36
mvva 116,140,44
mvva 116,152,52
dl 247,36,51,3072
s_lshl_b32 s51,s35,5
s_add_u32 m0,s52,49152
s_xor_b32 s51,s65,s51
mvva 116,156,60
mvva 112,156,28
mvva 112,152,20
s_waitcnt vmcnt(11)
v_cmp_ne_u32_e32 vcc,4,v253
mvva 112,140,12
dl 245,36,51,0
s_cbranch_vccnz L_spa3e
L_spra3e:
mvva 112,136,4
s_xor_b32 s50,s67,s33
tv 152,225,8192
s_waitcnt lgkmcnt(1)
mvvv 0,128,164
tv 140,225,4096
tv 142,225,6144
mvvv 0,132,172
tv 136,225,0
tv 138,225,2048
mvvv 0,144,180
dl 246,36,51,1024
mvvv 0,148,188
tv 154,225,10240
mvvv 4,148,220
tv 156,225,12288
tv 158,225,14336
mvvv 4,144,212
mvvv 4,132,204
la 196,252,50,0
mvvv 4,128,196
mvvv 8,128,160
s_xor_b32 s53,s60,s34
mvvv 8,132,168
mvvv 8,144,176
la 204,252,50,2048
mvvv 8,148,184
mvvv 12,148,216
mvvv 12,144,208
mvvv 12,132,200
buffer_load_dword v253,v242,s[8:11],s53 offen sc1
mvvv 12,128,192
tv 144,226,8192
s_waitcnt lgkmcnt(1)
mvvv 28,136,192
tv 132,226,4096
tv 134,226,6144
mvvv 28,140,200
tv 128,226,0
tv 130,226,2048
mvvv 28,152,208
la 212,250,50,0
mvvv 28,156,216
tv 146,226,10240
mvvv 24,156,184
tv 148,226,12288
tv 150,226,14336
mvvv 24,152,176
mvvv 24,140,168
la 220,250,50,2048
mvvv 24,136,160
mvvv 20,136,196
mvvv 20,140,204
mvvv 20,152,212
la 252,250,50,3072
mvvv 20,156,220
mvvv 16,156,188
mvvv 16,152,180
mvvv 16,140,172
la 244,250,50,1024
mvvv 16,136,164
tv 152,227,8192
s_waitcnt lgkmcnt(1)
mvvv 32,128,164
tv 140,227,4096
tv 142,227,6144
mvvv 32,132,172
tv 136,227,0
tv 138,227,2048
mvvv 32,144,180
la 236,252,50,3072
mvvv 32,148,188
tv 154,227,10240
mvvv 36,148,220
tv 156,227,12288
tv 158,227,14336
mvvv 36,144,212
mvvv 36,132,204
la 228,252,50,1024
s_add_u32 s50,s50,s55
mvvv 36,128,196
mvvv 40,128,160
mvvv 40,132,168
mvvv 40,144,176
la 192,252,50,0
mvvv 40,148,184
mvvv 44,148,216
mvvv 44,144,208
mvvv 44,132,200
la 200,252,50,2048
mvvv 44,128,192
tv 144,224,24576
s_waitcnt lgkmcnt(1)
mvvv 60,136,192
tv 132,224,20480
tv 134,224,22528
mvvv 60,140,200
tv 128,224,16384
tv 130,224,18432
mvvv 60,152,208
la 208,250,50,0
mvvv 60,156,216
tv 146,224,26624
mvvv 56,156,184
tv 148,224,28672
tv 150,224,30720
mvvv 56,152,176
mvvv 56,140,168
la 216,250,50,2048
mvvv 56,136,160
mvvv 52,136,196
mvvv 52,140,204
mvvv 52,152,212
la 248,250,50,3072
mvvv 52,156,220
mvvv 48,156,188
mvvv 48,152,180
mvvv 48,140,172
la 240,250,50,1024
mvvv 48,136,164
tv 152,225,24576
s_waitcnt lgkmcnt(1)
mvvv 64,128,164
tv 140,225,20480
tv 142,225,22528
mvvv 64,132,172
tv 136,225,16384
tv 138,225,18432
mvvv 64,144,180
la 232,252,50,3072
mvvv 64,148,188
tv 154,225,26624
mvvv 68,148,220
tv 156,225,28672
tv 158,225,30720
mvvv 68,144,212
mvvv 68,132,204
la 224,252,50,1024
mvvv 68,128,196
mvvv 72,128,160
mvvv 72,132,168
mvvv 72,144,176
dl 249,36,51,2048
mvvv 72,148,184
mvvv 76,148,216
mvvv 76,144,208
mvvv 76,132,200
dl 247,36,51,3072
s_lshl_b32 s51,s98,5
s_xor_b32 s51,s64,s51
s_add_u32 m0,s52,131072
s_xor_b32 s51,s51,4194304
mvvv 76,128,192
tv 144,226,24576
s_waitcnt lgkmcnt(1)
mvvv 92,136,192
tv 132,226,20480
tv 134,226,22528
mvvv 92,140,200
tv 128,226,16384
tv 130,226,18432
mvvv 92,152,208
dl 245,36,51,0
mvvv 92,156,216
tv 146,226,26624
mvvv 88,156,184
tv 148,226,28672
tv 150,226,30720
mvvv 88,152,176
mvvv 88,140,168
dl 246,36,51,1024
mvvv 88,136,160
s_waitcnt vmcnt(18)
v_cmp_ne_u32_e32 vcc,4,v253
mvvv 84,136,196
s_cbranch_vccnz L_spa4e
L_spra4e:
mvvv 84,140,204
s_xor_b32 s50,s67,s34
mvvv 84,152,212
dl 249,36,51,2048
mvvv 84,156,220
mvvv 80,156,188
mvvv 80,152,180
mvvv 80,140,172
la 68,252,50,0
mvvv 80,136,164
tv 152,227,24576
s_waitcnt lgkmcnt(1)
mvvv 96,128,164
tv 140,227,20480
tv 142,227,22528
mvvv 96,132,172
tv 136,227,16384
tv 138,227,18432
mvvv 96,144,180
la 76,252,50,2048
s_xor_b32 s53,s60,s35
mvvv 96,148,188
tv 154,227,26624
mvvv 100,148,220
tv 156,227,28672
tv 158,227,30720
mvvv 100,144,212
mvvv 100,132,204
la 84,250,50,0
mvvv 100,128,196
mvvv 104,128,160
mvvv 104,132,168
mvvv 104,144,176
buffer_load_dword v253,v242,s[8:11],s53 offen sc1
mvvv 104,148,184
mvvv 108,148,216
mvvv 108,144,208
mvvv 108,132,200
la 92,250,50,2048
mvvv 108,128,192
s_barrier
s_waitcnt lgkmcnt(0)
mvvv 124,136,192
tv 132,228,36864
tv 134,228,38912
tv 144,228,40960
mvvv 124,140,200
tv 128,228,32768
tv 130,228,34816
mvvv 124,152,208
la 124,250,50,3072
mvvv 124,156,216
tv 146,228,43008
mvvv 120,156,184
tv 148,228,45056
tv 150,228,47104
mvvv 120,152,176
mvvv 120,140,168
la 116,250,50,1024
mvvv 120,136,160
mvvv 116,136,196
mvvv 116,140,204
mvvv 116,152,212
la 108,252,50,3072
mvvv 116,156,220
mvvv 112,156,188
mvvv 112,152,180
mvvv 112,140,172
la 100,252,50,1024
s_add_u32 s50,s50,s55
mvvv 112,136,164
tv 152,229,40960
s_waitcnt lgkmcnt(1)
mvva 0,128,196
tv 140,229,36864
tv 142,229,38912
mvva 0,132,204
tv 136,229,32768
tv 138,229,34816
s_waitcnt vmcnt(23)
mvva 0,144,212
la 64,252,50,0
mvva 0,148,220
tv 154,229,43008
mvva 4,148,252
tv 156,229,45056
tv 158,229,47104
mvva 4,144,244
mvva 4,132,236
la 72,252,50,2048
s_waitcnt vmcnt(23)
mvva 4,128,228
L_vs5:
s_cbranch_execz L_cw0
mvva 8,128,192
s_waitcnt vmcnt(22)
mvva 8,132,200
s_waitcnt vmcnt(21)
mvva 8,144,208
la 80,250,50,0
s_waitcnt vmcnt(21)
mvva 8,148,216
s_waitcnt vmcnt(20)
mvva 12,148,248
s_waitcnt vmcnt(19)
mvva 12,144,240
s_waitcnt vmcnt(18)
mvva 12,132,232
la 88,250,50,2048
s_waitcnt vmcnt(18)
mvva 12,128,224
tv 144,230,40960
s_waitcnt lgkmcnt(1)
mvva 28,136,224
tv 132,230,36864
tv 134,230,38912
mvva 28,140,232
tv 128,230,32768
tv 130,230,34816
mvva 28,152,240
la 120,250,50,3072
mvva 28,156,248
tv 146,230,43008
mvva 24,156,216
tv 148,230,45056
tv 150,230,47104
mvva 24,152,208
mvva 24,140,200
la 112,250,50,1024
mvva 24,136,192
mvva 20,136,228
mvva 20,140,236
mvva 20,152,244
la 104,252,50,3072
mvva 20,156,252
mvva 16,156,220
mvva 16,152,212
mvva 16,140,204
la 96,252,50,1024
mvva 16,136,196
)"
R"(tv 152,231,40960
s_waitcnt lgkmcnt(1)
mvva 32,128,196
tv 140,231,36864
tv 142,231,38912
mvva 32,132,204
tv 136,231,32768
tv 138,231,34816
mvva 32,144,212
dl 247,36,51,3072
s_lshl_b32 s51,s98,5
s_xor_b32 s51,s65,s51
s_add_u32 m0,s52,147456
s_xor_b32 s51,s51,4194304
mvva 32,148,220
tv 154,231,43008
mvva 36,148,252
tv 156,231,45056
tv 158,231,47104
mvva 36,144,244
mvva 36,132,236
dl 245,36,51,0
mvva 36,128,228
mvva 40,128,192
mvva 40,132,200
mvva 40,144,208
dl 246,36,51,1024
mvva 40,148,216
mvva 44,148,248
mvva 44,144,240
mvva 44,132,232
dl 249,36,51,2048
mvva 44,128,224
tv 144,228,57344
s_waitcnt lgkmcnt(1)
mvva 60,136,224
tv 132,228,53248
tv 134,228,55296
mvva 60,140,232
tv 128,228,49152
tv 130,228,51200
s_waitcnt vmcnt(17)
v_cmp_ne_u32_e32 vcc,4,v253
mvva 60,152,240
dl 247,36,51,3072
s_lshl_b32 s51,s98,5
s_xor_b32 s51,s64,s51
s_add_u32 m0,s52,0
s_xor_b32 s51,s51,4456448
s_cbranch_vccnz L_spa5e
L_spra5e:
mvva 60,156,248
s_xor_b32 s50,s67,s35
tv 146,228,59392
mvva 56,156,216
tv 148,228,61440
tv 150,228,63488
mvva 56,152,208
mvva 56,140,200
dl 245,36,51,0
mvva 56,136,192
mvva 52,136,228
mvva 52,140,236
mvva 52,152,244
la 132,252,50,0
mvva 52,156,252
mvva 48,156,220
s_xor_b32 s53,s60,s98
mvva 48,152,212
s_xor_b32 s53,s53,512
mvva 48,140,204
la 140,252,50,2048
mvva 48,136,196
tv 152,229,57344
s_waitcnt lgkmcnt(1)
mvva 64,128,196
tv 140,229,53248
tv 142,229,55296
mvva 64,132,204
tv 136,229,49152
tv 138,229,51200
mvva 64,144,212
buffer_load_dword v253,v242,s[8:11],s53 offen sc1
mvva 64,148,220
tv 154,229,59392
mvva 68,148,252
tv 156,229,61440
tv 158,229,63488
mvva 68,144,244
mvva 68,132,236
la 148,250,50,0
mvva 68,128,228
mvva 72,128,192
mvva 72,132,200
mvva 72,144,208
la 156,250,50,2048
mvva 72,148,216
mvva 76,148,248
mvva 76,144,240
mvva 76,132,232
la 188,250,50,3072
mvva 76,128,224
tv 144,230,57344
s_waitcnt lgkmcnt(1)
mvva 92,136,224
tv 132,230,53248
tv 134,230,55296
mvva 92,140,232
tv 128,230,49152
tv 130,230,51200
mvva 92,152,240
la 180,250,50,1024
mvva 92,156,248
tv 146,230,59392
mvva 88,156,216
tv 148,230,61440
tv 150,230,63488
mvva 88,152,208
mvva 88,140,200
la 172,252,50,3072
mvva 88,136,192
mvva 84,136,228
mvva 84,140,236
mvva 84,152,244
la 164,252,50,1024
s_add_u32 s50,s50,s55
mvva 84,156,252
mvva 80,156,220
mvva 80,152,212
mvva 80,140,204
la 128,252,50,0
mvva 80,136,196
tv 152,231,57344
s_waitcnt lgkmcnt(1)
mvva 96,128,196
tv 140,231,53248
tv 142,231,55296
mvva 96,132,204
tv 136,231,49152
tv 138,231,51200
mvva 96,144,212
la 136,252,50,2048
mvva 96,148,220
tv 154,231,59392
mvva 100,148,252
tv 156,231,61440
tv 158,231,63488
mvva 100,144,244
mvva 100,132,236
la 144,250,50,0
mvva 100,128,228
mvva 104,128,192
mvva 104,132,200
mvva 104,144,208
la 152,250,50,2048
mvva 104,148,216
mvva 108,148,248
mvva 108,144,240
mvva 108,132,232
la 184,250,50,3072
mvva 108,128,224
s_barrier
s_waitcnt lgkmcnt(0)
mvva 124,136,224
tv 132,228,4096
tv 134,228,6144
tv 144,228,8192
mvva 124,140,232
tv 128,228,0
tv 130,228,2048
mvva 124,152,240
la 176,250,50,1024
mvva 124,156,248
tv 146,228,10240
mvva 120,156,216
tv 148,228,12288
tv 150,228,14336
mvva 120,152,208
mvva 120,140,200
la 168,252,50,3072
mvva 120,136,192
mvva 116,136,228
mvva 116,140,236
mvva 116,152,244
la 160,252,50,1024
mvva 116,156,252
mvva 112,156,220
mvva 112,152,212
mvva 112,140,204
dl 246,36,51,1024
mvva 112,136,196
tv 152,229,8192
s_waitcnt lgkmcnt(1)
mvva 0,128,68
tv 140,229,4096
tv 142,229,6144
mvva 0,132,76
tv 136,229,0
tv 138,229,2048
mvva 0,144,84
dl 249,36,51,2048
s_waitcnt vmcnt(25)
mvva 0,148,92
tv 154,229,10240
mvva 4,148,124
tv 156,229,12288
tv 158,229,14336
mvva 4,144,116
mvva 4,132,108
dl 247,36,51,3072
s_lshl_b32 s51,s98,5
s_xor_b32 s51,s65,s51
s_add_u32 m0,s52,16384
s_xor_b32 s51,s51,4456448
mvva 4,128,100
mvva 8,128,64
mvva 8,132,72
mvva 8,144,80
dl 245,36,51,0
mvva 8,148,88
s_waitcnt vmcnt(18)
v_cmp_ne_u32_e32 vcc,4,v253
mvva 12,148,120
s_cbranch_vccnz L_spa6e
L_spra6e:
mvva 12,144,112
s_xor_b32 s50,s67,s98
mvva 12,132,104
dl 246,36,51,1024
s_xor_b32 s50,s50,131072
mvva 12,128,96
tv 144,230,8192
s_waitcnt lgkmcnt(1)
mvva 28,136,96
tv 132,230,4096
tv 134,230,6144
mvva 28,140,104
tv 128,230,0
tv 130,230,2048
mvva 28,152,112
la 4,252,50,0
mvva 28,156,120
tv 146,230,10240
mvva 24,156,88
tv 148,230,12288
tv 150,230,14336
mvva 24,152,80
mvva 24,140,72
la 12,252,50,2048
s_xor_b32 s53,s60,s98
s_xor_b32 s53,s53,8704
mvva 24,136,64
mvva 20,136,100
mvva 20,140,108
mvva 20,152,116
la 20,250,50,0
mvva 20,156,124
mvva 16,156,92
mvva 16,152,84
mvva 16,140,76
buffer_load_dword v253,v242,s[8:11],s53 offen sc1
mvva 16,136,68
tv 152,231,8192
s_waitcnt lgkmcnt(1)
mvva 32,128,68
tv 140,231,4096
tv 142,231,6144
mvva 32,132,76
tv 136,231,0
tv 138,231,2048
mvva 32,144,84
la 28,250,50,2048
mvva 32,148,92
tv 154,231,10240
mvva 36,148,124
tv 156,231,12288
tv 158,231,14336
mvva 36,144,116
mvva 36,132,108
la 60,250,50,3072
mvva 36,128,100
mvva 40,128,64
mvva 40,132,72
mvva 40,144,80
la 52,250,50,1024
mvva 40,148,88
mvva 44,148,120
mvva 44,144,112
mvva 44,132,104
la 44,252,50,3072
mvva 44,128,96
tv 144,228,24576
s_waitcnt lgkmcnt(1)
mvva 60,136,96
tv 132,228,20480
tv 134,228,22528
mvva 60,140,104
tv 128,228,16384
tv 130,228,18432
mvva 60,152,112
la 36,252,50,1024
s_add_u32 s50,s50,s55
mvva 60,156,120
tv 146,228,26624
mvva 56,156,88
tv 148,228,28672
tv 150,228,30720
mvva 56,152,80
mvva 56,140,72
la 0,252,50,0
mvva 56,136,64
mvva 52,136,100
mvva 52,140,108
mvva 52,152,116
la 8,252,50,2048
mvva 52,156,124
mvva 48,156,92
mvva 48,152,84
mvva 48,140,76
la 16,250,50,0
mvva 48,136,68
tv 152,229,24576
s_waitcnt lgkmcnt(1)
mvva 64,128,68
tv 140,229,20480
tv 142,229,22528
mvva 64,132,76
tv 136,229,16384
tv 138,229,18432
mvva 64,144,84
la 24,250,50,2048
mvva 64,148,92
tv 154,229,26624
mvva 68,148,124
tv 156,229,28672
tv 158,229,30720
mvva 68,144,116
mvva 68,132,108
la 56,250,50,3072
mvva 68,128,100
mvva 72,128,64
mvva 72,132,72
mvva 72,144,80
la 48,250,50,1024
mvva 72,148,88
mvva 76,148,120
mvva 76,144,112
mvva 76,132,104
la 40,252,50,3072
mvva 76,128,96
tv 144,230,24576
s_waitcnt lgkmcnt(1)
mvva 92,136,96
tv 132,230,20480
tv 134,230,22528
mvva 92,140,104
tv 128,230,16384
tv 130,230,18432
mvva 92,152,112
la 32,252,50,1024
mvva 92,156,120
tv 146,230,26624
mvva 88,156,88
tv 148,230,28672
tv 150,230,30720
mvva 88,152,80
mvva 88,140,72
dl 249,36,51,2048
mvva 88,136,64
mvva 84,136,100
mvva 84,140,108
mvva 84,152,116
dl 247,36,51,3072
s_lshl_b32 s51,s98,5
s_xor_b32 s51,s64,s51
s_add_u32 m0,s52,98304
s_xor_b32 s51,s51,4718592
mvva 84,156,124
mvva 80,156,92
mvva 80,152,84
mvva 80,140,76
dl 245,36,51,0
mvva 80,136,68
tv 152,231,24576
s_waitcnt lgkmcnt(1)
mvva 96,128,68
tv 140,231,20480
tv 142,231,22528
mvva 96,132,76
tv 136,231,16384
tv 138,231,18432
mvva 96,144,84
dl 246,36,51,1024
mvva 96,148,92
tv 154,231,26624
mvva 100,148,124
tv 156,231,28672
tv 158,231,30720
mvva 100,144,116
s_waitcnt vmcnt(17)
v_cmp_ne_u32_e32 vcc,4,v253
mvva 100,132,108
dl 249,36,51,2048
s_cbranch_vccnz L_spa7e
L_spra7e:
mvva 100,128,100
s_xor_b32 s50,s67,s98
mvva 104,128,64
s_xor_b32 s50,s50,139264
mvva 104,132,72
mvva 104,144,80
dl 247,36,51,3072
mvva 104,148,88
mvva 108,148,120
mvva 108,144,112
mvva 108,132,104
lv 164,252,50,0
mvva 108,128,96
s_barrier
s_waitcnt lgkmcnt(0)
mvva 124,136,96
s_xor_b32 s53,s60,s98
tv 132,224,36864
tv 134,224,38912
tv 144,224,40960
s_xor_b32 s53,s53,16896
mvva 124,140,104
s_lshl_b32 s51,s98,5
tv 128,224,32768
tv 130,224,34816
mvva 124,152,112
lv 172,252,50,2048
mvva 124,156,120
s_xor_b32 s51,s65,s51
tv 146,224,43008
s_add_u32 m0,s52,114688
mvva 120,156,88
s_xor_b32 s51,s51,4718592
tv 148,224,45056
tv 150,224,47104
mvva 120,152,80
mvva 120,140,72
buffer_load_dword v253,v242,s[8:11],s53 offen sc1
mvva 120,136,64
mvva 116,136,100
mvva 116,140,108
mvva 116,152,116
lv 180,250,50,0
mvva 116,156,124
mvva 112,156,92
mvva 112,152,84
mvva 112,140,76
lv 188,250,50,2048
mvva 112,136,68
tv 152,225,40960
s_waitcnt lgkmcnt(1)
mvva 0,128,132
tv 140,225,36864
tv 142,225,38912
mvva 0,132,140
tv 136,225,32768
tv 138,225,34816
mvva 0,144,148
lv 220,250,50,3072
mvva 0,148,156
tv 154,225,43008
mvva 4,148,188
tv 156,225,45056
tv 158,225,47104
mvva 4,144,180
mvva 4,132,172
lv 212,250,50,1024
mvva 4,128,164
mvva 8,128,128
mvva 8,132,136
mvva 8,144,144
lv 204,252,50,3072
mvva 8,148,152
mvva 12,148,184
mvva 12,144,176
mvva 12,132,168
lv 196,252,50,1024
s_add_u32 s50,s50,s55
mvva 12,128,160
tv 144,226,40960
s_waitcnt lgkmcnt(1)
mvva 28,136,160
tv 132,226,36864
tv 134,226,38912
mvva 28,140,168
tv 128,226,32768
tv 130,226,34816
mvva 28,152,176
lv 160,252,50,0
mvva 28,156,184
tv 146,226,43008
mvva 24,156,152
L_vs6:
s_cbranch_execz L_cw1
tv 148,226,45056
tv 150,226,47104
mvva 24,152,144
mvva 24,140,136
lv 168,252,50,2048
mvva 24,136,128
mvva 20,136,164
mvva 20,140,172
mvva 20,152,180
lv 176,250,50,0
mvva 20,156,188
mvva 16,156,156
mvva 16,152,148
mvva 16,140,140
lv 184,250,50,2048
mvva 16,136,132
tv 152,227,40960
s_waitcnt lgkmcnt(1)
mvva 32,128,132
tv 140,227,36864
tv 142,227,38912
mvva 32,132,140
tv 136,227,32768
tv 138,227,34816
mvva 32,144,148
lv 216,250,50,3072
mvva 32,148,156
tv 154,227,43008
mvva 36,148,188
tv 156,227,45056
tv 158,227,47104
mvva 36,144,180
mvva 36,132,172
lv 208,250,50,1024
mvva 36,128,164
mvva 40,128,128
mvva 40,132,136
mvva 40,144,144
lv 200,252,50,3072
mvva 40,148,152
mvva 44,148,184
mvva 44,144,176
mvva 44,132,168
lv 192,252,50,1024
mvva 44,128,160
tv 144,224,57344
s_waitcnt lgkmcnt(1)
mvva 60,136,160
tv 132,224,53248
tv 134,224,55296
mvva 60,140,168
tv 128,224,49152
tv 130,224,51200
mvva 60,152,176
dl 245,36,51,0
mvva 60,156,184
tv 146,224,59392
mvva 56,156,152
tv 148,224,61440
tv 150,224,63488
mvva 56,152,144
mvva 56,140,136
dl 246,36,51,1024
mvva 56,136,128
mvva 52,136,164
mvva 52,140,172
mvva 52,152,180
dl 249,36,51,2048
mvva 52,156,188
mvva 48,156,156
mvva 48,152,148
mvva 48,140,140
dl 247,36,51,3072
s_lshl_b32 s51,s98,5
s_xor_b32 s51,s64,s51
s_add_u32 m0,s52,65536
s_xor_b32 s51,s51,4980736
mvva 48,136,132
tv 152,225,57344
s_waitcnt vmcnt(18)
v_cmp_ne_u32_e32 vcc,4,v253
s_waitcnt lgkmcnt(1)
mvva 64,128,132
s_cbranch_vccnz L_spa8e
L_spra8e:
tv 140,225,53248
tv 142,225,55296
mvva 64,132,140
s_xor_b32 s50,s67,s98
tv 136,225,49152
s_xor_b32 s50,s50,147456
tv 138,225,51200
mvva 64,144,148
dl 245,36,51,0
mvva 64,148,156
tv 154,225,59392
mvva 68,148,188
tv 156,225,61440
tv 158,225,63488
mvva 68,144,180
mvva 68,132,172
la 196,252,50,0
mvva 68,128,164
mvva 72,128,128
mvva 72,132,136
mvva 72,144,144
la 204,252,50,2048
s_xor_b32 s53,s60,s98
s_xor_b32 s53,s53,25088
mvva 72,148,152
mvva 76,148,184
mvva 76,144,176
mvva 76,132,168
la 212,250,50,0
mvva 76,128,160
tv 144,226,57344
s_waitcnt lgkmcnt(1)
mvva 92,136,160
tv 132,226,53248
tv 134,226,55296
mvva 92,140,168
tv 128,226,49152
tv 130,226,51200
mvva 92,152,176
buffer_load_dword v253,v242,s[8:11],s53 offen sc1
mvva 92,156,184
tv 146,226,59392
mvva 88,156,152
tv 148,226,61440
tv 150,226,63488
mvva 88,152,144
mvva 88,140,136
la 220,250,50,2048
mvva 88,136,128
mvva 84,136,164
mvva 84,140,172
mvva 84,152,180
la 252,250,50,3072
mvva 84,156,188
mvva 80,156,156
mvva 80,152,148
mvva 80,140,140
la 244,250,50,1024
mvva 80,136,132
tv 152,227,57344
s_waitcnt lgkmcnt(1)
mvva 96,128,132
tv 140,227,53248
tv 142,227,55296
mvva 96,132,140
tv 136,227,49152
tv 138,227,51200
mvva 96,144,148
la 236,252,50,3072
mvva 96,148,156
tv 154,227,59392
mvva 100,148,188
tv 156,227,61440
tv 158,227,63488
mvva 100,144,180
mvva 100,132,172
la 228,252,50,1024
s_add_u32 s50,s50,s55
mvva 100,128,164
mvva 104,128,128
mvva 104,132,136
mvva 104,144,144
la 192,252,50,0
mvva 104,148,152
mvva 108,148,184
mvva 108,144,176
mvva 108,132,168
la 200,252,50,2048
mvva 108,128,160
s_barrier
s_waitcnt lgkmcnt(0)
mvva 124,136,160
tv 132,232,4096
tv 134,232,6144
tv 144,232,8192
mvva 124,140,168
tv 128,232,0
tv 130,232,2048
mvva 124,152,176
la 208,250,50,0
mvva 124,156,184
tv 146,232,10240
mvva 120,156,152
tv 148,232,12288
tv 150,232,14336
mvva 120,152,144
mvva 120,140,136
la 216,250,50,2048
mvva 120,136,128
mvva 116,136,164
mvva 116,140,172
mvva 116,152,180
la 248,250,50,3072
mvva 116,156,188
mvva 112,156,156
mvva 112,152,148
mvva 112,140,140
la 240,250,50,1024
mvva 112,136,132
tv 152,233,8192
s_waitcnt lgkmcnt(1)
mvva 0,128,4
tv 140,233,4096
tv 142,233,6144
mvva 0,132,12
tv 136,233,0
tv 138,233,2048
mvva 0,144,20
la 232,252,50,3072
mvva 0,148,28
tv 154,233,10240
mvva 4,148,60
tv 156,233,12288
tv 158,233,14336
mvva 4,144,52
mvva 4,132,44
la 224,252,50,1024
mvva 4,128,36
mvva 8,128,0
mvva 8,132,8
mvva 8,144,16
dl 246,36,51,1024
mvva 8,148,24
mvva 12,148,56
mvva 12,144,48
mvva 12,132,40
dl 249,36,51,2048
mvva 12,128,32
tv 144,234,8192
s_waitcnt lgkmcnt(1)
mvva 28,136,32
tv 132,234,4096
tv 134,234,6144
mvva 28,140,40
tv 128,234,0
tv 130,234,2048
mvva 28,152,48
dl 247,36,51,3072
s_lshl_b32 s51,s98,5
s_xor_b32 s51,s64,s51
s_add_u32 m0,s52,81920
s_xor_b32 s51,s51,6029312
mvva 28,156,56
tv 146,234,10240
mvva 24,156,24
tv 148,234,12288
tv 150,234,14336
mvva 24,152,16
mvva 24,140,8
dl 245,36,51,0
mvva 24,136,0
mvva 20,136,36
mvva 20,140,44
s_waitcnt vmcnt(17)
v_cmp_ne_u32_e32 vcc,4,v253
mvva 20,152,52
dl 246,36,51,1024
s_cbranch_vccnz L_spa9e
L_spra9e:
mvva 20,156,60
s_xor_b32 s50,s67,s98
mvva 16,156,28
s_xor_b32 s50,s50,155648
mvva 16,152,20
mvva 16,140,12
dl 249,36,51,2048
mvva 16,136,4
tv 152,235,8192
s_waitcnt lgkmcnt(1)
mvva 32,128,4
tv 140,235,4096
tv 142,235,6144
mvva 32,132,12
tv 136,235,0
tv 138,235,2048
mvva 32,144,20
dl 247,36,51,3072
mvva 32,148,28
s_lshl_b32 s51,s98,5
tv 154,235,10240
s_xor_b32 s51,s65,s51
mvva 36,148,60
s_xor_b32 s53,s60,s98
tv 156,235,12288
tv 158,235,14336
s_xor_b32 s53,s53,25216
mvva 36,144,52
s_add_u32 m0,s52,32768
s_xor_b32 s51,s51,4980736
mvva 36,132,44
la 68,252,50,0
mvva 36,128,36
mvva 40,128,0
mvva 40,132,8
mvva 40,144,16
buffer_load_dword v253,v242,s[8:11],s53 offen sc1
mvva 40,148,24
mvva 44,148,56
mvva 44,144,48
mvva 44,132,40
la 76,252,50,2048
mvva 44,128,32
tv 144,232,24576
s_waitcnt lgkmcnt(1)
mvva 60,136,32
tv 132,232,20480
tv 134,232,22528
mvva 60,140,40
tv 128,232,16384
tv 130,232,18432
mvva 60,152,48
la 84,250,50,0
mvva 60,156,56
tv 146,232,26624
mvva 56,156,24
tv 148,232,28672
tv 150,232,30720
mvva 56,152,16
mvva 56,140,8
la 92,250,50,2048
mvva 56,136,0
mvva 52,136,36
mvva 52,140,44
mvva 52,152,52
la 124,250,50,3072
mvva 52,156,60
mvva 48,156,28
mvva 48,152,20
mvva 48,140,12
la 116,250,50,1024
mvva 48,136,4
tv 152,233,24576
s_waitcnt lgkmcnt(1)
mvva 64,128,4
tv 140,233,20480
tv 142,233,22528
mvva 64,132,12
tv 136,233,16384
tv 138,233,18432
mvva 64,144,20
la 108,252,50,3072
mvva 64,148,28
tv 154,233,26624
mvva 68,148,60
tv 156,233,28672
tv 158,233,30720
mvva 68,144,52
mvva 68,132,44
la 100,252,50,1024
s_add_u32 s50,s50,s55
mvva 68,128,36
mvva 72,128,0
mvva 72,132,8
mvva 72,144,16
la 64,252,50,0
mvva 72,148,24
mvva 76,148,56
mvva 76,144,48
mvva 76,132,40
la 72,252,50,2048
mvva 76,128,32
tv 144,224,8192
s_waitcnt lgkmcnt(1)
mvva 92,136,32
tv 132,224,4096
tv 134,224,6144
mvva 92,140,40
tv 128,224,0
tv 130,224,2048
mvva 92,152,48
la 80,250,50,0
mvva 92,156,56
tv 146,224,10240
mvva 88,156,24
tv 148,224,12288
tv 150,224,14336
mvva 88,152,16
mvva 88,140,8
la 88,250,50,2048
mvva 88,136,0
mvva 84,136,36
mvva 84,140,44
mvva 84,152,52
la 120,250,50,3072
mvva 84,156,60
mvva 80,156,28
mvva 80,152,20
mvva 80,140,12
la 112,250,50,1024
mvva 80,136,4
tv 152,225,8192
s_waitcnt lgkmcnt(1)
mvvv 0,128,164
tv 140,225,4096
tv 142,225,6144
mvvv 0,132,172
tv 136,225,0
tv 138,225,2048
mvvv 0,144,180
la 104,252,50,3072
mvvv 0,148,188
tv 154,225,10240
mvvv 4,148,220
tv 156,225,12288
tv 158,225,14336
mvvv 4,144,212
mvvv 4,132,204
la 96,252,50,1024
mvvv 4,128,196
mvvv 8,128,160
mvvv 8,132,168
mvvv 8,144,176
dl 245,36,51,0
mvvv 8,148,184
mvvv 12,148,216
mvvv 12,144,208
mvvv 12,132,200
dl 246,36,51,1024
mvvv 12,128,192
s_barrier
tv 144,226,8192
s_waitcnt lgkmcnt(1)
mvvv 28,136,192
tv 132,226,4096
tv 134,226,6144
mvvv 28,140,200
tv 128,226,0
tv 130,226,2048
mvvv 28,152,208
dl 249,36,51,2048
mvvv 28,156,216
tv 146,226,10240
s_waitcnt vmcnt(18)
v_cmp_ne_u32_e32 vcc,4,v253
mvvv 24,156,184
s_cbranch_vccnz L_spa10e
L_spra10e:
tv 148,226,12288
tv 150,226,14336
mvvv 24,152,176
s_xor_b32 s50,s67,s98
v_and_b32_e32 v242,48,v244
s_xor_b32 s50,s50,188416
mvvv 24,140,168
dl 247,36,51,3072
mvvv 24,136,160
s_lshl_b32 s51,s98,5
v_lshlrev_b32_e32 v242,3,v242
s_xor_b32 s51,s65,s51
mvvv 20,136,196
s_add_u32 m0,s52,49152
s_xor_b32 s51,s51,6029312
mvvv 20,140,204
mvvv 20,152,212
la 132,252,50,0
mvvv 20,156,220
mvvv 16,156,188
s_andn2_b32 s53,s61,384
mvvv 16,152,180
mvvv 16,140,172
la 140,252,50,2048
mvvv 16,136,164
tv 152,227,8192
s_waitcnt lgkmcnt(1)
mvvv 32,128,164
tv 140,227,4096
tv 142,227,6144
mvvv 32,132,172
tv 136,227,0
tv 138,227,2048
mvvv 32,144,180
buffer_load_dword v253,v242,s[8:11],s53 offen sc1
mvvv 32,148,188
tv 154,227,10240
mvvv 36,148,220
tv 156,227,12288
tv 158,227,14336
mvvv 36,144,212
mvvv 36,132,204
la 148,250,50,0
mvvv 36,128,196
mvvv 40,128,160
mvvv 40,132,168
mvvv 40,144,176
la 156,250,50,2048
mvvv 40,148,184
mvvv 44,148,216
mvvv 44,144,208
mvvv 44,132,200
la 188,250,50,3072
mvvv 44,128,192
tv 144,224,24576
s_waitcnt lgkmcnt(1)
mvvv 60,136,192
tv 132,224,20480
tv 134,224,22528
)"
R"(mvvv 60,140,200
tv 128,224,16384
tv 130,224,18432
mvvv 60,152,208
la 180,250,50,1024
mvvv 60,156,216
tv 146,224,26624
mvvv 56,156,184
tv 148,224,28672
tv 150,224,30720
mvvv 56,152,176
mvvv 56,140,168
la 172,252,50,3072
mvvv 56,136,160
mvvv 52,136,196
mvvv 52,140,204
mvvv 52,152,212
la 164,252,50,1024
s_add_u32 s50,s50,s55
mvvv 52,156,220
mvvv 48,156,188
mvvv 48,152,180
mvvv 48,140,172
la 128,252,50,0
mvvv 48,136,164
tv 152,225,24576
s_waitcnt lgkmcnt(1)
mvvv 64,128,164
tv 140,225,20480
tv 142,225,22528
mvvv 64,132,172
tv 136,225,16384
tv 138,225,18432
mvvv 64,144,180
la 136,252,50,2048
mvvv 64,148,188
L_vs7:
s_cbranch_execz L_cw2
tv 154,225,26624
mvvv 68,148,220
tv 156,225,28672
tv 158,225,30720
mvvv 68,144,212
mvvv 68,132,204
la 144,250,50,0
mvvv 68,128,196
mvvv 72,128,160
mvvv 72,132,168
mvvv 72,144,176
la 152,250,50,2048
mvvv 72,148,184
mvvv 76,148,216
mvvv 76,144,208
mvvv 76,132,200
la 184,250,50,3072
mvvv 76,128,192
tv 144,228,40960
s_waitcnt lgkmcnt(1)
mvvv 92,136,192
tv 132,228,36864
tv 134,228,38912
mvvv 92,140,200
tv 128,228,32768
tv 130,228,34816
mvvv 92,152,208
la 176,250,50,1024
mvvv 92,156,216
tv 146,228,43008
mvvv 88,156,184
tv 148,228,45056
tv 150,228,47104
mvvv 88,152,176
mvvv 88,140,168
la 168,252,50,3072
mvvv 88,136,160
mvvv 84,136,196
mvvv 84,140,204
mvvv 84,152,212
la 160,252,50,1024
mvvv 84,156,220
mvvv 80,156,188
mvvv 80,152,180
mvvv 80,140,172
dl 245,36,51,0
mvvv 80,136,164
tv 152,229,40960
s_waitcnt lgkmcnt(1)
mvva 0,128,196
tv 140,229,36864
tv 142,229,38912
mvva 0,132,204
tv 136,229,32768
tv 138,229,34816
mvva 0,144,212
dl 246,36,51,1024
mvva 0,148,220
tv 154,229,43008
mvva 4,148,252
tv 156,229,45056
tv 158,229,47104
mvva 4,144,244
mvva 4,132,236
dl 249,36,51,2048
mvva 4,128,228
mvva 8,128,192
mvva 8,132,200
mvva 8,144,208
dl 247,36,51,3072
mvva 8,148,216
s_waitcnt vmcnt(18)
v_cmp_ne_u32_e32 vcc,4,v253
mvva 12,148,248
s_cbranch_vccnz L_sppe
L_sprpe:
mvva 12,144,240
mvva 12,132,232
tv 148,230,45056
mvva 12,128,224
tv 144,230,40960
s_waitcnt lgkmcnt(2)
mvva 28,136,224
tv 132,230,36864
mvva 28,140,232
tv 128,230,32768
tv 130,230,34816
mvva 28,152,240
tv 134,230,38912
mvva 28,156,248
tv 146,230,43008
mvva 24,156,216
tv 150,230,47104
mvva 24,152,208
mvva 24,140,200
mvva 24,136,192
mvva 20,136,228
mvva 20,140,236
mvva 20,152,244
mvva 20,156,252
mvva 16,156,220
mvva 16,152,212
mvva 16,140,204
tv 156,231,45056
mvva 16,136,196
tv 152,231,40960
s_waitcnt lgkmcnt(2)
mvva 32,128,196
tv 140,231,36864
mvva 32,132,204
tv 136,231,32768
tv 138,231,34816
mvva 32,144,212
tv 142,231,38912
mvva 32,148,220
tv 154,231,43008
mvva 36,148,252
tv 158,231,47104
mvva 36,144,244
mvva 36,132,236
mvva 36,128,228
mvva 40,128,192
mvva 40,132,200
mvva 40,144,208
mvva 40,148,216
mvva 44,148,248
mvva 44,144,240
mvva 44,132,232
mvva 44,128,224
s_barrier
s_waitcnt lgkmcnt(0)
mvva 60,136,224
tv 132,228,53248
tv 144,228,57344
mvva 60,140,232
tv 128,228,49152
tv 130,228,51200
mvva 60,152,240
tv 134,228,55296
mvva 60,156,248
tv 146,228,59392
mvva 56,156,216
tv 148,228,61440
tv 150,228,63488
mvva 56,152,208
mvva 56,140,200
mvva 56,136,192
mvva 52,136,228
mvva 52,140,236
mvva 52,152,244
mvva 52,156,252
mvva 48,156,220
mvva 48,152,212
mvva 48,140,204
tv 156,229,61440
mvva 48,136,196
tv 152,229,57344
s_waitcnt lgkmcnt(2)
mvva 64,128,196
tv 140,229,53248
mvva 64,132,204
tv 136,229,49152
tv 138,229,51200
mvva 64,144,212
tv 142,229,55296
mvva 64,148,220
tv 154,229,59392
mvva 68,148,252
tv 158,229,63488
mvva 68,144,244
mvva 68,132,236
mvva 68,128,228
mvva 72,128,192
mvva 72,132,200
mvva 72,144,208
mvva 72,148,216
mvva 76,148,248
mvva 76,144,240
mvva 76,132,232
tv 148,228,12288
mvva 76,128,224
tv 144,228,8192
s_waitcnt lgkmcnt(2)
mvva 92,136,224
tv 132,228,4096
mvva 92,140,232
tv 128,228,0
tv 130,228,2048
mvva 92,152,240
tv 134,228,6144
mvva 92,156,248
tv 146,228,10240
mvva 88,156,216
tv 150,228,14336
mvva 88,152,208
mvva 88,140,200
mvva 88,136,192
mvva 84,136,228
mvva 84,140,236
mvva 84,152,244
mvva 84,156,252
mvva 80,156,220
mvva 80,152,212
mvva 80,140,204
tv 156,229,12288
mvva 80,136,196
tv 152,229,8192
s_waitcnt lgkmcnt(2)
mvva 0,128,68
tv 140,229,4096
mvva 0,132,76
tv 136,229,0
tv 138,229,2048
mvva 0,144,84
tv 142,229,6144
mvva 0,148,92
tv 154,229,10240
mvva 4,148,124
tv 158,229,14336
mvva 4,144,116
mvva 4,132,108
mvva 4,128,100
mvva 8,128,64
mvva 8,132,72
mvva 8,144,80
mvva 8,148,88
mvva 12,148,120
mvva 12,144,112
mvva 12,132,104
tv 148,228,28672
mvva 12,128,96
tv 144,228,24576
s_waitcnt lgkmcnt(2)
mvva 28,136,96
tv 132,228,20480
mvva 28,140,104
tv 128,228,16384
tv 130,228,18432
mvva 28,152,112
tv 134,228,22528
mvva 28,156,120
tv 146,228,26624
mvva 24,156,88
tv 150,228,30720
mvva 24,152,80
mvva 24,140,72
mvva 24,136,64
mvva 20,136,100
mvva 20,140,108
mvva 20,152,116
mvva 20,156,124
mvva 16,156,92
mvva 16,152,84
mvva 16,140,76
tv 156,229,28672
mvva 16,136,68
tv 152,229,24576
s_waitcnt lgkmcnt(2)
mvva 0,128,132
tv 140,229,20480
mvva 0,132,140
tv 136,229,16384
tv 138,229,18432
s_waitcnt vmcnt(4)
mvva 0,144,148
tv 142,229,22528
mvva 0,148,156
tv 154,229,26624
mvva 4,148,188
tv 158,229,30720
mvva 4,144,180
mvva 4,132,172
mvva 4,128,164
mvva 8,128,128
mvva 8,132,136
cv 0,0,1
cv 1,2,3
mvva 8,144,144
mvva 8,148,152
mvva 12,148,184
mvva 12,144,176
cv 4,4,5
cv 5,6,7
mvva 12,132,168
tv 148,230,12288
mvva 12,128,160
tv 144,230,8192
cv 8,8,9
s_waitcnt lgkmcnt(2)
mvva 28,136,160
tv 132,230,4096
cv 9,10,11
mvva 28,140,168
tv 128,230,0
tv 130,230,2048
mvva 28,152,176
tv 134,230,6144
cv 12,12,13
mvva 28,156,184
tv 146,230,10240
cv 13,14,15
mvva 24,156,152
tv 150,230,14336
mvva 24,152,144
mvva 24,140,136
mvva 24,136,128
mvva 20,136,164
cv 14,28,29
cv 15,30,31
mvva 20,140,172
buffer_store_dwordx4 v[12:15],v244,s[8:11],s56 offen offset:3072 sc0 sc1
mvva 20,152,180
mvva 20,156,188
mvva 16,156,156
cv 10,24,25
cv 11,26,27
mvva 16,152,148
buffer_store_dwordx4 v[8:11],v244,s[8:11],s56 offen offset:2048 sc0 sc1
mvva 16,140,140
tv 156,231,12288
mvva 16,136,132
cv 6,20,21
cv 7,22,23
s_waitcnt lgkmcnt(1)
mvva 32,128,68
tv 140,231,4096
mvva 32,132,76
buffer_store_dwordx4 v[4:7],v244,s[8:11],s56 offen offset:1024 sc0 sc1
tv 136,231,0
tv 138,231,2048
mvva 32,144,84
tv 142,231,6144
cv 2,16,17
mvva 32,148,92
tv 152,231,8192
tv 154,231,10240
mvva 36,148,124
tv 158,231,14336
cv 3,18,19
mvva 36,144,116
buffer_store_dwordx4 v[0:3],v244,s[8:11],s56 offen offset:0 sc0 sc1
mvva 36,132,108
mvva 36,128,100
mvva 40,128,64
mvva 40,132,72
mvva 40,144,80
mvva 40,148,88
mvva 44,148,120
mvva 44,144,112
mvva 44,132,104
tv 148,230,28672
mvva 44,128,96
s_waitcnt vmcnt(4)
s_barrier
tv 144,230,24576
s_waitcnt lgkmcnt(2)
mvva 60,136,96
tv 132,230,20480
mvva 60,140,104
tv 128,230,16384
tv 130,230,18432
mvva 60,152,112
tv 134,230,22528
mvva 60,156,120
tv 146,230,26624
mvva 56,156,88
tv 150,230,30720
mvva 56,152,80
mvva 56,140,72
mvva 56,136,64
mvva 52,136,100
mvva 52,140,108
mvva 52,152,116
mvva 52,156,124
mvva 48,156,92
mvva 48,152,84
mvva 48,140,76
tv 156,231,28672
mvva 48,136,68
tv 152,231,24576
s_waitcnt lgkmcnt(2)
mvva 32,128,132
tv 140,231,20480
mvva 32,132,140
tv 136,231,16384
tv 138,231,18432
mvva 32,144,148
tv 142,231,22528
mvva 32,148,156
tv 154,231,26624
mvva 36,148,188
tv 158,231,30720
mvva 36,144,180
mvva 36,132,172
mvva 36,128,164
mvva 40,128,128
mvva 40,132,136
cv 32,32,33
cv 33,34,35
mvva 40,144,144
mvva 40,148,152
mvva 44,148,184
mvva 44,144,176
cv 36,36,37
cv 37,38,39
mvva 44,132,168
tv 148,224,45056
mvva 44,128,160
tv 144,224,40960
cv 40,40,41
s_waitcnt lgkmcnt(2)
mvva 60,136,160
tv 132,224,36864
cv 41,42,43
mvva 60,140,168
tv 128,224,32768
tv 130,224,34816
mvva 60,152,176
tv 134,224,38912
cv 44,44,45
mvva 60,156,184
tv 146,224,43008
cv 45,46,47
mvva 56,156,152
tv 150,224,47104
mvva 56,152,144
mvva 56,140,136
mvva 56,136,128
mvva 52,136,164
cv 46,60,61
cv 47,62,63
mvva 52,140,172
buffer_store_dwordx4 v[44:47],v244,s[8:11],s57 offen offset:3072 sc0 sc1
mvva 52,152,180
mvva 52,156,188
mvva 48,156,156
cv 42,56,57
cv 43,58,59
mvva 48,152,148
buffer_store_dwordx4 v[40:43],v244,s[8:11],s57 offen offset:2048 sc0 sc1
mvva 48,140,140
tv 156,225,45056
mvva 48,136,132
cv 38,52,53
cv 39,54,55
s_waitcnt lgkmcnt(1)
mvva 64,128,68
tv 140,225,36864
mvva 64,132,76
buffer_store_dwordx4 v[36:39],v244,s[8:11],s57 offen offset:1024 sc0 sc1
tv 136,225,32768
tv 138,225,34816
mvva 64,144,84
tv 142,225,38912
cv 34,48,49
mvva 64,148,92
tv 152,225,40960
tv 154,225,43008
mvva 68,148,124
tv 158,225,47104
cv 35,50,51
mvva 68,144,116
buffer_store_dwordx4 v[32:35],v244,s[8:11],s57 offen offset:0 sc0 sc1
mvva 68,132,108
mvva 68,128,100
mvva 72,128,64
mvva 72,132,72
mvva 72,144,80
mvva 72,148,88
mvva 76,148,120
mvva 76,144,112
mvva 76,132,104
tv 148,224,61440
mvva 76,128,96
tv 144,224,57344
s_waitcnt lgkmcnt(2)
mvva 92,136,96
tv 132,224,53248
mvva 92,140,104
tv 128,224,49152
tv 130,224,51200
mvva 92,152,112
tv 134,224,55296
mvva 92,156,120
tv 146,224,59392
mvva 88,156,88
tv 150,224,63488
mvva 88,152,80
mvva 88,140,72
mvva 88,136,64
mvva 84,136,100
mvva 84,140,108
mvva 84,152,116
mvva 84,156,124
mvva 80,156,92
mvva 80,152,84
mvva 80,140,76
tv 156,225,61440
mvva 80,136,68
tv 152,225,57344
s_waitcnt lgkmcnt(2)
mvva 64,128,132
tv 140,225,53248
mvva 64,132,140
tv 136,225,49152
tv 138,225,51200
mvva 64,144,148
tv 142,225,55296
mvva 64,148,156
tv 154,225,59392
mvva 68,148,188
tv 158,225,63488
mvva 68,144,180
mvva 68,132,172
mvva 68,128,164
s_xor_b32 s82,s59,12
mvva 72,128,128
s_mul_i32 s82,s82,12288
cv 64,64,65
s_add_u32 s82,s82,0
mvva 72,132,136
lv 16,244,82,0
mvva 72,144,144
cv 65,66,67
cv 68,68,69
mvva 72,148,152
L_vs8:
s_cbranch_execz L_cw3
cv 69,70,71
mvva 76,148,184
mvva 76,144,176
lv 20,244,82,1024
mvva 76,132,168
tv 148,234,28672
tv 150,234,30720
mvva 76,128,160
cv 72,72,73
cv 73,74,75
s_waitcnt lgkmcnt(2)
mvva 92,136,160
tv 132,234,20480
mvva 92,140,168
lv 24,244,82,2048
tv 128,234,16384
tv 130,234,18432
mvva 92,152,176
tv 134,234,22528
cv 76,76,77
mvva 92,156,184
tv 144,234,24576
tv 146,234,26624
mvva 88,156,152
cv 77,78,79
mvva 88,152,144
lv 28,244,82,3072
mvva 88,140,136
mvva 88,136,128
cv 78,92,93
cv 79,94,95
mvva 84,136,164
mvva 84,140,172
buffer_store_dwordx4 v[76:79],v244,s[8:11],s58 offen offset:3072 sc0 sc1
mvva 84,152,180
mvva 84,156,188
mvva 80,156,156
cv 74,88,89
cv 75,90,91
mvva 80,152,148
buffer_store_dwordx4 v[72:75],v244,s[8:11],s58 offen offset:2048 sc0 sc1
mvva 80,140,140
tv 156,235,28672
mvva 80,136,132
cv 70,84,85
cv 71,86,87
s_waitcnt lgkmcnt(1)
mvva 96,128,4
tv 140,235,20480
mvva 96,132,12
buffer_store_dwordx4 v[68:71],v244,s[8:11],s58 offen offset:1024 sc0 sc1
tv 136,235,16384
tv 138,235,18432
mvva 96,144,20
tv 142,235,22528
cv 66,80,81
mvva 96,148,28
tv 152,235,24576
tv 154,235,26624
mvva 100,148,60
tv 158,235,30720
cv 67,82,83
mvva 100,144,52
buffer_store_dwordx4 v[64:67],v244,s[8:11],s58 offen offset:0 sc0 sc1
mvva 100,132,44
mvva 100,128,36
mvva 104,128,0
mvva 104,132,8
mvva 104,144,16
mvva 104,148,24
mvva 108,148,56
mvva 108,144,48
mvva 108,132,40
tv 148,226,28672
mvva 108,128,32
tv 144,226,24576
s_waitcnt lgkmcnt(2)
mvva 124,136,32
tv 132,226,20480
mvva 124,140,40
tv 128,226,16384
tv 130,226,18432
mvva 124,152,48
tv 134,226,22528
mvva 124,156,56
tv 146,226,26624
mvva 120,156,24
tv 150,226,30720
mvva 120,152,16
mvva 120,140,8
mvva 120,136,0
mvva 116,136,36
mvva 116,140,44
s_waitcnt vmcnt(7)
v_max_u32_e32 v248,v16,v17
mvva 116,152,52
v_max3_u32 v248,v248,v18,v19
s_waitcnt vmcnt(6)
v_max3_u32 v248,v248,v20,v21
v_max3_u32 v248,v248,v22,v23
mvva 116,156,60
s_waitcnt vmcnt(5)
v_max3_u32 v248,v248,v24,v25
v_max3_u32 v248,v248,v26,v27
s_waitcnt vmcnt(4)
v_max3_u32 v248,v248,v28,v29
mvva 112,156,28
v_max3_u32 v248,v248,v30,v31
v_cmp_eq_u32_e32 vcc,-1,v248
mvva 112,152,20
s_cbranch_vccnz L_spx0e
L_sprx0e:
mvva 112,140,12
tv 156,227,28672
tv 158,227,30720
v_lshlrev_b32_e32 v251,16,v18
v_and_b32_e32 v255,4294901760,v18
mvva 112,136,4
tv 152,227,24576
tv 154,227,26624
s_waitcnt lgkmcnt(4)
mvvv 96,128,164
v_lshlrev_b32_e32 v253,16,v19
v_and_b32_e32 v242,4294901760,v19
mvvv 96,132,172
tv 136,227,16384
tv 138,227,18432
mvvv 96,144,180
tv 140,227,20480
tv 142,227,22528
v_add_f32_e32 v112,v112,v251
mvvv 96,148,188
v_add_f32_e32 v113,v113,v255
v_add_f32_e32 v114,v114,v253
mvvv 100,148,220
v_add_f32_e32 v115,v115,v242
v_lshlrev_b32_e32 v251,16,v22
mvvv 100,144,212
v_and_b32_e32 v255,4294901760,v22
v_lshlrev_b32_e32 v253,16,v23
mvvv 100,132,204
v_and_b32_e32 v242,4294901760,v23
v_add_f32_e32 v116,v116,v251
v_add_f32_e32 v117,v117,v255
mvvv 100,128,196
v_add_f32_e32 v118,v118,v253
v_add_f32_e32 v119,v119,v242
mvvv 104,128,160
v_lshlrev_b32_e32 v251,16,v26
v_and_b32_e32 v255,4294901760,v26
mvvv 104,132,168
v_lshlrev_b32_e32 v253,16,v27
v_and_b32_e32 v242,4294901760,v27
mvvv 104,144,176
v_add_f32_e32 v120,v120,v251
v_add_f32_e32 v121,v121,v255
v_add_f32_e32 v122,v122,v253
mvvv 104,148,184
v_add_f32_e32 v123,v123,v242
v_lshlrev_b32_e32 v251,16,v30
mvvv 108,148,216
v_and_b32_e32 v255,4294901760,v30
v_lshlrev_b32_e32 v253,16,v31
mvvv 108,144,208
v_and_b32_e32 v242,4294901760,v31
v_add_f32_e32 v124,v124,v251
mvvv 108,132,200
v_add_f32_e32 v125,v125,v255
v_add_f32_e32 v126,v126,v253
v_add_f32_e32 v127,v127,v242
mvvv 108,128,192
tv 144,230,57344
v_lshlrev_b32_e32 v251,16,v16
s_waitcnt lgkmcnt(1)
mvvv 124,136,192
tv 132,230,53248
v_and_b32_e32 v255,4294901760,v16
mvvv 124,140,200
tv 128,230,49152
tv 130,230,51200
mvvv 124,152,208
tv 134,230,55296
v_lshlrev_b32_e32 v253,16,v17
mvvv 124,156,216
tv 146,230,59392
v_and_b32_e32 v242,4294901760,v17
mvvv 120,156,184
tv 148,230,61440
tv 150,230,63488
mvvv 120,152,176
v_add_f32_e32 v96,v96,v251
v_add_f32_e32 v97,v97,v255
mvvv 120,140,168
v_add_f32_e32 v98,v98,v253
v_add_f32_e32 v99,v99,v242
mvvv 120,136,160
v_lshlrev_b32_e32 v251,16,v20
v_and_b32_e32 v255,4294901760,v20
mvvv 116,136,196
v_lshlrev_b32_e32 v253,16,v21
v_and_b32_e32 v242,4294901760,v21
mvvv 116,140,204
v_add_f32_e32 v100,v100,v251
v_add_f32_e32 v101,v101,v255
mvvv 116,152,212
v_add_f32_e32 v102,v102,v253
v_add_f32_e32 v103,v103,v242
mvvv 116,156,220
v_lshlrev_b32_e32 v251,16,v24
v_and_b32_e32 v255,4294901760,v24
mvvv 112,156,188
v_lshlrev_b32_e32 v253,16,v25
v_and_b32_e32 v242,4294901760,v25
mvvv 112,152,180
v_add_f32_e32 v104,v104,v251
v_add_f32_e32 v105,v105,v255
mvvv 112,140,172
tv 156,231,61440
v_add_f32_e32 v106,v106,v253
mvvv 112,136,164
tv 152,231,57344
v_add_f32_e32 v107,v107,v242
s_waitcnt lgkmcnt(2)
mvva 96,128,196
tv 140,231,53248
v_lshlrev_b32_e32 v251,16,v28
mvva 96,132,204
tv 136,231,49152
tv 138,231,51200
mvva 96,144,212
tv 142,231,55296
v_and_b32_e32 v255,4294901760,v28
mvva 96,148,220
tv 154,231,59392
v_lshlrev_b32_e32 v253,16,v29
mvva 100,148,252
tv 158,231,63488
v_and_b32_e32 v242,4294901760,v29
mvva 100,144,244
v_add_f32_e32 v108,v108,v251
v_add_f32_e32 v109,v109,v255
mvva 100,132,236
v_add_f32_e32 v110,v110,v253
v_add_f32_e32 v111,v111,v242
mvva 100,128,228
mvva 104,128,192
mvva 104,132,200
mvva 104,144,208
mvva 104,148,216
mvva 108,148,248
mvva 108,144,240
mvva 108,132,232
tv 148,226,45056
mvva 108,128,224
tv 144,226,40960
s_waitcnt lgkmcnt(2)
mvva 124,136,224
s_xor_b32 s83,s59,8
tv 132,226,36864
s_mul_i32 s83,s83,12288
mvva 124,140,232
s_add_u32 s83,s83,4096
tv 128,226,32768
tv 130,226,34816
mvva 124,152,240
lv 48,244,83,0
tv 134,226,38912
mvva 124,156,248
tv 146,226,43008
mvva 120,156,216
lv 52,244,83,1024
tv 150,226,47104
mvva 120,152,208
mvva 120,140,200
lv 56,244,83,2048
mvva 120,136,192
mvva 116,136,228
lv 60,244,83,3072
mvva 116,140,236
mvva 116,152,244
s_xor_b32 s84,s59,4
mvva 116,156,252
s_mul_i32 s84,s84,12288
mvva 112,156,220
s_add_u32 s84,s84,8192
mvva 112,152,212
lv 80,244,84,0
mvva 112,140,204
tv 156,227,45056
mvva 112,136,196
lv 84,244,84,1024
tv 152,227,40960
s_waitcnt lgkmcnt(2)
mvva 96,128,68
tv 140,227,36864
mvva 96,132,76
lv 88,244,84,2048
tv 136,227,32768
tv 138,227,34816
mvva 96,144,84
tv 142,227,38912
mvva 96,148,92
lv 92,244,84,3072
tv 154,227,43008
mvva 100,148,124
tv 158,227,47104
mvva 100,144,116
mvva 100,132,108
mvva 100,128,100
mvva 104,128,64
mvva 104,132,72
mvva 104,144,80
mvva 104,148,88
mvva 108,148,120
mvva 108,144,112
mvva 108,132,104
tv 148,226,61440
mvva 108,128,96
tv 144,226,57344
s_waitcnt lgkmcnt(2)
mvva 124,136,96
tv 132,226,53248
mvva 124,140,104
tv 128,226,49152
tv 130,226,51200
mvva 124,152,112
tv 134,226,55296
mvva 124,156,120
tv 146,226,59392
mvva 120,156,88
tv 150,226,63488
mvva 120,152,80
mvva 120,140,72
mvva 120,136,64
mvva 116,136,100
mvva 116,140,108
mvva 116,152,116
mvva 116,156,124
mvva 112,156,92
mvva 112,152,84
mvva 112,140,76
tv 156,227,61440
mvva 112,136,68
tv 152,227,57344
s_waitcnt lgkmcnt(2)
mvva 96,128,132
tv 140,227,53248
mvva 96,132,140
tv 136,227,49152
tv 138,227,51200
mvva 96,144,148
tv 142,227,55296
mvva 96,148,156
tv 154,227,59392
mvva 100,148,188
tv 158,227,63488
mvva 100,144,180
mvva 100,132,172
mvva 100,128,164
mvva 104,128,128
mvva 104,132,136
mvva 104,144,144
mvva 104,148,152
s_waitcnt vmcnt(7)
v_max_u32_e32 v248,v48,v49
mvva 108,148,184
v_max3_u32 v248,v248,v50,v51
s_waitcnt vmcnt(6)
v_max3_u32 v248,v248,v52,v53
mvva 108,144,176
v_max3_u32 v248,v248,v54,v55
s_waitcnt vmcnt(5)
v_max3_u32 v248,v248,v56,v57
mvva 108,132,168
v_max3_u32 v248,v248,v58,v59
s_waitcnt vmcnt(4)
v_max3_u32 v248,v248,v60,v61
mvva 108,128,160
v_max3_u32 v248,v248,v62,v63
v_cmp_eq_u32_e32 vcc,-1,v248
s_waitcnt lgkmcnt(0)
mvva 124,136,160
s_cbranch_vccnz L_spx1e
L_sprx1e:
mvva 124,140,168
mvva 124,152,176
mvva 124,156,184
s_waitcnt vmcnt(3)
v_max_u32_e32 v248,v80,v81
mvva 120,156,152
v_max3_u32 v248,v248,v82,v83
s_waitcnt vmcnt(2)
v_max3_u32 v248,v248,v84,v85
mvva 120,152,144
v_max3_u32 v248,v248,v86,v87
s_waitcnt vmcnt(1)
v_max3_u32 v248,v248,v88,v89
mvva 120,140,136
v_max3_u32 v248,v248,v90,v91
s_waitcnt vmcnt(0)
v_max3_u32 v248,v248,v92,v93
mvva 120,136,128
v_max3_u32 v248,v248,v94,v95
v_cmp_eq_u32_e32 vcc,-1,v248
mvva 116,136,164
s_cbranch_vccnz L_spx2e
L_sprx2e:
mvva 116,140,172
mvva 116,152,180
mvva 116,156,188
mvva 112,156,156
mvva 112,152,148
mvva 112,140,140
mvva 112,136,132
mvvv 108,238,60
mvvv 108,238,92
mvvv 124,236,60
mvvv 124,236,92
mvvv 104,238,56
mvvv 104,238,88
mvvv 120,236,56
mvvv 120,236,88
mvvv 100,238,52
mvvv 100,238,84
cv 108,108,109
cv 109,110,111
cv 110,124,125
cv 111,126,127
cv 104,104,105
v_permlane16_swap_b32_e32 v108,v110
mvvv 116,236,52
v_permlane16_swap_b32_e32 v109,v111
cv 105,106,107
cv 106,120,121
)"
R"(cv 107,122,123
mvvv 116,236,84
buffer_store_dwordx4 v[108:111],v243,s[44:47],s73 offen
v_permlane16_swap_b32_e32 v104,v106
v_permlane16_swap_b32_e32 v105,v107
mvvv 96,238,48
cv 100,100,101
cv 101,102,103
mvvv 96,238,80
buffer_store_dwordx4 v[104:107],v243,s[44:47],s72 offen
cv 102,116,117
cv 103,118,119
mvvv 112,236,48
v_permlane16_swap_b32_e32 v100,v102
v_permlane16_swap_b32_e32 v101,v103
mvvv 112,236,80
buffer_store_dwordx4 v[100:103],v243,s[44:47],s71 offen
cv 96,96,97
cv 97,98,99
s_nop 4
cv 98,112,113
cv 99,114,115
s_nop 0
v_permlane16_swap_b32_e32 v96,v98
v_permlane16_swap_b32_e32 v97,v99
buffer_store_dwordx4 v[96:99],v243,s[44:47],s70 offen
s_endpgm
L_odd:
L_vs9:
s_cbranch_execz L_vs13
s_lshl_b32 s86,s21,2
s_sub_u32 s87,s13,s67
s_add_u32 s86,s86,s12
mvaa 64,200,132
s_lshl_b32 s86,s86,8
v_add_u32_e32 v252,s87,v244
s_add_u32 s86,s86,L_odd-L_att_pc+1024
v_lshl_add_u32 v226,v244,3,s86
s_lshl_b32 s86,s12,1
s_bfe_u32 s87,s21,65539
mvaa 64,208,136
s_add_u32 s86,s86,s87
v_min_u32_e32 v226,L_att_end-L_att_pc-4,v226
s_lshl_b32 s86,s86,7
s_add_u32 s86,s86,L_pend2-L_att_pc
v_writelane_b32 v226,s86,2
mvaa 64,216,140
s_mov_b64 exec,7
global_load_dword v253,v226,s[96:97]
s_mov_b64 exec,-1
cvaa 68,196,128,232
s_add_u32 m0,s52,32768
v_and_b32_e32 v224,16,v244
v_lshlrev_b32_e32 v224,5,v224
mvaa 68,204,132
s_xor_b32 s51,s62,0
v_lshrrev_b32_e32 v239,4,v244
v_lshrrev_b32_e32 v236,3,v239
mvaa 68,212,136
s_lshl_b32 s87,s12,13
v_and_b32_e32 v237,112,v244
v_and_b32_e32 v238,48,v239
mvaa 68,220,140
s_xor_b32 s87,s87,8192
ra 64,225,0
v_lshlrev_b32_e32 v238,1,v238
cvaa 96,224,128,232
buffer_load_dword v253,v224,s[8:11],s53 offen sc1
mvaa 96,232,132
s_mov_b32 s84,7
ra 68,225,1024
v_xor_b32_e32 v237,v237,v238
mvaa 96,240,136
s_add_u32 s81,s81,1
ra 72,225,2048
v_lshl_add_u32 v245,v236,11,v237
mvaa 96,248,140
s_mov_b32 s84,5
v_add_u32_e32 v246,15360,v245
v_add_u32_e32 v249,30720,v245
cvaa 100,228,128,232
s_andn2_b32 s69,64,s54
ra 76,225,3072
s_waitcnt vmcnt(9)
v_add_u32_e32 v247,46080,v245
mvaa 100,236,132
dl 245,24,51,0
mvaa 100,244,136
s_add_u32 s88,s14,4096
ra 80,225,4096
cv 8,8,9
mvaa 100,252,140
s_lshl_b32 s35,s19,1
ra 84,225,5120
cv 9,10,11
cvaa 72,192,144,232
s_lshr_b32 s33,40569,s35
ra 88,225,6144
cv 10,12,13
mvaa 72,200,148
s_and_b32 s33,s33,3
ra 92,225,7168
v_add_u32_e32 v250,4096,v252
mvaa 72,208,152
dl 246,24,51,1024
mvaa 72,216,156
s_lshl_b32 s33,s33,13
ra 96,225,8192
v_mov_b32_e32 v251,4
cvaa 76,196,144,232
s_lshr_b32 s34,59294,s35
ra 100,225,9216
s_and_b32 s34,s34,3
mvaa 76,204,148
s_lshl_b32 s34,s34,13
ra 104,225,10240
s_xor_b32 s35,s33,s34
mvaa 76,212,152
s_and_b32 s98,s19,3
ra 108,225,11264
s_lshl_b32 s98,s98,13
mvaa 76,220,156
dl 249,24,51,2048
cvaa 104,224,144,232
s_mov_b32 s74,0x300003
ra 112,225,12288
s_mov_b32 s75,0x30000300
mvaa 104,232,148
s_lshl_b32 s71,s12,15
ra 116,225,13312
s_add_u32 s71,s71,s54
mvaa 104,240,152
s_mov_b64 s[44:45],s[30:31]
ra 120,225,14336
s_mov_b32 s46,4194304
mvaa 104,248,156
s_mov_b32 s47,131072
ex 14
cvaa 108,228,144,232
dl 247,24,51,3072
mvaa 108,236,148
s_add_u32 m0,s52,49152
s_xor_b32 s51,s63,0
ra 124,225,15360
mvaa 108,244,152
s_lshl_b32 s70,s23,11
ex 15
mvaa 108,252,156
s_add_u32 s70,s70,s71
ex 40
cvaa 80,192,160,232
L_vs10:
s_cbranch_execz L_vs14
ex 41
mvaa 80,200,164
dl 245,24,51,0
mvaa 80,208,168
ex 42
mvaa 80,216,172
ex 43
cvaa 84,196,160,232
ex 44
mvaa 84,204,164
ex 45
mvaa 84,212,168
dl 246,24,51,1024
mvaa 84,220,172
ex 46
cvaa 112,224,160,232
ex 47
mvaa 112,232,164
ex 16
mvaa 112,240,168
ex 17
mvaa 112,248,172
dl 249,24,51,2048
cvaa 116,228,160,232
ex 18
mvaa 116,236,164
ex 19
mvaa 116,244,168
ex 20
mvaa 116,252,172
ex 21
cvaa 88,192,176,232
dl 247,24,51,3072
s_waitcnt vmcnt(8)
v_cmp_ne_u32_e32 vcc,4,v253
mvaa 88,200,180
s_cbranch_vccnz L_spbo
L_sprbo:
ex 22
mvaa 88,208,184
s_xor_b32 s50,s66,262144
ex 23
mvaa 88,216,188
ex 48
cvaa 92,196,176,232
ex 49
mvaa 92,204,180
la 128,252,50,0
mvaa 92,212,184
s_waitcnt lgkmcnt(0)
s_barrier
mvaa 92,220,188
s_mov_b32 m0,s72
ex 50
cvaa 120,224,176,232
s_lshl_b32 s72,s22,11
ex 51
mvaa 120,232,180
s_add_u32 s72,s72,s71
ex 52
mvaa 120,240,184
ds 255,8,76,0
mvaa 120,248,188
s_add_u32 s71,s70,131072
ex 53
cvaa 124,228,176,232
ex 54
mvaa 124,236,180
ex 55
mvaa 124,244,184
ex 24
mvaa 124,252,188
ds 255,8,76,1024
maav 0,64,0
s_cmp_lg_u32 s19,7
s_cbranch_scc1 L_j7o
s_nop 1
s_lshl_b32 s86,s12,12
s_add_u32 s86,s86,131072
v_add_u32_e32 v236,s86,v244
ra 160,236,0
ra 164,236,1024
ra 168,236,2048
ra 172,236,3072
ra 176,236,16384
ra 180,236,17408
ra 184,236,18432
ra 188,236,19456
v_add_u32_e32 v224,s94,v224
s_waitcnt lgkmcnt(0)
L_j7o:
ex 25
maav 0,96,4
ex 26
maav 4,68,0
ex 27
maav 4,100,4
cv 11,14,15
cv 12,40,41
maav 8,72,0
ds 255,8,76,2048
maav 8,104,4
cv 13,42,43
cv 14,44,45
maav 12,76,0
cv 15,46,47
cv 16,16,17
maav 12,108,4
ex 28
v_smfmac_f32_16x16x64_bf16 v[144:147],v[228:231],v[0:7],v248
cv 17,18,19
maav 16,80,0
ds 255,8,76,3072
maav 16,112,4
s_mov_b32 m0,s73
ex 29
maav 20,84,0
s_add_u32 s73,s72,131072
ex 30
maav 20,116,4
ex 31
maav 24,88,0
ex 56
maav 24,120,4
ds 255,8,77,0
maav 28,92,0
ex 57
maav 28,124,4
ex 58
maav 32,64,8
ex 59
maav 32,96,12
ex 60
maav 36,68,8
ds 255,8,77,1024
maav 36,100,12
cv 18,20,21
cv 19,22,23
maav 40,72,8
cv 20,48,49
cv 21,50,51
maav 40,104,12
cv 22,52,53
cv 23,54,55
maav 44,76,8
ex 61
maav 44,108,12
ds 255,8,77,2048
v_smfmac_f32_16x16x64_bf16 v[148:151],v[228:231],v[8:15],v248
cv 24,24,25
maav 48,80,8
ex 62
maav 48,112,12
ex 63
maav 52,84,8
ra 192,244,0
cv 25,26,27
maav 52,116,12
ds 255,8,77,3072
maav 56,88,8
cv 26,28,29
cv 27,30,31
maav 56,120,12
cv 28,56,57
cv 29,58,59
maav 60,92,8
cv 30,60,61
cv 31,62,63
maav 60,124,12
ex 64
mvav 160,64,16
la 132,252,50,2048
mvav 160,96,20
ex 65
mvav 164,68,16
ex 66
mvav 164,100,20
ex 67
mvav 168,72,16
ex 68
mvav 168,104,20
la 136,250,50,0
mvav 172,76,16
ex 69
mvav 172,108,20
ex 70
v_smfmac_f32_16x16x64_bf16 v[152:155],v[228:231],v[16:23],v248
ra 200,244,2048
mvav 176,80,16
ra 208,244,4096
cv 64,64,65
mvav 176,112,20
la 140,250,50,2048
mvav 180,84,16
ra 216,244,6144
cv 65,66,67
mvav 180,116,20
ex 71
mvav 184,88,16
ex 96
mvav 184,120,20
s_barrier
ex 97
mvav 188,92,16
la 156,250,50,3072
mvav 188,124,20
s_add_u32 m0,s52,131072
ra 196,244,1024
s_xor_b32 s51,s62,1048576
mvav 192,64,24
ra 204,244,3072
cv 66,68,69
mvav 192,96,28
ra 212,244,5120
cv 67,70,71
mvav 196,68,24
ra 220,244,7168
cv 68,96,97
mvav 196,100,28
la 152,250,50,1024
mvav 200,72,24
ex 98
mvav 200,104,28
ex 99
mvav 204,76,24
ra 224,244,8192
cv 69,98,99
mvav 204,108,28
ra 232,244,10240
v_add_u32_e32 v6,s87,v244
v_smfmac_f32_16x16x64_bf16 v[156:159],v[228:231],v[24:31],v248
ra 240,244,12288
mvav 208,80,24
la 148,252,50,3072
mvav 208,112,28
s_xor_b32 s87,s52,4096
ra 248,244,14336
v_add_u32_e32 v7,98304,v6
mvav 212,84,24
s_add_u32 s87,s87,150528
ra 228,244,9216
v_add_u32_e32 v5,65536,v244
mvav 212,116,28
ra 236,244,11264
v_add_u32_e32 v32,s87,v244
mvav 216,88,24
ra 244,244,13312
v_and_b32_e32 v224,960,v244
mvav 216,120,28
la 144,252,50,1024
mvav 220,92,24
s_add_u32 s50,s50,s55
ra 252,244,15360
mvav 220,124,28
ra 64,225,32768
s_waitcnt lgkmcnt(8)
cvaa 24,192,176,232
dl 245,24,51,0
mvaa 24,200,180
ra 96,225,40960
mvaa 24,208,184
ra 68,225,33792
mvaa 24,216,188
dl 246,24,51,1024
cvaa 16,192,160,128
ra 100,225,41984
mvaa 16,200,164
ra 72,225,34816
mvaa 16,208,168
dl 249,24,51,2048
mvaa 16,216,172
ra 104,225,43008
cvaa 28,196,176,232
ra 76,225,35840
mvaa 28,204,180
dl 247,24,51,3072
mvaa 28,212,184
s_add_u32 m0,s52,147456
s_xor_b32 s51,s63,1048576
ra 108,225,44032
mvaa 28,220,188
ra 80,225,36864
cvaa 20,196,160,132
dl 245,24,51,0
mvaa 20,204,164
ra 112,225,45056
mvaa 20,212,168
ra 192,244,16384
mvaa 20,220,172
dl 246,24,51,1024
cvaa 56,224,176,232
ra 200,244,18432
s_waitcnt lgkmcnt(7)
mvaa 56,232,180
ra 208,244,20480
mvaa 56,240,184
dl 249,24,51,2048
mvaa 56,248,188
ra 216,244,22528
cvaa 48,224,160,136
ra 196,244,17408
mvaa 48,232,164
ra 204,244,19456
mvaa 48,240,168
ra 212,244,21504
mvaa 48,248,172
ra 220,244,23552
cvaa 60,228,176,232
ra 224,244,24576
mvaa 60,236,180
ra 232,244,26624
mvaa 60,244,184
ra 240,244,28672
mvaa 60,252,188
ra 248,244,30720
cvaa 52,228,160,140
ra 84,225,37888
mvaa 52,236,164
ra 116,225,46080
mvaa 52,244,168
la 160,252,50,0
ra 228,244,25600
mvaa 52,252,172
ra 236,244,27648
s_waitcnt lgkmcnt(10)
cvaa 8,192,176,128
ra 244,244,29696
mvaa 8,200,180
la 164,252,50,2048
ra 252,244,31744
mvaa 8,208,184
ra 88,225,38912
mvaa 8,216,188
ra 120,225,47104
cvaa 12,196,176,132
la 168,250,50,0
mvaa 12,204,180
ra 92,225,39936
ra 124,225,48128
s_waitcnt lgkmcnt(9)
mvaa 12,212,184
ex 100
mvaa 12,220,188
la 172,250,50,2048
cvaa 40,224,176,136
ex 101
mvaa 40,232,180
ex 102
mvaa 40,240,184
ex 103
mvaa 40,248,188
cv 70,100,101
cv 71,102,103
s_waitcnt lgkmcnt(4)
cvaa 44,228,176,140
ex 72
mvaa 44,236,180
s_waitcnt vmcnt(18) lgkmcnt(0)
s_barrier
ex 73
mvaa 44,244,184
la 176,252,50,1024
mvaa 44,252,188
ex 74
maav 0,64,64
ex 75
maav 0,96,68
la 188,250,50,3072
maav 4,68,64
ra 192,5,0
cv 72,72,73
maav 4,100,68
ra 196,5,1024
cv 73,74,75
maav 8,72,64
la 184,250,50,1024
ds_write_b128 v6,a[0:3] offset:0
maav 8,104,68
ex 76
maav 12,76,64
ex 77
maav 12,108,68
la 180,252,50,3072
ds_write_b128 v6,a[4:7] offset:1024
v_smfmac_f32_16x16x64_bf16 v[144:147],v[228:231],v[64:71],v248
cv 74,76,77
maav 16,80,64
ex 78
maav 16,112,68
ds_write_b128 v6,a[8:11] offset:2048
ex 79
maav 20,84,64
ex 104
maav 20,116,68
ds_write_b128 v6,a[12:15] offset:3072
ex 105
maav 24,88,64
ex 106
ex 107
ex 108
maav 24,120,68
ds_write_b128 v6,a[16:19] offset:4096
cv 75,78,79
maav 28,92,64
ex 109
ex 110
ex 111
maav 28,124,68
cv 76,104,105
cv 77,106,107
cv 78,108,109
cv 79,110,111
maav 32,64,72
ds_write_b128 v6,a[20:23] offset:5120
maav 32,96,76
ex 80
maav 36,68,72
ex 81
maav 36,100,76
ds_write_b128 v6,a[24:27] offset:6144
maav 40,72,72
ex 82
maav 40,104,76
ex 83
maav 44,76,72
ds_write_b128 v6,a[28:31] offset:7168
maav 44,108,76
cv 80,80,81
cv 81,82,83
v_smfmac_f32_16x16x64_bf16 v[148:151],v[228:231],v[72:79],v248
ex 84
maav 48,80,72
ds_write_b128 v7,a[32:35] offset:0
maav 48,112,76
ex 85
ex 86
ex 87
maav 52,84,72
ex 112
ex 113
maav 52,116,76
ds_write_b128 v7,a[36:39] offset:1024
cv 82,84,85
maav 56,88,72
ex 114
ex 115
ex 116
maav 56,120,76
ex 117
ex 118
ex 119
maav 60,92,72
ds_write_b128 v7,a[40:43] offset:2048
cv 83,86,87
maav 60,124,76
cv 84,112,113
cv 85,114,115
cv 86,116,117
cv 87,118,119
mvav 160,64,80
ex 88
mvav 160,96,84
ds_write_b128 v7,a[44:47] offset:3072
mvav 164,68,80
v_add_f32_e32 v130,v144,v145
v_add_f32_e32 v131,v148,v149
mvav 164,100,84
ex 89
mvav 168,72,80
ds_write_b128 v7,a[48:51] offset:4096
mvav 168,104,84
ex 90
mvav 172,76,80
ex 91
mvav 172,108,84
ds_write_b128 v32,v[128:131]
v_smfmac_f32_16x16x64_bf16 v[152:155],v[228:231],v[80:87],v248
ex 92
ex 93
mvav 176,80,80
ds_write_b128 v7,a[52:55] offset:5120
ex 94
mvav 176,112,84
ra 200,5,2048
ra 204,5,3072
ex 95
cv 88,88,89
mvav 180,84,80
ra 208,5,4096
ra 212,5,5120
cv 89,90,91
cv 90,92,93
ex 120
mvav 180,116,84
ds_write_b128 v7,a[56:59] offset:6144
ex 121
mvav 184,88,80
ex 122
ex 123
cv 91,94,95
mvav 184,120,84
ex 124
ex 125
mvav 188,92,80
ds_write_b128 v7,a[60:63] offset:7168
ex 126
mvav 188,124,84
ra 216,5,6144
ra 220,5,7168
ra 224,5,8192
ra 232,5,10240
ex 127
cv 92,120,121
mvav 192,64,88
ra 240,5,12288
ra 248,5,14336
ra 228,5,9216
ra 236,5,11264
ra 244,5,13312
ra 252,5,15360
cv 93,122,123
cv 94,124,125
cv 95,126,127
v_xor_b32_e32 v6,8192,v6
s_nop 0
mvav 192,96,92
ex 16
mvav 196,68,88
s_waitcnt lgkmcnt(10)
s_xor_b32 s87,s87,0
mvav 196,100,92
s_xor_b32 s50,s66,294912
s_barrier
v_xor_b32_e32 v7,8192,v7
mvav 200,72,88
la 4,252,50,0
ra 64,5,16384
ra 96,5,24576
rv 64,6,0
rv 68,6,1024
rv 72,6,2048
mvav 200,104,92
rv 76,6,3072
rv 80,6,4096
rv 84,6,5120
ex 17
ex 18
mvav 204,76,88
rv 96,7,0
rv 100,7,1024
ex 19
mvav 204,108,92
la 12,252,50,2048
rv 104,7,2048
rv 108,7,3072
cv 16,16,17
v_smfmac_f32_16x16x64_bf16 v[156:159],v[228:231],v[88:95],v248
rv 112,7,4096
rv 116,7,5120
ex 20
ex 21
mvav 208,80,88
rv 120,7,6144
rv 124,7,7168
ex 22
ex 23
mvav 208,112,92
la 20,250,50,0
ra 68,5,17408
ra 100,5,25600
cv 17,18,19
cv 18,20,21
mvav 212,84,88
ra 72,5,18432
ra 104,5,26624
ex 48
ex 49
mvav 212,116,92
ra 76,5,19456
ra 108,5,27648
ex 50
ex 51
mvav 216,88,88
la 28,250,50,2048
ra 80,5,20480
ra 112,5,28672
ra 84,5,21504
mvav 216,120,92
L_vs11:
s_cbranch_execz L_vs15
ra 116,5,29696
cv 19,22,23
ex 52
ex 53
mvav 220,92,88
ra 88,5,22528
cv 20,48,49
ex 54
ex 55
mvav 220,124,92
la 60,250,50,3072
ra 120,5,30720
cv 21,50,51
cv 22,52,53
cv 23,54,55
s_bitcmp1_b32 s82,s81
s_cbranch_scc1 L_pend2
L_ret_eb5:
mvav 160,192,16
ra 92,5,23552
rv 88,6,6144
ra 124,5,31744
rv 92,6,7168
ex 24
s_waitcnt lgkmcnt(10)
mvav 160,224,20
ex 25
ex 26
v_smfmac_f32_16x16x64_bf16 v[152:155],v[228:231],v[16:23],v248
ex 27
v_xor_b32_e32 v32,4096,v32
mvav 164,196,16
la 52,250,50,1024
ds_read_b128 v[236:239],v32
cv 24,24,25
ex 28
mvav 164,228,20
ex 29
cv 25,26,27
ex 30
ex 31
mvav 168,200,16
ex 56
ex 57
mvav 168,232,20
la 44,252,50,3072
cv 26,28,29
ex 58
ex 59
mvav 172,204,16
ex 60
ex 61
ex 62
mvav 172,236,20
s_waitcnt lgkmcnt(0)
s_barrier
cv 27,30,31
ex 63
mvav 176,208,16
la 36,252,50,1024
cv 28,56,57
cv 29,58,59
cv 30,60,61
cv 31,62,63
mvav 176,240,20
s_add_u32 m0,s52,147456
ex 8
s_xor_b32 s51,s63,1048576
ex 9
ex 10
mvav 180,212,16
s_add_u32 s50,s50,s55
ex 11
cv 8,8,9
cv 9,10,11
ex 12
mvav 180,244,20
dl 247,24,51,3072
ex 13
ex 14
mvav 184,216,16
s_add_u32 m0,s52,0
ex 15
ex 40
ex 41
mvav 184,248,20
s_xor_b32 s51,s64,0
ex 42
ex 43
ex 44
mvav 188,220,16
la 0,252,50,0
ex 45
ex 46
mvav 188,252,20
ex 47
cv 10,12,13
cv 11,14,15
cv 12,40,41
cv 13,42,43
cv 14,44,45
v_smfmac_f32_16x16x64_bf16 v[156:159],v[228:231],v[24:31],v248
cv 15,46,47
v_bfe_u32 v225,v244,1,5
s_nop 0
v_smfmac_f32_16x16x64_bf16 v[156:159],v[228:231],v[8:15],v248
v_lshl_add_u32 v224,v224,1,v225
v_bfe_u32 v225,v244,7,2
v_lshl_add_u32 v224,v225,5,v224
mvav 192,192,24
la 8,252,50,2048
v_xor_b32_e32 v224,s69,v224
v_add_f32_e32 v152,v152,v153
v_add_f32_e32 v137,v152,v238
v_subrev_u32_e32 v236,0x23800000,v137
mvav 192,224,28
v_rcp_f32_e32 v137,v137
mvav 192,64,8
tv 128,224,32768
v_mul_f32_e32 v232,v240,v137
v_mul_f32_e32 v64,v232,v64
v_mul_f32_e32 v65,v232,v65
v_mul_f32_e32 v66,v232,v66
mvav 192,96,12
la 16,250,50,0
v_mul_f32_e32 v67,v232,v67
v_mul_f32_e32 v68,v232,v68
v_mul_f32_e32 v69,v232,v69
v_mul_f32_e32 v70,v232,v70
mvav 196,196,24
v_fmac_f32_e32 v64,v232,v160
v_fmac_f32_e32 v65,v232,v161
v_mul_f32_e32 v71,v232,v71
mvav 196,228,28
v_fmac_f32_e32 v66,v232,v162
v_fmac_f32_e32 v67,v232,v163
v_fmac_f32_e32 v68,v232,v164
v_fmac_f32_e32 v69,v232,v165
v_fmac_f32_e32 v70,v232,v166
mvav 196,68,8
la 24,250,50,2048
v_fmac_f32_e32 v71,v232,v167
cv 160,64,65
cv 161,66,67
cv 162,68,69
cv 163,70,71
mvav 196,100,12
tv 130,224,34816
tv 132,224,36864
mvav 200,200,24
tv 134,224,38912
tv 144,224,40960
v_mul_f32_e32 v72,v232,v72
v_mul_f32_e32 v73,v232,v73
v_mul_f32_e32 v74,v232,v74
mvav 200,232,28
buffer_store_dwordx4 v[160:163],v244,s[8:11],s14 offen offset:0 sc0 sc1
v_mul_f32_e32 v75,v232,v75
v_mul_f32_e32 v76,v232,v76
v_mul_f32_e32 v77,v232,v77
v_mul_f32_e32 v78,v232,v78
mvav 200,72,8
v_fmac_f32_e32 v72,v232,v168
v_fmac_f32_e32 v73,v232,v169
v_mul_f32_e32 v79,v232,v79
mvav 200,104,12
v_fmac_f32_e32 v74,v232,v170
v_fmac_f32_e32 v75,v232,v171
v_fmac_f32_e32 v76,v232,v172
v_fmac_f32_e32 v77,v232,v173
v_fmac_f32_e32 v78,v232,v174
mvav 204,204,24
la 56,250,50,3072
v_fmac_f32_e32 v79,v232,v175
cv 168,72,73
cv 169,74,75
cv 170,76,77
cv 171,78,79
mvav 204,236,28
tv 146,224,43008
tv 148,224,45056
mvav 204,76,8
tv 150,224,47104
v_mul_f32_e32 v80,v232,v80
v_mul_f32_e32 v81,v232,v81
v_mul_f32_e32 v82,v232,v82
mvav 204,108,12
buffer_store_dwordx4 v[168:171],v244,s[8:11],s14 offen offset:2048 sc0 sc1
v_mul_f32_e32 v83,v232,v83
v_mul_f32_e32 v84,v232,v84
v_mul_f32_e32 v85,v232,v85
v_mul_f32_e32 v86,v232,v86
v_mul_f32_e32 v87,v232,v87
mvav 208,208,24
v_fmac_f32_e32 v80,v232,v176
v_fmac_f32_e32 v81,v232,v177
v_fmac_f32_e32 v82,v232,v178
mvav 208,240,28
v_fmac_f32_e32 v83,v232,v179
v_fmac_f32_e32 v84,v232,v180
v_fmac_f32_e32 v85,v232,v181
cv 176,80,81
cv 177,82,83
mvav 208,80,8
la 48,250,50,1024
v_fmac_f32_e32 v86,v232,v182
v_fmac_f32_e32 v87,v232,v183
cv 178,84,85
cv 179,86,87
mvav 208,112,12
v_mul_f32_e32 v88,v232,v88
v_mul_f32_e32 v89,v232,v89
v_mul_f32_e32 v90,v232,v90
mvav 212,212,24
v_mul_f32_e32 v91,v232,v91
v_mul_f32_e32 v92,v232,v92
v_mul_f32_e32 v93,v232,v93
v_mul_f32_e32 v94,v232,v94
v_mul_f32_e32 v95,v232,v95
mvav 212,244,28
buffer_store_dwordx4 v[176:179],v244,s[8:11],s88 offen offset:0 sc0 sc1
v_fmac_f32_e32 v88,v232,v184
v_fmac_f32_e32 v89,v232,v185
v_fmac_f32_e32 v90,v232,v186
v_fmac_f32_e32 v91,v232,v187
cv 184,88,89
mvav 212,84,8
v_fmac_f32_e32 v92,v232,v188
cv 185,90,91
mvav 212,116,12
v_fmac_f32_e32 v93,v232,v189
v_fmac_f32_e32 v94,v232,v190
v_fmac_f32_e32 v95,v232,v191
cv 186,92,93
cv 187,94,95
mvav 216,216,24
la 40,252,50,3072
v_add_f32_e32 v156,v156,v157
v_add_f32_e32 v141,v156,v239
v_subrev_u32_e32 v237,0x23800000,v141
v_rcp_f32_e32 v141,v141
mvav 216,248,28
v_mul_f32_e32 v233,v241,v141
v_mul_f32_e32 v96,v233,v96
mvav 216,88,8
v_mul_f32_e32 v97,v233,v97
v_mul_f32_e32 v98,v233,v98
v_mul_f32_e32 v99,v233,v99
v_mul_f32_e32 v100,v233,v100
mvav 216,120,12
buffer_store_dwordx4 v[184:187],v244,s[8:11],s88 offen offset:2048 sc0 sc1
v_fmac_f32_e32 v96,v233,v192
v_fmac_f32_e32 v97,v233,v193
v_mul_f32_e32 v101,v233,v101
v_mul_f32_e32 v102,v233,v102
v_mul_f32_e32 v103,v233,v103
mvav 220,220,24
)"
R"(v_fmac_f32_e32 v98,v233,v194
v_fmac_f32_e32 v99,v233,v195
mvav 220,252,28
v_fmac_f32_e32 v100,v233,v196
v_fmac_f32_e32 v101,v233,v197
cv 192,96,97
cv 193,98,99
mvav 220,92,8
la 32,252,50,1024
v_fmac_f32_e32 v102,v233,v198
v_fmac_f32_e32 v103,v233,v199
cv 194,100,101
cv 195,102,103
mvav 220,124,12
v_xor_b32_e32 v225,32,v224
v_xor_b32_e32 v226,64,v224
s_waitcnt lgkmcnt(0)
zvva 0,128,128
tv 138,225,34816
tv 142,225,38912
tv 152,225,40960
s_waitcnt vmcnt(36)
mvva 0,132,132
tv 136,225,32768
tv 154,225,43008
mvva 0,144,136
tv 140,225,36864
tv 158,225,47104
mvva 0,148,140
tv 156,225,45056
v_mul_f32_e32 v104,v233,v104
v_mul_f32_e32 v105,v233,v105
v_mul_f32_e32 v106,v233,v106
zvva 4,148,156
buffer_store_dwordx4 v[192:195],v244,s[8:11],s14 offen offset:1024 sc0 sc1
v_mul_f32_e32 v107,v233,v107
v_mul_f32_e32 v108,v233,v108
v_mul_f32_e32 v109,v233,v109
v_mul_f32_e32 v110,v233,v110
mvva 4,144,152
v_fmac_f32_e32 v104,v233,v200
v_fmac_f32_e32 v105,v233,v201
v_fmac_f32_e32 v106,v233,v202
v_mul_f32_e32 v111,v233,v111
mvva 4,132,148
v_fmac_f32_e32 v107,v233,v203
v_fmac_f32_e32 v108,v233,v204
v_fmac_f32_e32 v109,v233,v205
cv 200,104,105
cv 201,106,107
mvva 4,128,144
v_fmac_f32_e32 v110,v233,v206
v_fmac_f32_e32 v111,v233,v207
cv 202,108,109
cv 203,110,111
s_waitcnt vmcnt(16)
zvva 8,128,160
v_cmp_lt_u32_e64 s[42:43],s48,v236
v_mul_f32_e32 v112,v233,v112
s_or_b64 s[40:41],s[40:41],s[42:43]
v_cmp_lt_u32_e64 s[42:43],s48,v237
mvva 8,132,164
s_or_b64 s[40:41],s[40:41],s[42:43]
v_mul_f32_e32 v113,v233,v113
v_mul_f32_e32 v114,v233,v114
v_mul_f32_e32 v115,v233,v115
mvva 8,144,168
dl 245,36,51,0
v_mul_f32_e32 v116,v233,v116
v_mul_f32_e32 v117,v233,v117
v_mul_f32_e32 v118,v233,v118
v_mul_f32_e32 v119,v233,v119
v_mul_f32_e32 v120,v233,v120
mvva 8,148,172
v_fmac_f32_e32 v112,v233,v208
v_fmac_f32_e32 v113,v233,v209
v_fmac_f32_e32 v114,v233,v210
v_fmac_f32_e32 v115,v233,v211
zvva 12,148,188
v_fmac_f32_e32 v116,v233,v212
v_fmac_f32_e32 v117,v233,v213
cv 208,112,113
cv 209,114,115
mvva 12,144,184
v_fmac_f32_e32 v118,v233,v214
v_fmac_f32_e32 v119,v233,v215
cv 210,116,117
cv 211,118,119
mvva 12,132,180
v_mul_f32_e32 v121,v233,v121
v_mul_f32_e32 v122,v233,v122
v_mul_f32_e32 v123,v233,v123
mvva 12,128,176
v_mul_f32_e32 v124,v233,v124
v_mul_f32_e32 v125,v233,v125
v_mul_f32_e32 v126,v233,v126
v_mul_f32_e32 v127,v233,v127
s_waitcnt lgkmcnt(0)
zvva 28,136,176
buffer_store_dwordx4 v[200:203],v244,s[8:11],s14 offen offset:3072 sc0 sc1
tv 144,226,40960
v_fmac_f32_e32 v120,v233,v216
v_fmac_f32_e32 v121,v233,v217
v_fmac_f32_e32 v122,v233,v218
v_fmac_f32_e32 v123,v233,v219
mvva 28,140,180
tv 128,226,32768
tv 130,226,34816
cv 216,120,121
cv 217,122,123
mvva 28,152,184
tv 132,226,36864
tv 134,226,38912
v_fmac_f32_e32 v124,v233,v220
v_fmac_f32_e32 v125,v233,v221
v_fmac_f32_e32 v126,v233,v222
mvva 28,156,188
tv 146,226,43008
v_fmac_f32_e32 v127,v233,v223
cv 218,124,125
cv 219,126,127
zvva 24,156,172
tv 148,226,45056
tv 150,226,47104
mvva 24,152,168
v_xor_b32_e32 v227,96,v224
v_add_u32_e32 v232,131072,v224
mvva 24,140,164
buffer_store_dwordx4 v[208:211],v244,s[8:11],s88 offen offset:1024 sc0 sc1
mvva 24,136,160
v_add_u32_e32 v233,131072,v225
v_add_u32_e32 v234,131072,v226
zvva 20,136,144
v_add_u32_e32 v235,131072,v227
v_mov_b32_e32 v242,83914752
mvva 20,140,148
v_add_u32_e32 v228,65536,v224
v_add_u32_e32 v229,65536,v225
mvva 20,152,152
v_add_u32_e32 v230,65536,v226
v_add_u32_e32 v231,65536,v227
mvva 20,156,156
v_and_b32_e32 v243,240,v244
v_lshlrev_b32_e32 v243,7,v243
zvva 16,156,140
buffer_store_dwordx4 v[216:219],v244,s[8:11],s88 offen offset:3072 sc0 sc1
s_cmp_lg_u64 s[40:41],0
s_cbranch_scc1 L_redo
L_redo_r5:
mvva 16,152,136
tv 156,227,45056
v_mov_b32_e32 v236,16256
mvva 16,140,132
tv 158,227,47104
v_and_b32_e32 v248,256,v244
mvva 16,136,128
tv 152,227,40960
v_lshrrev_b32_e32 v248,3,v248
s_waitcnt lgkmcnt(3)
zvva 32,128,128
tv 140,227,36864
v_add_u32_e32 v243,v243,v248
mvva 32,132,132
tv 136,227,32768
tv 138,227,34816
mvva 32,144,136
dl 246,36,51,1024
tv 142,227,38912
mvva 32,148,140
tv 154,227,43008
v_and_b32_e32 v248,512,v244
zvva 36,148,156
v_lshrrev_b32_e32 v248,5,v248
v_add_u32_e32 v243,v243,v248
mvva 36,144,152
v_and_b32_e32 v248,16,v244
v_lshlrev_b32_e32 v248,v248,v236
mvva 36,132,148
v_cndmask_b32_e64 v238,0,v248,s[74:75]
s_mov_b32 s74,0xc0000c
mvva 36,128,144
s_mov_b32 s75,0xc0000c00
v_mov_b32_e32 v240,0
v_cndmask_b32_e64 v239,0,v248,s[74:75]
zvva 40,128,160
dl 249,36,51,2048
mvva 40,132,164
v_mov_b32_e32 v241,0
v_mov_b32_e32 v236,0
mvva 40,144,168
v_mov_b32_e32 v237,0
mvva 40,148,172
zvva 44,148,188
mvva 44,144,184
mvva 44,132,180
dl 247,36,51,3072
s_add_u32 m0,s52,16384
s_xor_b32 s51,s65,0
mvva 44,128,176
tv 144,224,57344
s_waitcnt lgkmcnt(1)
zvva 60,136,176
tv 132,224,53248
tv 134,224,55296
mvva 60,140,180
tv 128,224,49152
tv 130,224,51200
mvva 60,152,184
mvva 60,156,188
tv 146,224,59392
zvva 56,156,172
dl 245,36,51,0
tv 148,224,61440
tv 150,224,63488
mvva 56,152,168
mvva 56,140,164
mvva 56,136,160
zvva 52,136,144
mvva 52,140,148
mvva 52,152,152
dl 246,36,51,1024
mvva 52,156,156
zvva 48,156,140
mvva 48,152,136
mvva 48,140,132
tv 156,225,61440
mvva 48,136,128
tv 152,225,57344
s_waitcnt lgkmcnt(2)
zvva 64,128,128
dl 249,36,51,2048
tv 140,225,53248
mvva 64,132,132
tv 136,225,49152
tv 138,225,51200
mvva 64,144,136
tv 142,225,55296
mvva 64,148,140
tv 154,225,59392
zvva 68,148,156
tv 158,225,63488
mvva 68,144,152
mvva 68,132,148
dl 247,36,51,3072
s_lshl_b32 s51,s33,5
s_add_u32 m0,s52,98304
s_xor_b32 s51,s64,s51
mvva 68,128,144
zvva 72,128,160
mvva 72,132,164
s_waitcnt vmcnt(18)
s_mov_b32 s90,0
mvva 72,144,168
s_xor_b32 s50,s67,0
mvva 72,148,172
zvva 76,148,188
dl 245,36,51,0
mvva 76,144,184
mvva 76,132,180
tv 148,226,61440
mvva 76,128,176
tv 144,226,57344
s_waitcnt lgkmcnt(2)
zvva 92,136,176
tv 132,226,53248
mvva 92,140,180
tv 128,226,49152
tv 130,226,51200
mvva 92,152,184
dl 246,36,51,1024
tv 134,226,55296
mvva 92,156,188
tv 146,226,59392
zvva 88,156,172
tv 150,226,63488
mvva 88,152,168
mvva 88,140,164
mvva 88,136,160
zvva 84,136,144
dl 249,36,51,2048
mvva 84,140,148
mvva 84,152,152
mvva 84,156,156
zvva 80,156,140
mvva 80,152,136
mvva 80,140,132
dl 247,36,51,3072
mvva 80,136,128
s_lshl_b32 s51,s33,5
tv 152,227,57344
s_add_u32 m0,s52,114688
s_waitcnt lgkmcnt(1)
zvva 96,128,128
s_xor_b32 s51,s65,s51
tv 140,227,53248
tv 142,227,55296
mvva 96,132,132
tv 136,227,49152
tv 138,227,51200
mvva 96,144,136
mvva 96,148,140
tv 154,227,59392
zvva 100,148,156
lv 164,252,50,0
tv 156,227,61440
tv 158,227,63488
mvva 100,144,152
mvva 100,132,148
mvva 100,128,144
zvva 104,128,160
mvva 104,132,164
mvva 104,144,168
lv 172,252,50,2048
mvva 104,148,172
zvva 108,148,188
mvva 108,144,184
mvva 108,132,180
mvva 108,128,176
s_barrier
s_waitcnt lgkmcnt(0)
zvva 124,136,176
lv 180,250,50,0
tv 132,232,4096
tv 144,232,8192
tv 146,232,10240
mvva 124,140,180
tv 128,232,0
tv 130,232,2048
mvva 124,152,184
tv 134,232,6144
mvva 124,156,188
tv 148,232,12288
tv 150,232,14336
zvva 120,156,172
mvva 120,152,168
mvva 120,140,164
lv 188,250,50,2048
mvva 120,136,160
zvva 116,136,144
mvva 116,140,148
mvva 116,152,152
mvva 116,156,156
zvva 112,156,140
lv 220,250,50,3072
mvva 112,152,136
mvva 112,140,132
tv 156,233,12288
mvva 112,136,128
tv 152,233,8192
s_waitcnt lgkmcnt(2)
mvva 0,128,4
tv 140,233,4096
mvva 0,132,12
tv 136,233,0
tv 138,233,2048
mvva 0,144,20
lv 212,250,50,1024
tv 142,233,6144
mvva 0,148,28
tv 154,233,10240
mvva 4,148,60
tv 158,233,14336
mvva 4,144,52
mvva 4,132,44
mvva 4,128,36
L_vs12:
s_cbranch_execz L_cw3
mvva 8,128,0
lv 204,252,50,3072
mvva 8,132,8
mvva 8,144,16
mvva 8,148,24
mvva 12,148,56
s_waitcnt vmcnt(27)
mvva 12,144,48
s_waitcnt vmcnt(25)
mvva 12,132,40
lv 196,252,50,1024
s_waitcnt vmcnt(24)
mvva 12,128,32
tv 144,234,8192
s_waitcnt lgkmcnt(1)
mvva 28,136,32
tv 132,234,4096
tv 134,234,6144
mvva 28,140,40
tv 128,234,0
tv 130,234,2048
mvva 28,152,48
mvva 28,156,56
tv 146,234,10240
mvva 24,156,24
dl 245,36,51,0
s_waitcnt vmcnt(20)
s_barrier
tv 148,234,12288
tv 150,234,14336
mvva 24,152,16
mvva 24,140,8
mvva 24,136,0
mvva 20,136,36
mvva 20,140,44
mvva 20,152,52
s_cmp_eq_u32 s12,0
s_cselect_b64 exec,1,0
buffer_store_dword v251,off,s[8:11],s61 sc0 sc1
s_mov_b64 exec,-1
mvva 20,156,60
mvva 16,156,28
mvva 16,152,20
mvva 16,140,12
tv 156,235,12288
mvva 16,136,4
tv 152,235,8192
s_waitcnt lgkmcnt(2)
mvva 32,128,4
dl 246,36,51,1024
tv 140,235,4096
mvva 32,132,12
tv 136,235,0
tv 138,235,2048
mvva 32,144,20
tv 142,235,6144
mvva 32,148,28
tv 154,235,10240
mvva 36,148,60
tv 158,235,14336
mvva 36,144,52
mvva 36,132,44
dl 249,36,51,2048
mvva 36,128,36
mvva 40,128,0
mvva 40,132,8
mvva 40,144,16
mvva 40,148,24
mvva 44,148,56
dl 247,36,51,3072
s_lshl_b32 s51,s34,5
s_add_u32 m0,s52,65536
s_xor_b32 s51,s64,s51
mvva 44,144,48
tv 148,232,28672
mvva 44,132,40
s_xor_b32 s53,s60,s33
tv 150,232,30720
mvva 44,128,32
tv 144,232,24576
s_waitcnt lgkmcnt(3)
mvva 60,136,32
tv 132,232,20480
mvva 60,140,40
tv 128,232,16384
tv 130,232,18432
mvva 60,152,48
dl 245,36,51,0
tv 134,232,22528
mvva 60,156,56
tv 146,232,26624
mvva 56,156,24
mvva 56,152,16
mvva 56,140,8
mvva 56,136,0
mvva 52,136,36
buffer_load_dword v253,v242,s[8:11],s53 offen sc1
mvva 52,140,44
mvva 52,152,52
mvva 52,156,60
mvva 48,156,28
mvva 48,152,20
mvva 48,140,12
dl 246,36,51,1024
mvva 48,136,4
tv 152,233,24576
s_waitcnt lgkmcnt(1)
mvva 64,128,4
tv 140,233,20480
tv 142,233,22528
mvva 64,132,12
tv 136,233,16384
tv 138,233,18432
mvva 64,144,20
mvva 64,148,28
tv 154,233,26624
mvva 68,148,60
dl 249,36,51,2048
tv 156,233,28672
tv 158,233,30720
mvva 68,144,52
mvva 68,132,44
mvva 68,128,36
mvva 72,128,0
mvva 72,132,8
mvva 72,144,16
dl 247,36,51,3072
s_lshl_b32 s51,s34,5
s_add_u32 m0,s52,81920
s_xor_b32 s51,s65,s51
mvva 72,148,24
mvva 76,148,56
mvva 76,144,48
mvva 76,132,40
tv 148,234,28672
mvva 76,128,32
tv 144,234,24576
s_waitcnt lgkmcnt(2)
mvva 92,136,32
dl 245,36,51,0
tv 132,234,20480
mvva 92,140,40
tv 128,234,16384
tv 130,234,18432
mvva 92,152,48
tv 134,234,22528
mvva 92,156,56
tv 146,234,26624
mvva 88,156,24
tv 150,234,30720
mvva 88,152,16
mvva 88,140,8
dl 246,36,51,1024
mvva 88,136,0
mvva 84,136,36
mvva 84,140,44
mvva 84,152,52
mvva 84,156,60
mvva 80,156,28
dl 249,36,51,2048
mvva 80,152,20
mvva 80,140,12
tv 156,235,28672
mvva 80,136,4
tv 152,235,24576
s_waitcnt lgkmcnt(2)
mvva 96,128,4
tv 140,235,20480
mvva 96,132,12
tv 136,235,16384
tv 138,235,18432
mvva 96,144,20
dl 247,36,51,3072
s_lshl_b32 s51,s35,5
s_add_u32 m0,s52,32768
s_xor_b32 s51,s64,s51
tv 142,235,22528
mvva 96,148,28
tv 154,235,26624
mvva 100,148,60
tv 158,235,30720
mvva 100,144,52
mvva 100,132,44
mvva 100,128,36
mvva 104,128,0
dl 245,36,51,0
mvva 104,132,8
mvva 104,144,16
mvva 104,148,24
mvva 108,148,56
mvva 108,144,48
mvva 108,132,40
dl 246,36,51,1024
mvva 108,128,32
s_waitcnt vmcnt(27)
s_barrier
s_waitcnt lgkmcnt(0)
mvva 124,136,32
tv 132,224,4096
tv 134,224,6144
tv 144,224,8192
mvva 124,140,40
tv 128,224,0
tv 130,224,2048
mvva 124,152,48
mvva 124,156,56
tv 146,224,10240
mvva 120,156,24
dl 249,36,51,2048
tv 148,224,12288
tv 150,224,14336
mvva 120,152,16
mvva 120,140,8
mvva 120,136,0
mvva 116,136,36
mvva 116,140,44
mvva 116,152,52
dl 247,36,51,3072
s_lshl_b32 s51,s35,5
s_add_u32 m0,s52,49152
s_xor_b32 s51,s65,s51
mvva 116,156,60
mvva 112,156,28
mvva 112,152,20
s_waitcnt vmcnt(11)
v_cmp_ne_u32_e32 vcc,4,v253
mvva 112,140,12
dl 245,36,51,0
s_cbranch_vccnz L_spa3o
L_spra3o:
mvva 112,136,4
s_xor_b32 s50,s67,s33
tv 152,225,8192
s_waitcnt lgkmcnt(1)
mvvv 0,128,164
tv 140,225,4096
tv 142,225,6144
mvvv 0,132,172
tv 136,225,0
tv 138,225,2048
mvvv 0,144,180
dl 246,36,51,1024
mvvv 0,148,188
tv 154,225,10240
mvvv 4,148,220
tv 156,225,12288
tv 158,225,14336
mvvv 4,144,212
mvvv 4,132,204
la 196,252,50,0
mvvv 4,128,196
mvvv 8,128,160
s_xor_b32 s53,s60,s34
mvvv 8,132,168
mvvv 8,144,176
la 204,252,50,2048
mvvv 8,148,184
mvvv 12,148,216
mvvv 12,144,208
mvvv 12,132,200
buffer_load_dword v253,v242,s[8:11],s53 offen sc1
mvvv 12,128,192
tv 144,226,8192
s_waitcnt lgkmcnt(1)
mvvv 28,136,192
tv 132,226,4096
tv 134,226,6144
mvvv 28,140,200
tv 128,226,0
tv 130,226,2048
mvvv 28,152,208
la 212,250,50,0
mvvv 28,156,216
tv 146,226,10240
mvvv 24,156,184
tv 148,226,12288
tv 150,226,14336
mvvv 24,152,176
mvvv 24,140,168
la 220,250,50,2048
mvvv 24,136,160
mvvv 20,136,196
mvvv 20,140,204
mvvv 20,152,212
la 252,250,50,3072
mvvv 20,156,220
mvvv 16,156,188
mvvv 16,152,180
mvvv 16,140,172
la 244,250,50,1024
mvvv 16,136,164
tv 152,227,8192
s_waitcnt lgkmcnt(1)
mvvv 32,128,164
tv 140,227,4096
tv 142,227,6144
mvvv 32,132,172
tv 136,227,0
tv 138,227,2048
mvvv 32,144,180
la 236,252,50,3072
mvvv 32,148,188
tv 154,227,10240
mvvv 36,148,220
tv 156,227,12288
tv 158,227,14336
mvvv 36,144,212
mvvv 36,132,204
la 228,252,50,1024
s_add_u32 s50,s50,s55
mvvv 36,128,196
mvvv 40,128,160
mvvv 40,132,168
mvvv 40,144,176
la 192,252,50,0
mvvv 40,148,184
mvvv 44,148,216
mvvv 44,144,208
mvvv 44,132,200
la 200,252,50,2048
mvvv 44,128,192
tv 144,224,24576
s_waitcnt lgkmcnt(1)
mvvv 60,136,192
tv 132,224,20480
tv 134,224,22528
mvvv 60,140,200
tv 128,224,16384
tv 130,224,18432
mvvv 60,152,208
la 208,250,50,0
mvvv 60,156,216
tv 146,224,26624
mvvv 56,156,184
tv 148,224,28672
tv 150,224,30720
mvvv 56,152,176
mvvv 56,140,168
la 216,250,50,2048
mvvv 56,136,160
mvvv 52,136,196
mvvv 52,140,204
mvvv 52,152,212
la 248,250,50,3072
mvvv 52,156,220
mvvv 48,156,188
mvvv 48,152,180
mvvv 48,140,172
la 240,250,50,1024
mvvv 48,136,164
tv 152,225,24576
s_waitcnt lgkmcnt(1)
mvvv 64,128,164
tv 140,225,20480
tv 142,225,22528
mvvv 64,132,172
tv 136,225,16384
tv 138,225,18432
mvvv 64,144,180
la 232,252,50,3072
mvvv 64,148,188
tv 154,225,26624
mvvv 68,148,220
tv 156,225,28672
tv 158,225,30720
mvvv 68,144,212
mvvv 68,132,204
la 224,252,50,1024
mvvv 68,128,196
mvvv 72,128,160
mvvv 72,132,168
mvvv 72,144,176
dl 249,36,51,2048
mvvv 72,148,184
mvvv 76,148,216
mvvv 76,144,208
mvvv 76,132,200
dl 247,36,51,3072
s_lshl_b32 s51,s98,5
s_xor_b32 s51,s64,s51
s_add_u32 m0,s52,131072
s_xor_b32 s51,s51,4194304
mvvv 76,128,192
tv 144,226,24576
s_waitcnt lgkmcnt(1)
mvvv 92,136,192
tv 132,226,20480
tv 134,226,22528
mvvv 92,140,200
tv 128,226,16384
tv 130,226,18432
mvvv 92,152,208
dl 245,36,51,0
mvvv 92,156,216
tv 146,226,26624
mvvv 88,156,184
tv 148,226,28672
tv 150,226,30720
mvvv 88,152,176
mvvv 88,140,168
dl 246,36,51,1024
mvvv 88,136,160
s_waitcnt vmcnt(18)
v_cmp_ne_u32_e32 vcc,4,v253
mvvv 84,136,196
s_cbranch_vccnz L_spa4o
L_spra4o:
mvvv 84,140,204
s_xor_b32 s50,s67,s34
mvvv 84,152,212
dl 249,36,51,2048
mvvv 84,156,220
mvvv 80,156,188
mvvv 80,152,180
mvvv 80,140,172
la 68,252,50,0
mvvv 80,136,164
tv 152,227,24576
s_waitcnt lgkmcnt(1)
mvvv 96,128,164
tv 140,227,20480
tv 142,227,22528
mvvv 96,132,172
tv 136,227,16384
tv 138,227,18432
mvvv 96,144,180
la 76,252,50,2048
s_xor_b32 s53,s60,s35
mvvv 96,148,188
tv 154,227,26624
mvvv 100,148,220
tv 156,227,28672
tv 158,227,30720
mvvv 100,144,212
mvvv 100,132,204
la 84,250,50,0
mvvv 100,128,196
mvvv 104,128,160
mvvv 104,132,168
mvvv 104,144,176
buffer_load_dword v253,v242,s[8:11],s53 offen sc1
mvvv 104,148,184
mvvv 108,148,216
mvvv 108,144,208
mvvv 108,132,200
la 92,250,50,2048
mvvv 108,128,192
s_barrier
s_waitcnt lgkmcnt(0)
mvvv 124,136,192
tv 132,228,36864
tv 134,228,38912
tv 144,228,40960
mvvv 124,140,200
tv 128,228,32768
tv 130,228,34816
mvvv 124,152,208
la 124,250,50,3072
mvvv 124,156,216
tv 146,228,43008
mvvv 120,156,184
tv 148,228,45056
tv 150,228,47104
mvvv 120,152,176
mvvv 120,140,168
la 116,250,50,1024
mvvv 120,136,160
mvvv 116,136,196
mvvv 116,140,204
mvvv 116,152,212
la 108,252,50,3072
mvvv 116,156,220
mvvv 112,156,188
mvvv 112,152,180
mvvv 112,140,172
la 100,252,50,1024
s_add_u32 s50,s50,s55
mvvv 112,136,164
tv 152,229,40960
s_waitcnt lgkmcnt(1)
mvva 0,128,196
tv 140,229,36864
tv 142,229,38912
mvva 0,132,204
tv 136,229,32768
tv 138,229,34816
s_waitcnt vmcnt(23)
mvva 0,144,212
la 64,252,50,0
mvva 0,148,220
tv 154,229,43008
mvva 4,148,252
tv 156,229,45056
tv 158,229,47104
mvva 4,144,244
mvva 4,132,236
la 72,252,50,2048
s_waitcnt vmcnt(23)
mvva 4,128,228
mvva 8,128,192
s_waitcnt vmcnt(22)
mvva 8,132,200
s_waitcnt vmcnt(21)
mvva 8,144,208
la 80,250,50,0
s_waitcnt vmcnt(21)
mvva 8,148,216
s_waitcnt vmcnt(20)
mvva 12,148,248
s_waitcnt vmcnt(19)
mvva 12,144,240
s_waitcnt vmcnt(18)
mvva 12,132,232
la 88,250,50,2048
s_waitcnt vmcnt(18)
mvva 12,128,224
tv 144,230,40960
s_waitcnt lgkmcnt(1)
mvva 28,136,224
tv 132,230,36864
tv 134,230,38912
mvva 28,140,232
tv 128,230,32768
tv 130,230,34816
mvva 28,152,240
la 120,250,50,3072
mvva 28,156,248
tv 146,230,43008
mvva 24,156,216
tv 148,230,45056
tv 150,230,47104
mvva 24,152,208
mvva 24,140,200
la 112,250,50,1024
mvva 24,136,192
mvva 20,136,228
mvva 20,140,236
mvva 20,152,244
la 104,252,50,3072
mvva 20,156,252
mvva 16,156,220
mvva 16,152,212
mvva 16,140,204
L_vs13:
s_cbranch_execz L_cw0
la 96,252,50,1024
mvva 16,136,196
tv 152,231,40960
s_waitcnt lgkmcnt(1)
mvva 32,128,196
tv 140,231,36864
tv 142,231,38912
mvva 32,132,204
tv 136,231,32768
tv 138,231,34816
mvva 32,144,212
dl 247,36,51,3072
s_lshl_b32 s51,s98,5
s_xor_b32 s51,s65,s51
s_add_u32 m0,s52,147456
s_xor_b32 s51,s51,4194304
mvva 32,148,220
tv 154,231,43008
mvva 36,148,252
tv 156,231,45056
tv 158,231,47104
mvva 36,144,244
mvva 36,132,236
dl 245,36,51,0
mvva 36,128,228
mvva 40,128,192
mvva 40,132,200
mvva 40,144,208
dl 246,36,51,1024
mvva 40,148,216
mvva 44,148,248
mvva 44,144,240
mvva 44,132,232
dl 249,36,51,2048
mvva 44,128,224
tv 144,228,57344
s_waitcnt lgkmcnt(1)
mvva 60,136,224
tv 132,228,53248
tv 134,228,55296
mvva 60,140,232
tv 128,228,49152
tv 130,228,51200
s_waitcnt vmcnt(17)
v_cmp_ne_u32_e32 vcc,4,v253
mvva 60,152,240
dl 247,36,51,3072
s_lshl_b32 s51,s98,5
s_xor_b32 s51,s64,s51
s_add_u32 m0,s52,0
s_xor_b32 s51,s51,4456448
s_cbranch_vccnz L_spa5o
L_spra5o:
mvva 60,156,248
s_xor_b32 s50,s67,s35
tv 146,228,59392
mvva 56,156,216
tv 148,228,61440
tv 150,228,63488
mvva 56,152,208
mvva 56,140,200
dl 245,36,51,0
mvva 56,136,192
mvva 52,136,228
mvva 52,140,236
mvva 52,152,244
la 132,252,50,0
mvva 52,156,252
mvva 48,156,220
s_xor_b32 s53,s60,s98
mvva 48,152,212
s_xor_b32 s53,s53,512
mvva 48,140,204
la 140,252,50,2048
mvva 48,136,196
tv 152,229,57344
s_waitcnt lgkmcnt(1)
mvva 64,128,196
tv 140,229,53248
tv 142,229,55296
mvva 64,132,204
tv 136,229,49152
tv 138,229,51200
mvva 64,144,212
buffer_load_dword v253,v242,s[8:11],s53 offen sc1
mvva 64,148,220
tv 154,229,59392
mvva 68,148,252
)"
R"(tv 156,229,61440
tv 158,229,63488
mvva 68,144,244
mvva 68,132,236
la 148,250,50,0
mvva 68,128,228
mvva 72,128,192
mvva 72,132,200
mvva 72,144,208
la 156,250,50,2048
mvva 72,148,216
mvva 76,148,248
mvva 76,144,240
mvva 76,132,232
la 188,250,50,3072
mvva 76,128,224
tv 144,230,57344
s_waitcnt lgkmcnt(1)
mvva 92,136,224
tv 132,230,53248
tv 134,230,55296
mvva 92,140,232
tv 128,230,49152
tv 130,230,51200
mvva 92,152,240
la 180,250,50,1024
mvva 92,156,248
tv 146,230,59392
mvva 88,156,216
tv 148,230,61440
tv 150,230,63488
mvva 88,152,208
mvva 88,140,200
la 172,252,50,3072
mvva 88,136,192
mvva 84,136,228
mvva 84,140,236
mvva 84,152,244
la 164,252,50,1024
s_add_u32 s50,s50,s55
mvva 84,156,252
mvva 80,156,220
mvva 80,152,212
mvva 80,140,204
la 128,252,50,0
mvva 80,136,196
tv 152,231,57344
s_waitcnt lgkmcnt(1)
mvva 96,128,196
tv 140,231,53248
tv 142,231,55296
mvva 96,132,204
tv 136,231,49152
tv 138,231,51200
mvva 96,144,212
la 136,252,50,2048
mvva 96,148,220
tv 154,231,59392
mvva 100,148,252
tv 156,231,61440
tv 158,231,63488
mvva 100,144,244
mvva 100,132,236
la 144,250,50,0
mvva 100,128,228
mvva 104,128,192
mvva 104,132,200
mvva 104,144,208
la 152,250,50,2048
mvva 104,148,216
mvva 108,148,248
mvva 108,144,240
mvva 108,132,232
la 184,250,50,3072
mvva 108,128,224
s_barrier
s_waitcnt lgkmcnt(0)
mvva 124,136,224
tv 132,228,4096
tv 134,228,6144
tv 144,228,8192
mvva 124,140,232
tv 128,228,0
tv 130,228,2048
mvva 124,152,240
la 176,250,50,1024
mvva 124,156,248
tv 146,228,10240
mvva 120,156,216
tv 148,228,12288
tv 150,228,14336
mvva 120,152,208
mvva 120,140,200
la 168,252,50,3072
mvva 120,136,192
mvva 116,136,228
mvva 116,140,236
mvva 116,152,244
la 160,252,50,1024
mvva 116,156,252
mvva 112,156,220
mvva 112,152,212
mvva 112,140,204
dl 246,36,51,1024
mvva 112,136,196
tv 152,229,8192
s_waitcnt lgkmcnt(1)
mvva 0,128,68
tv 140,229,4096
tv 142,229,6144
mvva 0,132,76
tv 136,229,0
tv 138,229,2048
mvva 0,144,84
dl 249,36,51,2048
s_waitcnt vmcnt(25)
mvva 0,148,92
tv 154,229,10240
mvva 4,148,124
tv 156,229,12288
tv 158,229,14336
mvva 4,144,116
mvva 4,132,108
dl 247,36,51,3072
s_lshl_b32 s51,s98,5
s_xor_b32 s51,s64,s51
s_add_u32 m0,s52,16384
s_xor_b32 s51,s51,4718592
mvva 4,128,100
mvva 8,128,64
mvva 8,132,72
mvva 8,144,80
dl 245,36,51,0
mvva 8,148,88
s_waitcnt vmcnt(18)
v_cmp_ne_u32_e32 vcc,4,v253
mvva 12,148,120
s_cbranch_vccnz L_spa6o
L_spra6o:
mvva 12,144,112
s_xor_b32 s50,s67,s98
mvva 12,132,104
dl 246,36,51,1024
s_xor_b32 s50,s50,131072
mvva 12,128,96
tv 144,230,8192
s_waitcnt lgkmcnt(1)
mvva 28,136,96
tv 132,230,4096
tv 134,230,6144
mvva 28,140,104
tv 128,230,0
tv 130,230,2048
mvva 28,152,112
la 4,252,50,0
mvva 28,156,120
tv 146,230,10240
mvva 24,156,88
tv 148,230,12288
tv 150,230,14336
mvva 24,152,80
mvva 24,140,72
la 12,252,50,2048
s_xor_b32 s53,s60,s98
s_xor_b32 s53,s53,8704
mvva 24,136,64
mvva 20,136,100
mvva 20,140,108
mvva 20,152,116
la 20,250,50,0
mvva 20,156,124
mvva 16,156,92
mvva 16,152,84
mvva 16,140,76
buffer_load_dword v253,v242,s[8:11],s53 offen sc1
mvva 16,136,68
tv 152,231,8192
s_waitcnt lgkmcnt(1)
mvva 32,128,68
tv 140,231,4096
tv 142,231,6144
mvva 32,132,76
tv 136,231,0
tv 138,231,2048
mvva 32,144,84
la 28,250,50,2048
mvva 32,148,92
tv 154,231,10240
mvva 36,148,124
tv 156,231,12288
tv 158,231,14336
mvva 36,144,116
mvva 36,132,108
la 60,250,50,3072
mvva 36,128,100
mvva 40,128,64
mvva 40,132,72
mvva 40,144,80
la 52,250,50,1024
mvva 40,148,88
mvva 44,148,120
mvva 44,144,112
mvva 44,132,104
la 44,252,50,3072
mvva 44,128,96
tv 144,228,24576
s_waitcnt lgkmcnt(1)
mvva 60,136,96
tv 132,228,20480
tv 134,228,22528
mvva 60,140,104
tv 128,228,16384
tv 130,228,18432
mvva 60,152,112
la 36,252,50,1024
s_add_u32 s50,s50,s55
mvva 60,156,120
tv 146,228,26624
mvva 56,156,88
tv 148,228,28672
tv 150,228,30720
mvva 56,152,80
mvva 56,140,72
la 0,252,50,0
mvva 56,136,64
mvva 52,136,100
mvva 52,140,108
mvva 52,152,116
la 8,252,50,2048
mvva 52,156,124
mvva 48,156,92
mvva 48,152,84
mvva 48,140,76
la 16,250,50,0
mvva 48,136,68
tv 152,229,24576
s_waitcnt lgkmcnt(1)
mvva 64,128,68
tv 140,229,20480
tv 142,229,22528
mvva 64,132,76
tv 136,229,16384
tv 138,229,18432
mvva 64,144,84
la 24,250,50,2048
mvva 64,148,92
tv 154,229,26624
mvva 68,148,124
tv 156,229,28672
tv 158,229,30720
mvva 68,144,116
mvva 68,132,108
la 56,250,50,3072
mvva 68,128,100
mvva 72,128,64
mvva 72,132,72
mvva 72,144,80
la 48,250,50,1024
mvva 72,148,88
mvva 76,148,120
mvva 76,144,112
mvva 76,132,104
la 40,252,50,3072
mvva 76,128,96
tv 144,224,40960
s_waitcnt lgkmcnt(1)
mvva 92,136,96
tv 132,224,36864
tv 134,224,38912
mvva 92,140,104
tv 128,224,32768
tv 130,224,34816
mvva 92,152,112
la 32,252,50,1024
mvva 92,156,120
tv 146,224,43008
mvva 88,156,88
tv 148,224,45056
tv 150,224,47104
mvva 88,152,80
mvva 88,140,72
dl 249,36,51,2048
mvva 88,136,64
mvva 84,136,100
mvva 84,140,108
mvva 84,152,116
dl 247,36,51,3072
s_lshl_b32 s51,s98,5
s_xor_b32 s51,s65,s51
s_add_u32 m0,s52,98304
s_xor_b32 s51,s51,4456448
mvva 84,156,124
mvva 80,156,92
mvva 80,152,84
mvva 80,140,76
dl 245,36,51,0
mvva 80,136,68
tv 152,225,40960
s_waitcnt lgkmcnt(1)
mvva 0,128,132
tv 140,225,36864
tv 142,225,38912
mvva 0,132,140
tv 136,225,32768
tv 138,225,34816
s_waitcnt vmcnt(25)
mvva 0,144,148
dl 246,36,51,1024
mvva 0,148,156
tv 154,225,43008
mvva 4,148,188
tv 156,225,45056
tv 158,225,47104
mvva 4,144,180
s_waitcnt vmcnt(17)
v_cmp_ne_u32_e32 vcc,4,v253
mvva 4,132,172
dl 249,36,51,2048
s_cbranch_vccnz L_spa7o
L_spra7o:
mvva 4,128,164
s_xor_b32 s50,s67,s98
mvva 8,128,128
s_xor_b32 s50,s50,139264
mvva 8,132,136
mvva 8,144,144
dl 247,36,51,3072
mvva 8,148,152
mvva 12,148,184
mvva 12,144,176
mvva 12,132,168
lv 164,252,50,0
mvva 12,128,160
s_barrier
tv 144,226,40960
s_lshl_b32 s51,s98,5
s_waitcnt lgkmcnt(1)
mvva 28,136,160
s_xor_b32 s53,s60,s98
tv 132,226,36864
tv 134,226,38912
s_xor_b32 s53,s53,16896
mvva 28,140,168
s_xor_b32 s51,s65,s51
tv 128,226,32768
tv 130,226,34816
mvva 28,152,176
lv 172,252,50,2048
mvva 28,156,184
s_add_u32 m0,s52,114688
tv 146,226,43008
s_xor_b32 s51,s51,4718592
mvva 24,156,152
tv 148,226,45056
tv 150,226,47104
mvva 24,152,144
mvva 24,140,136
buffer_load_dword v253,v242,s[8:11],s53 offen sc1
mvva 24,136,128
mvva 20,136,164
mvva 20,140,172
mvva 20,152,180
lv 180,250,50,0
mvva 20,156,188
mvva 16,156,156
mvva 16,152,148
mvva 16,140,140
lv 188,250,50,2048
mvva 16,136,132
tv 152,227,40960
s_waitcnt lgkmcnt(1)
mvva 32,128,132
tv 140,227,36864
tv 142,227,38912
mvva 32,132,140
tv 136,227,32768
tv 138,227,34816
mvva 32,144,148
lv 220,250,50,3072
mvva 32,148,156
tv 154,227,43008
mvva 36,148,188
tv 156,227,45056
tv 158,227,47104
mvva 36,144,180
mvva 36,132,172
lv 212,250,50,1024
mvva 36,128,164
mvva 40,128,128
mvva 40,132,136
mvva 40,144,144
lv 204,252,50,3072
mvva 40,148,152
mvva 44,148,184
mvva 44,144,176
mvva 44,132,168
lv 196,252,50,1024
s_add_u32 s50,s50,s55
mvva 44,128,160
tv 144,224,57344
s_waitcnt lgkmcnt(1)
mvva 60,136,160
tv 132,224,53248
tv 134,224,55296
mvva 60,140,168
tv 128,224,49152
tv 130,224,51200
mvva 60,152,176
lv 160,252,50,0
mvva 60,156,184
tv 146,224,59392
mvva 56,156,152
tv 148,224,61440
tv 150,224,63488
mvva 56,152,144
mvva 56,140,136
lv 168,252,50,2048
mvva 56,136,128
mvva 52,136,164
mvva 52,140,172
mvva 52,152,180
lv 176,250,50,0
mvva 52,156,188
mvva 48,156,156
mvva 48,152,148
mvva 48,140,140
lv 184,250,50,2048
mvva 48,136,132
tv 152,225,57344
s_waitcnt lgkmcnt(1)
mvva 64,128,132
tv 140,225,53248
tv 142,225,55296
mvva 64,132,140
tv 136,225,49152
tv 138,225,51200
mvva 64,144,148
lv 216,250,50,3072
mvva 64,148,156
tv 154,225,59392
mvva 68,148,188
tv 156,225,61440
tv 158,225,63488
mvva 68,144,180
mvva 68,132,172
lv 208,250,50,1024
mvva 68,128,164
mvva 72,128,128
mvva 72,132,136
mvva 72,144,144
lv 200,252,50,3072
mvva 72,148,152
L_vs14:
s_cbranch_execz L_cw1
mvva 76,148,184
mvva 76,144,176
mvva 76,132,168
lv 192,252,50,1024
mvva 76,128,160
tv 144,232,8192
s_waitcnt lgkmcnt(1)
mvva 92,136,160
tv 132,232,4096
tv 134,232,6144
mvva 92,140,168
tv 128,232,0
tv 130,232,2048
mvva 92,152,176
dl 245,36,51,0
mvva 92,156,184
tv 146,232,10240
mvva 88,156,152
tv 148,232,12288
tv 150,232,14336
mvva 88,152,144
mvva 88,140,136
dl 246,36,51,1024
mvva 88,136,128
mvva 84,136,164
mvva 84,140,172
mvva 84,152,180
dl 249,36,51,2048
mvva 84,156,188
mvva 80,156,156
mvva 80,152,148
mvva 80,140,140
dl 247,36,51,3072
mvva 80,136,132
tv 152,233,8192
s_waitcnt vmcnt(18)
v_cmp_ne_u32_e32 vcc,4,v253
s_waitcnt lgkmcnt(1)
mvva 0,128,4
s_cbranch_vccnz L_spa8o
L_spra8o:
tv 140,233,4096
s_xor_b32 s50,s67,s98
mvva 0,132,12
s_xor_b32 s50,s50,147456
tv 136,233,0
tv 138,233,2048
mvva 0,144,20
la 196,252,50,0
tv 142,233,6144
mvva 0,148,28
tv 154,233,10240
v_and_b32_e32 v242,48,v244
mvva 4,148,60
tv 156,233,12288
tv 158,233,14336
mvva 4,144,52
v_lshlrev_b32_e32 v242,3,v242
mvva 4,132,44
la 204,252,50,2048
mvva 4,128,36
mvva 8,128,0
s_andn2_b32 s53,s61,384
mvva 8,132,8
mvva 8,144,16
buffer_load_dword v253,v242,s[8:11],s53 offen sc1
mvva 8,148,24
mvva 12,148,56
mvva 12,144,48
mvva 12,132,40
la 212,250,50,0
mvva 12,128,32
tv 144,234,8192
s_waitcnt lgkmcnt(1)
mvva 28,136,32
tv 132,234,4096
mvva 28,140,40
tv 128,234,0
tv 130,234,2048
mvva 28,152,48
la 220,250,50,2048
tv 134,234,6144
mvva 28,156,56
tv 146,234,10240
mvva 24,156,24
tv 148,234,12288
tv 150,234,14336
mvva 24,152,16
mvva 24,140,8
la 252,250,50,3072
mvva 24,136,0
mvva 20,136,36
mvva 20,140,44
mvva 20,152,52
la 244,250,50,1024
mvva 20,156,60
mvva 16,156,28
mvva 16,152,20
mvva 16,140,12
la 236,252,50,3072
mvva 16,136,4
tv 152,235,8192
s_waitcnt lgkmcnt(1)
mvva 32,128,4
tv 140,235,4096
mvva 32,132,12
tv 136,235,0
tv 138,235,2048
mvva 32,144,20
la 228,252,50,1024
tv 142,235,6144
mvva 32,148,28
s_add_u32 s50,s50,s55
tv 154,235,10240
mvva 36,148,60
tv 156,235,12288
tv 158,235,14336
mvva 36,144,52
mvva 36,132,44
la 192,252,50,0
mvva 36,128,36
mvva 40,128,0
mvva 40,132,8
mvva 40,144,16
la 200,252,50,2048
mvva 40,148,24
mvva 44,148,56
mvva 44,144,48
mvva 44,132,40
la 208,250,50,0
mvva 44,128,32
s_barrier
s_waitcnt lgkmcnt(0)
mvva 60,136,32
tv 132,232,20480
tv 144,232,24576
mvva 60,140,40
tv 128,232,16384
tv 130,232,18432
mvva 60,152,48
la 216,250,50,2048
tv 134,232,22528
mvva 60,156,56
tv 146,232,26624
mvva 56,156,24
tv 148,232,28672
tv 150,232,30720
mvva 56,152,16
mvva 56,140,8
la 248,250,50,3072
mvva 56,136,0
mvva 52,136,36
mvva 52,140,44
mvva 52,152,52
la 240,250,50,1024
mvva 52,156,60
mvva 48,156,28
mvva 48,152,20
mvva 48,140,12
la 232,252,50,3072
mvva 48,136,4
tv 152,233,24576
s_waitcnt lgkmcnt(1)
mvva 64,128,4
tv 140,233,20480
mvva 64,132,12
tv 136,233,16384
tv 138,233,18432
mvva 64,144,20
la 224,252,50,1024
tv 142,233,22528
mvva 64,148,28
tv 154,233,26624
mvva 68,148,60
tv 156,233,28672
tv 158,233,30720
mvva 68,144,52
mvva 68,132,44
mvva 68,128,36
mvva 72,128,0
mvva 72,132,8
mvva 72,144,16
mvva 72,148,24
mvva 76,148,56
mvva 76,144,48
mvva 76,132,40
tv 148,224,12288
mvva 76,128,32
tv 144,224,8192
s_waitcnt lgkmcnt(2)
mvva 92,136,32
tv 132,224,4096
mvva 92,140,40
tv 128,224,0
tv 130,224,2048
mvva 92,152,48
tv 134,224,6144
mvva 92,156,56
tv 146,224,10240
mvva 88,156,24
tv 150,224,14336
mvva 88,152,16
mvva 88,140,8
mvva 88,136,0
s_waitcnt vmcnt(14)
v_cmp_ne_u32_e32 vcc,4,v253
mvva 84,136,36
s_cbranch_vccnz L_sppo
L_sprpo:
mvva 84,140,44
mvva 84,152,52
mvva 84,156,60
mvva 80,156,28
mvva 80,152,20
mvva 80,140,12
tv 156,225,12288
mvva 80,136,4
tv 152,225,8192
s_waitcnt lgkmcnt(2)
mvvv 0,128,164
tv 140,225,4096
mvvv 0,132,172
tv 136,225,0
tv 138,225,2048
mvvv 0,144,180
tv 142,225,6144
mvvv 0,148,188
tv 154,225,10240
mvvv 4,148,220
tv 158,225,14336
mvvv 4,144,212
mvvv 4,132,204
mvvv 4,128,196
mvvv 8,128,160
mvvv 8,132,168
mvvv 8,144,176
mvvv 8,148,184
mvvv 12,148,216
mvvv 12,144,208
mvvv 12,132,200
tv 148,224,28672
mvvv 12,128,192
tv 144,224,24576
s_waitcnt lgkmcnt(2)
mvvv 28,136,192
tv 132,224,20480
mvvv 28,140,200
tv 128,224,16384
tv 130,224,18432
mvvv 28,152,208
tv 134,224,22528
mvvv 28,156,216
tv 146,224,26624
mvvv 24,156,184
tv 150,224,30720
mvvv 24,152,176
mvvv 24,140,168
mvvv 24,136,160
mvvv 20,136,196
mvvv 20,140,204
mvvv 20,152,212
mvvv 20,156,220
mvvv 16,156,188
mvvv 16,152,180
mvvv 16,140,172
tv 156,225,28672
mvvv 16,136,164
tv 152,225,24576
s_waitcnt lgkmcnt(2)
mvva 0,128,196
tv 140,225,20480
mvva 0,132,204
tv 136,225,16384
tv 138,225,18432
s_waitcnt vmcnt(8)
mvva 0,144,212
tv 142,225,22528
mvva 0,148,220
tv 154,225,26624
mvva 4,148,252
tv 158,225,30720
mvva 4,144,244
mvva 4,132,236
mvva 4,128,228
s_waitcnt vmcnt(6)
mvva 8,128,192
cv 0,0,1
cv 1,2,3
mvva 8,132,200
s_waitcnt vmcnt(5)
mvva 8,144,208
s_waitcnt vmcnt(4)
mvva 8,148,216
cv 4,4,5
cv 5,6,7
s_waitcnt vmcnt(3)
mvva 12,148,248
s_waitcnt vmcnt(2)
mvva 12,144,240
s_waitcnt vmcnt(1)
mvva 12,132,232
tv 148,226,12288
cv 8,8,9
s_waitcnt vmcnt(0)
mvva 12,128,224
tv 144,226,8192
cv 9,10,11
s_waitcnt lgkmcnt(2)
mvva 28,136,224
tv 132,226,4096
mvva 28,140,232
tv 128,226,0
tv 130,226,2048
mvva 28,152,240
tv 134,226,6144
cv 12,12,13
mvva 28,156,248
tv 146,226,10240
cv 13,14,15
mvva 24,156,216
tv 150,226,14336
mvva 24,152,208
mvva 24,140,200
mvva 24,136,192
mvva 20,136,228
cv 14,28,29
cv 15,30,31
mvva 20,140,236
buffer_store_dwordx4 v[12:15],v244,s[8:11],s56 offen offset:3072 sc0 sc1
mvva 20,152,244
mvva 20,156,252
mvva 16,156,220
cv 10,24,25
cv 11,26,27
mvva 16,152,212
buffer_store_dwordx4 v[8:11],v244,s[8:11],s56 offen offset:2048 sc0 sc1
mvva 16,140,204
tv 156,227,12288
mvva 16,136,196
cv 6,20,21
cv 7,22,23
s_waitcnt lgkmcnt(1)
mvvv 32,128,164
tv 140,227,4096
mvvv 32,132,172
buffer_store_dwordx4 v[4:7],v244,s[8:11],s56 offen offset:1024 sc0 sc1
tv 136,227,0
tv 138,227,2048
mvvv 32,144,180
tv 142,227,6144
cv 2,16,17
mvvv 32,148,188
tv 152,227,8192
tv 154,227,10240
mvvv 36,148,220
tv 158,227,14336
cv 3,18,19
mvvv 36,144,212
buffer_store_dwordx4 v[0:3],v244,s[8:11],s56 offen offset:0 sc0 sc1
mvvv 36,132,204
mvvv 36,128,196
mvvv 40,128,160
mvvv 40,132,168
mvvv 40,144,176
mvvv 40,148,184
mvvv 44,148,216
mvvv 44,144,208
mvvv 44,132,200
tv 148,226,28672
mvvv 44,128,192
s_barrier
tv 144,226,24576
s_waitcnt lgkmcnt(2)
mvvv 60,136,192
tv 132,226,20480
mvvv 60,140,200
tv 128,226,16384
tv 130,226,18432
mvvv 60,152,208
tv 134,226,22528
mvvv 60,156,216
tv 146,226,26624
mvvv 56,156,184
tv 150,226,30720
mvvv 56,152,176
mvvv 56,140,168
mvvv 56,136,160
mvvv 52,136,196
mvvv 52,140,204
mvvv 52,152,212
mvvv 52,156,220
mvvv 48,156,188
mvvv 48,152,180
mvvv 48,140,172
tv 156,227,28672
mvvv 48,136,164
tv 152,227,24576
s_waitcnt lgkmcnt(2)
mvva 32,128,196
tv 140,227,20480
mvva 32,132,204
tv 136,227,16384
tv 138,227,18432
mvva 32,144,212
tv 142,227,22528
mvva 32,148,220
tv 154,227,26624
mvva 36,148,252
tv 158,227,30720
mvva 36,144,244
mvva 36,132,236
mvva 36,128,228
mvva 40,128,192
mvva 40,132,200
cv 32,32,33
cv 33,34,35
mvva 40,144,208
mvva 40,148,216
mvva 44,148,248
mvva 44,144,240
cv 36,36,37
cv 37,38,39
mvva 44,132,232
tv 148,228,45056
mvva 44,128,224
tv 144,228,40960
cv 40,40,41
s_waitcnt lgkmcnt(2)
mvva 60,136,224
tv 132,228,36864
cv 41,42,43
mvva 60,140,232
tv 128,228,32768
tv 130,228,34816
mvva 60,152,240
tv 134,228,38912
cv 44,44,45
mvva 60,156,248
tv 146,228,43008
cv 45,46,47
mvva 56,156,216
tv 150,228,47104
mvva 56,152,208
mvva 56,140,200
mvva 56,136,192
mvva 52,136,228
cv 46,60,61
cv 47,62,63
mvva 52,140,236
buffer_store_dwordx4 v[44:47],v244,s[8:11],s57 offen offset:3072 sc0 sc1
mvva 52,152,244
mvva 52,156,252
mvva 48,156,220
cv 42,56,57
cv 43,58,59
mvva 48,152,212
buffer_store_dwordx4 v[40:43],v244,s[8:11],s57 offen offset:2048 sc0 sc1
mvva 48,140,204
tv 156,229,45056
mvva 48,136,196
cv 38,52,53
cv 39,54,55
s_waitcnt lgkmcnt(1)
mvvv 64,128,164
tv 140,229,36864
mvvv 64,132,172
buffer_store_dwordx4 v[36:39],v244,s[8:11],s57 offen offset:1024 sc0 sc1
tv 136,229,32768
tv 138,229,34816
mvvv 64,144,180
tv 142,229,38912
cv 34,48,49
mvvv 64,148,188
tv 152,229,40960
tv 154,229,43008
mvvv 68,148,220
tv 158,229,47104
cv 35,50,51
mvvv 68,144,212
buffer_store_dwordx4 v[32:35],v244,s[8:11],s57 offen offset:0 sc0 sc1
mvvv 68,132,204
mvvv 68,128,196
mvvv 72,128,160
mvvv 72,132,168
mvvv 72,144,176
mvvv 72,148,184
mvvv 76,148,216
mvvv 76,144,208
mvvv 76,132,200
tv 148,228,61440
mvvv 76,128,192
tv 144,228,57344
s_waitcnt lgkmcnt(2)
mvvv 92,136,192
tv 132,228,53248
mvvv 92,140,200
tv 128,228,49152
tv 130,228,51200
mvvv 92,152,208
tv 134,228,55296
mvvv 92,156,216
tv 146,228,59392
mvvv 88,156,184
tv 150,228,63488
mvvv 88,152,176
mvvv 88,140,168
mvvv 88,136,160
mvvv 84,136,196
mvvv 84,140,204
mvvv 84,152,212
mvvv 84,156,220
mvvv 80,156,188
mvvv 80,152,180
mvvv 80,140,172
tv 156,229,61440
mvvv 80,136,164
tv 152,229,57344
s_waitcnt lgkmcnt(2)
mvva 64,128,196
tv 140,229,53248
mvva 64,132,204
tv 136,229,49152
tv 138,229,51200
mvva 64,144,212
tv 142,229,55296
mvva 64,148,220
tv 154,229,59392
mvva 68,148,252
tv 158,229,63488
mvva 68,144,244
mvva 68,132,236
mvva 68,128,228
L_vs15:
s_cbranch_execz L_cw2
s_xor_b32 s82,s59,12
mvva 72,128,192
s_mul_i32 s82,s82,12288
cv 64,64,65
s_add_u32 s82,s82,0
mvva 72,132,200
lv 16,244,82,0
mvva 72,144,208
cv 65,66,67
cv 68,68,69
mvva 72,148,216
cv 69,70,71
)"
R"(mvva 76,148,248
mvva 76,144,240
lv 20,244,82,1024
mvva 76,132,232
tv 148,230,28672
tv 150,230,30720
mvva 76,128,224
cv 72,72,73
cv 73,74,75
s_waitcnt lgkmcnt(2)
mvva 92,136,224
tv 132,230,20480
mvva 92,140,232
lv 24,244,82,2048
tv 128,230,16384
tv 130,230,18432
mvva 92,152,240
tv 134,230,22528
cv 76,76,77
mvva 92,156,248
tv 144,230,24576
tv 146,230,26624
mvva 88,156,216
cv 77,78,79
mvva 88,152,208
lv 28,244,82,3072
mvva 88,140,200
mvva 88,136,192
cv 78,92,93
cv 79,94,95
mvva 84,136,228
mvva 84,140,236
buffer_store_dwordx4 v[76:79],v244,s[8:11],s58 offen offset:3072 sc0 sc1
mvva 84,152,244
mvva 84,156,252
mvva 80,156,220
cv 74,88,89
cv 75,90,91
mvva 80,152,212
buffer_store_dwordx4 v[72:75],v244,s[8:11],s58 offen offset:2048 sc0 sc1
mvva 80,140,204
tv 156,231,28672
mvva 80,136,196
cv 70,84,85
cv 71,86,87
s_waitcnt lgkmcnt(1)
mvva 96,128,68
tv 140,231,20480
mvva 96,132,76
buffer_store_dwordx4 v[68:71],v244,s[8:11],s58 offen offset:1024 sc0 sc1
tv 136,231,16384
tv 138,231,18432
mvva 96,144,84
tv 142,231,22528
cv 66,80,81
mvva 96,148,92
tv 152,231,24576
tv 154,231,26624
mvva 100,148,124
tv 158,231,30720
cv 67,82,83
mvva 100,144,116
buffer_store_dwordx4 v[64:67],v244,s[8:11],s58 offen offset:0 sc0 sc1
mvva 100,132,108
mvva 100,128,100
mvva 104,128,64
mvva 104,132,72
mvva 104,144,80
mvva 104,148,88
mvva 108,148,120
mvva 108,144,112
mvva 108,132,104
tv 148,226,61440
mvva 108,128,96
tv 144,226,57344
s_waitcnt lgkmcnt(2)
mvva 124,136,96
tv 132,226,53248
mvva 124,140,104
tv 128,226,49152
tv 130,226,51200
mvva 124,152,112
tv 134,226,55296
mvva 124,156,120
tv 146,226,59392
mvva 120,156,88
tv 150,226,63488
mvva 120,152,80
mvva 120,140,72
mvva 120,136,64
mvva 116,136,100
mvva 116,140,108
s_waitcnt vmcnt(7)
v_max_u32_e32 v248,v16,v17
mvva 116,152,116
v_max3_u32 v248,v248,v18,v19
s_waitcnt vmcnt(6)
v_max3_u32 v248,v248,v20,v21
v_max3_u32 v248,v248,v22,v23
mvva 116,156,124
s_waitcnt vmcnt(5)
v_max3_u32 v248,v248,v24,v25
v_max3_u32 v248,v248,v26,v27
s_waitcnt vmcnt(4)
v_max3_u32 v248,v248,v28,v29
mvva 112,156,92
v_max3_u32 v248,v248,v30,v31
v_cmp_eq_u32_e32 vcc,-1,v248
mvva 112,152,84
s_cbranch_vccnz L_spx0o
L_sprx0o:
mvva 112,140,76
tv 156,227,61440
tv 158,227,63488
v_lshlrev_b32_e32 v251,16,v18
v_and_b32_e32 v255,4294901760,v18
mvva 112,136,68
tv 152,227,57344
tv 154,227,59392
s_waitcnt lgkmcnt(4)
mvva 96,128,132
v_lshlrev_b32_e32 v253,16,v19
v_and_b32_e32 v242,4294901760,v19
mvva 96,132,140
tv 136,227,49152
tv 138,227,51200
mvva 96,144,148
tv 140,227,53248
tv 142,227,55296
v_add_f32_e32 v112,v112,v251
mvva 96,148,156
v_add_f32_e32 v113,v113,v255
v_add_f32_e32 v114,v114,v253
mvva 100,148,188
v_add_f32_e32 v115,v115,v242
v_lshlrev_b32_e32 v251,16,v22
mvva 100,144,180
v_and_b32_e32 v255,4294901760,v22
v_lshlrev_b32_e32 v253,16,v23
mvva 100,132,172
v_and_b32_e32 v242,4294901760,v23
v_add_f32_e32 v116,v116,v251
v_add_f32_e32 v117,v117,v255
mvva 100,128,164
v_add_f32_e32 v118,v118,v253
v_add_f32_e32 v119,v119,v242
mvva 104,128,128
v_lshlrev_b32_e32 v251,16,v26
v_and_b32_e32 v255,4294901760,v26
mvva 104,132,136
v_lshlrev_b32_e32 v253,16,v27
v_and_b32_e32 v242,4294901760,v27
mvva 104,144,144
v_add_f32_e32 v120,v120,v251
v_add_f32_e32 v121,v121,v255
v_add_f32_e32 v122,v122,v253
mvva 104,148,152
v_add_f32_e32 v123,v123,v242
v_lshlrev_b32_e32 v251,16,v30
mvva 108,148,184
v_and_b32_e32 v255,4294901760,v30
v_lshlrev_b32_e32 v253,16,v31
mvva 108,144,176
v_and_b32_e32 v242,4294901760,v31
v_add_f32_e32 v124,v124,v251
mvva 108,132,168
v_add_f32_e32 v125,v125,v255
v_add_f32_e32 v126,v126,v253
v_add_f32_e32 v127,v127,v242
mvva 108,128,160
tv 144,234,24576
v_lshlrev_b32_e32 v251,16,v16
s_waitcnt lgkmcnt(1)
mvva 124,136,160
tv 132,234,20480
v_and_b32_e32 v255,4294901760,v16
mvva 124,140,168
tv 128,234,16384
tv 130,234,18432
mvva 124,152,176
tv 134,234,22528
v_lshlrev_b32_e32 v253,16,v17
mvva 124,156,184
tv 146,234,26624
v_and_b32_e32 v242,4294901760,v17
mvva 120,156,152
tv 148,234,28672
tv 150,234,30720
mvva 120,152,144
v_add_f32_e32 v96,v96,v251
v_add_f32_e32 v97,v97,v255
mvva 120,140,136
v_add_f32_e32 v98,v98,v253
v_add_f32_e32 v99,v99,v242
mvva 120,136,128
v_lshlrev_b32_e32 v251,16,v20
v_and_b32_e32 v255,4294901760,v20
mvva 116,136,164
v_lshlrev_b32_e32 v253,16,v21
v_and_b32_e32 v242,4294901760,v21
mvva 116,140,172
v_add_f32_e32 v100,v100,v251
v_add_f32_e32 v101,v101,v255
mvva 116,152,180
v_add_f32_e32 v102,v102,v253
v_add_f32_e32 v103,v103,v242
mvva 116,156,188
v_lshlrev_b32_e32 v251,16,v24
v_and_b32_e32 v255,4294901760,v24
mvva 112,156,156
v_lshlrev_b32_e32 v253,16,v25
v_and_b32_e32 v242,4294901760,v25
mvva 112,152,148
v_add_f32_e32 v104,v104,v251
v_add_f32_e32 v105,v105,v255
mvva 112,140,140
tv 156,235,28672
v_add_f32_e32 v106,v106,v253
mvva 112,136,132
tv 152,235,24576
v_add_f32_e32 v107,v107,v242
s_waitcnt lgkmcnt(2)
mvva 96,128,4
tv 140,235,20480
v_lshlrev_b32_e32 v251,16,v28
mvva 96,132,12
tv 136,235,16384
tv 138,235,18432
mvva 96,144,20
tv 142,235,22528
v_and_b32_e32 v255,4294901760,v28
mvva 96,148,28
tv 154,235,26624
v_lshlrev_b32_e32 v253,16,v29
mvva 100,148,60
tv 158,235,30720
v_and_b32_e32 v242,4294901760,v29
mvva 100,144,52
v_add_f32_e32 v108,v108,v251
v_add_f32_e32 v109,v109,v255
mvva 100,132,44
v_add_f32_e32 v110,v110,v253
v_add_f32_e32 v111,v111,v242
mvva 100,128,36
mvva 104,128,0
mvva 104,132,8
mvva 104,144,16
mvva 104,148,24
mvva 108,148,56
mvva 108,144,48
mvva 108,132,40
tv 148,230,45056
mvva 108,128,32
tv 144,230,40960
s_waitcnt lgkmcnt(2)
mvva 124,136,32
s_xor_b32 s83,s59,8
tv 132,230,36864
s_mul_i32 s83,s83,12288
mvva 124,140,40
s_add_u32 s83,s83,4096
tv 128,230,32768
tv 130,230,34816
mvva 124,152,48
lv 48,244,83,0
tv 134,230,38912
mvva 124,156,56
tv 146,230,43008
mvva 120,156,24
lv 52,244,83,1024
tv 150,230,47104
mvva 120,152,16
mvva 120,140,8
lv 56,244,83,2048
mvva 120,136,0
mvva 116,136,36
lv 60,244,83,3072
mvva 116,140,44
mvva 116,152,52
s_xor_b32 s84,s59,4
mvva 116,156,60
s_mul_i32 s84,s84,12288
mvva 112,156,28
s_add_u32 s84,s84,8192
mvva 112,152,20
lv 80,244,84,0
mvva 112,140,12
tv 156,231,45056
mvva 112,136,4
lv 84,244,84,1024
tv 152,231,40960
s_waitcnt lgkmcnt(2)
mvvv 96,128,164
tv 140,231,36864
mvvv 96,132,172
lv 88,244,84,2048
tv 136,231,32768
tv 138,231,34816
mvvv 96,144,180
tv 142,231,38912
mvvv 96,148,188
lv 92,244,84,3072
tv 154,231,43008
mvvv 100,148,220
tv 158,231,47104
mvvv 100,144,212
mvvv 100,132,204
mvvv 100,128,196
mvvv 104,128,160
mvvv 104,132,168
mvvv 104,144,176
mvvv 104,148,184
mvvv 108,148,216
mvvv 108,144,208
mvvv 108,132,200
tv 148,230,61440
mvvv 108,128,192
tv 144,230,57344
s_waitcnt lgkmcnt(2)
mvvv 124,136,192
tv 132,230,53248
mvvv 124,140,200
tv 128,230,49152
tv 130,230,51200
mvvv 124,152,208
tv 134,230,55296
mvvv 124,156,216
tv 146,230,59392
mvvv 120,156,184
tv 150,230,63488
mvvv 120,152,176
mvvv 120,140,168
mvvv 120,136,160
mvvv 116,136,196
mvvv 116,140,204
mvvv 116,152,212
mvvv 116,156,220
mvvv 112,156,188
mvvv 112,152,180
mvvv 112,140,172
tv 156,231,61440
mvvv 112,136,164
tv 152,231,57344
s_waitcnt lgkmcnt(2)
mvva 96,128,196
tv 140,231,53248
mvva 96,132,204
tv 136,231,49152
tv 138,231,51200
mvva 96,144,212
tv 142,231,55296
mvva 96,148,220
tv 154,231,59392
mvva 100,148,252
tv 158,231,63488
mvva 100,144,244
mvva 100,132,236
mvva 100,128,228
mvva 104,128,192
mvva 104,132,200
mvva 104,144,208
mvva 104,148,216
s_waitcnt vmcnt(7)
v_max_u32_e32 v248,v48,v49
mvva 108,148,248
v_max3_u32 v248,v248,v50,v51
s_waitcnt vmcnt(6)
v_max3_u32 v248,v248,v52,v53
mvva 108,144,240
v_max3_u32 v248,v248,v54,v55
s_waitcnt vmcnt(5)
v_max3_u32 v248,v248,v56,v57
mvva 108,132,232
v_max3_u32 v248,v248,v58,v59
s_waitcnt vmcnt(4)
v_max3_u32 v248,v248,v60,v61
mvva 108,128,224
v_max3_u32 v248,v248,v62,v63
v_cmp_eq_u32_e32 vcc,-1,v248
s_waitcnt lgkmcnt(0)
mvva 124,136,224
s_cbranch_vccnz L_spx1o
L_sprx1o:
mvva 124,140,232
mvva 124,152,240
mvva 124,156,248
s_waitcnt vmcnt(3)
v_max_u32_e32 v248,v80,v81
mvva 120,156,216
v_max3_u32 v248,v248,v82,v83
s_waitcnt vmcnt(2)
v_max3_u32 v248,v248,v84,v85
mvva 120,152,208
v_max3_u32 v248,v248,v86,v87
s_waitcnt vmcnt(1)
v_max3_u32 v248,v248,v88,v89
mvva 120,140,200
v_max3_u32 v248,v248,v90,v91
s_waitcnt vmcnt(0)
v_max3_u32 v248,v248,v92,v93
mvva 120,136,192
v_max3_u32 v248,v248,v94,v95
v_cmp_eq_u32_e32 vcc,-1,v248
mvva 116,136,228
s_cbranch_vccnz L_spx2o
L_sprx2o:
mvva 116,140,236
mvva 116,152,244
mvva 116,156,252
mvva 112,156,220
mvva 112,152,212
mvva 112,140,204
mvva 112,136,196
mvvv 108,238,60
mvvv 108,238,92
mvvv 124,236,60
mvvv 124,236,92
mvvv 104,238,56
mvvv 104,238,88
mvvv 120,236,56
mvvv 120,236,88
mvvv 100,238,52
mvvv 100,238,84
cv 108,108,109
cv 109,110,111
cv 110,124,125
cv 111,126,127
cv 104,104,105
v_permlane16_swap_b32_e32 v108,v110
mvvv 116,236,52
v_permlane16_swap_b32_e32 v109,v111
cv 105,106,107
cv 106,120,121
cv 107,122,123
mvvv 116,236,84
buffer_store_dwordx4 v[108:111],v243,s[44:47],s73 offen
v_permlane16_swap_b32_e32 v104,v106
v_permlane16_swap_b32_e32 v105,v107
mvvv 96,238,48
cv 100,100,101
cv 101,102,103
mvvv 96,238,80
buffer_store_dwordx4 v[104:107],v243,s[44:47],s72 offen
cv 102,116,117
cv 103,118,119
mvvv 112,236,48
v_permlane16_swap_b32_e32 v100,v102
v_permlane16_swap_b32_e32 v101,v103
mvvv 112,236,80
buffer_store_dwordx4 v[100:103],v243,s[44:47],s71 offen
cv 96,96,97
cv 97,98,99
s_nop 4
cv 98,112,113
cv 99,114,115
s_nop 0
v_permlane16_swap_b32_e32 v96,v98
v_permlane16_swap_b32_e32 v97,v99
buffer_store_dwordx4 v[96:99],v243,s[44:47],s70 offen
L_oddx:
s_endpgm
L_pend2:
s_nop 7
v_add_f32_e32 v226,v152,v153
v_add_f32_e32 v227,v156,v157
v_subrev_u32_e32 v236,0x23800000,v226
v_subrev_u32_e32 v237,0x23800000,v227
v_max_u32_e32 v236,v236,v237
v_cmp_lt_u32_e64 s[40:41],s48,v236
v_rcp_f32_e32 v226,v226
v_rcp_f32_e32 v227,v227
s_add_u32 s86,s13,4096
v_mov_b32_e32 v236,0
v_mov_b32_e32 v237,0
v_mov_b32_e32 v238,0
v_mov_b32_e32 v239,0
v_mov_b64_e32 v[152:153],0
v_mov_b64_e32 v[156:157],0
v_mul_f32_e32 v226,v242,v226
v_mul_f32_e32 v227,v243,v227
v_add_u32_e32 v225,s94,v225
v_pk_mul_f32 v[160:161],v[160:161],v[226:227] op_sel:[0,0] op_sel_hi:[1,0]
v_pk_mul_f32 v[162:163],v[162:163],v[226:227] op_sel:[0,0] op_sel_hi:[1,0]
v_pk_mul_f32 v[164:165],v[164:165],v[226:227] op_sel:[0,0] op_sel_hi:[1,0]
v_pk_mul_f32 v[166:167],v[166:167],v[226:227] op_sel:[0,0] op_sel_hi:[1,0]
cv 160,160,161
cv 161,162,163
cv 162,164,165
cv 163,166,167
buffer_store_dwordx4 v[160:163],v244,s[8:11],s13 offen offset:0 sc0 sc1
v_pk_mul_f32 v[168:169],v[168:169],v[226:227] op_sel:[0,0] op_sel_hi:[1,0]
v_pk_mul_f32 v[170:171],v[170:171],v[226:227] op_sel:[0,0] op_sel_hi:[1,0]
v_pk_mul_f32 v[172:173],v[172:173],v[226:227] op_sel:[0,0] op_sel_hi:[1,0]
v_pk_mul_f32 v[174:175],v[174:175],v[226:227] op_sel:[0,0] op_sel_hi:[1,0]
cv 168,168,169
cv 169,170,171
cv 170,172,173
cv 171,174,175
buffer_store_dwordx4 v[168:171],v244,s[8:11],s13 offen offset:2048 sc0 sc1
v_pk_mul_f32 v[176:177],v[176:177],v[226:227] op_sel:[0,0] op_sel_hi:[1,0]
v_pk_mul_f32 v[178:179],v[178:179],v[226:227] op_sel:[0,0] op_sel_hi:[1,0]
v_pk_mul_f32 v[180:181],v[180:181],v[226:227] op_sel:[0,0] op_sel_hi:[1,0]
v_pk_mul_f32 v[182:183],v[182:183],v[226:227] op_sel:[0,0] op_sel_hi:[1,0]
cv 176,176,177
cv 177,178,179
cv 178,180,181
v_mfma_f32_32x32x16_bf16 v[160:175],v[236:239],v[236:239],0
cv 179,182,183
buffer_store_dwordx4 v[176:179],v244,s[8:11],s86 offen offset:0 sc0 sc1
v_pk_mul_f32 v[184:185],v[184:185],v[226:227] op_sel:[0,0] op_sel_hi:[1,0]
v_pk_mul_f32 v[186:187],v[186:187],v[226:227] op_sel:[0,0] op_sel_hi:[1,0]
v_pk_mul_f32 v[188:189],v[188:189],v[226:227] op_sel:[0,0] op_sel_hi:[1,0]
v_pk_mul_f32 v[190:191],v[190:191],v[226:227] op_sel:[0,0] op_sel_hi:[1,0]
cv 184,184,185
cv 185,186,187
cv 186,188,189
cv 187,190,191
buffer_store_dwordx4 v[184:187],v244,s[8:11],s86 offen offset:2048 sc0 sc1
v_pk_mul_f32 v[192:193],v[192:193],v[226:227] op_sel:[0,1] op_sel_hi:[1,1]
v_pk_mul_f32 v[194:195],v[194:195],v[226:227] op_sel:[0,1] op_sel_hi:[1,1]
v_pk_mul_f32 v[196:197],v[196:197],v[226:227] op_sel:[0,1] op_sel_hi:[1,1]
v_pk_mul_f32 v[198:199],v[198:199],v[226:227] op_sel:[0,1] op_sel_hi:[1,1]
cv 192,192,193
cv 193,194,195
cv 194,196,197
v_mfma_f32_32x32x16_bf16 v[176:191],v[236:239],v[236:239],0
cv 195,198,199
buffer_store_dwordx4 v[192:195],v244,s[8:11],s13 offen offset:1024 sc0 sc1
v_pk_mul_f32 v[200:201],v[200:201],v[226:227] op_sel:[0,1] op_sel_hi:[1,1]
v_pk_mul_f32 v[202:203],v[202:203],v[226:227] op_sel:[0,1] op_sel_hi:[1,1]
v_pk_mul_f32 v[204:205],v[204:205],v[226:227] op_sel:[0,1] op_sel_hi:[1,1]
v_pk_mul_f32 v[206:207],v[206:207],v[226:227] op_sel:[0,1] op_sel_hi:[1,1]
cv 200,200,201
cv 201,202,203
cv 202,204,205
cv 203,206,207
buffer_store_dwordx4 v[200:203],v244,s[8:11],s13 offen offset:3072 sc0 sc1
v_pk_mul_f32 v[208:209],v[208:209],v[226:227] op_sel:[0,1] op_sel_hi:[1,1]
v_pk_mul_f32 v[210:211],v[210:211],v[226:227] op_sel:[0,1] op_sel_hi:[1,1]
v_pk_mul_f32 v[212:213],v[212:213],v[226:227] op_sel:[0,1] op_sel_hi:[1,1]
v_pk_mul_f32 v[214:215],v[214:215],v[226:227] op_sel:[0,1] op_sel_hi:[1,1]
cv 208,208,209
cv 209,210,211
cv 210,212,213
v_mfma_f32_32x32x16_bf16 v[192:207],v[236:239],v[236:239],0
cv 211,214,215
buffer_store_dwordx4 v[208:211],v244,s[8:11],s86 offen offset:1024 sc0 sc1
v_pk_mul_f32 v[216:217],v[216:217],v[226:227] op_sel:[0,1] op_sel_hi:[1,1]
v_pk_mul_f32 v[218:219],v[218:219],v[226:227] op_sel:[0,1] op_sel_hi:[1,1]
v_pk_mul_f32 v[220:221],v[220:221],v[226:227] op_sel:[0,1] op_sel_hi:[1,1]
v_pk_mul_f32 v[222:223],v[222:223],v[226:227] op_sel:[0,1] op_sel_hi:[1,1]
cv 216,216,217
cv 217,218,219
cv 218,220,221
cv 219,222,223
buffer_store_dwordx4 v[216:219],v244,s[8:11],s86 offen offset:3072 sc0 sc1
s_nop 1
v_mfma_f32_32x32x16_bf16 v[208:223],v[236:239],v[236:239],0
s_nop 7
s_nop 1
s_cmp_eq_u32 s84,4
s_cbranch_scc1 L_ret_eb4
s_cmp_lg_u32 s80,s81
s_branch L_ret_eb5
L_redo:
s_waitcnt vmcnt(0) lgkmcnt(0)
s_nop 15
s_nop 3
s_load_dwordx2 s[76:77],%0,0
s_load_dwordx2 s[80:81],%0,16
s_mov_b32 s78,4194304
s_mov_b32 s79,131072
s_mov_b32 s82,8388608
s_mov_b32 s83,131072
s_mov_b32 s86,0
s_waitcnt lgkmcnt(0)
L_rd_t:
s_nop 15
s_nop 3
s_cmp_lt_u32 s86,2
s_cselect_b32 s88,s23,s22
s_cselect_b32 s95,s13,s14
s_and_b32 s89,s86,1
s_lshl_b32 s90,s89,10
s_add_u32 s95,s95,s90
s_lshl_b32 s89,s89,6
s_add_u32 s88,s88,s89
s_lshl_b32 s89,s12,4
s_add_u32 s88,s88,s89
v_bfe_u32 v213,v244,4,4
v_lshrrev_b32_e32 v214,8,v244
v_add_u32_e32 v210,s88,v213
v_lshlrev_b32_e32 v208,11,v210
v_lshl_add_u32 v208,v214,4,v208
v_lshlrev_b32_e32 v209,16,v214
v_lshl_add_u32 v209,v213,1,v209
s_lshl_b32 s89,s18,8
v_add_u32_e32 v209,s89,v209
v_lshlrev_b32_e32 v214,2,v214
v_sub_u32_e32 v210,v210,v214
v_accvgpr_write_b32 a192,0
v_accvgpr_write_b32 a193,0
v_accvgpr_write_b32 a194,0
v_accvgpr_write_b32 a195,0
v_accvgpr_write_b32 a196,0
v_accvgpr_write_b32 a197,0
v_accvgpr_write_b32 a198,0
v_accvgpr_write_b32 a199,0
v_accvgpr_write_b32 a200,0
v_accvgpr_write_b32 a201,0
v_accvgpr_write_b32 a202,0
v_accvgpr_write_b32 a203,0
v_accvgpr_write_b32 a204,0
v_accvgpr_write_b32 a205,0
v_accvgpr_write_b32 a206,0
v_accvgpr_write_b32 a207,0
v_accvgpr_write_b32 a208,0
v_accvgpr_write_b32 a209,0
v_accvgpr_write_b32 a210,0
v_accvgpr_write_b32 a211,0
v_accvgpr_write_b32 a212,0
v_accvgpr_write_b32 a213,0
v_accvgpr_write_b32 a214,0
v_accvgpr_write_b32 a215,0
v_accvgpr_write_b32 a216,0
v_accvgpr_write_b32 a217,0
v_accvgpr_write_b32 a218,0
v_accvgpr_write_b32 a219,0
v_accvgpr_write_b32 a220,0
v_accvgpr_write_b32 a221,0
v_accvgpr_write_b32 a222,0
v_accvgpr_write_b32 a223,0
s_mov_b32 s87,0
L_rd_q:
s_nop 15
s_nop 3
s_lshl_b32 s89,s87,6
s_lshl_b32 s90,s87,18
buffer_load_dwordx4 v[200:203],v208,s[76:79],s89 offen
buffer_load_ushort v160,v209,s[80:83],s90 offen offset:0
buffer_load_ushort v164,v209,s[80:83],s90 offen offset:32
buffer_load_ushort v168,v209,s[80:83],s90 offen offset:64
buffer_load_ushort v172,v209,s[80:83],s90 offen offset:96
buffer_load_ushort v176,v209,s[80:83],s90 offen offset:128
buffer_load_ushort v180,v209,s[80:83],s90 offen offset:160
buffer_load_ushort v184,v209,s[80:83],s90 offen offset:192
buffer_load_ushort v188,v209,s[80:83],s90 offen offset:224
s_add_u32 s90,s90,8192
buffer_load_ushort v192,v209,s[80:83],s90 offen offset:0
buffer_load_ushort v193,v209,s[80:83],s90 offen offset:32
buffer_load_ushort v194,v209,s[80:83],s90 offen offset:64
buffer_load_ushort v195,v209,s[80:83],s90 offen offset:96
buffer_load_ushort v196,v209,s[80:83],s90 offen offset:128
buffer_load_ushort v197,v209,s[80:83],s90 offen offset:160
buffer_load_ushort v198,v209,s[80:83],s90 offen offset:192
buffer_load_ushort v199,v209,s[80:83],s90 offen offset:224
s_add_u32 s90,s90,8192
s_waitcnt vmcnt(0)
v_lshl_or_b32 v160,v192,16,v160
v_lshl_or_b32 v164,v193,16,v164
v_lshl_or_b32 v168,v194,16,v168
v_lshl_or_b32 v172,v195,16,v172
v_lshl_or_b32 v176,v196,16,v176
v_lshl_or_b32 v180,v197,16,v180
v_lshl_or_b32 v184,v198,16,v184
v_lshl_or_b32 v188,v199,16,v188
buffer_load_ushort v161,v209,s[80:83],s90 offen offset:0
buffer_load_ushort v165,v209,s[80:83],s90 offen offset:32
buffer_load_ushort v169,v209,s[80:83],s90 offen offset:64
buffer_load_ushort v173,v209,s[80:83],s90 offen offset:96
buffer_load_ushort v177,v209,s[80:83],s90 offen offset:128
buffer_load_ushort v181,v209,s[80:83],s90 offen offset:160
buffer_load_ushort v185,v209,s[80:83],s90 offen offset:192
buffer_load_ushort v189,v209,s[80:83],s90 offen offset:224
s_add_u32 s90,s90,8192
buffer_load_ushort v192,v209,s[80:83],s90 offen offset:0
buffer_load_ushort v193,v209,s[80:83],s90 offen offset:32
buffer_load_ushort v194,v209,s[80:83],s90 offen offset:64
buffer_load_ushort v195,v209,s[80:83],s90 offen offset:96
buffer_load_ushort v196,v209,s[80:83],s90 offen offset:128
buffer_load_ushort v197,v209,s[80:83],s90 offen offset:160
buffer_load_ushort v198,v209,s[80:83],s90 offen offset:192
buffer_load_ushort v199,v209,s[80:83],s90 offen offset:224
s_add_u32 s90,s90,8192
s_waitcnt vmcnt(0)
v_lshl_or_b32 v161,v192,16,v161
v_lshl_or_b32 v165,v193,16,v165
v_lshl_or_b32 v169,v194,16,v169
v_lshl_or_b32 v173,v195,16,v173
v_lshl_or_b32 v177,v196,16,v177
v_lshl_or_b32 v181,v197,16,v181
v_lshl_or_b32 v185,v198,16,v185
v_lshl_or_b32 v189,v199,16,v189
buffer_load_ushort v162,v209,s[80:83],s90 offen offset:0
buffer_load_ushort v166,v209,s[80:83],s90 offen offset:32
buffer_load_ushort v170,v209,s[80:83],s90 offen offset:64
buffer_load_ushort v174,v209,s[80:83],s90 offen offset:96
buffer_load_ushort v178,v209,s[80:83],s90 offen offset:128
buffer_load_ushort v182,v209,s[80:83],s90 offen offset:160
buffer_load_ushort v186,v209,s[80:83],s90 offen offset:192
buffer_load_ushort v190,v209,s[80:83],s90 offen offset:224
s_add_u32 s90,s90,8192
buffer_load_ushort v192,v209,s[80:83],s90 offen offset:0
buffer_load_ushort v193,v209,s[80:83],s90 offen offset:32
buffer_load_ushort v194,v209,s[80:83],s90 offen offset:64
buffer_load_ushort v195,v209,s[80:83],s90 offen offset:96
buffer_load_ushort v196,v209,s[80:83],s90 offen offset:128
buffer_load_ushort v197,v209,s[80:83],s90 offen offset:160
buffer_load_ushort v198,v209,s[80:83],s90 offen offset:192
buffer_load_ushort v199,v209,s[80:83],s90 offen offset:224
s_add_u32 s90,s90,8192
s_waitcnt vmcnt(0)
v_lshl_or_b32 v162,v192,16,v162
v_lshl_or_b32 v166,v193,16,v166
v_lshl_or_b32 v170,v194,16,v170
v_lshl_or_b32 v174,v195,16,v174
v_lshl_or_b32 v178,v196,16,v178
v_lshl_or_b32 v182,v197,16,v182
v_lshl_or_b32 v186,v198,16,v186
v_lshl_or_b32 v190,v199,16,v190
buffer_load_ushort v163,v209,s[80:83],s90 offen offset:0
buffer_load_ushort v167,v209,s[80:83],s90 offen offset:32
buffer_load_ushort v171,v209,s[80:83],s90 offen offset:64
buffer_load_ushort v175,v209,s[80:83],s90 offen offset:96
buffer_load_ushort v179,v209,s[80:83],s90 offen offset:128
buffer_load_ushort v183,v209,s[80:83],s90 offen offset:160
buffer_load_ushort v187,v209,s[80:83],s90 offen offset:192
buffer_load_ushort v191,v209,s[80:83],s90 offen offset:224
s_add_u32 s90,s90,8192
buffer_load_ushort v192,v209,s[80:83],s90 offen offset:0
buffer_load_ushort v193,v209,s[80:83],s90 offen offset:32
buffer_load_ushort v194,v209,s[80:83],s90 offen offset:64
buffer_load_ushort v195,v209,s[80:83],s90 offen offset:96
buffer_load_ushort v196,v209,s[80:83],s90 offen offset:128
buffer_load_ushort v197,v209,s[80:83],s90 offen offset:160
buffer_load_ushort v198,v209,s[80:83],s90 offen offset:192
buffer_load_ushort v199,v209,s[80:83],s90 offen offset:224
s_add_u32 s90,s90,8192
s_waitcnt vmcnt(0)
v_lshl_or_b32 v163,v192,16,v163
v_lshl_or_b32 v167,v193,16,v167
v_lshl_or_b32 v171,v194,16,v171
v_lshl_or_b32 v175,v195,16,v175
v_lshl_or_b32 v179,v196,16,v179
v_lshl_or_b32 v183,v197,16,v183
v_lshl_or_b32 v187,v198,16,v187
v_lshl_or_b32 v191,v199,16,v191
mavv 192,160,200
mavv 196,164,200
mavv 200,168,200
mavv 204,172,200
mavv 208,176,200
mavv 212,180,200
mavv 216,184,200
mavv 220,188,200
s_add_u32 s87,s87,1
s_cmp_lt_u32 s87,32
s_cbranch_scc1 L_rd_q
s_nop 15
s_nop 3
v_accvgpr_read_b32 v192,a192
v_accvgpr_read_b32 v193,a193
v_accvgpr_read_b32 v194,a194
v_accvgpr_read_b32 v195,a195
v_accvgpr_read_b32 v196,a196
v_accvgpr_read_b32 v197,a197
v_accvgpr_read_b32 v198,a198
v_accvgpr_read_b32 v199,a199
v_mul_f32_e32 v192,0x3e0293ee,v192
v_mul_f32_e32 v193,0x3e0293ee,v193
v_mul_f32_e32 v194,0x3e0293ee,v194
v_mul_f32_e32 v195,0x3e0293ee,v195
v_mul_f32_e32 v196,0x3e0293ee,v196
v_mul_f32_e32 v197,0x3e0293ee,v197
v_mul_f32_e32 v198,0x3e0293ee,v198
v_mul_f32_e32 v199,0x3e0293ee,v199
cv 192,192,193
cv 193,194,195
cv 194,196,197
cv 195,198,199
v_accvgpr_write_b32 a224,v192
v_accvgpr_write_b32 a225,v193
v_accvgpr_write_b32 a226,v194
v_accvgpr_write_b32 a227,v195
v_accvgpr_read_b32 v192,a200
v_accvgpr_read_b32 v193,a201
v_accvgpr_read_b32 v194,a202
v_accvgpr_read_b32 v195,a203
v_accvgpr_read_b32 v196,a204
v_accvgpr_read_b32 v197,a205
v_accvgpr_read_b32 v198,a206
v_accvgpr_read_b32 v199,a207
v_mul_f32_e32 v192,0x3e0293ee,v192
v_mul_f32_e32 v193,0x3e0293ee,v193
v_mul_f32_e32 v194,0x3e0293ee,v194
v_mul_f32_e32 v195,0x3e0293ee,v195
v_mul_f32_e32 v196,0x3e0293ee,v196
v_mul_f32_e32 v197,0x3e0293ee,v197
v_mul_f32_e32 v198,0x3e0293ee,v198
v_mul_f32_e32 v199,0x3e0293ee,v199
cv 192,192,193
cv 193,194,195
cv 194,196,197
cv 195,198,199
v_accvgpr_write_b32 a228,v192
v_accvgpr_write_b32 a229,v193
v_accvgpr_write_b32 a230,v194
v_accvgpr_write_b32 a231,v195
v_accvgpr_read_b32 v192,a208
v_accvgpr_read_b32 v193,a209
v_accvgpr_read_b32 v194,a210
v_accvgpr_read_b32 v195,a211
v_accvgpr_read_b32 v196,a212
v_accvgpr_read_b32 v197,a213
v_accvgpr_read_b32 v198,a214
v_accvgpr_read_b32 v199,a215
v_mul_f32_e32 v192,0x3e0293ee,v192
v_mul_f32_e32 v193,0x3e0293ee,v193
v_mul_f32_e32 v194,0x3e0293ee,v194
v_mul_f32_e32 v195,0x3e0293ee,v195
v_mul_f32_e32 v196,0x3e0293ee,v196
v_mul_f32_e32 v197,0x3e0293ee,v197
v_mul_f32_e32 v198,0x3e0293ee,v198
v_mul_f32_e32 v199,0x3e0293ee,v199
cv 192,192,193
cv 193,194,195
cv 194,196,197
cv 195,198,199
v_accvgpr_write_b32 a232,v192
v_accvgpr_write_b32 a233,v193
v_accvgpr_write_b32 a234,v194
v_accvgpr_write_b32 a235,v195
v_accvgpr_read_b32 v192,a216
v_accvgpr_read_b32 v193,a217
v_accvgpr_read_b32 v194,a218
v_accvgpr_read_b32 v195,a219
v_accvgpr_read_b32 v196,a220
v_accvgpr_read_b32 v197,a221
v_accvgpr_read_b32 v198,a222
v_accvgpr_read_b32 v199,a223
v_mul_f32_e32 v192,0x3e0293ee,v192
v_mul_f32_e32 v193,0x3e0293ee,v193
v_mul_f32_e32 v194,0x3e0293ee,v194
v_mul_f32_e32 v195,0x3e0293ee,v195
v_mul_f32_e32 v196,0x3e0293ee,v196
v_mul_f32_e32 v197,0x3e0293ee,v197
v_mul_f32_e32 v198,0x3e0293ee,v198
v_mul_f32_e32 v199,0x3e0293ee,v199
cv 192,192,193
cv 193,194,195
cv 194,196,197
cv 195,198,199
v_accvgpr_write_b32 a236,v192
v_accvgpr_write_b32 a237,v193
v_accvgpr_write_b32 a238,v194
v_accvgpr_write_b32 a239,v195
v_mov_b32_e32 v160,0
v_mov_b32_e32 v161,0
v_mov_b32_e32 v162,0
v_mov_b32_e32 v163,0
v_mov_b32_e32 v164,0
v_mov_b32_e32 v165,0
v_mov_b32_e32 v166,0
v_mov_b32_e32 v167,0
v_mov_b32_e32 v168,0
v_mov_b32_e32 v169,0
v_mov_b32_e32 v170,0
v_mov_b32_e32 v171,0
v_mov_b32_e32 v172,0
v_mov_b32_e32 v173,0
v_mov_b32_e32 v174,0
v_mov_b32_e32 v175,0
v_mov_b32_e32 v176,0
v_mov_b32_e32 v177,0
v_mov_b32_e32 v178,0
v_mov_b32_e32 v179,0
v_mov_b32_e32 v180,0
v_mov_b32_e32 v181,0
v_mov_b32_e32 v182,0
v_mov_b32_e32 v183,0
v_mov_b32_e32 v184,0
v_mov_b32_e32 v185,0
v_mov_b32_e32 v186,0
v_mov_b32_e32 v187,0
v_mov_b32_e32 v188,0
v_mov_b32_e32 v189,0
v_mov_b32_e32 v190,0
v_mov_b32_e32 v191,0
v_mov_b32_e32 v216,0xf149f2ca
v_mov_b32_e32 v211,v216
v_mov_b32_e32 v212,0
s_lshr_b32 s91,s88,5
s_mov_b32 s87,0
L_rd_a:
s_nop 15
s_nop 3
s_mul_i32 s89,s18,524288
s_lshl_b32 s90,s87,13
s_add_u32 s89,s89,s90
s_add_u32 s90,s89,16777216
la 64,244,90,0
la 68,244,90,1024
la 72,244,90,2048
la 76,244,90,3072
s_add_u32 s90,s89,16781312
la 80,244,90,0
la 84,244,90,1024
la 88,244,90,2048
la 92,244,90,3072
s_add_u32 s90,s89,33554432
la 96,244,90,0
la 100,244,90,1024
la 104,244,90,2048
la 108,244,90,3072
s_add_u32 s90,s89,33558528
la 112,244,90,0
la 116,244,90,1024
la 120,244,90,2048
la 124,244,90,3072
s_waitcnt vmcnt(0)
zvaa 192,64,224
mvaa 192,72,228
mvaa 192,80,232
mvaa 192,88,236
zvaa 196,68,224
mvaa 196,76,228
mvaa 196,84,232
mvaa 196,92,236
v_cmp_gt_i32_e32 vcc,0,v210
s_nop 2
v_cndmask_b32_e32 v192,v192,v216,vcc
v_cmp_gt_i32_e32 vcc,1,v210
s_nop 1
v_cndmask_b32_e32 v193,v193,v216,vcc
v_cmp_gt_i32_e32 vcc,2,v210
s_nop 1
v_cndmask_b32_e32 v194,v194,v216,vcc
v_cmp_gt_i32_e32 vcc,3,v210
s_nop 1
v_cndmask_b32_e32 v195,v195,v216,vcc
v_cmp_gt_i32_e32 vcc,16,v210
s_nop 1
v_cndmask_b32_e32 v196,v196,v216,vcc
v_cmp_gt_i32_e32 vcc,17,v210
s_nop 1
v_cndmask_b32_e32 v197,v197,v216,vcc
v_cmp_gt_i32_e32 vcc,18,v210
s_nop 1
v_cndmask_b32_e32 v198,v198,v216,vcc
v_cmp_gt_i32_e32 vcc,19,v210
s_nop 1
v_cndmask_b32_e32 v199,v199,v216,vcc
v_max3_f32 v213,v192,v193,v194
v_max3_f32 v214,v195,v196,v197
v_max3_f32 v213,v213,v198,v199
v_max_f32_e32 v213,v213,v214
v_mov_b32_e32 v214,v213
s_nop 1
v_permlane16_swap_b32_e32 v213,v214
v_max_f32_e32 v213,v213,v214
v_mov_b32_e32 v214,v213
s_nop 1
v_permlane32_swap_b32_e32 v213,v214
v_max_f32_e32 v213,v213,v214
v_max_f32_e32 v213,v213,v211
v_sub_f32_e32 v215,v211,v213
ex 215
v_mov_b32_e32 v211,v213
v_sub_f32_e32 v192,v192,v211
v_sub_f32_e32 v193,v193,v211
v_sub_f32_e32 v194,v194,v211
v_sub_f32_e32 v195,v195,v211
v_sub_f32_e32 v196,v196,v211
v_sub_f32_e32 v197,v197,v211
v_sub_f32_e32 v198,v198,v211
v_sub_f32_e32 v199,v199,v211
ex 192
ex 193
ex 194
ex 195
ex 196
ex 197
ex 198
ex 199
v_mul_f32_e32 v212,v215,v212
v_add_f32_e32 v212,v212,v192
v_add_f32_e32 v212,v212,v193
v_add_f32_e32 v212,v212,v194
v_add_f32_e32 v212,v212,v195
v_add_f32_e32 v212,v212,v196
v_add_f32_e32 v212,v212,v197
v_add_f32_e32 v212,v212,v198
v_add_f32_e32 v212,v212,v199
cv 192,192,193
cv 193,194,195
cv 194,196,197
cv 195,198,199
v_mul_f32_e32 v160,v215,v160
v_mul_f32_e32 v161,v215,v161
v_mul_f32_e32 v162,v215,v162
)"
R"(v_mul_f32_e32 v163,v215,v163
v_mul_f32_e32 v164,v215,v164
v_mul_f32_e32 v165,v215,v165
v_mul_f32_e32 v166,v215,v166
v_mul_f32_e32 v167,v215,v167
v_mul_f32_e32 v168,v215,v168
v_mul_f32_e32 v169,v215,v169
v_mul_f32_e32 v170,v215,v170
v_mul_f32_e32 v171,v215,v171
v_mul_f32_e32 v172,v215,v172
v_mul_f32_e32 v173,v215,v173
v_mul_f32_e32 v174,v215,v174
v_mul_f32_e32 v175,v215,v175
v_mul_f32_e32 v176,v215,v176
v_mul_f32_e32 v177,v215,v177
v_mul_f32_e32 v178,v215,v178
v_mul_f32_e32 v179,v215,v179
v_mul_f32_e32 v180,v215,v180
v_mul_f32_e32 v181,v215,v181
v_mul_f32_e32 v182,v215,v182
v_mul_f32_e32 v183,v215,v183
v_mul_f32_e32 v184,v215,v184
v_mul_f32_e32 v185,v215,v185
v_mul_f32_e32 v186,v215,v186
v_mul_f32_e32 v187,v215,v187
v_mul_f32_e32 v188,v215,v188
v_mul_f32_e32 v189,v215,v189
v_mul_f32_e32 v190,v215,v190
v_mul_f32_e32 v191,v215,v191
mvav 160,96,192
mvav 164,100,192
mvav 168,104,192
mvav 172,108,192
mvav 176,112,192
mvav 180,116,192
mvav 184,120,192
mvav 188,124,192
v_subrev_u32_e32 v210,32,v210
s_add_u32 s87,s87,1
s_cmp_le_u32 s87,s91
s_cbranch_scc1 L_rd_a
s_nop 15
s_nop 3
s_lshl_b32 s89,s88,2
s_add_u32 s89,s89,83886080
v_bfe_u32 v213,v244,2,6
buffer_load_dword v215,v213,s[8:11],s89 offen sc1
v_mov_b32_e32 v214,v212
s_nop 1
v_permlane16_swap_b32_e32 v212,v214
v_add_f32_e32 v212,v212,v214
v_mov_b32_e32 v214,v212
s_nop 1
v_permlane32_swap_b32_e32 v212,v214
v_add_f32_e32 v212,v212,v214
v_rcp_f32_e32 v212,v212
s_waitcnt vmcnt(0)
v_mul_f32_e32 v215,v215,v212
v_mul_f32_e32 v160,v215,v160
v_mul_f32_e32 v161,v215,v161
v_mul_f32_e32 v162,v215,v162
v_mul_f32_e32 v163,v215,v163
v_mul_f32_e32 v164,v215,v164
v_mul_f32_e32 v165,v215,v165
v_mul_f32_e32 v166,v215,v166
v_mul_f32_e32 v167,v215,v167
v_mul_f32_e32 v168,v215,v168
v_mul_f32_e32 v169,v215,v169
v_mul_f32_e32 v170,v215,v170
v_mul_f32_e32 v171,v215,v171
v_mul_f32_e32 v172,v215,v172
v_mul_f32_e32 v173,v215,v173
v_mul_f32_e32 v174,v215,v174
v_mul_f32_e32 v175,v215,v175
v_mul_f32_e32 v176,v215,v176
v_mul_f32_e32 v177,v215,v177
v_mul_f32_e32 v178,v215,v178
v_mul_f32_e32 v179,v215,v179
v_mul_f32_e32 v180,v215,v180
v_mul_f32_e32 v181,v215,v181
v_mul_f32_e32 v182,v215,v182
v_mul_f32_e32 v183,v215,v183
v_mul_f32_e32 v184,v215,v184
v_mul_f32_e32 v185,v215,v185
v_mul_f32_e32 v186,v215,v186
v_mul_f32_e32 v187,v215,v187
v_mul_f32_e32 v188,v215,v188
v_mul_f32_e32 v189,v215,v189
v_mul_f32_e32 v190,v215,v190
v_mul_f32_e32 v191,v215,v191
s_add_u32 s89,s95,4096
cv 160,160,161
cv 161,162,163
cv 162,164,165
cv 163,166,167
buffer_store_dwordx4 v[160:163],v244,s[8:11],s95 offen offset:0 sc0 sc1
cv 168,168,169
cv 169,170,171
cv 170,172,173
cv 171,174,175
buffer_store_dwordx4 v[168:171],v244,s[8:11],s95 offen offset:2048 sc0 sc1
cv 176,176,177
cv 177,178,179
cv 178,180,181
cv 179,182,183
buffer_store_dwordx4 v[176:179],v244,s[8:11],s89 offen offset:0 sc0 sc1
cv 184,184,185
cv 185,186,187
cv 186,188,189
cv 187,190,191
buffer_store_dwordx4 v[184:187],v244,s[8:11],s89 offen offset:2048 sc0 sc1
s_waitcnt vmcnt(0)
v_mov_b32_e32 v192,v160
v_accvgpr_read_b32 v160,a240
v_accvgpr_write_b32 a240,v192
v_mov_b32_e32 v193,v161
v_accvgpr_read_b32 v161,a241
v_accvgpr_write_b32 a241,v193
v_mov_b32_e32 v194,v162
v_accvgpr_read_b32 v162,a242
v_accvgpr_write_b32 a242,v194
v_mov_b32_e32 v195,v163
v_accvgpr_read_b32 v163,a243
v_accvgpr_write_b32 a243,v195
v_mov_b32_e32 v200,v168
v_accvgpr_read_b32 v168,a244
v_accvgpr_write_b32 a244,v200
v_mov_b32_e32 v201,v169
v_accvgpr_read_b32 v169,a245
v_accvgpr_write_b32 a245,v201
v_mov_b32_e32 v202,v170
v_accvgpr_read_b32 v170,a246
v_accvgpr_write_b32 a246,v202
v_mov_b32_e32 v203,v171
v_accvgpr_read_b32 v171,a247
v_accvgpr_write_b32 a247,v203
v_mov_b32_e32 v208,v176
v_accvgpr_read_b32 v176,a248
v_accvgpr_write_b32 a248,v208
v_mov_b32_e32 v209,v177
v_accvgpr_read_b32 v177,a249
v_accvgpr_write_b32 a249,v209
v_mov_b32_e32 v210,v178
v_accvgpr_read_b32 v178,a250
v_accvgpr_write_b32 a250,v210
v_mov_b32_e32 v211,v179
v_accvgpr_read_b32 v179,a251
v_accvgpr_write_b32 a251,v211
v_mov_b32_e32 v216,v184
v_accvgpr_read_b32 v184,a252
v_accvgpr_write_b32 a252,v216
v_mov_b32_e32 v217,v185
v_accvgpr_read_b32 v185,a253
v_accvgpr_write_b32 a253,v217
v_mov_b32_e32 v218,v186
v_accvgpr_read_b32 v186,a254
v_accvgpr_write_b32 a254,v218
v_mov_b32_e32 v219,v187
v_accvgpr_read_b32 v187,a255
v_accvgpr_write_b32 a255,v219
s_add_u32 s86,s86,1
s_cmp_lt_u32 s86,4
s_cbranch_scc1 L_rd_t
s_nop 15
s_nop 3
s_nop 1
s_cmp_eq_u32 s84,4
s_cbranch_scc1 L_redo_r4
s_cmp_lg_u32 s80,s81
s_branch L_redo_r5
L_spbe:
s_mov_b32 s91,65536
L_splbe:
s_sleep 2
buffer_load_dword v253,v224,s[8:11],s53 offen sc1
s_waitcnt vmcnt(0)
v_cmp_ne_u32_e32 vcc,4,v253
s_nop 1
s_cbranch_vccz L_sprbe
s_sub_u32 s91,s91,1
s_cbranch_scc0 L_splbe
s_branch L_sprbe
L_spa3e:
s_mov_b32 s91,65536
L_spla3e:
s_sleep 2
buffer_load_dword v253,v242,s[8:11],s53 offen sc1
s_waitcnt vmcnt(0)
v_cmp_ne_u32_e32 vcc,4,v253
s_nop 1
s_cbranch_vccz L_spra3e
s_sub_u32 s91,s91,1
s_cbranch_scc0 L_spla3e
s_branch L_spra3e
L_spa4e:
s_mov_b32 s91,65536
L_spla4e:
s_sleep 2
buffer_load_dword v253,v242,s[8:11],s53 offen sc1
s_waitcnt vmcnt(0)
v_cmp_ne_u32_e32 vcc,4,v253
s_nop 1
s_cbranch_vccz L_spra4e
s_sub_u32 s91,s91,1
s_cbranch_scc0 L_spla4e
s_branch L_spra4e
L_spa5e:
s_mov_b32 s91,65536
L_spla5e:
s_sleep 2
buffer_load_dword v253,v242,s[8:11],s53 offen sc1
s_waitcnt vmcnt(0)
v_cmp_ne_u32_e32 vcc,4,v253
s_nop 1
s_cbranch_vccz L_spra5e
s_sub_u32 s91,s91,1
s_cbranch_scc0 L_spla5e
s_branch L_spra5e
L_spa6e:
s_mov_b32 s91,65536
L_spla6e:
s_sleep 2
buffer_load_dword v253,v242,s[8:11],s53 offen sc1
s_waitcnt vmcnt(0)
v_cmp_ne_u32_e32 vcc,4,v253
s_nop 1
s_cbranch_vccz L_spra6e
s_sub_u32 s91,s91,1
s_cbranch_scc0 L_spla6e
s_branch L_spra6e
L_spa7e:
s_mov_b32 s91,65536
L_spla7e:
s_sleep 2
buffer_load_dword v253,v242,s[8:11],s53 offen sc1
s_waitcnt vmcnt(0)
v_cmp_ne_u32_e32 vcc,4,v253
s_nop 1
s_cbranch_vccz L_spra7e
s_sub_u32 s91,s91,1
s_cbranch_scc0 L_spla7e
s_branch L_spra7e
L_spa8e:
s_mov_b32 s91,65536
L_spla8e:
s_sleep 2
buffer_load_dword v253,v242,s[8:11],s53 offen sc1
s_waitcnt vmcnt(0)
v_cmp_ne_u32_e32 vcc,4,v253
s_nop 1
s_cbranch_vccz L_spra8e
s_sub_u32 s91,s91,1
s_cbranch_scc0 L_spla8e
s_branch L_spra8e
L_spa9e:
s_mov_b32 s91,65536
L_spla9e:
s_sleep 2
buffer_load_dword v253,v242,s[8:11],s53 offen sc1
s_waitcnt vmcnt(0)
v_cmp_ne_u32_e32 vcc,4,v253
s_nop 1
s_cbranch_vccz L_spra9e
s_sub_u32 s91,s91,1
s_cbranch_scc0 L_spla9e
s_branch L_spra9e
L_spa10e:
s_mov_b32 s91,65536
L_spla10e:
s_sleep 2
buffer_load_dword v253,v242,s[8:11],s53 offen sc1
s_waitcnt vmcnt(0)
v_cmp_ne_u32_e32 vcc,4,v253
s_nop 1
s_cbranch_vccz L_spra10e
s_sub_u32 s91,s91,1
s_cbranch_scc0 L_spla10e
s_branch L_spra10e
L_sppe:
s_mov_b32 s91,65536
L_splpe:
s_sleep 2
buffer_load_dword v253,v242,s[8:11],s53 offen sc1
s_waitcnt vmcnt(0)
v_cmp_ne_u32_e32 vcc,4,v253
s_nop 1
s_cbranch_vccz L_sprpe
s_sub_u32 s91,s91,1
s_cbranch_scc0 L_splpe
s_branch L_sprpe
L_spx0e:
s_mov_b32 s91,65536
L_splx0e:
lv 16,244,82,0
lv 20,244,82,1024
lv 24,244,82,2048
lv 28,244,82,3072
s_waitcnt vmcnt(0)
v_max_u32_e32 v248,v16,v17
v_max3_u32 v248,v248,v18,v19
v_max3_u32 v248,v248,v20,v21
v_max3_u32 v248,v248,v22,v23
v_max3_u32 v248,v248,v24,v25
v_max3_u32 v248,v248,v26,v27
v_max3_u32 v248,v248,v28,v29
v_max3_u32 v248,v248,v30,v31
v_cmp_eq_u32_e32 vcc,-1,v248
s_nop 1
s_cbranch_vccz L_sprx0e
s_sub_u32 s91,s91,1
s_cbranch_scc0 L_splx0e
s_branch L_sprx0e
L_spx1e:
s_mov_b32 s91,65536
L_splx1e:
lv 48,244,83,0
lv 52,244,83,1024
lv 56,244,83,2048
lv 60,244,83,3072
s_waitcnt vmcnt(0)
v_max_u32_e32 v248,v48,v49
v_max3_u32 v248,v248,v50,v51
v_max3_u32 v248,v248,v52,v53
v_max3_u32 v248,v248,v54,v55
v_max3_u32 v248,v248,v56,v57
v_max3_u32 v248,v248,v58,v59
v_max3_u32 v248,v248,v60,v61
v_max3_u32 v248,v248,v62,v63
v_cmp_eq_u32_e32 vcc,-1,v248
s_nop 1
s_cbranch_vccz L_sprx1e
s_sub_u32 s91,s91,1
s_cbranch_scc0 L_splx1e
s_branch L_sprx1e
L_spx2e:
s_mov_b32 s91,65536
L_splx2e:
lv 80,244,84,0
lv 84,244,84,1024
lv 88,244,84,2048
lv 92,244,84,3072
s_waitcnt vmcnt(0)
v_max_u32_e32 v248,v80,v81
v_max3_u32 v248,v248,v82,v83
v_max3_u32 v248,v248,v84,v85
v_max3_u32 v248,v248,v86,v87
v_max3_u32 v248,v248,v88,v89
v_max3_u32 v248,v248,v90,v91
v_max3_u32 v248,v248,v92,v93
v_max3_u32 v248,v248,v94,v95
v_cmp_eq_u32_e32 vcc,-1,v248
s_nop 1
s_cbranch_vccz L_sprx2e
s_sub_u32 s91,s91,1
s_cbranch_scc0 L_splx2e
s_branch L_sprx2e
L_spbo:
s_mov_b32 s91,65536
L_splbo:
s_sleep 2
buffer_load_dword v253,v224,s[8:11],s53 offen sc1
s_waitcnt vmcnt(0)
v_cmp_ne_u32_e32 vcc,4,v253
s_nop 1
s_cbranch_vccz L_sprbo
s_sub_u32 s91,s91,1
s_cbranch_scc0 L_splbo
s_branch L_sprbo
L_spa3o:
s_mov_b32 s91,65536
L_spla3o:
s_sleep 2
buffer_load_dword v253,v242,s[8:11],s53 offen sc1
s_waitcnt vmcnt(0)
v_cmp_ne_u32_e32 vcc,4,v253
s_nop 1
s_cbranch_vccz L_spra3o
s_sub_u32 s91,s91,1
s_cbranch_scc0 L_spla3o
s_branch L_spra3o
L_spa4o:
s_mov_b32 s91,65536
L_spla4o:
s_sleep 2
buffer_load_dword v253,v242,s[8:11],s53 offen sc1
s_waitcnt vmcnt(0)
v_cmp_ne_u32_e32 vcc,4,v253
s_nop 1
s_cbranch_vccz L_spra4o
s_sub_u32 s91,s91,1
s_cbranch_scc0 L_spla4o
s_branch L_spra4o
L_spa5o:
s_mov_b32 s91,65536
L_spla5o:
s_sleep 2
buffer_load_dword v253,v242,s[8:11],s53 offen sc1
s_waitcnt vmcnt(0)
v_cmp_ne_u32_e32 vcc,4,v253
s_nop 1
s_cbranch_vccz L_spra5o
s_sub_u32 s91,s91,1
s_cbranch_scc0 L_spla5o
s_branch L_spra5o
L_spa6o:
s_mov_b32 s91,65536
L_spla6o:
s_sleep 2
buffer_load_dword v253,v242,s[8:11],s53 offen sc1
s_waitcnt vmcnt(0)
v_cmp_ne_u32_e32 vcc,4,v253
s_nop 1
s_cbranch_vccz L_spra6o
s_sub_u32 s91,s91,1
s_cbranch_scc0 L_spla6o
s_branch L_spra6o
L_spa7o:
s_mov_b32 s91,65536
L_spla7o:
s_sleep 2
buffer_load_dword v253,v242,s[8:11],s53 offen sc1
s_waitcnt vmcnt(0)
v_cmp_ne_u32_e32 vcc,4,v253
s_nop 1
s_cbranch_vccz L_spra7o
s_sub_u32 s91,s91,1
s_cbranch_scc0 L_spla7o
s_branch L_spra7o
L_spa8o:
s_mov_b32 s91,65536
L_spla8o:
s_sleep 2
buffer_load_dword v253,v242,s[8:11],s53 offen sc1
s_waitcnt vmcnt(0)
v_cmp_ne_u32_e32 vcc,4,v253
s_nop 1
s_cbranch_vccz L_spra8o
s_sub_u32 s91,s91,1
s_cbranch_scc0 L_spla8o
s_branch L_spra8o
L_sppo:
s_mov_b32 s91,65536
L_splpo:
s_sleep 2
buffer_load_dword v253,v242,s[8:11],s53 offen sc1
s_waitcnt vmcnt(0)
v_cmp_ne_u32_e32 vcc,4,v253
s_nop 1
s_cbranch_vccz L_sprpo
s_sub_u32 s91,s91,1
s_cbranch_scc0 L_splpo
s_branch L_sprpo
L_spx0o:
s_mov_b32 s91,65536
L_splx0o:
lv 16,244,82,0
lv 20,244,82,1024
lv 24,244,82,2048
lv 28,244,82,3072
s_waitcnt vmcnt(0)
v_max_u32_e32 v248,v16,v17
v_max3_u32 v248,v248,v18,v19
v_max3_u32 v248,v248,v20,v21
v_max3_u32 v248,v248,v22,v23
v_max3_u32 v248,v248,v24,v25
v_max3_u32 v248,v248,v26,v27
v_max3_u32 v248,v248,v28,v29
v_max3_u32 v248,v248,v30,v31
v_cmp_eq_u32_e32 vcc,-1,v248
s_nop 1
s_cbranch_vccz L_sprx0o
s_sub_u32 s91,s91,1
s_cbranch_scc0 L_splx0o
s_branch L_sprx0o
L_spx1o:
s_mov_b32 s91,65536
L_splx1o:
lv 48,244,83,0
lv 52,244,83,1024
lv 56,244,83,2048
lv 60,244,83,3072
s_waitcnt vmcnt(0)
v_max_u32_e32 v248,v48,v49
v_max3_u32 v248,v248,v50,v51
v_max3_u32 v248,v248,v52,v53
v_max3_u32 v248,v248,v54,v55
v_max3_u32 v248,v248,v56,v57
v_max3_u32 v248,v248,v58,v59
v_max3_u32 v248,v248,v60,v61
v_max3_u32 v248,v248,v62,v63
v_cmp_eq_u32_e32 vcc,-1,v248
s_nop 1
s_cbranch_vccz L_sprx1o
s_sub_u32 s91,s91,1
s_cbranch_scc0 L_splx1o
s_branch L_sprx1o
L_spx2o:
s_mov_b32 s91,65536
L_splx2o:
lv 80,244,84,0
lv 84,244,84,1024
lv 88,244,84,2048
lv 92,244,84,3072
s_waitcnt vmcnt(0)
v_max_u32_e32 v248,v80,v81
v_max3_u32 v248,v248,v82,v83
v_max3_u32 v248,v248,v84,v85
v_max3_u32 v248,v248,v86,v87
v_max3_u32 v248,v248,v88,v89
v_max3_u32 v248,v248,v90,v91
v_max3_u32 v248,v248,v92,v93
v_max3_u32 v248,v248,v94,v95
v_cmp_eq_u32_e32 vcc,-1,v248
s_nop 1
s_cbranch_vccz L_sprx2o
s_sub_u32 s91,s91,1
s_cbranch_scc0 L_splx2o
s_branch L_sprx2o
L_att_end:
s_endpgm
.p2align 12
.purgem cv
.purgem cvaa
.purgem cvvv
.purgem dl
.purgem ds
.purgem ex
.purgem la
.purgem lv
.purgem maaa
.purgem maav
.purgem mavv
.purgem mvaa
.purgem mvav
.purgem mvva
.purgem mvvv
.purgem ra
.purgem rv
.purgem ta
.purgem tv
.purgem zaaa
.purgem zaav
.purgem zvaa
.purgem zvav
.purgem zvva
.purgem zvvv
)"
        :: "s"(__builtin_amdgcn_kernarg_segment_ptr()), "s"(blockIdx.x), "v"(tid)
        : "v0", "v1", "v2", "v3", "v4", "v5", "v6", "v7", "v8", "v9", "v10", "v11", "v12", "v13", "v14", "v15", "v16", "v17", "v18", "v19", "v20", "v21", "v22", "v23", "v24", "v25", "v26", "v27", "v28", "v29", "v30", "v31", "v32", "v33", "v34", "v35", "v36", "v37", "v38", "v39", "v40", "v41", "v42", "v43", "v44", "v45", "v46", "v47", "v48", "v49", "v50", "v51", "v52", "v53", "v54", "v55", "v56", "v57", "v58", "v59", "v60", "v61", "v62", "v63", "v64", "v65", "v66", "v67", "v68", "v69", "v70", "v71", "v72", "v73", "v74", "v75", "v76", "v77", "v78", "v79", "v80", "v81", "v82", "v83", "v84", "v85", "v86", "v87", "v88", "v89", "v90", "v91", "v92", "v93", "v94", "v95", "v96", "v97", "v98", "v99", "v100", "v101", "v102", "v103", "v104", "v105", "v106", "v107", "v108", "v109", "v110", "v111", "v112", "v113", "v114", "v115", "v116", "v117", "v118", "v119", "v120", "v121", "v122", "v123", "v124", "v125", "v126", "v127", "v128", "v129", "v130", "v131", "v132", "v133", "v134", "v135", "v136", "v137", "v138", "v139", "v140", "v141", "v142", "v143", "v144", "v145", "v146", "v147", "v148", "v149", "v150", "v151", "v152", "v153", "v154", "v155", "v156", "v157", "v158", "v159", "v160", "v161", "v162", "v163", "v164", "v165", "v166", "v167", "v168", "v169", "v170", "v171", "v172", "v173", "v174", "v175", "v176", "v177", "v178", "v179", "v180", "v181", "v182", "v183", "v184", "v185", "v186", "v187", "v188", "v189", "v190", "v191", "v192", "v193", "v194", "v195", "v196", "v197", "v198", "v199", "v200", "v201", "v202", "v203", "v204", "v205", "v206", "v207", "v208", "v209", "v210", "v211", "v212", "v213", "v214", "v215", "v216", "v217", "v218", "v219", "v220", "v221", "v222", "v223", "v224", "v225", "v226", "v227", "v228", "v229", "v230", "v231", "v232", "v233", "v234", "v235", "v236", "v237", "v238", "v239", "v240", "v241", "v242", "v243", "v244", "v245", "v246", "v247", "v248", "v249", "v250", "v251", "v252", "v253", "v254", "a0", "a1", "a2", "a3", "a4", "a5", "a6", "a7", "a8", "a9", "a10", "a11", "a12", "a13", "a14", "a15", "a16", "a17", "a18", "a19", "a20", "a21", "a22", "a23", "a24", "a25", "a26", "a27", "a28", "a29", "a30", "a31", "a32", "a33", "a34", "a35", "a36", "a37", "a38", "a39", "a40", "a41", "a42", "a43", "a44", "a45", "a46", "a47", "a48", "a49", "a50", "a51", "a52", "a53", "a54", "a55", "a56", "a57", "a58", "a59", "a60", "a61", "a62", "a63", "a64", "a65", "a66", "a67", "a68", "a69", "a70", "a71", "a72", "a73", "a74", "a75", "a76", "a77", "a78", "a79", "a80", "a81", "a82", "a83", "a84", "a85", "a86", "a87", "a88", "a89", "a90", "a91", "a92", "a93", "a94", "a95", "a96", "a97", "a98", "a99", "a100", "a101", "a102", "a103", "a104", "a105", "a106", "a107", "a108", "a109", "a110", "a111", "a112", "a113", "a114", "a115", "a116", "a117", "a118", "a119", "a120", "a121", "a122", "a123", "a124", "a125", "a126", "a127", "a128", "a129", "a130", "a131", "a132", "a133", "a134", "a135", "a136", "a137", "a138", "a139", "a140", "a141", "a142", "a143", "a144", "a145", "a146", "a147", "a148", "a149", "a150", "a151", "a152", "a153", "a154", "a155", "a156", "a157", "a158", "a159", "a160", "a161", "a162", "a163", "a164", "a165", "a166", "a167", "a168", "a169", "a170", "a171", "a172", "a173", "a174", "a175", "a176", "a177", "a178", "a179", "a180", "a181", "a182", "a183", "a184", "a185", "a186", "a187", "a188", "a189", "a190", "a191", "a192", "a193", "a194", "a195", "a196", "a197", "a198", "a199", "a200", "a201", "a202", "a203", "a204", "a205", "a206", "a207", "a208", "a209", "a210", "a211", "a212", "a213", "a214", "a215", "a216", "a217", "a218", "a219", "a220", "a221", "a222", "a223", "a224", "a225", "a226", "a227", "a228", "a229", "a230", "a231", "a232", "a233", "a234", "a235", "a236", "a237", "a238", "a239", "a240", "a241", "a242", "a243", "a244", "a245", "a246", "a247", "a248", "a249", "a250", "a251", "a252", "a253", "a254", "a255", "s4", "s5", "s6", "s7", "s8", "s9", "s10", "s11", "s12", "s13", "s14", "s15", "s16", "s17", "s18", "s19", "s20", "s21", "s22", "s23", "s24", "s25", "s26", "s27", "s28", "s29", "s30", "s31", "s32", "s33", "s34", "s35", "s36", "s37", "s38", "s39", "s40", "s41", "s42", "s43", "s44", "s45", "s46", "s47", "s48", "s49", "s50", "s51", "s52", "s53", "s54", "s55", "s56", "s57", "s58", "s59", "s60", "s61", "s62", "s63", "s64", "s65", "s66", "s67", "s68", "s69", "s70", "s71", "s72", "s73", "s74", "s75", "s76", "s77", "s78", "s79", "s80", "s81", "s82", "s83", "s84", "s85", "s86", "s87", "s88", "s89", "s90", "s91", "s92", "s93", "s94", "s95", "s96", "s97", "s98", "s99", "s100", "s101", "vcc", "scc", "m0", "memory");
}

extern "C" void kernel_launch(void* const* d_in, const int* in_sizes, int n_in, void* d_out, int out_size, void* d_ws, size_t ws_size, hipStream_t stream) {
    (void)in_sizes; (void)n_in; (void)out_size; (void)ws_size;
    char* ws = (char*)d_ws + 0ull;
    Args a;
    a.p[0] = d_in[0]; a.p[1] = d_in[1]; a.p[2] = d_in[2]; a.p[3] = d_in[3]; a.p[4] = d_in[4]; a.p[5] = d_in[5]; a.p[6] = d_in[7]; a.p[7] = d_in[8]; a.p[8] = d_in[9]; a.p[9] = d_out; a.p[10] = ws;
    mega<<<256, 256, 0, stream>>>(a);
}
```

```cpp
#include <hip/hip_runtime.h>
#include <hip/hip_bf16.h>
#include <stdint.h>
#include <type_traits>

#define LAS __attribute__((address_space(3)))
struct Args { const void* p[11]; };
__global__ __launch_bounds__(256, 1)
void mega(Args args) {
    __shared__ __attribute__((aligned(1024))) char smem[163840];
    const unsigned tid = threadIdx.x + (unsigned)(uintptr_t)(LAS char*)smem;
    asm volatile(
R"(.macro cv a,b,c
v_cvt_pk_bf16_f32 v\a,v\b,v\c
.endm
.macro cvaa a,b,c,d
v_mfma_f32_16x16x32_bf16 v[\a:\a+3],a[\b:\b+3],a[\c:\c+3],v[\d:\d+3]
.endm
.macro cvvv a,b,c,d
v_mfma_f32_16x16x32_bf16 v[\a:\a+3],v[\b:\b+3],v[\c:\c+3],v[\d:\d+3]
.endm
.macro dl a,b,c,d
buffer_load_dwordx4 v\a,s[\b:\b+3],s\c offen offset:\d lds
.endm
.macro ds a,b,c,d
buffer_load_dwordx4 v\a,s[\b:\b+3],s\c offen offset:\d sc1 lds
.endm
.macro ex a
v_exp_f32_e32 v\a,v\a
.endm
.macro la a,b,c,d
buffer_load_dwordx4 a[\a:\a+3],v\b,s[8:11],s\c offen offset:\d sc1
.endm
.macro lv a,b,c,d
buffer_load_dwordx4 v[\a:\a+3],v\b,s[8:11],s\c offen offset:\d sc1
.endm
.macro maaa a,b,c
v_mfma_f32_16x16x32_bf16 a[\a:\a+3],a[\b:\b+3],a[\c:\c+3],a[\a:\a+3]
.endm
.macro maav a,b,c
v_mfma_f32_16x16x32_bf16 a[\a:\a+3],a[\b:\b+3],v[\c:\c+3],a[\a:\a+3]
.endm
.macro mavv a,b,c
v_mfma_f32_16x16x32_bf16 a[\a:\a+3],v[\b:\b+3],v[\c:\c+3],a[\a:\a+3]
.endm
.macro mvaa a,b,c
v_mfma_f32_16x16x32_bf16 v[\a:\a+3],a[\b:\b+3],a[\c:\c+3],v[\a:\a+3]
.endm
.macro mvav a,b,c
v_mfma_f32_16x16x32_bf16 v[\a:\a+3],a[\b:\b+3],v[\c:\c+3],v[\a:\a+3]
.endm
.macro mvva a,b,c
v_mfma_f32_16x16x32_bf16 v[\a:\a+3],v[\b:\b+3],a[\c:\c+3],v[\a:\a+3]
.endm
.macro mvvv a,b,c
v_mfma_f32_16x16x32_bf16 v[\a:\a+3],v[\b:\b+3],v[\c:\c+3],v[\a:\a+3]
.endm
.macro ra a,b,c
ds_read_b128 a[\a:\a+3],v\b offset:\c
.endm
.macro rv a,b,c
ds_read_b128 v[\a:\a+3],v\b offset:\c
.endm
.macro ta a,b,c
ds_read_b64_tr_b16 a[\a:\a+1],v\b offset:\c
.endm
.macro tv a,b,c
ds_read_b64_tr_b16 v[\a:\a+1],v\b offset:\c
.endm
.macro zaaa a,b,c
v_mfma_f32_16x16x32_bf16 a[\a:\a+3],a[\b:\b+3],a[\c:\c+3],0
.endm
.macro zaav a,b,c
v_mfma_f32_16x16x32_bf16 a[\a:\a+3],a[\b:\b+3],v[\c:\c+3],0
.endm
.macro zvaa a,b,c
v_mfma_f32_16x16x32_bf16 v[\a:\a+3],a[\b:\b+3],a[\c:\c+3],0
.endm
.macro zvav a,b,c
v_mfma_f32_16x16x32_bf16 v[\a:\a+3],a[\b:\b+3],v[\c:\c+3],0
.endm
.macro zvva a,b,c
v_mfma_f32_16x16x32_bf16 v[\a:\a+3],v[\b:\b+3],a[\c:\c+3],0
.endm
.macro zvvv a,b,c
v_mfma_f32_16x16x32_bf16 v[\a:\a+3],v[\b:\b+3],v[\c:\c+3],0
.endm
L_begin:
s_load_dwordx2 s[28:29],%0,72
s_load_dwordx16 s[68:83],%0,0
s_load_dwordx2 s[84:85],%0,64
s_load_dwordx2 s[8:9],%0,80
v_lshrrev_b32_e32 v246,6,%2
v_and_b32_e32 v247,63,%2
v_lshlrev_b32_e32 v244,4,v247
v_readfirstlane_b32 s12,v246
s_and_b32 s20,%1,7
s_lshr_b32 s21,%1,3
s_getpc_b64 s[96:97]
L_att_pc:
s_lshl_b32 s86,s21,2
s_add_u32 s86,s86,s12
s_mul_i32 s86,s86,256
v_lshl_add_u32 v246,v247,7,s86
v_min_u32_e32 v246,L_att_end-L_att_pc-4,v246
s_mov_b64 exec,3
global_load_dword a188,v246,s[96:97]
s_mov_b64 exec,-1
s_and_b32 s19,s21,7
s_lshr_b32 s86,s21,3
s_lshl_b32 s18,s20,2
s_add_u32 s18,s18,s86
v_lshrrev_b32_e32 v248,3,v247
v_and_b32_e32 v250,112,v244
v_and_b32_e32 v249,48,v247
v_xor_b32_e32 v249,v249,v250
v_and_b32_e32 v251,16,v247
v_lshlrev_b32_e32 v251,1,v251
v_xor_b32_e32 v250,v250,v251
v_lshl_add_u32 v224,v248,10,v249
v_xor_b32_e32 v225,64,v224
v_add_u32_e32 v227,21504,v225
v_add_u32_e32 v226,14336,v224
v_add_u32_e32 v225,7168,v225
v_lshl_add_u32 v228,v248,13,v250
v_xor_b32_e32 v229,64,v228
v_add_u32_e32 v231,193536,v229
v_add_u32_e32 v230,129024,v228
v_add_u32_e32 v229,64512,v229
s_lshl_b32 s86,s19,2
s_add_u32 s86,s86,s12
s_lshl_b32 s52,s86,16
s_bfe_u32 s87,s21,65539
s_lshl_b32 s88,s20,1
s_add_u32 s87,s87,s88
s_lshl_b32 s87,s87,2
s_add_u32 s87,s87,s12
s_lshl_b32 s53,s87,7
s_mov_b32 s54,s53
s_mov_b32 s39,131072
s_mov_b32 s43,131072
s_mov_b32 s47,131072
s_mov_b32 s51,131072
s_mov_b32 s38,2097152
s_mov_b32 s42,4194304
s_mov_b32 s46,4194304
s_mov_b32 s56,32768
s_mov_b32 s60,-32640
s_mov_b32 s57,262144
s_mov_b32 s58,262144
s_bfe_u32 s91,s21,65536
s_lshl_b32 s91,s91,18
s_add_u32 s53,s53,s91
s_mov_b32 s54,s53
s_lshl_b32 s91,s91,1
s_sub_u32 s57,s57,s91
s_add_u32 s58,s58,s91
s_lshl_b32 s98,s12,13
s_lshl_b32 s62,s12,12
s_add_u32 s62,s62,98304
s_lshl_b32 s35,%1,2
s_add_u32 s35,s35,83896320
v_lshrrev_b32_e32 v48,2,v247
v_and_b32_e32 v49,3,v247
v_lshlrev_b32_e32 v49,7,v49
v_lshl_add_u32 v48,v48,13,v49
s_lshl_b32 s89,s86,14
s_and_b32 s90,s53,7680
s_add_u32 s90,s90,s89
s_lshr_b32 s89,s21,3
s_lshl_b32 s89,s89,2
s_add_u32 s89,s89,s12
s_lshl_b32 s89,s89,14
s_lshl_b32 s91,s19,18
s_add_u32 s89,s89,s91
s_cmp_ge_u32 s21,16
s_waitcnt lgkmcnt(0)
s_mov_b64 s[36:37],s[70:71]
s_cselect_b64 s[40:41],s[76:77],s[74:75]
s_mov_b64 s[44:45],s[40:41]
s_bfm_b64 exec,8,0
buffer_load_dword a189,v48,s[40:43],s90 offen
s_mov_b64 exec,-1
s_and_b32 s9,s9,65535
s_mov_b32 s10,83951616
s_mov_b32 s11,131072
buffer_load_dword v44,off,s[8:11],s35 sc1
buffer_load_dword v45,off,s[8:11],s35 offset:1024 sc1
s_add_u32 m0,s62,0
s_nop 0
buffer_load_dwordx4 v228,s[40:43],s53 offen offset:0 nt lds
buffer_load_dwordx4 v229,s[40:43],s53 offen offset:1024 nt lds
s_add_u32 s53,s53,s57
buffer_load_dwordx4 v230,s[44:47],s54 offen offset:2048 nt lds
buffer_load_dwordx4 v231,s[44:47],s54 offen offset:3072 nt lds
s_add_u32 s54,s54,s57
s_add_u32 m0,s98,0
s_nop 0
dl 224,36,52,0
dl 225,36,52,1024
dl 226,36,52,2048
dl 227,36,52,3072
s_add_u32 s52,s52,s56
s_add_u32 m0,s98,4096
s_nop 0
dl 224,36,52,0
dl 225,36,52,1024
dl 226,36,52,2048
dl 227,36,52,3072
s_add_u32 s52,s52,s60
s_add_u32 s61,s98,65536
s_mov_b32 s63,0
s_lshl_b32 s88,%1,2
s_add_u32 s88,s88,s12
s_lshl_b32 s88,s88,12
s_add_u32 s30,s68,s88
s_addc_u32 s31,s69,0
v_mov_b32_e32 v40,v247
v_and_b32_e32 v246,15,v40
v_lshlrev_b32_e32 v248,3,v40
v_xor_b32_e32 v248,v248,v40
v_and_b32_e32 v248,112,v248
v_lshl_add_u32 v232,v246,7,v248
s_and_b32 s86,s98,16384
v_add_u32_e32 v232,s86,v232
s_bfe_u32 s86,s21,65536
s_lshl_b32 s86,s86,6
v_xor_b32_e32 v232,s86,v232
v_xor_b32_e32 v233,64,v232
v_and_b32_e32 v246,12,v40
v_lshlrev_b32_e32 v246,5,v246
v_and_b32_e32 v248,3,v40
v_lshl_add_u32 v246,v248,3,v246
v_and_b32_e32 v248,24,v40
v_lshl_add_u32 v246,v248,2,v246
s_and_b32 s87,s98,8192
s_add_u32 s87,s87,98304
v_add_u32_e32 v246,s87,v246
v_and_b32_e32 v248,48,v40
v_lshl_add_u32 v234,v248,6,v246
v_xor_b32_e32 v235,32,v234
v_xor_b32_e32 v236,64,v234
v_xor_b32_e32 v237,96,v234
s_lshl_b32 s86,s19,1
s_add_u32 s86,s86,2
s_lshl_b32 s15,1,s86
s_xor_b32 s87,s19,15
s_lshl_b32 s22,s87,7
s_lshl_b32 s23,s19,7
s_lshl_b32 s87,s87,2
s_lshl_b32 s88,s19,2
s_add_u32 s87,s87,s12
s_add_u32 s88,s88,s12
s_lshl_b32 s90,s18,13
s_add_u32 s90,s90,62914560
s_mul_i32 s14,s87,327680
s_add_u32 s14,s14,s90
s_mul_i32 s13,s88,327680
s_add_u32 s13,s13,s90
s_mul_i32 s90,s18,524288
s_lshl_b32 s91,s12,12
s_add_u32 s90,s90,s91
s_add_u32 s90,s90,16777216
v_add_u32_e32 v249,s90,v244
v_add_u32_e32 v255,16777216,v249
v_accvgpr_write_b32 a240,v249
v_accvgpr_write_b32 a241,v255
s_or_b32 s86,%1,s12
s_cselect_b32 s65,0,4
v_lshrrev_b32_e32 v241,3,v40
v_lshlrev_b32_e32 v241,27,v241
v_lshl_add_u32 v241,v40,7,v241
s_cmp_lt_u32 s12,2
s_cselect_b64 s[48:49],s[72:73],s[80:81]
s_cselect_b32 s50,8,6
s_lshl_b32 s50,s50,20
s_and_b32 s86,s12,1
s_lshl_b32 s87,%1,1
s_add_u32 s86,s86,s87
s_lshl_b32 s55,s86,10
s_mov_b32 s59,524288
s_cmp_lt_u32 s12,2
s_cselect_b32 s87,10,7
v_cmp_gt_u32_e32 vcc,s87,v40
v_lshlrev_b32_e32 v241,7,v40
v_mov_b32_e32 v41,134217728
s_lshl_b32 s87,s87,7
s_mul_i32 s55,s86,s87
s_lshl_b32 s59,s87,9
v_cndmask_b32_e32 v241,v41,v241,vcc
s_cmp_lt_u32 s12,2
s_cbranch_scc1 L_lx
s_cmp_eq_u32 s12,2
s_cselect_b32 s87,14,10
v_cmp_gt_u32_e32 vcc,s87,v40
v_lshlrev_b32_e32 v241,7,v40
s_mul_i32 s55,%1,1792
v_cndmask_b32_e32 v241,v41,v241,vcc
s_cmp_eq_u32 s12,2
s_cbranch_scc1 L_lx
s_lshl_b32 s55,%1,14
s_add_u32 s86,s55,16384
s_mov_b64 s[48:49],s[68:69]
s_mov_b32 s50,s86
s_mov_b32 s55,s55
s_mov_b32 s59,1280
L_lx:
v_and_b32_e32 v246,15,v40
v_and_b32_e32 v248,48,v40
v_lshlrev_b32_e32 v249,2,v40
v_and_b32_e32 v249,32,v249
v_xor_b32_e32 v248,v248,v249
v_lshl_add_u32 v240,v246,6,v248
s_lshr_b32 s86,s12,1
s_lshl_b32 s86,s86,10
s_add_u32 s86,s86,112640
v_add_u32_e32 v240,s86,v240
v_and_b32_e32 v246,12,v40
v_lshlrev_b32_e32 v246,5,v246
v_and_b32_e32 v248,3,v40
v_lshl_add_u32 v246,v248,3,v246
v_and_b32_e32 v248,24,v40
v_lshl_add_u32 v246,v248,2,v246
v_add_u32_e32 v246,98304,v246
v_and_b32_e32 v248,32,v40
v_lshl_add_u32 v238,v248,5,v246
v_xor_b32_e32 v239,32,v238
v_lshrrev_b32_e32 v248,3,v40
v_and_b32_e32 v250,112,v244
v_and_b32_e32 v249,48,v40
v_xor_b32_e32 v249,v249,v250
v_and_b32_e32 v251,16,v40
v_lshlrev_b32_e32 v251,1,v251
v_xor_b32_e32 v250,v250,v251
v_lshl_add_u32 v48,v248,11,v249
v_xor_b32_e32 v49,64,v48
v_add_u32_e32 v51,46080,v49
v_add_u32_e32 v50,30720,v48
v_add_u32_e32 v49,15360,v49
v_mov_b32_e32 v52,v228
v_mov_b32_e32 v53,v229
v_mov_b32_e32 v54,v230
v_mov_b32_e32 v55,v231
s_cmp_lt_u32 s12,2
s_cbranch_scc1 L_pqx
v_mul_u32_u24_e32 v246,6144,v248
v_and_b32_e32 v251,48,v250
v_add_u32_e32 v246,v246,v251
v_and_b32_e32 v251,64,v250
v_lshl_add_u32 v52,v251,5,v246
v_xor_b32_e32 v252,64,v251
v_lshl_add_u32 v53,v252,5,v246
v_add_u32_e32 v55,144384,v53
v_add_u32_e32 v54,96256,v52
v_add_u32_e32 v53,48128,v53
s_cmp_eq_u32 s12,2
s_cbranch_scc1 L_pqx
v_mul_u32_u24_e32 v251,768,v251
v_add_u32_e32 v246,v246,v251
v_add_u32_e32 v52,4096,v246
v_add_u32_e32 v53,101376,v246
v_lshrrev_b32_e32 v252,2,v40
v_and_b32_e32 v253,32,v40
v_and_b32_e32 v250,48,v244
v_xor_b32_e32 v253,v253,v250
v_lshl_add_u32 v246,v252,11,v253
s_lshl_b32 s86,s22,11
s_sub_u32 s86,s86,34816
v_add_u32_e32 v54,s86,v246
s_lshl_b32 s86,s23,11
s_sub_u32 s86,s86,35840
v_add_u32_e32 v55,s86,v246
v_max_i32_e32 v55,0,v55
L_pqx:
v_accvgpr_write_b32 a242,v48
v_accvgpr_write_b32 a243,v49
v_accvgpr_write_b32 a244,v50
v_accvgpr_write_b32 a245,v51
v_accvgpr_write_b32 a246,v52
v_accvgpr_write_b32 a247,v53
v_accvgpr_write_b32 a248,v54
v_accvgpr_write_b32 a249,v55
s_add_u32 m0,s62,16384
s_nop 0
buffer_load_dwordx4 v228,s[40:43],s53 offen offset:0 nt lds
buffer_load_dwordx4 v229,s[40:43],s53 offen offset:1024 nt lds
s_add_u32 s53,s53,s58
buffer_load_dwordx4 v230,s[44:47],s54 offen offset:2048 nt lds
buffer_load_dwordx4 v231,s[44:47],s54 offen offset:3072 nt lds
s_add_u32 s54,s54,s58
s_add_u32 m0,s98,32768
s_nop 0
dl 224,36,52,0
dl 225,36,52,1024
dl 226,36,52,2048
dl 227,36,52,3072
s_add_u32 s52,s52,s56
s_waitcnt vmcnt(20)
s_cmp_lg_u32 s12,0
s_cbranch_scc1 L_rqx
v_readfirstlane_b32 s32,v44
v_readfirstlane_b32 s86,v45
s_add_u32 s32,s32,1
s_cmp_eq_u32 s32,s86
s_addc_u32 s32,s32,0
v_mov_b32_e32 v44,s32
v_mov_b32_e32 v45,0
v_lshlrev_b32_e32 v46,3,v244
v_mov_b32_e32 v47,0
s_lshl_b32 s86,%1,7
s_add_u32 s87,s86,83914752
s_and_b32 s86,s86,16383
s_add_u32 s86,s86,83898368
s_mov_b64 exec,1
buffer_store_dword v44,off,s[8:11],s35 sc0 sc1
v_mov_b32_e32 v48,0
v_mov_b32_e32 v49,0
buffer_store_dwordx4 v[46:49],off,s[8:11],s86 sc0 sc1
buffer_store_dwordx4 v[46:49],off,s[8:11],s87 sc0 sc1
s_cmp_lg_u32 %1,0
s_cbranch_scc1 L_rqy
s_mov_b64 exec,65535
s_mov_b32 s86,83894272
buffer_store_dword v45,v46,s[8:11],s86 offen sc0 sc1
L_rqy:
s_mov_b64 exec,-1
L_rqx:
L_vr0:
s_cmp_ge_u32 s21,16
s_cbranch_scc1 L_vtile
s_waitcnt vmcnt(8)
s_barrier
ta 64,234,0
ta 66,234,512
ra 0,232,0
ta 68,235,0
ta 70,235,512
ra 4,232,2048
ra 8,232,4096
ra 12,232,6144
ra 16,232,8192
ra 20,232,10240
ra 24,232,12288
ra 28,232,14336
s_add_u32 m0,s62,32768
v_mov_b32_e32 v216,0
buffer_load_dwordx4 v228,s[40:43],s53 offen offset:0 nt lds
v_mov_b32_e32 v217,0
v_mov_b32_e32 v218,0
v_mov_b32_e32 v219,0
v_mov_b32_e32 v220,0
v_mov_b32_e32 v221,0
v_mov_b32_e32 v222,0
v_mov_b32_e32 v223,0
v_mov_b32_e32 v212,0
v_mov_b32_e32 v213,0
v_mov_b32_e32 v214,0
v_mov_b32_e32 v215,0
v_mov_b32_e32 v208,0
v_mov_b32_e32 v209,0
v_mov_b32_e32 v210,0
v_mov_b32_e32 v211,0
v_mov_b32_e32 v152,0
s_waitcnt lgkmcnt(9)
zaaa 128,64,0
s_mov_b32 s63,32768
ta 72,236,0
v_mov_b32_e32 v153,0
s_waitcnt lgkmcnt(8)
zaaa 132,68,0
s_mov_b32 s61,s98
ta 74,236,512
v_mov_b32_e32 v154,0
s_waitcnt lgkmcnt(8)
zaaa 140,68,4
s_mov_b32 s64,1
ta 76,237,0
ta 78,237,512
zaaa 136,64,4
buffer_load_dwordx4 v229,s[40:43],s53 offen offset:1024 nt lds
s_waitcnt lgkmcnt(4)
zvaa 32,64,8
s_add_u32 s53,s53,s57
v_mov_b32_e32 v155,0
v_mov_b32_e32 v156,0
zvaa 36,68,8
v_mov_b32_e32 v157,0
v_mov_b32_e32 v158,0
zvaa 44,68,12
ta 80,234,4096
v_mov_b32_e32 v159,0
zvaa 40,64,12
buffer_load_dwordx4 v230,s[44:47],s54 offen offset:2048 nt lds
zvaa 96,64,16
ta 82,234,4608
v_mov_b32_e32 v148,0
zvaa 100,68,16
ta 84,235,4096
v_mov_b32_e32 v149,0
zvaa 108,68,20
ta 86,235,4608
v_mov_b32_e32 v150,0
zvaa 104,64,20
buffer_load_dwordx4 v231,s[44:47],s54 offen offset:3072 nt lds
zvaa 160,64,24
s_add_u32 m0,s98,36864
ra 32,233,0
s_add_u32 s54,s54,s57
zvaa 164,68,24
ra 36,233,2048
v_mov_b32_e32 v151,0
zvaa 172,68,28
ra 40,233,4096
v_mov_b32_e32 v144,0
zvaa 168,64,28
dl 224,36,52,0
s_waitcnt lgkmcnt(7)
zvaa 184,72,28
ra 44,233,6144
v_mov_b32_e32 v145,0
zvaa 188,76,28
ra 48,233,8192
v_mov_b32_e32 v146,0
zvaa 180,76,24
ra 52,233,10240
v_mov_b32_e32 v147,0
zvaa 176,72,24
dl 225,36,52,1024
zvaa 120,72,20
ra 56,233,12288
v_mov_b32_e32 v88,0
zvaa 124,76,20
ra 60,233,14336
v_mov_b32_e32 v89,0
zvaa 116,76,16
v_mov_b32_e32 v90,0
v_mov_b32_e32 v91,0
zvaa 112,72,16
dl 226,36,52,2048
zvaa 56,72,12
s_waitcnt vmcnt(11)
s_barrier
zvaa 60,76,12
ta 96,234,16384
v_mov_b32_e32 v92,0
zvaa 52,76,8
ta 98,234,16896
v_mov_b32_e32 v93,0
zvaa 48,72,8
dl 227,36,52,3072
zaaa 152,72,4
s_add_u32 m0,s62,49152
ta 100,235,16384
s_add_u32 s52,s52,s60
zaaa 156,76,4
ta 102,235,16896
v_mov_b32_e32 v94,0
zaaa 148,76,0
ta 104,236,16384
v_mov_b32_e32 v95,0
zaaa 144,72,0
buffer_load_dwordx4 v228,s[40:43],s53 offen offset:0 nt lds
s_waitcnt lgkmcnt(5)
zvaa 0,80,0
ta 106,236,16896
v_mov_b32_e32 v84,0
zvaa 4,84,0
ta 108,237,16384
v_mov_b32_e32 v85,0
zvaa 12,84,4
ta 110,237,16896
v_mov_b32_e32 v86,0
zvaa 8,80,4
buffer_load_dwordx4 v229,s[40:43],s53 offen offset:1024 nt lds
zvaa 64,80,8
s_add_u32 s53,s53,s58
ta 112,234,20480
v_mov_b32_e32 v87,0
zvaa 68,84,8
ta 114,234,20992
v_mov_b32_e32 v80,0
zvaa 76,84,12
ta 116,235,20480
v_mov_b32_e32 v81,0
zvaa 72,80,12
buffer_load_dwordx4 v230,s[44:47],s54 offen offset:2048 nt lds
zvaa 128,80,16
ta 118,235,20992
v_mov_b32_e32 v82,0
zvaa 132,84,16
ta 208,236,4096
v_mov_b32_e32 v83,0
zvaa 140,84,20
ta 210,236,4608
v_mov_b32_e32 v24,0
zvaa 136,80,20
buffer_load_dwordx4 v231,s[44:47],s54 offen offset:3072 nt lds
zvaa 192,80,24
s_add_u32 m0,s98,65536
ta 216,237,4096
v_mov_b32_e32 v25,0
zvaa 196,84,24
s_add_u32 s54,s54,s58
ta 218,237,4608
v_mov_b32_e32 v26,0
zvaa 204,84,28
ta 212,236,20480
v_mov_b32_e32 v27,0
zvaa 200,80,28
dl 224,36,52,0
s_waitcnt lgkmcnt(7)
maaa 128,96,32
ta 214,236,20992
v_mov_b32_e32 v28,0
maaa 132,100,32
ta 220,237,20480
v_mov_b32_e32 v29,0
maaa 140,100,36
ta 222,237,20992
v_mov_b32_e32 v30,0
maaa 136,96,36
dl 225,36,52,1024
mvaa 32,96,40
v_mov_b32_e32 v31,0
v_mov_b32_e32 v20,0
mvaa 36,100,40
v_mov_b32_e32 v21,0
v_mov_b32_e32 v22,0
mvaa 44,100,44
v_mov_b32_e32 v23,0
v_mov_b32_e32 v16,0
mvaa 40,96,44
dl 226,36,52,2048
mvaa 96,96,48
v_mov_b32_e32 v17,0
v_mov_b32_e32 v18,0
mvaa 100,100,48
v_mov_b32_e32 v19,0
v_add_u32_e32 v232,32768,v232
mvaa 108,100,52
v_add_u32_e32 v233,32768,v233
mvaa 104,96,52
dl 227,36,52,3072
mvaa 160,96,56
s_add_u32 s52,s52,s56
s_add_u32 m0,s98,69632
mvaa 164,100,56
mvaa 172,100,60
mvaa 168,96,60
dl 224,36,52,0
mvaa 184,104,60
mvaa 188,108,60
mvaa 180,108,56
mvaa 176,104,56
dl 225,36,52,1024
mvaa 120,104,52
mvaa 124,108,52
mvaa 116,108,48
mvaa 112,104,48
dl 226,36,52,2048
mvaa 56,104,44
mvaa 60,108,44
mvaa 52,108,40
mvaa 48,104,40
dl 227,36,52,3072
maaa 152,104,36
s_add_u32 s52,s52,s60
maaa 156,108,36
maaa 148,108,32
maaa 144,104,32
mvaa 0,112,32
s_waitcnt lgkmcnt(0)
mvaa 4,116,32
mvaa 12,116,36
mvaa 8,112,36
mvaa 64,112,40
mvaa 68,116,40
mvaa 76,116,44
mvaa 72,112,44
mvaa 128,112,48
mvaa 132,116,48
mvaa 140,116,52
mvaa 136,112,52
mvaa 192,112,56
mvaa 196,116,56
mvaa 204,116,60
mvaa 200,112,60
s_waitcnt vmcnt(8)
s_waitcnt lgkmcnt(0)
s_barrier
L_k_head:
mvaa 216,208,28
ra 64,232,0
mvaa 216,212,60
s_add_u32 m0,s62,0
ta 192,234,32768
mvaa 220,220,60
buffer_load_dwordx4 v228,s[40:43],s53 offen offset:0 nt lds
mvaa 220,216,28
ta 194,234,33280
mvaa 212,216,24
ra 96,233,0
mvaa 212,220,56
ta 196,234,49152
mvaa 208,212,56
ta 198,234,49664
mvaa 208,208,24
ta 204,235,49152
mvaa 152,208,20
buffer_load_dwordx4 v229,s[40:43],s53 offen offset:1024 nt lds
mvaa 152,212,52
s_add_u32 s53,s53,s57
ta 206,235,49664
mvaa 156,220,52
ta 200,235,32768
mvaa 156,216,20
ta 202,235,33280
mvaa 148,216,16
ra 68,232,2048
mvaa 148,220,48
ra 100,233,2048
mvaa 144,212,48
ra 72,232,4096
mvaa 144,208,16
buffer_load_dwordx4 v230,s[44:47],s54 offen offset:2048 nt lds
mvaa 88,208,12
ra 104,233,4096
mvaa 88,212,44
ra 76,232,6144
mvaa 92,220,44
ra 108,233,6144
mvaa 92,216,12
ra 80,232,8192
mvaa 84,216,8
ra 112,233,8192
mvaa 84,220,40
buffer_load_dwordx4 v231,s[44:47],s54 offen offset:3072 nt lds
mvaa 80,212,40
s_add_u32 s54,s54,s57
ra 84,232,10240
mvaa 80,208,8
ra 116,233,10240
mvaa 24,208,4
ra 88,232,12288
mvaa 24,212,36
ra 120,233,12288
mvaa 28,220,36
ra 92,232,14336
mvaa 28,216,4
s_add_u32 m0,s62,16384
ra 124,233,14336
mvaa 20,216,0
buffer_load_dwordx4 v228,s[40:43],s53 offen offset:0 nt lds
mvaa 20,220,32
mvaa 16,212,32
mvaa 16,208,0
s_waitcnt lgkmcnt(8)
maaa 128,192,64
maaa 128,196,96
maaa 132,204,96
buffer_load_dwordx4 v229,s[40:43],s53 offen offset:1024 nt lds
maaa 132,200,64
s_add_u32 s53,s53,s58
maaa 140,200,68
maaa 140,204,100
maaa 136,196,100
maaa 136,192,68
mvaa 32,192,72
mvaa 32,196,104
buffer_load_dwordx4 v230,s[44:47],s54 offen offset:2048 nt lds
mvaa 36,204,104
mvaa 36,200,72
s_waitcnt lgkmcnt(0)
mvaa 44,200,76
ta 208,236,32768
mvaa 44,204,108
ta 210,236,33280
mvaa 40,196,108
ta 212,236,49152
mvaa 40,192,76
buffer_load_dwordx4 v231,s[44:47],s54 offen offset:3072 nt lds
mvaa 96,192,80
s_add_u32 s54,s54,s58
ta 214,236,49664
mvaa 96,196,112
ta 220,237,49152
mvaa 100,204,112
ta 222,237,49664
mvaa 100,200,80
ta 216,237,32768
mvaa 108,200,84
ta 218,237,33280
mvaa 108,204,116
mvaa 104,196,116
buffer_load_dword a184,v241,s[48:51],s55 offen
mvaa 104,192,84
s_add_u32 s55,s55,s59
mvaa 160,192,88
mvaa 160,196,120
mvaa 164,204,120
mvaa 164,200,88
mvaa 172,200,92
s_add_u32 m0,s61,0
mvaa 172,204,124
dl 224,36,52,0
mvaa 168,196,124
mvaa 168,192,92
s_waitcnt lgkmcnt(0)
mvaa 184,208,92
mvaa 184,212,124
mvaa 188,220,124
ta 192,234,36864
mvaa 188,216,92
ta 194,234,37376
mvaa 180,216,88
dl 225,36,52,1024
mvaa 180,220,120
ta 196,234,53248
mvaa 176,212,120
ta 198,234,53760
mvaa 176,208,88
ta 204,235,53248
mvaa 120,208,84
ta 206,235,53760
mvaa 120,212,116
ta 200,235,36864
mvaa 124,220,116
ta 202,235,37376
mvaa 124,216,84
dl 226,36,52,2048
mvaa 116,216,80
mvaa 116,220,112
mvaa 112,212,112
mvaa 112,208,80
mvaa 56,208,76
mvaa 56,212,108
mvaa 60,220,108
dl 227,36,52,3072
mvaa 60,216,76
s_add_u32 s52,s52,s56
mvaa 52,216,72
mvaa 52,220,104
mvaa 48,212,104
mvaa 48,208,72
maaa 152,208,68
maaa 152,212,100
buffer_load_dword a185,v241,s[48:51],s55 offen
maaa 156,220,100
s_add_u32 s55,s55,s59
maaa 156,216,68
maaa 148,216,64
maaa 148,220,96
maaa 144,212,96
s_waitcnt lgkmcnt(0)
maaa 144,208,64
s_add_u32 m0,s61,4096
mvaa 0,192,64
dl 224,36,52,0
mvaa 0,196,96
s_add_u32 s61,s63,s98
mvaa 4,204,96
ta 208,236,36864
mvaa 4,200,64
ta 210,236,37376
mvaa 12,200,68
ta 212,236,53248
mvaa 12,204,100
ta 214,236,53760
mvaa 8,196,100
ta 220,237,53248
mvaa 8,192,68
ta 222,237,53760
mvaa 64,192,72
dl 225,36,52,1024
mvaa 64,196,104
ta 216,237,36864
mvaa 68,204,104
ta 218,237,37376
mvaa 68,200,72
s_cmp_ge_u32 s63,65536
s_cselect_b32 s99,-2,1
s_lshl_b32 s99,s99,15
mvaa 76,200,76
s_add_u32 s63,s63,s99
v_add_u32_e32 v232,s99,v232
v_add_u32_e32 v233,s99,v233
mvaa 76,204,108
mvaa 72,196,108
mvaa 72,192,76
dl 226,36,52,2048
mvaa 128,192,80
mvaa 128,196,112
mvaa 132,204,112
mvaa 132,200,80
mvaa 140,200,84
mvaa 140,204,116
mvaa 136,196,116
dl 227,36,52,3072
mvaa 136,192,84
s_add_u32 s52,s52,s60
mvaa 192,192,88
mvaa 192,196,120
mvaa 196,204,120
mvaa 196,200,88
mvaa 204,200,92
mvaa 204,204,124
s_cmp_eq_u32 s64,3
mvaa 200,196,124
s_waitcnt vmcnt(10) lgkmcnt(0)
s_barrier
mvaa 200,192,92
s_cbranch_scc1 L_k_exit
mvaa 216,208,92
ra 0,232,0
mvaa 216,212,124
s_add_u32 m0,s62,32768
ta 192,234,0
mvaa 220,220,124
buffer_load_dwordx4 v228,s[40:43],s53 offen offset:0 nt lds
mvaa 220,216,92
ta 194,234,512
mvaa 212,216,88
ra 32,233,0
mvaa 212,220,120
ta 196,234,16384
mvaa 208,212,120
ta 198,234,16896
mvaa 208,208,88
ta 204,235,16384
mvaa 152,208,84
buffer_load_dwordx4 v229,s[40:43],s53 offen offset:1024 nt lds
mvaa 152,212,116
s_add_u32 s53,s53,s57
ta 206,235,16896
mvaa 156,220,116
ta 200,235,0
mvaa 156,216,84
ta 202,235,512
mvaa 148,216,80
ra 4,232,2048
mvaa 148,220,112
ra 36,233,2048
mvaa 144,212,112
ra 8,232,4096
mvaa 144,208,80
buffer_load_dwordx4 v230,s[44:47],s54 offen offset:2048 nt lds
mvaa 88,208,76
ra 40,233,4096
mvaa 88,212,108
ra 12,232,6144
mvaa 92,220,108
ra 44,233,6144
mvaa 92,216,76
ra 16,232,8192
mvaa 84,216,72
ra 48,233,8192
mvaa 84,220,104
buffer_load_dwordx4 v231,s[44:47],s54 offen offset:3072 nt lds
mvaa 80,212,104
s_add_u32 s54,s54,s57
ra 20,232,10240
mvaa 80,208,72
ra 52,233,10240
mvaa 24,208,68
ra 24,232,12288
mvaa 24,212,100
ra 56,233,12288
mvaa 28,220,100
ra 28,232,14336
mvaa 28,216,68
s_add_u32 m0,s62,49152
ra 60,233,14336
mvaa 20,216,64
buffer_load_dwordx4 v228,s[40:43],s53 offen offset:0 nt lds
mvaa 20,220,96
mvaa 16,212,96
mvaa 16,208,64
s_waitcnt lgkmcnt(8)
maaa 128,192,0
maaa 128,196,32
maaa 132,204,32
buffer_load_dwordx4 v229,s[40:43],s53 offen offset:1024 nt lds
maaa 132,200,0
s_add_u32 s53,s53,s58
maaa 140,200,4
maaa 140,204,36
maaa 136,196,36
maaa 136,192,4
mvaa 32,192,8
mvaa 32,196,40
buffer_load_dwordx4 v230,s[44:47],s54 offen offset:2048 nt lds
mvaa 36,204,40
mvaa 36,200,8
s_waitcnt lgkmcnt(0)
mvaa 44,200,12
ta 208,236,0
mvaa 44,204,44
ta 210,236,512
mvaa 40,196,44
ta 212,236,16384
mvaa 40,192,12
buffer_load_dwordx4 v231,s[44:47],s54 offen offset:3072 nt lds
mvaa 96,192,16
s_add_u32 s54,s54,s58
ta 214,236,16896
mvaa 96,196,48
ta 220,237,16384
mvaa 100,204,48
ta 222,237,16896
mvaa 100,200,16
ta 216,237,0
mvaa 108,200,20
)"
R"(ta 218,237,512
mvaa 108,204,52
mvaa 104,196,52
buffer_load_dword a186,v241,s[48:51],s55 offen
mvaa 104,192,20
s_add_u32 s55,s55,s59
mvaa 160,192,24
s_lshl_b32 s86,s21,2
s_add_u32 s86,s86,s12
s_mul_i32 s86,s86,384
mvaa 160,196,56
s_add_u32 s86,s86,4096
v_lshl_add_u32 v253,v244,3,s86
mvaa 164,204,56
s_mov_b64 exec,7
global_load_dword a188,v253,s[96:97]
s_mov_b64 exec,-1
mvaa 164,200,24
mvaa 172,200,28
s_add_u32 m0,s61,0
mvaa 172,204,60
dl 224,36,52,0
mvaa 168,196,60
mvaa 168,192,28
s_waitcnt lgkmcnt(0)
mvaa 184,208,28
mvaa 184,212,60
mvaa 188,220,60
ta 192,234,4096
mvaa 188,216,28
ta 194,234,4608
mvaa 180,216,24
dl 225,36,52,1024
mvaa 180,220,56
ta 196,234,20480
mvaa 176,212,56
ta 198,234,20992
mvaa 176,208,24
ta 204,235,20480
mvaa 120,208,20
ta 206,235,20992
mvaa 120,212,52
ta 200,235,4096
mvaa 124,220,52
ta 202,235,4608
mvaa 124,216,20
dl 226,36,52,2048
mvaa 116,216,16
mvaa 116,220,48
mvaa 112,212,48
mvaa 112,208,16
mvaa 56,208,12
mvaa 56,212,44
s_bitcmp1_b32 s65,s64
s_cbranch_scc1 L_k_ev
L_k_evr:
mvaa 60,220,44
dl 227,36,52,3072
mvaa 60,216,12
s_add_u32 s52,s52,s56
mvaa 52,216,8
mvaa 52,220,40
mvaa 48,212,40
mvaa 48,208,8
maaa 152,208,4
maaa 152,212,36
buffer_load_dword a187,v241,s[48:51],s55 offen
maaa 156,220,36
s_add_u32 s55,s55,s59
maaa 156,216,4
maaa 148,216,0
maaa 148,220,32
maaa 144,212,32
s_waitcnt lgkmcnt(0)
maaa 144,208,0
s_add_u32 m0,s61,4096
mvaa 0,192,0
dl 224,36,52,0
mvaa 0,196,32
s_add_u32 s61,s63,s98
mvaa 4,204,32
ta 208,236,4096
mvaa 4,200,0
ta 210,236,4608
mvaa 12,200,4
ta 212,236,20480
mvaa 12,204,36
ta 214,236,20992
mvaa 8,196,36
ta 220,237,20480
mvaa 8,192,4
ta 222,237,20992
mvaa 64,192,8
dl 225,36,52,1024
mvaa 64,196,40
ta 216,237,4096
mvaa 68,204,40
ta 218,237,4608
mvaa 68,200,8
s_cmp_ge_u32 s63,65536
s_cselect_b32 s99,-2,1
s_lshl_b32 s99,s99,15
mvaa 76,200,12
s_add_u32 s63,s63,s99
v_add_u32_e32 v232,s99,v232
v_add_u32_e32 v233,s99,v233
mvaa 76,204,44
mvaa 72,196,44
mvaa 72,192,12
dl 226,36,52,2048
mvaa 128,192,16
mvaa 128,196,48
mvaa 132,204,48
mvaa 132,200,16
mvaa 140,200,20
mvaa 140,204,52
mvaa 136,196,52
dl 227,36,52,3072
mvaa 136,192,20
s_add_u32 s52,s52,s60
mvaa 192,192,24
mvaa 192,196,56
mvaa 196,204,56
s_add_u32 s64,s64,1
mvaa 196,200,24
mvaa 204,200,28
mvaa 204,204,60
mvaa 200,196,60
s_waitcnt vmcnt(10) lgkmcnt(0)
s_barrier
mvaa 200,192,28
s_branch L_k_head
L_k_exit:
global_load_dwordx4 a[160:163],v244,s[30:31] offset:0
s_add_u32 m0,s62,32768
mvaa 216,208,92
s_cmp_lt_u32 s12,2
s_cselect_b32 s86,s22,s23
ta 32,234,0
s_and_b32 s87,s12,1
mvaa 216,212,124
s_lshl_b32 s86,s86,11
ta 34,234,512
s_lshl_b32 s87,s87,17
mvaa 220,220,124
s_add_u32 s86,s86,s87
ra 0,232,0
s_mov_b64 s[36:37],s[68:69]
mvaa 220,216,92
s_mov_b32 s38,4194304
ta 36,235,0
s_mov_b32 s52,s86
mvaa 212,216,88
s_mov_b32 s56,65536
ta 38,235,512
v_accvgpr_read_b32 v224,a242
mvaa 212,220,120
buffer_load_dwordx4 v228,s[40:43],s53 offen offset:0 nt lds
mvaa 208,212,120
s_lshl_b32 s88,s18,1
ra 4,232,2048
v_accvgpr_read_b32 v225,a243
mvaa 208,208,88
s_add_u32 s88,s88,s12
ra 8,232,4096
v_accvgpr_read_b32 v226,a244
mvaa 152,208,84
s_mov_b32 s91,196608
ra 12,232,6144
v_accvgpr_read_b32 v227,a245
mvaa 152,212,116
s_lshl_b32 s88,s88,7
ra 16,232,8192
s_lshl_b32 s89,s18,6
mvaa 156,220,116
s_mov_b32 s90,6291456
ra 20,232,10240
s_mov_b32 s60,-65408
mvaa 156,216,84
buffer_load_dwordx4 v229,s[40:43],s53 offen offset:1024 nt lds
mvaa 148,216,80
s_add_u32 s53,s53,s57
ra 24,232,12288
s_lshl_b32 s87,s20,2
mvaa 148,220,112
s_mov_b32 s26,32768
ra 28,232,14336
s_mov_b32 s27,131072
mvaa 144,212,112
s_add_u32 s61,s98,32768
ta 40,236,0
s_mov_b32 s63,65536
mvaa 144,208,80
s_cmp_ge_u32 s63,65536
s_cselect_b32 s99,-2,1
ta 42,236,512
s_lshl_b32 s99,s99,15
mvaa 88,208,76
s_mov_b32 s67,2048
ta 44,237,0
s_mov_b32 s66,8
mvaa 88,212,108
buffer_load_dwordx4 v230,s[44:47],s54 offen offset:2048 nt lds
mvaa 92,220,108
s_mov_b32 s64,1
ta 46,237,512
mvaa 92,216,76
s_bitcmp1_b32 s12,0
s_cselect_b64 vcc,-1,0
ta 48,234,4096
mvaa 84,216,72
ta 50,234,4608
mvaa 84,220,104
ta 52,235,4096
mvaa 80,212,104
ta 54,235,4608
mvaa 80,208,72
buffer_load_dwordx4 v231,s[44:47],s54 offen offset:3072 nt lds
s_add_u32 m0,s62,49152
mvaa 24,208,68
s_add_u32 s54,s54,s57
ta 56,236,4096
mvaa 24,212,100
s_cmp_lt_u32 s12,2
s_cselect_b32 s57,262144,s91
ta 58,236,4608
mvaa 28,220,100
ta 60,237,4096
mvaa 28,216,68
ta 62,237,4608
mvaa 20,216,64
mvaa 20,220,96
buffer_load_dwordx4 v228,s[40:43],s53 offen offset:0 nt lds
mvaa 16,212,96
v_accvgpr_read_b32 v228,a246
mvaa 16,208,64
s_waitcnt lgkmcnt(5)
maaa 128,32,0
maaa 132,36,0
maaa 140,36,4
buffer_load_dwordx4 v229,s[40:43],s53 offen offset:1024 nt lds
maaa 136,32,4
s_waitcnt lgkmcnt(0)
s_barrier
mvaa 32,32,8
s_add_u32 s53,s53,s58
ta 96,234,16384
v_accvgpr_read_b32 v229,a247
mvaa 36,36,8
s_cmp_lt_u32 s12,2
s_cselect_b32 s42,8388608,s90
ta 98,234,16896
mvaa 44,36,12
s_cmp_lt_u32 s12,2
s_cselect_b32 s53,s88,s89
ra 64,233,0
s_and_b32 s89,s98,8192
mvaa 40,32,12
buffer_load_dwordx4 v230,s[44:47],s54 offen offset:2048 nt lds
mvaa 96,32,16
s_bitcmp1_b32 s21,0
s_cselect_b32 s90,s57,0
ta 100,235,16384
s_add_u32 s53,s53,s90
mvaa 100,36,16
s_cmp_lt_u32 s12,2
s_cselect_b64 s[40:41],s[72:73],s[80:81]
ta 102,235,16896
v_accvgpr_read_b32 v230,a248
mvaa 108,36,20
s_lshl_b32 s90,s90,1
ra 68,233,2048
s_add_u32 s91,s89,65536
mvaa 104,32,20
s_bfe_u32 s88,s21,65539
ra 72,233,4096
s_lshl_b32 s88,s88,1
mvaa 160,32,24
buffer_load_dwordx4 v231,s[44:47],s54 offen offset:3072 nt lds
mvaa 164,36,24
s_add_u32 m0,s98,65536
ra 76,233,6144
s_add_u32 s54,s54,s58
mvaa 172,36,28
s_cmp_eq_u32 s12,3
s_cselect_b64 s[44:45],s[68:69],s[40:41]
ra 80,233,8192
v_accvgpr_read_b32 v231,a249
mvaa 168,32,28
s_cmp_eq_u32 s12,3
s_cselect_b32 s46,0,s42
ra 84,233,10240
s_add_u32 s58,s57,s90
mvaa 184,40,28
s_cmp_eq_u32 s12,3
s_cselect_b32 s54,0,s53
ra 88,233,12288
s_sub_u32 s57,s57,s90
mvaa 188,44,28
global_load_dwordx4 a[168:171],v244,s[30:31] offset:2048
mvaa 180,44,24
s_add_u32 s90,s89,65536
ta 104,236,16384
s_add_u32 s88,s88,s87
mvaa 176,40,24
s_and_b32 s87,s12,1
ra 92,233,14336
s_add_u32 s88,s88,s87
mvaa 120,40,20
s_mul_i32 s88,s88,524288
ta 106,236,16896
s_and_b32 s87,s21,7
mvaa 124,44,20
s_add_u32 s86,s88,16777216
ta 108,237,16384
s_lshl_b32 s87,s87,16
mvaa 116,44,16
global_load_dwordx4 a[176:179],v244,s[84:85] offset:0
mvaa 112,40,16
s_add_u32 s86,s86,s87
ta 110,237,16896
s_lshr_b32 s87,s12,1
mvaa 56,40,12
s_lshl_b32 s87,s87,15
ta 112,234,20480
s_add_u32 s86,s86,s87
mvaa 60,44,12
s_add_u32 s24,s8,s86
s_addc_u32 s25,s9,0
ta 114,234,20992
s_lshl_b32 s86,%1,5
mvaa 52,44,8
s_lshl_b32 s87,s12,3
ta 116,235,20480
s_add_u32 s86,s86,s87
mvaa 48,40,8
global_load_dwordx4 a[164:167],v244,s[30:31] offset:1024
maaa 152,40,4
s_add_u32 s86,s86,83886080
ta 118,235,20992
s_mov_b32 s88,4096
maaa 156,44,4
s_cmp_lg_u32 s65,0
s_cbranch_scc1 L_k_ec1
L_k_ecr1:
ta 120,236,20480
maaa 148,44,0
s_cmp_eq_u32 s12,0
s_cselect_b32 s65,96,0
ta 122,236,20992
s_or_b32 s65,s65,128
maaa 144,40,0
ta 124,237,20480
mvaa 0,48,0
global_load_dwordx4 a[172:175],v244,s[30:31] offset:3072
mvaa 4,52,0
s_mov_b32 s30,1048577
ta 126,237,20992
s_mov_b32 s31,268435712
mvaa 12,52,4
mvaa 8,48,4
mvaa 64,48,8
mvaa 68,52,8
global_load_dwordx4 a[180:183],v244,s[84:85] offset:1024
mvaa 76,52,12
mvaa 72,48,12
mvaa 128,48,16
mvaa 132,52,16
mvaa 140,52,20
dl 224,36,52,0
mvaa 136,48,20
mvaa 192,48,24
mvaa 196,52,24
mvaa 204,52,28
mvaa 200,48,28
dl 225,36,52,1024
mvaa 216,56,28
mvaa 220,60,28
mvaa 212,60,24
mvaa 208,56,24
mvaa 152,56,20
dl 226,36,52,2048
mvaa 156,60,20
mvaa 148,60,16
mvaa 144,56,16
mvaa 88,56,12
s_waitcnt vmcnt(12) lgkmcnt(0)
s_barrier
mvaa 92,60,12
dl 227,36,52,3072
mvaa 84,60,8
s_add_u32 s52,s52,s56
s_add_u32 m0,s98,69632
ta 32,234,32768
mvaa 80,56,8
ta 34,234,33280
mvaa 24,56,4
ta 36,235,32768
mvaa 28,60,4
ta 38,235,33280
mvaa 20,60,0
dl 224,36,52,0
mvaa 16,56,0
ta 40,236,32768
maaa 128,96,64
ta 42,236,33280
maaa 132,100,64
ta 44,237,32768
maaa 140,100,68
ta 46,237,33280
maaa 136,96,68
dl 225,36,52,1024
mvaa 32,96,72
ta 48,234,36864
mvaa 36,100,72
ta 50,234,37376
mvaa 44,100,76
ta 52,235,36864
mvaa 40,96,76
ta 54,235,37376
mvaa 96,96,80
dl 226,36,52,2048
mvaa 100,100,80
ra 20,232,43008
mvaa 108,100,84
ra 24,232,45056
mvaa 104,96,84
ra 0,232,32768
mvaa 160,96,88
ra 28,232,47104
mvaa 164,100,88
dl 227,36,52,3072
mvaa 172,100,92
s_add_u32 m0,s62,0
ra 4,232,34816
s_add_u32 s52,s52,s60
mvaa 168,96,92
ra 16,232,40960
mvaa 184,104,92
ra 8,232,36864
mvaa 188,108,92
ra 12,232,38912
v_add_u32_e32 v232,s90,v232
mvaa 180,108,88
dl 228,40,53,0
mvaa 176,104,88
ta 60,237,36864
mvaa 120,104,84
ta 62,237,37376
mvaa 124,108,84
ta 56,236,36864
mvaa 116,108,80
ta 58,236,37376
mvaa 112,104,80
dl 229,40,53,1024
mvaa 56,104,76
s_add_u32 s53,s53,s57
mvaa 60,108,76
mvaa 52,108,72
mvaa 48,104,72
maaa 152,104,68
dl 230,44,54,2048
maaa 156,108,68
maaa 148,108,64
maaa 144,104,64
mvaa 0,112,64
mvaa 4,116,64
dl 231,44,54,3072
mvaa 12,116,68
s_add_u32 m0,s62,16384
s_add_u32 s54,s54,s57
mvaa 8,112,68
mvaa 64,112,72
mvaa 68,116,72
mvaa 76,116,76
dl 228,40,53,0
mvaa 72,112,76
mvaa 128,112,80
mvaa 132,116,80
mvaa 140,116,84
mvaa 136,112,84
dl 229,40,53,1024
mvaa 192,112,88
s_add_u32 s53,s53,s58
mvaa 196,116,88
mvaa 204,116,92
mvaa 200,112,92
mvaa 216,120,92
dl 230,44,54,2048
mvaa 220,124,92
mvaa 212,124,88
s_waitcnt vmcnt(20) lgkmcnt(0)
s_barrier
mvaa 208,120,88
ra 92,233,47104
mvaa 152,120,84
ta 96,234,49152
mvaa 156,124,84
dl 231,44,54,3072
mvaa 148,124,80
s_add_u32 m0,s98,0
ra 88,233,45056
s_add_u32 s54,s54,s58
mvaa 144,120,80
ta 98,234,49664
mvaa 88,120,76
ta 112,234,53248
mvaa 92,124,76
ta 116,235,53248
mvaa 84,124,72
dl 224,36,52,0
mvaa 80,120,72
ta 118,235,53760
mvaa 24,120,68
ta 114,234,53760
v_subrev_u32_e32 v234,s89,v234
mvaa 28,124,68
ta 100,235,49152
mvaa 20,124,64
ta 102,235,49664
v_subrev_u32_e32 v235,s89,v235
mvaa 16,120,64
dl 225,36,52,1024
maaa 128,32,0
ta 124,237,53248
maaa 132,36,0
ta 126,237,53760
maaa 140,36,4
ta 120,236,53248
maaa 136,32,4
ta 122,236,53760
mvaa 32,32,8
dl 226,36,52,2048
mvaa 36,36,8
ta 104,236,49152
mvaa 44,36,12
ta 106,236,49664
v_subrev_u32_e32 v236,s89,v236
mvaa 40,32,12
ta 108,237,49152
mvaa 56,40,12
ta 110,237,49664
v_subrev_u32_e32 v237,s89,v237
mvaa 60,44,12
dl 227,36,52,3072
mvaa 52,44,8
s_add_u32 s52,s52,s56
s_add_u32 m0,s98,4096
ra 84,233,43008
mvaa 48,40,8
ra 80,233,40960
maaa 152,40,4
ra 76,233,38912
maaa 156,44,4
ra 68,233,34816
maaa 148,44,0
dl 224,36,52,0
maaa 144,40,0
ra 72,233,36864
mvaa 0,48,0
ra 64,233,32768
v_add_u32_e32 v233,s91,v233
mvaa 4,52,0
mvaa 12,52,4
mvaa 8,48,4
dl 225,36,52,1024
mvaa 24,56,4
mvaa 28,60,4
mvaa 20,60,0
mvaa 16,56,0
mvaa 220,60,28
dl 226,36,52,2048
s_waitcnt lgkmcnt(2)
mvaa 220,124,92
mvaa 216,120,92
mvaa 216,56,28
mvaa 212,60,24
mvaa 212,124,88
dl 227,36,52,3072
mvaa 208,120,88
s_add_u32 m0,s62,32768
s_add_u32 s52,s52,s60
s_waitcnt vmcnt(25)
zvaa 246,160,176
mvaa 208,56,24
cv 223,222,223
cv 222,220,221
mvaa 204,52,28
cv 221,218,219
zvaa 250,168,176
mvaa 204,116,92
cv 220,216,217
cv 219,214,215
mvaa 200,112,92
dl 228,40,53,0
s_waitcnt vmcnt(25)
mvaa 246,164,180
mvaa 200,48,28
cv 218,212,213
mvaa 250,172,180
mvaa 196,52,24
cv 217,210,211
cv 216,208,209
mvaa 196,116,88
cv 215,206,207
v_cndmask_b32_e64 v246,0,v246,s[30:31]
mvaa 192,112,88
cv 214,204,205
v_cndmask_b32_e64 v250,0,v250,s[30:31]
mvaa 192,48,24
dl 229,40,53,1024
s_lshl_b64 s[30:31],s[30:31],1
v_cndmask_b32_e64 v246,v246,v247,s[30:31]
v_cndmask_b32_e64 v250,v250,v251,s[30:31]
mvaa 188,44,28
s_lshl_b64 s[30:31],s[30:31],1
s_add_u32 s53,s53,s57
v_cndmask_b32_e64 v246,v246,v248,s[30:31]
v_cndmask_b32_e64 v250,v250,v252,s[30:31]
mvaa 188,108,92
s_lshl_b64 s[30:31],s[30:31],1
cv 213,202,203
v_cndmask_b32_e64 v246,v246,v249,s[30:31]
mvaa 184,104,92
v_cndmask_b32_e64 v250,v250,v253,s[30:31]
v_add_f32_dpp v246,v246,v246 quad_perm:[1,0,3,2] row_mask:0xf bank_mask:0xf
mvaa 184,40,28
v_add_f32_dpp v250,v250,v250 quad_perm:[1,0,3,2] row_mask:0xf bank_mask:0xf
v_add_f32_dpp v246,v246,v246 quad_perm:[2,3,0,1] row_mask:0xf bank_mask:0xf
mvaa 180,44,24
dl 230,44,54,2048
v_add_f32_dpp v250,v250,v250 quad_perm:[2,3,0,1] row_mask:0xf bank_mask:0xf
mvaa 180,108,88
v_add_f32_dpp v246,v246,v246 row_half_mirror row_mask:0xf bank_mask:0xf
v_add_f32_dpp v250,v250,v250 row_half_mirror row_mask:0xf bank_mask:0xf
mvaa 176,104,88
v_add_f32_dpp v246,v246,v246 row_mirror row_mask:0xf bank_mask:0xf
v_add_f32_dpp v250,v250,v250 row_mirror row_mask:0xf bank_mask:0xf
mvaa 176,40,24
cv 212,200,201
cv 211,198,199
mvaa 172,36,28
v_permlane16_swap_b32_e32 v246,v250
mvaa 172,100,92
dl 231,44,54,3072
v_add_f32_e32 v246,v246,v250
v_mov_b32_e32 v250,v246
mvaa 168,96,92
s_add_u32 s54,s54,s57
v_permlane32_swap_b32_e32 v246,v250
mvaa 168,32,28
v_add_f32_e32 v246,v246,v250
v_lshrrev_b32_e32 v250,6,v244
mvaa 164,36,24
v_mul_f32_e32 v246,0xbfb8aa3b,v246
cv 210,196,197
mvaa 164,100,88
ex 246
mvaa 160,96,88
buffer_load_dword a242,v241,s[48:51],s55 offen
mvaa 160,32,24
s_add_u32 s55,s55,s59
v_add_f32_e32 v246,1.0,v246
cv 209,194,195
mvaa 156,60,20
v_rcp_f32_e32 v246,v246
mvaa 156,124,84
s_waitcnt vmcnt(17) lgkmcnt(0)
s_barrier
s_add_u32 m0,s62,49152
mvaa 152,120,84
cv 208,192,193
cv 207,190,191
mvaa 152,56,20
dl 228,40,53,0
mvaa 148,60,16
cv 206,188,189
cv 205,186,187
mvaa 148,124,80
cv 204,184,185
cv 203,182,183
mvaa 144,120,80
cv 202,180,181
cv 201,178,179
mvaa 144,56,16
cv 200,176,177
cv 199,174,175
mvaa 140,52,20
dl 229,40,53,1024
mvaa 140,116,84
s_add_u32 s53,s53,s58
cv 198,172,173
cv 197,170,171
mvaa 136,112,84
ra 4,232,0
cv 196,168,169
mvaa 136,48,20
ta 92,239,12288
cv 195,166,167
mvaa 132,52,16
ta 94,239,12800
cv 194,164,165
mvaa 132,116,80
dl 230,44,54,2048
mvaa 128,112,80
ta 88,238,12288
cv 193,162,163
mvaa 128,48,16
ta 90,238,12800
cv 192,160,161
mvaa 124,44,20
cv 191,158,159
cv 190,156,157
mvaa 124,108,84
cv 189,154,155
cv 188,152,153
mvaa 120,104,84
dl 231,44,54,3072
mvaa 120,40,20
s_add_u32 m0,s98,32768
cv 187,150,151
cv 186,148,149
mvaa 116,44,16
s_add_u32 s54,s54,s58
cv 185,146,147
cv 184,144,145
mvaa 116,108,80
cv 183,142,143
cv 182,140,141
mvaa 112,104,80
cv 181,138,139
cv 180,136,137
mvaa 112,40,16
dl 224,36,52,0
mvaa 108,36,20
cv 179,134,135
cv 178,132,133
mvaa 108,100,84
cv 177,130,131
cv 176,128,129
mvaa 104,96,84
cv 175,126,127
cv 174,124,125
mvaa 104,32,20
cv 173,122,123
cv 172,120,121
mvaa 100,36,16
dl 225,36,52,1024
mvaa 100,100,80
ta 44,235,0
cv 171,118,119
mvaa 96,96,80
ta 46,235,512
cv 170,116,117
mvaa 96,32,16
ta 40,234,0
cv 169,114,115
mvaa 92,60,12
ta 42,234,512
cv 168,112,113
mvaa 92,124,76
dl 226,36,52,2048
mvaa 88,120,76
cv 167,110,111
cv 166,108,109
mvaa 88,56,12
ta 84,237,8192
cv 165,106,107
mvaa 84,60,8
ta 86,237,8704
cv 164,104,105
mvaa 84,124,72
ta 80,236,8192
cv 163,102,103
mvaa 80,120,72
dl 227,36,52,3072
mvaa 80,56,8
s_add_u32 m0,s62,0
ta 82,236,8704
cv 162,100,101
mvaa 76,52,12
s_add_u32 s52,s52,s56
ra 16,232,6144
cv 161,98,99
mvaa 76,116,76
ta 60,235,4096
cv 160,96,97
mvaa 72,112,76
ta 56,234,4096
cv 159,94,95
mvaa 72,48,12
buffer_load_dword a243,v241,s[48:51],s55 offen
mvaa 68,52,8
s_add_u32 s55,s55,s59
ta 62,235,4608
cv 151,78,79
mvaa 68,116,72
ta 58,234,4608
cv 150,76,77
mvaa 64,112,72
ra 12,232,4096
cv 158,92,93
mvaa 64,48,8
ta 52,237,0
cv 157,90,91
mvaa 60,108,76
buffer_load_dword a244,v241,s[48:51],s55 offen
mvaa 56,104,76
s_add_u32 s55,s55,s59
ta 54,237,512
cv 147,70,71
mvaa 52,108,72
ra 8,232,2048
cv 146,68,69
mvaa 48,104,72
ta 48,236,0
cv 145,66,67
mvaa 44,100,76
ta 50,236,512
cv 144,64,65
mvaa 40,96,76
buffer_load_dword a245,v241,s[48:51],s55 offen
mvaa 36,100,72
s_add_u32 s55,s55,s59
cv 143,62,63
cv 142,60,61
mvaa 32,96,72
cv 141,58,59
cv 140,56,57
mvaa 28,124,68
cv 139,54,55
cv 138,52,53
mvaa 24,120,68
cv 137,50,51
cv 136,48,49
mvaa 20,124,64
s_mov_b64 exec,65537
buffer_store_dword v246,v250,s[8:11],s86 offen sc0 sc1
s_mov_b64 exec,-1
mvaa 16,120,64
cv 135,46,47
cv 134,44,45
mvaa 12,116,68
ta 76,235,8192
cv 133,42,43
mvaa 8,112,68
ta 78,235,8704
cv 132,40,41
mvaa 4,116,64
cv 131,38,39
cv 130,36,37
mvaa 0,112,64
ta 72,234,8192
cv 129,34,35
maaa 156,108,68
ta 74,234,8704
cv 128,32,33
maaa 152,104,68
cv 127,30,31
cv 126,28,29
maaa 148,108,64
cv 125,26,27
cv 124,24,25
maaa 144,104,64
v_accvgpr_read_b32 v63,a159
v_accvgpr_read_b32 v62,a158
maaa 140,100,68
v_accvgpr_read_b32 v61,a157
v_accvgpr_read_b32 v60,a156
maaa 136,96,68
v_accvgpr_read_b32 v59,a155
v_accvgpr_read_b32 v58,a154
maaa 132,100,64
v_accvgpr_read_b32 v57,a153
v_accvgpr_read_b32 v56,a152
maaa 128,96,64
ta 68,237,4096
ta 70,237,4608
s_waitcnt lgkmcnt(4)
zaaa 236,40,4
cv 56,56,57
cv 57,58,59
zaaa 220,44,4
ta 64,236,4096
ta 66,236,4608
zaaa 216,44,8
cv 58,60,61
cv 59,62,63
zaaa 232,40,8
buffer_store_dwordx4 v[56:59],v244,s[24:27],0 offen offset:3072 sc0 sc1
zaaa 228,40,12
v_accvgpr_read_b32 v55,a151
v_accvgpr_read_b32 v54,a150
zaaa 212,44,12
v_accvgpr_read_b32 v53,a149
v_accvgpr_read_b32 v52,a148
zaaa 208,44,16
v_accvgpr_read_b32 v51,a147
v_accvgpr_read_b32 v50,a146
zaaa 224,40,16
v_accvgpr_read_b32 v49,a145
v_accvgpr_read_b32 v48,a144
zaaa 192,48,16
cv 48,48,49
cv 49,50,51
zaaa 176,52,16
cv 50,52,53
cv 51,54,55
zaaa 180,52,12
buffer_store_dwordx4 v[48:51],v244,s[24:27],0 offen offset:2048 sc0 sc1
zaaa 196,48,12
s_waitcnt vmcnt(27) lgkmcnt(0)
s_barrier
ra 24,233,0
v_accvgpr_read_b32 v47,a143
zaaa 200,48,8
ra 28,233,2048
v_accvgpr_read_b32 v46,a142
v_accvgpr_read_b32 v45,a141
zaaa 184,52,8
ra 32,233,4096
ra 36,233,6144
zaaa 188,52,4
v_accvgpr_read_b32 v43,a139
v_accvgpr_read_b32 v42,a138
zaaa 204,48,4
dl 228,40,53,0
ta 96,236,24576
zaaa 172,56,4
ta 98,236,25088
v_accvgpr_read_b32 v41,a137
zaaa 156,60,4
ta 100,237,24576
v_accvgpr_read_b32 v39,a135
zaaa 152,60,8
ta 102,237,25088
ta 104,238,28672
zaaa 168,56,8
v_accvgpr_read_b32 v38,a134
v_accvgpr_read_b32 v35,a131
zaaa 164,56,12
dl 229,40,53,1024
ta 106,238,29184
zaaa 148,60,12
s_add_u32 s53,s53,s57
ta 108,239,28672
v_accvgpr_read_b32 v34,a130
zaaa 144,60,16
ta 110,239,29184
v_accvgpr_read_b32 v33,a129
zaaa 160,56,16
ta 44,235,16384
ta 46,235,16896
ta 40,234,16384
ta 42,234,16896
ta 52,237,16384
ta 54,237,16896
ta 48,236,16384
ta 50,236,16896
ta 60,235,20480
ta 62,235,20992
ta 56,234,20480
ta 58,234,20992
v_accvgpr_read_b32 v32,a128
zaaa 128,64,16
v_accvgpr_read_b32 v37,a133
v_accvgpr_read_b32 v36,a132
zaaa 112,68,16
dl 230,44,54,2048
zaaa 116,68,12
v_accvgpr_read_b32 v40,a136
v_accvgpr_read_b32 v44,a140
zaaa 132,64,12
cv 251,42,43
cv 250,40,41
zaaa 136,64,8
cv 253,46,47
cv 252,44,45
zaaa 120,68,8
cv 249,38,39
cv 247,34,35
zaaa 124,68,4
dl 231,44,54,3072
zaaa 140,64,4
s_add_u32 m0,s61,4096
cv 246,32,33
cv 248,36,37
zvaa 40,72,4
s_add_u32 s54,s54,s57
ta 68,237,20480
cv 123,22,23
zvaa 44,76,4
s_add_u32 s61,s63,s98
ta 70,237,20992
cv 121,18,19
zvaa 52,76,8
s_add_u32 s63,s63,s99
ta 64,236,20480
cv 120,16,17
zvaa 48,72,8
)"
R"(dl 224,36,52,0
zvaa 56,72,12
ta 66,236,20992
cv 122,20,21
zvaa 60,76,12
cv 119,14,15
cv 118,12,13
zvaa 68,76,16
cv 117,10,11
cv 116,8,9
zvaa 64,72,16
cv 156,88,89
cv 153,82,83
zvaa 32,80,16
dl 225,36,52,1024
ta 76,235,24576
ta 78,235,25088
zvaa 36,84,16
ta 72,234,24576
ta 74,234,25088
zvaa 28,84,12
cv 152,80,81
cv 155,86,87
zvaa 24,80,12
cv 154,84,85
cv 149,74,75
zvaa 16,80,8
cv 148,72,73
v_add_u32_e32 v232,s99,v232
zvaa 20,84,8
dl 226,36,52,2048
zvaa 12,84,4
cv 115,6,7
cv 114,4,5
zvaa 8,80,4
cv 113,2,3
cv 112,0,1
zvaa 76,4,88
zvaa 96,4,92
zvaa 100,8,92
dl 227,36,52,3072
zvaa 80,8,88
s_add_u32 s52,s52,s60
zvaa 84,12,88
zvaa 104,12,92
zvaa 108,16,92
s_waitcnt vmcnt(23) lgkmcnt(0)
s_barrier
zvaa 88,16,88
buffer_store_dwordx4 v[250:253],v244,s[24:27],0 offen offset:1024 sc0 sc1
buffer_store_dwordx4 v[246:249],v244,s[24:27],0 offen offset:0 sc0 sc1
s_branch L_kvx
L_vtile:
s_waitcnt vmcnt(8)
s_barrier
ta 64,234,0
ta 66,234,512
ra 0,232,0
ta 68,235,0
ta 70,235,512
ra 4,232,2048
ra 8,232,4096
ra 12,232,6144
ra 16,232,8192
ra 20,232,10240
ra 24,232,12288
ra 28,232,14336
s_add_u32 m0,s62,32768
v_mov_b32_e32 v212,0
buffer_load_dwordx4 v228,s[40:43],s53 offen offset:0 nt lds
v_mov_b32_e32 v213,0
v_mov_b32_e32 v214,0
v_mov_b32_e32 v215,0
v_mov_b32_e32 v220,0
v_mov_b32_e32 v221,0
v_mov_b32_e32 v222,0
v_mov_b32_e32 v223,0
v_mov_b32_e32 v216,0
v_mov_b32_e32 v217,0
v_mov_b32_e32 v218,0
v_mov_b32_e32 v219,0
v_mov_b32_e32 v208,0
v_mov_b32_e32 v209,0
v_mov_b32_e32 v210,0
v_mov_b32_e32 v211,0
v_mov_b32_e32 v148,0
s_waitcnt lgkmcnt(9)
zaaa 128,0,64
s_mov_b32 s63,32768
ta 72,236,0
v_mov_b32_e32 v149,0
s_waitcnt lgkmcnt(8)
zaaa 136,0,68
s_mov_b32 s61,s98
ta 74,236,512
v_mov_b32_e32 v150,0
s_waitcnt lgkmcnt(8)
zaaa 140,4,68
s_mov_b32 s64,1
ta 76,237,0
ta 78,237,512
zaaa 132,4,64
buffer_load_dwordx4 v229,s[40:43],s53 offen offset:1024 nt lds
s_waitcnt lgkmcnt(4)
zvaa 32,8,64
s_add_u32 s53,s53,s57
v_mov_b32_e32 v151,0
v_mov_b32_e32 v156,0
zvaa 40,8,68
v_mov_b32_e32 v157,0
v_mov_b32_e32 v158,0
zvaa 44,12,68
ta 80,234,4096
v_mov_b32_e32 v159,0
zvaa 36,12,64
buffer_load_dwordx4 v230,s[44:47],s54 offen offset:2048 nt lds
zvaa 96,16,64
ta 82,234,4608
v_mov_b32_e32 v152,0
zvaa 104,16,68
ta 84,235,4096
v_mov_b32_e32 v153,0
zvaa 108,20,68
ta 86,235,4608
v_mov_b32_e32 v154,0
zvaa 100,20,64
buffer_load_dwordx4 v231,s[44:47],s54 offen offset:3072 nt lds
zvaa 160,24,64
s_add_u32 m0,s98,36864
ra 32,233,0
s_add_u32 s54,s54,s57
zvaa 168,24,68
ra 36,233,2048
v_mov_b32_e32 v155,0
zvaa 172,28,68
ra 40,233,4096
v_mov_b32_e32 v144,0
zvaa 164,28,64
dl 224,36,52,0
s_waitcnt lgkmcnt(7)
zvaa 180,28,72
ra 44,233,6144
v_mov_b32_e32 v145,0
zvaa 188,28,76
ra 48,233,8192
v_mov_b32_e32 v146,0
zvaa 184,24,76
ra 52,233,10240
v_mov_b32_e32 v147,0
zvaa 176,24,72
dl 225,36,52,1024
zvaa 116,20,72
ra 56,233,12288
v_mov_b32_e32 v84,0
zvaa 124,20,76
ra 60,233,14336
v_mov_b32_e32 v85,0
zvaa 120,16,76
v_mov_b32_e32 v86,0
v_mov_b32_e32 v87,0
zvaa 112,16,72
dl 226,36,52,2048
zvaa 52,12,72
s_waitcnt vmcnt(11)
s_barrier
zvaa 60,12,76
ta 96,234,16384
v_mov_b32_e32 v92,0
zvaa 56,8,76
ta 98,234,16896
v_mov_b32_e32 v93,0
zvaa 48,8,72
dl 227,36,52,3072
zaaa 148,4,72
s_add_u32 m0,s62,49152
ta 100,235,16384
s_add_u32 s52,s52,s60
zaaa 156,4,76
ta 102,235,16896
v_mov_b32_e32 v94,0
zaaa 152,0,76
ta 104,236,16384
v_mov_b32_e32 v95,0
zaaa 144,0,72
buffer_load_dwordx4 v228,s[40:43],s53 offen offset:0 nt lds
s_waitcnt lgkmcnt(5)
zvaa 0,0,80
ta 106,236,16896
v_mov_b32_e32 v88,0
zvaa 8,0,84
ta 108,237,16384
v_mov_b32_e32 v89,0
zvaa 12,4,84
ta 110,237,16896
v_mov_b32_e32 v90,0
zvaa 4,4,80
buffer_load_dwordx4 v229,s[40:43],s53 offen offset:1024 nt lds
zvaa 64,8,80
s_add_u32 s53,s53,s58
ta 112,234,20480
v_mov_b32_e32 v91,0
zvaa 72,8,84
ta 114,234,20992
v_mov_b32_e32 v80,0
zvaa 76,12,84
ta 116,235,20480
v_mov_b32_e32 v81,0
zvaa 68,12,80
buffer_load_dwordx4 v230,s[44:47],s54 offen offset:2048 nt lds
zvaa 128,16,80
ta 118,235,20992
v_mov_b32_e32 v82,0
zvaa 136,16,84
ta 208,236,4096
v_mov_b32_e32 v83,0
zvaa 140,20,84
ta 210,236,4608
v_mov_b32_e32 v20,0
zvaa 132,20,80
buffer_load_dwordx4 v231,s[44:47],s54 offen offset:3072 nt lds
zvaa 192,24,80
s_add_u32 m0,s98,65536
ta 216,237,4096
v_mov_b32_e32 v21,0
zvaa 200,24,84
s_add_u32 s54,s54,s58
ta 218,237,4608
v_mov_b32_e32 v22,0
zvaa 204,28,84
ta 212,236,20480
v_mov_b32_e32 v23,0
zvaa 196,28,80
dl 224,36,52,0
s_waitcnt lgkmcnt(7)
maaa 128,32,96
ta 214,236,20992
v_mov_b32_e32 v28,0
maaa 136,32,100
ta 220,237,20480
v_mov_b32_e32 v29,0
maaa 140,36,100
ta 222,237,20992
v_mov_b32_e32 v30,0
maaa 132,36,96
dl 225,36,52,1024
mvaa 32,40,96
v_mov_b32_e32 v31,0
v_mov_b32_e32 v24,0
mvaa 40,40,100
v_mov_b32_e32 v25,0
v_mov_b32_e32 v26,0
mvaa 44,44,100
v_mov_b32_e32 v27,0
v_mov_b32_e32 v16,0
mvaa 36,44,96
dl 226,36,52,2048
mvaa 96,48,96
v_mov_b32_e32 v17,0
v_mov_b32_e32 v18,0
mvaa 104,48,100
v_mov_b32_e32 v19,0
v_add_u32_e32 v232,32768,v232
mvaa 108,52,100
v_add_u32_e32 v233,32768,v233
mvaa 100,52,96
dl 227,36,52,3072
mvaa 160,56,96
s_add_u32 s52,s52,s56
s_add_u32 m0,s98,69632
mvaa 168,56,100
mvaa 172,60,100
mvaa 164,60,96
dl 224,36,52,0
mvaa 180,60,104
mvaa 188,60,108
mvaa 184,56,108
mvaa 176,56,104
dl 225,36,52,1024
mvaa 116,52,104
mvaa 124,52,108
mvaa 120,48,108
mvaa 112,48,104
dl 226,36,52,2048
mvaa 52,44,104
mvaa 60,44,108
mvaa 56,40,108
mvaa 48,40,104
dl 227,36,52,3072
maaa 148,36,104
s_add_u32 s52,s52,s60
maaa 156,36,108
maaa 152,32,108
maaa 144,32,104
mvaa 0,32,112
s_waitcnt lgkmcnt(0)
mvaa 8,32,116
mvaa 12,36,116
mvaa 4,36,112
mvaa 64,40,112
mvaa 72,40,116
mvaa 76,44,116
mvaa 68,44,112
mvaa 128,48,112
mvaa 136,48,116
mvaa 140,52,116
mvaa 132,52,112
mvaa 192,56,112
mvaa 200,56,116
mvaa 204,60,116
mvaa 196,60,112
s_waitcnt vmcnt(8)
s_waitcnt lgkmcnt(0)
s_barrier
L_v_head:
mvaa 212,28,208
ra 64,232,0
mvaa 212,60,212
s_add_u32 m0,s62,0
ta 192,234,32768
mvaa 220,60,220
buffer_load_dwordx4 v228,s[40:43],s53 offen offset:0 nt lds
mvaa 220,28,216
ta 194,234,33280
mvaa 216,24,216
ra 96,233,0
mvaa 216,56,220
ta 196,234,49152
mvaa 208,56,212
ta 198,234,49664
mvaa 208,24,208
ta 204,235,49152
mvaa 148,20,208
buffer_load_dwordx4 v229,s[40:43],s53 offen offset:1024 nt lds
mvaa 148,52,212
s_add_u32 s53,s53,s57
ta 206,235,49664
mvaa 156,52,220
ta 200,235,32768
mvaa 156,20,216
ta 202,235,33280
mvaa 152,16,216
ra 68,232,2048
mvaa 152,48,220
ra 100,233,2048
mvaa 144,48,212
ra 72,232,4096
mvaa 144,16,208
buffer_load_dwordx4 v230,s[44:47],s54 offen offset:2048 nt lds
mvaa 84,12,208
ra 104,233,4096
mvaa 84,44,212
ra 76,232,6144
mvaa 92,44,220
ra 108,233,6144
mvaa 92,12,216
ra 80,232,8192
mvaa 88,8,216
ra 112,233,8192
mvaa 88,40,220
buffer_load_dwordx4 v231,s[44:47],s54 offen offset:3072 nt lds
mvaa 80,40,212
s_add_u32 s54,s54,s57
ra 84,232,10240
mvaa 80,8,208
ra 116,233,10240
mvaa 20,4,208
ra 88,232,12288
mvaa 20,36,212
ra 120,233,12288
mvaa 28,36,220
ra 92,232,14336
mvaa 28,4,216
s_add_u32 m0,s62,16384
ra 124,233,14336
mvaa 24,0,216
buffer_load_dwordx4 v228,s[40:43],s53 offen offset:0 nt lds
mvaa 24,32,220
mvaa 16,32,212
mvaa 16,0,208
s_waitcnt lgkmcnt(8)
maaa 128,64,192
maaa 128,96,196
maaa 136,96,204
buffer_load_dwordx4 v229,s[40:43],s53 offen offset:1024 nt lds
maaa 136,64,200
s_add_u32 s53,s53,s58
maaa 140,68,200
maaa 140,100,204
maaa 132,100,196
maaa 132,68,192
mvaa 32,72,192
mvaa 32,104,196
buffer_load_dwordx4 v230,s[44:47],s54 offen offset:2048 nt lds
mvaa 40,104,204
mvaa 40,72,200
s_waitcnt lgkmcnt(0)
mvaa 44,76,200
ta 208,236,32768
mvaa 44,108,204
ta 210,236,33280
mvaa 36,108,196
ta 212,236,49152
mvaa 36,76,192
buffer_load_dwordx4 v231,s[44:47],s54 offen offset:3072 nt lds
mvaa 96,80,192
s_add_u32 s54,s54,s58
ta 214,236,49664
mvaa 96,112,196
ta 220,237,49152
mvaa 104,112,204
ta 222,237,49664
mvaa 104,80,200
ta 216,237,32768
mvaa 108,84,200
ta 218,237,33280
mvaa 108,116,204
mvaa 100,116,196
buffer_load_dword a184,v241,s[48:51],s55 offen
mvaa 100,84,192
s_add_u32 s55,s55,s59
mvaa 160,88,192
mvaa 160,120,196
mvaa 168,120,204
mvaa 168,88,200
mvaa 172,92,200
s_add_u32 m0,s61,0
mvaa 172,124,204
dl 224,36,52,0
mvaa 164,124,196
mvaa 164,92,192
s_waitcnt lgkmcnt(0)
mvaa 180,92,208
mvaa 180,124,212
mvaa 188,124,220
ta 192,234,36864
mvaa 188,92,216
ta 194,234,37376
mvaa 184,88,216
dl 225,36,52,1024
mvaa 184,120,220
ta 196,234,53248
mvaa 176,120,212
ta 198,234,53760
mvaa 176,88,208
ta 204,235,53248
mvaa 116,84,208
ta 206,235,53760
mvaa 116,116,212
ta 200,235,36864
mvaa 124,116,220
ta 202,235,37376
mvaa 124,84,216
dl 226,36,52,2048
mvaa 120,80,216
mvaa 120,112,220
mvaa 112,112,212
mvaa 112,80,208
mvaa 52,76,208
mvaa 52,108,212
mvaa 60,108,220
dl 227,36,52,3072
mvaa 60,76,216
s_add_u32 s52,s52,s56
mvaa 56,72,216
mvaa 56,104,220
mvaa 48,104,212
mvaa 48,72,208
maaa 148,68,208
maaa 148,100,212
buffer_load_dword a185,v241,s[48:51],s55 offen
maaa 156,100,220
s_add_u32 s55,s55,s59
maaa 156,68,216
maaa 152,64,216
maaa 152,96,220
maaa 144,96,212
s_waitcnt lgkmcnt(0)
maaa 144,64,208
s_add_u32 m0,s61,4096
mvaa 0,64,192
dl 224,36,52,0
mvaa 0,96,196
s_add_u32 s61,s63,s98
mvaa 8,96,204
ta 208,236,36864
mvaa 8,64,200
ta 210,236,37376
mvaa 12,68,200
ta 212,236,53248
mvaa 12,100,204
ta 214,236,53760
mvaa 4,100,196
ta 220,237,53248
mvaa 4,68,192
ta 222,237,53760
mvaa 64,72,192
dl 225,36,52,1024
mvaa 64,104,196
ta 216,237,36864
mvaa 72,104,204
ta 218,237,37376
mvaa 72,72,200
s_cmp_ge_u32 s63,65536
s_cselect_b32 s99,-2,1
s_lshl_b32 s99,s99,15
mvaa 76,76,200
s_add_u32 s63,s63,s99
v_add_u32_e32 v232,s99,v232
v_add_u32_e32 v233,s99,v233
mvaa 76,108,204
mvaa 68,108,196
mvaa 68,76,192
dl 226,36,52,2048
mvaa 128,80,192
mvaa 128,112,196
mvaa 136,112,204
mvaa 136,80,200
mvaa 140,84,200
mvaa 140,116,204
mvaa 132,116,196
dl 227,36,52,3072
mvaa 132,84,192
s_add_u32 s52,s52,s60
mvaa 192,88,192
mvaa 192,120,196
mvaa 200,120,204
mvaa 200,88,200
mvaa 204,92,200
mvaa 204,124,204
s_cmp_eq_u32 s64,3
mvaa 196,124,196
s_waitcnt vmcnt(10) lgkmcnt(0)
s_barrier
mvaa 196,92,192
s_cbranch_scc1 L_v_exit
mvaa 212,92,208
ra 0,232,0
mvaa 212,124,212
s_add_u32 m0,s62,32768
ta 192,234,0
mvaa 220,124,220
buffer_load_dwordx4 v228,s[40:43],s53 offen offset:0 nt lds
mvaa 220,92,216
ta 194,234,512
mvaa 216,88,216
ra 32,233,0
mvaa 216,120,220
ta 196,234,16384
mvaa 208,120,212
ta 198,234,16896
mvaa 208,88,208
ta 204,235,16384
mvaa 148,84,208
buffer_load_dwordx4 v229,s[40:43],s53 offen offset:1024 nt lds
mvaa 148,116,212
s_add_u32 s53,s53,s57
ta 206,235,16896
mvaa 156,116,220
ta 200,235,0
mvaa 156,84,216
ta 202,235,512
mvaa 152,80,216
ra 4,232,2048
mvaa 152,112,220
ra 36,233,2048
mvaa 144,112,212
ra 8,232,4096
mvaa 144,80,208
buffer_load_dwordx4 v230,s[44:47],s54 offen offset:2048 nt lds
mvaa 84,76,208
ra 40,233,4096
mvaa 84,108,212
ra 12,232,6144
mvaa 92,108,220
ra 44,233,6144
mvaa 92,76,216
ra 16,232,8192
mvaa 88,72,216
ra 48,233,8192
mvaa 88,104,220
buffer_load_dwordx4 v231,s[44:47],s54 offen offset:3072 nt lds
mvaa 80,104,212
s_add_u32 s54,s54,s57
ra 20,232,10240
mvaa 80,72,208
ra 52,233,10240
mvaa 20,68,208
ra 24,232,12288
mvaa 20,100,212
ra 56,233,12288
mvaa 28,100,220
ra 28,232,14336
mvaa 28,68,216
s_add_u32 m0,s62,49152
ra 60,233,14336
mvaa 24,64,216
buffer_load_dwordx4 v228,s[40:43],s53 offen offset:0 nt lds
mvaa 24,96,220
mvaa 16,96,212
mvaa 16,64,208
s_waitcnt lgkmcnt(8)
maaa 128,0,192
maaa 128,32,196
maaa 136,32,204
buffer_load_dwordx4 v229,s[40:43],s53 offen offset:1024 nt lds
maaa 136,0,200
s_add_u32 s53,s53,s58
maaa 140,4,200
maaa 140,36,204
maaa 132,36,196
maaa 132,4,192
mvaa 32,8,192
mvaa 32,40,196
buffer_load_dwordx4 v230,s[44:47],s54 offen offset:2048 nt lds
mvaa 40,40,204
mvaa 40,8,200
s_waitcnt lgkmcnt(0)
mvaa 44,12,200
ta 208,236,0
mvaa 44,44,204
ta 210,236,512
mvaa 36,44,196
ta 212,236,16384
mvaa 36,12,192
buffer_load_dwordx4 v231,s[44:47],s54 offen offset:3072 nt lds
mvaa 96,16,192
s_add_u32 s54,s54,s58
ta 214,236,16896
mvaa 96,48,196
ta 220,237,16384
mvaa 104,48,204
ta 222,237,16896
mvaa 104,16,200
ta 216,237,0
mvaa 108,20,200
ta 218,237,512
mvaa 108,52,204
mvaa 100,52,196
buffer_load_dword a186,v241,s[48:51],s55 offen
mvaa 100,20,192
s_add_u32 s55,s55,s59
mvaa 160,24,192
s_lshl_b32 s86,s21,2
s_add_u32 s86,s86,s12
s_mul_i32 s86,s86,384
mvaa 160,56,196
s_add_u32 s86,s86,4096
v_lshl_add_u32 v253,v244,3,s86
mvaa 168,56,204
s_mov_b64 exec,7
global_load_dword a188,v253,s[96:97]
s_mov_b64 exec,-1
mvaa 168,24,200
mvaa 172,28,200
s_add_u32 m0,s61,0
mvaa 172,60,204
dl 224,36,52,0
mvaa 164,60,196
mvaa 164,28,192
s_waitcnt lgkmcnt(0)
mvaa 180,28,208
mvaa 180,60,212
mvaa 188,60,220
ta 192,234,4096
mvaa 188,28,216
ta 194,234,4608
mvaa 184,24,216
dl 225,36,52,1024
mvaa 184,56,220
ta 196,234,20480
mvaa 176,56,212
ta 198,234,20992
mvaa 176,24,208
ta 204,235,20480
mvaa 116,20,208
ta 206,235,20992
mvaa 116,52,212
ta 200,235,4096
mvaa 124,52,220
ta 202,235,4608
mvaa 124,20,216
dl 226,36,52,2048
mvaa 120,16,216
mvaa 120,48,220
mvaa 112,48,212
mvaa 112,16,208
mvaa 52,12,208
mvaa 52,44,212
s_bitcmp1_b32 s65,s64
s_cbranch_scc1 L_v_ev
L_v_evr:
mvaa 60,44,220
dl 227,36,52,3072
mvaa 60,12,216
s_add_u32 s52,s52,s56
mvaa 56,8,216
mvaa 56,40,220
mvaa 48,40,212
mvaa 48,8,208
maaa 148,4,208
maaa 148,36,212
buffer_load_dword a187,v241,s[48:51],s55 offen
maaa 156,36,220
s_add_u32 s55,s55,s59
maaa 156,4,216
maaa 152,0,216
maaa 152,32,220
maaa 144,32,212
s_waitcnt lgkmcnt(0)
maaa 144,0,208
s_add_u32 m0,s61,4096
mvaa 0,0,192
dl 224,36,52,0
mvaa 0,32,196
s_add_u32 s61,s63,s98
mvaa 8,32,204
ta 208,236,4096
mvaa 8,0,200
ta 210,236,4608
mvaa 12,4,200
ta 212,236,20480
mvaa 12,36,204
ta 214,236,20992
mvaa 4,36,196
ta 220,237,20480
mvaa 4,4,192
ta 222,237,20992
mvaa 64,8,192
dl 225,36,52,1024
mvaa 64,40,196
ta 216,237,4096
mvaa 72,40,204
ta 218,237,4608
mvaa 72,8,200
s_cmp_ge_u32 s63,65536
s_cselect_b32 s99,-2,1
s_lshl_b32 s99,s99,15
mvaa 76,12,200
s_add_u32 s63,s63,s99
v_add_u32_e32 v232,s99,v232
v_add_u32_e32 v233,s99,v233
mvaa 76,44,204
mvaa 68,44,196
mvaa 68,12,192
dl 226,36,52,2048
mvaa 128,16,192
mvaa 128,48,196
mvaa 136,48,204
mvaa 136,16,200
mvaa 140,20,200
mvaa 140,52,204
mvaa 132,52,196
dl 227,36,52,3072
mvaa 132,20,192
s_add_u32 s52,s52,s60
mvaa 192,24,192
mvaa 192,56,196
mvaa 200,56,204
s_add_u32 s64,s64,1
mvaa 200,24,200
mvaa 204,28,200
mvaa 204,60,204
mvaa 196,60,196
s_waitcnt vmcnt(10) lgkmcnt(0)
s_barrier
mvaa 196,28,192
s_branch L_v_head
L_v_exit:
global_load_dwordx4 a[160:163],v244,s[30:31] offset:0
s_add_u32 m0,s62,32768
mvaa 212,92,208
s_cmp_lt_u32 s12,2
s_cselect_b32 s86,s22,s23
ta 32,234,0
s_and_b32 s87,s12,1
mvaa 212,124,212
s_lshl_b32 s86,s86,11
ta 34,234,512
s_lshl_b32 s87,s87,17
mvaa 220,124,220
s_add_u32 s86,s86,s87
ra 0,232,0
s_mov_b64 s[36:37],s[68:69]
mvaa 220,92,216
s_mov_b32 s38,4194304
ta 36,235,0
s_mov_b32 s52,s86
mvaa 216,88,216
s_mov_b32 s56,65536
ta 38,235,512
v_accvgpr_read_b32 v224,a242
mvaa 216,120,220
buffer_load_dwordx4 v228,s[40:43],s53 offen offset:0 nt lds
mvaa 208,120,212
s_lshl_b32 s88,s18,1
ra 4,232,2048
v_accvgpr_read_b32 v225,a243
mvaa 208,88,208
s_add_u32 s88,s88,s12
ra 8,232,4096
v_accvgpr_read_b32 v226,a244
mvaa 148,84,208
s_mov_b32 s91,196608
ra 12,232,6144
v_accvgpr_read_b32 v227,a245
mvaa 148,116,212
s_lshl_b32 s88,s88,7
ra 16,232,8192
s_lshl_b32 s89,s18,6
mvaa 156,116,220
s_mov_b32 s90,6291456
ra 20,232,10240
s_mov_b32 s60,-65408
mvaa 156,84,216
buffer_load_dwordx4 v229,s[40:43],s53 offen offset:1024 nt lds
mvaa 152,80,216
s_add_u32 s53,s53,s57
ra 24,232,12288
s_lshl_b32 s87,s20,2
mvaa 152,112,220
s_mov_b32 s26,32768
ra 28,232,14336
s_mov_b32 s27,131072
mvaa 144,112,212
s_add_u32 s61,s98,32768
ta 40,236,0
s_mov_b32 s63,65536
mvaa 144,80,208
s_cmp_ge_u32 s63,65536
s_cselect_b32 s99,-2,1
ta 42,236,512
s_lshl_b32 s99,s99,15
mvaa 84,76,208
s_cmp_eq_u32 s12,0
s_cselect_b32 s65,96,0
ta 44,237,0
s_mov_b32 s67,2048
mvaa 84,108,212
buffer_load_dwordx4 v230,s[44:47],s54 offen offset:2048 nt lds
mvaa 92,108,220
s_mov_b32 s66,8
ta 46,237,512
s_mov_b32 s64,1
mvaa 92,76,216
s_or_b32 s65,s65,128
ta 48,234,4096
mvaa 88,72,216
s_bitcmp1_b32 s12,0
s_cselect_b64 vcc,-1,0
ta 50,234,4608
mvaa 88,104,220
ta 52,235,4096
mvaa 80,104,212
ta 54,235,4608
mvaa 80,72,208
buffer_load_dwordx4 v231,s[44:47],s54 offen offset:3072 nt lds
s_add_u32 m0,s62,49152
mvaa 20,68,208
s_add_u32 s54,s54,s57
ta 56,236,4096
mvaa 20,100,212
s_cmp_lt_u32 s12,2
s_cselect_b32 s57,262144,s91
ta 58,236,4608
mvaa 28,100,220
ta 60,237,4096
mvaa 28,68,216
ta 62,237,4608
mvaa 24,64,216
mvaa 24,96,220
buffer_load_dwordx4 v228,s[40:43],s53 offen offset:0 nt lds
mvaa 16,96,212
v_accvgpr_read_b32 v228,a246
mvaa 16,64,208
s_waitcnt lgkmcnt(5)
maaa 128,0,32
maaa 136,0,36
maaa 140,4,36
buffer_load_dwordx4 v229,s[40:43],s53 offen offset:1024 nt lds
maaa 132,4,32
s_waitcnt lgkmcnt(0)
s_barrier
mvaa 32,8,32
s_add_u32 s53,s53,s58
ta 96,234,16384
v_accvgpr_read_b32 v229,a247
mvaa 40,8,36
s_cmp_lt_u32 s12,2
s_cselect_b32 s42,8388608,s90
ta 98,234,16896
mvaa 44,12,36
s_cmp_lt_u32 s12,2
s_cselect_b32 s53,s88,s89
ra 64,233,0
s_and_b32 s89,s98,8192
mvaa 36,12,32
buffer_load_dwordx4 v230,s[44:47],s54 offen offset:2048 nt lds
mvaa 96,16,32
s_bitcmp1_b32 s21,0
s_cselect_b32 s90,s57,0
ta 100,235,16384
s_add_u32 s53,s53,s90
mvaa 104,16,36
s_cmp_lt_u32 s12,2
s_cselect_b64 s[40:41],s[72:73],s[80:81]
ta 102,235,16896
v_accvgpr_read_b32 v230,a248
mvaa 108,20,36
s_lshl_b32 s90,s90,1
ra 68,233,2048
s_add_u32 s91,s89,65536
mvaa 100,20,32
s_bfe_u32 s88,s21,65539
ra 72,233,4096
s_lshl_b32 s88,s88,1
mvaa 160,24,32
buffer_load_dwordx4 v231,s[44:47],s54 offen offset:3072 nt lds
mvaa 168,24,36
s_add_u32 m0,s98,65536
ra 76,233,6144
s_add_u32 s54,s54,s58
mvaa 172,28,36
s_cmp_eq_u32 s12,3
s_cselect_b64 s[44:45],s[68:69],s[40:41]
ra 80,233,8192
v_accvgpr_read_b32 v231,a249
mvaa 164,28,32
s_cmp_eq_u32 s12,3
s_cselect_b32 s46,0,s42
ra 84,233,10240
s_add_u32 s58,s57,s90
mvaa 180,28,40
s_cmp_eq_u32 s12,3
s_cselect_b32 s54,0,s53
ra 88,233,12288
s_sub_u32 s57,s57,s90
mvaa 188,28,44
global_load_dwordx4 a[168:171],v244,s[30:31] offset:2048
mvaa 184,24,44
s_add_u32 s90,s89,65536
ta 104,236,16384
s_add_u32 s88,s88,s87
mvaa 176,24,40
s_and_b32 s87,s12,1
ra 92,233,14336
s_add_u32 s88,s88,s87
mvaa 116,20,40
s_mul_i32 s88,s88,524288
ta 106,236,16896
s_and_b32 s87,s21,7
mvaa 124,20,44
s_add_u32 s86,s88,33554432
ta 108,237,16384
s_lshl_b32 s87,s87,16
mvaa 120,16,44
global_load_dwordx4 a[176:179],v244,s[84:85] offset:0
mvaa 112,16,40
s_add_u32 s86,s86,s87
ta 110,237,16896
s_lshr_b32 s87,s12,1
mvaa 52,12,40
s_lshl_b32 s87,s87,15
ta 112,234,20480
s_add_u32 s86,s86,s87
mvaa 60,12,44
s_add_u32 s24,s8,s86
s_addc_u32 s25,s9,0
ta 114,234,20992
s_lshl_b32 s86,%1,5
mvaa 56,8,44
s_lshl_b32 s87,s12,3
ta 116,235,20480
s_add_u32 s86,s86,s87
mvaa 48,8,40
global_load_dwordx4 a[164:167],v244,s[30:31] offset:1024
maaa 148,4,40
s_add_u32 s86,s86,83886080
ta 118,235,20992
s_mov_b32 s88,4096
maaa 156,4,44
ta 120,236,20480
maaa 152,0,44
ta 122,236,20992
maaa 144,0,40
ta 124,237,20480
mvaa 0,0,48
global_load_dwordx4 a[172:175],v244,s[30:31] offset:3072
mvaa 8,0,52
s_mov_b32 s30,1048577
ta 126,237,20992
s_mov_b32 s31,268435712
mvaa 12,4,52
mvaa 4,4,48
mvaa 64,8,48
mvaa 72,8,52
global_load_dwordx4 a[180:183],v244,s[84:85] offset:1024
mvaa 76,12,52
mvaa 68,12,48
mvaa 128,16,48
mvaa 136,16,52
)"
R"(mvaa 140,20,52
dl 224,36,52,0
mvaa 132,20,48
mvaa 192,24,48
mvaa 200,24,52
mvaa 204,28,52
mvaa 196,28,48
dl 225,36,52,1024
mvaa 212,28,56
mvaa 220,28,60
mvaa 216,24,60
mvaa 208,24,56
mvaa 148,20,56
dl 226,36,52,2048
mvaa 156,20,60
mvaa 152,16,60
mvaa 144,16,56
mvaa 84,12,56
s_waitcnt vmcnt(12) lgkmcnt(0)
s_barrier
mvaa 92,12,60
dl 227,36,52,3072
mvaa 88,8,60
s_add_u32 s52,s52,s56
s_add_u32 m0,s98,69632
ta 32,234,32768
mvaa 80,8,56
ta 34,234,33280
mvaa 20,4,56
ta 36,235,32768
mvaa 28,4,60
ta 38,235,33280
mvaa 24,0,60
dl 224,36,52,0
mvaa 16,0,56
ta 40,236,32768
maaa 128,64,96
ta 42,236,33280
maaa 136,64,100
ta 44,237,32768
maaa 140,68,100
ta 46,237,33280
maaa 132,68,96
dl 225,36,52,1024
mvaa 32,72,96
ta 48,234,36864
mvaa 40,72,100
ta 50,234,37376
mvaa 44,76,100
ta 52,235,36864
mvaa 36,76,96
ta 54,235,37376
mvaa 96,80,96
dl 226,36,52,2048
mvaa 104,80,100
ra 20,232,43008
mvaa 108,84,100
ra 0,232,32768
mvaa 100,84,96
ra 24,232,45056
mvaa 160,88,96
ra 4,232,34816
mvaa 168,88,100
dl 227,36,52,3072
mvaa 172,92,100
s_add_u32 m0,s62,0
ra 28,232,47104
s_add_u32 s52,s52,s60
mvaa 164,92,96
ra 8,232,36864
mvaa 180,92,104
ra 12,232,38912
mvaa 188,92,108
ra 16,232,40960
v_add_u32_e32 v232,s90,v232
mvaa 184,88,108
dl 228,40,53,0
mvaa 176,88,104
ta 60,237,36864
mvaa 116,84,104
ta 62,237,37376
mvaa 124,84,108
ta 56,236,36864
mvaa 120,80,108
ta 58,236,37376
mvaa 112,80,104
dl 229,40,53,1024
mvaa 52,76,104
s_add_u32 s53,s53,s57
mvaa 60,76,108
mvaa 56,72,108
mvaa 48,72,104
maaa 148,68,104
dl 230,44,54,2048
maaa 156,68,108
maaa 152,64,108
maaa 144,64,104
mvaa 0,64,112
mvaa 8,64,116
dl 231,44,54,3072
mvaa 12,68,116
s_add_u32 m0,s62,16384
s_add_u32 s54,s54,s57
mvaa 4,68,112
mvaa 64,72,112
mvaa 72,72,116
mvaa 76,76,116
dl 228,40,53,0
mvaa 68,76,112
mvaa 128,80,112
mvaa 136,80,116
mvaa 140,84,116
mvaa 132,84,112
dl 229,40,53,1024
mvaa 192,88,112
s_add_u32 s53,s53,s58
mvaa 200,88,116
mvaa 204,92,116
mvaa 196,92,112
mvaa 212,92,120
dl 230,44,54,2048
mvaa 220,92,124
mvaa 216,88,124
s_waitcnt vmcnt(20) lgkmcnt(0)
s_barrier
mvaa 208,88,120
ra 92,233,47104
mvaa 148,84,120
ta 96,234,49152
mvaa 156,84,124
dl 231,44,54,3072
mvaa 152,80,124
s_add_u32 m0,s98,0
ra 88,233,45056
s_add_u32 s54,s54,s58
mvaa 144,80,120
ta 98,234,49664
mvaa 84,76,120
ta 100,235,49152
mvaa 92,76,124
ta 116,235,53248
mvaa 88,72,124
dl 224,36,52,0
mvaa 80,72,120
ta 118,235,53760
mvaa 20,68,120
ta 102,235,49664
v_subrev_u32_e32 v235,s89,v235
mvaa 28,68,124
ta 104,236,49152
mvaa 24,64,124
ta 112,234,53248
mvaa 16,64,120
dl 225,36,52,1024
maaa 128,0,32
ta 124,237,53248
maaa 136,0,36
ta 126,237,53760
maaa 140,4,36
ta 120,236,53248
maaa 132,4,32
ta 122,236,53760
mvaa 32,8,32
dl 226,36,52,2048
mvaa 40,8,36
ta 114,234,53760
v_subrev_u32_e32 v234,s89,v234
mvaa 44,12,36
ta 106,236,49664
v_subrev_u32_e32 v236,s89,v236
mvaa 36,12,32
ta 108,237,49152
mvaa 52,12,40
ta 110,237,49664
v_subrev_u32_e32 v237,s89,v237
mvaa 60,12,44
dl 227,36,52,3072
mvaa 56,8,44
s_add_u32 s52,s52,s56
s_add_u32 m0,s98,4096
ra 84,233,43008
mvaa 48,8,40
ra 80,233,40960
maaa 148,4,40
ra 76,233,38912
maaa 156,4,44
ra 68,233,34816
maaa 152,0,44
dl 224,36,52,0
maaa 144,0,40
ra 72,233,36864
mvaa 0,0,48
ra 64,233,32768
v_add_u32_e32 v233,s91,v233
mvaa 8,0,52
mvaa 12,4,52
mvaa 4,4,48
dl 225,36,52,1024
mvaa 20,4,56
mvaa 28,4,60
mvaa 24,0,60
mvaa 16,0,56
mvaa 220,28,60
dl 226,36,52,2048
s_waitcnt lgkmcnt(2)
mvaa 220,92,124
mvaa 216,88,124
mvaa 216,24,60
mvaa 212,28,56
mvaa 212,92,120
dl 227,36,52,3072
mvaa 208,88,120
s_add_u32 m0,s62,32768
s_add_u32 s52,s52,s60
s_waitcnt vmcnt(25)
zvaa 246,160,176
mvaa 208,24,56
cv 223,222,223
cv 222,220,221
mvaa 204,28,52
cv 221,218,219
zvaa 250,168,176
mvaa 204,92,116
cv 220,216,217
cv 219,214,215
mvaa 200,88,116
dl 228,40,53,0
s_waitcnt vmcnt(25)
mvaa 246,164,180
mvaa 200,24,52
cv 218,212,213
mvaa 250,172,180
mvaa 196,28,48
cv 217,210,211
cv 216,208,209
mvaa 196,92,112
cv 215,206,207
v_cndmask_b32_e64 v246,0,v246,s[30:31]
mvaa 192,88,112
cv 214,204,205
v_cndmask_b32_e64 v250,0,v250,s[30:31]
mvaa 192,24,48
dl 229,40,53,1024
s_lshl_b64 s[30:31],s[30:31],1
v_cndmask_b32_e64 v246,v246,v247,s[30:31]
v_cndmask_b32_e64 v250,v250,v251,s[30:31]
mvaa 188,28,44
s_lshl_b64 s[30:31],s[30:31],1
s_add_u32 s53,s53,s57
v_cndmask_b32_e64 v246,v246,v248,s[30:31]
v_cndmask_b32_e64 v250,v250,v252,s[30:31]
mvaa 188,92,108
s_lshl_b64 s[30:31],s[30:31],1
cv 213,202,203
v_cndmask_b32_e64 v246,v246,v249,s[30:31]
mvaa 184,88,108
v_cndmask_b32_e64 v250,v250,v253,s[30:31]
v_add_f32_dpp v246,v246,v246 quad_perm:[1,0,3,2] row_mask:0xf bank_mask:0xf
mvaa 184,24,44
v_add_f32_dpp v250,v250,v250 quad_perm:[1,0,3,2] row_mask:0xf bank_mask:0xf
v_add_f32_dpp v246,v246,v246 quad_perm:[2,3,0,1] row_mask:0xf bank_mask:0xf
mvaa 180,28,40
dl 230,44,54,2048
v_add_f32_dpp v250,v250,v250 quad_perm:[2,3,0,1] row_mask:0xf bank_mask:0xf
mvaa 180,92,104
v_add_f32_dpp v246,v246,v246 row_half_mirror row_mask:0xf bank_mask:0xf
v_add_f32_dpp v250,v250,v250 row_half_mirror row_mask:0xf bank_mask:0xf
mvaa 176,88,104
v_add_f32_dpp v246,v246,v246 row_mirror row_mask:0xf bank_mask:0xf
v_add_f32_dpp v250,v250,v250 row_mirror row_mask:0xf bank_mask:0xf
mvaa 176,24,40
cv 212,200,201
cv 211,198,199
mvaa 172,28,36
v_permlane16_swap_b32_e32 v246,v250
mvaa 172,92,100
dl 231,44,54,3072
v_add_f32_e32 v246,v246,v250
v_mov_b32_e32 v250,v246
mvaa 168,88,100
s_add_u32 s54,s54,s57
v_permlane32_swap_b32_e32 v246,v250
mvaa 168,24,36
v_add_f32_e32 v246,v246,v250
v_lshrrev_b32_e32 v250,6,v244
mvaa 164,28,32
v_mul_f32_e32 v246,0xbfb8aa3b,v246
cv 210,196,197
mvaa 164,92,96
ex 246
mvaa 160,88,96
buffer_load_dword a242,v241,s[48:51],s55 offen
mvaa 160,24,32
s_add_u32 s55,s55,s59
v_add_f32_e32 v246,1.0,v246
cv 209,194,195
mvaa 156,20,60
v_rcp_f32_e32 v246,v246
mvaa 156,84,124
s_waitcnt vmcnt(17) lgkmcnt(0)
s_barrier
s_add_u32 m0,s62,49152
mvaa 152,80,124
cv 208,192,193
cv 207,190,191
mvaa 152,16,60
dl 228,40,53,0
mvaa 148,20,56
cv 206,188,189
cv 205,186,187
mvaa 148,84,120
cv 204,184,185
cv 203,182,183
mvaa 144,80,120
cv 202,180,181
cv 201,178,179
mvaa 144,16,56
cv 200,176,177
cv 199,174,175
mvaa 140,20,52
dl 229,40,53,1024
mvaa 140,84,116
s_add_u32 s53,s53,s58
cv 198,172,173
cv 197,170,171
mvaa 136,80,116
ra 4,232,0
cv 196,168,169
mvaa 136,16,52
ta 92,239,12288
cv 195,166,167
mvaa 132,20,48
ta 94,239,12800
cv 194,164,165
mvaa 132,84,112
dl 230,44,54,2048
mvaa 128,80,112
ta 88,238,12288
cv 193,162,163
mvaa 128,16,48
ta 90,238,12800
cv 192,160,161
mvaa 124,20,44
cv 191,158,159
cv 190,156,157
mvaa 124,84,108
cv 189,154,155
cv 188,152,153
mvaa 120,80,108
dl 231,44,54,3072
mvaa 120,16,44
s_add_u32 m0,s98,32768
cv 187,150,151
cv 186,148,149
mvaa 116,20,40
s_add_u32 s54,s54,s58
cv 185,146,147
cv 184,144,145
mvaa 116,84,104
cv 183,142,143
cv 182,140,141
mvaa 112,80,104
cv 181,138,139
cv 180,136,137
mvaa 112,16,40
dl 224,36,52,0
mvaa 108,20,36
cv 179,134,135
cv 178,132,133
mvaa 108,84,100
cv 177,130,131
cv 176,128,129
mvaa 104,80,100
cv 175,126,127
cv 174,124,125
mvaa 104,16,36
ta 44,235,0
cv 173,122,123
mvaa 100,20,32
dl 225,36,52,1024
mvaa 100,84,96
ta 46,235,512
cv 172,120,121
mvaa 96,80,96
cv 171,118,119
cv 170,116,117
mvaa 96,16,32
ta 40,234,0
cv 169,114,115
mvaa 92,12,60
ta 42,234,512
cv 168,112,113
mvaa 92,76,124
dl 226,36,52,2048
mvaa 88,72,124
cv 167,110,111
cv 166,108,109
mvaa 88,8,60
cv 165,106,107
cv 164,104,105
mvaa 84,12,56
ta 84,237,8192
cv 163,102,103
mvaa 84,76,120
ta 80,236,8192
cv 162,100,101
mvaa 80,72,120
dl 227,36,52,3072
mvaa 80,8,56
s_add_u32 m0,s62,0
ta 86,237,8704
cv 161,98,99
mvaa 76,12,52
s_add_u32 s52,s52,s56
ta 82,236,8704
cv 160,96,97
mvaa 76,76,116
ra 16,232,6144
cv 159,94,95
mvaa 72,72,116
ta 60,235,4096
cv 158,92,93
mvaa 72,8,52
buffer_load_dword a243,v241,s[48:51],s55 offen
mvaa 68,12,48
s_add_u32 s55,s55,s59
ta 56,234,4096
cv 151,78,79
mvaa 68,76,112
ta 62,235,4608
cv 150,76,77
mvaa 64,72,112
ta 58,234,4608
cv 157,90,91
mvaa 64,8,48
ta 52,237,0
cv 156,88,89
mvaa 60,76,108
buffer_load_dword a244,v241,s[48:51],s55 offen
mvaa 56,72,108
s_add_u32 s55,s55,s59
ra 12,232,4096
cv 147,70,71
mvaa 52,76,104
ta 54,237,512
cv 146,68,69
mvaa 48,72,104
ra 8,232,2048
cv 145,66,67
mvaa 44,76,100
ta 48,236,0
cv 144,64,65
mvaa 40,72,100
buffer_load_dword a245,v241,s[48:51],s55 offen
mvaa 36,76,96
s_add_u32 s55,s55,s59
ta 50,236,512
cv 143,62,63
mvaa 32,72,96
cv 142,60,61
cv 141,58,59
mvaa 28,68,124
cv 140,56,57
cv 139,54,55
mvaa 24,64,124
cv 138,52,53
cv 137,50,51
mvaa 20,68,120
s_mov_b64 exec,65537
buffer_store_dword v246,v250,s[8:11],s86 offen sc0 sc1
s_mov_b64 exec,-1
mvaa 16,64,120
cv 136,48,49
cv 135,46,47
mvaa 12,68,116
cv 134,44,45
cv 133,42,43
mvaa 8,64,116
ta 76,235,8192
cv 132,40,41
mvaa 4,68,112
ta 78,235,8704
cv 131,38,39
mvaa 0,64,112
ta 72,234,8192
cv 130,36,37
maaa 156,68,108
cv 129,34,35
cv 128,32,33
maaa 152,64,108
ta 74,234,8704
cv 127,30,31
maaa 148,68,104
cv 126,28,29
cv 125,26,27
maaa 144,64,104
v_accvgpr_read_b32 v63,a159
v_accvgpr_read_b32 v62,a158
maaa 140,68,100
v_accvgpr_read_b32 v61,a157
v_accvgpr_read_b32 v60,a156
maaa 136,64,100
v_accvgpr_read_b32 v59,a155
v_accvgpr_read_b32 v58,a154
maaa 132,68,96
v_accvgpr_read_b32 v57,a153
v_accvgpr_read_b32 v56,a152
maaa 128,64,96
cv 56,56,57
cv 57,58,59
s_waitcnt lgkmcnt(3)
zaaa 236,40,4
ta 68,237,4096
ta 70,237,4608
zaaa 220,44,4
ta 64,236,4096
ta 66,236,4608
zaaa 216,44,8
cv 58,60,61
cv 59,62,63
zaaa 232,40,8
buffer_store_dwordx4 v[56:59],v244,s[24:27],0 offen offset:3072 sc0 sc1
zaaa 228,40,12
v_accvgpr_read_b32 v55,a151
v_accvgpr_read_b32 v54,a150
zaaa 212,44,12
v_accvgpr_read_b32 v53,a149
v_accvgpr_read_b32 v52,a148
zaaa 208,44,16
v_accvgpr_read_b32 v51,a147
v_accvgpr_read_b32 v50,a146
zaaa 224,40,16
v_accvgpr_read_b32 v49,a145
v_accvgpr_read_b32 v48,a144
zaaa 192,48,16
cv 48,48,49
cv 49,50,51
zaaa 176,52,16
cv 50,52,53
cv 51,54,55
zaaa 180,52,12
buffer_store_dwordx4 v[48:51],v244,s[24:27],0 offen offset:2048 sc0 sc1
zaaa 196,48,12
s_waitcnt vmcnt(27) lgkmcnt(0)
s_barrier
ra 24,233,0
v_accvgpr_read_b32 v47,a143
zaaa 200,48,8
ra 28,233,2048
v_accvgpr_read_b32 v46,a142
v_accvgpr_read_b32 v45,a141
zaaa 184,52,8
ra 32,233,4096
ra 36,233,6144
zaaa 188,52,4
v_accvgpr_read_b32 v43,a139
v_accvgpr_read_b32 v42,a138
zaaa 204,48,4
dl 228,40,53,0
ta 96,236,24576
zaaa 172,56,4
ta 98,236,25088
v_accvgpr_read_b32 v41,a137
zaaa 156,60,4
ta 100,237,24576
v_accvgpr_read_b32 v39,a135
zaaa 152,60,8
ta 102,237,25088
ta 104,238,28672
zaaa 168,56,8
v_accvgpr_read_b32 v38,a134
v_accvgpr_read_b32 v35,a131
zaaa 164,56,12
dl 229,40,53,1024
ta 106,238,29184
zaaa 148,60,12
s_add_u32 s53,s53,s57
ta 108,239,28672
v_accvgpr_read_b32 v34,a130
zaaa 144,60,16
ta 110,239,29184
v_accvgpr_read_b32 v33,a129
zaaa 160,56,16
ta 44,235,16384
ta 46,235,16896
ta 40,234,16384
ta 42,234,16896
ta 52,237,16384
ta 54,237,16896
ta 48,236,16384
ta 50,236,16896
ta 60,235,20480
ta 62,235,20992
ta 56,234,20480
ta 58,234,20992
v_accvgpr_read_b32 v32,a128
zaaa 128,64,16
v_accvgpr_read_b32 v37,a133
v_accvgpr_read_b32 v36,a132
zaaa 112,68,16
dl 230,44,54,2048
zaaa 116,68,12
v_accvgpr_read_b32 v40,a136
v_accvgpr_read_b32 v44,a140
zaaa 132,64,12
cv 251,42,43
cv 250,40,41
zaaa 136,64,8
cv 253,46,47
cv 252,44,45
zaaa 120,68,8
cv 249,38,39
cv 247,34,35
zaaa 124,68,4
dl 231,44,54,3072
zaaa 140,64,4
s_add_u32 m0,s61,4096
cv 246,32,33
cv 248,36,37
zvaa 40,72,4
s_add_u32 s54,s54,s57
ta 68,237,20480
cv 124,24,25
zvaa 44,76,4
s_add_u32 s61,s63,s98
ta 70,237,20992
cv 123,22,23
zvaa 52,76,8
s_add_u32 s63,s63,s99
ta 64,236,20480
cv 121,18,19
zvaa 48,72,8
dl 224,36,52,0
zvaa 56,72,12
ta 66,236,20992
cv 120,16,17
zvaa 60,76,12
cv 122,20,21
cv 119,14,15
zvaa 68,76,16
cv 118,12,13
cv 117,10,11
zvaa 64,72,16
cv 116,8,9
cv 153,82,83
zvaa 32,80,16
dl 225,36,52,1024
ta 76,235,24576
ta 78,235,25088
zvaa 36,84,16
ta 72,234,24576
ta 74,234,25088
zvaa 28,84,12
cv 152,80,81
cv 155,86,87
zvaa 24,80,12
cv 154,84,85
cv 149,74,75
zvaa 16,80,8
cv 148,72,73
v_add_u32_e32 v232,s99,v232
zvaa 20,84,8
dl 226,36,52,2048
zvaa 12,84,4
cv 115,6,7
cv 114,4,5
zvaa 8,80,4
cv 113,2,3
cv 112,0,1
zvaa 76,4,88
zvaa 96,4,92
zvaa 100,8,92
dl 227,36,52,3072
zvaa 80,8,88
s_add_u32 s52,s52,s60
zvaa 84,12,88
zvaa 104,12,92
zvaa 108,16,92
s_waitcnt vmcnt(23) lgkmcnt(0)
s_barrier
zvaa 88,16,88
buffer_store_dwordx4 v[250:253],v244,s[24:27],0 offen offset:1024 sc0 sc1
buffer_store_dwordx4 v[246:249],v244,s[24:27],0 offen offset:0 sc0 sc1
L_kvx:
L_p_exit:
s_add_u32 m0,s62,16384
ra 4,232,0
s_lshl_b32 s86,s21,2
maaa 236,40,24
dl 228,40,53,0
maaa 220,44,24
s_add_u32 s86,s86,s12
ra 8,232,2048
s_lshl_b32 s86,s86,8
maaa 216,44,28
s_add_u32 s86,s86,L_p_exit-L_att_pc+2048
ra 12,232,4096
v_lshl_add_u32 v253,v244,3,s86
maaa 232,40,28
s_mov_b32 s67,4096
ra 16,232,6144
v_add_u32_e32 v233,0xffff0000,v233
maaa 228,40,32
s_cmp_lt_u32 s12,2
s_cselect_b32 s94,s22,s23
ra 80,233,0
v_mov_b32_e32 v248,-1
maaa 212,44,32
dl 229,40,53,1024
maaa 208,44,36
s_lshr_b32 s94,s94,6
ra 84,233,2048
v_mov_b32_e32 v249,-1
maaa 224,40,36
s_and_b32 s87,s12,1
ra 88,233,4096
v_mov_b32_e32 v250,-1
maaa 192,48,36
s_add_u32 s94,s94,s87
ra 92,233,6144
v_mov_b32_e32 v251,-1
maaa 176,52,36
s_lshl_b32 s87,s18,5
ta 0,234,32768
s_add_u32 s94,s94,s87
maaa 180,52,32
dl 230,44,54,2048
maaa 196,48,32
s_lshl_b32 s94,s94,11
ta 2,234,33280
s_add_u32 s94,s94,12582912
maaa 200,48,28
s_add_u32 s53,s53,s58
ta 40,235,32768
s_bfe_u32 s88,s21,65539
maaa 184,52,28
s_lshl_b32 s87,s20,1
ta 42,235,33280
s_add_u32 s88,s88,s87
maaa 188,52,24
s_lshl_b32 s88,s88,7
v_mov_b32_e32 v247,1
s_add_u32 s85,s88,83894272
maaa 204,48,24
dl 231,44,54,3072
maaa 172,56,24
s_add_u32 m0,s98,65536
ta 52,236,32768
s_add_u32 s54,s54,s58
maaa 156,60,24
s_lshl_b32 s91,s12,4
ta 54,236,33280
v_bfe_u32 v252,v244,4,4
maaa 152,60,28
s_cmp_lt_u32 s12,2
s_cselect_b32 s90,s22,s23
v_bfe_u32 v241,v244,6,4
v_and_b32_e32 v241,12,v241
maaa 168,56,28
s_mul_i32 s33,s90,10240
v_sub_u32_e32 v252,v252,v241
v_add_u32_e32 v252,s91,v252
maaa 164,56,32
s_mov_b64 exec,3
global_load_dword a250,v253,s[96:97]
s_mov_b64 exec,-1
maaa 148,60,32
s_cmp_lt_u32 s12,2
s_cselect_b32 s91,32768,0
v_add_u32_e32 v253,10240,v253
s_add_u32 s91,s91,98304
maaa 144,60,36
s_mov_b32 s64,0x55505555
v_accvgpr_read_b32 v255,a241
v_mov_b32_e32 v241,0xf149f2ca
maaa 160,56,36
s_mov_b32 s65,0x50005500
s_lshl_b32 s95,s12,12
s_mov_b32 s66,0x50000
maaa 128,64,36
s_add_u32 s70,s95,32768
s_add_u32 s72,s95,65536
s_lshl_b32 s4,s21,2
maaa 112,68,36
dl 224,36,52,0
maaa 116,68,32
s_add_u32 s4,s4,s12
ta 56,237,32768
s_lshl_b32 s4,s4,8
maaa 132,64,32
s_bitcmp1_b32 170,s20
s_cselect_b32 s5,L_odd-L_last,0
ta 58,237,33280
s_add_u32 s4,s4,s5
maaa 136,64,28
s_add_u32 s4,s4,L_last-L_att_pc
s_mov_b64 s[30:31],s[28:29]
s_mov_b64 s[28:29],s[78:79]
maaa 120,68,28
s_add_u32 s68,s95,0
v_cvt_f32_i32_e32 v252,v252
v_mul_f32_e32 v252,0x7149f2ca,v252
maaa 124,68,24
dl 225,36,52,1024
maaa 140,64,24
s_add_u32 s69,s95,16384
s_add_u32 s74,s95,98304
s_add_u32 s71,s95,49152
mvaa 40,72,24
s_waitcnt vmcnt(17) lgkmcnt(2)
s_barrier
mvaa 44,76,24
s_add_u32 s73,s95,81920
ta 20,234,49152
v_and_b32_e32 v245,48,v244
mvaa 52,76,28
s_lshl_b32 s59,%1,2
ta 22,234,49664
v_lshlrev_b32_e32 v245,3,v245
mvaa 48,72,28
s_lshl_b32 s84,%1,2
ta 44,235,49152
s_add_u32 s59,s59,s12
mvaa 56,72,32
dl 226,36,52,2048
mvaa 60,76,32
s_add_u32 s75,s95,114688
ta 46,235,49664
s_add_u32 s84,s84,s12
mvaa 68,76,36
s_mul_i32 s84,s84,12288
ta 48,236,49152
s_sub_u32 s55,s14,s13
mvaa 64,72,36
s_mov_b32 s48,0x38000000
ta 50,236,49664
s_mov_b32 s81,1
mvaa 32,96,36
s_mov_b32 s84,0
ta 60,237,49152
mvaa 36,100,36
dl 227,36,52,3072
mvaa 28,100,32
s_add_u32 s52,s52,s56
s_add_u32 m0,s98,69632
ta 62,237,49664
mvaa 24,96,32
ta 68,234,36864
mvaa 16,96,28
ta 70,234,37376
mvaa 20,100,28
ta 64,234,53248
mvaa 12,100,24
dl 224,36,52,0
mvaa 8,96,24
ta 66,234,53760
mvaa 76,24,104
ta 76,235,53248
mvaa 96,24,108
ta 78,235,53760
mvaa 100,28,108
ta 72,235,36864
mvaa 80,28,104
dl 225,36,52,1024
mvaa 84,32,104
ta 74,235,37376
mvaa 104,32,108
mvaa 108,36,108
mvaa 88,36,104
maaa 236,0,4
s_waitcnt lgkmcnt(5)
maaa 236,20,80
dl 226,36,52,2048
maaa 220,44,80
maaa 220,40,4
maaa 216,40,8
maaa 216,44,84
maaa 232,20,84
dl 227,36,52,3072
maaa 232,0,8
s_add_u32 s52,s52,s60
s_add_u32 m0,s98,0
maaa 228,0,12
maaa 228,20,88
maaa 212,44,88
maaa 212,40,12
dl 224,36,52,0
maaa 208,40,16
maaa 208,44,92
maaa 224,20,92
ta 40,236,36864
maaa 224,0,16
ta 42,236,37376
maaa 192,52,16
dl 225,36,52,1024
maaa 192,48,92
ta 44,236,53248
maaa 176,60,92
ta 46,236,53760
maaa 176,56,16
ta 20,237,53248
maaa 180,56,12
ta 22,237,53760
maaa 180,60,88
dl 226,36,52,2048
maaa 196,48,88
ta 0,237,36864
maaa 196,52,12
ta 2,237,37376
maaa 200,52,8
s_waitcnt vmcnt(18)
ra 24,232,32768
maaa 200,48,84
ra 28,232,34816
maaa 184,60,84
ra 32,232,36864
maaa 184,56,8
buffer_store_dwordx4 v[112:115],v244,s[24:27],s67 offen offset:0 sc0 sc1
maaa 188,56,4
ra 36,232,38912
v_add_u32_e32 v232,0x10000,v232
maaa 188,60,80
ra 96,233,32768
v_cndmask_b32_e64 v112,v241,0,s[64:65]
maaa 204,48,80
s_mov_b32 s64,0xaaa0aaaa
ra 100,233,34816
s_mov_b32 s65,0xa000aa00
maaa 204,52,4
ra 104,233,36864
v_cndmask_b32_e64 v113,v241,0,s[64:65]
maaa 172,68,4
dl 227,36,52,3072
s_waitcnt lgkmcnt(8)
maaa 172,64,80
s_add_u32 s52,s52,s56
s_add_u32 m0,s98,4096
ra 108,233,38912
maaa 156,76,80
s_mov_b32 s64,0x55405554
ta 56,234,40960
v_add_u32_e32 v233,0x10000,v233
maaa 156,72,4
s_mov_b32 s65,0x40005400
ta 58,234,41472
v_cndmask_b32_e64 v114,v241,0,s[64:65]
maaa 152,72,8
s_mov_b32 s64,0xaa80aaa8
ta 60,234,57344
s_mov_b32 s65,0x8000a800
maaa 152,76,84
dl 224,36,52,0
maaa 168,64,84
ta 62,234,57856
v_cndmask_b32_e64 v115,v241,0,s[64:65]
maaa 168,68,8
ta 48,235,57344
maaa 164,68,12
ta 50,235,57856
maaa 164,64,88
ta 52,235,40960
maaa 148,76,88
buffer_store_dwordx4 v[248:251],v244,s[8:11],s94 offen offset:0 sc0 sc1
maaa 148,72,12
ta 54,235,41472
maaa 144,72,16
maaa 144,76,92
maaa 160,64,92
ta 72,236,40960
maaa 160,68,16
ta 74,236,41472
maaa 128,40,16
dl 225,36,52,1024
maaa 128,44,92
ta 76,236,57344
maaa 112,20,92
ta 78,236,57856
s_waitcnt lgkmcnt(6)
maaa 112,0,16
ta 64,237,57344
maaa 116,0,12
ta 66,237,57856
maaa 116,20,88
dl 226,36,52,2048
maaa 132,44,88
ta 68,237,40960
maaa 132,40,12
ta 70,237,41472
maaa 136,40,8
maaa 136,44,84
maaa 120,20,84
dl 227,36,52,3072
maaa 120,0,8
s_add_u32 s52,s52,s60
maaa 124,0,4
maaa 124,20,80
maaa 140,44,80
ta 0,238,45056
ta 2,238,45568
maaa 140,40,4
)"
R"(buffer_store_dwordx4 v[248:251],v244,s[8:11],s94 offen offset:1024 sc0 sc1
ta 20,238,61440
ta 22,238,61952
mvaa 40,56,4
ta 44,239,61440
ta 46,239,61952
mvaa 40,60,80
ta 40,239,45056
ta 42,239,45568
mvaa 44,48,80
v_and_b32_e32 v250,240,v244
v_accvgpr_read_b32 v249,a240
s_waitcnt lgkmcnt(8)
mvaa 44,52,4
mvaa 52,52,8
mvaa 52,48,84
mvaa 48,60,84
mvaa 48,56,8
mvaa 56,56,12
mvaa 56,60,88
mvaa 60,48,88
mvaa 60,52,12
mvaa 68,52,16
mvaa 68,48,92
mvaa 64,60,92
mvaa 64,56,16
mvaa 32,72,16
s_waitcnt vmcnt(11) lgkmcnt(0)
s_barrier
s_add_u32 m0,s62,32768
mvaa 32,76,92
dl 228,40,53,0
mvaa 36,64,92
ta 52,234,0
mvaa 36,68,16
ta 54,234,512
mvaa 28,68,12
ta 48,234,16384
mvaa 28,64,88
ta 50,234,16896
mvaa 24,76,88
ta 60,235,16384
mvaa 24,72,12
dl 229,40,53,1024
mvaa 16,72,8
s_add_u32 s53,s53,s57
ta 62,235,16896
mvaa 16,76,84
ta 56,235,0
mvaa 20,64,84
ta 58,235,512
mvaa 20,68,8
mvaa 12,68,4
dl 230,44,54,2048
mvaa 12,64,80
mvaa 8,76,80
ta 68,236,0
mvaa 8,72,4
ta 70,236,512
mvaa 76,4,0
ta 64,236,16384
mvaa 76,80,20
dl 231,44,54,3072
mvaa 96,80,44
s_add_u32 m0,s62,49152
s_add_u32 s54,s54,s57
ta 66,236,16896
mvaa 96,4,40
ta 76,237,16384
mvaa 100,8,40
ta 78,237,16896
mvaa 100,84,44
ta 72,237,0
mvaa 80,84,20
ta 74,237,512
mvaa 80,8,0
dl 228,40,53,0
mvaa 84,12,0
ra 80,233,0
mvaa 84,88,20
ra 4,232,0
mvaa 104,88,44
ra 84,233,2048
mvaa 104,12,40
ra 8,232,2048
mvaa 108,16,40
dl 229,40,53,1024
mvaa 108,92,44
s_add_u32 s53,s53,s58
ra 88,233,4096
mvaa 88,92,20
ta 40,234,4096
mvaa 88,16,0
ta 42,234,4608
s_waitcnt lgkmcnt(8)
maaa 236,52,24
ta 44,234,20480
maaa 236,48,96
dl 230,44,54,2048
maaa 220,60,96
ta 46,234,20992
maaa 220,56,24
ta 20,235,20480
maaa 216,56,28
ta 22,235,20992
maaa 216,60,100
ta 0,235,4096
maaa 232,48,100
dl 231,44,54,3072
maaa 232,52,28
s_add_u32 m0,s98,32768
ta 2,235,4608
s_add_u32 s54,s54,s58
maaa 228,52,32
ra 12,232,4096
maaa 228,48,104
ra 16,232,6144
v_add_u32_e32 v232,0xffff0000,v232
maaa 212,60,104
ra 92,233,6144
v_add_u32_e32 v233,0xffff0000,v233
maaa 212,56,32
maaa 208,56,36
dl 224,36,52,0
maaa 208,60,108
maaa 224,48,108
ta 56,236,4096
maaa 224,52,36
ta 58,236,4608
maaa 192,68,36
ta 60,236,20480
maaa 192,64,108
dl 225,36,52,1024
maaa 176,76,108
ta 62,236,20992
s_waitcnt lgkmcnt(6)
maaa 176,72,36
ta 48,237,20480
maaa 180,72,32
ta 50,237,20992
maaa 180,76,104
ta 52,237,4096
maaa 196,64,104
buffer_store_dwordx4 v[116:119],v244,s[24:27],s67 offen offset:1024 sc0 sc1
maaa 196,68,32
ta 54,237,4608
maaa 200,68,28
maaa 200,64,100
maaa 184,76,100
maaa 184,72,28
dl 226,36,52,2048
maaa 188,72,24
maaa 188,76,96
maaa 204,64,96
ta 72,234,8192
maaa 204,68,24
ta 74,234,8704
maaa 172,40,24
ta 76,234,24576
maaa 172,44,96
dl 227,36,52,3072
maaa 156,20,96
s_add_u32 s52,s52,s56
s_add_u32 m0,s98,36864
ta 78,234,25088
maaa 156,0,24
ta 64,235,24576
maaa 152,0,28
ta 66,235,25088
maaa 152,20,100
ta 68,235,8192
maaa 168,44,100
dl 224,36,52,0
maaa 168,40,28
ta 70,235,8704
maaa 164,40,32
maaa 164,44,104
maaa 148,20,104
maaa 148,0,32
buffer_store_dwordx4 v[120:123],v244,s[24:27],s67 offen offset:2048 sc0 sc1
maaa 144,0,36
maaa 144,20,108
maaa 160,44,108
ta 0,236,8192
maaa 160,40,36
ta 2,236,8704
s_waitcnt lgkmcnt(3)
maaa 128,56,36
dl 225,36,52,1024
maaa 128,60,108
ta 20,236,24576
maaa 112,48,108
ta 22,236,25088
maaa 112,52,36
ta 44,237,24576
maaa 116,52,32
ta 46,237,25088
maaa 116,48,104
dl 226,36,52,2048
maaa 132,60,104
ta 40,237,8192
maaa 132,56,32
ta 42,237,8704
maaa 136,56,28
maaa 136,60,100
maaa 120,48,100
maaa 120,52,28
buffer_load_dword v246,off,s[8:11],s35 offset:1024 sc1
maaa 124,52,24
maaa 124,48,96
maaa 140,60,96
ta 52,238,12288
ta 54,238,12800
maaa 140,56,24
ta 48,238,28672
ta 50,238,29184
mvaa 40,72,24
buffer_store_dwordx4 v[124:127],v244,s[24:27],s67 offen offset:3072 sc0 sc1
ta 60,239,28672
mvaa 40,76,96
s_mov_b32 s67,8192
ta 62,239,29184
mvaa 44,64,96
ta 56,239,12288
ta 58,239,12800
s_waitcnt lgkmcnt(8)
mvaa 44,68,24
mvaa 52,68,28
mvaa 52,64,100
dl 227,36,52,3072
mvaa 48,76,100
s_add_u32 s52,s52,s60
mvaa 48,72,28
mvaa 56,72,32
mvaa 56,76,104
mvaa 60,64,104
mvaa 60,68,32
mvaa 68,68,36
mvaa 68,64,108
mvaa 64,76,108
mvaa 64,72,36
mvaa 32,0,36
s_waitcnt vmcnt(12) lgkmcnt(0)
s_barrier
s_add_u32 m0,s62,0
mvaa 32,20,108
dl 228,40,53,0
mvaa 36,44,108
ta 68,234,32768
mvaa 36,40,36
ta 70,234,33280
mvaa 28,40,32
ta 64,234,49152
mvaa 28,44,104
ta 66,234,49664
mvaa 24,20,104
ta 76,235,49152
mvaa 24,0,32
dl 229,40,53,1024
mvaa 16,0,28
s_add_u32 s53,s53,s57
ta 78,235,49664
mvaa 16,20,100
ta 72,235,32768
mvaa 20,44,100
ta 74,235,33280
mvaa 20,40,28
mvaa 12,40,24
mvaa 12,44,96
dl 230,44,54,2048
mvaa 8,20,96
ta 40,236,32768
mvaa 8,0,24
ta 42,236,33280
mvaa 76,24,52
ta 44,236,49152
mvaa 76,96,48
ta 46,236,49664
mvaa 96,96,60
dl 231,44,54,3072
mvaa 96,24,56
s_add_u32 m0,s62,16384
s_add_u32 s54,s54,s57
ta 20,237,49152
mvaa 100,28,56
ta 22,237,49664
mvaa 100,100,60
ta 0,237,32768
mvaa 80,100,48
ta 2,237,33280
mvaa 80,28,52
dl 228,40,53,0
mvaa 84,32,52
ra 96,233,0
mvaa 84,104,48
ra 24,232,0
mvaa 104,104,60
ra 100,233,2048
mvaa 104,32,56
ra 28,232,2048
mvaa 108,36,56
dl 229,40,53,1024
mvaa 108,108,60
s_add_u32 s53,s53,s58
ra 104,233,4096
mvaa 88,108,48
ta 56,234,36864
mvaa 88,36,52
ta 58,234,37376
s_waitcnt lgkmcnt(8)
maaa 236,68,4
ta 60,234,53248
maaa 236,64,80
ta 62,234,53760
maaa 220,76,80
dl 230,44,54,2048
maaa 220,72,4
ta 48,235,53248
maaa 216,72,8
ta 50,235,53760
maaa 216,76,84
ta 52,235,36864
maaa 232,64,84
ta 54,235,37376
maaa 232,68,8
dl 231,44,54,3072
maaa 228,68,12
s_add_u32 m0,s98,65536
ra 32,232,4096
s_add_u32 s54,s54,s58
maaa 228,64,88
ra 36,232,6144
maaa 212,76,88
ra 108,233,6144
maaa 212,72,12
s_waitcnt vmcnt(10)
v_readfirstlane_b32 s87,v246
maaa 208,72,16
dl 224,36,52,0
maaa 208,76,92
s_cmp_eq_u32 s12,0
s_cselect_b32 s89,s32,s87
s_cmp_lg_u32 s87,s89
s_cbranch_scc1 L_q_ack
L_q_acked:
maaa 224,64,92
s_and_b32 s87,s12,1
ta 72,236,36864
s_lshl_b32 s88,s87,6
maaa 224,68,16
s_add_u32 s90,s90,s88
ta 74,236,37376
s_lshl_b32 s88,s87,10
maaa 192,40,16
s_lshl_b32 s87,s87,14
ta 76,236,53248
s_add_u32 s91,s91,s87
maaa 192,44,92
dl 225,36,52,1024
maaa 176,20,92
s_lshr_b32 s87,s18,1
ta 78,236,53760
s_lshl_b32 s87,s87,4
s_waitcnt lgkmcnt(5)
maaa 176,0,16
s_add_u32 s33,s33,s88
ta 64,237,53248
v_cmp_eq_u32_e64 s[100:101],s87,v250
maaa 180,0,12
s_lshl_b32 s88,s18,11
ta 66,237,53760
v_lshlrev_b32_e32 v250,3,v250
maaa 180,20,88
s_add_u32 s88,s33,s88
ta 68,237,36864
v_add_u32_e32 v250,83894272,v250
maaa 196,44,88
s_add_u32 s33,s88,63176704
ta 70,237,37376
v_add_u32_e32 v251,s91,v244
maaa 196,40,12
buffer_store_dwordx4 v[128:131],v244,s[24:27],s67 offen offset:0 sc0 sc1
maaa 200,40,8
s_add_u32 s34,s88,63504384
v_cndmask_b32_e64 v242,31,-1,s[100:101]
v_and_b32_e32 v243,528,v242
maaa 200,44,84
s_add_u32 s50,s88,63832064
s_add_u32 s51,s88,64159744
s_lshr_b32 s91,s21,3
maaa 184,20,84
s_and_b32 s88,s20,4
s_and_b32 s89,s20,3
s_add_u32 s91,s91,s88
maaa 184,0,8
s_lshl_b32 s88,s89,6
s_lshl_b32 s86,s21,2
s_add_u32 s86,s86,s12
maaa 188,0,4
dl 226,36,52,2048
maaa 188,20,80
s_cmp_eq_u32 s90,0
s_cselect_b64 s[64:65],-1,0
s_and_b32 s89,s86,1
ta 0,234,40960
maaa 204,44,80
s_lshl_b32 s89,s89,9
ta 2,234,41472
s_lshr_b32 s49,s86,3
maaa 204,40,4
s_lshl_b32 s49,s49,11
ta 20,234,57344
v_add_f32_e32 v128,0x70c9f2ca,v252
maaa 172,56,4
ta 22,234,57856
v_add_f32_e32 v129,0xf0c9f2ca,v252
maaa 172,60,80
dl 227,36,52,3072
maaa 156,48,80
s_add_u32 s52,s52,s56
s_add_u32 m0,s98,69632
ta 44,235,57344
maaa 156,52,4
ta 46,235,57856
v_add_f32_e32 v130,0xf1977617,v252
maaa 152,52,8
ta 40,235,40960
v_add_f32_e32 v131,0xf1fc6f7c,v252
maaa 152,48,84
ta 42,235,41472
v_min_f32_e32 v128,0xc1a00000,v128
maaa 168,60,84
dl 224,36,52,0
maaa 168,56,8
v_min_f32_e32 v129,0xc1a00000,v129
v_min_f32_e32 v130,0xc1a00000,v130
maaa 164,56,12
v_min_f32_e32 v131,0xc1a00000,v131
v_lshrrev_b32_e32 v246,6,v244
maaa 164,60,88
v_lshl_add_u32 v245,v246,22,v245
v_lshlrev_b32_e32 v246,16,v244
maaa 148,48,88
maaa 148,52,12
maaa 144,52,16
buffer_store_dwordx4 v[132:135],v244,s[24:27],s67 offen offset:1024 sc0 sc1
maaa 144,48,92
maaa 160,60,92
ta 52,236,40960
v_add_f32_e32 v132,0xf343a333,v252
maaa 160,56,16
ta 54,236,41472
v_add_f32_e32 v133,0xf3504260,v252
s_waitcnt lgkmcnt(2)
maaa 128,72,16
ta 48,236,57344
v_add_f32_e32 v134,0xf35ce18d,v252
maaa 128,76,92
dl 225,36,52,1024
maaa 112,64,92
ta 50,236,57856
v_min_f32_e32 v132,0xc1a00000,v132
maaa 112,68,16
ta 60,237,57344
v_add_f32_e32 v135,0xf36980b9,v252
maaa 116,68,12
ta 62,237,57856
v_min_f32_e32 v133,0xc1a00000,v133
maaa 116,64,88
ta 56,237,40960
v_min_f32_e32 v134,0xc1a00000,v134
maaa 132,76,88
dl 226,36,52,2048
maaa 132,72,12
ta 58,237,41472
v_min_f32_e32 v135,0xc1a00000,v135
maaa 136,72,8
maaa 136,76,84
maaa 120,64,84
maaa 120,68,8
dl 227,36,52,3072
maaa 124,68,4
s_add_u32 s52,s52,s60
maaa 124,64,80
maaa 140,76,80
ta 68,238,45056
ta 70,238,45568
maaa 140,72,4
ta 64,238,61440
ta 66,238,61952
mvaa 40,0,4
ta 76,239,61440
mvaa 40,20,80
buffer_store_dwordx4 v[136:139],v244,s[24:27],s67 offen offset:2048 sc0 sc1
ta 78,239,61952
mvaa 44,44,80
ta 72,239,45056
ta 74,239,45568
mvaa 44,40,4
v_add_f32_e32 v136,0xf3c6caff,v252
v_add_f32_e32 v137,0xf3cd1a95,v252
mvaa 52,40,8
v_add_f32_e32 v138,0xf3d36a2b,v252
v_add_f32_e32 v139,0xf3d9b9c2,v252
mvaa 52,44,84
v_min_f32_e32 v136,0xc1a00000,v136
v_min_f32_e32 v137,0xc1a00000,v137
mvaa 48,20,84
v_min_f32_e32 v138,0xc1a00000,v138
v_min_f32_e32 v139,0xc1a00000,v139
mvaa 48,0,8
mvaa 56,0,12
mvaa 56,20,88
mvaa 60,44,88
mvaa 60,40,12
mvaa 68,40,16
mvaa 68,44,92
mvaa 64,20,92
mvaa 64,0,16
s_waitcnt lgkmcnt(0)
mvaa 32,52,16
s_waitcnt vmcnt(11)
s_barrier
mvaa 32,48,92
s_cmp_eq_u32 s12,0
s_cselect_b64 exec,1,0
buffer_atomic_add v247,off,s[8:11],s85 sc1
s_mov_b64 exec,-1
mvaa 36,60,92
s_add_u32 m0,s62,32768
ta 40,234,0
v_mov_b32_e32 v247,32
mvaa 36,56,16
ta 42,234,512
mvaa 28,56,12
ta 44,234,16384
mvaa 28,60,88
ta 46,234,16896
mvaa 24,48,88
dl 228,40,53,0
mvaa 24,52,12
ta 20,235,16384
mvaa 16,52,8
ta 22,235,16896
mvaa 16,48,84
ta 0,235,0
mvaa 20,60,84
ta 2,235,512
mvaa 20,56,8
dl 229,40,53,1024
mvaa 12,56,4
s_add_u32 s53,s53,s57
mvaa 12,60,80
mvaa 8,48,80
ta 56,236,0
mvaa 8,52,4
ta 58,236,512
mvaa 76,4,68
dl 230,44,54,2048
mvaa 76,80,64
ta 60,236,16384
mvaa 96,80,76
ta 62,236,16896
mvaa 96,4,72
ta 48,237,16384
mvaa 100,8,72
ta 50,237,16896
mvaa 100,84,76
dl 231,44,54,3072
mvaa 80,84,64
s_add_u32 m0,s62,49152
s_add_u32 s54,s54,s57
ta 52,237,0
mvaa 80,8,68
ta 54,237,512
mvaa 84,12,68
ra 80,233,32768
mvaa 84,88,64
ra 4,232,32768
mvaa 104,88,76
dl 228,40,53,0
mvaa 104,12,72
ra 84,233,34816
mvaa 108,16,72
ra 8,232,34816
mvaa 108,92,76
ra 12,232,36864
mvaa 88,92,64
ta 72,234,4096
mvaa 88,16,68
ta 74,234,4608
s_waitcnt lgkmcnt(9)
maaa 236,40,24
dl 229,40,53,1024
maaa 236,44,96
s_add_u32 s53,s53,s58
ta 76,234,20480
maaa 220,20,96
ta 78,234,20992
maaa 220,0,24
ta 64,235,20480
maaa 216,0,28
ta 66,235,20992
maaa 216,20,100
dl 230,44,54,2048
maaa 232,44,100
ta 68,235,4096
maaa 232,40,28
ta 70,235,4608
maaa 228,40,32
ra 88,233,36864
maaa 228,44,104
ra 92,233,38912
v_add_u32_e32 v233,0x10000,v233
maaa 212,20,104
dl 231,44,54,3072
maaa 212,0,32
s_add_u32 m0,s98,0
ra 16,232,38912
s_add_u32 s54,s54,s58
maaa 208,0,36
v_add_u32_e32 v232,0x10000,v232
maaa 208,20,108
maaa 224,44,108
ta 0,236,4096
maaa 224,40,36
dl 224,36,52,0
maaa 192,56,36
ta 2,236,4608
maaa 192,60,108
ta 20,236,20480
maaa 176,48,108
ta 22,236,20992
s_waitcnt lgkmcnt(6)
maaa 176,52,36
ta 44,237,20480
maaa 180,52,32
ta 46,237,20992
maaa 180,48,104
dl 225,36,52,1024
maaa 196,60,104
ta 40,237,4096
maaa 196,56,32
ta 42,237,4608
maaa 200,56,28
maaa 200,60,100
maaa 184,48,100
buffer_store_dwordx4 v[140:143],v244,s[24:27],s67 offen offset:3072 sc0 sc1
maaa 184,52,28
s_mov_b32 s67,12288
v_add_f32_e32 v140,0xf415e232,v252
v_add_f32_e32 v141,0xf41909fd,v252
maaa 188,52,24
v_add_f32_e32 v142,0xf41c31c8,v252
v_min_f32_e32 v140,0xc1a00000,v140
maaa 188,48,96
v_add_f32_e32 v143,0xf41f5993,v252
v_bfe_i32 v252,v244,4,1
maaa 204,60,96
ta 52,234,8192
v_min_f32_e32 v141,0xc1a00000,v141
maaa 204,56,24
dl 226,36,52,2048
maaa 172,72,24
ta 54,234,8704
v_min_f32_e32 v142,0xc1a00000,v142
maaa 172,76,96
ta 48,234,24576
v_min_f32_e32 v143,0xc1a00000,v143
maaa 156,64,96
ta 50,234,25088
v_xor_b32_e32 v252,65535,v252
maaa 156,68,24
ta 60,235,24576
maaa 152,68,28
dl 227,36,52,3072
maaa 152,64,100
s_add_u32 s52,s52,s56
s_add_u32 m0,s98,4096
ta 62,235,25088
maaa 168,76,100
ta 56,235,8192
maaa 168,72,28
ta 58,235,8704
maaa 164,72,32
maaa 164,76,104
maaa 148,64,104
dl 224,36,52,0
maaa 148,68,32
maaa 144,68,36
maaa 144,64,108
maaa 160,76,108
ta 68,236,8192
maaa 160,72,36
buffer_store_dwordx4 v[144:147],v244,s[24:27],s67 offen offset:0 sc0 sc1
s_waitcnt lgkmcnt(3)
maaa 128,0,36
ta 70,236,8704
maaa 128,20,108
ta 64,236,24576
maaa 112,44,108
ta 66,236,25088
maaa 112,40,36
ta 76,237,24576
maaa 116,40,32
dl 225,36,52,1024
maaa 116,44,104
ta 78,237,25088
maaa 132,20,104
ta 72,237,8192
maaa 132,0,32
ta 74,237,8704
maaa 136,0,28
maaa 136,20,100
dl 226,36,52,2048
maaa 120,44,100
maaa 120,40,28
maaa 124,40,24
maaa 124,44,96
maaa 140,20,96
buffer_store_dwordx4 v[148:151],v244,s[24:27],s67 offen offset:1024 sc0 sc1
ta 40,238,12288
ta 42,238,12800
maaa 140,0,24
ta 44,238,28672
ta 46,238,29184
mvaa 40,52,24
ta 20,239,28672
mvaa 40,48,96
ta 22,239,29184
mvaa 44,60,96
ta 0,239,12288
ta 2,239,12800
s_waitcnt lgkmcnt(8)
mvaa 44,56,24
mvaa 52,56,28
dl 227,36,52,3072
mvaa 52,60,100
s_add_u32 s52,s52,s60
mvaa 48,48,100
mvaa 48,52,28
mvaa 56,52,32
mvaa 56,48,104
mvaa 60,60,104
mvaa 60,56,32
mvaa 68,56,36
mvaa 68,60,108
mvaa 64,48,108
mvaa 64,52,36
mvaa 32,68,36
s_waitcnt vmcnt(11) lgkmcnt(0)
s_barrier
s_add_u32 m0,s62,0
mvaa 32,64,108
dl 228,40,53,0
mvaa 36,76,108
ta 56,234,32768
mvaa 36,72,36
ta 58,234,33280
mvaa 28,72,32
ta 60,234,49152
mvaa 28,76,104
ta 62,234,49664
mvaa 24,64,104
dl 229,40,53,1024
mvaa 24,68,32
s_add_u32 s53,s53,s57
ta 48,235,49152
mvaa 16,68,28
ta 50,235,49664
mvaa 16,64,100
ta 52,235,32768
mvaa 20,76,100
ta 54,235,33280
mvaa 20,72,28
dl 230,44,54,2048
mvaa 12,72,24
mvaa 12,76,96
mvaa 8,64,96
ta 72,236,32768
mvaa 8,68,24
ta 74,236,33280
mvaa 76,24,40
ta 76,236,49152
mvaa 76,96,44
dl 231,44,54,3072
mvaa 96,96,20
s_add_u32 m0,s62,16384
s_add_u32 s54,s54,s57
ta 78,236,49664
mvaa 96,24,0
ta 64,237,49152
mvaa 100,28,0
ta 66,237,49664
mvaa 100,100,20
ta 68,237,32768
mvaa 80,100,44
dl 228,40,53,0
mvaa 80,28,40
ta 70,237,33280
mvaa 84,32,40
ra 96,233,0
mvaa 84,104,44
ra 24,232,0
mvaa 104,104,20
ra 100,233,2048
mvaa 104,32,0
dl 229,40,53,1024
mvaa 108,36,0
s_add_u32 s53,s53,s58
ra 28,232,2048
mvaa 108,108,20
ra 104,233,4096
mvaa 88,108,44
ta 0,234,36864
mvaa 88,36,40
ta 2,234,37376
s_waitcnt lgkmcnt(8)
maaa 236,56,4
dl 230,44,54,2048
maaa 236,60,80
ta 20,234,53248
maaa 220,48,80
ta 22,234,53760
maaa 220,52,4
ta 44,235,53248
maaa 216,52,8
ta 46,235,53760
maaa 216,48,84
dl 231,44,54,3072
maaa 232,60,84
s_add_u32 m0,s98,32768
ta 40,235,36864
s_add_u32 s54,s54,s58
maaa 232,56,8
ta 42,235,37376
maaa 228,56,12
ra 32,232,4096
maaa 228,60,88
ra 108,233,6144
v_add_u32_e32 v233,0xffff0000,v233
maaa 212,48,88
ra 36,232,6144
v_add_u32_e32 v232,0xffff0000,v232
maaa 212,52,12
dl 224,36,52,0
maaa 208,52,16
maaa 208,48,92
maaa 224,60,92
ta 52,236,36864
maaa 224,56,16
ta 54,236,37376
maaa 192,72,16
dl 225,36,52,1024
maaa 192,76,92
ta 48,236,53248
maaa 176,64,92
ta 50,236,53760
s_waitcnt lgkmcnt(6)
maaa 176,68,16
ta 60,237,53248
maaa 180,68,12
ta 62,237,53760
maaa 180,64,88
dl 226,36,52,2048
maaa 196,76,88
ta 56,237,36864
maaa 196,72,12
ta 58,237,37376
maaa 200,72,8
maaa 200,76,84
maaa 184,64,84
buffer_store_dwordx4 v[152:155],v244,s[24:27],s67 offen offset:2048 sc0 sc1
maaa 184,68,8
maaa 188,68,4
maaa 188,64,80
maaa 204,76,80
ta 68,234,40960
maaa 204,72,4
ta 70,234,41472
maaa 172,0,4
dl 227,36,52,3072
maaa 172,20,80
s_add_u32 s52,s52,s56
s_add_u32 m0,s98,36864
ta 64,234,57344
maaa 156,44,80
ta 66,234,57856
maaa 156,40,4
ta 76,235,57344
maaa 152,40,8
ta 78,235,57856
maaa 152,44,84
dl 224,36,52,0
maaa 168,20,84
ta 72,235,40960
maaa 168,0,8
ta 74,235,41472
maaa 164,0,12
maaa 164,20,88
maaa 148,44,88
buffer_store_dwordx4 v[156:159],v244,s[24:27],s67 offen offset:3072 sc0 sc1
maaa 148,40,12
s_mov_b32 s67,16384
maaa 144,40,16
maaa 144,44,92
maaa 160,20,92
ta 40,236,40960
maaa 160,0,16
dl 225,36,52,1024
s_waitcnt lgkmcnt(3)
maaa 128,52,16
ta 42,236,41472
maaa 128,48,92
ta 44,236,57344
maaa 112,60,92
ta 46,236,57856
maaa 112,56,16
ta 20,237,57344
maaa 116,56,12
ta 22,237,57856
maaa 116,60,88
dl 226,36,52,2048
maaa 132,48,88
ta 0,237,40960
maaa 132,52,12
ta 2,237,41472
maaa 136,52,8
maaa 136,48,84
maaa 120,60,84
dl 227,36,52,3072
maaa 120,56,8
s_add_u32 s52,s52,s60
maaa 124,56,4
maaa 124,60,80
maaa 140,48,80
ta 56,238,45056
ta 58,238,45568
maaa 140,52,4
buffer_store_dwordx4 v[160:163],v244,s[24:27],s67 offen offset:0 sc0 sc1
ta 60,238,61440
ta 62,238,61952
mvaa 40,68,4
ta 48,239,61440
mvaa 40,64,80
ta 50,239,61952
mvaa 44,76,80
ta 52,239,45056
ta 54,239,45568
s_waitcnt lgkmcnt(8)
mvaa 44,72,4
mvaa 52,72,8
mvaa 52,76,84
mvaa 48,64,84
mvaa 48,68,8
mvaa 56,68,12
mvaa 56,64,88
mvaa 60,76,88
mvaa 60,72,12
mvaa 68,72,16
mvaa 68,76,92
mvaa 64,64,92
mvaa 64,68,16
mvaa 32,40,16
s_waitcnt vmcnt(11) lgkmcnt(0)
s_barrier
s_add_u32 m0,s62,32768
mvaa 32,44,92
dl 228,40,53,0
mvaa 36,20,92
ta 72,234,0
mvaa 36,0,16
ta 74,234,512
mvaa 28,0,12
ta 76,234,16384
mvaa 28,20,88
ta 78,234,16896
mvaa 24,44,88
ta 64,235,16384
mvaa 24,40,12
dl 229,40,53,1024
mvaa 16,40,8
s_add_u32 s53,s53,s57
ta 66,235,16896
mvaa 16,44,84
ta 68,235,0
mvaa 20,20,84
ta 70,235,512
mvaa 20,0,8
mvaa 12,0,4
dl 230,44,54,2048
mvaa 12,20,80
mvaa 8,44,80
ta 0,236,0
mvaa 8,40,4
ta 2,236,512
mvaa 76,4,56
ta 20,236,16384
mvaa 76,80,60
dl 231,44,54,3072
)"
R"(mvaa 96,80,48
s_add_u32 m0,s62,49152
s_add_u32 s54,s54,s57
ta 22,236,16896
mvaa 96,4,52
ta 44,237,16384
mvaa 100,8,52
ta 46,237,16896
mvaa 100,84,48
ta 40,237,0
mvaa 80,84,60
dl 228,40,53,0
mvaa 80,8,56
ta 42,237,512
mvaa 84,12,56
ra 80,233,0
mvaa 84,88,60
ra 4,232,0
mvaa 104,88,48
ra 84,233,2048
mvaa 104,12,52
ra 8,232,2048
mvaa 108,16,52
dl 229,40,53,1024
mvaa 108,92,48
s_add_u32 s53,s53,s58
ra 88,233,4096
mvaa 88,92,60
ta 52,234,4096
mvaa 88,16,56
ta 54,234,4608
s_waitcnt lgkmcnt(8)
maaa 236,72,24
ta 48,234,20480
maaa 236,76,96
dl 230,44,54,2048
maaa 220,64,96
ta 50,234,20992
maaa 220,68,24
ta 60,235,20480
maaa 216,68,28
ta 62,235,20992
maaa 216,64,100
ta 56,235,4096
v_accvgpr_read_b32 v0,a236
maaa 232,76,100
dl 231,44,54,3072
maaa 232,72,28
s_add_u32 m0,s98,65536
ta 58,235,4608
s_add_u32 s54,s54,s58
maaa 228,72,32
ra 12,232,4096
v_accvgpr_read_b32 v1,a237
maaa 228,76,104
ra 92,233,6144
v_accvgpr_read_b32 v2,a238
maaa 212,64,104
ra 16,232,6144
v_accvgpr_read_b32 v3,a239
maaa 212,68,32
dl 224,36,52,0
maaa 208,68,36
v_accvgpr_read_b32 v4,a220
v_accvgpr_read_b32 v5,a221
maaa 208,64,108
v_accvgpr_read_b32 v6,a222
v_accvgpr_read_b32 v7,a223
maaa 224,76,108
ta 68,236,4096
v_accvgpr_read_b32 v92,a216
maaa 224,72,36
ta 70,236,4608
v_accvgpr_read_b32 v93,a217
maaa 192,0,36
dl 225,36,52,1024
maaa 192,20,108
ta 64,236,20480
v_accvgpr_read_b32 v94,a218
maaa 176,44,108
ta 66,236,20992
v_accvgpr_read_b32 v95,a219
s_waitcnt lgkmcnt(6)
maaa 176,40,36
ta 76,237,20480
v_accvgpr_read_b32 v72,a232
maaa 180,40,32
ta 78,237,20992
v_accvgpr_read_b32 v73,a233
maaa 180,44,104
ta 72,237,4096
v_accvgpr_read_b32 v74,a234
maaa 196,20,104
s_mov_b64 exec,3
global_load_dword a250,v253,s[96:97]
s_mov_b64 exec,-1
maaa 196,0,32
ta 74,237,4608
v_accvgpr_read_b32 v75,a235
maaa 200,0,28
maaa 200,20,100
maaa 184,44,100
maaa 184,40,28
buffer_store_dwordx4 v[164:167],v244,s[24:27],s67 offen offset:1024 sc0 sc1
maaa 188,40,24
maaa 188,44,96
maaa 204,20,96
ta 40,234,8192
maaa 204,0,24
ta 42,234,8704
maaa 172,52,24
dl 226,36,52,2048
maaa 172,48,96
ta 44,234,24576
maaa 156,60,96
ta 46,234,25088
maaa 156,56,24
ta 20,235,24576
maaa 152,56,28
ta 22,235,25088
maaa 152,60,100
dl 227,36,52,3072
maaa 168,48,100
s_add_u32 s52,s52,s56
s_add_u32 m0,s98,69632
ta 0,235,8192
maaa 168,52,28
ta 2,235,8704
maaa 164,52,32
maaa 164,48,104
maaa 148,60,104
maaa 148,56,32
buffer_store_dwordx4 v[168:171],v244,s[24:27],s67 offen offset:2048 sc0 sc1
maaa 144,56,36
maaa 144,60,108
maaa 160,48,108
ta 56,236,8192
maaa 160,52,36
ta 58,236,8704
s_waitcnt lgkmcnt(4)
maaa 128,68,36
dl 224,36,52,0
maaa 128,64,108
ta 60,236,24576
maaa 112,76,108
ta 62,236,25088
maaa 112,72,36
ta 48,237,24576
maaa 116,72,32
ta 50,237,25088
maaa 116,76,104
dl 225,36,52,1024
maaa 132,64,104
ta 52,237,8192
maaa 132,68,32
ta 54,237,8704
maaa 136,68,28
maaa 136,64,100
maaa 120,76,100
dl 226,36,52,2048
maaa 120,72,28
maaa 124,72,24
maaa 124,76,96
maaa 140,64,96
ta 72,238,12288
ta 74,238,12800
maaa 140,68,24
ta 76,238,28672
ta 78,238,29184
mvaa 40,40,24
buffer_store_dwordx4 v[172:175],v244,s[24:27],s67 offen offset:3072 sc0 sc1
ta 64,239,28672
mvaa 40,44,96
s_mov_b32 s67,20480
ta 66,239,29184
mvaa 44,20,96
ta 68,239,12288
ta 70,239,12800
s_waitcnt lgkmcnt(8)
mvaa 44,0,24
mvaa 52,0,28
mvaa 52,20,100
dl 227,36,52,3072
mvaa 48,44,100
s_add_u32 s52,s52,s60
mvaa 48,40,28
mvaa 56,40,32
mvaa 56,44,104
mvaa 60,20,104
mvaa 60,0,32
mvaa 68,0,36
mvaa 68,20,108
mvaa 64,44,108
mvaa 64,40,36
mvaa 32,56,36
s_waitcnt vmcnt(12) lgkmcnt(0)
s_barrier
s_add_u32 m0,s62,0
mvaa 32,60,108
dl 228,40,53,0
mvaa 36,48,108
ta 0,234,32768
mvaa 36,52,36
ta 2,234,33280
mvaa 28,52,32
ta 20,234,49152
mvaa 28,48,104
ta 22,234,49664
mvaa 24,60,104
ta 44,235,49152
mvaa 24,56,32
dl 229,40,53,1024
mvaa 16,56,28
s_add_u32 s53,s53,s57
ta 46,235,49664
mvaa 16,60,100
ta 40,235,32768
mvaa 20,48,100
ta 42,235,33280
mvaa 20,52,28
ta 236,236,36864
mvaa 12,52,24
dl 230,44,54,2048
mvaa 12,48,96
ta 238,236,37376
mvaa 8,60,96
ta 52,236,32768
mvaa 8,56,24
ta 54,236,33280
mvaa 76,24,72
ta 48,236,49152
mvaa 76,96,76
ta 50,236,49664
mvaa 96,96,64
dl 231,44,54,3072
mvaa 96,24,68
s_add_u32 m0,s62,16384
s_add_u32 s54,s54,s57
ta 60,237,49152
mvaa 100,28,68
ta 62,237,49664
mvaa 100,100,64
ta 56,237,32768
mvaa 80,100,76
ta 58,237,33280
mvaa 80,28,72
dl 228,40,53,0
mvaa 84,32,72
ta 220,236,53248
mvaa 84,104,76
ta 222,236,53760
mvaa 104,104,64
ta 232,237,53248
mvaa 104,32,68
ta 234,237,53760
mvaa 108,36,68
dl 229,40,53,1024
mvaa 108,108,64
s_add_u32 s53,s53,s58
ta 216,237,36864
mvaa 88,108,76
ta 68,234,36864
mvaa 88,36,72
ta 70,234,37376
s_waitcnt lgkmcnt(8)
mvaa 0,0,4
ta 64,234,53248
mvaa 0,20,80
dl 230,44,54,2048
mvaa 4,44,80
ta 66,234,53760
mvaa 4,40,4
ta 76,235,53248
mvaa 92,40,8
ta 78,235,53760
mvaa 92,44,84
ta 72,235,36864
mvaa 72,20,84
ta 74,235,37376
mvaa 72,0,8
dl 231,44,54,3072
maaa 228,0,12
s_add_u32 m0,s98,0
ta 218,237,37376
s_add_u32 s54,s54,s58
maaa 228,20,88
ra 96,233,32768
maaa 212,44,88
ra 24,232,32768
maaa 212,40,12
ra 100,233,34816
maaa 208,40,16
dl 224,36,52,0
maaa 208,44,92
ra 28,232,34816
v_accvgpr_read_b32 v144,a228
maaa 224,20,92
ra 104,233,36864
v_accvgpr_read_b32 v145,a229
maaa 224,0,16
ra 32,232,36864
v_accvgpr_read_b32 v146,a230
maaa 192,52,16
ra 36,232,38912
v_accvgpr_read_b32 v147,a231
maaa 192,48,92
dl 225,36,52,1024
maaa 176,60,92
ra 108,233,38912
v_accvgpr_read_b32 v148,a212
s_waitcnt lgkmcnt(8)
maaa 176,56,16
ta 40,234,40960
v_accvgpr_read_b32 v149,a213
maaa 180,56,12
ta 42,234,41472
v_accvgpr_read_b32 v150,a214
maaa 180,60,88
ta 44,234,57344
v_accvgpr_read_b32 v151,a215
maaa 196,48,88
buffer_store_dwordx4 v[176:179],v244,s[24:27],s67 offen offset:0 sc0 sc1
maaa 196,52,12
ta 46,234,57856
v_accvgpr_read_b32 v156,a208
maaa 200,52,8
ta 20,235,57344
v_accvgpr_read_b32 v157,a209
maaa 200,48,84
ta 22,235,57856
v_accvgpr_read_b32 v158,a210
maaa 184,60,84
ta 0,235,40960
v_accvgpr_read_b32 v159,a211
maaa 184,56,8
ta 2,235,41472
v_accvgpr_read_b32 v152,a224
maaa 188,56,4
dl 226,36,52,2048
maaa 188,60,80
v_accvgpr_read_b32 v153,a225
v_accvgpr_read_b32 v154,a226
maaa 204,48,80
ta 56,236,40960
v_accvgpr_read_b32 v155,a227
maaa 204,52,4
ta 58,236,41472
v_add_u32_e32 v233,0x10000,v233
maaa 172,68,4
ta 60,236,57344
v_add_u32_e32 v232,0x10000,v232
maaa 172,64,80
dl 227,36,52,3072
maaa 156,76,80
s_add_u32 s52,s52,s56
s_add_u32 m0,s98,4096
ta 62,236,57856
maaa 156,72,4
ta 48,237,57344
maaa 152,72,8
ta 50,237,57856
maaa 152,76,84
ta 52,237,40960
maaa 168,64,84
dl 224,36,52,0
maaa 168,68,8
ta 54,237,41472
maaa 164,68,12
maaa 164,64,88
maaa 148,76,88
maaa 148,72,12
buffer_store_dwordx4 v[180:183],v244,s[24:27],s67 offen offset:1024 sc0 sc1
maaa 144,72,16
maaa 144,76,92
maaa 160,64,92
ta 72,238,45056
maaa 160,68,16
ta 74,238,45568
maaa 128,236,16
dl 225,36,52,1024
maaa 128,220,92
ta 76,238,61440
maaa 112,232,92
ta 78,238,61952
maaa 112,216,16
ta 64,239,61440
maaa 116,216,12
ta 66,239,61952
maaa 116,232,88
ta 68,239,45056
maaa 132,220,88
dl 226,36,52,2048
maaa 132,236,12
ta 70,239,45568
maaa 136,236,8
maaa 136,220,84
maaa 120,232,84
maaa 120,216,8
dl 227,36,52,3072
maaa 124,216,4
s_add_u32 s52,s52,s60
maaa 124,232,80
maaa 140,220,80
maaa 140,236,4
s_waitcnt lgkmcnt(1)
mvaa 40,40,4
buffer_store_dwordx4 v[184:187],v244,s[24:27],s67 offen offset:2048 sc0 sc1
mvaa 40,44,80
mvaa 44,20,80
mvaa 44,0,4
mvaa 52,0,8
mvaa 52,20,84
mvaa 48,44,84
mvaa 48,40,8
mvaa 56,40,12
mvaa 56,44,88
mvaa 60,20,88
mvaa 60,0,12
mvaa 68,0,16
mvaa 68,20,92
mvaa 64,44,92
mvaa 64,40,16
mvaa 32,56,16
s_waitcnt vmcnt(11) lgkmcnt(0)
s_barrier
s_add_u32 m0,s62,32768
mvaa 32,60,92
dl 228,40,53,0
mvaa 36,48,92
ta 216,234,0
mvaa 36,52,16
ta 218,234,512
mvaa 28,52,12
ta 232,234,16384
mvaa 28,48,88
ta 234,234,16896
mvaa 24,60,88
dl 229,40,53,1024
mvaa 24,56,12
s_add_u32 s53,s53,s57
ta 220,235,16384
mvaa 16,56,8
ta 222,235,16896
mvaa 16,60,84
ta 236,235,0
mvaa 20,48,84
ta 238,235,512
mvaa 20,52,8
dl 230,44,54,2048
mvaa 12,52,4
ta 0,236,0
mvaa 12,48,80
ta 2,236,512
mvaa 8,60,80
ta 20,236,16384
mvaa 8,56,4
ta 22,236,16896
mvaa 76,4,72
dl 231,44,54,3072
mvaa 76,80,76
s_add_u32 m0,s62,49152
s_add_u32 s54,s54,s57
ta 44,237,16384
mvaa 96,80,64
ta 46,237,16896
mvaa 96,4,68
ta 40,237,0
mvaa 100,8,68
ta 42,237,512
mvaa 100,84,64
dl 228,40,53,0
mvaa 80,84,76
ta 52,234,4096
mvaa 80,8,72
ta 54,234,4608
mvaa 84,12,72
ta 48,234,20480
mvaa 84,88,76
ta 50,234,20992
mvaa 104,88,64
dl 229,40,53,1024
mvaa 104,12,68
s_add_u32 s53,s53,s58
ta 60,235,20480
mvaa 108,16,68
ta 62,235,20992
mvaa 108,92,64
ta 56,235,4096
mvaa 88,92,76
ta 58,235,4608
mvaa 88,16,72
dl 230,44,54,2048
s_waitcnt lgkmcnt(8)
mvaa 0,216,24
ta 228,234,8192
mvaa 0,232,96
ta 68,236,4096
mvaa 4,220,96
ta 70,236,4608
mvaa 4,236,24
ta 64,236,20480
mvaa 92,236,28
ta 66,236,20992
mvaa 92,220,100
dl 231,44,54,3072
mvaa 72,232,100
s_add_u32 m0,s98,32768
ta 76,237,20480
s_add_u32 s54,s54,s58
mvaa 72,216,28
ta 78,237,20992
mvaa 144,216,32
ta 72,237,4096
mvaa 144,232,104
ta 74,237,4608
mvaa 148,220,104
dl 224,36,52,0
mvaa 148,236,32
ta 230,234,8704
mvaa 156,236,36
ta 212,234,24576
mvaa 156,220,108
ta 214,234,25088
mvaa 152,232,108
ta 224,235,24576
mvaa 152,216,36
dl 225,36,52,1024
maaa 192,0,36
ta 226,235,25088
maaa 192,20,108
ta 208,235,8192
maaa 176,44,108
ra 80,233,0
maaa 176,40,36
ta 210,235,8704
maaa 180,40,32
dl 226,36,52,2048
maaa 180,44,104
ra 4,232,0
v_accvgpr_read_b32 v160,a192
maaa 196,20,104
ra 84,233,2048
v_accvgpr_read_b32 v161,a193
maaa 196,0,32
ra 8,232,2048
v_accvgpr_read_b32 v162,a194
maaa 200,0,28
ra 12,232,4096
v_accvgpr_read_b32 v163,a195
maaa 200,20,100
buffer_store_dwordx4 v[188:191],v244,s[24:27],s67 offen offset:3072 sc0 sc1
maaa 184,44,100
s_mov_b32 s67,24576
ra 88,233,4096
v_accvgpr_read_b32 v164,a176
maaa 184,40,28
ra 92,233,6144
v_accvgpr_read_b32 v165,a177
maaa 188,40,24
ra 16,232,6144
v_accvgpr_read_b32 v166,a178
maaa 188,44,96
ta 236,236,8192
v_accvgpr_read_b32 v167,a179
maaa 204,20,96
ta 238,236,8704
v_accvgpr_read_b32 v172,a180
maaa 204,0,24
dl 227,36,52,3072
s_waitcnt lgkmcnt(8)
maaa 172,52,24
s_add_u32 s52,s52,s56
s_add_u32 m0,s98,36864
ta 220,236,24576
maaa 172,48,96
ta 222,236,25088
v_accvgpr_read_b32 v173,a181
maaa 156,60,96
ta 232,237,24576
v_accvgpr_read_b32 v174,a182
maaa 156,56,24
ta 234,237,25088
v_accvgpr_read_b32 v175,a183
maaa 152,56,28
dl 224,36,52,0
maaa 152,60,100
ta 216,237,8192
v_accvgpr_read_b32 v168,a196
maaa 168,48,100
ta 218,237,8704
v_accvgpr_read_b32 v169,a197
maaa 168,52,28
ta 40,238,12288
v_accvgpr_read_b32 v170,a198
maaa 164,52,32
ta 42,238,12800
v_accvgpr_read_b32 v171,a199
maaa 164,48,104
dl 225,36,52,1024
maaa 148,60,104
ta 44,238,28672
v_add_u32_e32 v233,0xffff0000,v233
maaa 148,56,32
ta 46,238,29184
v_add_u32_e32 v232,0xffff0000,v232
maaa 144,56,36
ta 20,239,28672
maaa 144,60,108
ta 22,239,29184
maaa 160,48,108
buffer_store_dwordx4 v[192:195],v244,s[24:27],s67 offen offset:0 sc0 sc1
maaa 160,52,36
ta 0,239,12288
maaa 128,68,36
ta 2,239,12800
maaa 128,64,108
maaa 112,76,108
maaa 112,72,36
maaa 116,72,32
dl 226,36,52,2048
maaa 116,76,104
maaa 132,64,104
maaa 132,68,32
maaa 136,68,28
maaa 136,64,100
dl 227,36,52,3072
maaa 120,76,100
s_add_u32 s52,s52,s60
maaa 120,72,28
maaa 124,72,24
maaa 124,76,96
maaa 140,64,96
buffer_store_dwordx4 v[196:199],v244,s[24:27],s67 offen offset:1024 sc0 sc1
maaa 140,68,24
mvaa 40,228,24
mvaa 40,212,96
mvaa 44,224,96
mvaa 44,208,24
mvaa 52,208,28
mvaa 52,224,100
mvaa 48,212,100
mvaa 48,228,28
mvaa 56,228,32
mvaa 56,212,104
mvaa 60,224,104
mvaa 60,208,32
mvaa 68,208,36
mvaa 68,224,108
mvaa 64,212,108
mvaa 64,228,36
s_waitcnt lgkmcnt(0)
mvaa 32,236,36
s_waitcnt vmcnt(11)
s_barrier
s_add_u32 m0,s62,0
mvaa 32,220,108
dl 228,40,53,0
mvaa 36,232,108
ta 56,234,32768
mvaa 36,216,36
ta 58,234,33280
mvaa 28,216,32
ta 60,234,49152
mvaa 28,232,104
ta 62,234,49664
mvaa 24,220,104
dl 229,40,53,1024
mvaa 24,236,32
s_add_u32 s53,s53,s57
ta 48,235,49152
mvaa 16,236,28
ta 50,235,49664
mvaa 16,220,100
ta 52,235,32768
mvaa 20,232,100
ta 54,235,33280
mvaa 20,216,28
dl 230,44,54,2048
mvaa 12,216,24
ta 72,236,32768
mvaa 12,232,96
ta 74,236,33280
mvaa 8,220,96
ta 76,236,49152
mvaa 8,236,24
ta 78,236,49664
mvaa 76,24,40
dl 231,44,54,3072
mvaa 76,96,44
s_add_u32 m0,s62,16384
s_add_u32 s54,s54,s57
ta 64,237,49152
mvaa 96,96,20
ta 66,237,49664
mvaa 96,24,0
ta 68,237,32768
mvaa 100,28,0
ta 70,237,33280
mvaa 100,100,20
dl 228,40,53,0
mvaa 80,100,44
ta 208,234,36864
mvaa 80,28,40
ta 210,234,37376
mvaa 84,32,40
ta 224,234,53248
mvaa 84,104,44
ta 226,234,53760
mvaa 104,104,20
ta 212,235,53248
mvaa 104,32,0
dl 229,40,53,1024
mvaa 108,36,0
s_add_u32 s53,s53,s58
ta 214,235,53760
mvaa 108,108,20
ta 228,235,36864
mvaa 88,108,44
ta 230,235,37376
mvaa 88,36,40
ta 192,236,40960
s_waitcnt lgkmcnt(9)
mvaa 0,56,4
dl 230,44,54,2048
mvaa 0,60,80
ta 216,236,36864
mvaa 4,48,80
ta 218,236,37376
mvaa 4,52,4
ta 232,236,53248
mvaa 92,52,8
ta 234,236,53760
mvaa 92,48,84
dl 231,44,54,3072
mvaa 72,60,84
s_add_u32 m0,s98,65536
ta 220,237,53248
s_add_u32 s54,s54,s58
mvaa 72,56,8
ta 222,237,53760
mvaa 144,56,12
ta 236,237,36864
mvaa 144,60,88
ta 238,237,37376
mvaa 148,48,88
dl 224,36,52,0
mvaa 148,52,12
ta 194,236,41472
mvaa 156,52,16
ta 176,236,57344
mvaa 156,48,92
ta 178,236,57856
mvaa 152,60,92
ta 196,237,57344
mvaa 152,56,16
ta 198,237,57856
mvaa 160,72,16
dl 225,36,52,1024
mvaa 160,76,92
ta 180,237,40960
mvaa 164,64,92
ta 182,237,41472
mvaa 164,68,16
ra 96,233,0
mvaa 172,68,12
ta 0,234,40960
mvaa 172,64,88
dl 226,36,52,2048
mvaa 168,76,88
ta 2,234,41472
mvaa 168,72,12
ta 20,234,57344
maaa 200,72,8
ta 22,234,57856
maaa 200,76,84
ta 44,235,57344
maaa 184,64,84
buffer_store_dwordx4 v[200:203],v244,s[24:27],s67 offen offset:2048 sc0 sc1
maaa 184,68,8
ta 46,235,57856
maaa 188,68,4
ta 40,235,40960
maaa 188,64,80
ta 42,235,41472
v_accvgpr_read_b32 v176,a200
maaa 204,76,80
ra 24,232,0
v_accvgpr_read_b32 v177,a201
maaa 204,72,4
dl 227,36,52,3072
s_waitcnt lgkmcnt(8)
maaa 172,208,4
s_add_u32 s52,s52,s56
s_add_u32 m0,s98,69632
ra 100,233,2048
maaa 172,224,80
ra 28,232,2048
v_accvgpr_read_b32 v178,a202
maaa 156,212,80
ra 32,232,4096
v_accvgpr_read_b32 v179,a203
maaa 156,228,4
ra 104,233,4096
v_accvgpr_read_b32 v180,a184
maaa 152,228,8
ra 108,233,6144
v_accvgpr_read_b32 v181,a185
maaa 152,212,84
dl 224,36,52,0
maaa 168,224,84
ra 36,232,6144
v_accvgpr_read_b32 v182,a186
maaa 168,208,8
ta 52,238,45056
v_accvgpr_read_b32 v183,a187
maaa 164,208,12
ta 54,238,45568
v_accvgpr_read_b32 v188,a188
maaa 164,224,88
ta 48,238,61440
v_accvgpr_read_b32 v189,a189
maaa 148,212,88
buffer_store_dwordx4 v[204:207],v244,s[24:27],s67 offen offset:3072 sc0 sc1
maaa 148,228,12
s_mov_b32 s67,28672
ta 50,238,61952
v_accvgpr_read_b32 v190,a190
maaa 144,228,16
ta 60,239,61440
v_accvgpr_read_b32 v184,a204
maaa 144,212,92
ta 62,239,61952
v_accvgpr_read_b32 v191,a191
maaa 160,224,92
ta 56,239,45056
v_accvgpr_read_b32 v185,a205
maaa 160,208,16
dl 225,36,52,1024
maaa 128,216,16
ta 58,239,45568
v_accvgpr_read_b32 v186,a206
maaa 128,232,92
v_accvgpr_read_b32 v187,a207
maaa 112,220,92
maaa 112,236,16
maaa 116,236,12
dl 226,36,52,2048
maaa 116,220,88
maaa 132,232,88
maaa 132,216,12
maaa 136,216,8
maaa 136,232,84
dl 227,36,52,3072
maaa 120,220,84
s_add_u32 s52,s52,s60
maaa 120,236,8
maaa 124,236,4
maaa 124,220,80
maaa 140,232,80
maaa 140,216,4
buffer_store_dwordx4 v[208:211],v244,s[24:27],s67 offen offset:0 sc0 sc1
s_waitcnt lgkmcnt(0)
mvaa 40,0,4
mvaa 40,20,80
mvaa 44,44,80
mvaa 44,40,4
mvaa 52,40,8
mvaa 52,44,84
mvaa 48,20,84
mvaa 48,0,8
mvaa 56,0,12
mvaa 56,20,88
mvaa 60,44,88
mvaa 60,40,12
mvaa 68,40,16
mvaa 68,44,92
mvaa 64,20,92
mvaa 64,0,16
mvaa 32,192,16
s_waitcnt vmcnt(11)
s_barrier
s_add_u32 m0,s62,32768
mvaa 32,176,92
dl 228,40,53,0
mvaa 36,196,92
ta 68,234,0
mvaa 36,180,16
ta 70,234,512
mvaa 28,180,12
ta 64,234,16384
mvaa 28,196,88
ta 66,234,16896
mvaa 24,176,88
dl 229,40,53,1024
mvaa 24,192,12
s_add_u32 s53,s53,s57
ta 76,235,16384
mvaa 16,192,8
ta 78,235,16896
mvaa 16,176,84
ta 72,235,0
mvaa 20,196,84
ta 74,235,512
mvaa 20,180,8
ta 228,236,0
mvaa 12,180,4
dl 230,44,54,2048
mvaa 12,196,80
ta 230,236,512
mvaa 8,176,80
ta 212,236,16384
mvaa 8,192,4
ta 214,236,16896
mvaa 76,4,52
ta 224,237,16384
mvaa 76,80,48
dl 231,44,54,3072
mvaa 96,80,60
s_add_u32 m0,s62,49152
s_add_u32 s54,s54,s57
ta 226,237,16896
mvaa 96,4,56
ta 208,237,0
mvaa 100,8,56
ta 210,237,512
mvaa 100,84,60
ta 236,234,4096
mvaa 80,84,48
dl 228,40,53,0
mvaa 80,8,52
ta 238,234,4608
mvaa 84,12,52
ta 220,234,20480
mvaa 84,88,48
ta 222,234,20992
mvaa 104,88,60
ta 232,235,20480
mvaa 104,12,56
dl 229,40,53,1024
mvaa 108,16,56
s_add_u32 s53,s53,s58
ta 234,235,20992
mvaa 108,92,60
ta 216,235,4096
mvaa 88,92,48
ta 218,235,4608
mvaa 88,16,52
ta 40,236,4096
s_waitcnt lgkmcnt(8)
mvaa 0,68,24
dl 230,44,54,2048
mvaa 0,64,96
ta 42,236,4608
mvaa 4,76,96
ta 44,236,20480
mvaa 4,72,24
ta 46,236,20992
mvaa 92,72,28
ta 20,237,20480
mvaa 92,76,100
ta 22,237,20992
mvaa 72,64,100
dl 231,44,54,3072
mvaa 72,68,28
s_add_u32 m0,s98,0
ta 200,238,12288
s_add_u32 s54,s54,s58
mvaa 144,68,32
ta 202,238,12800
mvaa 144,64,104
ta 0,237,4096
mvaa 148,76,104
ta 2,237,4608
mvaa 148,72,32
dl 224,36,52,0
mvaa 156,72,36
ta 184,238,28672
mvaa 156,76,108
ta 186,238,29184
mvaa 152,64,108
ta 204,239,28672
mvaa 152,68,36
ta 206,239,29184
mvaa 160,228,36
dl 225,36,52,1024
mvaa 160,212,108
ta 188,239,12288
mvaa 164,224,108
ta 190,239,12800
mvaa 164,208,36
ta 180,234,8192
mvaa 172,208,32
ta 182,234,8704
mvaa 172,224,104
dl 226,36,52,2048
mvaa 168,212,104
ta 196,234,24576
mvaa 168,228,32
ta 198,234,25088
mvaa 176,228,28
)"
R"(ta 176,235,24576
mvaa 176,212,100
ta 178,235,25088
mvaa 180,224,100
ta 192,235,8192
mvaa 180,208,28
buffer_store_dwordx4 v[212:215],v244,s[24:27],s67 offen offset:1024 sc0 sc1
mvaa 188,208,24
ta 194,235,8704
mvaa 188,224,96
ra 4,232,32768
mvaa 184,212,96
ra 80,233,32768
mvaa 184,228,24
ra 84,233,34816
s_waitcnt lgkmcnt(9)
maaa 172,236,24
dl 227,36,52,3072
maaa 172,220,96
s_add_u32 s52,s52,s56
s_add_u32 m0,s98,4096
ra 8,232,34816
maaa 156,232,96
ra 88,233,36864
maaa 156,216,24
ta 56,236,8192
maaa 152,216,28
ra 12,232,36864
v_accvgpr_read_b32 v192,a172
maaa 152,232,100
dl 224,36,52,0
maaa 168,220,100
ta 58,236,8704
v_accvgpr_read_b32 v193,a173
maaa 168,236,28
ta 60,236,24576
v_accvgpr_read_b32 v194,a174
maaa 164,236,32
ta 62,236,25088
v_accvgpr_read_b32 v195,a175
maaa 164,220,104
ta 48,237,24576
v_accvgpr_read_b32 v196,a156
maaa 148,232,104
buffer_store_dwordx4 v[216:219],v244,s[24:27],s67 offen offset:2048 sc0 sc1
maaa 148,216,32
ta 50,237,25088
v_accvgpr_read_b32 v197,a157
maaa 144,216,36
ra 92,233,38912
v_accvgpr_read_b32 v198,a158
maaa 144,232,108
ra 16,232,38912
v_accvgpr_read_b32 v199,a159
maaa 160,220,108
ta 52,237,8192
v_accvgpr_read_b32 v204,a152
maaa 160,236,36
ta 54,237,8704
v_accvgpr_read_b32 v205,a153
maaa 128,40,36
dl 225,36,52,1024
maaa 128,44,108
v_accvgpr_read_b32 v206,a154
v_accvgpr_read_b32 v207,a155
maaa 112,20,108
v_accvgpr_read_b32 v200,a168
v_accvgpr_read_b32 v201,a169
maaa 112,0,36
v_accvgpr_read_b32 v202,a170
v_accvgpr_read_b32 v203,a171
maaa 116,0,32
v_add_u32_e32 v233,0x10000,v233
v_add_u32_e32 v232,0x10000,v232
maaa 116,20,104
dl 226,36,52,2048
maaa 132,44,104
maaa 132,40,32
maaa 136,40,28
maaa 136,44,100
maaa 120,20,100
dl 227,36,52,3072
maaa 120,0,28
s_add_u32 s52,s52,s60
maaa 124,0,24
maaa 124,20,96
maaa 140,44,96
maaa 140,40,24
buffer_store_dwordx4 v[220:223],v244,s[24:27],s67 offen offset:3072 sc0 sc1
mvaa 40,180,24
s_mov_b32 s67,0x5550055
s_mov_b32 s26,2097152
v_cndmask_b32_e64 v116,v241,0,s[66:67]
s_waitcnt lgkmcnt(0)
mvaa 40,196,96
s_mov_b32 s66,0xa0000
s_mov_b32 s67,0xaaa00aa
v_cndmask_b32_e64 v117,v241,0,s[66:67]
mvaa 44,176,96
s_mov_b32 s66,0x150001
s_mov_b32 s67,0x15550155
v_cndmask_b32_e64 v118,v241,0,s[66:67]
mvaa 44,192,24
s_mov_b32 s66,0x2a0002
s_mov_b32 s67,0x2aaa02aa
v_cndmask_b32_e64 v119,v241,0,s[66:67]
mvaa 52,192,28
s_lshr_b32 s66,s18,2
s_lshl_b32 s67,s18,13
s_lshl_b32 s66,s66,13
mvaa 52,176,100
s_mov_b32 s27,131072
v_cndmask_b32_e64 v120,v116,v241,s[64:65]
v_cndmask_b32_e64 v121,v117,v241,s[64:65]
mvaa 48,196,100
s_mov_b64 s[24:25],s[82:83]
v_cndmask_b32_e64 v122,v118,v241,s[64:65]
v_cndmask_b32_e64 v123,v119,v241,s[64:65]
mvaa 48,180,28
s_mov_b32 s83,0
s_or_b32 s82,s15,16384
mvaa 56,180,32
mvaa 56,196,104
mvaa 60,176,104
mvaa 60,192,32
mvaa 68,192,36
mvaa 68,176,108
mvaa 64,196,108
mvaa 64,180,36
mvaa 32,56,36
s_waitcnt vmcnt(11)
s_barrier
s_add_u32 m0,s62,0
mvaa 32,60,108
dl 228,40,53,0
mvaa 36,48,108
ta 72,234,32768
mvaa 36,52,36
ta 74,234,33280
mvaa 28,52,32
ta 76,234,49152
mvaa 28,48,104
ta 78,234,49664
mvaa 24,60,104
ta 64,235,49152
mvaa 24,56,32
dl 229,40,53,1024
mvaa 16,56,28
s_add_u32 s53,s53,s57
ta 66,235,49664
mvaa 16,60,100
ta 68,235,32768
mvaa 20,48,100
ta 70,235,33280
mvaa 20,52,28
ta 208,236,32768
mvaa 12,52,24
dl 230,44,54,2048
mvaa 12,48,96
ta 210,236,33280
mvaa 8,60,96
ta 224,236,49152
mvaa 8,56,24
ta 226,236,49664
mvaa 76,24,200
ta 212,237,49152
mvaa 76,96,184
dl 231,44,54,3072
mvaa 96,96,204
s_add_u32 m0,s62,16384
s_add_u32 s54,s54,s57
ta 214,237,49664
mvaa 96,24,188
ta 228,237,32768
mvaa 100,28,188
ta 230,237,33280
mvaa 100,100,204
ta 216,234,36864
mvaa 80,100,184
ta 218,234,37376
mvaa 80,28,200
dl 228,40,53,0
mvaa 84,32,200
ta 232,234,53248
mvaa 84,104,184
ta 234,234,53760
mvaa 104,104,204
ta 220,235,53248
mvaa 104,32,188
ta 222,235,53760
mvaa 108,36,188
dl 229,40,53,1024
mvaa 108,108,204
s_add_u32 s53,s53,s58
ta 236,235,36864
mvaa 88,108,184
ta 238,235,37376
mvaa 88,36,200
ta 0,236,36864
s_waitcnt lgkmcnt(8)
mvaa 0,72,4
ta 2,236,37376
mvaa 0,76,80
dl 230,44,54,2048
mvaa 4,64,80
ta 20,236,53248
mvaa 4,68,4
ta 22,236,53760
mvaa 92,68,8
ta 44,237,53248
mvaa 92,64,84
ta 46,237,53760
mvaa 72,76,84
dl 231,44,54,3072
mvaa 72,72,8
s_add_u32 m0,s98,32768
ta 40,237,36864
s_add_u32 s54,s54,s58
mvaa 144,72,12
ta 42,237,37376
mvaa 144,76,88
ta 192,234,40960
mvaa 148,64,88
ta 194,234,41472
mvaa 148,68,12
ta 176,234,57344
mvaa 156,68,16
dl 224,36,52,0
mvaa 156,64,92
ta 178,234,57856
mvaa 152,76,92
ta 196,235,57344
mvaa 152,72,16
ta 198,235,57856
mvaa 160,208,16
ta 180,235,40960
mvaa 160,224,92
dl 225,36,52,1024
mvaa 164,212,92
ta 182,235,41472
mvaa 164,228,16
ta 52,236,40960
mvaa 172,228,12
ta 54,236,41472
mvaa 172,212,88
ta 48,236,57344
mvaa 168,224,88
s_mov_b64 exec,3
global_load_dword a250,v253,s[96:97]
s_mov_b64 exec,-1
mvaa 168,208,12
ta 50,236,57856
v_lshrrev_b32_e32 v253,1,v244
mvaa 176,208,8
ta 60,237,57344
mvaa 176,224,84
ta 62,237,57856
mvaa 180,212,84
ta 56,237,40960
mvaa 180,228,8
dl 226,36,52,2048
mvaa 188,228,4
ta 58,237,41472
mvaa 188,212,80
ra 96,233,0
mvaa 184,224,80
ra 24,232,0
mvaa 184,208,4
ra 100,233,2048
s_waitcnt lgkmcnt(8)
mvaa 192,216,4
dl 227,36,52,3072
mvaa 192,232,80
s_add_u32 s52,s52,s56
s_add_u32 m0,s98,36864
ra 28,232,2048
mvaa 196,220,80
ra 104,233,4096
mvaa 196,236,4
ra 32,232,4096
mvaa 204,236,8
ta 188,238,45056
mvaa 204,220,84
ta 190,238,45568
mvaa 200,232,84
dl 224,36,52,0
mvaa 200,216,8
ta 204,238,61440
maaa 164,216,12
ta 206,238,61952
maaa 164,232,88
ta 184,239,61440
maaa 148,220,88
ta 186,239,61952
maaa 148,236,12
dl 225,36,52,1024
maaa 144,236,16
ra 108,233,6144
v_add_u32_e32 v233,0xffff0000,v233
maaa 144,220,92
ra 36,232,6144
v_add_u32_e32 v232,0xffff0000,v232
maaa 160,232,92
ta 200,239,45056
v_accvgpr_read_b32 v208,a164
maaa 160,216,16
ta 202,239,45568
v_accvgpr_read_b32 v209,a165
maaa 128,0,16
dl 226,36,52,2048
maaa 128,20,92
v_accvgpr_read_b32 v210,a166
v_accvgpr_read_b32 v211,a167
maaa 112,44,92
v_accvgpr_read_b32 v212,a148
v_accvgpr_read_b32 v213,a149
maaa 112,40,16
v_accvgpr_read_b32 v214,a150
v_accvgpr_read_b32 v215,a151
maaa 116,40,12
v_accvgpr_read_b32 v220,a144
v_accvgpr_read_b32 v221,a145
maaa 116,44,88
dl 227,36,52,3072
maaa 132,20,88
s_add_u32 s52,s52,s60
v_accvgpr_read_b32 v222,a146
v_accvgpr_read_b32 v223,a147
maaa 132,0,12
s_lshl_b32 s60,%1,7
v_accvgpr_read_b32 v216,a160
v_accvgpr_read_b32 v217,a161
maaa 136,0,8
s_and_b32 s87,s60,7552
v_accvgpr_read_b32 v218,a162
v_accvgpr_read_b32 v219,a163
maaa 136,20,84
s_add_u32 s61,s60,83914752
maaa 120,44,84
maaa 120,40,8
maaa 124,40,4
maaa 124,44,80
maaa 140,20,80
maaa 140,0,4
mvaa 40,192,4
mvaa 40,176,80
mvaa 44,196,80
mvaa 44,180,4
mvaa 52,180,8
mvaa 52,196,84
mvaa 48,176,84
mvaa 48,192,8
mvaa 56,192,12
s_waitcnt vmcnt(9) lgkmcnt(0)
s_barrier
mvaa 56,176,88
s_cmp_eq_u32 s12,0
s_cselect_b64 exec,1,0
buffer_atomic_add v247,off,s[8:11],s85 sc1
s_mov_b64 exec,-1
mvaa 60,196,88
s_add_u32 m0,s62,32768
ta 172,234,0
s_lshr_b32 s85,s86,1
mvaa 60,180,12
s_lshl_b32 s85,s85,11
ta 174,234,512
v_bfe_u32 v247,v244,2,6
mvaa 68,180,16
ta 156,234,16384
mvaa 68,196,92
ta 158,234,16896
mvaa 64,176,92
dl 228,40,53,0
mvaa 64,192,16
ta 168,235,16384
mvaa 32,52,16
ta 170,235,16896
mvaa 32,48,92
ta 152,235,0
mvaa 36,60,92
ta 154,235,512
mvaa 36,56,16
dl 229,40,53,1024
mvaa 28,56,12
s_add_u32 s53,s53,s57
ta 68,236,0
mvaa 28,60,88
ta 70,236,512
mvaa 24,48,88
ta 64,236,16384
mvaa 24,52,12
ta 66,236,16896
mvaa 16,52,8
dl 230,44,54,2048
mvaa 16,48,84
ta 76,237,16384
mvaa 20,60,84
ta 78,237,16896
mvaa 20,56,8
ta 72,237,0
mvaa 12,56,4
ta 74,237,512
mvaa 12,60,80
dl 231,44,54,3072
mvaa 8,48,80
s_add_u32 m0,s62,49152
s_add_u32 s54,s54,s57
ta 228,234,4096
mvaa 8,52,4
ta 230,234,4608
mvaa 76,4,188
ta 212,234,20480
mvaa 76,80,204
ta 214,234,20992
mvaa 96,80,184
dl 228,40,53,0
mvaa 96,4,200
ta 224,235,20480
mvaa 100,8,200
ta 226,235,20992
mvaa 100,84,184
ta 208,235,4096
mvaa 80,84,204
ta 210,235,4608
mvaa 80,8,188
dl 229,40,53,1024
mvaa 84,12,188
s_add_u32 s53,s53,s58
ta 236,236,4096
mvaa 84,88,204
ta 238,236,4608
mvaa 104,88,184
ta 220,236,20480
mvaa 104,12,200
ta 222,236,20992
mvaa 108,16,200
dl 230,44,54,2048
mvaa 108,92,184
ta 232,237,20480
mvaa 88,92,204
ta 234,237,20992
mvaa 88,16,188
ta 216,237,4096
s_waitcnt lgkmcnt(8)
mvaa 0,172,24
ta 218,237,4608
mvaa 0,156,96
dl 231,44,54,3072
mvaa 4,168,96
s_add_u32 m0,s98,65536
ta 40,234,8192
s_add_u32 s54,s54,s58
mvaa 4,152,24
ta 42,234,8704
mvaa 92,152,28
ta 44,234,24576
mvaa 92,168,100
ta 46,234,25088
mvaa 72,156,100
dl 224,36,52,0
mvaa 72,172,28
ta 20,235,24576
mvaa 144,172,32
ta 22,235,25088
mvaa 144,156,104
ta 0,235,8192
mvaa 148,168,104
ta 2,235,8704
mvaa 148,152,32
dl 225,36,52,1024
mvaa 156,152,36
ta 180,236,8192
mvaa 156,168,108
ta 182,236,8704
mvaa 152,156,108
ta 196,236,24576
mvaa 152,172,36
ta 198,236,25088
mvaa 160,68,36
dl 226,36,52,2048
mvaa 160,64,108
ta 176,237,24576
mvaa 164,76,108
ta 178,237,25088
mvaa 164,72,36
ta 192,237,8192
mvaa 172,72,32
ta 194,237,8704
mvaa 172,76,104
dl 227,36,52,3072
mvaa 168,64,104
s_add_u32 s52,s52,s56
s_add_u32 m0,s98,69632
ta 56,238,12288
mvaa 168,68,32
s_mov_b32 s98,s94
ta 58,238,12800
s_mov_b32 s99,s98
mvaa 176,68,28
s_and_b32 s94,s12,1
ta 60,238,28672
s_lshl_b32 s94,s94,14
mvaa 176,64,100
s_mul_i32 s56,s59,12288
ta 62,238,29184
mvaa 180,76,100
dl 224,36,52,0
mvaa 180,72,28
ta 48,239,28672
v_mov_b32_e32 v224,v244
mvaa 188,72,24
ta 50,239,29184
mvaa 188,76,96
ta 52,239,12288
mvaa 184,64,96
ta 54,239,12800
mvaa 184,68,24
dl 225,36,52,1024
mvaa 192,228,24
ra 80,233,0
v_add_u32_e32 v225,65536,v224
mvaa 192,212,96
ra 4,232,0
mvaa 196,224,96
ra 84,233,2048
s_waitcnt lgkmcnt(8)
mvaa 196,208,24
ra 8,232,2048
mvaa 204,208,28
dl 226,36,52,2048
mvaa 204,224,100
ra 88,233,4096
v_mov_b32_e32 v226,1065369472
mvaa 200,212,100
ra 12,232,4096
mvaa 200,228,28
ra 16,232,6144
v_add_u32_e32 v232,0x8000,v232
mvaa 208,228,32
ra 92,233,6144
v_add_u32_e32 v233,0x8000,v233
mvaa 208,212,104
dl 227,36,52,3072
mvaa 212,224,104
s_mov_b64 s[36:37],s[28:29]
s_mov_b32 s38,8388608
s_mov_b32 s39,131072
mvaa 212,208,32
s_lshl_b32 s52,s12,12
mvaa 220,208,36
mvaa 220,224,108
mvaa 216,212,108
mvaa 216,228,36
maaa 128,236,36
maaa 128,220,108
maaa 112,232,108
maaa 112,216,36
maaa 116,216,32
maaa 116,232,104
maaa 132,220,104
maaa 132,236,32
maaa 136,236,28
maaa 136,220,100
maaa 120,232,100
maaa 120,216,28
maaa 124,216,24
maaa 124,232,96
maaa 140,220,96
s_waitcnt vmcnt(8) lgkmcnt(0)
s_barrier
s_add_u32 m0,s62,0
maaa 140,236,24
dl 228,40,53,0
mvaa 40,40,24
ta 200,234,32768
mvaa 40,44,96
ta 202,234,33280
mvaa 44,20,96
ta 184,234,49152
mvaa 44,0,24
ta 186,234,49664
mvaa 52,0,28
dl 229,40,53,1024
mvaa 52,20,100
s_add_u32 s53,s53,s57
ta 204,235,49152
mvaa 48,44,100
ta 206,235,49664
mvaa 48,40,28
ta 188,235,32768
mvaa 56,40,32
ta 190,235,33280
mvaa 56,44,104
dl 230,44,54,2048
mvaa 60,20,104
ta 164,236,32768
mvaa 60,0,32
ta 166,236,33280
mvaa 68,0,36
ta 148,236,49152
mvaa 68,20,108
ta 150,236,49664
mvaa 64,44,108
dl 231,44,54,3072
mvaa 64,40,36
s_add_u32 m0,s62,16384
s_add_u32 s54,s54,s57
ta 160,237,49152
mvaa 32,180,36
ta 162,237,49664
mvaa 32,196,108
ta 144,237,32768
mvaa 36,176,108
ta 146,237,33280
mvaa 36,192,36
dl 228,40,53,0
mvaa 28,192,32
ta 152,234,36864
mvaa 28,176,104
ta 154,234,37376
mvaa 24,196,104
ta 168,234,53248
mvaa 24,180,32
ta 170,234,53760
mvaa 16,180,28
dl 229,40,53,1024
mvaa 16,196,100
s_add_u32 s53,s53,s58
ta 156,235,53248
mvaa 20,176,100
ta 158,235,53760
mvaa 20,192,28
ta 172,235,36864
mvaa 12,192,24
ta 174,235,37376
mvaa 12,176,96
dl 230,44,54,2048
mvaa 8,196,96
ta 208,236,36864
mvaa 8,180,24
ta 210,236,37376
mvaa 76,24,56
ta 224,236,53248
mvaa 76,96,60
ta 226,236,53760
mvaa 96,96,48
dl 231,44,54,3072
mvaa 96,24,52
s_add_u32 s54,s54,s58
ta 212,237,53248
s_add_u32 s58,s56,8192
mvaa 100,28,52
ta 214,237,53760
mvaa 100,100,48
ta 228,237,36864
mvaa 80,100,60
ta 230,237,37376
mvaa 80,28,56
ta 216,234,40960
mvaa 84,32,56
ta 218,234,41472
mvaa 84,104,60
ta 232,234,57344
mvaa 104,104,48
ta 234,234,57856
mvaa 104,32,52
ta 220,235,57344
mvaa 108,36,52
ta 222,235,57856
mvaa 108,108,48
ta 236,235,40960
mvaa 88,108,60
ta 238,235,41472
mvaa 88,36,56
ta 0,236,40960
s_waitcnt lgkmcnt(10)
mvaa 0,200,4
ta 2,236,41472
mvaa 0,184,80
ta 20,236,57344
mvaa 4,204,80
ta 22,236,57856
mvaa 4,188,4
ta 192,237,57344
mvaa 92,188,8
ta 194,237,57856
mvaa 92,204,84
ta 176,237,40960
mvaa 72,184,84
ta 178,237,41472
mvaa 72,200,8
ta 196,238,45056
mvaa 144,200,12
ta 198,238,45568
mvaa 144,184,88
ta 180,238,61440
mvaa 148,204,88
ta 182,238,61952
mvaa 148,188,12
ra 24,232,0
mvaa 156,188,16
ra 28,232,2048
mvaa 156,204,92
ra 32,232,4096
mvaa 152,184,92
ra 36,232,6144
mvaa 152,200,16
ta 188,239,61440
mvaa 160,164,16
ta 190,239,61952
mvaa 160,148,92
ta 204,239,45056
mvaa 164,160,92
ta 206,239,45568
mvaa 164,144,16
mvaa 172,144,12
mvaa 172,160,88
mvaa 168,148,88
mvaa 168,164,12
mvaa 176,164,8
mvaa 176,148,84
mvaa 180,160,84
mvaa 180,144,8
mvaa 188,144,4
mvaa 188,160,80
mvaa 184,148,80
mvaa 184,164,4
mvaa 192,152,4
mvaa 192,168,80
mvaa 196,156,80
mvaa 196,172,4
mvaa 204,172,8
mvaa 204,156,84
mvaa 200,168,84
mvaa 200,152,8
mvaa 208,152,12
mvaa 208,168,88
mvaa 212,156,88
mvaa 212,172,12
mvaa 220,172,16
mvaa 220,156,92
mvaa 216,168,92
mvaa 216,152,16
maaa 128,208,16
maaa 128,224,92
maaa 112,212,92
s_waitcnt vmcnt(0) lgkmcnt(0)
s_barrier
s_add_u32 m0,s62,32768
maaa 112,228,16
dl 228,40,53,0
maaa 116,228,12
ta 40,234,0
maaa 116,212,88
ta 42,234,512
maaa 132,224,88
ta 44,235,0
maaa 132,208,12
ta 46,235,512
maaa 136,208,8
dl 229,40,53,1024
maaa 136,224,84
s_add_u32 s53,s53,s57
ta 48,236,0
maaa 120,212,84
ta 50,236,512
maaa 120,228,8
ta 52,237,0
maaa 124,228,4
ta 54,237,512
maaa 124,212,80
dl 230,44,54,2048
maaa 140,224,80
ta 56,234,4096
maaa 140,208,4
ta 58,234,4608
mvaa 40,216,4
ta 60,235,4096
mvaa 40,232,80
ta 62,235,4608
mvaa 44,220,80
dl 231,44,54,3072
mvaa 44,236,4
s_add_u32 m0,s62,49152
ta 64,236,4096
s_add_u32 s54,s54,s57
mvaa 52,236,8
s_add_u32 s57,s56,4096
ta 66,236,4608
mvaa 52,220,84
ta 68,237,4096
mvaa 48,232,84
ta 70,237,4608
mvaa 48,216,8
buffer_load_dword v248,v250,s[8:11],0 offen sc1
mvaa 56,216,12
ta 72,234,8192
mvaa 56,232,88
ta 74,234,8704
mvaa 60,220,88
ta 76,235,8192
mvaa 60,236,12
ta 78,235,8704
mvaa 68,236,16
dl 228,40,53,0
mvaa 68,220,92
ta 96,236,8192
mvaa 64,232,92
ta 98,236,8704
mvaa 64,216,16
ta 100,237,8192
mvaa 32,0,16
ta 102,237,8704
mvaa 32,20,92
dl 229,40,53,1024
mvaa 36,192,92
s_lshl_b32 s43,s19,15
ta 104,238,12288
s_add_u32 s42,s43,16384
mvaa 36,176,16
s_add_u32 s53,s87,83898368
ta 106,238,12800
s_lshl_b32 s87,s91,8
mvaa 28,176,12
s_lshl_b32 s91,s23,2
ta 108,239,12288
s_mov_b32 s40,858993459
mvaa 28,192,88
s_mov_b32 s41,858993459
ta 110,239,12800
v_cndmask_b32_e64 v228,0,v226,s[40:41]
mvaa 24,20,88
dl 230,44,54,2048
mvaa 24,0,12
ta 144,234,16384
v_cndmask_b32_e64 v229,0,v226,s[40:41]
mvaa 16,0,8
ta 146,234,16896
v_cndmask_b32_e64 v230,0,v226,s[40:41]
mvaa 16,20,84
ta 164,235,16384
mvaa 20,192,84
ta 166,235,16896
mvaa 20,176,8
dl 231,44,54,3072
mvaa 12,176,4
s_add_u32 s54,s87,s88
ta 168,236,16384
s_lshl_b32 s87,s12,16
mvaa 12,192,80
s_add_u32 s87,s87,s54
ta 170,236,16896
s_andn2_b32 s87,s87,64
mvaa 8,20,80
s_lshl_b32 s88,s66,5
ta 244,235,24576
s_add_u32 s63,s87,s88
mvaa 8,0,4
s_lshl_b32 s88,s67,5
ta 246,235,25088
s_add_u32 s65,s87,s88
mvaa 76,4,196
s_and_b32 s87,s20,4
ta 212,235,20480
s_lshl_b32 s87,s87,8
mvaa 76,80,180
s_and_b32 s88,s20,3
ta 214,235,20992
s_add_u32 s85,s85,s87
mvaa 96,80,188
s_add_u32 s85,s85,s89
ta 236,237,20480
s_lshl_b32 s89,s88,20
mvaa 96,4,204
s_add_u32 s85,s85,s89
ta 238,237,20992
s_and_b32 s89,s86,7
mvaa 100,8,204
s_add_u32 s49,s49,s87
ta 216,236,20480
s_lshl_b32 s89,s89,7
mvaa 100,84,188
s_add_u32 s49,s49,s89
ta 218,236,20992
s_lshl_b32 s89,s88,18
mvaa 80,84,180
s_add_u32 s49,s49,s89
ta 192,234,20480
s_lshl_b32 s89,s12,6
mvaa 80,8,196
s_add_u32 s89,s89,83886080
ta 194,234,20992
s_add_u32 s91,s91,s89
mvaa 84,12,196
s_mov_b32 s46,16384
ra 20,233,0
s_mov_b32 s86,268435456
mvaa 84,88,180
s_bfe_u32 s87,s98,327691
s_cselect_b32 s87,2048,0
ra 0,232,32768
v_cndmask_b32_e64 v231,0,v226,s[40:41]
mvaa 104,88,188
s_mov_b32 s40,0xf0000
ra 80,233,4096
s_mov_b32 s41,0xfff00ff
mvaa 104,12,204
s_sub_u32 s98,s98,s87
ra 4,233,32768
v_cndmask_b32_e64 v226,0,v252,s[40:41]
mvaa 108,16,204
s_mov_b32 s40,0x3f0003
ra 84,232,36864
s_mov_b32 s41,0x3fff03ff
mvaa 108,92,188
s_sub_u32 s47,32768,s43
ra 8,233,2048
v_cndmask_b32_e64 v227,0,v252,s[40:41]
mvaa 88,92,180
s_mov_b32 s40,0xfff0ffff
ra 88,233,36864
s_mov_b32 s41,0xf000ff00
mvaa 88,16,196
s_add_u32 s47,s47,32768
ra 12,232,34816
s_lshr_b32 s45,s15,1
s_waitcnt lgkmcnt(10)
mvaa 0,40,24
s_xor_b32 s62,s63,128
ta 188,237,16384
s_xor_b32 s64,s65,128
mvaa 4,44,24
s_ff1_i32_b32 s80,s45
ta 190,237,16896
s_sub_u32 s44,s56,2048
mvaa 92,44,28
ra 92,233,6144
mvaa 72,40,28
ra 16,233,34816
mvaa 144,40,32
mvaa 148,44,32
mvaa 156,44,36
mvaa 152,40,36
mvaa 160,48,36
ra 44,232,38912
v_mov_b32_e32 v232,0xc1a00000
mvaa 164,52,36
ra 40,233,38912
v_mov_b32_e32 v233,0xc1a00000
mvaa 172,52,32
mvaa 168,48,32
mvaa 176,48,28
mvaa 180,52,28
mvaa 188,52,24
mvaa 184,48,24
mvaa 192,56,24
ta 52,239,28672
mvaa 196,60,24
ta 54,239,29184
mvaa 204,60,28
mvaa 200,56,28
mvaa 208,56,32
mvaa 212,60,32
mvaa 220,60,36
mvaa 216,56,36
maaa 128,64,36
maaa 112,68,36
maaa 116,68,32
maaa 132,64,32
maaa 136,64,28
maaa 120,68,28
maaa 124,68,24
maaa 140,64,24
mvaa 40,72,24
mvaa 44,76,24
ta 64,238,28672
ta 66,238,29184
mvaa 52,76,28
s_waitcnt vmcnt(4)
v_and_b32_e32 v248,v242,v248
v_cmp_ne_u32_e64 s[100:101],v243,v248
mvaa 48,72,28
s_waitcnt vmcnt(0)
s_barrier
ta 148,234,32768
ta 150,234,33280
ta 152,234,49152
ta 154,234,49664
ta 156,235,49152
mvaa 56,72,32
s_cmp_lg_u64 s[100:101],0
s_cbranch_scc1 L_q_spin
L_q_ok:
ta 158,235,49664
ta 160,235,32768
ta 162,235,33280
ta 172,236,32768
ta 174,236,33280
ta 176,236,49152
)"
R"(ta 178,236,49664
mvaa 60,76,32
buffer_load_dword v242,v247,s[8:11],s91 offen offset:0 sc1
ta 180,237,49152
ta 182,237,49664
ta 184,237,32768
ta 186,237,33280
ta 196,234,36864
mvaa 68,76,36
s_mov_b32 m0,s95
ta 198,234,37376
ta 200,234,53248
ta 202,234,53760
ta 204,235,53248
ta 206,235,53760
ta 208,235,36864
ta 210,235,37376
mvaa 64,72,36
s_lshl_b32 s35,s90,2
ta 220,236,36864
ta 222,236,37376
ta 224,236,53248
mvaa 32,96,36
s_lshl_b32 s90,s22,2
ta 226,236,53760
ta 228,237,53248
mvaa 36,100,36
s_add_u32 s90,s90,s89
ta 230,237,53760
ta 232,237,36864
mvaa 28,100,32
ds 249,8,42,0
ta 234,237,37376
ta 240,235,40960
mvaa 24,96,32
s_add_u32 s35,s35,83886080
ta 242,235,41472
ta 48,239,45056
mvaa 16,96,28
ta 50,239,45568
ta 60,239,61440
mvaa 20,100,28
ta 62,239,61952
ta 56,238,61440
mvaa 12,100,24
ta 58,238,61952
ta 68,238,45056
mvaa 8,96,24
ds 249,8,42,1024
ta 70,238,45568
ta 76,237,24576
mvaa 76,24,104
ta 78,237,25088
ta 72,237,40960
mvaa 96,24,108
ta 74,237,41472
ta 100,237,57344
mvaa 100,28,108
ta 102,237,57856
ta 96,236,57344
mvaa 80,28,104
ta 98,236,57856
ta 24,236,40960
mvaa 84,32,104
ds 249,8,42,2048
ta 26,236,41472
mvaa 104,32,108
ta 28,236,24576
ta 30,236,25088
mvaa 108,36,108
v_lshl_add_u32 v238,v244,3,s4
v_min_u32_e32 v238,L_att_end-L_att_pc-4,v238
mvaa 88,36,104
ta 32,234,24576
ta 34,234,25088
s_waitcnt lgkmcnt(15)
mvaa 0,144,20
ta 108,234,40960
ta 110,234,41472
mvaa 0,148,0
ds 249,8,42,3072
ta 36,234,57344
ta 38,234,57856
ta 104,235,57344
ta 106,235,57856
mvaa 0,152,4
v_mov_b32_e32 v234,0xc1a00000
v_mov_b32_e32 v235,0xc1a00000
mvaa 4,156,4
v_cndmask_b32_e64 v250,0,v252,s[40:41]
s_mov_b32 s40,0xffc0fffc
mvaa 4,160,0
s_mov_b32 s41,0xc000fc00
v_bfe_i32 v248,v244,4,1
v_mul_f32_e32 v0,0x3e0293ee,v0
mvaa 4,164,20
v_mul_f32_e32 v1,0x3e0293ee,v1
v_mul_f32_e32 v2,0x3e0293ee,v2
mvaa 92,164,8
buffer_load_dword v240,v247,s[8:11],s90 offen offset:0 sc1
mvaa 92,160,12
v_mul_f32_e32 v3,0x3e0293ee,v3
cv 0,0,1
mvaa 92,156,16
v_mul_f32_e32 v4,0x3e0293ee,v4
cv 1,2,3
mvaa 72,152,16
v_mul_f32_e32 v5,0x3e0293ee,v5
s_waitcnt lgkmcnt(0)
s_barrier
mvaa 72,148,12
s_mov_b32 m0,s70
v_mul_f32_e32 v6,0x3e0293ee,v6
v_mul_f32_e32 v7,0x3e0293ee,v7
mvaa 72,144,8
buffer_load_dword v241,v247,s[8:11],s90 offen offset:256 sc1
mvaa 144,144,80
cv 2,4,5
cv 3,6,7
mvaa 144,148,84
v_mul_f32_e32 v92,0x3e0293ee,v92
v_mul_f32_e32 v93,0x3e0293ee,v93
mvaa 144,152,88
v_mul_f32_e32 v94,0x3e0293ee,v94
v_mul_f32_e32 v72,0x3e0293ee,v72
mvaa 148,156,88
v_mul_f32_e32 v95,0x3e0293ee,v95
v_mul_f32_e32 v73,0x3e0293ee,v73
mvaa 148,160,84
ds 249,8,43,0
mvaa 148,164,80
v_mul_f32_e32 v74,0x3e0293ee,v74
v_mul_f32_e32 v75,0x3e0293ee,v75
mvaa 156,164,92
cv 72,72,73
cv 73,74,75
mvaa 156,160,44
cv 74,92,93
cv 75,94,95
mvaa 156,156,40
v_mul_f32_e32 v144,0x3e0293ee,v144
v_mul_f32_e32 v145,0x3e0293ee,v145
mvaa 152,152,40
ds 249,8,43,1024
mvaa 152,148,44
v_mul_f32_e32 v146,0x3e0293ee,v146
v_mul_f32_e32 v147,0x3e0293ee,v147
mvaa 152,144,92
cv 144,144,145
v_mul_f32_e32 v148,0x3e0293ee,v148
mvaa 160,168,92
cv 145,146,147
v_mul_f32_e32 v149,0x3e0293ee,v149
mvaa 160,172,44
v_mul_f32_e32 v150,0x3e0293ee,v150
v_mul_f32_e32 v151,0x3e0293ee,v151
mvaa 160,176,40
ds 249,8,43,2048
mvaa 164,180,40
cv 146,148,149
cv 147,150,151
mvaa 164,184,44
v_mul_f32_e32 v156,0x3e0293ee,v156
v_mul_f32_e32 v157,0x3e0293ee,v157
mvaa 164,188,92
v_mul_f32_e32 v152,0x3e0293ee,v152
v_mul_f32_e32 v158,0x3e0293ee,v158
mvaa 172,188,80
v_mul_f32_e32 v159,0x3e0293ee,v159
v_mul_f32_e32 v153,0x3e0293ee,v153
mvaa 172,184,84
ds 249,8,43,3072
s_mov_b32 m0,s72
mvaa 172,180,88
v_mul_f32_e32 v154,0x3e0293ee,v154
v_mul_f32_e32 v155,0x3e0293ee,v155
mvaa 168,176,88
cv 152,152,153
cv 153,154,155
mvaa 168,172,84
ds_write_b128 v251,v[0:3] offset:0
mvaa 168,168,80
cv 154,156,157
cv 155,158,159
mvaa 176,168,8
ds 255,8,42,0
mvaa 176,172,12
v_mul_f32_e32 v160,0x3e0293ee,v160
v_mul_f32_e32 v161,0x3e0293ee,v161
mvaa 176,176,16
v_mul_f32_e32 v162,0x3e0293ee,v162
v_mul_f32_e32 v163,0x3e0293ee,v163
mvaa 180,180,16
cv 160,160,161
v_mul_f32_e32 v164,0x3e0293ee,v164
mvaa 180,184,12
v_mul_f32_e32 v165,0x3e0293ee,v165
cv 161,162,163
mvaa 180,188,8
ds 255,8,42,1024
ds_write_b128 v251,v[72:75] offset:4096
mvaa 188,188,20
v_mul_f32_e32 v166,0x3e0293ee,v166
v_mul_f32_e32 v167,0x3e0293ee,v167
mvaa 188,184,0
cv 162,164,165
cv 163,166,167
mvaa 188,180,4
v_mul_f32_e32 v172,0x3e0293ee,v172
v_mul_f32_e32 v173,0x3e0293ee,v173
mvaa 184,176,4
v_mul_f32_e32 v174,0x3e0293ee,v174
v_mul_f32_e32 v175,0x3e0293ee,v175
mvaa 184,172,0
ds 255,8,42,2048
mvaa 184,168,20
v_mul_f32_e32 v168,0x3e0293ee,v168
v_mul_f32_e32 v169,0x3e0293ee,v169
mvaa 192,192,20
ds_write_b128 v251,v[144:147] offset:8192
mvaa 192,196,0
v_mul_f32_e32 v170,0x3e0293ee,v170
v_mul_f32_e32 v171,0x3e0293ee,v171
mvaa 192,200,4
cv 168,168,169
cv 169,170,171
mvaa 196,204,4
ds 255,8,42,3072
mvaa 196,208,0
cv 170,172,173
cv 171,174,175
mvaa 196,212,20
v_mul_f32_e32 v176,0x3e0293ee,v176
v_mul_f32_e32 v177,0x3e0293ee,v177
mvaa 204,212,8
v_mul_f32_e32 v178,0x3e0293ee,v178
v_mul_f32_e32 v179,0x3e0293ee,v179
mvaa 204,208,12
ds_write_b128 v251,v[152:155] offset:12288
mvaa 204,204,16
buffer_load_dword v243,v247,s[8:11],s91 offen offset:256 sc1
mvaa 200,200,16
cv 176,176,177
cv 177,178,179
mvaa 200,196,12
v_mul_f32_e32 v180,0x3e0293ee,v180
v_mul_f32_e32 v181,0x3e0293ee,v181
mvaa 200,192,8
v_mul_f32_e32 v182,0x3e0293ee,v182
v_mul_f32_e32 v183,0x3e0293ee,v183
mvaa 208,192,80
cv 178,180,181
cv 179,182,183
mvaa 208,196,84
v_mul_f32_e32 v188,0x3e0293ee,v188
v_mul_f32_e32 v189,0x3e0293ee,v189
mvaa 208,200,88
ds_write_b128 v251,v[160:163] offset:13312
mvaa 212,204,88
v_mul_f32_e32 v190,0x3e0293ee,v190
v_mul_f32_e32 v191,0x3e0293ee,v191
mvaa 212,208,84
v_mul_f32_e32 v184,0x3e0293ee,v184
v_mul_f32_e32 v185,0x3e0293ee,v185
mvaa 212,212,80
v_mul_f32_e32 v186,0x3e0293ee,v186
v_mul_f32_e32 v187,0x3e0293ee,v187
mvaa 220,212,92
cv 184,184,185
cv 185,186,187
mvaa 220,208,44
cv 186,188,189
cv 187,190,191
mvaa 220,204,40
v_mul_f32_e32 v192,0x3e0293ee,v192
v_mul_f32_e32 v193,0x3e0293ee,v193
mvaa 216,200,40
ds_write_b128 v251,v[168:171] offset:9216
mvaa 216,196,44
v_mul_f32_e32 v194,0x3e0293ee,v194
v_mul_f32_e32 v195,0x3e0293ee,v195
mvaa 216,192,92
cv 192,192,193
cv 193,194,195
maaa 128,216,92
v_mul_f32_e32 v196,0x3e0293ee,v196
v_mul_f32_e32 v197,0x3e0293ee,v197
maaa 128,220,44
v_mul_f32_e32 v198,0x3e0293ee,v198
v_mul_f32_e32 v199,0x3e0293ee,v199
maaa 128,224,40
cv 194,196,197
cv 195,198,199
maaa 112,228,40
v_mul_f32_e32 v204,0x3e0293ee,v204
v_mul_f32_e32 v205,0x3e0293ee,v205
maaa 112,232,44
ds_write_b128 v251,v[176:179] offset:5120
maaa 112,236,92
v_mul_f32_e32 v206,0x3e0293ee,v206
v_mul_f32_e32 v207,0x3e0293ee,v207
maaa 116,236,80
v_mul_f32_e32 v200,0x3e0293ee,v200
v_mul_f32_e32 v201,0x3e0293ee,v201
maaa 116,232,84
v_mul_f32_e32 v202,0x3e0293ee,v202
v_mul_f32_e32 v203,0x3e0293ee,v203
maaa 116,228,88
cv 200,200,201
cv 201,202,203
maaa 132,224,88
cv 202,204,205
cv 203,206,207
maaa 132,220,84
v_mul_f32_e32 v208,0x3e0293ee,v208
v_mul_f32_e32 v209,0x3e0293ee,v209
maaa 132,216,80
ds_write_b128 v251,v[184:187] offset:1024
maaa 136,216,8
v_mul_f32_e32 v210,0x3e0293ee,v210
v_mul_f32_e32 v211,0x3e0293ee,v211
maaa 136,220,12
cv 208,208,209
cv 209,210,211
maaa 136,224,16
v_mul_f32_e32 v212,0x3e0293ee,v212
v_mul_f32_e32 v213,0x3e0293ee,v213
maaa 120,228,16
s_waitcnt vmcnt(11)
s_barrier
maaa 120,232,12
v_mul_f32_e32 v214,0x3e0293ee,v214
v_mul_f32_e32 v215,0x3e0293ee,v215
maaa 120,236,8
cv 210,212,213
cv 211,214,215
maaa 124,236,20
ds_write_b128 v251,v[192:195] offset:2048
maaa 124,232,0
v_mul_f32_e32 v220,0x3e0293ee,v220
v_mul_f32_e32 v221,0x3e0293ee,v221
maaa 124,228,4
v_mul_f32_e32 v216,0x3e0293ee,v216
v_mul_f32_e32 v217,0x3e0293ee,v217
maaa 140,224,4
v_mul_f32_e32 v222,0x3e0293ee,v222
v_mul_f32_e32 v223,0x3e0293ee,v223
maaa 140,220,0
v_mul_f32_e32 v218,0x3e0293ee,v218
v_mul_f32_e32 v219,0x3e0293ee,v219
maaa 140,216,20
cv 216,216,217
cv 217,218,219
mvaa 88,92,64
cv 218,220,221
cv 219,222,223
mvaa 88,44,68
ds_write_b128 v251,v[200:203] offset:6144
mvaa 88,40,56
v_accvgpr_read_b32 v0,a128
v_accvgpr_read_b32 v1,a129
mvaa 108,40,60
v_accvgpr_read_b32 v2,a130
v_accvgpr_read_b32 v3,a131
mvaa 108,44,48
v_mul_f32_e32 v0,0x3e0293ee,v0
v_mul_f32_e32 v1,0x3e0293ee,v1
cv 88,88,89
cv 89,90,91
mvaa 108,92,52
buffer_store_dwordx2 v[88:89],v253,s[8:11],s99 offen offset:1024 sc0 sc1
mvaa 36,76,92
v_mul_f32_e32 v2,0x3e0293ee,v2
v_mul_f32_e32 v3,0x3e0293ee,v3
mvaa 36,72,44
cv 0,0,1
cv 1,2,3
mvaa 36,100,40
ds_write_b128 v251,v[208:211] offset:10240
mvaa 32,96,40
cv 108,108,109
cv 109,110,111
mvaa 32,24,44
buffer_store_dwordx2 v[108:109],v253,s[8:11],s99 offen offset:1536 sc0 sc1
mvaa 32,28,92
v_accvgpr_read_b32 v4,a112
v_accvgpr_read_b32 v5,a113
mvaa 64,32,92
v_accvgpr_read_b32 v6,a114
v_mul_f32_e32 v4,0x3e0293ee,v4
mvaa 64,108,44
v_mul_f32_e32 v5,0x3e0293ee,v5
v_mul_f32_e32 v6,0x3e0293ee,v6
mvaa 64,36,40
cv 32,32,33
cv 33,34,35
cv 34,36,37
cv 35,38,39
mvaa 68,104,40
buffer_store_dwordx4 v[32:35],v244,s[8:11],s99 offen sc0 sc1
ds_write_b128 v251,v[216:219] offset:14336
mvaa 68,240,44
v_accvgpr_read_b32 v7,a115
v_mul_f32_e32 v7,0x3e0293ee,v7
mvaa 68,244,92
cv 2,4,5
cv 3,6,7
mvaa 60,244,80
ra 192,224,0
v_accvgpr_read_b32 v92,a116
mvaa 60,240,84
ra 196,224,1024
v_accvgpr_read_b32 v93,a117
mvaa 60,104,88
ra 200,224,2048
v_mul_f32_e32 v92,0x3e0293ee,v92
mvaa 56,36,88
ra 204,224,3072
v_mul_f32_e32 v93,0x3e0293ee,v93
mvaa 56,108,84
ds_write_b128 v251,v[0:3] offset:15360
mvaa 56,32,80
v_accvgpr_read_b32 v94,a118
v_accvgpr_read_b32 v95,a119
mvaa 24,28,80
v_accvgpr_read_b32 v72,a132
v_accvgpr_read_b32 v73,a133
mvaa 24,24,84
v_accvgpr_read_b32 v74,a134
v_mul_f32_e32 v94,0x3e0293ee,v94
mvaa 24,96,88
v_mul_f32_e32 v95,0x3e0293ee,v95
v_mul_f32_e32 v72,0x3e0293ee,v72
mvaa 28,100,88
v_mul_f32_e32 v73,0x3e0293ee,v73
v_mul_f32_e32 v74,0x3e0293ee,v74
v_accvgpr_read_b32 v75,a135
mvaa 28,72,84
v_mul_f32_e32 v75,0x3e0293ee,v75
cv 72,72,73
cv 73,74,75
cv 74,92,93
mvaa 28,76,80
ra 208,224,4096
cv 75,94,95
mvaa 20,76,8
ds_write_b128 v251,v[72:75] offset:11264
mvaa 20,72,12
v_accvgpr_read_b32 v144,a136
v_accvgpr_read_b32 v145,a137
mvaa 20,100,16
s_mov_b64 exec,3
global_load_dword v239,v238,s[96:97]
s_mov_b64 exec,-1
ra 212,224,5120
ra 248,224,14336
ra 252,224,15360
mvaa 16,96,16
v_accvgpr_read_b32 v146,a138
v_mul_f32_e32 v144,0x3e0293ee,v144
v_mul_f32_e32 v145,0x3e0293ee,v145
v_mul_f32_e32 v146,0x3e0293ee,v146
v_accvgpr_read_b32 v147,a139
v_mul_f32_e32 v147,0x3e0293ee,v147
cv 144,144,145
mvaa 16,24,12
ra 236,224,11264
v_accvgpr_read_b32 v148,a120
v_accvgpr_read_b32 v149,a121
v_accvgpr_read_b32 v150,a122
cv 145,146,147
mvaa 16,28,8
v_mul_f32_e32 v148,0x3e0293ee,v148
v_mul_f32_e32 v149,0x3e0293ee,v149
v_mul_f32_e32 v150,0x3e0293ee,v150
v_accvgpr_read_b32 v151,a123
v_mul_f32_e32 v151,0x3e0293ee,v151
cv 146,148,149
mvaa 48,32,8
ra 228,224,9216
cv 147,150,151
mvaa 48,108,12
ds_write_b128 v251,v[144:147] offset:7168
mvaa 48,36,16
v_accvgpr_read_b32 v156,a124
v_accvgpr_read_b32 v157,a125
mvaa 52,104,16
ra 232,224,10240
ra 216,224,6144
ra 220,224,7168
ra 224,224,8192
v_accvgpr_read_b32 v158,a126
v_accvgpr_read_b32 v159,a127
mvaa 52,240,12
v_accvgpr_read_b32 v152,a140
v_mul_f32_e32 v156,0x3e0293ee,v156
v_mul_f32_e32 v157,0x3e0293ee,v157
v_mul_f32_e32 v158,0x3e0293ee,v158
v_mul_f32_e32 v159,0x3e0293ee,v159
mvaa 52,244,8
v_mul_f32_e32 v152,0x3e0293ee,v152
v_accvgpr_read_b32 v153,a141
v_mul_f32_e32 v153,0x3e0293ee,v153
v_accvgpr_read_b32 v154,a142
v_accvgpr_read_b32 v155,a143
mvaa 44,244,20
v_mul_f32_e32 v154,0x3e0293ee,v154
v_mul_f32_e32 v155,0x3e0293ee,v155
cv 152,152,153
cv 153,154,155
cv 154,156,157
mvaa 44,240,0
ra 244,224,13312
cv 155,158,159
mvaa 44,104,4
ds_write_b128 v251,v[152:155] offset:3072
mvaa 40,36,4
cv 64,64,65
cv 56,56,57
mvaa 40,108,0
cv 57,58,59
cv 58,60,61
mvaa 40,32,20
ra 240,224,12288
cv 59,62,63
mvaa 8,28,20
cv 16,16,17
cv 17,18,19
mvaa 8,24,0
cv 48,48,49
cv 194,44,45
mvaa 8,96,4
cv 195,46,47
cv 192,40,41
mvaa 12,100,4
cv 193,42,43
cv 49,50,51
mvaa 12,72,0
s_cmp_eq_u32 s19,0
s_cselect_b32 s76,32768,0
s_cselect_b32 s77,49152,s86
s_cselect_b32 s78,65536,s46
s_cselect_b32 s79,81920,s86
mvaa 12,76,20
s_add_u32 s86,s95,98304
cv 8,8,9
v_add_u32_e32 v236,s86,v244
mvaa 76,20,64
s_add_u32 s46,s46,16384
v_xor_b32_e32 v237,4096,v236
s_waitcnt lgkmcnt(0)
s_barrier
s_mov_b32 m0,s68
mvaa 76,0,68
ds 249,8,76,0
ra 128,237,32768
ra 132,237,33792
mvaa 76,4,56
s_add_u32 s95,s47,32768
ra 136,237,34816
cv 9,10,11
mvaa 96,4,60
ra 140,237,35840
ra 144,237,49152
mvaa 96,0,48
cv 10,12,13
cv 11,14,15
mvaa 96,20,52
ra 148,237,50176
cv 50,52,53
mvaa 100,8,52
ds 249,8,76,1024
ra 152,237,51200
ra 156,237,52224
mvaa 100,12,48
ra 176,236,16384
cv 51,54,55
mvaa 100,16,60
ra 180,236,17408
cv 18,20,21
mvaa 80,16,56
ra 184,236,18432
cv 19,22,23
mvaa 80,12,68
ra 188,236,19456
cv 24,24,25
mvaa 80,8,64
ds 249,8,76,2048
ra 160,236,0
mvaa 84,80,64
ra 164,236,1024
cv 25,26,27
mvaa 84,84,68
ra 168,236,2048
ra 172,236,3072
mvaa 84,88,56
cv 26,28,29
cv 27,30,31
mvaa 104,88,60
cv 65,66,67
cv 66,68,69
mvaa 104,84,48
ds 249,8,76,3072
mvaa 104,80,52
s_mov_b32 m0,s69
cv 67,70,71
v_cndmask_b32_e64 v252,0,v252,s[40:41]
cvvv 164,8,192,112
cv 70,76,77
cv 71,78,79
cvvv 168,8,48,116
ds 249,8,77,0
cvvv 172,16,48,112
cv 94,96,97
v_mov_b32_e32 v76,v88
cvvv 176,16,56,116
cv 95,98,99
s_waitcnt vmcnt(9) lgkmcnt(0)
s_barrier
cvvv 180,24,56,112
ds 249,8,77,1024
cvvv 184,24,64,116
ra 0,224,32768
cv 96,100,101
cvvv 188,32,64,112
ra 4,224,33792
cv 97,102,103
cvaa 0,192,128,232
buffer_load_dword v17,v247,s[8:11],s35 offen offset:64 sc1
mvaa 0,200,132
ra 8,224,34816
cv 72,80,81
mvaa 0,208,136
ra 12,224,35840
cv 73,82,83
mvaa 0,216,140
ds 249,8,77,2048
cvaa 4,220,140,232
ra 16,224,36864
cv 74,84,85
mvaa 4,212,136
ra 20,224,37888
v_max3_f32 v172,v168,v169,v172
mvaa 4,204,132
ds 249,8,77,3072
mvaa 4,196,128
s_mov_b32 m0,s74
ra 24,224,38912
v_max_f32_e32 v172,v172,v173
cvaa 32,224,128,232
ra 28,224,39936
v_max3_f32 v174,v170,v171,v174
mvaa 32,232,132
buffer_load_dword v19,v247,s[8:11],s35 offen offset:192 sc1
mvaa 32,240,136
ra 32,224,40960
v_max_f32_e32 v174,v174,v175
mvaa 32,248,140
ra 36,224,41984
ra 40,224,43008
cvaa 36,252,140,232
buffer_load_dword v18,v247,s[8:11],s35 offen offset:128 sc1
ra 44,224,44032
mvaa 36,244,136
ra 48,224,45056
v_max_f32_e32 v148,v172,v174
mvaa 36,236,132
ra 56,224,47104
ra 60,224,48128
mvaa 36,228,128
buffer_load_dword v16,v247,s[8:11],s35 offen offset:0 sc1
ra 52,224,46080
cvaa 8,192,144,232
v_mov_b32_e32 v149,v148
cv 75,86,87
mvaa 8,200,148
v_permlane16_swap_b32_e32 v148,v149
mvaa 8,208,152
ds 255,8,43,0
mvaa 8,216,156
v_max_f32_e32 v148,v148,v149
v_mov_b32_e32 v149,v148
cvaa 12,220,156,232
cv 98,104,105
v_max3_f32 v180,v176,v177,v180
mvaa 12,212,152
ds 255,8,43,1024
mvaa 12,204,148
v_permlane32_swap_b32_e32 v148,v149
mvaa 12,196,144
v_max_f32_e32 v148,v148,v149
s_waitcnt lgkmcnt(0)
s_barrier
cvaa 40,224,144,232
ds 255,8,43,2048
mvaa 40,232,148
ra 64,225,0
v_mul_f32_e32 v148,0xbe8293ee,v148
mvaa 40,240,152
ra 68,225,1024
v_fmamk_f32 v172,v172,0x3e8293ee,v148
mvaa 40,248,156
ds 255,8,43,3072
cvaa 44,252,156,232
s_mov_b32 m0,s70
ra 72,225,2048
v_fmamk_f32 v174,v174,0x3e8293ee,v148
mvaa 44,244,152
ra 76,225,3072
v_max3_f32 v182,v178,v179,v182
mvaa 44,236,148
ds 249,8,78,0
mvaa 44,228,144
ra 80,225,4096
v_max_f32_e32 v180,v180,v181
cvaa 24,192,176,128
ra 84,225,5120
v_max_f32_e32 v182,v182,v183
mvaa 24,200,180
ds 249,8,78,1024
mvaa 24,208,184
ra 88,225,6144
v_max_f32_e32 v152,v180,v182
mvaa 24,216,188
ra 92,225,7168
v_mov_b32_e32 v153,v152
cvaa 28,220,188,132
ds 249,8,78,2048
mvaa 28,212,184
ra 96,225,8192
v_max3_f32 v188,v184,v185,v188
mvaa 28,204,180
ra 100,225,9216
ra 104,225,10240
mvaa 28,196,176
ds 249,8,78,3072
ra 108,225,11264
cvaa 56,224,176,136
s_mov_b32 m0,s71
ra 112,225,12288
v_max3_f32 v190,v186,v187,v190
mvaa 56,232,180
ra 116,225,13312
ra 120,225,14336
mvaa 56,240,184
ds 249,8,79,0
ra 124,225,15360
mvaa 56,248,188
ex 172
cvaa 60,252,188,140
ex 174
mvaa 60,244,184
ds 249,8,79,1024
mvaa 60,236,180
v_permlane16_swap_b32_e32 v152,v153
mvaa 60,228,176
cv 172,172,172
cv 174,174,174
cvaa 80,0,160,128
ds 249,8,79,2048
mvaa 80,8,164
v_and_b32_e32 v168,v226,v172
v_max_f32_e32 v152,v152,v153
mvaa 80,16,168
v_and_b32_e32 v169,v227,v174
v_and_b32_e32 v170,v250,v172
v_and_b32_e32 v171,v252,v174
mvaa 80,24,172
ds 249,8,79,3072
v_mov_b32_e32 v153,v152
cvaa 84,28,172,132
v_max_f32_e32 v188,v188,v189
v_permlane32_swap_b32_e32 v152,v153
v_max_f32_e32 v152,v152,v153
mvaa 84,20,168
v_mul_f32_e32 v152,0xbe8293ee,v152
v_fmamk_f32 v180,v180,0x3e8293ee,v152
mvaa 84,12,164
v_fmamk_f32 v182,v182,0x3e8293ee,v152
v_max_f32_e32 v190,v190,v191
mvaa 84,4,160
ex 180
cvaa 112,32,160,136
ex 182
v_max_f32_e32 v156,v188,v190
mvaa 112,40,164
v_mov_b32_e32 v157,v156
cv 180,180,180
mvaa 112,48,168
v_permlane16_swap_b32_e32 v156,v157
mvaa 112,56,172
cv 182,182,182
v_and_b32_e32 v176,v226,v180
v_max_f32_e32 v156,v156,v157
s_waitcnt vmcnt(15) lgkmcnt(0)
s_barrier
s_mov_b32 m0,s72
cvaa 116,60,172,140
ds 255,8,76,0
v_and_b32_e32 v177,v227,v182
mvaa 116,52,168
v_and_b32_e32 v178,v250,v180
v_and_b32_e32 v179,v252,v182
v_mov_b32_e32 v157,v156
mvaa 116,44,164
cv 99,106,107
v_permlane32_swap_b32_e32 v156,v157
v_max_f32_e32 v156,v156,v157
mvaa 116,36,160
ds 255,8,76,1024
v_mul_f32_e32 v156,0xbe8293ee,v156
zvvv 148,228,168
v_fmamk_f32 v188,v188,0x3e8293ee,v156
ex 188
zvvv 200,70,168
v_fmamk_f32 v190,v190,0x3e8293ee,v156
ex 190
zvvv 204,94,168
ds 255,8,76,2048
zvvv 152,228,176
cv 188,188,188
cv 190,190,190
v_and_b32_e32 v184,v226,v188
zvvv 208,72,176
v_and_b32_e32 v185,v227,v190
v_and_b32_e32 v186,v250,v188
v_and_b32_e32 v187,v252,v190
zvvv 212,96,176
ds 255,8,76,3072
s_mov_b32 m0,s73
v_mov_b32_e32 v77,v89
zvvv 156,228,184
v_mov_b32_e32 v100,v108
v_mov_b32_e32 v101,v109
zvvv 216,74,184
ex 0
zvvv 220,98,184
ds 255,8,77,0
cvaa 64,0,128,232
ex 1
mvaa 64,8,132
ex 2
mvaa 64,16,136
ds 255,8,77,1024
mvaa 64,24,140
ds 255,8,77,2048
v_mov_b32_e32 v170,v70
v_mov_b32_e32 v171,v71
cvaa 68,28,140,232
ex 3
mvaa 68,20,136
ex 4
mvaa 68,12,132
ds 255,8,77,3072
mvaa 68,4,128
ex 5
v_and_b32_e32 v248,2863311530,v248
cvaa 96,32,128,232
ex 6
mvaa 96,40,132
v_sub_f32_e32 v17,1.0,v17
v_rcp_f32_e32 v148,v148
mvaa 96,48,136
v_mul_f32_e32 v148,v148,v17
s_waitcnt vmcnt(21)
v_sub_f32_e32 v18,1.0,v18
v_sub_f32_e32 v19,1.0,v19
mvaa 96,56,140
v_mul_f32_e32 v200,v148,v200
v_mul_f32_e32 v201,v148,v201
v_mul_f32_e32 v202,v148,v202
cvaa 100,60,140,232
v_mul_f32_e32 v203,v148,v203
cv 200,200,201
mvaa 100,52,136
v_mul_f32_e32 v204,v148,v204
v_mul_f32_e32 v205,v148,v205
cv 201,202,203
mvaa 100,44,132
v_mul_f32_e32 v206,v148,v206
v_mul_f32_e32 v207,v148,v207
cv 202,204,205
mvaa 100,36,128
buffer_load_dwordx4 v[176:179],v244,s[8:11],s98 offen sc1
cv 203,206,207
cvaa 72,0,144,232
v_rcp_f32_e32 v152,v152
mvaa 72,8,148
v_mul_f32_e32 v152,v152,v18
v_mul_f32_e32 v208,v152,v208
v_mul_f32_e32 v209,v152,v209
mvaa 72,16,152
buffer_load_dwordx2 v[168:169],v253,s[8:11],s98 offen offset:1024 sc1
v_mul_f32_e32 v210,v152,v210
mvaa 72,24,156
v_mul_f32_e32 v211,v152,v211
cv 208,208,209
cv 209,210,211
cvaa 76,28,156,232
v_mul_f32_e32 v212,v152,v212
v_mul_f32_e32 v213,v152,v213
mvaa 76,20,152
buffer_load_dwordx2 v[172:173],v253,s[8:11],s98 offen offset:1536 sc1
v_mul_f32_e32 v214,v152,v214
mvaa 76,12,148
v_mul_f32_e32 v215,v152,v215
v_rcp_f32_e32 v156,v156
mvaa 76,4,144
cv 210,212,213
cv 211,214,215
v_mul_f32_e32 v156,v156,v19
cvaa 104,32,144,232
buffer_store_dwordx4 v[200:203],v244,s[8:11],s34 offen sc0 sc1
mvaa 104,40,148
v_mul_f32_e32 v216,v156,v216
v_mul_f32_e32 v217,v156,v217
v_mul_f32_e32 v218,v156,v218
mvaa 104,48,152
v_mul_f32_e32 v219,v156,v219
cv 216,216,217
cv 217,218,219
mvaa 104,56,156
buffer_store_dwordx4 v[208:211],v244,s[8:11],s50 offen sc0 sc1
v_mul_f32_e32 v220,v156,v220
cvaa 108,60,156,232
v_mul_f32_e32 v221,v156,v221
v_mul_f32_e32 v222,v156,v222
mvaa 108,52,152
v_mul_f32_e32 v223,v156,v223
cv 218,220,221
cv 219,222,223
mvaa 108,44,148
buffer_store_dwordx4 v[216:219],v244,s[8:11],s51 offen sc0 sc1
v_mov_b32_e32 v160,v120
mvaa 108,36,144
v_mov_b32_e32 v161,v121
ex 7
cvaa 88,0,176,232
ex 32
mvaa 88,8,180
v_mov_b32_e32 v162,v122
ex 33
mvaa 88,16,184
v_mov_b32_e32 v175,v95
ex 34
mvaa 88,24,188
s_cmp_ge_i32 s47,s46
s_cselect_b32 s76,s95,s46
s_cselect_b32 s77,s95,268435456
v_mov_b32_e32 v174,v94
ex 35
cvaa 92,28,188,232
s_add_u32 s77,s77,16384
ex 36
mvaa 92,20,184
v_mov_b32_e32 v163,v123
ex 37
mvaa 92,12,180
cv 0,0,1
ex 38
mvaa 92,4,176
cv 1,2,3
ex 39
cvaa 120,32,176,232
buffer_load_dword v247,v246,s[24:27],s49 offen
mvaa 120,40,180
s_add_u32 s49,s49,32768
cv 2,4,5
ex 8
mvaa 120,48,184
cv 3,6,7
ex 9
mvaa 120,56,188
buffer_load_dword v247,v245,s[36:39],s85 offen
cv 4,32,33
cvaa 124,60,188,232
s_add_u32 s85,s85,131072
ex 10
mvaa 124,52,184
cv 5,34,35
ex 11
mvaa 124,44,180
buffer_load_dword v247,v246,s[24:27],s49 offen
cv 6,36,37
mvaa 124,36,176
s_cmp_lg_u32 s19,0
s_cbranch_scc1 L_j0
s_nop 1
s_lshl_b32 s86,s12,12
s_add_u32 s86,s86,131072
v_add_u32_e32 v236,s86,v244
ra 160,236,0
ra 164,236,1024
ra 168,236,2048
ra 172,236,3072
ra 176,236,16384
ra 180,236,17408
ra 184,236,18432
ra 188,236,19456
v_add_u32_e32 v224,s94,v224
)"
R"(s_waitcnt lgkmcnt(0)
L_j0:
cv 7,38,39
ex 12
zaav 0,64,0
s_add_u32 s49,s49,32768
ex 13
maav 0,96,4
ex 14
v_or_b32_e32 v248,1145324612,v248
zaav 4,68,0
ex 15
v_mov_b64_e32 v[144:145],0
maav 4,100,4
cv 8,8,9
ex 40
zaav 8,72,0
ex 41
maav 8,104,4
cv 9,10,11
ex 42
zaav 12,76,0
cv 10,12,13
ex 43
maav 12,108,4
cv 11,14,15
ex 44
v_smfmac_f32_16x16x64_bf16 v[144:147],v[228:231],v[0:7],v248
ex 45
zaav 16,80,0
cv 12,40,41
ex 46
maav 16,112,4
s_waitcnt vmcnt(8)
v_max3_u32 v180,v176,v177,v178
cv 13,42,43
zaav 20,84,0
s_waitcnt vmcnt(7)
v_max3_u32 v180,v180,v179,v168
s_waitcnt vmcnt(6)
v_max3_u32 v180,v180,v169,v172
v_max_u32_e32 v180,v180,v173
v_cmp_eq_u32_e64 s[40:41],-1,v180
maav 20,116,4
s_cmp_lg_u64 s[40:41],0
s_cbranch_scc1 L_bx
L_bxr:
cv 14,44,45
ex 47
mvvv 160,176,192
zaav 24,88,0
cv 15,46,47
v_sub_f32_e32 v16,1.0,v16
maav 24,120,4
ex 24
zaav 28,92,0
ex 25
maav 28,124,4
v_max3_f32 v164,v160,v161,v164
v_max_f32_e32 v164,v164,v165
v_max3_f32 v166,v162,v163,v166
v_max_f32_e32 v166,v166,v167
v_max_f32_e32 v180,v164,v166
v_mov_b32_e32 v181,v180
zaav 32,64,8
ex 26
v_permlane16_swap_b32_e32 v180,v181
v_max_f32_e32 v180,v180,v181
v_mov_b32_e32 v181,v180
maav 32,96,12
ex 27
v_permlane32_swap_b32_e32 v180,v181
v_max_f32_e32 v180,v180,v181
v_mul_f32_e32 v180,0xbe8293ee,v180
v_fmamk_f32 v164,v164,0x3e8293ee,v180
zaav 36,68,8
ex 164
v_fmamk_f32 v166,v166,0x3e8293ee,v180
ex 166
cv 164,164,164
cv 166,166,166
maav 36,100,12
v_and_b32_e32 v160,v226,v164
v_and_b32_e32 v161,v227,v166
v_and_b32_e32 v162,v250,v164
v_and_b32_e32 v163,v252,v166
zaav 40,72,8
v_mov_b64_e32 v[148:149],0
zvvv 180,228,160
maav 40,104,12
ra 192,224,0
zvvv 184,168,160
zaav 44,76,8
ra 196,224,1024
zvvv 188,172,160
maav 44,108,12
ra 200,224,2048
v_rcp_f32_e32 v180,v180
cv 24,24,25
v_mul_f32_e32 v180,v180,v16
v_mul_f32_e32 v184,v180,v184
v_mul_f32_e32 v185,v180,v185
v_smfmac_f32_16x16x64_bf16 v[148:151],v[228:231],v[8:15],v248
v_mul_f32_e32 v186,v180,v186
v_mul_f32_e32 v187,v180,v187
v_mul_f32_e32 v188,v180,v188
v_mul_f32_e32 v189,v180,v189
v_mul_f32_e32 v190,v180,v190
v_mul_f32_e32 v191,v180,v191
zaav 48,80,8
cv 184,184,185
cv 185,186,187
cv 186,188,189
cv 187,190,191
cv 25,26,27
ex 28
maav 48,112,12
buffer_store_dwordx4 v[184:187],v244,s[8:11],s33 offen sc0 sc1
ex 29
ex 30
cv 26,28,29
zaav 52,84,8
ex 31
ex 56
cv 27,30,31
ex 57
maav 52,116,12
ex 58
ex 59
ex 60
zaav 56,88,8
ex 61
maav 56,120,12
cv 28,56,57
ex 62
zaav 60,92,8
ra 204,224,3072
ra 208,224,4096
ex 63
maav 60,124,12
ra 212,224,5120
cv 29,58,59
cv 30,60,61
cv 31,62,63
zvav 192,64,24
ra 216,224,6144
ra 220,224,7168
ex 64
mvav 192,96,28
ra 224,224,8192
v_mov_b32_e32 v251,-1
ex 65
zvav 196,68,24
ra 228,224,9216
ra 232,224,10240
ex 66
mvav 196,100,28
ra 236,224,11264
v_mov_b64_e32 v[152:153],0
ex 67
zvav 200,72,24
ra 240,224,12288
ra 244,224,13312
v_mov_b64_e32 v[156:157],0
cv 64,64,65
mvav 200,104,28
ra 248,224,14336
ra 252,224,15360
ex 68
zvav 204,76,24
ex 69
ex 70
mvav 204,108,28
cv 65,66,67
ex 71
cv 66,68,69
v_smfmac_f32_16x16x64_bf16 v[156:159],v[228:231],v[24:31],v248
ex 96
ex 97
zvav 208,80,24
ex 98
ex 99
mvav 208,112,28
cv 67,70,71
cv 68,96,97
ex 100
zvav 212,84,24
ex 101
ex 102
mvav 212,116,28
cv 69,98,99
ex 103
cv 70,100,101
zvav 216,88,24
ex 72
ex 73
mvav 216,120,28
ex 74
ex 75
zvav 220,92,24
ex 76
ex 77
mvav 220,124,28
v_mov_b32_e32 v250,-1
v_mov_b32_e32 v252,-1
v_mov_b32_e32 v253,-1
cv 71,102,103
s_mov_b64 exec,0
s_bitcmp1_b32 170,s20
s_cbranch_scc1 L_vbO
s_cmp_eq_u32 s12,0
s_cbranch_scc1 L_vs2
s_cmp_eq_u32 s12,1
s_cbranch_scc1 L_vs3
s_cmp_eq_u32 s12,2
s_cbranch_scc1 L_vs4
s_branch L_vs5
L_vbO:
s_cmp_eq_u32 s12,0
s_cbranch_scc1 L_vs9
s_cmp_eq_u32 s12,1
s_cbranch_scc1 L_vs10
s_cmp_eq_u32 s12,2
s_cbranch_scc1 L_vs11
s_branch L_vs12
L_vr1:
s_mov_b64 exec,-1
s_waitcnt lgkmcnt(0)
s_barrier
cvaa 0,192,128,232
mvaa 0,200,132
s_add_u32 s81,s81,1
ex 78
mvaa 0,208,136
s_add_u32 s44,s44,1024
ex 79
mvaa 0,216,140
cv 72,72,73
cv 73,74,75
cvaa 4,220,140,232
s_mov_b32 m0,s68
ex 104
mvaa 4,212,136
ds 249,8,76,0
mvaa 4,204,132
ex 105
mvaa 4,196,128
ex 106
cvaa 32,224,128,232
ra 64,225,32768
cv 74,76,77
mvaa 32,232,132
ex 107
mvaa 32,240,136
ra 68,225,33792
cv 75,78,79
mvaa 32,248,140
ex 108
cvaa 36,252,140,232
ds 249,8,76,1024
mvaa 36,244,136
ex 109
mvaa 36,236,132
ra 72,225,34816
cv 76,104,105
mvaa 36,228,128
ex 110
cvaa 8,192,144,232
ra 76,225,35840
cv 77,106,107
mvaa 8,200,148
ex 111
mvaa 8,208,152
ex 80
mvaa 8,216,156
ds 249,8,76,2048
cvaa 12,220,156,232
ra 80,225,36864
cv 78,108,109
mvaa 12,212,152
ex 81
mvaa 12,204,148
ra 84,225,37888
cv 79,110,111
mvaa 12,196,144
ex 82
cvaa 40,224,144,232
ex 83
mvaa 40,232,148
ra 88,225,38912
cv 80,80,81
mvaa 40,240,152
ds 249,8,76,3072
mvaa 40,248,156
ex 84
cvaa 44,252,156,232
ra 92,225,39936
cv 81,82,83
mvaa 44,244,152
ex 85
mvaa 44,236,148
ex 86
mvaa 44,228,144
ra 96,225,40960
cv 82,84,85
cvaa 16,192,160,232
ex 87
mvaa 16,200,164
s_mov_b32 m0,s69
ex 112
mvaa 16,208,168
ds 249,8,77,0
mvaa 16,216,172
ra 100,225,41984
cv 83,86,87
cvaa 20,220,172,232
ex 113
mvaa 20,212,168
ra 104,225,43008
cv 84,112,113
mvaa 20,204,164
ex 114
mvaa 20,196,160
ex 115
cvaa 48,224,160,232
ra 108,225,44032
cv 85,114,115
mvaa 48,232,164
ds 249,8,77,1024
mvaa 48,240,168
ex 116
mvaa 48,248,172
ra 112,225,45056
cvaa 52,252,172,232
ex 117
mvaa 52,244,168
ex 118
mvaa 52,236,164
ra 116,225,46080
cv 86,116,117
mvaa 52,228,160
ex 119
cvaa 24,192,176,232
ds 249,8,77,2048
mvaa 24,200,180
ra 120,225,47104
cv 87,118,119
mvaa 24,208,184
ex 88
mvaa 24,216,188
ex 89
cvaa 28,220,188,232
ra 124,225,48128
cv 88,88,89
mvaa 28,212,184
ex 90
mvaa 28,204,180
ex 91
mvaa 28,196,176
ds 249,8,77,3072
cvaa 56,224,176,232
ex 92
mvaa 56,232,180
ex 93
mvaa 56,240,184
ex 94
mvaa 56,248,188
ex 95
cvaa 60,252,188,232
mvaa 60,244,184
ex 120
mvaa 60,236,180
ex 121
mvaa 60,228,176
s_waitcnt vmcnt(8) lgkmcnt(0)
s_barrier
maav 0,64,64
cv 89,90,91
cv 90,92,93
maav 0,96,68
s_mov_b32 m0,s74
ex 122
maav 4,68,64
ds 255,8,78,0
maav 4,100,68
ex 123
maav 8,72,64
ex 124
maav 8,104,68
ex 125
maav 12,76,64
cv 91,94,95
cv 92,120,121
maav 12,108,68
ex 126
v_smfmac_f32_16x16x64_bf16 v[144:147],v[228:231],v[64:71],v248
cv 93,122,123
maav 16,80,64
ex 127
maav 16,112,68
buffer_load_dword v247,v245,s[36:39],s85 offen
maav 20,84,64
cv 94,124,125
cv 95,126,127
maav 20,116,68
s_lshl_b32 s86,s19,3
s_lshr_b32 s87,s18,2
s_add_u32 s86,s86,s87
maav 24,88,64
s_lshl_b32 s86,s86,7
s_add_u32 s86,s86,83898368
v_mov_b32_e32 v238,1
maav 24,120,68
ra 192,224,32768
maav 28,92,64
ds 255,8,78,1024
maav 28,124,68
s_cmp_eq_u32 s12,0
s_cselect_b64 exec,1,0
buffer_atomic_add v238,off,s[8:11],s86 sc1
s_mov_b64 exec,-1
maav 32,64,72
ra 196,224,33792
maav 32,96,76
s_add_u32 s85,s85,131072
maav 36,68,72
ra 200,224,34816
maav 36,100,76
maav 40,72,72
ds 255,8,78,2048
maav 40,104,76
maav 44,76,72
ra 204,224,35840
maav 44,108,76
v_smfmac_f32_16x16x64_bf16 v[148:151],v[228:231],v[72:79],v248
ra 208,224,36864
maav 48,80,72
maav 48,112,76
maav 52,84,72
ds 255,8,78,3072
maav 52,116,76
ra 212,224,37888
maav 56,88,72
ex 0
maav 56,120,76
ra 216,224,38912
maav 60,92,72
ex 1
maav 60,124,76
s_mov_b32 m0,s75
ex 2
zvav 160,64,80
ra 220,224,39936
cv 0,0,1
mvav 160,96,84
L_vs1:
s_cbranch_execz L_vr1
ds 255,8,79,0
zvav 164,68,80
ex 3
mvav 164,100,84
ra 224,224,40960
cv 1,2,3
zvav 168,72,80
ex 4
mvav 168,104,84
ra 228,224,41984
zvav 172,76,80
ds 255,8,79,1024
mvav 172,108,84
s_add_u32 s46,s46,16384
ex 5
v_smfmac_f32_16x16x64_bf16 v[152:155],v[228:231],v[80:87],v248
ra 232,224,43008
zvav 176,80,80
s_add_u32 s47,s47,32768
ex 6
mvav 176,112,84
s_add_u32 s95,s47,32768
ex 7
zvav 180,84,80
ra 236,224,44032
cv 2,4,5
mvav 180,116,84
ex 32
zvav 184,88,80
ds 255,8,79,2048
mvav 184,120,84
ex 33
zvav 188,92,80
ra 240,224,45056
cv 3,6,7
mvav 188,124,84
ex 34
mvav 192,64,88
ra 244,224,46080
cv 4,32,33
mvav 192,96,92
ex 35
mvav 196,68,88
ex 36
mvav 196,100,92
ds 255,8,79,3072
mvav 200,72,88
ra 248,224,47104
cv 5,34,35
mvav 200,104,92
s_cmp_ge_i32 s47,s46
ex 37
mvav 204,76,88
s_cselect_b32 s78,s95,s46
ex 38
mvav 204,108,92
s_cselect_b32 s79,s95,268435456
ex 39
v_smfmac_f32_16x16x64_bf16 v[156:159],v[228:231],v[88:95],v248
ra 252,224,48128
mvav 208,80,88
s_add_u32 s79,s79,16384
ex 8
mvav 208,112,92
ex 9
mvav 212,84,88
ex 10
mvav 212,116,92
ex 11
mvav 216,88,88
cv 6,36,37
cv 7,38,39
mvav 216,120,92
ex 12
mvav 220,92,88
ex 13
mvav 220,124,92
s_waitcnt vmcnt(16) lgkmcnt(0)
s_barrier
cvaa 64,192,128,232
s_bitcmp0_b32 s82,s81
s_cbranch_scc1 L_loop
s_bitcmp1_b32 s15,s81
s_cbranch_scc1 L_short
.p2align 6
L_loop:
mvaa 64,200,132
s_add_u32 s81,s81,1
ex 14
mvaa 64,208,136
s_add_u32 s44,s44,1024
ex 15
mvaa 64,216,140
cv 8,8,9
cv 9,10,11
cvaa 68,220,140,232
s_mov_b32 m0,s70
ex 40
mvaa 68,212,136
ds 249,8,78,0
mvaa 68,204,132
ex 41
mvaa 68,196,128
ex 42
cvaa 96,224,128,232
ra 64,225,0
cv 10,12,13
mvaa 96,232,132
ex 43
mvaa 96,240,136
ra 68,225,1024
cv 11,14,15
mvaa 96,248,140
ex 44
cvaa 100,252,140,232
ds 249,8,78,1024
mvaa 100,244,136
ex 45
mvaa 100,236,132
ra 72,225,2048
cv 12,40,41
mvaa 100,228,128
ex 46
cvaa 72,192,144,232
ra 76,225,3072
cv 13,42,43
mvaa 72,200,148
ex 47
mvaa 72,208,152
ex 16
mvaa 72,216,156
ds 249,8,78,2048
cvaa 76,220,156,232
ra 80,225,4096
cv 14,44,45
mvaa 76,212,152
ex 17
mvaa 76,204,148
ra 84,225,5120
cv 15,46,47
mvaa 76,196,144
ex 18
cvaa 104,224,144,232
ex 19
mvaa 104,232,148
ra 88,225,6144
cv 16,16,17
mvaa 104,240,152
ds 249,8,78,3072
mvaa 104,248,156
ex 20
cvaa 108,252,156,232
ra 92,225,7168
cv 17,18,19
mvaa 108,244,152
ex 21
mvaa 108,236,148
ex 22
mvaa 108,228,144
ra 96,225,8192
cv 18,20,21
cvaa 80,192,160,232
ex 23
mvaa 80,200,164
s_mov_b32 m0,s71
ex 48
mvaa 80,208,168
ds 249,8,79,0
mvaa 80,216,172
ra 100,225,9216
cv 19,22,23
cvaa 84,220,172,232
ex 49
mvaa 84,212,168
ra 104,225,10240
cv 20,48,49
mvaa 84,204,164
ex 50
mvaa 84,196,160
ex 51
cvaa 112,224,160,232
ra 108,225,11264
cv 21,50,51
mvaa 112,232,164
ds 249,8,79,1024
mvaa 112,240,168
ex 52
mvaa 112,248,172
ra 112,225,12288
cvaa 116,252,172,232
ex 53
mvaa 116,244,168
ex 54
mvaa 116,236,164
ra 116,225,13312
cv 22,52,53
mvaa 116,228,160
ex 55
cvaa 88,192,176,232
ds 249,8,79,2048
mvaa 88,200,180
ra 120,225,14336
cv 23,54,55
mvaa 88,208,184
ex 24
mvaa 88,216,188
ex 25
cvaa 92,220,188,232
ra 124,225,15360
cv 24,24,25
mvaa 92,212,184
ex 26
mvaa 92,204,180
ex 27
mvaa 92,196,176
ds 249,8,79,3072
cvaa 120,224,176,232
ex 28
mvaa 120,232,180
ex 29
mvaa 120,240,184
ex 30
mvaa 120,248,188
ex 31
cvaa 124,252,188,232
buffer_store_dwordx4 v[250:253],v244,s[8:11],s44 offen sc0 sc1
mvaa 124,244,184
ex 56
mvaa 124,236,180
ex 57
mvaa 124,228,176
s_waitcnt vmcnt(16) lgkmcnt(0)
s_barrier
maav 0,64,0
cv 25,26,27
cv 26,28,29
maav 0,96,4
s_mov_b32 m0,s72
ex 58
maav 4,68,0
ds 255,8,76,0
maav 4,100,4
ex 59
maav 8,72,0
ex 60
maav 8,104,4
ex 61
maav 12,76,0
s_cmp_lg_u32 s80,s81
cv 27,30,31
cv 28,56,57
maav 12,108,4
ex 62
v_smfmac_f32_16x16x64_bf16 v[144:147],v[228:231],v[0:7],v248
cv 29,58,59
maav 16,80,0
ex 63
maav 16,112,4
buffer_load_dword v247,v246,s[24:27],s49 offen
maav 20,84,0
cv 30,60,61
cv 31,62,63
maav 20,116,4
maav 24,88,0
s_cbranch_scc1 L_evb_r
v_add_u32_e32 v224,s94,v224
s_lshl_b32 s86,s12,12
s_add_u32 s86,s86,131072
v_add_u32_e32 v236,s86,v244
ra 160,236,0
ra 164,236,1024
ra 168,236,2048
ra 172,236,3072
ra 176,236,16384
ra 180,236,17408
ra 184,236,18432
ra 188,236,19456
L_evb_r:
maav 24,120,4
ra 192,224,0
maav 28,92,0
ds 255,8,76,1024
maav 28,124,4
maav 32,64,8
ra 196,224,1024
maav 32,96,12
s_add_u32 s49,s49,32768
maav 36,68,8
ra 200,224,2048
maav 36,100,12
maav 40,72,8
ds 255,8,76,2048
maav 40,104,12
maav 44,76,8
ra 204,224,3072
maav 44,108,12
v_smfmac_f32_16x16x64_bf16 v[148:151],v[228:231],v[8:15],v248
ra 208,224,4096
maav 48,80,8
maav 48,112,12
maav 52,84,8
ds 255,8,76,3072
maav 52,116,12
ra 212,224,5120
maav 56,88,8
ex 64
maav 56,120,12
ra 216,224,6144
maav 60,92,8
ex 65
maav 60,124,12
s_mov_b32 m0,s73
ex 66
mvav 160,64,16
ra 220,224,7168
cv 64,64,65
mvav 160,96,20
ds 255,8,77,0
mvav 164,68,16
ex 67
mvav 164,100,20
ra 224,224,8192
cv 65,66,67
mvav 168,72,16
ex 68
mvav 168,104,20
ra 228,224,9216
mvav 172,76,16
ds 255,8,77,1024
mvav 172,108,20
s_add_u32 s46,s46,16384
ex 69
v_smfmac_f32_16x16x64_bf16 v[152:155],v[228:231],v[16:23],v248
ra 232,224,10240
mvav 176,80,16
s_add_u32 s47,s47,32768
ex 70
mvav 176,112,20
s_add_u32 s95,s47,32768
ex 71
mvav 180,84,16
ra 236,224,11264
cv 66,68,69
mvav 180,116,20
ex 96
mvav 184,88,16
ds 255,8,77,2048
mvav 184,120,20
ex 97
mvav 188,92,16
ra 240,224,12288
cv 67,70,71
mvav 188,124,20
ex 98
mvav 192,64,24
ra 244,224,13312
cv 68,96,97
mvav 192,96,28
ex 99
mvav 196,68,24
ex 100
mvav 196,100,28
ds 255,8,77,3072
mvav 200,72,24
ra 248,224,14336
cv 69,98,99
mvav 200,104,28
s_cmp_ge_i32 s47,s46
ex 101
mvav 204,76,24
s_cselect_b32 s76,s95,s46
ex 102
mvav 204,108,28
s_cselect_b32 s77,s95,268435456
ex 103
v_smfmac_f32_16x16x64_bf16 v[156:159],v[228:231],v[24:31],v248
ra 252,224,15360
mvav 208,80,24
s_add_u32 s77,s77,16384
ex 72
mvav 208,112,28
ex 73
mvav 212,84,24
ex 74
mvav 212,116,28
ex 75
mvav 216,88,24
cv 70,100,101
cv 71,102,103
mvav 216,120,28
ex 76
mvav 220,92,24
ex 77
mvav 220,124,28
s_waitcnt vmcnt(16) lgkmcnt(0)
s_barrier
cvaa 0,192,128,232
mvaa 0,200,132
s_add_u32 s81,s81,1
ex 78
mvaa 0,208,136
s_add_u32 s44,s44,1024
ex 79
mvaa 0,216,140
cv 72,72,73
cv 73,74,75
cvaa 4,220,140,232
s_mov_b32 m0,s68
ex 104
mvaa 4,212,136
ds 249,8,76,0
mvaa 4,204,132
ex 105
mvaa 4,196,128
ex 106
cvaa 32,224,128,232
ra 64,225,32768
cv 74,76,77
mvaa 32,232,132
ex 107
mvaa 32,240,136
ra 68,225,33792
cv 75,78,79
mvaa 32,248,140
ex 108
cvaa 36,252,140,232
ds 249,8,76,1024
mvaa 36,244,136
ex 109
mvaa 36,236,132
ra 72,225,34816
cv 76,104,105
mvaa 36,228,128
ex 110
cvaa 8,192,144,232
ra 76,225,35840
cv 77,106,107
mvaa 8,200,148
ex 111
mvaa 8,208,152
ex 80
mvaa 8,216,156
ds 249,8,76,2048
cvaa 12,220,156,232
ra 80,225,36864
cv 78,108,109
mvaa 12,212,152
ex 81
mvaa 12,204,148
ra 84,225,37888
cv 79,110,111
mvaa 12,196,144
ex 82
cvaa 40,224,144,232
ex 83
mvaa 40,232,148
ra 88,225,38912
cv 80,80,81
mvaa 40,240,152
ds 249,8,76,3072
mvaa 40,248,156
ex 84
cvaa 44,252,156,232
ra 92,225,39936
cv 81,82,83
mvaa 44,244,152
ex 85
mvaa 44,236,148
ex 86
mvaa 44,228,144
ra 96,225,40960
cv 82,84,85
cvaa 16,192,160,232
ex 87
mvaa 16,200,164
s_mov_b32 m0,s69
ex 112
mvaa 16,208,168
ds 249,8,77,0
mvaa 16,216,172
ra 100,225,41984
cv 83,86,87
cvaa 20,220,172,232
ex 113
mvaa 20,212,168
ra 104,225,43008
cv 84,112,113
mvaa 20,204,164
ex 114
mvaa 20,196,160
ex 115
cvaa 48,224,160,232
ra 108,225,44032
cv 85,114,115
mvaa 48,232,164
ds 249,8,77,1024
mvaa 48,240,168
ex 116
mvaa 48,248,172
ra 112,225,45056
cvaa 52,252,172,232
ex 117
mvaa 52,244,168
ex 118
mvaa 52,236,164
ra 116,225,46080
cv 86,116,117
mvaa 52,228,160
ex 119
cvaa 24,192,176,232
ds 249,8,77,2048
mvaa 24,200,180
ra 120,225,47104
cv 87,118,119
mvaa 24,208,184
ex 88
mvaa 24,216,188
ex 89
cvaa 28,220,188,232
ra 124,225,48128
cv 88,88,89
mvaa 28,212,184
ex 90
mvaa 28,204,180
ex 91
mvaa 28,196,176
ds 249,8,77,3072
cvaa 56,224,176,232
ex 92
mvaa 56,232,180
ex 93
mvaa 56,240,184
ex 94
mvaa 56,248,188
ex 95
cvaa 60,252,188,232
buffer_store_dwordx4 v[250:253],v244,s[8:11],s44 offen sc0 sc1
mvaa 60,244,184
ex 120
mvaa 60,236,180
ex 121
mvaa 60,228,176
s_waitcnt vmcnt(16) lgkmcnt(0)
)"
R"(s_barrier
maav 0,64,64
cv 89,90,91
cv 90,92,93
maav 0,96,68
s_mov_b32 m0,s74
ex 122
maav 4,68,64
ds 255,8,78,0
maav 4,100,68
ex 123
maav 8,72,64
ex 124
maav 8,104,68
ex 125
maav 12,76,64
cv 91,94,95
cv 92,120,121
maav 12,108,68
ex 126
v_smfmac_f32_16x16x64_bf16 v[144:147],v[228:231],v[64:71],v248
cv 93,122,123
maav 16,80,64
ex 127
maav 16,112,68
buffer_load_dword v247,v245,s[36:39],s85 offen
maav 20,84,64
cv 94,124,125
cv 95,126,127
maav 20,116,68
maav 24,88,64
maav 24,120,68
ra 192,224,32768
maav 28,92,64
ds 255,8,78,1024
maav 28,124,68
maav 32,64,72
ra 196,224,33792
maav 32,96,76
s_add_u32 s85,s85,131072
maav 36,68,72
ra 200,224,34816
maav 36,100,76
maav 40,72,72
ds 255,8,78,2048
maav 40,104,76
maav 44,76,72
ra 204,224,35840
maav 44,108,76
v_smfmac_f32_16x16x64_bf16 v[148:151],v[228:231],v[72:79],v248
ra 208,224,36864
maav 48,80,72
maav 48,112,76
maav 52,84,72
ds 255,8,78,3072
maav 52,116,76
ra 212,224,37888
maav 56,88,72
ex 0
maav 56,120,76
ra 216,224,38912
maav 60,92,72
ex 1
maav 60,124,76
s_mov_b32 m0,s75
ex 2
mvav 160,64,80
ra 220,224,39936
cv 0,0,1
mvav 160,96,84
ds 255,8,79,0
mvav 164,68,80
ex 3
mvav 164,100,84
ra 224,224,40960
cv 1,2,3
mvav 168,72,80
ex 4
mvav 168,104,84
ra 228,224,41984
mvav 172,76,80
ds 255,8,79,1024
mvav 172,108,84
s_add_u32 s46,s46,16384
ex 5
v_smfmac_f32_16x16x64_bf16 v[152:155],v[228:231],v[80:87],v248
ra 232,224,43008
mvav 176,80,80
s_add_u32 s47,s47,32768
ex 6
mvav 176,112,84
s_add_u32 s95,s47,32768
ex 7
mvav 180,84,80
ra 236,224,44032
cv 2,4,5
mvav 180,116,84
ex 32
mvav 184,88,80
ds 255,8,79,2048
mvav 184,120,84
ex 33
mvav 188,92,80
ra 240,224,45056
cv 3,6,7
mvav 188,124,84
ex 34
mvav 192,64,88
ra 244,224,46080
cv 4,32,33
mvav 192,96,92
ex 35
mvav 196,68,88
ex 36
mvav 196,100,92
ds 255,8,79,3072
mvav 200,72,88
ra 248,224,47104
cv 5,34,35
mvav 200,104,92
s_cmp_ge_i32 s47,s46
ex 37
mvav 204,76,88
s_cselect_b32 s78,s95,s46
ex 38
mvav 204,108,92
s_cselect_b32 s79,s95,268435456
ex 39
v_smfmac_f32_16x16x64_bf16 v[156:159],v[228:231],v[88:95],v248
ra 252,224,48128
mvav 208,80,88
s_add_u32 s79,s79,16384
ex 8
mvav 208,112,92
ex 9
mvav 212,84,88
ex 10
mvav 212,116,92
ex 11
mvav 216,88,88
cv 6,36,37
cv 7,38,39
mvav 216,120,92
ex 12
mvav 220,92,88
ex 13
mvav 220,124,92
s_waitcnt vmcnt(16) lgkmcnt(0)
s_barrier
cvaa 64,192,128,232
s_bitcmp0_b32 s82,s81
s_cbranch_scc1 L_loop
s_bitcmp0_b32 s15,s81
s_cbranch_scc1 L_eva_r
L_short:
v_add_f32_e32 v226,v152,v153
v_add_f32_e32 v227,v156,v157
v_subrev_u32_e32 v236,0x23800000,v226
v_subrev_u32_e32 v237,0x23800000,v227
v_max_u32_e32 v236,v236,v237
v_cmp_lt_u32_e64 s[40:41],s48,v236
v_rcp_f32_e32 v226,v226
v_rcp_f32_e32 v227,v227
s_add_u32 s86,s13,4096
v_mov_b32_e32 v236,0
v_mov_b32_e32 v237,0
v_mov_b32_e32 v238,0
v_mov_b32_e32 v239,0
v_mov_b64_e32 v[152:153],0
v_mov_b64_e32 v[156:157],0
v_mul_f32_e32 v226,v242,v226
v_mul_f32_e32 v227,v243,v227
v_add_u32_e32 v225,s94,v225
v_pk_mul_f32 v[160:161],v[160:161],v[226:227] op_sel:[0,0] op_sel_hi:[1,0]
v_pk_mul_f32 v[162:163],v[162:163],v[226:227] op_sel:[0,0] op_sel_hi:[1,0]
v_pk_mul_f32 v[164:165],v[164:165],v[226:227] op_sel:[0,0] op_sel_hi:[1,0]
v_pk_mul_f32 v[166:167],v[166:167],v[226:227] op_sel:[0,0] op_sel_hi:[1,0]
cv 160,160,161
cv 161,162,163
cv 162,164,165
cv 163,166,167
buffer_store_dwordx4 v[160:163],v244,s[8:11],s13 offen offset:0 sc0 sc1
v_pk_mul_f32 v[168:169],v[168:169],v[226:227] op_sel:[0,0] op_sel_hi:[1,0]
v_pk_mul_f32 v[170:171],v[170:171],v[226:227] op_sel:[0,0] op_sel_hi:[1,0]
v_pk_mul_f32 v[172:173],v[172:173],v[226:227] op_sel:[0,0] op_sel_hi:[1,0]
v_pk_mul_f32 v[174:175],v[174:175],v[226:227] op_sel:[0,0] op_sel_hi:[1,0]
cv 168,168,169
cv 169,170,171
cv 170,172,173
cv 171,174,175
buffer_store_dwordx4 v[168:171],v244,s[8:11],s13 offen offset:2048 sc0 sc1
v_pk_mul_f32 v[176:177],v[176:177],v[226:227] op_sel:[0,0] op_sel_hi:[1,0]
v_pk_mul_f32 v[178:179],v[178:179],v[226:227] op_sel:[0,0] op_sel_hi:[1,0]
v_pk_mul_f32 v[180:181],v[180:181],v[226:227] op_sel:[0,0] op_sel_hi:[1,0]
v_pk_mul_f32 v[182:183],v[182:183],v[226:227] op_sel:[0,0] op_sel_hi:[1,0]
cv 176,176,177
cv 177,178,179
cv 178,180,181
v_mfma_f32_32x32x16_bf16 v[160:175],v[236:239],v[236:239],0
cv 179,182,183
buffer_store_dwordx4 v[176:179],v244,s[8:11],s86 offen offset:0 sc0 sc1
v_pk_mul_f32 v[184:185],v[184:185],v[226:227] op_sel:[0,0] op_sel_hi:[1,0]
v_pk_mul_f32 v[186:187],v[186:187],v[226:227] op_sel:[0,0] op_sel_hi:[1,0]
v_pk_mul_f32 v[188:189],v[188:189],v[226:227] op_sel:[0,0] op_sel_hi:[1,0]
v_pk_mul_f32 v[190:191],v[190:191],v[226:227] op_sel:[0,0] op_sel_hi:[1,0]
cv 184,184,185
cv 185,186,187
cv 186,188,189
cv 187,190,191
buffer_store_dwordx4 v[184:187],v244,s[8:11],s86 offen offset:2048 sc0 sc1
v_pk_mul_f32 v[192:193],v[192:193],v[226:227] op_sel:[0,1] op_sel_hi:[1,1]
v_pk_mul_f32 v[194:195],v[194:195],v[226:227] op_sel:[0,1] op_sel_hi:[1,1]
v_pk_mul_f32 v[196:197],v[196:197],v[226:227] op_sel:[0,1] op_sel_hi:[1,1]
v_pk_mul_f32 v[198:199],v[198:199],v[226:227] op_sel:[0,1] op_sel_hi:[1,1]
cv 192,192,193
cv 193,194,195
cv 194,196,197
v_mfma_f32_32x32x16_bf16 v[176:191],v[236:239],v[236:239],0
cv 195,198,199
buffer_store_dwordx4 v[192:195],v244,s[8:11],s13 offen offset:1024 sc0 sc1
v_pk_mul_f32 v[200:201],v[200:201],v[226:227] op_sel:[0,1] op_sel_hi:[1,1]
v_pk_mul_f32 v[202:203],v[202:203],v[226:227] op_sel:[0,1] op_sel_hi:[1,1]
v_pk_mul_f32 v[204:205],v[204:205],v[226:227] op_sel:[0,1] op_sel_hi:[1,1]
v_pk_mul_f32 v[206:207],v[206:207],v[226:227] op_sel:[0,1] op_sel_hi:[1,1]
cv 200,200,201
cv 201,202,203
cv 202,204,205
cv 203,206,207
buffer_store_dwordx4 v[200:203],v244,s[8:11],s13 offen offset:3072 sc0 sc1
v_pk_mul_f32 v[208:209],v[208:209],v[226:227] op_sel:[0,1] op_sel_hi:[1,1]
v_pk_mul_f32 v[210:211],v[210:211],v[226:227] op_sel:[0,1] op_sel_hi:[1,1]
v_pk_mul_f32 v[212:213],v[212:213],v[226:227] op_sel:[0,1] op_sel_hi:[1,1]
v_pk_mul_f32 v[214:215],v[214:215],v[226:227] op_sel:[0,1] op_sel_hi:[1,1]
cv 208,208,209
cv 209,210,211
cv 210,212,213
v_mfma_f32_32x32x16_bf16 v[192:207],v[236:239],v[236:239],0
cv 211,214,215
buffer_store_dwordx4 v[208:211],v244,s[8:11],s86 offen offset:1024 sc0 sc1
v_pk_mul_f32 v[216:217],v[216:217],v[226:227] op_sel:[0,1] op_sel_hi:[1,1]
v_pk_mul_f32 v[218:219],v[218:219],v[226:227] op_sel:[0,1] op_sel_hi:[1,1]
v_pk_mul_f32 v[220:221],v[220:221],v[226:227] op_sel:[0,1] op_sel_hi:[1,1]
v_pk_mul_f32 v[222:223],v[222:223],v[226:227] op_sel:[0,1] op_sel_hi:[1,1]
cv 216,216,217
cv 217,218,219
cv 218,220,221
cv 219,222,223
buffer_store_dwordx4 v[216:219],v244,s[8:11],s86 offen offset:3072 sc0 sc1
s_nop 1
v_mfma_f32_32x32x16_bf16 v[208:223],v[236:239],v[236:239],0
L_eva_r:
s_cmp_lg_u32 s81,14
s_cbranch_scc1 L_loop
s_add_u32 s81,s81,1
s_bitcmp1_b32 170,s20
s_cbranch_scc1 L_odd
L_last:
s_lshl_b32 s86,s21,2
s_sub_u32 s87,s13,s67
s_add_u32 s86,s86,s12
mvaa 64,200,132
s_lshl_b32 s86,s86,8
v_add_u32_e32 v252,s87,v244
s_add_u32 s86,s86,L_last-L_att_pc+1024
v_lshl_add_u32 v226,v244,3,s86
s_lshl_b32 s86,s12,1
s_bfe_u32 s87,s21,65539
mvaa 64,208,136
s_add_u32 s86,s86,s87
v_min_u32_e32 v226,L_att_end-L_att_pc-4,v226
s_lshl_b32 s86,s86,7
s_add_u32 s86,s86,L_pend2-L_att_pc
v_writelane_b32 v226,s86,2
mvaa 64,216,140
s_mov_b64 exec,7
global_load_dword v253,v226,s[96:97]
s_mov_b64 exec,-1
cvaa 68,220,140,232
s_add_u32 m0,s52,32768
v_and_b32_e32 v224,16,v244
v_lshlrev_b32_e32 v224,5,v224
mvaa 68,212,136
s_xor_b32 s51,s62,0
v_lshrrev_b32_e32 v239,4,v244
v_lshrrev_b32_e32 v236,3,v239
mvaa 68,204,132
s_lshl_b32 s87,s12,13
v_and_b32_e32 v237,112,v244
v_and_b32_e32 v238,48,v239
mvaa 68,196,128
s_xor_b32 s87,s87,8192
ra 64,225,0
v_lshlrev_b32_e32 v238,1,v238
cvaa 96,224,128,232
buffer_load_dword v253,v224,s[8:11],s53 offen sc1
mvaa 96,232,132
s_mov_b32 s84,6
ra 68,225,1024
v_xor_b32_e32 v237,v237,v238
mvaa 96,240,136
s_add_u32 s81,s81,1
ra 72,225,2048
v_lshl_add_u32 v245,v236,11,v237
mvaa 96,248,140
s_mov_b32 s84,4
v_add_u32_e32 v246,15360,v245
v_add_u32_e32 v249,30720,v245
cvaa 100,252,140,232
s_andn2_b32 s69,64,s54
ra 76,225,3072
s_waitcnt vmcnt(9)
v_add_u32_e32 v247,46080,v245
mvaa 100,244,136
dl 245,24,51,0
mvaa 100,236,132
s_add_u32 s88,s14,4096
ra 80,225,4096
cv 8,8,9
mvaa 100,228,128
s_lshl_b32 s35,s19,1
ra 84,225,5120
cv 9,10,11
cvaa 72,192,144,232
s_lshr_b32 s33,40569,s35
ra 88,225,6144
cv 10,12,13
mvaa 72,200,148
s_and_b32 s33,s33,3
ra 92,225,7168
v_add_u32_e32 v250,4096,v252
mvaa 72,208,152
dl 246,24,51,1024
mvaa 72,216,156
s_lshl_b32 s33,s33,13
ra 96,225,8192
v_mov_b32_e32 v251,4
cvaa 76,220,156,232
s_lshr_b32 s34,59294,s35
ra 100,225,9216
s_and_b32 s34,s34,3
mvaa 76,212,152
s_lshl_b32 s34,s34,13
ra 104,225,10240
s_xor_b32 s35,s33,s34
mvaa 76,204,148
s_and_b32 s98,s19,3
ra 108,225,11264
s_lshl_b32 s98,s98,13
mvaa 76,196,144
dl 249,24,51,2048
cvaa 104,224,144,232
s_mov_b32 s74,0x300003
ra 112,225,12288
s_mov_b32 s75,0x30000300
mvaa 104,232,148
s_lshl_b32 s71,s12,15
ra 116,225,13312
s_add_u32 s71,s71,s54
mvaa 104,240,152
s_mov_b64 s[44:45],s[30:31]
ra 120,225,14336
s_mov_b32 s46,4194304
mvaa 104,248,156
s_mov_b32 s47,131072
ex 14
cvaa 108,252,156,232
dl 247,24,51,3072
mvaa 108,244,152
s_add_u32 m0,s52,49152
s_xor_b32 s51,s63,0
ra 124,225,15360
mvaa 108,236,148
s_lshl_b32 s70,s23,11
ex 15
mvaa 108,228,144
s_add_u32 s70,s70,s71
ex 40
cvaa 80,192,160,232
ex 41
mvaa 80,200,164
dl 245,24,51,0
mvaa 80,208,168
ex 42
mvaa 80,216,172
ex 43
cvaa 84,220,172,232
ex 44
mvaa 84,212,168
ex 45
mvaa 84,204,164
dl 246,24,51,1024
mvaa 84,196,160
ex 46
cvaa 112,224,160,232
ex 47
mvaa 112,232,164
ex 16
mvaa 112,240,168
ex 17
mvaa 112,248,172
dl 249,24,51,2048
cvaa 116,252,172,232
ex 18
mvaa 116,244,168
ex 19
mvaa 116,236,164
ex 20
mvaa 116,228,160
ex 21
cvaa 88,192,176,232
dl 247,24,51,3072
s_waitcnt vmcnt(8)
v_cmp_ne_u32_e32 vcc,4,v253
mvaa 88,200,180
s_cbranch_vccnz L_spbe
L_sprbe:
ex 22
mvaa 88,208,184
s_xor_b32 s50,s66,262144
ex 23
mvaa 88,216,188
ex 48
cvaa 92,220,188,232
ex 49
mvaa 92,212,184
la 128,252,50,0
mvaa 92,204,180
s_waitcnt lgkmcnt(0)
s_barrier
mvaa 92,196,176
s_mov_b32 m0,s72
ex 50
cvaa 120,224,176,232
s_lshl_b32 s72,s22,11
ex 51
mvaa 120,232,180
s_add_u32 s72,s72,s71
ex 52
mvaa 120,240,184
ds 255,8,76,0
mvaa 120,248,188
s_add_u32 s71,s70,131072
ex 53
cvaa 124,252,188,232
ex 54
mvaa 124,244,184
ex 55
mvaa 124,236,180
ex 24
mvaa 124,228,176
ds 255,8,76,1024
maav 0,64,0
s_cmp_lg_u32 s19,7
s_cbranch_scc1 L_j7e
s_nop 1
s_lshl_b32 s86,s12,12
s_add_u32 s86,s86,131072
v_add_u32_e32 v236,s86,v244
ra 160,236,0
ra 164,236,1024
ra 168,236,2048
ra 172,236,3072
ra 176,236,16384
ra 180,236,17408
ra 184,236,18432
ra 188,236,19456
v_add_u32_e32 v224,s94,v224
s_waitcnt lgkmcnt(0)
L_j7e:
ex 25
maav 0,96,4
ex 26
maav 4,68,0
ex 27
maav 4,100,4
cv 11,14,15
cv 12,40,41
maav 8,72,0
ds 255,8,76,2048
maav 8,104,4
cv 13,42,43
cv 14,44,45
maav 12,76,0
cv 15,46,47
cv 16,16,17
maav 12,108,4
ex 28
v_smfmac_f32_16x16x64_bf16 v[144:147],v[228:231],v[0:7],v248
cv 17,18,19
maav 16,80,0
ds 255,8,76,3072
maav 16,112,4
s_mov_b32 m0,s73
ex 29
maav 20,84,0
s_add_u32 s73,s72,131072
ex 30
maav 20,116,4
ex 31
maav 24,88,0
ex 56
maav 24,120,4
ds 255,8,77,0
maav 28,92,0
ex 57
maav 28,124,4
ex 58
maav 32,64,8
ex 59
maav 32,96,12
ex 60
maav 36,68,8
ds 255,8,77,1024
maav 36,100,12
cv 18,20,21
cv 19,22,23
maav 40,72,8
cv 20,48,49
cv 21,50,51
maav 40,104,12
cv 22,52,53
cv 23,54,55
maav 44,76,8
ex 61
maav 44,108,12
ds 255,8,77,2048
v_smfmac_f32_16x16x64_bf16 v[148:151],v[228:231],v[8:15],v248
cv 24,24,25
maav 48,80,8
ex 62
maav 48,112,12
ex 63
maav 52,84,8
ra 192,244,0
cv 25,26,27
maav 52,116,12
ds 255,8,77,3072
maav 56,88,8
cv 26,28,29
cv 27,30,31
maav 56,120,12
cv 28,56,57
cv 29,58,59
maav 60,92,8
cv 30,60,61
cv 31,62,63
maav 60,124,12
ex 64
mvav 160,64,16
la 132,252,50,2048
mvav 160,96,20
ex 65
mvav 164,68,16
ex 66
mvav 164,100,20
ex 67
mvav 168,72,16
ex 68
mvav 168,104,20
la 136,250,50,0
mvav 172,76,16
ex 69
mvav 172,108,20
ex 70
v_smfmac_f32_16x16x64_bf16 v[152:155],v[228:231],v[16:23],v248
ra 200,244,2048
mvav 176,80,16
ra 208,244,4096
cv 64,64,65
mvav 176,112,20
la 140,250,50,2048
mvav 180,84,16
ra 216,244,6144
cv 65,66,67
mvav 180,116,20
ex 71
mvav 184,88,16
ex 96
mvav 184,120,20
s_barrier
ex 97
mvav 188,92,16
la 156,250,50,3072
mvav 188,124,20
s_add_u32 m0,s52,131072
ra 196,244,1024
s_xor_b32 s51,s62,1048576
mvav 192,64,24
ra 204,244,3072
cv 66,68,69
mvav 192,96,28
ra 212,244,5120
cv 67,70,71
mvav 196,68,24
ra 220,244,7168
cv 68,96,97
mvav 196,100,28
la 152,250,50,1024
mvav 200,72,24
ex 98
mvav 200,104,28
ex 99
mvav 204,76,24
ra 224,244,8192
cv 69,98,99
mvav 204,108,28
ra 232,244,10240
v_add_u32_e32 v6,s87,v244
v_smfmac_f32_16x16x64_bf16 v[156:159],v[228:231],v[24:31],v248
ra 240,244,12288
mvav 208,80,24
la 148,252,50,3072
mvav 208,112,28
s_xor_b32 s87,s52,4096
ra 248,244,14336
v_add_u32_e32 v7,98304,v6
mvav 212,84,24
s_add_u32 s87,s87,150528
ra 252,244,15360
v_add_u32_e32 v5,65536,v244
mvav 212,116,28
ra 244,244,13312
v_add_u32_e32 v32,s87,v244
mvav 216,88,24
ra 228,244,9216
v_and_b32_e32 v224,960,v244
mvav 216,120,28
la 144,252,50,1024
mvav 220,92,24
s_add_u32 s50,s50,s55
ra 236,244,11264
mvav 220,124,28
ra 64,225,32768
s_waitcnt lgkmcnt(8)
cvaa 24,192,176,232
dl 245,24,51,0
mvaa 24,200,180
ra 96,225,40960
mvaa 24,208,184
L_vs2:
s_cbranch_execz L_vs6
ra 68,225,33792
mvaa 24,216,188
dl 246,24,51,1024
cvaa 16,192,160,128
ra 100,225,41984
mvaa 16,200,164
ra 72,225,34816
mvaa 16,208,168
dl 249,24,51,2048
mvaa 16,216,172
ra 104,225,43008
cvaa 28,196,176,232
ra 76,225,35840
mvaa 28,204,180
dl 247,24,51,3072
mvaa 28,212,184
s_add_u32 m0,s52,147456
s_xor_b32 s51,s63,1048576
ra 108,225,44032
mvaa 28,220,188
ra 80,225,36864
cvaa 20,196,160,132
dl 245,24,51,0
mvaa 20,204,164
ra 112,225,45056
mvaa 20,212,168
ra 192,244,16384
mvaa 20,220,172
dl 246,24,51,1024
cvaa 56,224,176,232
ra 200,244,18432
s_waitcnt lgkmcnt(7)
mvaa 56,232,180
ra 208,244,20480
mvaa 56,240,184
dl 249,24,51,2048
mvaa 56,248,188
ra 216,244,22528
cvaa 48,224,160,136
ra 196,244,17408
mvaa 48,232,164
ra 204,244,19456
mvaa 48,240,168
ra 212,244,21504
mvaa 48,248,172
ra 220,244,23552
cvaa 60,228,176,232
ra 224,244,24576
mvaa 60,236,180
ra 232,244,26624
mvaa 60,244,184
ra 240,244,28672
mvaa 60,252,188
ra 248,244,30720
cvaa 52,228,160,140
ra 84,225,37888
mvaa 52,236,164
ra 116,225,46080
mvaa 52,244,168
la 160,252,50,0
ra 228,244,25600
mvaa 52,252,172
ra 236,244,27648
s_waitcnt lgkmcnt(10)
cvaa 8,192,176,128
ra 244,244,29696
mvaa 8,200,180
la 164,252,50,2048
ra 252,244,31744
mvaa 8,208,184
ra 88,225,38912
mvaa 8,216,188
ra 120,225,47104
cvaa 12,196,176,132
la 168,250,50,0
mvaa 12,204,180
ra 92,225,39936
ra 124,225,48128
s_waitcnt lgkmcnt(9)
mvaa 12,212,184
ex 100
mvaa 12,220,188
la 172,250,50,2048
cvaa 40,224,176,136
ex 101
mvaa 40,232,180
ex 102
mvaa 40,240,184
ex 103
mvaa 40,248,188
cv 70,100,101
cv 71,102,103
s_waitcnt lgkmcnt(4)
cvaa 44,228,176,140
ex 72
mvaa 44,236,180
s_waitcnt vmcnt(18) lgkmcnt(0)
s_barrier
ex 73
mvaa 44,244,184
la 176,252,50,1024
mvaa 44,252,188
ex 74
maav 0,64,64
ex 75
maav 0,96,68
la 188,250,50,3072
maav 4,68,64
ra 192,5,0
cv 72,72,73
maav 4,100,68
ra 196,5,1024
cv 73,74,75
maav 8,72,64
la 184,250,50,1024
ds_write_b128 v6,a[0:3] offset:0
maav 8,104,68
ex 76
maav 12,76,64
ex 77
maav 12,108,68
la 180,252,50,3072
ds_write_b128 v6,a[4:7] offset:1024
v_smfmac_f32_16x16x64_bf16 v[144:147],v[228:231],v[64:71],v248
cv 74,76,77
maav 16,80,64
ex 78
maav 16,112,68
ds_write_b128 v6,a[8:11] offset:2048
ex 79
maav 20,84,64
ex 104
maav 20,116,68
ds_write_b128 v6,a[12:15] offset:3072
ex 105
maav 24,88,64
ex 106
ex 107
ex 108
maav 24,120,68
ds_write_b128 v6,a[16:19] offset:4096
cv 75,78,79
maav 28,92,64
ex 109
ex 110
ex 111
maav 28,124,68
cv 76,104,105
cv 77,106,107
cv 78,108,109
cv 79,110,111
maav 32,64,72
ds_write_b128 v6,a[20:23] offset:5120
maav 32,96,76
ex 80
maav 36,68,72
ex 81
maav 36,100,76
ds_write_b128 v6,a[24:27] offset:6144
maav 40,72,72
ex 82
maav 40,104,76
ex 83
maav 44,76,72
ds_write_b128 v6,a[28:31] offset:7168
maav 44,108,76
cv 80,80,81
cv 81,82,83
v_smfmac_f32_16x16x64_bf16 v[148:151],v[228:231],v[72:79],v248
ex 84
maav 48,80,72
ds_write_b128 v7,a[32:35] offset:0
maav 48,112,76
ex 85
ex 86
ex 87
maav 52,84,72
ex 112
ex 113
maav 52,116,76
ds_write_b128 v7,a[36:39] offset:1024
cv 82,84,85
maav 56,88,72
ex 114
ex 115
ex 116
maav 56,120,76
ex 117
ex 118
ex 119
maav 60,92,72
ds_write_b128 v7,a[40:43] offset:2048
cv 83,86,87
maav 60,124,76
cv 84,112,113
cv 85,114,115
cv 86,116,117
cv 87,118,119
mvav 160,64,80
ex 88
mvav 160,96,84
ds_write_b128 v7,a[44:47] offset:3072
mvav 164,68,80
v_add_f32_e32 v130,v144,v145
v_add_f32_e32 v131,v148,v149
mvav 164,100,84
ex 89
mvav 168,72,80
ds_write_b128 v7,a[48:51] offset:4096
mvav 168,104,84
ex 90
mvav 172,76,80
ex 91
mvav 172,108,84
ds_write_b128 v32,v[128:131]
v_smfmac_f32_16x16x64_bf16 v[152:155],v[228:231],v[80:87],v248
ex 92
ex 93
mvav 176,80,80
ds_write_b128 v7,a[52:55] offset:5120
ex 94
mvav 176,112,84
ra 200,5,2048
ra 204,5,3072
ex 95
cv 88,88,89
mvav 180,84,80
ra 208,5,4096
ra 212,5,5120
cv 89,90,91
cv 90,92,93
ex 120
mvav 180,116,84
ds_write_b128 v7,a[56:59] offset:6144
ex 121
mvav 184,88,80
ex 122
ex 123
cv 91,94,95
mvav 184,120,84
ex 124
ex 125
mvav 188,92,80
ds_write_b128 v7,a[60:63] offset:7168
ex 126
mvav 188,124,84
ra 216,5,6144
ra 220,5,7168
ra 224,5,8192
ra 232,5,10240
ex 127
cv 92,120,121
mvav 192,64,88
ra 240,5,12288
ra 248,5,14336
ra 228,5,9216
ra 236,5,11264
ra 244,5,13312
ra 252,5,15360
cv 93,122,123
cv 94,124,125
cv 95,126,127
v_xor_b32_e32 v6,8192,v6
s_nop 0
mvav 192,96,92
ex 16
mvav 196,68,88
s_waitcnt lgkmcnt(10)
s_xor_b32 s87,s87,0
mvav 196,100,92
s_xor_b32 s50,s66,294912
s_barrier
v_xor_b32_e32 v7,8192,v7
mvav 200,72,88
la 4,252,50,0
ra 64,5,16384
ra 96,5,24576
rv 64,6,0
rv 68,6,1024
rv 72,6,2048
mvav 200,104,92
rv 76,6,3072
rv 80,6,4096
rv 84,6,5120
ex 17
ex 18
mvav 204,76,88
rv 96,7,0
rv 100,7,1024
ex 19
mvav 204,108,92
la 12,252,50,2048
rv 104,7,2048
rv 108,7,3072
cv 16,16,17
v_smfmac_f32_16x16x64_bf16 v[156:159],v[228:231],v[88:95],v248
rv 112,7,4096
rv 116,7,5120
ex 20
ex 21
mvav 208,80,88
rv 120,7,6144
rv 124,7,7168
ex 22
ex 23
mvav 208,112,92
la 20,250,50,0
ra 68,5,17408
ra 100,5,25600
cv 17,18,19
cv 18,20,21
mvav 212,84,88
ra 72,5,18432
ra 104,5,26624
ex 48
ex 49
mvav 212,116,92
ra 76,5,19456
ra 108,5,27648
ex 50
ex 51
mvav 216,88,88
la 28,250,50,2048
ra 80,5,20480
ra 112,5,28672
ra 84,5,21504
mvav 216,120,92
ra 116,5,29696
cv 19,22,23
ex 52
ex 53
mvav 220,92,88
ra 88,5,22528
cv 20,48,49
ex 54
ex 55
mvav 220,124,92
la 60,250,50,3072
ra 120,5,30720
cv 21,50,51
cv 22,52,53
cv 23,54,55
s_bitcmp1_b32 s82,s81
s_cbranch_scc1 L_pend2
L_ret_eb4:
mvav 160,192,16
ra 92,5,23552
rv 88,6,6144
ra 124,5,31744
rv 92,6,7168
ex 24
s_waitcnt lgkmcnt(10)
mvav 160,224,20
ex 25
ex 26
v_smfmac_f32_16x16x64_bf16 v[152:155],v[228:231],v[16:23],v248
ex 27
v_xor_b32_e32 v32,4096,v32
mvav 164,196,16
)"
R"(la 52,250,50,1024
ds_read_b128 v[236:239],v32
cv 24,24,25
ex 28
mvav 164,228,20
ex 29
cv 25,26,27
ex 30
ex 31
mvav 168,200,16
ex 56
ex 57
mvav 168,232,20
la 44,252,50,3072
cv 26,28,29
ex 58
ex 59
mvav 172,204,16
ex 60
ex 61
ex 62
mvav 172,236,20
s_waitcnt lgkmcnt(0)
s_barrier
cv 27,30,31
ex 63
mvav 176,208,16
la 36,252,50,1024
cv 28,56,57
cv 29,58,59
cv 30,60,61
cv 31,62,63
mvav 176,240,20
s_add_u32 m0,s52,147456
ex 8
s_xor_b32 s51,s63,1048576
ex 9
ex 10
mvav 180,212,16
s_add_u32 s50,s50,s55
ex 11
cv 8,8,9
cv 9,10,11
ex 12
mvav 180,244,20
dl 247,24,51,3072
ex 13
ex 14
mvav 184,216,16
s_add_u32 m0,s52,0
ex 15
ex 40
ex 41
mvav 184,248,20
s_xor_b32 s51,s64,0
ex 42
ex 43
ex 44
mvav 188,220,16
la 0,252,50,0
ex 45
ex 46
mvav 188,252,20
ex 47
cv 10,12,13
cv 11,14,15
cv 12,40,41
cv 13,42,43
cv 14,44,45
v_smfmac_f32_16x16x64_bf16 v[156:159],v[228:231],v[24:31],v248
cv 15,46,47
v_bfe_u32 v225,v244,1,5
s_nop 0
v_smfmac_f32_16x16x64_bf16 v[156:159],v[228:231],v[8:15],v248
v_lshl_add_u32 v224,v224,1,v225
v_bfe_u32 v225,v244,7,2
v_lshl_add_u32 v224,v225,5,v224
mvav 192,192,24
la 8,252,50,2048
v_xor_b32_e32 v224,s69,v224
v_add_f32_e32 v152,v152,v153
v_add_f32_e32 v137,v152,v238
v_subrev_u32_e32 v236,0x23800000,v137
mvav 192,224,28
v_rcp_f32_e32 v137,v137
mvav 192,64,8
tv 128,224,32768
v_mul_f32_e32 v232,v240,v137
v_mul_f32_e32 v64,v232,v64
v_mul_f32_e32 v65,v232,v65
v_mul_f32_e32 v66,v232,v66
mvav 192,96,12
la 16,250,50,0
v_mul_f32_e32 v67,v232,v67
v_mul_f32_e32 v68,v232,v68
v_mul_f32_e32 v69,v232,v69
v_mul_f32_e32 v70,v232,v70
mvav 196,196,24
v_fmac_f32_e32 v64,v232,v160
v_fmac_f32_e32 v65,v232,v161
v_mul_f32_e32 v71,v232,v71
mvav 196,228,28
v_fmac_f32_e32 v66,v232,v162
v_fmac_f32_e32 v67,v232,v163
v_fmac_f32_e32 v68,v232,v164
v_fmac_f32_e32 v69,v232,v165
v_fmac_f32_e32 v70,v232,v166
mvav 196,68,8
la 24,250,50,2048
v_fmac_f32_e32 v71,v232,v167
cv 160,64,65
cv 161,66,67
cv 162,68,69
cv 163,70,71
mvav 196,100,12
tv 130,224,34816
tv 132,224,36864
mvav 200,200,24
tv 134,224,38912
tv 144,224,40960
v_mul_f32_e32 v72,v232,v72
v_mul_f32_e32 v73,v232,v73
v_mul_f32_e32 v74,v232,v74
mvav 200,232,28
buffer_store_dwordx4 v[160:163],v244,s[8:11],s14 offen offset:0 sc0 sc1
v_mul_f32_e32 v75,v232,v75
v_mul_f32_e32 v76,v232,v76
v_mul_f32_e32 v77,v232,v77
v_mul_f32_e32 v78,v232,v78
mvav 200,72,8
v_fmac_f32_e32 v72,v232,v168
v_fmac_f32_e32 v73,v232,v169
v_mul_f32_e32 v79,v232,v79
mvav 200,104,12
v_fmac_f32_e32 v74,v232,v170
v_fmac_f32_e32 v75,v232,v171
v_fmac_f32_e32 v76,v232,v172
v_fmac_f32_e32 v77,v232,v173
v_fmac_f32_e32 v78,v232,v174
mvav 204,204,24
la 56,250,50,3072
v_fmac_f32_e32 v79,v232,v175
cv 168,72,73
cv 169,74,75
cv 170,76,77
cv 171,78,79
mvav 204,236,28
tv 146,224,43008
tv 148,224,45056
mvav 204,76,8
tv 150,224,47104
v_mul_f32_e32 v80,v232,v80
v_mul_f32_e32 v81,v232,v81
v_mul_f32_e32 v82,v232,v82
mvav 204,108,12
buffer_store_dwordx4 v[168:171],v244,s[8:11],s14 offen offset:2048 sc0 sc1
v_mul_f32_e32 v83,v232,v83
v_mul_f32_e32 v84,v232,v84
v_mul_f32_e32 v85,v232,v85
v_mul_f32_e32 v86,v232,v86
v_mul_f32_e32 v87,v232,v87
mvav 208,208,24
v_fmac_f32_e32 v80,v232,v176
v_fmac_f32_e32 v81,v232,v177
v_fmac_f32_e32 v82,v232,v178
mvav 208,240,28
v_fmac_f32_e32 v83,v232,v179
v_fmac_f32_e32 v84,v232,v180
v_fmac_f32_e32 v85,v232,v181
cv 176,80,81
cv 177,82,83
mvav 208,80,8
la 48,250,50,1024
v_fmac_f32_e32 v86,v232,v182
v_fmac_f32_e32 v87,v232,v183
cv 178,84,85
cv 179,86,87
mvav 208,112,12
v_mul_f32_e32 v88,v232,v88
v_mul_f32_e32 v89,v232,v89
v_mul_f32_e32 v90,v232,v90
mvav 212,212,24
v_mul_f32_e32 v91,v232,v91
v_mul_f32_e32 v92,v232,v92
v_mul_f32_e32 v93,v232,v93
v_mul_f32_e32 v94,v232,v94
v_mul_f32_e32 v95,v232,v95
mvav 212,244,28
buffer_store_dwordx4 v[176:179],v244,s[8:11],s88 offen offset:0 sc0 sc1
v_fmac_f32_e32 v88,v232,v184
v_fmac_f32_e32 v89,v232,v185
v_fmac_f32_e32 v90,v232,v186
v_fmac_f32_e32 v91,v232,v187
cv 184,88,89
mvav 212,84,8
v_fmac_f32_e32 v92,v232,v188
cv 185,90,91
mvav 212,116,12
v_fmac_f32_e32 v93,v232,v189
v_fmac_f32_e32 v94,v232,v190
v_fmac_f32_e32 v95,v232,v191
cv 186,92,93
cv 187,94,95
mvav 216,216,24
la 40,252,50,3072
v_add_f32_e32 v156,v156,v157
v_add_f32_e32 v141,v156,v239
v_subrev_u32_e32 v237,0x23800000,v141
v_rcp_f32_e32 v141,v141
mvav 216,248,28
v_mul_f32_e32 v233,v241,v141
v_mul_f32_e32 v96,v233,v96
mvav 216,88,8
v_mul_f32_e32 v97,v233,v97
v_mul_f32_e32 v98,v233,v98
v_mul_f32_e32 v99,v233,v99
v_mul_f32_e32 v100,v233,v100
mvav 216,120,12
buffer_store_dwordx4 v[184:187],v244,s[8:11],s88 offen offset:2048 sc0 sc1
v_fmac_f32_e32 v96,v233,v192
v_fmac_f32_e32 v97,v233,v193
v_mul_f32_e32 v101,v233,v101
v_mul_f32_e32 v102,v233,v102
v_mul_f32_e32 v103,v233,v103
mvav 220,220,24
v_fmac_f32_e32 v98,v233,v194
v_fmac_f32_e32 v99,v233,v195
mvav 220,252,28
v_fmac_f32_e32 v100,v233,v196
v_fmac_f32_e32 v101,v233,v197
cv 192,96,97
cv 193,98,99
mvav 220,92,8
la 32,252,50,1024
v_fmac_f32_e32 v102,v233,v198
v_fmac_f32_e32 v103,v233,v199
cv 194,100,101
cv 195,102,103
mvav 220,124,12
v_xor_b32_e32 v225,32,v224
v_xor_b32_e32 v226,64,v224
s_waitcnt lgkmcnt(0)
zvva 0,128,128
tv 138,225,34816
tv 142,225,38912
tv 152,225,40960
s_waitcnt vmcnt(36)
mvva 0,132,132
tv 136,225,32768
tv 154,225,43008
mvva 0,144,136
tv 140,225,36864
tv 158,225,47104
mvva 0,148,140
tv 156,225,45056
v_mul_f32_e32 v104,v233,v104
v_mul_f32_e32 v105,v233,v105
v_mul_f32_e32 v106,v233,v106
zvva 4,148,156
buffer_store_dwordx4 v[192:195],v244,s[8:11],s14 offen offset:1024 sc0 sc1
v_mul_f32_e32 v107,v233,v107
v_mul_f32_e32 v108,v233,v108
v_mul_f32_e32 v109,v233,v109
v_mul_f32_e32 v110,v233,v110
mvva 4,144,152
L_vs3:
s_cbranch_execz L_vs7
v_fmac_f32_e32 v104,v233,v200
v_fmac_f32_e32 v105,v233,v201
v_fmac_f32_e32 v106,v233,v202
v_mul_f32_e32 v111,v233,v111
mvva 4,132,148
v_fmac_f32_e32 v107,v233,v203
v_fmac_f32_e32 v108,v233,v204
v_fmac_f32_e32 v109,v233,v205
cv 200,104,105
cv 201,106,107
mvva 4,128,144
v_fmac_f32_e32 v110,v233,v206
v_fmac_f32_e32 v111,v233,v207
cv 202,108,109
cv 203,110,111
s_waitcnt vmcnt(16)
zvva 8,128,160
v_cmp_lt_u32_e64 s[42:43],s48,v236
v_mul_f32_e32 v112,v233,v112
s_or_b64 s[40:41],s[40:41],s[42:43]
v_cmp_lt_u32_e64 s[42:43],s48,v237
mvva 8,132,164
s_or_b64 s[40:41],s[40:41],s[42:43]
v_mul_f32_e32 v113,v233,v113
v_mul_f32_e32 v114,v233,v114
v_mul_f32_e32 v115,v233,v115
mvva 8,144,168
dl 245,36,51,0
v_mul_f32_e32 v116,v233,v116
v_mul_f32_e32 v117,v233,v117
v_mul_f32_e32 v118,v233,v118
v_mul_f32_e32 v119,v233,v119
v_mul_f32_e32 v120,v233,v120
mvva 8,148,172
v_fmac_f32_e32 v112,v233,v208
v_fmac_f32_e32 v113,v233,v209
v_fmac_f32_e32 v114,v233,v210
v_fmac_f32_e32 v115,v233,v211
zvva 12,148,188
v_fmac_f32_e32 v116,v233,v212
v_fmac_f32_e32 v117,v233,v213
cv 208,112,113
cv 209,114,115
mvva 12,144,184
v_fmac_f32_e32 v118,v233,v214
v_fmac_f32_e32 v119,v233,v215
cv 210,116,117
cv 211,118,119
mvva 12,132,180
v_mul_f32_e32 v121,v233,v121
v_mul_f32_e32 v122,v233,v122
v_mul_f32_e32 v123,v233,v123
mvva 12,128,176
v_mul_f32_e32 v124,v233,v124
v_mul_f32_e32 v125,v233,v125
v_mul_f32_e32 v126,v233,v126
v_mul_f32_e32 v127,v233,v127
s_waitcnt lgkmcnt(0)
zvva 28,136,176
buffer_store_dwordx4 v[200:203],v244,s[8:11],s14 offen offset:3072 sc0 sc1
tv 144,226,40960
v_fmac_f32_e32 v120,v233,v216
v_fmac_f32_e32 v121,v233,v217
v_fmac_f32_e32 v122,v233,v218
v_fmac_f32_e32 v123,v233,v219
mvva 28,140,180
tv 128,226,32768
tv 130,226,34816
cv 216,120,121
cv 217,122,123
mvva 28,152,184
tv 132,226,36864
tv 134,226,38912
v_fmac_f32_e32 v124,v233,v220
v_fmac_f32_e32 v125,v233,v221
v_fmac_f32_e32 v126,v233,v222
mvva 28,156,188
tv 146,226,43008
v_fmac_f32_e32 v127,v233,v223
cv 218,124,125
cv 219,126,127
zvva 24,156,172
tv 148,226,45056
tv 150,226,47104
mvva 24,152,168
v_xor_b32_e32 v227,96,v224
v_add_u32_e32 v232,131072,v224
mvva 24,140,164
buffer_store_dwordx4 v[208:211],v244,s[8:11],s88 offen offset:1024 sc0 sc1
mvva 24,136,160
v_add_u32_e32 v233,131072,v225
v_add_u32_e32 v234,131072,v226
zvva 20,136,144
v_add_u32_e32 v235,131072,v227
v_mov_b32_e32 v242,83914752
mvva 20,140,148
v_add_u32_e32 v228,65536,v224
v_add_u32_e32 v229,65536,v225
mvva 20,152,152
v_add_u32_e32 v230,65536,v226
v_add_u32_e32 v231,65536,v227
mvva 20,156,156
v_and_b32_e32 v243,240,v244
v_lshlrev_b32_e32 v243,7,v243
zvva 16,156,140
buffer_store_dwordx4 v[216:219],v244,s[8:11],s88 offen offset:3072 sc0 sc1
s_cmp_lg_u64 s[40:41],0
s_cbranch_scc1 L_redo
L_redo_r4:
mvva 16,152,136
tv 156,227,45056
v_mov_b32_e32 v236,16256
mvva 16,140,132
tv 158,227,47104
v_and_b32_e32 v248,256,v244
mvva 16,136,128
tv 152,227,40960
v_lshrrev_b32_e32 v248,3,v248
s_waitcnt lgkmcnt(3)
zvva 32,128,128
tv 140,227,36864
v_add_u32_e32 v243,v243,v248
mvva 32,132,132
tv 136,227,32768
tv 138,227,34816
mvva 32,144,136
dl 246,36,51,1024
tv 142,227,38912
mvva 32,148,140
tv 154,227,43008
v_and_b32_e32 v248,512,v244
zvva 36,148,156
v_lshrrev_b32_e32 v248,5,v248
v_add_u32_e32 v243,v243,v248
mvva 36,144,152
v_and_b32_e32 v248,16,v244
v_lshlrev_b32_e32 v248,v248,v236
mvva 36,132,148
v_cndmask_b32_e64 v238,0,v248,s[74:75]
s_mov_b32 s74,0xc0000c
mvva 36,128,144
s_mov_b32 s75,0xc0000c00
v_mov_b32_e32 v240,0
v_cndmask_b32_e64 v239,0,v248,s[74:75]
zvva 40,128,160
dl 249,36,51,2048
mvva 40,132,164
v_mov_b32_e32 v241,0
v_mov_b32_e32 v236,0
mvva 40,144,168
v_mov_b32_e32 v237,0
mvva 40,148,172
zvva 44,148,188
mvva 44,144,184
mvva 44,132,180
dl 247,36,51,3072
s_add_u32 m0,s52,16384
s_xor_b32 s51,s65,0
mvva 44,128,176
tv 144,224,57344
s_waitcnt lgkmcnt(1)
zvva 60,136,176
tv 132,224,53248
tv 134,224,55296
mvva 60,140,180
tv 128,224,49152
tv 130,224,51200
mvva 60,152,184
mvva 60,156,188
tv 146,224,59392
zvva 56,156,172
dl 245,36,51,0
tv 148,224,61440
tv 150,224,63488
mvva 56,152,168
mvva 56,140,164
mvva 56,136,160
zvva 52,136,144
mvva 52,140,148
mvva 52,152,152
dl 246,36,51,1024
mvva 52,156,156
zvva 48,156,140
mvva 48,152,136
mvva 48,140,132
tv 156,225,61440
mvva 48,136,128
tv 152,225,57344
s_waitcnt lgkmcnt(2)
zvva 64,128,128
dl 249,36,51,2048
tv 140,225,53248
mvva 64,132,132
tv 136,225,49152
tv 138,225,51200
mvva 64,144,136
tv 142,225,55296
mvva 64,148,140
tv 154,225,59392
zvva 68,148,156
tv 158,225,63488
mvva 68,144,152
mvva 68,132,148
dl 247,36,51,3072
s_lshl_b32 s51,s33,5
s_add_u32 m0,s52,98304
s_xor_b32 s51,s64,s51
mvva 68,128,144
zvva 72,128,160
mvva 72,132,164
s_waitcnt vmcnt(18)
s_mov_b32 s90,0
mvva 72,144,168
s_xor_b32 s50,s67,0
mvva 72,148,172
zvva 76,148,188
dl 245,36,51,0
mvva 76,144,184
mvva 76,132,180
tv 148,226,61440
mvva 76,128,176
tv 144,226,57344
s_waitcnt lgkmcnt(2)
zvva 92,136,176
tv 132,226,53248
mvva 92,140,180
tv 128,226,49152
tv 130,226,51200
mvva 92,152,184
dl 246,36,51,1024
tv 134,226,55296
mvva 92,156,188
tv 146,226,59392
zvva 88,156,172
tv 150,226,63488
mvva 88,152,168
mvva 88,140,164
mvva 88,136,160
zvva 84,136,144
dl 249,36,51,2048
mvva 84,140,148
mvva 84,152,152
mvva 84,156,156
zvva 80,156,140
mvva 80,152,136
mvva 80,140,132
dl 247,36,51,3072
mvva 80,136,128
s_lshl_b32 s51,s33,5
tv 152,227,57344
s_add_u32 m0,s52,114688
s_waitcnt lgkmcnt(1)
zvva 96,128,128
s_xor_b32 s51,s65,s51
tv 140,227,53248
tv 142,227,55296
mvva 96,132,132
tv 136,227,49152
tv 138,227,51200
mvva 96,144,136
mvva 96,148,140
tv 154,227,59392
zvva 100,148,156
lv 164,252,50,0
tv 156,227,61440
tv 158,227,63488
mvva 100,144,152
mvva 100,132,148
mvva 100,128,144
zvva 104,128,160
mvva 104,132,164
mvva 104,144,168
lv 172,252,50,2048
mvva 104,148,172
zvva 108,148,188
mvva 108,144,184
mvva 108,132,180
mvva 108,128,176
s_barrier
s_waitcnt lgkmcnt(0)
zvva 124,136,176
lv 180,250,50,0
tv 132,232,4096
tv 144,232,8192
tv 146,232,10240
mvva 124,140,180
tv 128,232,0
tv 130,232,2048
mvva 124,152,184
tv 134,232,6144
mvva 124,156,188
tv 148,232,12288
tv 150,232,14336
zvva 120,156,172
mvva 120,152,168
mvva 120,140,164
lv 188,250,50,2048
mvva 120,136,160
zvva 116,136,144
mvva 116,140,148
mvva 116,152,152
mvva 116,156,156
zvva 112,156,140
lv 220,250,50,3072
mvva 112,152,136
mvva 112,140,132
tv 156,233,12288
mvva 112,136,128
tv 152,233,8192
s_waitcnt lgkmcnt(2)
mvva 0,128,4
tv 140,233,4096
mvva 0,132,12
tv 136,233,0
tv 138,233,2048
mvva 0,144,20
lv 212,250,50,1024
tv 142,233,6144
mvva 0,148,28
tv 154,233,10240
mvva 4,148,60
tv 158,233,14336
mvva 4,144,52
mvva 4,132,44
mvva 4,128,36
mvva 8,128,0
lv 204,252,50,3072
mvva 8,132,8
mvva 8,144,16
mvva 8,148,24
mvva 12,148,56
s_waitcnt vmcnt(27)
mvva 12,144,48
s_waitcnt vmcnt(25)
mvva 12,132,40
lv 196,252,50,1024
s_waitcnt vmcnt(24)
mvva 12,128,32
tv 144,234,8192
s_waitcnt lgkmcnt(1)
mvva 28,136,32
tv 132,234,4096
tv 134,234,6144
mvva 28,140,40
tv 128,234,0
tv 130,234,2048
mvva 28,152,48
mvva 28,156,56
tv 146,234,10240
mvva 24,156,24
dl 245,36,51,0
s_waitcnt vmcnt(20)
s_barrier
tv 148,234,12288
tv 150,234,14336
mvva 24,152,16
mvva 24,140,8
mvva 24,136,0
mvva 20,136,36
mvva 20,140,44
mvva 20,152,52
s_cmp_eq_u32 s12,0
s_cselect_b64 exec,1,0
buffer_store_dword v251,off,s[8:11],s61 sc0 sc1
s_mov_b64 exec,-1
mvva 20,156,60
mvva 16,156,28
mvva 16,152,20
mvva 16,140,12
tv 156,235,12288
mvva 16,136,4
tv 152,235,8192
s_waitcnt lgkmcnt(2)
mvva 32,128,4
dl 246,36,51,1024
tv 140,235,4096
mvva 32,132,12
tv 136,235,0
tv 138,235,2048
mvva 32,144,20
tv 142,235,6144
mvva 32,148,28
tv 154,235,10240
mvva 36,148,60
tv 158,235,14336
mvva 36,144,52
mvva 36,132,44
dl 249,36,51,2048
mvva 36,128,36
mvva 40,128,0
mvva 40,132,8
mvva 40,144,16
mvva 40,148,24
mvva 44,148,56
dl 247,36,51,3072
s_lshl_b32 s51,s34,5
s_add_u32 m0,s52,65536
s_xor_b32 s51,s64,s51
mvva 44,144,48
tv 148,232,28672
mvva 44,132,40
s_xor_b32 s53,s60,s33
tv 150,232,30720
mvva 44,128,32
tv 144,232,24576
s_waitcnt lgkmcnt(3)
mvva 60,136,32
tv 132,232,20480
mvva 60,140,40
tv 128,232,16384
tv 130,232,18432
mvva 60,152,48
dl 245,36,51,0
tv 134,232,22528
mvva 60,156,56
tv 146,232,26624
mvva 56,156,24
mvva 56,152,16
mvva 56,140,8
mvva 56,136,0
mvva 52,136,36
buffer_load_dword v253,v242,s[8:11],s53 offen sc1
mvva 52,140,44
mvva 52,152,52
mvva 52,156,60
mvva 48,156,28
mvva 48,152,20
mvva 48,140,12
dl 246,36,51,1024
mvva 48,136,4
tv 152,233,24576
s_waitcnt lgkmcnt(1)
mvva 64,128,4
tv 140,233,20480
tv 142,233,22528
mvva 64,132,12
tv 136,233,16384
tv 138,233,18432
mvva 64,144,20
mvva 64,148,28
tv 154,233,26624
mvva 68,148,60
dl 249,36,51,2048
tv 156,233,28672
tv 158,233,30720
mvva 68,144,52
mvva 68,132,44
mvva 68,128,36
mvva 72,128,0
mvva 72,132,8
mvva 72,144,16
dl 247,36,51,3072
s_lshl_b32 s51,s34,5
s_add_u32 m0,s52,81920
s_xor_b32 s51,s65,s51
mvva 72,148,24
mvva 76,148,56
mvva 76,144,48
mvva 76,132,40
tv 148,234,28672
mvva 76,128,32
tv 144,234,24576
s_waitcnt lgkmcnt(2)
mvva 92,136,32
dl 245,36,51,0
tv 132,234,20480
mvva 92,140,40
tv 128,234,16384
tv 130,234,18432
mvva 92,152,48
tv 134,234,22528
mvva 92,156,56
tv 146,234,26624
mvva 88,156,24
tv 150,234,30720
mvva 88,152,16
mvva 88,140,8
dl 246,36,51,1024
mvva 88,136,0
mvva 84,136,36
mvva 84,140,44
mvva 84,152,52
mvva 84,156,60
mvva 80,156,28
dl 249,36,51,2048
mvva 80,152,20
mvva 80,140,12
tv 156,235,28672
mvva 80,136,4
tv 152,235,24576
s_waitcnt lgkmcnt(2)
mvva 96,128,4
tv 140,235,20480
mvva 96,132,12
tv 136,235,16384
tv 138,235,18432
mvva 96,144,20
dl 247,36,51,3072
s_lshl_b32 s51,s35,5
s_add_u32 m0,s52,32768
s_xor_b32 s51,s64,s51
tv 142,235,22528
mvva 96,148,28
tv 154,235,26624
mvva 100,148,60
tv 158,235,30720
mvva 100,144,52
mvva 100,132,44
mvva 100,128,36
mvva 104,128,0
dl 245,36,51,0
mvva 104,132,8
mvva 104,144,16
mvva 104,148,24
mvva 108,148,56
mvva 108,144,48
mvva 108,132,40
dl 246,36,51,1024
mvva 108,128,32
s_waitcnt vmcnt(27)
s_barrier
s_waitcnt lgkmcnt(0)
mvva 124,136,32
tv 132,224,4096
tv 134,224,6144
tv 144,224,8192
mvva 124,140,40
tv 128,224,0
tv 130,224,2048
mvva 124,152,48
mvva 124,156,56
tv 146,224,10240
mvva 120,156,24
dl 249,36,51,2048
tv 148,224,12288
tv 150,224,14336
mvva 120,152,16
mvva 120,140,8
mvva 120,136,0
mvva 116,136,36
mvva 116,140,44
mvva 116,152,52
dl 247,36,51,3072
s_lshl_b32 s51,s35,5
s_add_u32 m0,s52,49152
s_xor_b32 s51,s65,s51
mvva 116,156,60
mvva 112,156,28
mvva 112,152,20
s_waitcnt vmcnt(11)
v_cmp_ne_u32_e32 vcc,4,v253
mvva 112,140,12
dl 245,36,51,0
s_cbranch_vccnz L_spa3e
L_spra3e:
mvva 112,136,4
s_xor_b32 s50,s67,s33
tv 152,225,8192
s_waitcnt lgkmcnt(1)
mvvv 0,128,164
tv 140,225,4096
tv 142,225,6144
mvvv 0,132,172
tv 136,225,0
tv 138,225,2048
mvvv 0,144,180
L_vs4:
s_cbranch_execz L_vs8
mvvv 0,148,188
dl 246,36,51,1024
tv 154,225,10240
mvvv 4,148,220
tv 156,225,12288
tv 158,225,14336
mvvv 4,144,212
mvvv 4,132,204
mvvv 4,128,196
mvvv 8,128,160
la 196,252,50,0
s_xor_b32 s53,s60,s34
mvvv 8,132,168
mvvv 8,144,176
mvvv 8,148,184
mvvv 12,148,216
mvvv 12,144,208
la 204,252,50,2048
mvvv 12,132,200
tv 148,226,12288
mvvv 12,128,192
tv 144,226,8192
s_waitcnt lgkmcnt(2)
mvvv 28,136,192
tv 132,226,4096
mvvv 28,140,200
tv 128,226,0
tv 130,226,2048
mvvv 28,152,208
buffer_load_dword v253,v242,s[8:11],s53 offen sc1
tv 134,226,6144
mvvv 28,156,216
tv 146,226,10240
mvvv 24,156,184
tv 150,226,14336
mvvv 24,152,176
mvvv 24,140,168
mvvv 24,136,160
la 212,250,50,0
mvvv 20,136,196
mvvv 20,140,204
mvvv 20,152,212
mvvv 20,156,220
la 220,250,50,2048
mvvv 16,156,188
mvvv 16,152,180
mvvv 16,140,172
tv 156,227,12288
mvvv 16,136,164
la 252,250,50,3072
tv 152,227,8192
s_waitcnt lgkmcnt(2)
mvvv 32,128,164
tv 140,227,4096
mvvv 32,132,172
tv 136,227,0
tv 138,227,2048
mvvv 32,144,180
tv 142,227,6144
mvvv 32,148,188
la 244,250,50,1024
tv 154,227,10240
mvvv 36,148,220
tv 158,227,14336
mvvv 36,144,212
mvvv 36,132,204
mvvv 36,128,196
la 236,252,50,3072
mvvv 40,128,160
mvvv 40,132,168
mvvv 40,144,176
mvvv 40,148,184
mvvv 44,148,216
la 228,252,50,1024
s_add_u32 s50,s50,s55
mvvv 44,144,208
mvvv 44,132,200
tv 148,224,28672
tv 150,224,30720
mvvv 44,128,192
s_waitcnt lgkmcnt(2)
mvvv 60,136,192
la 192,252,50,0
tv 132,224,20480
mvvv 60,140,200
tv 128,224,16384
tv 130,224,18432
mvvv 60,152,208
tv 134,224,22528
mvvv 60,156,216
tv 144,224,24576
tv 146,224,26624
mvvv 56,156,184
la 200,252,50,2048
mvvv 56,152,176
mvvv 56,140,168
mvvv 56,136,160
mvvv 52,136,196
la 208,250,50,0
mvvv 52,140,204
mvvv 52,152,212
mvvv 52,156,220
mvvv 48,156,188
la 216,250,50,2048
mvvv 48,152,180
mvvv 48,140,172
tv 156,225,28672
mvvv 48,136,164
tv 152,225,24576
s_waitcnt lgkmcnt(2)
mvvv 64,128,164
tv 140,225,20480
mvvv 64,132,172
la 248,250,50,3072
tv 136,225,16384
tv 138,225,18432
mvvv 64,144,180
tv 142,225,22528
mvvv 64,148,188
tv 154,225,26624
mvvv 68,148,220
tv 158,225,30720
mvvv 68,144,212
la 240,250,50,1024
mvvv 68,132,204
mvvv 68,128,196
mvvv 72,128,160
mvvv 72,132,168
la 232,252,50,3072
mvvv 72,144,176
mvvv 72,148,184
mvvv 76,148,216
mvvv 76,144,208
mvvv 76,132,200
la 224,252,50,1024
mvvv 76,128,192
tv 144,226,24576
s_waitcnt lgkmcnt(1)
mvvv 92,136,192
tv 132,226,20480
tv 134,226,22528
mvvv 92,140,200
tv 128,226,16384
tv 130,226,18432
mvvv 92,152,208
dl 249,36,51,2048
mvvv 92,156,216
tv 146,226,26624
mvvv 88,156,184
tv 148,226,28672
tv 150,226,30720
mvvv 88,152,176
mvvv 88,140,168
dl 247,36,51,3072
s_lshl_b32 s51,s98,5
s_xor_b32 s51,s64,s51
s_add_u32 m0,s52,131072
s_xor_b32 s51,s51,4194304
mvvv 88,136,160
s_waitcnt vmcnt(16)
v_cmp_ne_u32_e32 vcc,4,v253
mvvv 84,136,196
s_cbranch_vccnz L_spa4e
L_spra4e:
mvvv 84,140,204
)"
R"(s_xor_b32 s50,s67,s34
mvvv 84,152,212
dl 245,36,51,0
mvvv 84,156,220
mvvv 80,156,188
mvvv 80,152,180
mvvv 80,140,172
la 68,252,50,0
mvvv 80,136,164
tv 152,227,24576
s_waitcnt lgkmcnt(1)
mvvv 96,128,164
tv 140,227,20480
tv 142,227,22528
mvvv 96,132,172
tv 136,227,16384
tv 138,227,18432
mvvv 96,144,180
s_xor_b32 s53,s60,s35
mvvv 96,148,188
la 76,252,50,2048
tv 154,227,26624
mvvv 100,148,220
tv 156,227,28672
tv 158,227,30720
mvvv 100,144,212
mvvv 100,132,204
mvvv 100,128,196
la 84,250,50,0
mvvv 104,128,160
mvvv 104,132,168
mvvv 104,144,176
mvvv 104,148,184
buffer_load_dword v253,v242,s[8:11],s53 offen sc1
mvvv 108,148,216
mvvv 108,144,208
mvvv 108,132,200
mvvv 108,128,192
s_barrier
s_waitcnt lgkmcnt(0)
mvvv 124,136,192
la 92,250,50,2048
tv 132,228,36864
tv 144,228,40960
tv 146,228,43008
mvvv 124,140,200
tv 128,228,32768
tv 130,228,34816
mvvv 124,152,208
tv 134,228,38912
mvvv 124,156,216
tv 148,228,45056
tv 150,228,47104
mvvv 120,156,184
la 124,250,50,3072
mvvv 120,152,176
mvvv 120,140,168
mvvv 120,136,160
mvvv 116,136,196
la 116,250,50,1024
mvvv 116,140,204
mvvv 116,152,212
mvvv 116,156,220
mvvv 112,156,188
la 108,252,50,3072
mvvv 112,152,180
mvvv 112,140,172
tv 156,229,45056
tv 158,229,47104
mvvv 112,136,164
s_waitcnt lgkmcnt(2)
mvva 0,128,196
la 100,252,50,1024
s_add_u32 s50,s50,s55
tv 140,229,36864
mvva 0,132,204
tv 136,229,32768
tv 138,229,34816
s_waitcnt vmcnt(21)
mvva 0,144,212
tv 142,229,38912
mvva 0,148,220
tv 152,229,40960
tv 154,229,43008
mvva 4,148,252
mvva 4,144,244
la 64,252,50,0
mvva 4,132,236
s_waitcnt vmcnt(21)
mvva 4,128,228
s_waitcnt vmcnt(20)
mvva 8,128,192
s_waitcnt vmcnt(19)
mvva 8,132,200
la 72,252,50,2048
s_waitcnt vmcnt(19)
mvva 8,144,208
s_waitcnt vmcnt(18)
mvva 8,148,216
s_waitcnt vmcnt(17)
mvva 12,148,248
s_waitcnt vmcnt(16)
mvva 12,144,240
la 80,250,50,0
s_waitcnt vmcnt(16)
mvva 12,132,232
tv 148,230,45056
s_waitcnt vmcnt(15)
mvva 12,128,224
tv 144,230,40960
s_waitcnt lgkmcnt(2)
mvva 28,136,224
tv 132,230,36864
mvva 28,140,232
la 88,250,50,2048
tv 128,230,32768
tv 130,230,34816
mvva 28,152,240
tv 134,230,38912
mvva 28,156,248
tv 146,230,43008
mvva 24,156,216
tv 150,230,47104
mvva 24,152,208
la 120,250,50,3072
mvva 24,140,200
mvva 24,136,192
mvva 20,136,228
mvva 20,140,236
mvva 20,152,244
la 112,250,50,1024
mvva 20,156,252
mvva 16,156,220
mvva 16,152,212
mvva 16,140,204
la 104,252,50,3072
mvva 16,136,196
tv 152,231,40960
s_waitcnt lgkmcnt(1)
mvva 32,128,196
tv 140,231,36864
tv 142,231,38912
mvva 32,132,204
tv 136,231,32768
tv 138,231,34816
mvva 32,144,212
la 96,252,50,1024
mvva 32,148,220
tv 154,231,43008
mvva 36,148,252
tv 156,231,45056
tv 158,231,47104
mvva 36,144,244
mvva 36,132,236
mvva 36,128,228
dl 246,36,51,1024
mvva 40,128,192
mvva 40,132,200
mvva 40,144,208
mvva 40,148,216
dl 249,36,51,2048
mvva 44,148,248
mvva 44,144,240
mvva 44,132,232
tv 148,228,61440
mvva 44,128,224
dl 247,36,51,3072
s_lshl_b32 s51,s98,5
s_xor_b32 s51,s65,s51
s_add_u32 m0,s52,147456
s_xor_b32 s51,s51,4194304
tv 144,228,57344
s_waitcnt lgkmcnt(2)
mvva 60,136,224
tv 132,228,53248
mvva 60,140,232
tv 128,228,49152
tv 130,228,51200
s_waitcnt vmcnt(16)
v_cmp_ne_u32_e32 vcc,4,v253
mvva 60,152,240
s_cbranch_vccnz L_spa5e
L_spra5e:
tv 134,228,55296
s_xor_b32 s50,s67,s35
mvva 60,156,248
dl 245,36,51,0
tv 146,228,59392
mvva 56,156,216
tv 150,228,63488
mvva 56,152,208
mvva 56,140,200
mvva 56,136,192
la 132,252,50,0
mvva 52,136,228
mvva 52,140,236
mvva 52,152,244
mvva 52,156,252
mvva 48,156,220
la 140,252,50,2048
s_xor_b32 s53,s60,s98
s_xor_b32 s53,s53,512
mvva 48,152,212
tv 156,229,61440
mvva 48,140,204
tv 158,229,63488
mvva 48,136,196
tv 152,229,57344
s_waitcnt lgkmcnt(3)
mvva 64,128,196
la 148,250,50,0
tv 140,229,53248
mvva 64,132,204
tv 136,229,49152
tv 138,229,51200
mvva 64,144,212
tv 142,229,55296
mvva 64,148,220
tv 154,229,59392
mvva 68,148,252
buffer_load_dword v253,v242,s[8:11],s53 offen sc1
mvva 68,144,244
mvva 68,132,236
mvva 68,128,228
mvva 72,128,192
mvva 72,132,200
la 156,250,50,2048
mvva 72,144,208
mvva 72,148,216
mvva 76,148,248
mvva 76,144,240
la 188,250,50,3072
mvva 76,132,232
tv 148,230,61440
mvva 76,128,224
tv 144,230,57344
s_waitcnt lgkmcnt(2)
mvva 92,136,224
tv 132,230,53248
mvva 92,140,232
la 180,250,50,1024
tv 128,230,49152
tv 130,230,51200
mvva 92,152,240
tv 134,230,55296
mvva 92,156,248
tv 146,230,59392
mvva 88,156,216
tv 150,230,63488
mvva 88,152,208
la 172,252,50,3072
mvva 88,140,200
mvva 88,136,192
mvva 84,136,228
mvva 84,140,236
la 164,252,50,1024
s_add_u32 s50,s50,s55
mvva 84,152,244
mvva 84,156,252
mvva 80,156,220
mvva 80,152,212
mvva 80,140,204
la 128,252,50,0
mvva 80,136,196
tv 152,231,57344
s_waitcnt lgkmcnt(1)
mvva 96,128,196
tv 140,231,53248
tv 142,231,55296
mvva 96,132,204
tv 136,231,49152
tv 138,231,51200
mvva 96,144,212
la 136,252,50,2048
mvva 96,148,220
tv 154,231,59392
mvva 100,148,252
tv 156,231,61440
tv 158,231,63488
mvva 100,144,244
mvva 100,132,236
la 144,250,50,0
mvva 100,128,228
mvva 104,128,192
mvva 104,132,200
mvva 104,144,208
la 152,250,50,2048
mvva 104,148,216
mvva 108,148,248
mvva 108,144,240
mvva 108,132,232
la 184,250,50,3072
mvva 108,128,224
s_barrier
s_waitcnt lgkmcnt(0)
mvva 124,136,224
tv 132,228,4096
tv 134,228,6144
tv 144,228,8192
mvva 124,140,232
tv 128,228,0
tv 130,228,2048
mvva 124,152,240
mvva 124,156,248
la 176,250,50,1024
tv 146,228,10240
mvva 120,156,216
tv 148,228,12288
tv 150,228,14336
mvva 120,152,208
mvva 120,140,200
mvva 120,136,192
la 168,252,50,3072
mvva 116,136,228
mvva 116,140,236
mvva 116,152,244
mvva 116,156,252
la 160,252,50,1024
mvva 112,156,220
mvva 112,152,212
mvva 112,140,204
tv 156,229,12288
tv 158,229,14336
mvva 112,136,196
s_waitcnt lgkmcnt(2)
mvva 0,128,68
dl 246,36,51,1024
tv 140,229,4096
mvva 0,132,76
tv 136,229,0
tv 138,229,2048
mvva 0,144,84
tv 142,229,6144
s_waitcnt vmcnt(22)
mvva 0,148,92
tv 152,229,8192
tv 154,229,10240
mvva 4,148,124
dl 249,36,51,2048
mvva 4,144,116
mvva 4,132,108
mvva 4,128,100
mvva 8,128,64
dl 247,36,51,3072
s_lshl_b32 s51,s98,5
s_xor_b32 s51,s64,s51
s_add_u32 m0,s52,0
s_xor_b32 s51,s51,4456448
mvva 8,132,72
mvva 8,144,80
mvva 8,148,88
s_waitcnt vmcnt(16)
v_cmp_ne_u32_e32 vcc,4,v253
mvva 12,148,120
dl 245,36,51,0
s_cbranch_vccnz L_spa6e
L_spra6e:
mvva 12,144,112
s_xor_b32 s50,s67,s98
mvva 12,132,104
s_xor_b32 s50,s50,131072
tv 148,230,12288
tv 150,230,14336
mvva 12,128,96
s_waitcnt lgkmcnt(2)
mvva 28,136,96
dl 246,36,51,1024
tv 132,230,4096
mvva 28,140,104
tv 128,230,0
tv 130,230,2048
mvva 28,152,112
tv 134,230,6144
mvva 28,156,120
tv 144,230,8192
tv 146,230,10240
mvva 24,156,88
L_vs5:
s_cbranch_execz L_vs1
mvva 24,152,80
dl 249,36,51,2048
mvva 24,140,72
s_xor_b32 s53,s60,s98
mvva 24,136,64
s_xor_b32 s53,s53,8704
mvva 20,136,100
mvva 20,140,108
dl 247,36,51,3072
mvva 20,152,116
s_lshl_b32 s51,s98,5
s_add_u32 m0,s52,16384
s_xor_b32 s51,s65,s51
mvva 20,156,124
s_xor_b32 s51,s51,4456448
mvva 16,156,92
mvva 16,152,84
buffer_load_dword v253,v242,s[8:11],s53 offen sc1
mvva 16,140,76
tv 156,231,12288
mvva 16,136,68
tv 152,231,8192
s_waitcnt lgkmcnt(2)
mvva 32,128,68
tv 140,231,4096
mvva 32,132,76
la 4,252,50,0
tv 136,231,0
tv 138,231,2048
mvva 32,144,84
tv 142,231,6144
mvva 32,148,92
tv 154,231,10240
mvva 36,148,124
tv 158,231,14336
mvva 36,144,116
la 12,252,50,2048
mvva 36,132,108
mvva 36,128,100
mvva 40,128,64
mvva 40,132,72
mvva 40,144,80
la 20,250,50,0
mvva 40,148,88
mvva 44,148,120
mvva 44,144,112
mvva 44,132,104
la 28,250,50,2048
mvva 44,128,96
tv 144,228,24576
s_waitcnt lgkmcnt(1)
mvva 60,136,96
tv 132,228,20480
tv 134,228,22528
mvva 60,140,104
tv 128,228,16384
tv 130,228,18432
mvva 60,152,112
la 60,250,50,3072
mvva 60,156,120
tv 146,228,26624
mvva 56,156,88
tv 148,228,28672
tv 150,228,30720
mvva 56,152,80
mvva 56,140,72
mvva 56,136,64
la 52,250,50,1024
mvva 52,136,100
mvva 52,140,108
mvva 52,152,116
mvva 52,156,124
la 44,252,50,3072
mvva 48,156,92
mvva 48,152,84
mvva 48,140,76
tv 156,229,28672
mvva 48,136,68
la 36,252,50,1024
s_add_u32 s50,s50,s55
tv 152,229,24576
s_waitcnt lgkmcnt(2)
mvva 64,128,68
tv 140,229,20480
mvva 64,132,76
tv 136,229,16384
tv 138,229,18432
mvva 64,144,84
tv 142,229,22528
mvva 64,148,92
la 0,252,50,0
tv 154,229,26624
mvva 68,148,124
tv 158,229,30720
mvva 68,144,116
mvva 68,132,108
mvva 68,128,100
la 8,252,50,2048
mvva 72,128,64
mvva 72,132,72
mvva 72,144,80
mvva 72,148,88
mvva 76,148,120
la 16,250,50,0
mvva 76,144,112
mvva 76,132,104
tv 148,230,28672
tv 150,230,30720
mvva 76,128,96
s_waitcnt lgkmcnt(2)
mvva 92,136,96
la 24,250,50,2048
tv 132,230,20480
mvva 92,140,104
tv 128,230,16384
tv 130,230,18432
mvva 92,152,112
tv 134,230,22528
mvva 92,156,120
tv 144,230,24576
tv 146,230,26624
mvva 88,156,88
la 56,250,50,3072
mvva 88,152,80
mvva 88,140,72
mvva 88,136,64
mvva 84,136,100
mvva 84,140,108
la 48,250,50,1024
mvva 84,152,116
mvva 84,156,124
mvva 80,156,92
mvva 80,152,84
la 40,252,50,3072
mvva 80,140,76
tv 156,231,28672
mvva 80,136,68
tv 152,231,24576
s_waitcnt lgkmcnt(2)
mvva 96,128,68
tv 140,231,20480
mvva 96,132,76
la 32,252,50,1024
tv 136,231,16384
tv 138,231,18432
mvva 96,144,84
tv 142,231,22528
mvva 96,148,92
tv 154,231,26624
mvva 100,148,124
tv 158,231,30720
mvva 100,144,116
dl 245,36,51,0
s_waitcnt vmcnt(17)
v_cmp_ne_u32_e32 vcc,4,v253
mvva 100,132,108
s_cbranch_vccnz L_spa7e
L_spra7e:
mvva 100,128,100
s_xor_b32 s50,s67,s98
mvva 104,128,64
s_xor_b32 s50,s50,139264
mvva 104,132,72
dl 246,36,51,1024
mvva 104,144,80
mvva 104,148,88
mvva 108,148,120
mvva 108,144,112
mvva 108,132,104
lv 164,252,50,0
mvva 108,128,96
s_barrier
s_waitcnt lgkmcnt(0)
mvva 124,136,96
s_xor_b32 s53,s60,s98
tv 132,224,36864
tv 134,224,38912
tv 144,224,40960
s_xor_b32 s53,s53,16896
mvva 124,140,104
tv 128,224,32768
tv 130,224,34816
mvva 124,152,112
lv 172,252,50,2048
mvva 124,156,120
tv 146,224,43008
mvva 120,156,88
tv 148,224,45056
tv 150,224,47104
mvva 120,152,80
mvva 120,140,72
buffer_load_dword v253,v242,s[8:11],s53 offen sc1
mvva 120,136,64
mvva 116,136,100
mvva 116,140,108
mvva 116,152,116
lv 180,250,50,0
mvva 116,156,124
mvva 112,156,92
mvva 112,152,84
mvva 112,140,76
lv 188,250,50,2048
mvva 112,136,68
tv 152,225,40960
s_waitcnt lgkmcnt(1)
mvva 0,128,132
tv 140,225,36864
tv 142,225,38912
mvva 0,132,140
tv 136,225,32768
tv 138,225,34816
mvva 0,144,148
mvva 0,148,156
lv 220,250,50,3072
tv 154,225,43008
mvva 4,148,188
tv 156,225,45056
tv 158,225,47104
mvva 4,144,180
mvva 4,132,172
mvva 4,128,164
lv 212,250,50,1024
mvva 8,128,128
mvva 8,132,136
mvva 8,144,144
mvva 8,148,152
lv 204,252,50,3072
mvva 12,148,184
mvva 12,144,176
mvva 12,132,168
tv 148,226,45056
tv 150,226,47104
mvva 12,128,160
s_waitcnt lgkmcnt(2)
mvva 28,136,160
lv 196,252,50,1024
s_add_u32 s50,s50,s55
tv 132,226,36864
mvva 28,140,168
tv 128,226,32768
tv 130,226,34816
mvva 28,152,176
tv 134,226,38912
mvva 28,156,184
tv 144,226,40960
tv 146,226,43008
mvva 24,156,152
lv 160,252,50,0
mvva 24,152,144
mvva 24,140,136
mvva 24,136,128
mvva 20,136,164
lv 168,252,50,2048
mvva 20,140,172
mvva 20,152,180
mvva 20,156,188
mvva 16,156,156
lv 176,250,50,0
mvva 16,152,148
mvva 16,140,140
tv 156,227,45056
tv 158,227,47104
mvva 16,136,132
s_waitcnt lgkmcnt(2)
mvva 32,128,132
lv 184,250,50,2048
tv 140,227,36864
mvva 32,132,140
tv 136,227,32768
tv 138,227,34816
mvva 32,144,148
tv 142,227,38912
mvva 32,148,156
tv 152,227,40960
tv 154,227,43008
mvva 36,148,188
mvva 36,144,180
lv 216,250,50,3072
mvva 36,132,172
mvva 36,128,164
mvva 40,128,128
mvva 40,132,136
lv 208,250,50,1024
mvva 40,144,144
mvva 40,148,152
mvva 44,148,184
mvva 44,144,176
lv 200,252,50,3072
mvva 44,132,168
tv 148,224,61440
mvva 44,128,160
tv 144,224,57344
s_waitcnt lgkmcnt(2)
mvva 60,136,160
tv 132,224,53248
mvva 60,140,168
tv 128,224,49152
tv 130,224,51200
mvva 60,152,176
lv 192,252,50,1024
tv 134,224,55296
mvva 60,156,184
tv 146,224,59392
mvva 56,156,152
tv 150,224,63488
mvva 56,152,144
mvva 56,140,136
dl 249,36,51,2048
mvva 56,136,128
mvva 52,136,164
mvva 52,140,172
mvva 52,152,180
dl 247,36,51,3072
s_lshl_b32 s51,s98,5
s_xor_b32 s51,s64,s51
s_add_u32 m0,s52,98304
s_xor_b32 s51,s51,4718592
mvva 52,156,188
mvva 48,156,156
mvva 48,152,148
mvva 48,140,140
dl 245,36,51,0
mvva 48,136,132
tv 152,225,57344
s_waitcnt vmcnt(17)
v_cmp_ne_u32_e32 vcc,4,v253
s_waitcnt lgkmcnt(1)
mvva 64,128,132
s_cbranch_vccnz L_spa8e
L_spra8e:
tv 140,225,53248
tv 142,225,55296
mvva 64,132,140
s_xor_b32 s50,s67,s98
tv 136,225,49152
tv 138,225,51200
mvva 64,144,148
dl 246,36,51,1024
mvva 64,148,156
s_xor_b32 s50,s50,147456
tv 154,225,59392
mvva 68,148,188
tv 156,225,61440
tv 158,225,63488
mvva 68,144,180
mvva 68,132,172
mvva 68,128,164
dl 249,36,51,2048
mvva 72,128,128
mvva 72,132,136
mvva 72,144,144
s_xor_b32 s53,s60,s98
mvva 72,148,152
dl 247,36,51,3072
s_xor_b32 s53,s53,25088
mvva 76,148,184
s_lshl_b32 s51,s98,5
s_add_u32 m0,s52,114688
s_xor_b32 s51,s65,s51
mvva 76,144,176
s_xor_b32 s51,s51,4718592
mvva 76,132,168
tv 148,226,61440
mvva 76,128,160
buffer_load_dword v253,v242,s[8:11],s53 offen sc1
tv 144,226,57344
s_waitcnt lgkmcnt(2)
mvva 92,136,160
tv 132,226,53248
mvva 92,140,168
tv 128,226,49152
tv 130,226,51200
mvva 92,152,176
tv 134,226,55296
mvva 92,156,184
la 196,252,50,0
tv 146,226,59392
mvva 88,156,152
tv 150,226,63488
mvva 88,152,144
mvva 88,140,136
mvva 88,136,128
la 204,252,50,2048
mvva 84,136,164
mvva 84,140,172
mvva 84,152,180
mvva 84,156,188
mvva 80,156,156
la 212,250,50,0
mvva 80,152,148
mvva 80,140,140
tv 156,227,61440
tv 158,227,63488
mvva 80,136,132
s_waitcnt lgkmcnt(2)
mvva 96,128,132
la 220,250,50,2048
tv 140,227,53248
mvva 96,132,140
tv 136,227,49152
tv 138,227,51200
mvva 96,144,148
tv 142,227,55296
mvva 96,148,156
tv 152,227,57344
tv 154,227,59392
mvva 100,148,188
la 252,250,50,3072
mvva 100,144,180
mvva 100,132,172
mvva 100,128,164
mvva 104,128,128
mvva 104,132,136
la 244,250,50,1024
mvva 104,144,144
mvva 104,148,152
mvva 108,148,184
mvva 108,144,176
la 236,252,50,3072
mvva 108,132,168
mvva 108,128,160
s_barrier
s_waitcnt lgkmcnt(0)
mvva 124,136,160
tv 132,232,4096
tv 144,232,8192
mvva 124,140,168
la 228,252,50,1024
s_add_u32 s50,s50,s55
tv 128,232,0
tv 130,232,2048
mvva 124,152,176
tv 134,232,6144
mvva 124,156,184
tv 146,232,10240
mvva 120,156,152
tv 148,232,12288
tv 150,232,14336
mvva 120,152,144
la 192,252,50,0
mvva 120,140,136
mvva 120,136,128
mvva 116,136,164
mvva 116,140,172
la 200,252,50,2048
mvva 116,152,180
mvva 116,156,188
mvva 112,156,156
mvva 112,152,148
mvva 112,140,140
la 208,250,50,0
mvva 112,136,132
tv 152,233,8192
s_waitcnt lgkmcnt(1)
mvva 0,128,4
tv 140,233,4096
tv 142,233,6144
mvva 0,132,12
tv 136,233,0
tv 138,233,2048
mvva 0,144,20
la 216,250,50,2048
mvva 0,148,28
tv 154,233,10240
mvva 4,148,60
tv 156,233,12288
tv 158,233,14336
mvva 4,144,52
mvva 4,132,44
la 248,250,50,3072
mvva 4,128,36
mvva 8,128,0
mvva 8,132,8
mvva 8,144,16
la 240,250,50,1024
mvva 8,148,24
mvva 12,148,56
mvva 12,144,48
mvva 12,132,40
la 232,252,50,3072
mvva 12,128,32
tv 144,234,8192
s_waitcnt lgkmcnt(1)
mvva 28,136,32
tv 132,234,4096
tv 134,234,6144
mvva 28,140,40
tv 128,234,0
tv 130,234,2048
mvva 28,152,48
mvva 28,156,56
la 224,252,50,1024
tv 146,234,10240
mvva 24,156,24
tv 148,234,12288
tv 150,234,14336
mvva 24,152,16
mvva 24,140,8
mvva 24,136,0
dl 245,36,51,0
mvva 20,136,36
mvva 20,140,44
s_waitcnt vmcnt(17)
v_cmp_ne_u32_e32 vcc,4,v253
mvva 20,152,52
s_cbranch_vccnz L_spa9e
L_spra9e:
mvva 20,156,60
dl 246,36,51,1024
mvva 16,156,28
s_xor_b32 s50,s67,s98
mvva 16,152,20
s_xor_b32 s50,s50,155648
mvva 16,140,12
tv 156,235,12288
tv 158,235,14336
mvva 16,136,4
s_waitcnt lgkmcnt(2)
mvva 32,128,4
dl 249,36,51,2048
tv 140,235,4096
mvva 32,132,12
tv 136,235,0
tv 138,235,2048
mvva 32,144,20
tv 142,235,6144
mvva 32,148,28
tv 152,235,8192
tv 154,235,10240
mvva 36,148,60
dl 247,36,51,3072
s_lshl_b32 s51,s98,5
s_xor_b32 s51,s64,s51
s_add_u32 m0,s52,65536
s_xor_b32 s51,s51,4980736
s_xor_b32 s53,s60,s98
mvva 36,144,52
L_vs6:
s_cbranch_execz L_vr1
s_xor_b32 s53,s53,25216
mvva 36,132,44
mvva 36,128,36
mvva 40,128,0
dl 245,36,51,0
mvva 40,132,8
mvva 40,144,16
mvva 40,148,24
mvva 44,148,56
buffer_load_dword v253,v242,s[8:11],s53 offen sc1
mvva 44,144,48
mvva 44,132,40
tv 148,232,28672
tv 150,232,30720
mvva 44,128,32
s_waitcnt lgkmcnt(2)
mvva 60,136,32
dl 246,36,51,1024
tv 132,232,20480
mvva 60,140,40
tv 128,232,16384
tv 130,232,18432
mvva 60,152,48
tv 134,232,22528
mvva 60,156,56
tv 144,232,24576
tv 146,232,26624
mvva 56,156,24
mvva 56,152,16
dl 249,36,51,2048
mvva 56,140,8
mvva 56,136,0
mvva 52,136,36
mvva 52,140,44
dl 247,36,51,3072
s_lshl_b32 s51,s98,5
s_xor_b32 s51,s64,s51
s_add_u32 m0,s52,81920
s_xor_b32 s51,s51,6029312
mvva 52,152,52
mvva 52,156,60
mvva 48,156,28
mvva 48,152,20
dl 245,36,51,0
mvva 48,140,12
tv 156,233,28672
mvva 48,136,4
tv 152,233,24576
s_waitcnt lgkmcnt(2)
mvva 64,128,4
tv 140,233,20480
mvva 64,132,12
tv 136,233,16384
tv 138,233,18432
mvva 64,144,20
dl 246,36,51,1024
tv 142,233,22528
mvva 64,148,28
tv 154,233,26624
mvva 68,148,60
tv 158,233,30720
mvva 68,144,52
mvva 68,132,44
dl 249,36,51,2048
mvva 68,128,36
mvva 72,128,0
mvva 72,132,8
mvva 72,144,16
dl 247,36,51,3072
mvva 72,148,24
s_lshl_b32 s51,s98,5
s_add_u32 m0,s52,32768
s_xor_b32 s51,s65,s51
mvva 76,148,56
s_xor_b32 s51,s51,4980736
mvva 76,144,48
mvva 76,132,40
la 68,252,50,0
mvva 76,128,32
tv 144,224,8192
s_waitcnt lgkmcnt(1)
mvva 92,136,32
)"
R"(tv 132,224,4096
tv 134,224,6144
mvva 92,140,40
tv 128,224,0
tv 130,224,2048
mvva 92,152,48
la 76,252,50,2048
mvva 92,156,56
tv 146,224,10240
mvva 88,156,24
tv 148,224,12288
tv 150,224,14336
mvva 88,152,16
mvva 88,140,8
mvva 88,136,0
la 84,250,50,0
mvva 84,136,36
mvva 84,140,44
mvva 84,152,52
mvva 84,156,60
la 92,250,50,2048
mvva 80,156,28
mvva 80,152,20
mvva 80,140,12
tv 156,225,12288
mvva 80,136,4
la 124,250,50,3072
tv 152,225,8192
s_waitcnt lgkmcnt(2)
mvvv 0,128,164
tv 140,225,4096
mvvv 0,132,172
tv 136,225,0
tv 138,225,2048
mvvv 0,144,180
tv 142,225,6144
mvvv 0,148,188
la 116,250,50,1024
tv 154,225,10240
mvvv 4,148,220
tv 158,225,14336
mvvv 4,144,212
mvvv 4,132,204
mvvv 4,128,196
la 108,252,50,3072
mvvv 8,128,160
mvvv 8,132,168
mvvv 8,144,176
mvvv 8,148,184
mvvv 12,148,216
la 100,252,50,1024
s_add_u32 s50,s50,s55
mvvv 12,144,208
mvvv 12,132,200
tv 148,226,12288
tv 150,226,14336
mvvv 12,128,192
s_barrier
tv 144,226,8192
s_waitcnt lgkmcnt(3)
mvvv 28,136,192
la 64,252,50,0
mvvv 28,140,200
tv 128,226,0
tv 130,226,2048
mvvv 28,152,208
tv 132,226,4096
tv 134,226,6144
mvvv 28,156,216
tv 146,226,10240
s_waitcnt vmcnt(16)
v_cmp_ne_u32_e32 vcc,4,v253
mvvv 24,156,184
la 72,252,50,2048
s_cbranch_vccnz L_spa10e
L_spra10e:
mvvv 24,152,176
v_and_b32_e32 v242,48,v244
v_lshlrev_b32_e32 v242,3,v242
mvvv 24,140,168
mvvv 24,136,160
mvvv 20,136,196
mvvv 20,140,204
la 80,250,50,0
mvvv 20,152,212
mvvv 20,156,220
mvvv 16,156,188
s_andn2_b32 s53,s61,384
mvvv 16,152,180
la 88,250,50,2048
mvvv 16,140,172
tv 156,227,12288
mvvv 16,136,164
tv 152,227,8192
s_waitcnt lgkmcnt(2)
mvvv 32,128,164
tv 140,227,4096
mvvv 32,132,172
buffer_load_dword v253,v242,s[8:11],s53 offen sc1
tv 136,227,0
tv 138,227,2048
mvvv 32,144,180
tv 142,227,6144
mvvv 32,148,188
tv 154,227,10240
mvvv 36,148,220
tv 158,227,14336
mvvv 36,144,212
la 120,250,50,3072
mvvv 36,132,204
mvvv 36,128,196
mvvv 40,128,160
mvvv 40,132,168
la 112,250,50,1024
mvvv 40,144,176
mvvv 40,148,184
mvvv 44,148,216
mvvv 44,144,208
mvvv 44,132,200
la 104,252,50,3072
mvvv 44,128,192
tv 144,224,24576
s_waitcnt lgkmcnt(1)
mvvv 60,136,192
tv 132,224,20480
tv 134,224,22528
mvvv 60,140,200
tv 128,224,16384
tv 130,224,18432
mvvv 60,152,208
la 96,252,50,1024
s_xor_b32 s50,s67,s98
s_xor_b32 s50,s50,188416
mvvv 60,156,216
tv 146,224,26624
mvvv 56,156,184
tv 148,224,28672
tv 150,224,30720
mvvv 56,152,176
mvvv 56,140,168
la 132,252,50,0
mvvv 56,136,160
mvvv 52,136,196
mvvv 52,140,204
mvvv 52,152,212
la 140,252,50,2048
mvvv 52,156,220
mvvv 48,156,188
mvvv 48,152,180
mvvv 48,140,172
la 148,250,50,0
mvvv 48,136,164
tv 152,225,24576
s_waitcnt lgkmcnt(1)
mvvv 64,128,164
tv 140,225,20480
tv 142,225,22528
mvvv 64,132,172
tv 136,225,16384
tv 138,225,18432
mvvv 64,144,180
mvvv 64,148,188
la 156,250,50,2048
tv 154,225,26624
mvvv 68,148,220
tv 156,225,28672
tv 158,225,30720
mvvv 68,144,212
mvvv 68,132,204
mvvv 68,128,196
la 188,250,50,3072
mvvv 72,128,160
mvvv 72,132,168
mvvv 72,144,176
mvvv 72,148,184
la 180,250,50,1024
mvvv 76,148,216
mvvv 76,144,208
mvvv 76,132,200
tv 148,228,45056
tv 150,228,47104
mvvv 76,128,192
s_waitcnt lgkmcnt(2)
mvvv 92,136,192
la 172,252,50,3072
tv 132,228,36864
mvvv 92,140,200
tv 128,228,32768
tv 130,228,34816
mvvv 92,152,208
tv 134,228,38912
mvvv 92,156,216
tv 144,228,40960
tv 146,228,43008
mvvv 88,156,184
la 164,252,50,1024
s_add_u32 s50,s50,s55
mvvv 88,152,176
mvvv 88,140,168
mvvv 88,136,160
mvvv 84,136,196
la 128,252,50,0
mvvv 84,140,204
mvvv 84,152,212
mvvv 84,156,220
mvvv 80,156,188
la 136,252,50,2048
mvvv 80,152,180
mvvv 80,140,172
tv 156,229,45056
tv 158,229,47104
mvvv 80,136,164
s_waitcnt lgkmcnt(2)
mvva 0,128,196
la 144,250,50,0
tv 140,229,36864
mvva 0,132,204
tv 136,229,32768
tv 138,229,34816
mvva 0,144,212
tv 142,229,38912
mvva 0,148,220
tv 152,229,40960
tv 154,229,43008
mvva 4,148,252
mvva 4,144,244
la 152,250,50,2048
mvva 4,132,236
mvva 4,128,228
mvva 8,128,192
mvva 8,132,200
la 184,250,50,3072
mvva 8,144,208
mvva 8,148,216
s_waitcnt vmcnt(17)
v_cmp_ne_u32_e32 vcc,4,v253
mvva 12,148,248
s_cbranch_vccnz L_sppe
L_sprpe:
mvva 12,144,240
la 176,250,50,1024
mvva 12,132,232
tv 148,230,45056
mvva 12,128,224
tv 144,230,40960
s_waitcnt lgkmcnt(2)
mvva 28,136,224
tv 132,230,36864
mvva 28,140,232
tv 128,230,32768
tv 130,230,34816
mvva 28,152,240
la 168,252,50,3072
tv 134,230,38912
mvva 28,156,248
tv 146,230,43008
mvva 24,156,216
tv 150,230,47104
mvva 24,152,208
mvva 24,140,200
la 160,252,50,1024
mvva 24,136,192
mvva 20,136,228
mvva 20,140,236
mvva 20,152,244
dl 245,36,51,0
mvva 20,156,252
mvva 16,156,220
mvva 16,152,212
mvva 16,140,204
dl 246,36,51,1024
mvva 16,136,196
tv 152,231,40960
s_waitcnt lgkmcnt(1)
mvva 32,128,196
tv 140,231,36864
tv 142,231,38912
mvva 32,132,204
tv 136,231,32768
tv 138,231,34816
mvva 32,144,212
dl 249,36,51,2048
mvva 32,148,220
tv 154,231,43008
mvva 36,148,252
tv 156,231,45056
tv 158,231,47104
mvva 36,144,244
mvva 36,132,236
mvva 36,128,228
dl 247,36,51,3072
s_lshl_b32 s51,s98,5
s_xor_b32 s51,s65,s51
s_add_u32 m0,s52,49152
s_xor_b32 s51,s51,6029312
mvva 40,128,192
mvva 40,132,200
mvva 40,144,208
mvva 40,148,216
dl 245,36,51,0
mvva 44,148,248
mvva 44,144,240
mvva 44,132,232
mvva 44,128,224
dl 246,36,51,1024
s_waitcnt lgkmcnt(0)
mvva 60,136,224
s_barrier
mvva 60,140,232
tv 144,228,57344
tv 146,228,59392
tv 128,228,49152
tv 130,228,51200
mvva 60,152,240
tv 132,228,53248
tv 134,228,55296
mvva 60,156,248
dl 249,36,51,2048
mvva 56,156,216
tv 148,228,61440
tv 150,228,63488
mvva 56,152,208
mvva 56,140,200
mvva 56,136,192
dl 247,36,51,3072
mvva 52,136,228
mvva 52,140,236
mvva 52,152,244
mvva 52,156,252
mvva 48,156,220
mvva 48,152,212
mvva 48,140,204
tv 156,229,61440
mvva 48,136,196
tv 152,229,57344
s_waitcnt lgkmcnt(2)
mvva 64,128,196
tv 140,229,53248
mvva 64,132,204
tv 136,229,49152
tv 138,229,51200
mvva 64,144,212
tv 142,229,55296
mvva 64,148,220
tv 154,229,59392
mvva 68,148,252
tv 158,229,63488
mvva 68,144,244
mvva 68,132,236
mvva 68,128,228
mvva 72,128,192
mvva 72,132,200
mvva 72,144,208
mvva 72,148,216
mvva 76,148,248
mvva 76,144,240
mvva 76,132,232
tv 148,228,12288
mvva 76,128,224
tv 144,228,8192
s_waitcnt lgkmcnt(2)
mvva 92,136,224
tv 132,228,4096
mvva 92,140,232
tv 128,228,0
tv 130,228,2048
mvva 92,152,240
tv 134,228,6144
mvva 92,156,248
tv 146,228,10240
mvva 88,156,216
tv 150,228,14336
mvva 88,152,208
mvva 88,140,200
mvva 88,136,192
mvva 84,136,228
mvva 84,140,236
mvva 84,152,244
mvva 84,156,252
mvva 80,156,220
mvva 80,152,212
mvva 80,140,204
tv 156,229,12288
mvva 80,136,196
tv 152,229,8192
s_waitcnt lgkmcnt(2)
mvva 0,128,68
tv 140,229,4096
mvva 0,132,76
tv 136,229,0
tv 138,229,2048
mvva 0,144,84
tv 142,229,6144
mvva 0,148,92
tv 154,229,10240
mvva 4,148,124
tv 158,229,14336
mvva 4,144,116
mvva 4,132,108
mvva 4,128,100
mvva 8,128,64
mvva 8,132,72
mvva 8,144,80
mvva 8,148,88
s_waitcnt vmcnt(10)
mvva 12,148,120
mvva 12,144,112
mvva 12,132,104
tv 148,228,28672
mvva 12,128,96
tv 144,228,24576
s_waitcnt lgkmcnt(2)
mvva 28,136,96
tv 132,228,20480
mvva 28,140,104
tv 128,228,16384
tv 130,228,18432
mvva 28,152,112
tv 134,228,22528
mvva 28,156,120
tv 146,228,26624
mvva 24,156,88
tv 150,228,30720
mvva 24,152,80
mvva 24,140,72
mvva 24,136,64
mvva 20,136,100
mvva 20,140,108
mvva 20,152,116
mvva 20,156,124
mvva 16,156,92
mvva 16,152,84
mvva 16,140,76
tv 156,229,28672
mvva 16,136,68
tv 152,229,24576
s_waitcnt lgkmcnt(2)
mvva 0,128,132
tv 140,229,20480
mvva 0,132,140
tv 136,229,16384
tv 138,229,18432
mvva 0,144,148
tv 142,229,22528
mvva 0,148,156
tv 154,229,26624
mvva 4,148,188
tv 158,229,30720
mvva 4,144,180
mvva 4,132,172
L_vs7:
s_cbranch_execz L_vr1
mvva 4,128,164
mvva 8,128,128
mvva 8,132,136
cv 0,0,1
cv 1,2,3
mvva 8,144,144
mvva 8,148,152
mvva 12,148,184
mvva 12,144,176
cv 4,4,5
cv 5,6,7
s_waitcnt vmcnt(8)
mvva 12,132,168
tv 148,230,12288
mvva 12,128,160
tv 144,230,8192
cv 8,8,9
s_waitcnt lgkmcnt(2)
mvva 28,136,160
tv 132,230,4096
cv 9,10,11
mvva 28,140,168
tv 128,230,0
tv 130,230,2048
mvva 28,152,176
tv 134,230,6144
cv 12,12,13
mvva 28,156,184
tv 146,230,10240
cv 13,14,15
mvva 24,156,152
tv 150,230,14336
mvva 24,152,144
mvva 24,140,136
mvva 24,136,128
mvva 20,136,164
cv 14,28,29
cv 15,30,31
mvva 20,140,172
buffer_store_dwordx4 v[12:15],v244,s[8:11],s56 offen offset:3072 sc0 sc1
mvva 20,152,180
mvva 20,156,188
mvva 16,156,156
cv 10,24,25
cv 11,26,27
mvva 16,152,148
buffer_store_dwordx4 v[8:11],v244,s[8:11],s56 offen offset:2048 sc0 sc1
mvva 16,140,140
tv 156,231,12288
mvva 16,136,132
cv 6,20,21
cv 7,22,23
s_waitcnt lgkmcnt(1)
mvva 32,128,68
tv 140,231,4096
mvva 32,132,76
tv 136,231,0
tv 138,231,2048
mvva 32,144,84
buffer_store_dwordx4 v[4:7],v244,s[8:11],s56 offen offset:1024 sc0 sc1
tv 142,231,6144
mvva 32,148,92
tv 152,231,8192
tv 154,231,10240
mvva 36,148,124
tv 158,231,14336
cv 2,16,17
cv 3,18,19
mvva 36,144,116
mvva 36,132,108
buffer_store_dwordx4 v[0:3],v244,s[8:11],s56 offen offset:0 sc0 sc1
mvva 36,128,100
mvva 40,128,64
mvva 40,132,72
mvva 40,144,80
mvva 40,148,88
mvva 44,148,120
mvva 44,144,112
mvva 44,132,104
tv 148,230,28672
mvva 44,128,96
s_waitcnt vmcnt(4)
s_barrier
tv 144,230,24576
s_waitcnt lgkmcnt(2)
mvva 60,136,96
tv 132,230,20480
mvva 60,140,104
tv 128,230,16384
tv 130,230,18432
mvva 60,152,112
tv 134,230,22528
mvva 60,156,120
tv 146,230,26624
mvva 56,156,88
tv 150,230,30720
mvva 56,152,80
mvva 56,140,72
mvva 56,136,64
mvva 52,136,100
mvva 52,140,108
mvva 52,152,116
mvva 52,156,124
mvva 48,156,92
mvva 48,152,84
mvva 48,140,76
tv 156,231,28672
mvva 48,136,68
tv 152,231,24576
s_waitcnt lgkmcnt(2)
mvva 32,128,132
tv 140,231,20480
mvva 32,132,140
tv 136,231,16384
tv 138,231,18432
mvva 32,144,148
tv 142,231,22528
mvva 32,148,156
tv 154,231,26624
mvva 36,148,188
tv 158,231,30720
mvva 36,144,180
mvva 36,132,172
mvva 36,128,164
mvva 40,128,128
mvva 40,132,136
cv 32,32,33
cv 33,34,35
mvva 40,144,144
mvva 40,148,152
mvva 44,148,184
mvva 44,144,176
cv 36,36,37
cv 37,38,39
mvva 44,132,168
tv 148,224,45056
mvva 44,128,160
tv 144,224,40960
cv 40,40,41
s_waitcnt lgkmcnt(2)
mvva 60,136,160
tv 132,224,36864
cv 41,42,43
mvva 60,140,168
tv 128,224,32768
tv 130,224,34816
mvva 60,152,176
tv 134,224,38912
cv 44,44,45
mvva 60,156,184
tv 146,224,43008
cv 45,46,47
mvva 56,156,152
tv 150,224,47104
mvva 56,152,144
mvva 56,140,136
mvva 56,136,128
mvva 52,136,164
cv 46,60,61
cv 47,62,63
mvva 52,140,172
buffer_store_dwordx4 v[44:47],v244,s[8:11],s57 offen offset:3072 sc0 sc1
mvva 52,152,180
mvva 52,156,188
mvva 48,156,156
cv 42,56,57
cv 43,58,59
mvva 48,152,148
buffer_store_dwordx4 v[40:43],v244,s[8:11],s57 offen offset:2048 sc0 sc1
mvva 48,140,140
tv 156,225,45056
mvva 48,136,132
cv 38,52,53
cv 39,54,55
s_waitcnt lgkmcnt(1)
mvva 64,128,68
tv 140,225,36864
mvva 64,132,76
buffer_store_dwordx4 v[36:39],v244,s[8:11],s57 offen offset:1024 sc0 sc1
tv 136,225,32768
tv 138,225,34816
mvva 64,144,84
tv 142,225,38912
cv 34,48,49
mvva 64,148,92
tv 152,225,40960
tv 154,225,43008
mvva 68,148,124
tv 158,225,47104
cv 35,50,51
mvva 68,144,116
buffer_store_dwordx4 v[32:35],v244,s[8:11],s57 offen offset:0 sc0 sc1
mvva 68,132,108
mvva 68,128,100
mvva 72,128,64
mvva 72,132,72
mvva 72,144,80
mvva 72,148,88
mvva 76,148,120
mvva 76,144,112
mvva 76,132,104
tv 148,224,61440
mvva 76,128,96
tv 144,224,57344
s_waitcnt lgkmcnt(2)
mvva 92,136,96
tv 132,224,53248
mvva 92,140,104
tv 128,224,49152
tv 130,224,51200
mvva 92,152,112
tv 134,224,55296
mvva 92,156,120
tv 146,224,59392
mvva 88,156,88
tv 150,224,63488
mvva 88,152,80
mvva 88,140,72
mvva 88,136,64
mvva 84,136,100
mvva 84,140,108
mvva 84,152,116
mvva 84,156,124
mvva 80,156,92
mvva 80,152,84
mvva 80,140,76
tv 156,225,61440
mvva 80,136,68
tv 152,225,57344
s_waitcnt lgkmcnt(2)
mvva 64,128,132
tv 140,225,53248
mvva 64,132,140
tv 136,225,49152
tv 138,225,51200
mvva 64,144,148
tv 142,225,55296
mvva 64,148,156
tv 154,225,59392
mvva 68,148,188
tv 158,225,63488
mvva 68,144,180
mvva 68,132,172
mvva 68,128,164
s_xor_b32 s82,s59,12
mvva 72,128,128
s_mul_i32 s82,s82,12288
cv 64,64,65
s_add_u32 s82,s82,0
mvva 72,132,136
lv 16,244,82,0
mvva 72,144,144
cv 65,66,67
cv 68,68,69
mvva 72,148,152
cv 69,70,71
mvva 76,148,184
mvva 76,144,176
mvva 76,132,168
lv 20,244,82,1024
mvva 76,128,160
tv 144,234,24576
s_waitcnt lgkmcnt(1)
mvva 92,136,160
tv 132,234,20480
tv 134,234,22528
mvva 92,140,168
tv 128,234,16384
tv 130,234,18432
mvva 92,152,176
cv 72,72,73
cv 73,74,75
mvva 92,156,184
lv 24,244,82,2048
tv 146,234,26624
mvva 88,156,152
tv 148,234,28672
tv 150,234,30720
mvva 88,152,144
cv 76,76,77
cv 77,78,79
mvva 88,140,136
cv 78,92,93
cv 79,94,95
mvva 88,136,128
mvva 84,136,164
lv 28,244,82,3072
mvva 84,140,172
mvva 84,152,180
mvva 84,156,188
mvva 80,156,156
buffer_store_dwordx4 v[76:79],v244,s[8:11],s58 offen offset:3072 sc0 sc1
mvva 80,152,148
cv 74,88,89
cv 75,90,91
mvva 80,140,140
tv 156,235,28672
mvva 80,136,132
cv 70,84,85
cv 71,86,87
s_waitcnt lgkmcnt(1)
mvva 96,128,4
buffer_store_dwordx4 v[72:75],v244,s[8:11],s58 offen offset:2048 sc0 sc1
tv 140,235,20480
mvva 96,132,12
tv 136,235,16384
tv 138,235,18432
mvva 96,144,20
tv 142,235,22528
cv 66,80,81
mvva 96,148,28
tv 152,235,24576
tv 154,235,26624
mvva 100,148,60
tv 158,235,30720
cv 67,82,83
mvva 100,144,52
buffer_store_dwordx4 v[68:71],v244,s[8:11],s58 offen offset:1024 sc0 sc1
mvva 100,132,44
mvva 100,128,36
mvva 104,128,0
mvva 104,132,8
buffer_store_dwordx4 v[64:67],v244,s[8:11],s58 offen offset:0 sc0 sc1
mvva 104,144,16
mvva 104,148,24
mvva 108,148,56
mvva 108,144,48
mvva 108,132,40
tv 148,226,28672
mvva 108,128,32
tv 144,226,24576
s_waitcnt lgkmcnt(2)
mvva 124,136,32
tv 132,226,20480
mvva 124,140,40
tv 128,226,16384
tv 130,226,18432
mvva 124,152,48
tv 134,226,22528
mvva 124,156,56
tv 146,226,26624
mvva 120,156,24
tv 150,226,30720
mvva 120,152,16
mvva 120,140,8
mvva 120,136,0
mvva 116,136,36
mvva 116,140,44
s_waitcnt vmcnt(7)
v_max_u32_e32 v248,v16,v17
mvva 116,152,52
v_max3_u32 v248,v248,v18,v19
s_waitcnt vmcnt(6)
v_max3_u32 v248,v248,v20,v21
v_max3_u32 v248,v248,v22,v23
mvva 116,156,60
s_waitcnt vmcnt(5)
v_max3_u32 v248,v248,v24,v25
v_max3_u32 v248,v248,v26,v27
s_waitcnt vmcnt(4)
v_max3_u32 v248,v248,v28,v29
mvva 112,156,28
v_max3_u32 v248,v248,v30,v31
v_cmp_eq_u32_e32 vcc,-1,v248
mvva 112,152,20
s_cbranch_vccnz L_spx0e
L_sprx0e:
mvva 112,140,12
tv 156,227,28672
tv 158,227,30720
v_lshlrev_b32_e32 v251,16,v18
v_and_b32_e32 v255,4294901760,v18
mvva 112,136,4
tv 152,227,24576
tv 154,227,26624
s_waitcnt lgkmcnt(4)
mvvv 96,128,164
v_lshlrev_b32_e32 v253,16,v19
v_and_b32_e32 v242,4294901760,v19
mvvv 96,132,172
tv 136,227,16384
tv 138,227,18432
mvvv 96,144,180
tv 140,227,20480
tv 142,227,22528
v_add_f32_e32 v112,v112,v251
mvvv 96,148,188
v_add_f32_e32 v113,v113,v255
v_add_f32_e32 v114,v114,v253
mvvv 100,148,220
v_add_f32_e32 v115,v115,v242
v_lshlrev_b32_e32 v251,16,v22
mvvv 100,144,212
v_and_b32_e32 v255,4294901760,v22
v_lshlrev_b32_e32 v253,16,v23
mvvv 100,132,204
v_and_b32_e32 v242,4294901760,v23
v_add_f32_e32 v116,v116,v251
v_add_f32_e32 v117,v117,v255
mvvv 100,128,196
v_add_f32_e32 v118,v118,v253
v_add_f32_e32 v119,v119,v242
mvvv 104,128,160
v_lshlrev_b32_e32 v251,16,v26
v_and_b32_e32 v255,4294901760,v26
mvvv 104,132,168
v_lshlrev_b32_e32 v253,16,v27
v_and_b32_e32 v242,4294901760,v27
mvvv 104,144,176
v_add_f32_e32 v120,v120,v251
v_add_f32_e32 v121,v121,v255
v_add_f32_e32 v122,v122,v253
mvvv 104,148,184
v_add_f32_e32 v123,v123,v242
v_lshlrev_b32_e32 v251,16,v30
mvvv 108,148,216
v_and_b32_e32 v255,4294901760,v30
v_lshlrev_b32_e32 v253,16,v31
mvvv 108,144,208
v_and_b32_e32 v242,4294901760,v31
v_add_f32_e32 v124,v124,v251
mvvv 108,132,200
v_add_f32_e32 v125,v125,v255
v_add_f32_e32 v126,v126,v253
v_add_f32_e32 v127,v127,v242
mvvv 108,128,192
tv 144,230,57344
v_lshlrev_b32_e32 v251,16,v16
s_waitcnt lgkmcnt(1)
mvvv 124,136,192
tv 132,230,53248
v_and_b32_e32 v255,4294901760,v16
mvvv 124,140,200
tv 128,230,49152
tv 130,230,51200
mvvv 124,152,208
tv 134,230,55296
v_lshlrev_b32_e32 v253,16,v17
mvvv 124,156,216
tv 146,230,59392
v_and_b32_e32 v242,4294901760,v17
mvvv 120,156,184
tv 148,230,61440
tv 150,230,63488
mvvv 120,152,176
v_add_f32_e32 v96,v96,v251
v_add_f32_e32 v97,v97,v255
mvvv 120,140,168
v_add_f32_e32 v98,v98,v253
v_add_f32_e32 v99,v99,v242
mvvv 120,136,160
v_lshlrev_b32_e32 v251,16,v20
v_and_b32_e32 v255,4294901760,v20
mvvv 116,136,196
v_lshlrev_b32_e32 v253,16,v21
v_and_b32_e32 v242,4294901760,v21
mvvv 116,140,204
v_add_f32_e32 v100,v100,v251
v_add_f32_e32 v101,v101,v255
mvvv 116,152,212
v_add_f32_e32 v102,v102,v253
v_add_f32_e32 v103,v103,v242
mvvv 116,156,220
v_lshlrev_b32_e32 v251,16,v24
v_and_b32_e32 v255,4294901760,v24
mvvv 112,156,188
v_lshlrev_b32_e32 v253,16,v25
v_and_b32_e32 v242,4294901760,v25
mvvv 112,152,180
v_add_f32_e32 v104,v104,v251
v_add_f32_e32 v105,v105,v255
mvvv 112,140,172
tv 156,231,61440
v_add_f32_e32 v106,v106,v253
mvvv 112,136,164
tv 152,231,57344
v_add_f32_e32 v107,v107,v242
s_waitcnt lgkmcnt(2)
mvva 96,128,196
tv 140,231,53248
v_lshlrev_b32_e32 v251,16,v28
mvva 96,132,204
tv 136,231,49152
tv 138,231,51200
mvva 96,144,212
tv 142,231,55296
v_and_b32_e32 v255,4294901760,v28
mvva 96,148,220
tv 154,231,59392
v_lshlrev_b32_e32 v253,16,v29
mvva 100,148,252
tv 158,231,63488
v_and_b32_e32 v242,4294901760,v29
mvva 100,144,244
v_add_f32_e32 v108,v108,v251
v_add_f32_e32 v109,v109,v255
mvva 100,132,236
v_add_f32_e32 v110,v110,v253
v_add_f32_e32 v111,v111,v242
mvva 100,128,228
mvva 104,128,192
mvva 104,132,200
mvva 104,144,208
mvva 104,148,216
mvva 108,148,248
mvva 108,144,240
mvva 108,132,232
tv 148,226,45056
mvva 108,128,224
tv 144,226,40960
s_waitcnt lgkmcnt(2)
mvva 124,136,224
s_xor_b32 s83,s59,8
tv 132,226,36864
s_mul_i32 s83,s83,12288
mvva 124,140,232
s_add_u32 s83,s83,4096
tv 128,226,32768
tv 130,226,34816
mvva 124,152,240
lv 48,244,83,0
tv 134,226,38912
mvva 124,156,248
tv 146,226,43008
mvva 120,156,216
lv 52,244,83,1024
tv 150,226,47104
mvva 120,152,208
mvva 120,140,200
lv 56,244,83,2048
mvva 120,136,192
mvva 116,136,228
lv 60,244,83,3072
mvva 116,140,236
mvva 116,152,244
s_xor_b32 s84,s59,4
mvva 116,156,252
s_mul_i32 s84,s84,12288
mvva 112,156,220
s_add_u32 s84,s84,8192
mvva 112,152,212
lv 80,244,84,0
mvva 112,140,204
tv 156,227,45056
mvva 112,136,196
lv 84,244,84,1024
tv 152,227,40960
s_waitcnt lgkmcnt(2)
)"
R"(mvva 96,128,68
tv 140,227,36864
mvva 96,132,76
lv 88,244,84,2048
tv 136,227,32768
tv 138,227,34816
mvva 96,144,84
L_vs8:
s_cbranch_execz L_vr1
tv 142,227,38912
mvva 96,148,92
lv 92,244,84,3072
tv 154,227,43008
mvva 100,148,124
tv 158,227,47104
mvva 100,144,116
mvva 100,132,108
mvva 100,128,100
mvva 104,128,64
mvva 104,132,72
mvva 104,144,80
mvva 104,148,88
mvva 108,148,120
mvva 108,144,112
mvva 108,132,104
tv 148,226,61440
mvva 108,128,96
tv 144,226,57344
s_waitcnt lgkmcnt(2)
mvva 124,136,96
tv 132,226,53248
mvva 124,140,104
tv 128,226,49152
tv 130,226,51200
mvva 124,152,112
tv 134,226,55296
mvva 124,156,120
tv 146,226,59392
mvva 120,156,88
tv 150,226,63488
mvva 120,152,80
mvva 120,140,72
mvva 120,136,64
mvva 116,136,100
mvva 116,140,108
mvva 116,152,116
mvva 116,156,124
mvva 112,156,92
mvva 112,152,84
mvva 112,140,76
tv 156,227,61440
mvva 112,136,68
tv 152,227,57344
s_waitcnt lgkmcnt(2)
mvva 96,128,132
tv 140,227,53248
mvva 96,132,140
tv 136,227,49152
tv 138,227,51200
mvva 96,144,148
tv 142,227,55296
mvva 96,148,156
tv 154,227,59392
mvva 100,148,188
tv 158,227,63488
mvva 100,144,180
mvva 100,132,172
mvva 100,128,164
mvva 104,128,128
mvva 104,132,136
mvva 104,144,144
mvva 104,148,152
s_waitcnt vmcnt(7)
v_max_u32_e32 v248,v48,v49
mvva 108,148,184
v_max3_u32 v248,v248,v50,v51
s_waitcnt vmcnt(6)
v_max3_u32 v248,v248,v52,v53
mvva 108,144,176
v_max3_u32 v248,v248,v54,v55
s_waitcnt vmcnt(5)
v_max3_u32 v248,v248,v56,v57
mvva 108,132,168
v_max3_u32 v248,v248,v58,v59
s_waitcnt vmcnt(4)
v_max3_u32 v248,v248,v60,v61
mvva 108,128,160
v_max3_u32 v248,v248,v62,v63
v_cmp_eq_u32_e32 vcc,-1,v248
s_waitcnt lgkmcnt(0)
mvva 124,136,160
s_cbranch_vccnz L_spx1e
L_sprx1e:
mvva 124,140,168
mvva 124,152,176
mvva 124,156,184
s_waitcnt vmcnt(3)
v_max_u32_e32 v248,v80,v81
mvva 120,156,152
v_max3_u32 v248,v248,v82,v83
s_waitcnt vmcnt(2)
v_max3_u32 v248,v248,v84,v85
mvva 120,152,144
v_max3_u32 v248,v248,v86,v87
s_waitcnt vmcnt(1)
v_max3_u32 v248,v248,v88,v89
mvva 120,140,136
v_max3_u32 v248,v248,v90,v91
s_waitcnt vmcnt(0)
v_max3_u32 v248,v248,v92,v93
mvva 120,136,128
v_max3_u32 v248,v248,v94,v95
v_cmp_eq_u32_e32 vcc,-1,v248
mvva 116,136,164
s_cbranch_vccnz L_spx2e
L_sprx2e:
mvva 116,140,172
mvva 116,152,180
mvva 116,156,188
mvva 112,156,156
mvva 112,152,148
mvva 112,140,140
mvva 112,136,132
mvvv 108,238,60
mvvv 108,238,92
mvvv 124,236,60
mvvv 124,236,92
mvvv 104,238,56
mvvv 104,238,88
mvvv 120,236,56
mvvv 120,236,88
mvvv 100,238,52
mvvv 100,238,84
cv 108,108,109
cv 109,110,111
cv 110,124,125
cv 111,126,127
cv 104,104,105
v_permlane16_swap_b32_e32 v108,v110
mvvv 116,236,52
v_permlane16_swap_b32_e32 v109,v111
cv 105,106,107
cv 106,120,121
cv 107,122,123
mvvv 116,236,84
buffer_store_dwordx4 v[108:111],v243,s[44:47],s73 offen
v_permlane16_swap_b32_e32 v104,v106
v_permlane16_swap_b32_e32 v105,v107
mvvv 96,238,48
cv 100,100,101
cv 101,102,103
mvvv 96,238,80
buffer_store_dwordx4 v[104:107],v243,s[44:47],s72 offen
cv 102,116,117
cv 103,118,119
mvvv 112,236,48
v_permlane16_swap_b32_e32 v100,v102
v_permlane16_swap_b32_e32 v101,v103
mvvv 112,236,80
buffer_store_dwordx4 v[100:103],v243,s[44:47],s71 offen
cv 96,96,97
cv 97,98,99
s_nop 4
cv 98,112,113
cv 99,114,115
s_nop 0
v_permlane16_swap_b32_e32 v96,v98
v_permlane16_swap_b32_e32 v97,v99
buffer_store_dwordx4 v[96:99],v243,s[44:47],s70 offen
s_endpgm
L_odd:
L_vs9:
s_cbranch_execz L_vs13
s_lshl_b32 s86,s21,2
s_sub_u32 s87,s13,s67
s_add_u32 s86,s86,s12
mvaa 64,200,132
s_lshl_b32 s86,s86,8
v_add_u32_e32 v252,s87,v244
s_add_u32 s86,s86,L_odd-L_att_pc+1024
v_lshl_add_u32 v226,v244,3,s86
s_lshl_b32 s86,s12,1
s_bfe_u32 s87,s21,65539
mvaa 64,208,136
s_add_u32 s86,s86,s87
v_min_u32_e32 v226,L_att_end-L_att_pc-4,v226
s_lshl_b32 s86,s86,7
s_add_u32 s86,s86,L_pend2-L_att_pc
v_writelane_b32 v226,s86,2
mvaa 64,216,140
s_mov_b64 exec,7
global_load_dword v253,v226,s[96:97]
s_mov_b64 exec,-1
cvaa 68,220,140,232
s_add_u32 m0,s52,32768
v_and_b32_e32 v224,16,v244
v_lshlrev_b32_e32 v224,5,v224
mvaa 68,212,136
s_xor_b32 s51,s62,0
v_lshrrev_b32_e32 v239,4,v244
v_lshrrev_b32_e32 v236,3,v239
mvaa 68,204,132
s_lshl_b32 s87,s12,13
v_and_b32_e32 v237,112,v244
v_and_b32_e32 v238,48,v239
mvaa 68,196,128
s_xor_b32 s87,s87,8192
ra 64,225,0
v_lshlrev_b32_e32 v238,1,v238
cvaa 96,224,128,232
buffer_load_dword v253,v224,s[8:11],s53 offen sc1
mvaa 96,232,132
s_mov_b32 s84,7
ra 68,225,1024
v_xor_b32_e32 v237,v237,v238
mvaa 96,240,136
s_add_u32 s81,s81,1
ra 72,225,2048
v_lshl_add_u32 v245,v236,11,v237
mvaa 96,248,140
s_mov_b32 s84,5
v_add_u32_e32 v246,15360,v245
v_add_u32_e32 v249,30720,v245
cvaa 100,252,140,232
s_andn2_b32 s69,64,s54
ra 76,225,3072
s_waitcnt vmcnt(9)
v_add_u32_e32 v247,46080,v245
mvaa 100,244,136
dl 245,24,51,0
mvaa 100,236,132
s_add_u32 s88,s14,4096
ra 80,225,4096
cv 8,8,9
mvaa 100,228,128
s_lshl_b32 s35,s19,1
ra 84,225,5120
cv 9,10,11
cvaa 72,192,144,232
s_lshr_b32 s33,40569,s35
ra 88,225,6144
cv 10,12,13
mvaa 72,200,148
s_and_b32 s33,s33,3
ra 92,225,7168
v_add_u32_e32 v250,4096,v252
mvaa 72,208,152
dl 246,24,51,1024
mvaa 72,216,156
s_lshl_b32 s33,s33,13
ra 96,225,8192
v_mov_b32_e32 v251,4
cvaa 76,220,156,232
s_lshr_b32 s34,59294,s35
ra 100,225,9216
s_and_b32 s34,s34,3
mvaa 76,212,152
s_lshl_b32 s34,s34,13
ra 104,225,10240
s_xor_b32 s35,s33,s34
mvaa 76,204,148
s_and_b32 s98,s19,3
ra 108,225,11264
s_lshl_b32 s98,s98,13
mvaa 76,196,144
dl 249,24,51,2048
cvaa 104,224,144,232
s_mov_b32 s74,0x300003
ra 112,225,12288
s_mov_b32 s75,0x30000300
mvaa 104,232,148
s_lshl_b32 s71,s12,15
ra 116,225,13312
s_add_u32 s71,s71,s54
mvaa 104,240,152
s_mov_b64 s[44:45],s[30:31]
ra 120,225,14336
s_mov_b32 s46,4194304
mvaa 104,248,156
s_mov_b32 s47,131072
ex 14
cvaa 108,252,156,232
dl 247,24,51,3072
mvaa 108,244,152
s_add_u32 m0,s52,49152
s_xor_b32 s51,s63,0
ra 124,225,15360
mvaa 108,236,148
s_lshl_b32 s70,s23,11
ex 15
mvaa 108,228,144
s_add_u32 s70,s70,s71
ex 40
cvaa 80,192,160,232
ex 41
mvaa 80,200,164
dl 245,24,51,0
mvaa 80,208,168
ex 42
mvaa 80,216,172
ex 43
cvaa 84,220,172,232
ex 44
mvaa 84,212,168
ex 45
mvaa 84,204,164
dl 246,24,51,1024
mvaa 84,196,160
ex 46
cvaa 112,224,160,232
ex 47
mvaa 112,232,164
ex 16
mvaa 112,240,168
ex 17
mvaa 112,248,172
dl 249,24,51,2048
cvaa 116,252,172,232
ex 18
mvaa 116,244,168
ex 19
mvaa 116,236,164
ex 20
mvaa 116,228,160
ex 21
cvaa 88,192,176,232
dl 247,24,51,3072
s_waitcnt vmcnt(8)
v_cmp_ne_u32_e32 vcc,4,v253
mvaa 88,200,180
s_cbranch_vccnz L_spbo
L_sprbo:
ex 22
mvaa 88,208,184
s_xor_b32 s50,s66,262144
ex 23
mvaa 88,216,188
ex 48
cvaa 92,220,188,232
ex 49
mvaa 92,212,184
la 128,252,50,0
mvaa 92,204,180
s_waitcnt lgkmcnt(0)
s_barrier
mvaa 92,196,176
s_mov_b32 m0,s72
ex 50
cvaa 120,224,176,232
s_lshl_b32 s72,s22,11
ex 51
mvaa 120,232,180
s_add_u32 s72,s72,s71
ex 52
mvaa 120,240,184
ds 255,8,76,0
mvaa 120,248,188
s_add_u32 s71,s70,131072
ex 53
cvaa 124,252,188,232
ex 54
mvaa 124,244,184
ex 55
mvaa 124,236,180
ex 24
mvaa 124,228,176
ds 255,8,76,1024
maav 0,64,0
s_cmp_lg_u32 s19,7
s_cbranch_scc1 L_j7o
s_nop 1
s_lshl_b32 s86,s12,12
s_add_u32 s86,s86,131072
v_add_u32_e32 v236,s86,v244
ra 160,236,0
ra 164,236,1024
ra 168,236,2048
ra 172,236,3072
ra 176,236,16384
ra 180,236,17408
ra 184,236,18432
ra 188,236,19456
v_add_u32_e32 v224,s94,v224
s_waitcnt lgkmcnt(0)
L_j7o:
ex 25
maav 0,96,4
ex 26
maav 4,68,0
ex 27
maav 4,100,4
cv 11,14,15
cv 12,40,41
maav 8,72,0
ds 255,8,76,2048
maav 8,104,4
cv 13,42,43
cv 14,44,45
maav 12,76,0
cv 15,46,47
cv 16,16,17
maav 12,108,4
ex 28
v_smfmac_f32_16x16x64_bf16 v[144:147],v[228:231],v[0:7],v248
cv 17,18,19
maav 16,80,0
ds 255,8,76,3072
maav 16,112,4
s_mov_b32 m0,s73
ex 29
maav 20,84,0
s_add_u32 s73,s72,131072
ex 30
maav 20,116,4
ex 31
maav 24,88,0
ex 56
maav 24,120,4
ds 255,8,77,0
maav 28,92,0
ex 57
maav 28,124,4
ex 58
maav 32,64,8
ex 59
maav 32,96,12
ex 60
maav 36,68,8
ds 255,8,77,1024
maav 36,100,12
cv 18,20,21
cv 19,22,23
maav 40,72,8
cv 20,48,49
cv 21,50,51
maav 40,104,12
cv 22,52,53
cv 23,54,55
maav 44,76,8
ex 61
maav 44,108,12
ds 255,8,77,2048
v_smfmac_f32_16x16x64_bf16 v[148:151],v[228:231],v[8:15],v248
cv 24,24,25
maav 48,80,8
ex 62
maav 48,112,12
ex 63
maav 52,84,8
ra 192,244,0
cv 25,26,27
maav 52,116,12
ds 255,8,77,3072
maav 56,88,8
cv 26,28,29
cv 27,30,31
maav 56,120,12
cv 28,56,57
cv 29,58,59
maav 60,92,8
cv 30,60,61
cv 31,62,63
maav 60,124,12
ex 64
mvav 160,64,16
la 132,252,50,2048
mvav 160,96,20
ex 65
mvav 164,68,16
ex 66
mvav 164,100,20
ex 67
mvav 168,72,16
ex 68
mvav 168,104,20
la 136,250,50,0
mvav 172,76,16
ex 69
mvav 172,108,20
ex 70
v_smfmac_f32_16x16x64_bf16 v[152:155],v[228:231],v[16:23],v248
ra 200,244,2048
mvav 176,80,16
ra 208,244,4096
cv 64,64,65
mvav 176,112,20
la 140,250,50,2048
mvav 180,84,16
ra 216,244,6144
cv 65,66,67
mvav 180,116,20
ex 71
mvav 184,88,16
ex 96
mvav 184,120,20
s_barrier
ex 97
mvav 188,92,16
la 156,250,50,3072
mvav 188,124,20
s_add_u32 m0,s52,131072
ra 196,244,1024
s_xor_b32 s51,s62,1048576
mvav 192,64,24
ra 204,244,3072
cv 66,68,69
mvav 192,96,28
ra 212,244,5120
cv 67,70,71
mvav 196,68,24
ra 220,244,7168
cv 68,96,97
mvav 196,100,28
la 152,250,50,1024
mvav 200,72,24
ex 98
mvav 200,104,28
ex 99
mvav 204,76,24
ra 224,244,8192
cv 69,98,99
mvav 204,108,28
ra 232,244,10240
v_add_u32_e32 v6,s87,v244
v_smfmac_f32_16x16x64_bf16 v[156:159],v[228:231],v[24:31],v248
ra 240,244,12288
mvav 208,80,24
la 148,252,50,3072
mvav 208,112,28
s_xor_b32 s87,s52,4096
ra 248,244,14336
v_add_u32_e32 v7,98304,v6
mvav 212,84,24
s_add_u32 s87,s87,150528
ra 252,244,15360
v_add_u32_e32 v5,65536,v244
mvav 212,116,28
ra 244,244,13312
v_add_u32_e32 v32,s87,v244
mvav 216,88,24
ra 228,244,9216
v_and_b32_e32 v224,960,v244
mvav 216,120,28
la 144,252,50,1024
mvav 220,92,24
s_add_u32 s50,s50,s55
ra 236,244,11264
mvav 220,124,28
ra 64,225,32768
s_waitcnt lgkmcnt(8)
cvaa 24,192,176,232
dl 245,24,51,0
mvaa 24,200,180
ra 96,225,40960
mvaa 24,208,184
ra 68,225,33792
mvaa 24,216,188
dl 246,24,51,1024
cvaa 16,192,160,128
ra 100,225,41984
mvaa 16,200,164
ra 72,225,34816
mvaa 16,208,168
dl 249,24,51,2048
mvaa 16,216,172
ra 104,225,43008
cvaa 28,196,176,232
ra 76,225,35840
mvaa 28,204,180
dl 247,24,51,3072
mvaa 28,212,184
s_add_u32 m0,s52,147456
s_xor_b32 s51,s63,1048576
ra 108,225,44032
mvaa 28,220,188
ra 80,225,36864
cvaa 20,196,160,132
dl 245,24,51,0
mvaa 20,204,164
ra 112,225,45056
mvaa 20,212,168
ra 192,244,16384
mvaa 20,220,172
dl 246,24,51,1024
cvaa 56,224,176,232
ra 200,244,18432
s_waitcnt lgkmcnt(7)
mvaa 56,232,180
ra 208,244,20480
mvaa 56,240,184
dl 249,24,51,2048
mvaa 56,248,188
ra 216,244,22528
cvaa 48,224,160,136
ra 196,244,17408
mvaa 48,232,164
L_vs10:
s_cbranch_execz L_vs14
ra 204,244,19456
mvaa 48,240,168
ra 212,244,21504
mvaa 48,248,172
ra 220,244,23552
cvaa 60,228,176,232
ra 224,244,24576
mvaa 60,236,180
ra 232,244,26624
mvaa 60,244,184
ra 240,244,28672
mvaa 60,252,188
ra 248,244,30720
cvaa 52,228,160,140
ra 84,225,37888
mvaa 52,236,164
ra 116,225,46080
mvaa 52,244,168
la 160,252,50,0
ra 228,244,25600
mvaa 52,252,172
ra 236,244,27648
s_waitcnt lgkmcnt(10)
cvaa 8,192,176,128
ra 244,244,29696
mvaa 8,200,180
la 164,252,50,2048
ra 252,244,31744
mvaa 8,208,184
ra 88,225,38912
mvaa 8,216,188
ra 120,225,47104
cvaa 12,196,176,132
la 168,250,50,0
mvaa 12,204,180
ra 92,225,39936
ra 124,225,48128
s_waitcnt lgkmcnt(9)
mvaa 12,212,184
ex 100
mvaa 12,220,188
la 172,250,50,2048
cvaa 40,224,176,136
ex 101
mvaa 40,232,180
ex 102
mvaa 40,240,184
ex 103
mvaa 40,248,188
cv 70,100,101
cv 71,102,103
s_waitcnt lgkmcnt(4)
cvaa 44,228,176,140
ex 72
mvaa 44,236,180
s_waitcnt vmcnt(18) lgkmcnt(0)
s_barrier
ex 73
mvaa 44,244,184
la 176,252,50,1024
mvaa 44,252,188
ex 74
maav 0,64,64
ex 75
maav 0,96,68
la 188,250,50,3072
maav 4,68,64
ra 192,5,0
cv 72,72,73
maav 4,100,68
ra 196,5,1024
cv 73,74,75
maav 8,72,64
la 184,250,50,1024
ds_write_b128 v6,a[0:3] offset:0
maav 8,104,68
ex 76
maav 12,76,64
ex 77
maav 12,108,68
la 180,252,50,3072
ds_write_b128 v6,a[4:7] offset:1024
v_smfmac_f32_16x16x64_bf16 v[144:147],v[228:231],v[64:71],v248
cv 74,76,77
maav 16,80,64
ex 78
maav 16,112,68
ds_write_b128 v6,a[8:11] offset:2048
ex 79
maav 20,84,64
ex 104
maav 20,116,68
ds_write_b128 v6,a[12:15] offset:3072
ex 105
maav 24,88,64
ex 106
ex 107
ex 108
maav 24,120,68
ds_write_b128 v6,a[16:19] offset:4096
cv 75,78,79
maav 28,92,64
ex 109
ex 110
ex 111
maav 28,124,68
cv 76,104,105
cv 77,106,107
cv 78,108,109
cv 79,110,111
maav 32,64,72
ds_write_b128 v6,a[20:23] offset:5120
maav 32,96,76
ex 80
maav 36,68,72
ex 81
maav 36,100,76
ds_write_b128 v6,a[24:27] offset:6144
maav 40,72,72
ex 82
maav 40,104,76
ex 83
maav 44,76,72
ds_write_b128 v6,a[28:31] offset:7168
maav 44,108,76
cv 80,80,81
cv 81,82,83
v_smfmac_f32_16x16x64_bf16 v[148:151],v[228:231],v[72:79],v248
ex 84
maav 48,80,72
ds_write_b128 v7,a[32:35] offset:0
maav 48,112,76
ex 85
ex 86
ex 87
maav 52,84,72
ex 112
ex 113
maav 52,116,76
ds_write_b128 v7,a[36:39] offset:1024
cv 82,84,85
maav 56,88,72
ex 114
ex 115
ex 116
maav 56,120,76
ex 117
ex 118
ex 119
maav 60,92,72
ds_write_b128 v7,a[40:43] offset:2048
cv 83,86,87
maav 60,124,76
cv 84,112,113
cv 85,114,115
cv 86,116,117
cv 87,118,119
mvav 160,64,80
ex 88
mvav 160,96,84
ds_write_b128 v7,a[44:47] offset:3072
mvav 164,68,80
v_add_f32_e32 v130,v144,v145
v_add_f32_e32 v131,v148,v149
mvav 164,100,84
ex 89
mvav 168,72,80
ds_write_b128 v7,a[48:51] offset:4096
mvav 168,104,84
ex 90
mvav 172,76,80
ex 91
mvav 172,108,84
ds_write_b128 v32,v[128:131]
v_smfmac_f32_16x16x64_bf16 v[152:155],v[228:231],v[80:87],v248
ex 92
ex 93
mvav 176,80,80
ds_write_b128 v7,a[52:55] offset:5120
ex 94
mvav 176,112,84
ra 200,5,2048
ra 204,5,3072
ex 95
cv 88,88,89
mvav 180,84,80
ra 208,5,4096
ra 212,5,5120
cv 89,90,91
cv 90,92,93
ex 120
mvav 180,116,84
ds_write_b128 v7,a[56:59] offset:6144
ex 121
mvav 184,88,80
ex 122
ex 123
cv 91,94,95
mvav 184,120,84
ex 124
ex 125
mvav 188,92,80
ds_write_b128 v7,a[60:63] offset:7168
ex 126
mvav 188,124,84
ra 216,5,6144
ra 220,5,7168
ra 224,5,8192
ra 232,5,10240
ex 127
cv 92,120,121
mvav 192,64,88
ra 240,5,12288
ra 248,5,14336
ra 228,5,9216
ra 236,5,11264
ra 244,5,13312
ra 252,5,15360
cv 93,122,123
cv 94,124,125
cv 95,126,127
v_xor_b32_e32 v6,8192,v6
s_nop 0
mvav 192,96,92
ex 16
mvav 196,68,88
s_waitcnt lgkmcnt(10)
s_xor_b32 s87,s87,0
mvav 196,100,92
s_xor_b32 s50,s66,294912
s_barrier
v_xor_b32_e32 v7,8192,v7
mvav 200,72,88
la 4,252,50,0
ra 64,5,16384
ra 96,5,24576
rv 64,6,0
rv 68,6,1024
rv 72,6,2048
mvav 200,104,92
rv 76,6,3072
rv 80,6,4096
rv 84,6,5120
ex 17
ex 18
mvav 204,76,88
rv 96,7,0
rv 100,7,1024
ex 19
mvav 204,108,92
la 12,252,50,2048
rv 104,7,2048
rv 108,7,3072
cv 16,16,17
v_smfmac_f32_16x16x64_bf16 v[156:159],v[228:231],v[88:95],v248
rv 112,7,4096
rv 116,7,5120
ex 20
ex 21
mvav 208,80,88
rv 120,7,6144
rv 124,7,7168
ex 22
ex 23
mvav 208,112,92
la 20,250,50,0
ra 68,5,17408
ra 100,5,25600
cv 17,18,19
cv 18,20,21
mvav 212,84,88
ra 72,5,18432
ra 104,5,26624
ex 48
ex 49
mvav 212,116,92
ra 76,5,19456
ra 108,5,27648
ex 50
ex 51
mvav 216,88,88
la 28,250,50,2048
ra 80,5,20480
ra 112,5,28672
ra 84,5,21504
mvav 216,120,92
ra 116,5,29696
cv 19,22,23
ex 52
ex 53
mvav 220,92,88
ra 88,5,22528
cv 20,48,49
ex 54
ex 55
mvav 220,124,92
la 60,250,50,3072
ra 120,5,30720
cv 21,50,51
cv 22,52,53
cv 23,54,55
s_bitcmp1_b32 s82,s81
s_cbranch_scc1 L_pend2
L_ret_eb5:
mvav 160,192,16
ra 92,5,23552
rv 88,6,6144
ra 124,5,31744
rv 92,6,7168
ex 24
s_waitcnt lgkmcnt(10)
mvav 160,224,20
ex 25
ex 26
v_smfmac_f32_16x16x64_bf16 v[152:155],v[228:231],v[16:23],v248
ex 27
v_xor_b32_e32 v32,4096,v32
mvav 164,196,16
la 52,250,50,1024
ds_read_b128 v[236:239],v32
cv 24,24,25
ex 28
mvav 164,228,20
ex 29
cv 25,26,27
ex 30
ex 31
mvav 168,200,16
ex 56
ex 57
mvav 168,232,20
la 44,252,50,3072
cv 26,28,29
ex 58
ex 59
mvav 172,204,16
ex 60
ex 61
ex 62
mvav 172,236,20
s_waitcnt lgkmcnt(0)
s_barrier
cv 27,30,31
ex 63
mvav 176,208,16
la 36,252,50,1024
cv 28,56,57
cv 29,58,59
cv 30,60,61
cv 31,62,63
mvav 176,240,20
s_add_u32 m0,s52,147456
ex 8
s_xor_b32 s51,s63,1048576
ex 9
ex 10
mvav 180,212,16
s_add_u32 s50,s50,s55
ex 11
cv 8,8,9
cv 9,10,11
ex 12
mvav 180,244,20
dl 247,24,51,3072
ex 13
ex 14
mvav 184,216,16
s_add_u32 m0,s52,0
ex 15
ex 40
ex 41
mvav 184,248,20
s_xor_b32 s51,s64,0
ex 42
ex 43
ex 44
mvav 188,220,16
la 0,252,50,0
ex 45
ex 46
mvav 188,252,20
ex 47
cv 10,12,13
cv 11,14,15
cv 12,40,41
cv 13,42,43
cv 14,44,45
v_smfmac_f32_16x16x64_bf16 v[156:159],v[228:231],v[24:31],v248
cv 15,46,47
v_bfe_u32 v225,v244,1,5
s_nop 0
v_smfmac_f32_16x16x64_bf16 v[156:159],v[228:231],v[8:15],v248
v_lshl_add_u32 v224,v224,1,v225
v_bfe_u32 v225,v244,7,2
v_lshl_add_u32 v224,v225,5,v224
mvav 192,192,24
la 8,252,50,2048
v_xor_b32_e32 v224,s69,v224
v_add_f32_e32 v152,v152,v153
v_add_f32_e32 v137,v152,v238
v_subrev_u32_e32 v236,0x23800000,v137
mvav 192,224,28
)"
R"(v_rcp_f32_e32 v137,v137
mvav 192,64,8
tv 128,224,32768
v_mul_f32_e32 v232,v240,v137
v_mul_f32_e32 v64,v232,v64
v_mul_f32_e32 v65,v232,v65
v_mul_f32_e32 v66,v232,v66
mvav 192,96,12
la 16,250,50,0
v_mul_f32_e32 v67,v232,v67
v_mul_f32_e32 v68,v232,v68
v_mul_f32_e32 v69,v232,v69
v_mul_f32_e32 v70,v232,v70
mvav 196,196,24
v_fmac_f32_e32 v64,v232,v160
v_fmac_f32_e32 v65,v232,v161
v_mul_f32_e32 v71,v232,v71
mvav 196,228,28
v_fmac_f32_e32 v66,v232,v162
v_fmac_f32_e32 v67,v232,v163
v_fmac_f32_e32 v68,v232,v164
v_fmac_f32_e32 v69,v232,v165
v_fmac_f32_e32 v70,v232,v166
mvav 196,68,8
la 24,250,50,2048
v_fmac_f32_e32 v71,v232,v167
cv 160,64,65
cv 161,66,67
cv 162,68,69
cv 163,70,71
mvav 196,100,12
tv 130,224,34816
tv 132,224,36864
mvav 200,200,24
tv 134,224,38912
tv 144,224,40960
v_mul_f32_e32 v72,v232,v72
v_mul_f32_e32 v73,v232,v73
v_mul_f32_e32 v74,v232,v74
mvav 200,232,28
buffer_store_dwordx4 v[160:163],v244,s[8:11],s14 offen offset:0 sc0 sc1
v_mul_f32_e32 v75,v232,v75
v_mul_f32_e32 v76,v232,v76
v_mul_f32_e32 v77,v232,v77
v_mul_f32_e32 v78,v232,v78
mvav 200,72,8
v_fmac_f32_e32 v72,v232,v168
v_fmac_f32_e32 v73,v232,v169
v_mul_f32_e32 v79,v232,v79
mvav 200,104,12
v_fmac_f32_e32 v74,v232,v170
v_fmac_f32_e32 v75,v232,v171
v_fmac_f32_e32 v76,v232,v172
v_fmac_f32_e32 v77,v232,v173
v_fmac_f32_e32 v78,v232,v174
mvav 204,204,24
la 56,250,50,3072
v_fmac_f32_e32 v79,v232,v175
cv 168,72,73
cv 169,74,75
cv 170,76,77
cv 171,78,79
mvav 204,236,28
tv 146,224,43008
tv 148,224,45056
mvav 204,76,8
tv 150,224,47104
v_mul_f32_e32 v80,v232,v80
v_mul_f32_e32 v81,v232,v81
v_mul_f32_e32 v82,v232,v82
mvav 204,108,12
buffer_store_dwordx4 v[168:171],v244,s[8:11],s14 offen offset:2048 sc0 sc1
v_mul_f32_e32 v83,v232,v83
v_mul_f32_e32 v84,v232,v84
v_mul_f32_e32 v85,v232,v85
v_mul_f32_e32 v86,v232,v86
v_mul_f32_e32 v87,v232,v87
mvav 208,208,24
v_fmac_f32_e32 v80,v232,v176
v_fmac_f32_e32 v81,v232,v177
v_fmac_f32_e32 v82,v232,v178
mvav 208,240,28
v_fmac_f32_e32 v83,v232,v179
v_fmac_f32_e32 v84,v232,v180
v_fmac_f32_e32 v85,v232,v181
cv 176,80,81
cv 177,82,83
mvav 208,80,8
la 48,250,50,1024
v_fmac_f32_e32 v86,v232,v182
v_fmac_f32_e32 v87,v232,v183
cv 178,84,85
cv 179,86,87
mvav 208,112,12
v_mul_f32_e32 v88,v232,v88
v_mul_f32_e32 v89,v232,v89
v_mul_f32_e32 v90,v232,v90
mvav 212,212,24
v_mul_f32_e32 v91,v232,v91
v_mul_f32_e32 v92,v232,v92
v_mul_f32_e32 v93,v232,v93
v_mul_f32_e32 v94,v232,v94
v_mul_f32_e32 v95,v232,v95
mvav 212,244,28
buffer_store_dwordx4 v[176:179],v244,s[8:11],s88 offen offset:0 sc0 sc1
v_fmac_f32_e32 v88,v232,v184
v_fmac_f32_e32 v89,v232,v185
v_fmac_f32_e32 v90,v232,v186
v_fmac_f32_e32 v91,v232,v187
cv 184,88,89
mvav 212,84,8
v_fmac_f32_e32 v92,v232,v188
cv 185,90,91
mvav 212,116,12
v_fmac_f32_e32 v93,v232,v189
v_fmac_f32_e32 v94,v232,v190
v_fmac_f32_e32 v95,v232,v191
cv 186,92,93
cv 187,94,95
mvav 216,216,24
la 40,252,50,3072
v_add_f32_e32 v156,v156,v157
v_add_f32_e32 v141,v156,v239
v_subrev_u32_e32 v237,0x23800000,v141
v_rcp_f32_e32 v141,v141
mvav 216,248,28
v_mul_f32_e32 v233,v241,v141
v_mul_f32_e32 v96,v233,v96
mvav 216,88,8
v_mul_f32_e32 v97,v233,v97
v_mul_f32_e32 v98,v233,v98
v_mul_f32_e32 v99,v233,v99
v_mul_f32_e32 v100,v233,v100
mvav 216,120,12
buffer_store_dwordx4 v[184:187],v244,s[8:11],s88 offen offset:2048 sc0 sc1
v_fmac_f32_e32 v96,v233,v192
v_fmac_f32_e32 v97,v233,v193
v_mul_f32_e32 v101,v233,v101
v_mul_f32_e32 v102,v233,v102
v_mul_f32_e32 v103,v233,v103
mvav 220,220,24
v_fmac_f32_e32 v98,v233,v194
v_fmac_f32_e32 v99,v233,v195
mvav 220,252,28
v_fmac_f32_e32 v100,v233,v196
v_fmac_f32_e32 v101,v233,v197
cv 192,96,97
cv 193,98,99
mvav 220,92,8
la 32,252,50,1024
v_fmac_f32_e32 v102,v233,v198
v_fmac_f32_e32 v103,v233,v199
cv 194,100,101
cv 195,102,103
mvav 220,124,12
v_xor_b32_e32 v225,32,v224
v_xor_b32_e32 v226,64,v224
s_waitcnt lgkmcnt(0)
zvva 0,128,128
tv 138,225,34816
tv 142,225,38912
tv 152,225,40960
s_waitcnt vmcnt(36)
mvva 0,132,132
tv 136,225,32768
tv 154,225,43008
mvva 0,144,136
tv 140,225,36864
tv 158,225,47104
mvva 0,148,140
tv 156,225,45056
v_mul_f32_e32 v104,v233,v104
v_mul_f32_e32 v105,v233,v105
v_mul_f32_e32 v106,v233,v106
zvva 4,148,156
buffer_store_dwordx4 v[192:195],v244,s[8:11],s14 offen offset:1024 sc0 sc1
v_mul_f32_e32 v107,v233,v107
v_mul_f32_e32 v108,v233,v108
v_mul_f32_e32 v109,v233,v109
v_mul_f32_e32 v110,v233,v110
mvva 4,144,152
v_fmac_f32_e32 v104,v233,v200
v_fmac_f32_e32 v105,v233,v201
v_fmac_f32_e32 v106,v233,v202
v_mul_f32_e32 v111,v233,v111
mvva 4,132,148
v_fmac_f32_e32 v107,v233,v203
v_fmac_f32_e32 v108,v233,v204
v_fmac_f32_e32 v109,v233,v205
cv 200,104,105
cv 201,106,107
mvva 4,128,144
v_fmac_f32_e32 v110,v233,v206
v_fmac_f32_e32 v111,v233,v207
cv 202,108,109
cv 203,110,111
s_waitcnt vmcnt(16)
zvva 8,128,160
v_cmp_lt_u32_e64 s[42:43],s48,v236
v_mul_f32_e32 v112,v233,v112
s_or_b64 s[40:41],s[40:41],s[42:43]
v_cmp_lt_u32_e64 s[42:43],s48,v237
mvva 8,132,164
s_or_b64 s[40:41],s[40:41],s[42:43]
v_mul_f32_e32 v113,v233,v113
v_mul_f32_e32 v114,v233,v114
v_mul_f32_e32 v115,v233,v115
mvva 8,144,168
dl 245,36,51,0
v_mul_f32_e32 v116,v233,v116
v_mul_f32_e32 v117,v233,v117
v_mul_f32_e32 v118,v233,v118
v_mul_f32_e32 v119,v233,v119
v_mul_f32_e32 v120,v233,v120
mvva 8,148,172
v_fmac_f32_e32 v112,v233,v208
v_fmac_f32_e32 v113,v233,v209
v_fmac_f32_e32 v114,v233,v210
v_fmac_f32_e32 v115,v233,v211
zvva 12,148,188
v_fmac_f32_e32 v116,v233,v212
v_fmac_f32_e32 v117,v233,v213
cv 208,112,113
cv 209,114,115
mvva 12,144,184
v_fmac_f32_e32 v118,v233,v214
v_fmac_f32_e32 v119,v233,v215
cv 210,116,117
cv 211,118,119
mvva 12,132,180
v_mul_f32_e32 v121,v233,v121
v_mul_f32_e32 v122,v233,v122
v_mul_f32_e32 v123,v233,v123
mvva 12,128,176
v_mul_f32_e32 v124,v233,v124
v_mul_f32_e32 v125,v233,v125
v_mul_f32_e32 v126,v233,v126
v_mul_f32_e32 v127,v233,v127
s_waitcnt lgkmcnt(0)
zvva 28,136,176
L_vs11:
s_cbranch_execz L_vs15
buffer_store_dwordx4 v[200:203],v244,s[8:11],s14 offen offset:3072 sc0 sc1
tv 144,226,40960
v_fmac_f32_e32 v120,v233,v216
v_fmac_f32_e32 v121,v233,v217
v_fmac_f32_e32 v122,v233,v218
v_fmac_f32_e32 v123,v233,v219
mvva 28,140,180
tv 128,226,32768
tv 130,226,34816
cv 216,120,121
cv 217,122,123
mvva 28,152,184
tv 132,226,36864
tv 134,226,38912
v_fmac_f32_e32 v124,v233,v220
v_fmac_f32_e32 v125,v233,v221
v_fmac_f32_e32 v126,v233,v222
mvva 28,156,188
tv 146,226,43008
v_fmac_f32_e32 v127,v233,v223
cv 218,124,125
cv 219,126,127
zvva 24,156,172
tv 148,226,45056
tv 150,226,47104
mvva 24,152,168
v_xor_b32_e32 v227,96,v224
v_add_u32_e32 v232,131072,v224
mvva 24,140,164
buffer_store_dwordx4 v[208:211],v244,s[8:11],s88 offen offset:1024 sc0 sc1
mvva 24,136,160
v_add_u32_e32 v233,131072,v225
v_add_u32_e32 v234,131072,v226
zvva 20,136,144
v_add_u32_e32 v235,131072,v227
v_mov_b32_e32 v242,83914752
mvva 20,140,148
v_add_u32_e32 v228,65536,v224
v_add_u32_e32 v229,65536,v225
mvva 20,152,152
v_add_u32_e32 v230,65536,v226
v_add_u32_e32 v231,65536,v227
mvva 20,156,156
v_and_b32_e32 v243,240,v244
v_lshlrev_b32_e32 v243,7,v243
zvva 16,156,140
buffer_store_dwordx4 v[216:219],v244,s[8:11],s88 offen offset:3072 sc0 sc1
s_cmp_lg_u64 s[40:41],0
s_cbranch_scc1 L_redo
L_redo_r5:
mvva 16,152,136
tv 156,227,45056
v_mov_b32_e32 v236,16256
mvva 16,140,132
tv 158,227,47104
v_and_b32_e32 v248,256,v244
mvva 16,136,128
tv 152,227,40960
v_lshrrev_b32_e32 v248,3,v248
s_waitcnt lgkmcnt(3)
zvva 32,128,128
tv 140,227,36864
v_add_u32_e32 v243,v243,v248
mvva 32,132,132
tv 136,227,32768
tv 138,227,34816
mvva 32,144,136
dl 246,36,51,1024
tv 142,227,38912
mvva 32,148,140
tv 154,227,43008
v_and_b32_e32 v248,512,v244
zvva 36,148,156
v_lshrrev_b32_e32 v248,5,v248
v_add_u32_e32 v243,v243,v248
mvva 36,144,152
v_and_b32_e32 v248,16,v244
v_lshlrev_b32_e32 v248,v248,v236
mvva 36,132,148
v_cndmask_b32_e64 v238,0,v248,s[74:75]
s_mov_b32 s74,0xc0000c
mvva 36,128,144
s_mov_b32 s75,0xc0000c00
v_mov_b32_e32 v240,0
v_cndmask_b32_e64 v239,0,v248,s[74:75]
zvva 40,128,160
dl 249,36,51,2048
mvva 40,132,164
v_mov_b32_e32 v241,0
v_mov_b32_e32 v236,0
mvva 40,144,168
v_mov_b32_e32 v237,0
mvva 40,148,172
zvva 44,148,188
mvva 44,144,184
mvva 44,132,180
dl 247,36,51,3072
s_add_u32 m0,s52,16384
s_xor_b32 s51,s65,0
mvva 44,128,176
tv 144,224,57344
s_waitcnt lgkmcnt(1)
zvva 60,136,176
tv 132,224,53248
tv 134,224,55296
mvva 60,140,180
tv 128,224,49152
tv 130,224,51200
mvva 60,152,184
mvva 60,156,188
tv 146,224,59392
zvva 56,156,172
dl 245,36,51,0
tv 148,224,61440
tv 150,224,63488
mvva 56,152,168
mvva 56,140,164
mvva 56,136,160
zvva 52,136,144
mvva 52,140,148
mvva 52,152,152
dl 246,36,51,1024
mvva 52,156,156
zvva 48,156,140
mvva 48,152,136
mvva 48,140,132
tv 156,225,61440
mvva 48,136,128
tv 152,225,57344
s_waitcnt lgkmcnt(2)
zvva 64,128,128
dl 249,36,51,2048
tv 140,225,53248
mvva 64,132,132
tv 136,225,49152
tv 138,225,51200
mvva 64,144,136
tv 142,225,55296
mvva 64,148,140
tv 154,225,59392
zvva 68,148,156
tv 158,225,63488
mvva 68,144,152
mvva 68,132,148
dl 247,36,51,3072
s_lshl_b32 s51,s33,5
s_add_u32 m0,s52,98304
s_xor_b32 s51,s64,s51
mvva 68,128,144
zvva 72,128,160
mvva 72,132,164
s_waitcnt vmcnt(18)
s_mov_b32 s90,0
mvva 72,144,168
s_xor_b32 s50,s67,0
mvva 72,148,172
zvva 76,148,188
dl 245,36,51,0
mvva 76,144,184
mvva 76,132,180
tv 148,226,61440
mvva 76,128,176
tv 144,226,57344
s_waitcnt lgkmcnt(2)
zvva 92,136,176
tv 132,226,53248
mvva 92,140,180
tv 128,226,49152
tv 130,226,51200
mvva 92,152,184
dl 246,36,51,1024
tv 134,226,55296
mvva 92,156,188
tv 146,226,59392
zvva 88,156,172
tv 150,226,63488
mvva 88,152,168
mvva 88,140,164
mvva 88,136,160
zvva 84,136,144
dl 249,36,51,2048
mvva 84,140,148
mvva 84,152,152
mvva 84,156,156
zvva 80,156,140
mvva 80,152,136
mvva 80,140,132
dl 247,36,51,3072
mvva 80,136,128
s_lshl_b32 s51,s33,5
tv 152,227,57344
s_add_u32 m0,s52,114688
s_waitcnt lgkmcnt(1)
zvva 96,128,128
s_xor_b32 s51,s65,s51
tv 140,227,53248
tv 142,227,55296
mvva 96,132,132
tv 136,227,49152
tv 138,227,51200
mvva 96,144,136
mvva 96,148,140
tv 154,227,59392
zvva 100,148,156
lv 164,252,50,0
tv 156,227,61440
tv 158,227,63488
mvva 100,144,152
mvva 100,132,148
mvva 100,128,144
zvva 104,128,160
mvva 104,132,164
mvva 104,144,168
lv 172,252,50,2048
mvva 104,148,172
zvva 108,148,188
mvva 108,144,184
mvva 108,132,180
mvva 108,128,176
s_barrier
s_waitcnt lgkmcnt(0)
zvva 124,136,176
lv 180,250,50,0
tv 132,232,4096
tv 144,232,8192
tv 146,232,10240
mvva 124,140,180
tv 128,232,0
tv 130,232,2048
mvva 124,152,184
tv 134,232,6144
mvva 124,156,188
tv 148,232,12288
tv 150,232,14336
zvva 120,156,172
mvva 120,152,168
mvva 120,140,164
lv 188,250,50,2048
mvva 120,136,160
zvva 116,136,144
mvva 116,140,148
mvva 116,152,152
mvva 116,156,156
zvva 112,156,140
lv 220,250,50,3072
mvva 112,152,136
mvva 112,140,132
tv 156,233,12288
mvva 112,136,128
tv 152,233,8192
s_waitcnt lgkmcnt(2)
mvva 0,128,4
tv 140,233,4096
mvva 0,132,12
tv 136,233,0
tv 138,233,2048
mvva 0,144,20
lv 212,250,50,1024
tv 142,233,6144
mvva 0,148,28
tv 154,233,10240
mvva 4,148,60
tv 158,233,14336
mvva 4,144,52
mvva 4,132,44
mvva 4,128,36
mvva 8,128,0
lv 204,252,50,3072
mvva 8,132,8
mvva 8,144,16
mvva 8,148,24
mvva 12,148,56
s_waitcnt vmcnt(27)
mvva 12,144,48
s_waitcnt vmcnt(25)
mvva 12,132,40
lv 196,252,50,1024
s_waitcnt vmcnt(24)
mvva 12,128,32
tv 144,234,8192
s_waitcnt lgkmcnt(1)
mvva 28,136,32
tv 132,234,4096
tv 134,234,6144
mvva 28,140,40
tv 128,234,0
tv 130,234,2048
mvva 28,152,48
mvva 28,156,56
tv 146,234,10240
mvva 24,156,24
dl 245,36,51,0
s_waitcnt vmcnt(20)
s_barrier
tv 148,234,12288
tv 150,234,14336
mvva 24,152,16
mvva 24,140,8
mvva 24,136,0
mvva 20,136,36
mvva 20,140,44
mvva 20,152,52
s_cmp_eq_u32 s12,0
s_cselect_b64 exec,1,0
buffer_store_dword v251,off,s[8:11],s61 sc0 sc1
s_mov_b64 exec,-1
mvva 20,156,60
mvva 16,156,28
mvva 16,152,20
mvva 16,140,12
tv 156,235,12288
mvva 16,136,4
tv 152,235,8192
s_waitcnt lgkmcnt(2)
mvva 32,128,4
dl 246,36,51,1024
tv 140,235,4096
mvva 32,132,12
tv 136,235,0
tv 138,235,2048
mvva 32,144,20
tv 142,235,6144
mvva 32,148,28
tv 154,235,10240
mvva 36,148,60
tv 158,235,14336
mvva 36,144,52
mvva 36,132,44
dl 249,36,51,2048
mvva 36,128,36
mvva 40,128,0
mvva 40,132,8
mvva 40,144,16
mvva 40,148,24
mvva 44,148,56
dl 247,36,51,3072
s_lshl_b32 s51,s34,5
s_add_u32 m0,s52,65536
s_xor_b32 s51,s64,s51
mvva 44,144,48
tv 148,232,28672
mvva 44,132,40
s_xor_b32 s53,s60,s33
tv 150,232,30720
mvva 44,128,32
tv 144,232,24576
s_waitcnt lgkmcnt(3)
mvva 60,136,32
tv 132,232,20480
mvva 60,140,40
tv 128,232,16384
tv 130,232,18432
mvva 60,152,48
dl 245,36,51,0
tv 134,232,22528
mvva 60,156,56
tv 146,232,26624
mvva 56,156,24
mvva 56,152,16
mvva 56,140,8
mvva 56,136,0
mvva 52,136,36
buffer_load_dword v253,v242,s[8:11],s53 offen sc1
mvva 52,140,44
mvva 52,152,52
mvva 52,156,60
mvva 48,156,28
mvva 48,152,20
mvva 48,140,12
dl 246,36,51,1024
mvva 48,136,4
tv 152,233,24576
s_waitcnt lgkmcnt(1)
mvva 64,128,4
tv 140,233,20480
tv 142,233,22528
mvva 64,132,12
tv 136,233,16384
tv 138,233,18432
mvva 64,144,20
mvva 64,148,28
tv 154,233,26624
mvva 68,148,60
dl 249,36,51,2048
tv 156,233,28672
tv 158,233,30720
mvva 68,144,52
mvva 68,132,44
mvva 68,128,36
mvva 72,128,0
mvva 72,132,8
mvva 72,144,16
dl 247,36,51,3072
s_lshl_b32 s51,s34,5
s_add_u32 m0,s52,81920
s_xor_b32 s51,s65,s51
mvva 72,148,24
mvva 76,148,56
mvva 76,144,48
mvva 76,132,40
tv 148,234,28672
mvva 76,128,32
tv 144,234,24576
s_waitcnt lgkmcnt(2)
mvva 92,136,32
dl 245,36,51,0
tv 132,234,20480
mvva 92,140,40
tv 128,234,16384
tv 130,234,18432
mvva 92,152,48
tv 134,234,22528
mvva 92,156,56
tv 146,234,26624
mvva 88,156,24
tv 150,234,30720
mvva 88,152,16
mvva 88,140,8
dl 246,36,51,1024
mvva 88,136,0
mvva 84,136,36
mvva 84,140,44
mvva 84,152,52
mvva 84,156,60
mvva 80,156,28
dl 249,36,51,2048
mvva 80,152,20
mvva 80,140,12
tv 156,235,28672
mvva 80,136,4
tv 152,235,24576
s_waitcnt lgkmcnt(2)
mvva 96,128,4
tv 140,235,20480
mvva 96,132,12
tv 136,235,16384
tv 138,235,18432
mvva 96,144,20
dl 247,36,51,3072
s_lshl_b32 s51,s35,5
s_add_u32 m0,s52,32768
s_xor_b32 s51,s64,s51
tv 142,235,22528
mvva 96,148,28
tv 154,235,26624
mvva 100,148,60
tv 158,235,30720
mvva 100,144,52
mvva 100,132,44
mvva 100,128,36
mvva 104,128,0
dl 245,36,51,0
mvva 104,132,8
mvva 104,144,16
mvva 104,148,24
mvva 108,148,56
mvva 108,144,48
mvva 108,132,40
dl 246,36,51,1024
mvva 108,128,32
s_waitcnt vmcnt(27)
s_barrier
s_waitcnt lgkmcnt(0)
mvva 124,136,32
tv 132,224,4096
tv 134,224,6144
tv 144,224,8192
mvva 124,140,40
tv 128,224,0
tv 130,224,2048
mvva 124,152,48
mvva 124,156,56
tv 146,224,10240
mvva 120,156,24
dl 249,36,51,2048
tv 148,224,12288
tv 150,224,14336
mvva 120,152,16
mvva 120,140,8
mvva 120,136,0
mvva 116,136,36
mvva 116,140,44
mvva 116,152,52
dl 247,36,51,3072
s_lshl_b32 s51,s35,5
s_add_u32 m0,s52,49152
s_xor_b32 s51,s65,s51
mvva 116,156,60
mvva 112,156,28
mvva 112,152,20
s_waitcnt vmcnt(11)
v_cmp_ne_u32_e32 vcc,4,v253
mvva 112,140,12
dl 245,36,51,0
s_cbranch_vccnz L_spa3o
L_spra3o:
mvva 112,136,4
s_xor_b32 s50,s67,s33
tv 152,225,8192
s_waitcnt lgkmcnt(1)
mvvv 0,128,164
tv 140,225,4096
tv 142,225,6144
mvvv 0,132,172
tv 136,225,0
tv 138,225,2048
mvvv 0,144,180
dl 246,36,51,1024
mvvv 0,148,188
tv 154,225,10240
mvvv 4,148,220
tv 156,225,12288
tv 158,225,14336
mvvv 4,144,212
mvvv 4,132,204
la 196,252,50,0
mvvv 4,128,196
mvvv 8,128,160
s_xor_b32 s53,s60,s34
mvvv 8,132,168
mvvv 8,144,176
la 204,252,50,2048
mvvv 8,148,184
mvvv 12,148,216
mvvv 12,144,208
mvvv 12,132,200
buffer_load_dword v253,v242,s[8:11],s53 offen sc1
mvvv 12,128,192
tv 144,226,8192
s_waitcnt lgkmcnt(1)
mvvv 28,136,192
tv 132,226,4096
tv 134,226,6144
mvvv 28,140,200
tv 128,226,0
tv 130,226,2048
mvvv 28,152,208
la 212,250,50,0
mvvv 28,156,216
tv 146,226,10240
mvvv 24,156,184
tv 148,226,12288
tv 150,226,14336
mvvv 24,152,176
mvvv 24,140,168
mvvv 24,136,160
la 220,250,50,2048
mvvv 20,136,196
L_vs12:
s_cbranch_execz L_vs1
mvvv 20,140,204
mvvv 20,152,212
mvvv 20,156,220
la 252,250,50,3072
mvvv 16,156,188
mvvv 16,152,180
mvvv 16,140,172
tv 156,227,12288
mvvv 16,136,164
la 244,250,50,1024
tv 152,227,8192
s_waitcnt lgkmcnt(2)
mvvv 32,128,164
tv 140,227,4096
mvvv 32,132,172
tv 136,227,0
tv 138,227,2048
mvvv 32,144,180
tv 142,227,6144
mvvv 32,148,188
la 236,252,50,3072
tv 154,227,10240
mvvv 36,148,220
tv 158,227,14336
mvvv 36,144,212
mvvv 36,132,204
mvvv 36,128,196
la 228,252,50,1024
s_add_u32 s50,s50,s55
mvvv 40,128,160
mvvv 40,132,168
mvvv 40,144,176
mvvv 40,148,184
mvvv 44,148,216
la 192,252,50,0
mvvv 44,144,208
mvvv 44,132,200
tv 148,224,28672
tv 150,224,30720
mvvv 44,128,192
s_waitcnt lgkmcnt(2)
mvvv 60,136,192
la 200,252,50,2048
tv 132,224,20480
mvvv 60,140,200
tv 128,224,16384
tv 130,224,18432
mvvv 60,152,208
tv 134,224,22528
mvvv 60,156,216
tv 144,224,24576
tv 146,224,26624
mvvv 56,156,184
la 208,250,50,0
mvvv 56,152,176
mvvv 56,140,168
mvvv 56,136,160
mvvv 52,136,196
la 216,250,50,2048
mvvv 52,140,204
mvvv 52,152,212
mvvv 52,156,220
mvvv 48,156,188
la 248,250,50,3072
mvvv 48,152,180
mvvv 48,140,172
tv 156,225,28672
mvvv 48,136,164
tv 152,225,24576
s_waitcnt lgkmcnt(2)
mvvv 64,128,164
tv 140,225,20480
mvvv 64,132,172
la 240,250,50,1024
tv 136,225,16384
tv 138,225,18432
mvvv 64,144,180
tv 142,225,22528
mvvv 64,148,188
tv 154,225,26624
mvvv 68,148,220
tv 158,225,30720
mvvv 68,144,212
la 232,252,50,3072
mvvv 68,132,204
mvvv 68,128,196
mvvv 72,128,160
mvvv 72,132,168
la 224,252,50,1024
mvvv 72,144,176
mvvv 72,148,184
mvvv 76,148,216
mvvv 76,144,208
mvvv 76,132,200
dl 249,36,51,2048
mvvv 76,128,192
tv 144,226,24576
s_waitcnt lgkmcnt(1)
mvvv 92,136,192
tv 132,226,20480
tv 134,226,22528
mvvv 92,140,200
tv 128,226,16384
tv 130,226,18432
mvvv 92,152,208
dl 247,36,51,3072
s_lshl_b32 s51,s98,5
s_xor_b32 s51,s64,s51
s_add_u32 m0,s52,131072
s_xor_b32 s51,s51,4194304
mvvv 92,156,216
tv 146,226,26624
mvvv 88,156,184
tv 148,226,28672
tv 150,226,30720
mvvv 88,152,176
mvvv 88,140,168
dl 245,36,51,0
mvvv 88,136,160
s_waitcnt vmcnt(17)
v_cmp_ne_u32_e32 vcc,4,v253
mvvv 84,136,196
s_cbranch_vccnz L_spa4o
L_spra4o:
mvvv 84,140,204
s_xor_b32 s50,s67,s34
mvvv 84,152,212
dl 246,36,51,1024
mvvv 84,156,220
mvvv 80,156,188
mvvv 80,152,180
mvvv 80,140,172
la 68,252,50,0
mvvv 80,136,164
tv 152,227,24576
s_waitcnt lgkmcnt(1)
mvvv 96,128,164
tv 140,227,20480
tv 142,227,22528
mvvv 96,132,172
tv 136,227,16384
tv 138,227,18432
mvvv 96,144,180
s_xor_b32 s53,s60,s35
mvvv 96,148,188
la 76,252,50,2048
tv 154,227,26624
mvvv 100,148,220
tv 156,227,28672
tv 158,227,30720
mvvv 100,144,212
mvvv 100,132,204
mvvv 100,128,196
la 84,250,50,0
mvvv 104,128,160
mvvv 104,132,168
mvvv 104,144,176
mvvv 104,148,184
buffer_load_dword v253,v242,s[8:11],s53 offen sc1
mvvv 108,148,216
mvvv 108,144,208
mvvv 108,132,200
mvvv 108,128,192
s_barrier
s_waitcnt lgkmcnt(0)
mvvv 124,136,192
la 92,250,50,2048
tv 132,228,36864
tv 144,228,40960
tv 146,228,43008
mvvv 124,140,200
tv 128,228,32768
tv 130,228,34816
mvvv 124,152,208
tv 134,228,38912
mvvv 124,156,216
tv 148,228,45056
tv 150,228,47104
mvvv 120,156,184
la 124,250,50,3072
mvvv 120,152,176
mvvv 120,140,168
mvvv 120,136,160
mvvv 116,136,196
la 116,250,50,1024
mvvv 116,140,204
mvvv 116,152,212
mvvv 116,156,220
mvvv 112,156,188
la 108,252,50,3072
mvvv 112,152,180
mvvv 112,140,172
tv 156,229,45056
tv 158,229,47104
mvvv 112,136,164
s_waitcnt lgkmcnt(2)
mvva 0,128,196
la 100,252,50,1024
s_add_u32 s50,s50,s55
tv 140,229,36864
mvva 0,132,204
tv 136,229,32768
tv 138,229,34816
s_waitcnt vmcnt(21)
mvva 0,144,212
tv 142,229,38912
mvva 0,148,220
tv 152,229,40960
)"
R"(tv 154,229,43008
mvva 4,148,252
mvva 4,144,244
la 64,252,50,0
mvva 4,132,236
mvva 4,128,228
s_waitcnt vmcnt(21)
mvva 8,128,192
s_waitcnt vmcnt(20)
mvva 8,132,200
la 72,252,50,2048
s_waitcnt vmcnt(20)
mvva 8,144,208
s_waitcnt vmcnt(19)
mvva 8,148,216
s_waitcnt vmcnt(18)
mvva 12,148,248
s_waitcnt vmcnt(17)
mvva 12,144,240
la 80,250,50,0
s_waitcnt vmcnt(17)
mvva 12,132,232
tv 148,230,45056
s_waitcnt vmcnt(16)
mvva 12,128,224
tv 144,230,40960
s_waitcnt lgkmcnt(2)
mvva 28,136,224
tv 132,230,36864
mvva 28,140,232
la 88,250,50,2048
tv 128,230,32768
tv 130,230,34816
mvva 28,152,240
tv 134,230,38912
mvva 28,156,248
tv 146,230,43008
mvva 24,156,216
tv 150,230,47104
mvva 24,152,208
la 120,250,50,3072
mvva 24,140,200
mvva 24,136,192
mvva 20,136,228
mvva 20,140,236
mvva 20,152,244
la 112,250,50,1024
mvva 20,156,252
mvva 16,156,220
mvva 16,152,212
mvva 16,140,204
la 104,252,50,3072
mvva 16,136,196
tv 152,231,40960
s_waitcnt lgkmcnt(1)
mvva 32,128,196
tv 140,231,36864
tv 142,231,38912
mvva 32,132,204
tv 136,231,32768
tv 138,231,34816
mvva 32,144,212
la 96,252,50,1024
mvva 32,148,220
tv 154,231,43008
mvva 36,148,252
tv 156,231,45056
tv 158,231,47104
mvva 36,144,244
mvva 36,132,236
mvva 36,128,228
dl 249,36,51,2048
mvva 40,128,192
mvva 40,132,200
mvva 40,144,208
mvva 40,148,216
dl 247,36,51,3072
s_lshl_b32 s51,s98,5
s_xor_b32 s51,s65,s51
s_add_u32 m0,s52,147456
s_xor_b32 s51,s51,4194304
mvva 44,148,248
mvva 44,144,240
mvva 44,132,232
tv 148,228,61440
mvva 44,128,224
dl 245,36,51,0
tv 144,228,57344
s_waitcnt lgkmcnt(2)
mvva 60,136,224
tv 132,228,53248
mvva 60,140,232
tv 128,228,49152
tv 130,228,51200
s_waitcnt vmcnt(16)
v_cmp_ne_u32_e32 vcc,4,v253
mvva 60,152,240
s_cbranch_vccnz L_spa5o
L_spra5o:
tv 134,228,55296
s_xor_b32 s50,s67,s35
mvva 60,156,248
dl 246,36,51,1024
tv 146,228,59392
mvva 56,156,216
tv 150,228,63488
mvva 56,152,208
mvva 56,140,200
mvva 56,136,192
la 132,252,50,0
mvva 52,136,228
mvva 52,140,236
mvva 52,152,244
mvva 52,156,252
mvva 48,156,220
la 140,252,50,2048
s_xor_b32 s53,s60,s98
s_xor_b32 s53,s53,512
mvva 48,152,212
tv 156,229,61440
mvva 48,140,204
tv 158,229,63488
mvva 48,136,196
tv 152,229,57344
s_waitcnt lgkmcnt(3)
mvva 64,128,196
la 148,250,50,0
tv 140,229,53248
mvva 64,132,204
tv 136,229,49152
tv 138,229,51200
mvva 64,144,212
tv 142,229,55296
mvva 64,148,220
tv 154,229,59392
mvva 68,148,252
buffer_load_dword v253,v242,s[8:11],s53 offen sc1
mvva 68,144,244
mvva 68,132,236
mvva 68,128,228
mvva 72,128,192
mvva 72,132,200
la 156,250,50,2048
mvva 72,144,208
mvva 72,148,216
mvva 76,148,248
mvva 76,144,240
la 188,250,50,3072
mvva 76,132,232
tv 148,230,61440
mvva 76,128,224
tv 144,230,57344
s_waitcnt lgkmcnt(2)
mvva 92,136,224
tv 132,230,53248
mvva 92,140,232
la 180,250,50,1024
tv 128,230,49152
tv 130,230,51200
mvva 92,152,240
tv 134,230,55296
mvva 92,156,248
tv 146,230,59392
mvva 88,156,216
tv 150,230,63488
mvva 88,152,208
la 172,252,50,3072
mvva 88,140,200
mvva 88,136,192
mvva 84,136,228
mvva 84,140,236
la 164,252,50,1024
s_add_u32 s50,s50,s55
mvva 84,152,244
mvva 84,156,252
mvva 80,156,220
mvva 80,152,212
mvva 80,140,204
la 128,252,50,0
mvva 80,136,196
tv 152,231,57344
s_waitcnt lgkmcnt(1)
mvva 96,128,196
tv 140,231,53248
tv 142,231,55296
mvva 96,132,204
tv 136,231,49152
tv 138,231,51200
mvva 96,144,212
la 136,252,50,2048
mvva 96,148,220
tv 154,231,59392
mvva 100,148,252
tv 156,231,61440
tv 158,231,63488
mvva 100,144,244
mvva 100,132,236
la 144,250,50,0
mvva 100,128,228
mvva 104,128,192
mvva 104,132,200
mvva 104,144,208
la 152,250,50,2048
mvva 104,148,216
mvva 108,148,248
mvva 108,144,240
mvva 108,132,232
la 184,250,50,3072
mvva 108,128,224
s_barrier
s_waitcnt lgkmcnt(0)
mvva 124,136,224
tv 132,228,4096
tv 134,228,6144
tv 144,228,8192
mvva 124,140,232
tv 128,228,0
tv 130,228,2048
mvva 124,152,240
mvva 124,156,248
la 176,250,50,1024
tv 146,228,10240
mvva 120,156,216
tv 148,228,12288
tv 150,228,14336
mvva 120,152,208
mvva 120,140,200
mvva 120,136,192
la 168,252,50,3072
mvva 116,136,228
mvva 116,140,236
mvva 116,152,244
mvva 116,156,252
la 160,252,50,1024
mvva 112,156,220
mvva 112,152,212
mvva 112,140,204
tv 156,229,12288
tv 158,229,14336
mvva 112,136,196
s_waitcnt lgkmcnt(2)
mvva 0,128,68
dl 249,36,51,2048
tv 140,229,4096
mvva 0,132,76
tv 136,229,0
tv 138,229,2048
mvva 0,144,84
tv 142,229,6144
s_waitcnt vmcnt(22)
mvva 0,148,92
tv 152,229,8192
tv 154,229,10240
mvva 4,148,124
dl 247,36,51,3072
s_lshl_b32 s51,s98,5
s_xor_b32 s51,s64,s51
s_add_u32 m0,s52,0
s_xor_b32 s51,s51,4456448
mvva 4,144,116
mvva 4,132,108
mvva 4,128,100
mvva 8,128,64
dl 245,36,51,0
mvva 8,132,72
mvva 8,144,80
mvva 8,148,88
s_waitcnt vmcnt(16)
v_cmp_ne_u32_e32 vcc,4,v253
mvva 12,148,120
dl 246,36,51,1024
s_cbranch_vccnz L_spa6o
L_spra6o:
mvva 12,144,112
s_xor_b32 s50,s67,s98
mvva 12,132,104
s_xor_b32 s50,s50,131072
tv 148,230,12288
tv 150,230,14336
mvva 12,128,96
s_waitcnt lgkmcnt(2)
mvva 28,136,96
dl 249,36,51,2048
tv 132,230,4096
mvva 28,140,104
tv 128,230,0
tv 130,230,2048
mvva 28,152,112
tv 134,230,6144
mvva 28,156,120
tv 144,230,8192
tv 146,230,10240
mvva 24,156,88
mvva 24,152,80
la 4,252,50,0
mvva 24,140,72
s_xor_b32 s53,s60,s98
mvva 24,136,64
s_xor_b32 s53,s53,8704
mvva 20,136,100
mvva 20,140,108
la 12,252,50,2048
mvva 20,152,116
mvva 20,156,124
mvva 16,156,92
mvva 16,152,84
buffer_load_dword v253,v242,s[8:11],s53 offen sc1
mvva 16,140,76
tv 156,231,12288
mvva 16,136,68
tv 152,231,8192
s_waitcnt lgkmcnt(2)
mvva 32,128,68
tv 140,231,4096
mvva 32,132,76
la 20,250,50,0
tv 136,231,0
tv 138,231,2048
mvva 32,144,84
tv 142,231,6144
mvva 32,148,92
tv 154,231,10240
mvva 36,148,124
tv 158,231,14336
mvva 36,144,116
la 28,250,50,2048
mvva 36,132,108
mvva 36,128,100
mvva 40,128,64
mvva 40,132,72
mvva 40,144,80
la 60,250,50,3072
mvva 40,148,88
L_vs13:
s_cbranch_execz L_vr1
mvva 44,148,120
mvva 44,144,112
mvva 44,132,104
la 52,250,50,1024
mvva 44,128,96
tv 144,228,24576
s_waitcnt lgkmcnt(1)
mvva 60,136,96
tv 132,228,20480
tv 134,228,22528
mvva 60,140,104
tv 128,228,16384
tv 130,228,18432
mvva 60,152,112
la 44,252,50,3072
mvva 60,156,120
tv 146,228,26624
mvva 56,156,88
tv 148,228,28672
tv 150,228,30720
mvva 56,152,80
mvva 56,140,72
mvva 56,136,64
la 36,252,50,1024
s_add_u32 s50,s50,s55
mvva 52,136,100
mvva 52,140,108
mvva 52,152,116
mvva 52,156,124
la 0,252,50,0
mvva 48,156,92
mvva 48,152,84
mvva 48,140,76
tv 156,229,28672
mvva 48,136,68
la 8,252,50,2048
tv 152,229,24576
s_waitcnt lgkmcnt(2)
mvva 64,128,68
tv 140,229,20480
mvva 64,132,76
tv 136,229,16384
tv 138,229,18432
mvva 64,144,84
tv 142,229,22528
mvva 64,148,92
la 16,250,50,0
tv 154,229,26624
mvva 68,148,124
tv 158,229,30720
mvva 68,144,116
mvva 68,132,108
mvva 68,128,100
la 24,250,50,2048
mvva 72,128,64
mvva 72,132,72
mvva 72,144,80
mvva 72,148,88
mvva 76,148,120
la 56,250,50,3072
mvva 76,144,112
mvva 76,132,104
tv 148,224,45056
tv 150,224,47104
mvva 76,128,96
s_waitcnt lgkmcnt(2)
mvva 92,136,96
la 48,250,50,1024
tv 132,224,36864
mvva 92,140,104
tv 128,224,32768
tv 130,224,34816
mvva 92,152,112
tv 134,224,38912
mvva 92,156,120
tv 144,224,40960
tv 146,224,43008
mvva 88,156,88
la 40,252,50,3072
mvva 88,152,80
mvva 88,140,72
mvva 88,136,64
mvva 84,136,100
mvva 84,140,108
la 32,252,50,1024
mvva 84,152,116
mvva 84,156,124
mvva 80,156,92
mvva 80,152,84
dl 247,36,51,3072
s_lshl_b32 s51,s98,5
s_xor_b32 s51,s64,s51
s_add_u32 m0,s52,16384
s_xor_b32 s51,s51,4718592
mvva 80,140,76
tv 152,225,40960
mvva 80,136,68
tv 154,225,43008
s_waitcnt lgkmcnt(2)
mvva 0,128,132
tv 140,225,36864
mvva 0,132,140
dl 245,36,51,0
tv 136,225,32768
tv 138,225,34816
mvva 0,144,148
tv 142,225,38912
s_waitcnt vmcnt(24)
mvva 0,148,156
mvva 4,148,188
tv 156,225,45056
tv 158,225,47104
mvva 4,144,180
dl 246,36,51,1024
s_waitcnt vmcnt(17)
v_cmp_ne_u32_e32 vcc,4,v253
mvva 4,132,172
s_cbranch_vccnz L_spa7o
L_spra7o:
mvva 4,128,164
s_xor_b32 s50,s67,s98
mvva 8,128,128
s_xor_b32 s50,s50,139264
mvva 8,132,136
dl 249,36,51,2048
mvva 8,144,144
mvva 8,148,152
mvva 12,148,184
mvva 12,144,176
mvva 12,132,168
dl 247,36,51,3072
mvva 12,128,160
s_barrier
tv 144,226,40960
s_lshl_b32 s51,s98,5
s_waitcnt lgkmcnt(1)
mvva 28,136,160
s_xor_b32 s53,s60,s98
tv 132,226,36864
tv 134,226,38912
s_xor_b32 s53,s53,16896
mvva 28,140,168
s_xor_b32 s51,s65,s51
tv 128,226,32768
tv 130,226,34816
mvva 28,152,176
lv 164,252,50,0
mvva 28,156,184
s_add_u32 m0,s52,98304
tv 146,226,43008
s_xor_b32 s51,s51,4456448
mvva 24,156,152
tv 148,226,45056
tv 150,226,47104
mvva 24,152,144
mvva 24,140,136
buffer_load_dword v253,v242,s[8:11],s53 offen sc1
mvva 24,136,128
mvva 20,136,164
mvva 20,140,172
mvva 20,152,180
lv 172,252,50,2048
mvva 20,156,188
mvva 16,156,156
mvva 16,152,148
mvva 16,140,140
lv 180,250,50,0
mvva 16,136,132
tv 152,227,40960
s_waitcnt lgkmcnt(1)
mvva 32,128,132
tv 140,227,36864
tv 142,227,38912
mvva 32,132,140
tv 136,227,32768
tv 138,227,34816
mvva 32,144,148
mvva 32,148,156
lv 188,250,50,2048
tv 154,227,43008
mvva 36,148,188
tv 156,227,45056
tv 158,227,47104
mvva 36,144,180
mvva 36,132,172
mvva 36,128,164
lv 220,250,50,3072
mvva 40,128,128
mvva 40,132,136
mvva 40,144,144
mvva 40,148,152
lv 212,250,50,1024
mvva 44,148,184
mvva 44,144,176
mvva 44,132,168
tv 148,224,61440
tv 150,224,63488
mvva 44,128,160
s_waitcnt lgkmcnt(2)
mvva 60,136,160
lv 204,252,50,3072
tv 132,224,53248
mvva 60,140,168
tv 128,224,49152
tv 130,224,51200
mvva 60,152,176
tv 134,224,55296
mvva 60,156,184
tv 144,224,57344
tv 146,224,59392
mvva 56,156,152
lv 196,252,50,1024
s_add_u32 s50,s50,s55
mvva 56,152,144
mvva 56,140,136
mvva 56,136,128
mvva 52,136,164
lv 160,252,50,0
mvva 52,140,172
mvva 52,152,180
mvva 52,156,188
mvva 48,156,156
lv 168,252,50,2048
mvva 48,152,148
mvva 48,140,140
tv 156,225,61440
tv 158,225,63488
mvva 48,136,132
s_waitcnt lgkmcnt(2)
mvva 64,128,132
lv 176,250,50,0
tv 140,225,53248
mvva 64,132,140
tv 136,225,49152
tv 138,225,51200
mvva 64,144,148
tv 142,225,55296
mvva 64,148,156
tv 152,225,57344
tv 154,225,59392
mvva 68,148,188
mvva 68,144,180
lv 184,250,50,2048
mvva 68,132,172
mvva 68,128,164
mvva 72,128,128
mvva 72,132,136
lv 216,250,50,3072
mvva 72,144,144
mvva 72,148,152
mvva 76,148,184
mvva 76,144,176
lv 208,250,50,1024
mvva 76,132,168
tv 148,232,12288
mvva 76,128,160
tv 144,232,8192
s_waitcnt lgkmcnt(2)
mvva 92,136,160
tv 132,232,4096
mvva 92,140,168
tv 128,232,0
tv 130,232,2048
mvva 92,152,176
lv 200,252,50,3072
tv 134,232,6144
mvva 92,156,184
tv 146,232,10240
mvva 88,156,152
tv 150,232,14336
mvva 88,152,144
mvva 88,140,136
lv 192,252,50,1024
mvva 88,136,128
mvva 84,136,164
mvva 84,140,172
mvva 84,152,180
dl 245,36,51,0
mvva 84,156,188
mvva 80,156,156
mvva 80,152,148
mvva 80,140,140
dl 246,36,51,1024
mvva 80,136,132
tv 152,233,8192
s_waitcnt vmcnt(17)
v_cmp_ne_u32_e32 vcc,4,v253
s_waitcnt lgkmcnt(1)
mvva 0,128,4
s_cbranch_vccnz L_spa8o
L_spra8o:
tv 140,233,4096
tv 142,233,6144
mvva 0,132,12
s_xor_b32 s50,s67,s98
tv 136,233,0
s_xor_b32 s50,s50,147456
tv 138,233,2048
mvva 0,144,20
dl 249,36,51,2048
mvva 0,148,28
tv 154,233,10240
v_and_b32_e32 v242,48,v244
mvva 4,148,60
tv 156,233,12288
tv 158,233,14336
mvva 4,144,52
v_lshlrev_b32_e32 v242,3,v242
mvva 4,132,44
mvva 4,128,36
la 196,252,50,0
mvva 8,128,0
s_andn2_b32 s53,s61,384
mvva 8,132,8
mvva 8,144,16
mvva 8,148,24
la 204,252,50,2048
mvva 12,148,56
mvva 12,144,48
mvva 12,132,40
tv 148,234,12288
mvva 12,128,32
buffer_load_dword v253,v242,s[8:11],s53 offen sc1
tv 144,234,8192
s_waitcnt lgkmcnt(2)
mvva 28,136,32
tv 132,234,4096
mvva 28,140,40
tv 128,234,0
tv 130,234,2048
mvva 28,152,48
tv 134,234,6144
mvva 28,156,56
la 212,250,50,0
tv 146,234,10240
mvva 24,156,24
tv 150,234,14336
mvva 24,152,16
mvva 24,140,8
mvva 24,136,0
la 220,250,50,2048
mvva 20,136,36
mvva 20,140,44
mvva 20,152,52
mvva 20,156,60
mvva 16,156,28
la 252,250,50,3072
mvva 16,152,20
mvva 16,140,12
tv 156,235,12288
tv 158,235,14336
mvva 16,136,4
s_waitcnt lgkmcnt(2)
mvva 32,128,4
la 244,250,50,1024
tv 140,235,4096
mvva 32,132,12
tv 136,235,0
tv 138,235,2048
mvva 32,144,20
tv 142,235,6144
mvva 32,148,28
tv 152,235,8192
tv 154,235,10240
mvva 36,148,60
la 236,252,50,3072
mvva 36,144,52
mvva 36,132,44
mvva 36,128,36
mvva 40,128,0
mvva 40,132,8
la 228,252,50,1024
s_add_u32 s50,s50,s55
mvva 40,144,16
mvva 40,148,24
mvva 44,148,56
mvva 44,144,48
la 192,252,50,0
mvva 44,132,40
mvva 44,128,32
s_barrier
s_waitcnt lgkmcnt(0)
mvva 60,136,32
tv 132,232,20480
tv 144,232,24576
mvva 60,140,40
la 200,252,50,2048
tv 128,232,16384
tv 130,232,18432
mvva 60,152,48
tv 134,232,22528
mvva 60,156,56
tv 146,232,26624
mvva 56,156,24
tv 148,232,28672
tv 150,232,30720
mvva 56,152,16
la 208,250,50,0
mvva 56,140,8
mvva 56,136,0
mvva 52,136,36
mvva 52,140,44
la 216,250,50,2048
mvva 52,152,52
mvva 52,156,60
mvva 48,156,28
mvva 48,152,20
mvva 48,140,12
la 248,250,50,3072
mvva 48,136,4
tv 152,233,24576
s_waitcnt lgkmcnt(1)
mvva 64,128,4
tv 140,233,20480
tv 142,233,22528
mvva 64,132,12
tv 136,233,16384
tv 138,233,18432
mvva 64,144,20
la 240,250,50,1024
mvva 64,148,28
tv 154,233,26624
mvva 68,148,60
tv 156,233,28672
tv 158,233,30720
mvva 68,144,52
mvva 68,132,44
la 232,252,50,3072
mvva 68,128,36
mvva 72,128,0
mvva 72,132,8
mvva 72,144,16
la 224,252,50,1024
mvva 72,148,24
mvva 76,148,56
mvva 76,144,48
mvva 76,132,40
dl 247,36,51,3072
s_lshl_b32 s51,s98,5
s_xor_b32 s51,s65,s51
s_add_u32 m0,s52,114688
s_xor_b32 s51,s51,4718592
mvva 76,128,32
tv 144,224,8192
s_waitcnt lgkmcnt(1)
mvva 92,136,32
tv 132,224,4096
tv 134,224,6144
mvva 92,140,40
tv 128,224,0
tv 130,224,2048
mvva 92,152,48
mvva 92,156,56
dl 245,36,51,0
tv 146,224,10240
mvva 88,156,24
tv 148,224,12288
tv 150,224,14336
mvva 88,152,16
mvva 88,140,8
mvva 88,136,0
dl 246,36,51,1024
s_waitcnt vmcnt(17)
v_cmp_ne_u32_e32 vcc,4,v253
mvva 84,136,36
s_cbranch_vccnz L_sppo
L_sprpo:
mvva 84,140,44
mvva 84,152,52
mvva 84,156,60
dl 249,36,51,2048
mvva 80,156,28
mvva 80,152,20
mvva 80,140,12
tv 156,225,12288
mvva 80,136,4
tv 152,225,8192
s_waitcnt lgkmcnt(2)
mvvv 0,128,164
dl 247,36,51,3072
tv 140,225,4096
mvvv 0,132,172
tv 136,225,0
tv 138,225,2048
mvvv 0,144,180
tv 142,225,6144
mvvv 0,148,188
tv 154,225,10240
mvvv 4,148,220
tv 158,225,14336
mvvv 4,144,212
mvvv 4,132,204
mvvv 4,128,196
mvvv 8,128,160
mvvv 8,132,168
mvvv 8,144,176
mvvv 8,148,184
mvvv 12,148,216
mvvv 12,144,208
mvvv 12,132,200
tv 148,224,28672
mvvv 12,128,192
tv 144,224,24576
s_waitcnt lgkmcnt(2)
mvvv 28,136,192
tv 132,224,20480
mvvv 28,140,200
tv 128,224,16384
tv 130,224,18432
mvvv 28,152,208
tv 134,224,22528
mvvv 28,156,216
tv 146,224,26624
mvvv 24,156,184
tv 150,224,30720
mvvv 24,152,176
mvvv 24,140,168
mvvv 24,136,160
mvvv 20,136,196
mvvv 20,140,204
mvvv 20,152,212
mvvv 20,156,220
mvvv 16,156,188
mvvv 16,152,180
mvvv 16,140,172
tv 156,225,28672
mvvv 16,136,164
tv 152,225,24576
s_waitcnt lgkmcnt(2)
mvva 0,128,196
tv 140,225,20480
mvva 0,132,204
tv 136,225,16384
tv 138,225,18432
s_waitcnt vmcnt(14)
mvva 0,144,212
L_vs14:
s_cbranch_execz L_vr1
tv 142,225,22528
mvva 0,148,220
tv 154,225,26624
mvva 4,148,252
tv 158,225,30720
mvva 4,144,244
mvva 4,132,236
s_waitcnt vmcnt(13)
mvva 4,128,228
s_waitcnt vmcnt(12)
mvva 8,128,192
cv 0,0,1
cv 1,2,3
s_waitcnt vmcnt(11)
mvva 8,132,200
s_waitcnt vmcnt(10)
mvva 8,144,208
cv 4,4,5
cv 5,6,7
s_waitcnt vmcnt(9)
mvva 8,148,216
s_waitcnt vmcnt(8)
mvva 12,148,248
s_waitcnt vmcnt(7)
mvva 12,144,240
s_waitcnt vmcnt(6)
mvva 12,132,232
tv 148,226,12288
s_waitcnt vmcnt(5)
mvva 12,128,224
tv 144,226,8192
cv 8,8,9
s_waitcnt lgkmcnt(2)
mvva 28,136,224
tv 132,226,4096
cv 9,10,11
mvva 28,140,232
tv 128,226,0
tv 130,226,2048
mvva 28,152,240
tv 134,226,6144
cv 12,12,13
mvva 28,156,248
tv 146,226,10240
cv 13,14,15
mvva 24,156,216
tv 150,226,14336
mvva 24,152,208
mvva 24,140,200
mvva 24,136,192
mvva 20,136,228
cv 14,28,29
cv 15,30,31
mvva 20,140,236
buffer_store_dwordx4 v[12:15],v244,s[8:11],s56 offen offset:3072 sc0 sc1
mvva 20,152,244
mvva 20,156,252
mvva 16,156,220
cv 10,24,25
cv 11,26,27
mvva 16,152,212
buffer_store_dwordx4 v[8:11],v244,s[8:11],s56 offen offset:2048 sc0 sc1
mvva 16,140,204
tv 156,227,12288
mvva 16,136,196
cv 6,20,21
cv 7,22,23
s_waitcnt lgkmcnt(1)
mvvv 32,128,164
tv 140,227,4096
mvvv 32,132,172
buffer_store_dwordx4 v[4:7],v244,s[8:11],s56 offen offset:1024 sc0 sc1
tv 136,227,0
tv 138,227,2048
mvvv 32,144,180
tv 142,227,6144
cv 2,16,17
mvvv 32,148,188
tv 152,227,8192
tv 154,227,10240
mvvv 36,148,220
tv 158,227,14336
cv 3,18,19
mvvv 36,144,212
buffer_store_dwordx4 v[0:3],v244,s[8:11],s56 offen offset:0 sc0 sc1
mvvv 36,132,204
mvvv 36,128,196
mvvv 40,128,160
mvvv 40,132,168
mvvv 40,144,176
mvvv 40,148,184
mvvv 44,148,216
mvvv 44,144,208
mvvv 44,132,200
tv 148,226,28672
mvvv 44,128,192
s_waitcnt vmcnt(4)
s_barrier
tv 144,226,24576
s_waitcnt lgkmcnt(2)
mvvv 60,136,192
tv 132,226,20480
mvvv 60,140,200
tv 128,226,16384
tv 130,226,18432
mvvv 60,152,208
tv 134,226,22528
mvvv 60,156,216
tv 146,226,26624
mvvv 56,156,184
tv 150,226,30720
mvvv 56,152,176
mvvv 56,140,168
mvvv 56,136,160
mvvv 52,136,196
mvvv 52,140,204
mvvv 52,152,212
mvvv 52,156,220
mvvv 48,156,188
mvvv 48,152,180
mvvv 48,140,172
tv 156,227,28672
mvvv 48,136,164
tv 152,227,24576
s_waitcnt lgkmcnt(2)
mvva 32,128,196
tv 140,227,20480
)"
R"(mvva 32,132,204
tv 136,227,16384
tv 138,227,18432
mvva 32,144,212
tv 142,227,22528
mvva 32,148,220
tv 154,227,26624
mvva 36,148,252
tv 158,227,30720
mvva 36,144,244
mvva 36,132,236
mvva 36,128,228
mvva 40,128,192
mvva 40,132,200
cv 32,32,33
cv 33,34,35
mvva 40,144,208
mvva 40,148,216
mvva 44,148,248
mvva 44,144,240
cv 36,36,37
cv 37,38,39
mvva 44,132,232
tv 148,228,45056
mvva 44,128,224
tv 144,228,40960
cv 40,40,41
s_waitcnt lgkmcnt(2)
mvva 60,136,224
tv 132,228,36864
cv 41,42,43
mvva 60,140,232
tv 128,228,32768
tv 130,228,34816
mvva 60,152,240
tv 134,228,38912
cv 44,44,45
mvva 60,156,248
tv 146,228,43008
cv 45,46,47
mvva 56,156,216
tv 150,228,47104
mvva 56,152,208
mvva 56,140,200
mvva 56,136,192
mvva 52,136,228
cv 46,60,61
cv 47,62,63
mvva 52,140,236
buffer_store_dwordx4 v[44:47],v244,s[8:11],s57 offen offset:3072 sc0 sc1
mvva 52,152,244
mvva 52,156,252
mvva 48,156,220
cv 42,56,57
cv 43,58,59
mvva 48,152,212
buffer_store_dwordx4 v[40:43],v244,s[8:11],s57 offen offset:2048 sc0 sc1
mvva 48,140,204
tv 156,229,45056
mvva 48,136,196
cv 38,52,53
cv 39,54,55
s_waitcnt lgkmcnt(1)
mvvv 64,128,164
tv 140,229,36864
mvvv 64,132,172
buffer_store_dwordx4 v[36:39],v244,s[8:11],s57 offen offset:1024 sc0 sc1
tv 136,229,32768
tv 138,229,34816
mvvv 64,144,180
tv 142,229,38912
cv 34,48,49
mvvv 64,148,188
tv 152,229,40960
tv 154,229,43008
mvvv 68,148,220
tv 158,229,47104
cv 35,50,51
mvvv 68,144,212
buffer_store_dwordx4 v[32:35],v244,s[8:11],s57 offen offset:0 sc0 sc1
mvvv 68,132,204
mvvv 68,128,196
mvvv 72,128,160
mvvv 72,132,168
mvvv 72,144,176
mvvv 72,148,184
mvvv 76,148,216
mvvv 76,144,208
mvvv 76,132,200
tv 148,228,61440
mvvv 76,128,192
tv 144,228,57344
s_waitcnt lgkmcnt(2)
mvvv 92,136,192
tv 132,228,53248
mvvv 92,140,200
tv 128,228,49152
tv 130,228,51200
mvvv 92,152,208
tv 134,228,55296
mvvv 92,156,216
tv 146,228,59392
mvvv 88,156,184
tv 150,228,63488
mvvv 88,152,176
mvvv 88,140,168
mvvv 88,136,160
mvvv 84,136,196
mvvv 84,140,204
mvvv 84,152,212
mvvv 84,156,220
mvvv 80,156,188
mvvv 80,152,180
mvvv 80,140,172
tv 156,229,61440
mvvv 80,136,164
tv 152,229,57344
s_waitcnt lgkmcnt(2)
mvva 64,128,196
tv 140,229,53248
mvva 64,132,204
tv 136,229,49152
tv 138,229,51200
mvva 64,144,212
tv 142,229,55296
mvva 64,148,220
tv 154,229,59392
mvva 68,148,252
tv 158,229,63488
mvva 68,144,244
mvva 68,132,236
mvva 68,128,228
s_xor_b32 s82,s59,12
mvva 72,128,192
s_mul_i32 s82,s82,12288
cv 64,64,65
s_add_u32 s82,s82,0
mvva 72,132,200
lv 16,244,82,0
mvva 72,144,208
cv 65,66,67
cv 68,68,69
mvva 72,148,216
cv 69,70,71
mvva 76,148,248
mvva 76,144,240
lv 20,244,82,1024
mvva 76,132,232
tv 148,230,28672
mvva 76,128,224
tv 144,230,24576
cv 72,72,73
s_waitcnt lgkmcnt(2)
mvva 92,136,224
tv 132,230,20480
cv 73,74,75
mvva 92,140,232
tv 128,230,16384
tv 130,230,18432
mvva 92,152,240
lv 24,244,82,2048
tv 134,230,22528
mvva 92,156,248
tv 146,230,26624
cv 76,76,77
mvva 88,156,216
tv 150,230,30720
cv 77,78,79
mvva 88,152,208
mvva 88,140,200
lv 28,244,82,3072
mvva 88,136,192
cv 78,92,93
cv 79,94,95
mvva 84,136,228
mvva 84,140,236
mvva 84,152,244
buffer_store_dwordx4 v[76:79],v244,s[8:11],s58 offen offset:3072 sc0 sc1
mvva 84,156,252
mvva 80,156,220
cv 74,88,89
cv 75,90,91
mvva 80,152,212
mvva 80,140,204
buffer_store_dwordx4 v[72:75],v244,s[8:11],s58 offen offset:2048 sc0 sc1
tv 156,231,28672
mvva 80,136,196
cv 70,84,85
cv 71,86,87
s_waitcnt lgkmcnt(1)
mvva 96,128,68
tv 140,231,20480
mvva 96,132,76
tv 136,231,16384
tv 138,231,18432
mvva 96,144,84
buffer_store_dwordx4 v[68:71],v244,s[8:11],s58 offen offset:1024 sc0 sc1
tv 142,231,22528
mvva 96,148,92
tv 152,231,24576
tv 154,231,26624
mvva 100,148,124
tv 158,231,30720
cv 66,80,81
cv 67,82,83
mvva 100,144,116
mvva 100,132,108
mvva 100,128,100
buffer_store_dwordx4 v[64:67],v244,s[8:11],s58 offen offset:0 sc0 sc1
mvva 104,128,64
mvva 104,132,72
mvva 104,144,80
mvva 104,148,88
mvva 108,148,120
mvva 108,144,112
mvva 108,132,104
tv 148,226,61440
mvva 108,128,96
tv 144,226,57344
s_waitcnt lgkmcnt(2)
mvva 124,136,96
tv 132,226,53248
mvva 124,140,104
tv 128,226,49152
tv 130,226,51200
mvva 124,152,112
tv 134,226,55296
mvva 124,156,120
tv 146,226,59392
mvva 120,156,88
tv 150,226,63488
mvva 120,152,80
mvva 120,140,72
mvva 120,136,64
mvva 116,136,100
mvva 116,140,108
s_waitcnt vmcnt(7)
v_max_u32_e32 v248,v16,v17
mvva 116,152,116
v_max3_u32 v248,v248,v18,v19
s_waitcnt vmcnt(6)
v_max3_u32 v248,v248,v20,v21
v_max3_u32 v248,v248,v22,v23
mvva 116,156,124
s_waitcnt vmcnt(5)
v_max3_u32 v248,v248,v24,v25
v_max3_u32 v248,v248,v26,v27
s_waitcnt vmcnt(4)
v_max3_u32 v248,v248,v28,v29
mvva 112,156,92
v_max3_u32 v248,v248,v30,v31
v_cmp_eq_u32_e32 vcc,-1,v248
mvva 112,152,84
s_cbranch_vccnz L_spx0o
L_sprx0o:
mvva 112,140,76
tv 156,227,61440
tv 158,227,63488
v_lshlrev_b32_e32 v251,16,v18
v_and_b32_e32 v255,4294901760,v18
mvva 112,136,68
tv 152,227,57344
tv 154,227,59392
s_waitcnt lgkmcnt(4)
mvva 96,128,132
v_lshlrev_b32_e32 v253,16,v19
v_and_b32_e32 v242,4294901760,v19
mvva 96,132,140
tv 136,227,49152
tv 138,227,51200
mvva 96,144,148
tv 140,227,53248
tv 142,227,55296
v_add_f32_e32 v112,v112,v251
mvva 96,148,156
v_add_f32_e32 v113,v113,v255
v_add_f32_e32 v114,v114,v253
mvva 100,148,188
v_add_f32_e32 v115,v115,v242
v_lshlrev_b32_e32 v251,16,v22
mvva 100,144,180
v_and_b32_e32 v255,4294901760,v22
v_lshlrev_b32_e32 v253,16,v23
mvva 100,132,172
v_and_b32_e32 v242,4294901760,v23
v_add_f32_e32 v116,v116,v251
v_add_f32_e32 v117,v117,v255
mvva 100,128,164
v_add_f32_e32 v118,v118,v253
v_add_f32_e32 v119,v119,v242
mvva 104,128,128
v_lshlrev_b32_e32 v251,16,v26
v_and_b32_e32 v255,4294901760,v26
mvva 104,132,136
v_lshlrev_b32_e32 v253,16,v27
v_and_b32_e32 v242,4294901760,v27
mvva 104,144,144
v_add_f32_e32 v120,v120,v251
v_add_f32_e32 v121,v121,v255
v_add_f32_e32 v122,v122,v253
mvva 104,148,152
v_add_f32_e32 v123,v123,v242
v_lshlrev_b32_e32 v251,16,v30
mvva 108,148,184
v_and_b32_e32 v255,4294901760,v30
v_lshlrev_b32_e32 v253,16,v31
mvva 108,144,176
v_and_b32_e32 v242,4294901760,v31
v_add_f32_e32 v124,v124,v251
mvva 108,132,168
v_add_f32_e32 v125,v125,v255
v_add_f32_e32 v126,v126,v253
v_add_f32_e32 v127,v127,v242
mvva 108,128,160
tv 144,234,24576
v_lshlrev_b32_e32 v251,16,v16
s_waitcnt lgkmcnt(1)
mvva 124,136,160
tv 132,234,20480
v_and_b32_e32 v255,4294901760,v16
mvva 124,140,168
tv 128,234,16384
tv 130,234,18432
mvva 124,152,176
tv 134,234,22528
v_lshlrev_b32_e32 v253,16,v17
mvva 124,156,184
tv 146,234,26624
v_and_b32_e32 v242,4294901760,v17
mvva 120,156,152
tv 148,234,28672
tv 150,234,30720
mvva 120,152,144
v_add_f32_e32 v96,v96,v251
v_add_f32_e32 v97,v97,v255
mvva 120,140,136
v_add_f32_e32 v98,v98,v253
v_add_f32_e32 v99,v99,v242
mvva 120,136,128
v_lshlrev_b32_e32 v251,16,v20
v_and_b32_e32 v255,4294901760,v20
mvva 116,136,164
v_lshlrev_b32_e32 v253,16,v21
v_and_b32_e32 v242,4294901760,v21
mvva 116,140,172
v_add_f32_e32 v100,v100,v251
v_add_f32_e32 v101,v101,v255
mvva 116,152,180
v_add_f32_e32 v102,v102,v253
v_add_f32_e32 v103,v103,v242
mvva 116,156,188
v_lshlrev_b32_e32 v251,16,v24
v_and_b32_e32 v255,4294901760,v24
mvva 112,156,156
v_lshlrev_b32_e32 v253,16,v25
v_and_b32_e32 v242,4294901760,v25
mvva 112,152,148
v_add_f32_e32 v104,v104,v251
v_add_f32_e32 v105,v105,v255
mvva 112,140,140
tv 156,235,28672
v_add_f32_e32 v106,v106,v253
mvva 112,136,132
tv 152,235,24576
v_add_f32_e32 v107,v107,v242
s_waitcnt lgkmcnt(2)
mvva 96,128,4
tv 140,235,20480
v_lshlrev_b32_e32 v251,16,v28
mvva 96,132,12
tv 136,235,16384
tv 138,235,18432
mvva 96,144,20
tv 142,235,22528
v_and_b32_e32 v255,4294901760,v28
mvva 96,148,28
tv 154,235,26624
v_lshlrev_b32_e32 v253,16,v29
mvva 100,148,60
tv 158,235,30720
v_and_b32_e32 v242,4294901760,v29
mvva 100,144,52
v_add_f32_e32 v108,v108,v251
v_add_f32_e32 v109,v109,v255
mvva 100,132,44
v_add_f32_e32 v110,v110,v253
v_add_f32_e32 v111,v111,v242
mvva 100,128,36
mvva 104,128,0
mvva 104,132,8
mvva 104,144,16
mvva 104,148,24
mvva 108,148,56
mvva 108,144,48
mvva 108,132,40
tv 148,230,45056
mvva 108,128,32
tv 144,230,40960
s_waitcnt lgkmcnt(2)
mvva 124,136,32
s_xor_b32 s83,s59,8
tv 132,230,36864
s_mul_i32 s83,s83,12288
mvva 124,140,40
s_add_u32 s83,s83,4096
tv 128,230,32768
tv 130,230,34816
mvva 124,152,48
lv 48,244,83,0
tv 134,230,38912
mvva 124,156,56
tv 146,230,43008
mvva 120,156,24
lv 52,244,83,1024
tv 150,230,47104
mvva 120,152,16
mvva 120,140,8
lv 56,244,83,2048
mvva 120,136,0
mvva 116,136,36
lv 60,244,83,3072
mvva 116,140,44
mvva 116,152,52
s_xor_b32 s84,s59,4
mvva 116,156,60
s_mul_i32 s84,s84,12288
mvva 112,156,28
s_add_u32 s84,s84,8192
mvva 112,152,20
L_vs15:
s_cbranch_execz L_vr1
lv 80,244,84,0
mvva 112,140,12
tv 156,231,45056
mvva 112,136,4
lv 84,244,84,1024
tv 152,231,40960
s_waitcnt lgkmcnt(2)
mvvv 96,128,164
tv 140,231,36864
mvvv 96,132,172
lv 88,244,84,2048
tv 136,231,32768
tv 138,231,34816
mvvv 96,144,180
tv 142,231,38912
mvvv 96,148,188
lv 92,244,84,3072
tv 154,231,43008
mvvv 100,148,220
tv 158,231,47104
mvvv 100,144,212
mvvv 100,132,204
mvvv 100,128,196
mvvv 104,128,160
mvvv 104,132,168
mvvv 104,144,176
mvvv 104,148,184
mvvv 108,148,216
mvvv 108,144,208
mvvv 108,132,200
tv 148,230,61440
mvvv 108,128,192
tv 144,230,57344
s_waitcnt lgkmcnt(2)
mvvv 124,136,192
tv 132,230,53248
mvvv 124,140,200
tv 128,230,49152
tv 130,230,51200
mvvv 124,152,208
tv 134,230,55296
mvvv 124,156,216
tv 146,230,59392
mvvv 120,156,184
tv 150,230,63488
mvvv 120,152,176
mvvv 120,140,168
mvvv 120,136,160
mvvv 116,136,196
mvvv 116,140,204
mvvv 116,152,212
mvvv 116,156,220
mvvv 112,156,188
mvvv 112,152,180
mvvv 112,140,172
tv 156,231,61440
mvvv 112,136,164
tv 152,231,57344
s_waitcnt lgkmcnt(2)
mvva 96,128,196
tv 140,231,53248
mvva 96,132,204
tv 136,231,49152
tv 138,231,51200
mvva 96,144,212
tv 142,231,55296
mvva 96,148,220
tv 154,231,59392
mvva 100,148,252
tv 158,231,63488
mvva 100,144,244
mvva 100,132,236
mvva 100,128,228
mvva 104,128,192
mvva 104,132,200
mvva 104,144,208
mvva 104,148,216
s_waitcnt vmcnt(7)
v_max_u32_e32 v248,v48,v49
mvva 108,148,248
v_max3_u32 v248,v248,v50,v51
s_waitcnt vmcnt(6)
v_max3_u32 v248,v248,v52,v53
mvva 108,144,240
v_max3_u32 v248,v248,v54,v55
s_waitcnt vmcnt(5)
v_max3_u32 v248,v248,v56,v57
mvva 108,132,232
v_max3_u32 v248,v248,v58,v59
s_waitcnt vmcnt(4)
v_max3_u32 v248,v248,v60,v61
mvva 108,128,224
v_max3_u32 v248,v248,v62,v63
v_cmp_eq_u32_e32 vcc,-1,v248
s_waitcnt lgkmcnt(0)
mvva 124,136,224
s_cbranch_vccnz L_spx1o
L_sprx1o:
mvva 124,140,232
mvva 124,152,240
mvva 124,156,248
s_waitcnt vmcnt(3)
v_max_u32_e32 v248,v80,v81
mvva 120,156,216
v_max3_u32 v248,v248,v82,v83
s_waitcnt vmcnt(2)
v_max3_u32 v248,v248,v84,v85
mvva 120,152,208
v_max3_u32 v248,v248,v86,v87
s_waitcnt vmcnt(1)
v_max3_u32 v248,v248,v88,v89
mvva 120,140,200
v_max3_u32 v248,v248,v90,v91
s_waitcnt vmcnt(0)
v_max3_u32 v248,v248,v92,v93
mvva 120,136,192
v_max3_u32 v248,v248,v94,v95
v_cmp_eq_u32_e32 vcc,-1,v248
mvva 116,136,228
s_cbranch_vccnz L_spx2o
L_sprx2o:
mvva 116,140,236
mvva 116,152,244
mvva 116,156,252
mvva 112,156,220
mvva 112,152,212
mvva 112,140,204
mvva 112,136,196
mvvv 108,238,60
mvvv 108,238,92
mvvv 124,236,60
mvvv 124,236,92
mvvv 104,238,56
mvvv 104,238,88
mvvv 120,236,56
mvvv 120,236,88
mvvv 100,238,52
mvvv 100,238,84
cv 108,108,109
cv 109,110,111
cv 110,124,125
cv 111,126,127
cv 104,104,105
v_permlane16_swap_b32_e32 v108,v110
mvvv 116,236,52
v_permlane16_swap_b32_e32 v109,v111
cv 105,106,107
cv 106,120,121
cv 107,122,123
mvvv 116,236,84
buffer_store_dwordx4 v[108:111],v243,s[44:47],s73 offen
v_permlane16_swap_b32_e32 v104,v106
v_permlane16_swap_b32_e32 v105,v107
mvvv 96,238,48
cv 100,100,101
cv 101,102,103
mvvv 96,238,80
buffer_store_dwordx4 v[104:107],v243,s[44:47],s72 offen
cv 102,116,117
cv 103,118,119
mvvv 112,236,48
v_permlane16_swap_b32_e32 v100,v102
v_permlane16_swap_b32_e32 v101,v103
mvvv 112,236,80
buffer_store_dwordx4 v[100:103],v243,s[44:47],s71 offen
cv 96,96,97
cv 97,98,99
s_nop 4
cv 98,112,113
cv 99,114,115
s_nop 0
v_permlane16_swap_b32_e32 v96,v98
v_permlane16_swap_b32_e32 v97,v99
buffer_store_dwordx4 v[96:99],v243,s[44:47],s70 offen
L_oddx:
s_endpgm
L_k_ec1:
buffer_store_dwordx4 a[252:255],v244,s[8:11],s35 offen offset:1024 sc0 sc1
s_branch L_k_ecr1
L_q_ack:
s_mov_b32 s86,65536
L_q_ack2:
s_sleep 8
buffer_load_dword v246,off,s[8:11],s35 offset:1024 sc1
s_waitcnt vmcnt(0)
v_readfirstlane_b32 s87,v246
s_nop 0
s_cmp_eq_u32 s87,s32
s_cbranch_scc1 L_q_acked
s_sub_u32 s86,s86,1
s_cbranch_scc0 L_q_ack2
s_branch L_q_acked
L_q_spin:
s_mov_b32 s6,65536
L_q_spin1:
s_sleep 8
buffer_load_dword v248,v250,s[8:11],0 offen sc1
s_or_b32 s7,%1,s12
s_cbranch_scc1 L_q_spin2
buffer_load_dwordx4 a[252:255],v244,s[8:11],s35 offen sc1
s_waitcnt vmcnt(0)
buffer_store_dwordx4 a[252:255],v244,s[8:11],s35 offen offset:1024 sc0 sc1
L_q_spin2:
s_waitcnt vmcnt(0)
v_and_b32_e32 v248,v242,v248
v_cmp_ne_u32_e64 s[100:101],v243,v248
s_nop 1
s_cmp_eq_u64 s[100:101],0
s_cbranch_scc1 L_q_ok
s_sub_u32 s6,s6,1
s_cbranch_scc0 L_q_spin1
s_branch L_q_ok
L_bx:
s_mov_b32 s87,65536
L_bx1:
s_sleep 2
buffer_load_dwordx4 v[176:179],v244,s[8:11],s98 offen sc1
buffer_load_dwordx2 v[168:169],v253,s[8:11],s98 offen offset:1024 sc1
buffer_load_dwordx2 v[172:173],v253,s[8:11],s98 offen offset:1536 sc1
s_waitcnt vmcnt(0)
v_max3_u32 v180,v176,v177,v178
v_max3_u32 v180,v180,v179,v168
v_max3_u32 v180,v180,v169,v172
v_max_u32_e32 v180,v180,v173
v_cmp_eq_u32_e64 s[40:41],-1,v180
s_nop 1
s_cmp_eq_u64 s[40:41],0
s_cbranch_scc1 L_bxr
s_sub_u32 s87,s87,1
s_cbranch_scc0 L_bx1
s_branch L_bxr
L_pend2:
s_nop 7
v_add_f32_e32 v226,v152,v153
v_add_f32_e32 v227,v156,v157
v_subrev_u32_e32 v236,0x23800000,v226
v_subrev_u32_e32 v237,0x23800000,v227
v_max_u32_e32 v236,v236,v237
v_cmp_lt_u32_e64 s[40:41],s48,v236
v_rcp_f32_e32 v226,v226
v_rcp_f32_e32 v227,v227
s_add_u32 s86,s13,4096
v_mov_b32_e32 v236,0
v_mov_b32_e32 v237,0
v_mov_b32_e32 v238,0
v_mov_b32_e32 v239,0
v_mov_b64_e32 v[152:153],0
v_mov_b64_e32 v[156:157],0
v_mul_f32_e32 v226,v242,v226
v_mul_f32_e32 v227,v243,v227
v_add_u32_e32 v225,s94,v225
v_pk_mul_f32 v[160:161],v[160:161],v[226:227] op_sel:[0,0] op_sel_hi:[1,0]
v_pk_mul_f32 v[162:163],v[162:163],v[226:227] op_sel:[0,0] op_sel_hi:[1,0]
v_pk_mul_f32 v[164:165],v[164:165],v[226:227] op_sel:[0,0] op_sel_hi:[1,0]
v_pk_mul_f32 v[166:167],v[166:167],v[226:227] op_sel:[0,0] op_sel_hi:[1,0]
cv 160,160,161
cv 161,162,163
cv 162,164,165
cv 163,166,167
buffer_store_dwordx4 v[160:163],v244,s[8:11],s13 offen offset:0 sc0 sc1
v_pk_mul_f32 v[168:169],v[168:169],v[226:227] op_sel:[0,0] op_sel_hi:[1,0]
v_pk_mul_f32 v[170:171],v[170:171],v[226:227] op_sel:[0,0] op_sel_hi:[1,0]
v_pk_mul_f32 v[172:173],v[172:173],v[226:227] op_sel:[0,0] op_sel_hi:[1,0]
v_pk_mul_f32 v[174:175],v[174:175],v[226:227] op_sel:[0,0] op_sel_hi:[1,0]
cv 168,168,169
cv 169,170,171
cv 170,172,173
cv 171,174,175
buffer_store_dwordx4 v[168:171],v244,s[8:11],s13 offen offset:2048 sc0 sc1
v_pk_mul_f32 v[176:177],v[176:177],v[226:227] op_sel:[0,0] op_sel_hi:[1,0]
v_pk_mul_f32 v[178:179],v[178:179],v[226:227] op_sel:[0,0] op_sel_hi:[1,0]
v_pk_mul_f32 v[180:181],v[180:181],v[226:227] op_sel:[0,0] op_sel_hi:[1,0]
v_pk_mul_f32 v[182:183],v[182:183],v[226:227] op_sel:[0,0] op_sel_hi:[1,0]
cv 176,176,177
cv 177,178,179
cv 178,180,181
v_mfma_f32_32x32x16_bf16 v[160:175],v[236:239],v[236:239],0
cv 179,182,183
buffer_store_dwordx4 v[176:179],v244,s[8:11],s86 offen offset:0 sc0 sc1
v_pk_mul_f32 v[184:185],v[184:185],v[226:227] op_sel:[0,0] op_sel_hi:[1,0]
v_pk_mul_f32 v[186:187],v[186:187],v[226:227] op_sel:[0,0] op_sel_hi:[1,0]
v_pk_mul_f32 v[188:189],v[188:189],v[226:227] op_sel:[0,0] op_sel_hi:[1,0]
v_pk_mul_f32 v[190:191],v[190:191],v[226:227] op_sel:[0,0] op_sel_hi:[1,0]
cv 184,184,185
cv 185,186,187
cv 186,188,189
cv 187,190,191
buffer_store_dwordx4 v[184:187],v244,s[8:11],s86 offen offset:2048 sc0 sc1
v_pk_mul_f32 v[192:193],v[192:193],v[226:227] op_sel:[0,1] op_sel_hi:[1,1]
v_pk_mul_f32 v[194:195],v[194:195],v[226:227] op_sel:[0,1] op_sel_hi:[1,1]
v_pk_mul_f32 v[196:197],v[196:197],v[226:227] op_sel:[0,1] op_sel_hi:[1,1]
v_pk_mul_f32 v[198:199],v[198:199],v[226:227] op_sel:[0,1] op_sel_hi:[1,1]
cv 192,192,193
cv 193,194,195
cv 194,196,197
v_mfma_f32_32x32x16_bf16 v[176:191],v[236:239],v[236:239],0
cv 195,198,199
buffer_store_dwordx4 v[192:195],v244,s[8:11],s13 offen offset:1024 sc0 sc1
v_pk_mul_f32 v[200:201],v[200:201],v[226:227] op_sel:[0,1] op_sel_hi:[1,1]
v_pk_mul_f32 v[202:203],v[202:203],v[226:227] op_sel:[0,1] op_sel_hi:[1,1]
v_pk_mul_f32 v[204:205],v[204:205],v[226:227] op_sel:[0,1] op_sel_hi:[1,1]
v_pk_mul_f32 v[206:207],v[206:207],v[226:227] op_sel:[0,1] op_sel_hi:[1,1]
cv 200,200,201
cv 201,202,203
cv 202,204,205
cv 203,206,207
buffer_store_dwordx4 v[200:203],v244,s[8:11],s13 offen offset:3072 sc0 sc1
v_pk_mul_f32 v[208:209],v[208:209],v[226:227] op_sel:[0,1] op_sel_hi:[1,1]
v_pk_mul_f32 v[210:211],v[210:211],v[226:227] op_sel:[0,1] op_sel_hi:[1,1]
v_pk_mul_f32 v[212:213],v[212:213],v[226:227] op_sel:[0,1] op_sel_hi:[1,1]
v_pk_mul_f32 v[214:215],v[214:215],v[226:227] op_sel:[0,1] op_sel_hi:[1,1]
cv 208,208,209
cv 209,210,211
cv 210,212,213
v_mfma_f32_32x32x16_bf16 v[192:207],v[236:239],v[236:239],0
cv 211,214,215
buffer_store_dwordx4 v[208:211],v244,s[8:11],s86 offen offset:1024 sc0 sc1
v_pk_mul_f32 v[216:217],v[216:217],v[226:227] op_sel:[0,1] op_sel_hi:[1,1]
v_pk_mul_f32 v[218:219],v[218:219],v[226:227] op_sel:[0,1] op_sel_hi:[1,1]
v_pk_mul_f32 v[220:221],v[220:221],v[226:227] op_sel:[0,1] op_sel_hi:[1,1]
v_pk_mul_f32 v[222:223],v[222:223],v[226:227] op_sel:[0,1] op_sel_hi:[1,1]
cv 216,216,217
cv 217,218,219
cv 218,220,221
cv 219,222,223
buffer_store_dwordx4 v[216:219],v244,s[8:11],s86 offen offset:3072 sc0 sc1
s_nop 1
v_mfma_f32_32x32x16_bf16 v[208:223],v[236:239],v[236:239],0
s_nop 7
s_nop 1
s_cmp_eq_u32 s84,4
s_cbranch_scc1 L_ret_eb4
s_cmp_lg_u32 s80,s81
s_branch L_ret_eb5
L_redo:
s_waitcnt vmcnt(0) lgkmcnt(0)
s_nop 15
s_nop 3
s_load_dwordx2 s[76:77],%0,0
s_load_dwordx2 s[80:81],%0,16
s_mov_b32 s78,4194304
s_mov_b32 s79,131072
s_mov_b32 s82,8388608
s_mov_b32 s83,131072
s_mov_b32 s86,0
s_waitcnt lgkmcnt(0)
L_rd_t:
s_nop 15
s_nop 3
s_cmp_lt_u32 s86,2
s_cselect_b32 s88,s23,s22
s_cselect_b32 s95,s13,s14
s_and_b32 s89,s86,1
s_lshl_b32 s90,s89,10
s_add_u32 s95,s95,s90
s_lshl_b32 s89,s89,6
s_add_u32 s88,s88,s89
s_lshl_b32 s89,s12,4
s_add_u32 s88,s88,s89
v_bfe_u32 v213,v244,4,4
v_lshrrev_b32_e32 v214,8,v244
v_add_u32_e32 v210,s88,v213
v_lshlrev_b32_e32 v208,11,v210
v_lshl_add_u32 v208,v214,4,v208
v_lshlrev_b32_e32 v209,16,v214
v_lshl_add_u32 v209,v213,1,v209
s_lshl_b32 s89,s18,8
v_add_u32_e32 v209,s89,v209
v_lshlrev_b32_e32 v214,2,v214
v_sub_u32_e32 v210,v210,v214
v_accvgpr_write_b32 a192,0
v_accvgpr_write_b32 a193,0
v_accvgpr_write_b32 a194,0
v_accvgpr_write_b32 a195,0
v_accvgpr_write_b32 a196,0
v_accvgpr_write_b32 a197,0
v_accvgpr_write_b32 a198,0
v_accvgpr_write_b32 a199,0
v_accvgpr_write_b32 a200,0
v_accvgpr_write_b32 a201,0
v_accvgpr_write_b32 a202,0
v_accvgpr_write_b32 a203,0
v_accvgpr_write_b32 a204,0
v_accvgpr_write_b32 a205,0
v_accvgpr_write_b32 a206,0
v_accvgpr_write_b32 a207,0
v_accvgpr_write_b32 a208,0
v_accvgpr_write_b32 a209,0
v_accvgpr_write_b32 a210,0
v_accvgpr_write_b32 a211,0
v_accvgpr_write_b32 a212,0
v_accvgpr_write_b32 a213,0
v_accvgpr_write_b32 a214,0
v_accvgpr_write_b32 a215,0
v_accvgpr_write_b32 a216,0
v_accvgpr_write_b32 a217,0
v_accvgpr_write_b32 a218,0
v_accvgpr_write_b32 a219,0
v_accvgpr_write_b32 a220,0
v_accvgpr_write_b32 a221,0
v_accvgpr_write_b32 a222,0
v_accvgpr_write_b32 a223,0
s_mov_b32 s87,0
L_rd_q:
s_nop 15
s_nop 3
s_lshl_b32 s89,s87,6
s_lshl_b32 s90,s87,18
buffer_load_dwordx4 v[200:203],v208,s[76:79],s89 offen
buffer_load_ushort v160,v209,s[80:83],s90 offen offset:0
buffer_load_ushort v164,v209,s[80:83],s90 offen offset:32
buffer_load_ushort v168,v209,s[80:83],s90 offen offset:64
buffer_load_ushort v172,v209,s[80:83],s90 offen offset:96
buffer_load_ushort v176,v209,s[80:83],s90 offen offset:128
buffer_load_ushort v180,v209,s[80:83],s90 offen offset:160
buffer_load_ushort v184,v209,s[80:83],s90 offen offset:192
buffer_load_ushort v188,v209,s[80:83],s90 offen offset:224
s_add_u32 s90,s90,8192
buffer_load_ushort v192,v209,s[80:83],s90 offen offset:0
buffer_load_ushort v193,v209,s[80:83],s90 offen offset:32
buffer_load_ushort v194,v209,s[80:83],s90 offen offset:64
buffer_load_ushort v195,v209,s[80:83],s90 offen offset:96
buffer_load_ushort v196,v209,s[80:83],s90 offen offset:128
buffer_load_ushort v197,v209,s[80:83],s90 offen offset:160
buffer_load_ushort v198,v209,s[80:83],s90 offen offset:192
buffer_load_ushort v199,v209,s[80:83],s90 offen offset:224
s_add_u32 s90,s90,8192
s_waitcnt vmcnt(0)
v_lshl_or_b32 v160,v192,16,v160
v_lshl_or_b32 v164,v193,16,v164
v_lshl_or_b32 v168,v194,16,v168
v_lshl_or_b32 v172,v195,16,v172
v_lshl_or_b32 v176,v196,16,v176
v_lshl_or_b32 v180,v197,16,v180
v_lshl_or_b32 v184,v198,16,v184
v_lshl_or_b32 v188,v199,16,v188
buffer_load_ushort v161,v209,s[80:83],s90 offen offset:0
buffer_load_ushort v165,v209,s[80:83],s90 offen offset:32
buffer_load_ushort v169,v209,s[80:83],s90 offen offset:64
buffer_load_ushort v173,v209,s[80:83],s90 offen offset:96
buffer_load_ushort v177,v209,s[80:83],s90 offen offset:128
buffer_load_ushort v181,v209,s[80:83],s90 offen offset:160
buffer_load_ushort v185,v209,s[80:83],s90 offen offset:192
buffer_load_ushort v189,v209,s[80:83],s90 offen offset:224
s_add_u32 s90,s90,8192
buffer_load_ushort v192,v209,s[80:83],s90 offen offset:0
buffer_load_ushort v193,v209,s[80:83],s90 offen offset:32
buffer_load_ushort v194,v209,s[80:83],s90 offen offset:64
buffer_load_ushort v195,v209,s[80:83],s90 offen offset:96
buffer_load_ushort v196,v209,s[80:83],s90 offen offset:128
buffer_load_ushort v197,v209,s[80:83],s90 offen offset:160
buffer_load_ushort v198,v209,s[80:83],s90 offen offset:192
buffer_load_ushort v199,v209,s[80:83],s90 offen offset:224
s_add_u32 s90,s90,8192
s_waitcnt vmcnt(0)
v_lshl_or_b32 v161,v192,16,v161
v_lshl_or_b32 v165,v193,16,v165
v_lshl_or_b32 v169,v194,16,v169
v_lshl_or_b32 v173,v195,16,v173
v_lshl_or_b32 v177,v196,16,v177
v_lshl_or_b32 v181,v197,16,v181
v_lshl_or_b32 v185,v198,16,v185
v_lshl_or_b32 v189,v199,16,v189
buffer_load_ushort v162,v209,s[80:83],s90 offen offset:0
buffer_load_ushort v166,v209,s[80:83],s90 offen offset:32
buffer_load_ushort v170,v209,s[80:83],s90 offen offset:64
buffer_load_ushort v174,v209,s[80:83],s90 offen offset:96
buffer_load_ushort v178,v209,s[80:83],s90 offen offset:128
buffer_load_ushort v182,v209,s[80:83],s90 offen offset:160
buffer_load_ushort v186,v209,s[80:83],s90 offen offset:192
buffer_load_ushort v190,v209,s[80:83],s90 offen offset:224
s_add_u32 s90,s90,8192
buffer_load_ushort v192,v209,s[80:83],s90 offen offset:0
buffer_load_ushort v193,v209,s[80:83],s90 offen offset:32
buffer_load_ushort v194,v209,s[80:83],s90 offen offset:64
buffer_load_ushort v195,v209,s[80:83],s90 offen offset:96
buffer_load_ushort v196,v209,s[80:83],s90 offen offset:128
buffer_load_ushort v197,v209,s[80:83],s90 offen offset:160
buffer_load_ushort v198,v209,s[80:83],s90 offen offset:192
buffer_load_ushort v199,v209,s[80:83],s90 offen offset:224
s_add_u32 s90,s90,8192
s_waitcnt vmcnt(0)
v_lshl_or_b32 v162,v192,16,v162
v_lshl_or_b32 v166,v193,16,v166
v_lshl_or_b32 v170,v194,16,v170
v_lshl_or_b32 v174,v195,16,v174
v_lshl_or_b32 v178,v196,16,v178
v_lshl_or_b32 v182,v197,16,v182
v_lshl_or_b32 v186,v198,16,v186
v_lshl_or_b32 v190,v199,16,v190
buffer_load_ushort v163,v209,s[80:83],s90 offen offset:0
buffer_load_ushort v167,v209,s[80:83],s90 offen offset:32
buffer_load_ushort v171,v209,s[80:83],s90 offen offset:64
buffer_load_ushort v175,v209,s[80:83],s90 offen offset:96
buffer_load_ushort v179,v209,s[80:83],s90 offen offset:128
buffer_load_ushort v183,v209,s[80:83],s90 offen offset:160
buffer_load_ushort v187,v209,s[80:83],s90 offen offset:192
buffer_load_ushort v191,v209,s[80:83],s90 offen offset:224
s_add_u32 s90,s90,8192
buffer_load_ushort v192,v209,s[80:83],s90 offen offset:0
buffer_load_ushort v193,v209,s[80:83],s90 offen offset:32
buffer_load_ushort v194,v209,s[80:83],s90 offen offset:64
buffer_load_ushort v195,v209,s[80:83],s90 offen offset:96
buffer_load_ushort v196,v209,s[80:83],s90 offen offset:128
buffer_load_ushort v197,v209,s[80:83],s90 offen offset:160
buffer_load_ushort v198,v209,s[80:83],s90 offen offset:192
buffer_load_ushort v199,v209,s[80:83],s90 offen offset:224
s_add_u32 s90,s90,8192
s_waitcnt vmcnt(0)
v_lshl_or_b32 v163,v192,16,v163
v_lshl_or_b32 v167,v193,16,v167
v_lshl_or_b32 v171,v194,16,v171
v_lshl_or_b32 v175,v195,16,v175
v_lshl_or_b32 v179,v196,16,v179
v_lshl_or_b32 v183,v197,16,v183
v_lshl_or_b32 v187,v198,16,v187
v_lshl_or_b32 v191,v199,16,v191
mavv 192,160,200
mavv 196,164,200
mavv 200,168,200
mavv 204,172,200
mavv 208,176,200
mavv 212,180,200
mavv 216,184,200
mavv 220,188,200
s_add_u32 s87,s87,1
s_cmp_lt_u32 s87,32
s_cbranch_scc1 L_rd_q
s_nop 15
s_nop 3
v_accvgpr_read_b32 v192,a192
v_accvgpr_read_b32 v193,a193
v_accvgpr_read_b32 v194,a194
v_accvgpr_read_b32 v195,a195
v_accvgpr_read_b32 v196,a196
v_accvgpr_read_b32 v197,a197
v_accvgpr_read_b32 v198,a198
v_accvgpr_read_b32 v199,a199
v_mul_f32_e32 v192,0x3e0293ee,v192
v_mul_f32_e32 v193,0x3e0293ee,v193
v_mul_f32_e32 v194,0x3e0293ee,v194
v_mul_f32_e32 v195,0x3e0293ee,v195
v_mul_f32_e32 v196,0x3e0293ee,v196
v_mul_f32_e32 v197,0x3e0293ee,v197
v_mul_f32_e32 v198,0x3e0293ee,v198
v_mul_f32_e32 v199,0x3e0293ee,v199
cv 192,192,193
cv 193,194,195
cv 194,196,197
cv 195,198,199
v_accvgpr_write_b32 a224,v192
v_accvgpr_write_b32 a225,v193
v_accvgpr_write_b32 a226,v194
v_accvgpr_write_b32 a227,v195
v_accvgpr_read_b32 v192,a200
v_accvgpr_read_b32 v193,a201
v_accvgpr_read_b32 v194,a202
v_accvgpr_read_b32 v195,a203
v_accvgpr_read_b32 v196,a204
v_accvgpr_read_b32 v197,a205
v_accvgpr_read_b32 v198,a206
v_accvgpr_read_b32 v199,a207
v_mul_f32_e32 v192,0x3e0293ee,v192
v_mul_f32_e32 v193,0x3e0293ee,v193
v_mul_f32_e32 v194,0x3e0293ee,v194
v_mul_f32_e32 v195,0x3e0293ee,v195
v_mul_f32_e32 v196,0x3e0293ee,v196
v_mul_f32_e32 v197,0x3e0293ee,v197
v_mul_f32_e32 v198,0x3e0293ee,v198
)"
R"(v_mul_f32_e32 v199,0x3e0293ee,v199
cv 192,192,193
cv 193,194,195
cv 194,196,197
cv 195,198,199
v_accvgpr_write_b32 a228,v192
v_accvgpr_write_b32 a229,v193
v_accvgpr_write_b32 a230,v194
v_accvgpr_write_b32 a231,v195
v_accvgpr_read_b32 v192,a208
v_accvgpr_read_b32 v193,a209
v_accvgpr_read_b32 v194,a210
v_accvgpr_read_b32 v195,a211
v_accvgpr_read_b32 v196,a212
v_accvgpr_read_b32 v197,a213
v_accvgpr_read_b32 v198,a214
v_accvgpr_read_b32 v199,a215
v_mul_f32_e32 v192,0x3e0293ee,v192
v_mul_f32_e32 v193,0x3e0293ee,v193
v_mul_f32_e32 v194,0x3e0293ee,v194
v_mul_f32_e32 v195,0x3e0293ee,v195
v_mul_f32_e32 v196,0x3e0293ee,v196
v_mul_f32_e32 v197,0x3e0293ee,v197
v_mul_f32_e32 v198,0x3e0293ee,v198
v_mul_f32_e32 v199,0x3e0293ee,v199
cv 192,192,193
cv 193,194,195
cv 194,196,197
cv 195,198,199
v_accvgpr_write_b32 a232,v192
v_accvgpr_write_b32 a233,v193
v_accvgpr_write_b32 a234,v194
v_accvgpr_write_b32 a235,v195
v_accvgpr_read_b32 v192,a216
v_accvgpr_read_b32 v193,a217
v_accvgpr_read_b32 v194,a218
v_accvgpr_read_b32 v195,a219
v_accvgpr_read_b32 v196,a220
v_accvgpr_read_b32 v197,a221
v_accvgpr_read_b32 v198,a222
v_accvgpr_read_b32 v199,a223
v_mul_f32_e32 v192,0x3e0293ee,v192
v_mul_f32_e32 v193,0x3e0293ee,v193
v_mul_f32_e32 v194,0x3e0293ee,v194
v_mul_f32_e32 v195,0x3e0293ee,v195
v_mul_f32_e32 v196,0x3e0293ee,v196
v_mul_f32_e32 v197,0x3e0293ee,v197
v_mul_f32_e32 v198,0x3e0293ee,v198
v_mul_f32_e32 v199,0x3e0293ee,v199
cv 192,192,193
cv 193,194,195
cv 194,196,197
cv 195,198,199
v_accvgpr_write_b32 a236,v192
v_accvgpr_write_b32 a237,v193
v_accvgpr_write_b32 a238,v194
v_accvgpr_write_b32 a239,v195
v_mov_b32_e32 v160,0
v_mov_b32_e32 v161,0
v_mov_b32_e32 v162,0
v_mov_b32_e32 v163,0
v_mov_b32_e32 v164,0
v_mov_b32_e32 v165,0
v_mov_b32_e32 v166,0
v_mov_b32_e32 v167,0
v_mov_b32_e32 v168,0
v_mov_b32_e32 v169,0
v_mov_b32_e32 v170,0
v_mov_b32_e32 v171,0
v_mov_b32_e32 v172,0
v_mov_b32_e32 v173,0
v_mov_b32_e32 v174,0
v_mov_b32_e32 v175,0
v_mov_b32_e32 v176,0
v_mov_b32_e32 v177,0
v_mov_b32_e32 v178,0
v_mov_b32_e32 v179,0
v_mov_b32_e32 v180,0
v_mov_b32_e32 v181,0
v_mov_b32_e32 v182,0
v_mov_b32_e32 v183,0
v_mov_b32_e32 v184,0
v_mov_b32_e32 v185,0
v_mov_b32_e32 v186,0
v_mov_b32_e32 v187,0
v_mov_b32_e32 v188,0
v_mov_b32_e32 v189,0
v_mov_b32_e32 v190,0
v_mov_b32_e32 v191,0
v_mov_b32_e32 v216,0xf149f2ca
v_mov_b32_e32 v211,v216
v_mov_b32_e32 v212,0
s_lshr_b32 s91,s88,5
s_mov_b32 s87,0
L_rd_a:
s_nop 15
s_nop 3
s_mul_i32 s89,s18,524288
s_lshl_b32 s90,s87,13
s_add_u32 s89,s89,s90
s_add_u32 s90,s89,16777216
la 64,244,90,0
la 68,244,90,1024
la 72,244,90,2048
la 76,244,90,3072
s_add_u32 s90,s89,16781312
la 80,244,90,0
la 84,244,90,1024
la 88,244,90,2048
la 92,244,90,3072
s_add_u32 s90,s89,33554432
la 96,244,90,0
la 100,244,90,1024
la 104,244,90,2048
la 108,244,90,3072
s_add_u32 s90,s89,33558528
la 112,244,90,0
la 116,244,90,1024
la 120,244,90,2048
la 124,244,90,3072
s_waitcnt vmcnt(0)
zvaa 192,64,224
mvaa 192,72,228
mvaa 192,80,232
mvaa 192,88,236
zvaa 196,68,224
mvaa 196,76,228
mvaa 196,84,232
mvaa 196,92,236
v_cmp_gt_i32_e32 vcc,0,v210
s_nop 2
v_cndmask_b32_e32 v192,v192,v216,vcc
v_cmp_gt_i32_e32 vcc,1,v210
s_nop 1
v_cndmask_b32_e32 v193,v193,v216,vcc
v_cmp_gt_i32_e32 vcc,2,v210
s_nop 1
v_cndmask_b32_e32 v194,v194,v216,vcc
v_cmp_gt_i32_e32 vcc,3,v210
s_nop 1
v_cndmask_b32_e32 v195,v195,v216,vcc
v_cmp_gt_i32_e32 vcc,16,v210
s_nop 1
v_cndmask_b32_e32 v196,v196,v216,vcc
v_cmp_gt_i32_e32 vcc,17,v210
s_nop 1
v_cndmask_b32_e32 v197,v197,v216,vcc
v_cmp_gt_i32_e32 vcc,18,v210
s_nop 1
v_cndmask_b32_e32 v198,v198,v216,vcc
v_cmp_gt_i32_e32 vcc,19,v210
s_nop 1
v_cndmask_b32_e32 v199,v199,v216,vcc
v_max3_f32 v213,v192,v193,v194
v_max3_f32 v214,v195,v196,v197
v_max3_f32 v213,v213,v198,v199
v_max_f32_e32 v213,v213,v214
v_mov_b32_e32 v214,v213
s_nop 1
v_permlane16_swap_b32_e32 v213,v214
v_max_f32_e32 v213,v213,v214
v_mov_b32_e32 v214,v213
s_nop 1
v_permlane32_swap_b32_e32 v213,v214
v_max_f32_e32 v213,v213,v214
v_max_f32_e32 v213,v213,v211
v_sub_f32_e32 v215,v211,v213
ex 215
v_mov_b32_e32 v211,v213
v_sub_f32_e32 v192,v192,v211
v_sub_f32_e32 v193,v193,v211
v_sub_f32_e32 v194,v194,v211
v_sub_f32_e32 v195,v195,v211
v_sub_f32_e32 v196,v196,v211
v_sub_f32_e32 v197,v197,v211
v_sub_f32_e32 v198,v198,v211
v_sub_f32_e32 v199,v199,v211
ex 192
ex 193
ex 194
ex 195
ex 196
ex 197
ex 198
ex 199
v_mul_f32_e32 v212,v215,v212
v_add_f32_e32 v212,v212,v192
v_add_f32_e32 v212,v212,v193
v_add_f32_e32 v212,v212,v194
v_add_f32_e32 v212,v212,v195
v_add_f32_e32 v212,v212,v196
v_add_f32_e32 v212,v212,v197
v_add_f32_e32 v212,v212,v198
v_add_f32_e32 v212,v212,v199
cv 192,192,193
cv 193,194,195
cv 194,196,197
cv 195,198,199
v_mul_f32_e32 v160,v215,v160
v_mul_f32_e32 v161,v215,v161
v_mul_f32_e32 v162,v215,v162
v_mul_f32_e32 v163,v215,v163
v_mul_f32_e32 v164,v215,v164
v_mul_f32_e32 v165,v215,v165
v_mul_f32_e32 v166,v215,v166
v_mul_f32_e32 v167,v215,v167
v_mul_f32_e32 v168,v215,v168
v_mul_f32_e32 v169,v215,v169
v_mul_f32_e32 v170,v215,v170
v_mul_f32_e32 v171,v215,v171
v_mul_f32_e32 v172,v215,v172
v_mul_f32_e32 v173,v215,v173
v_mul_f32_e32 v174,v215,v174
v_mul_f32_e32 v175,v215,v175
v_mul_f32_e32 v176,v215,v176
v_mul_f32_e32 v177,v215,v177
v_mul_f32_e32 v178,v215,v178
v_mul_f32_e32 v179,v215,v179
v_mul_f32_e32 v180,v215,v180
v_mul_f32_e32 v181,v215,v181
v_mul_f32_e32 v182,v215,v182
v_mul_f32_e32 v183,v215,v183
v_mul_f32_e32 v184,v215,v184
v_mul_f32_e32 v185,v215,v185
v_mul_f32_e32 v186,v215,v186
v_mul_f32_e32 v187,v215,v187
v_mul_f32_e32 v188,v215,v188
v_mul_f32_e32 v189,v215,v189
v_mul_f32_e32 v190,v215,v190
v_mul_f32_e32 v191,v215,v191
mvav 160,96,192
mvav 164,100,192
mvav 168,104,192
mvav 172,108,192
mvav 176,112,192
mvav 180,116,192
mvav 184,120,192
mvav 188,124,192
v_subrev_u32_e32 v210,32,v210
s_add_u32 s87,s87,1
s_cmp_le_u32 s87,s91
s_cbranch_scc1 L_rd_a
s_nop 15
s_nop 3
s_lshl_b32 s89,s88,2
s_add_u32 s89,s89,83886080
v_bfe_u32 v213,v244,2,6
buffer_load_dword v215,v213,s[8:11],s89 offen sc1
v_mov_b32_e32 v214,v212
s_nop 1
v_permlane16_swap_b32_e32 v212,v214
v_add_f32_e32 v212,v212,v214
v_mov_b32_e32 v214,v212
s_nop 1
v_permlane32_swap_b32_e32 v212,v214
v_add_f32_e32 v212,v212,v214
v_rcp_f32_e32 v212,v212
s_waitcnt vmcnt(0)
v_mul_f32_e32 v215,v215,v212
v_mul_f32_e32 v160,v215,v160
v_mul_f32_e32 v161,v215,v161
v_mul_f32_e32 v162,v215,v162
v_mul_f32_e32 v163,v215,v163
v_mul_f32_e32 v164,v215,v164
v_mul_f32_e32 v165,v215,v165
v_mul_f32_e32 v166,v215,v166
v_mul_f32_e32 v167,v215,v167
v_mul_f32_e32 v168,v215,v168
v_mul_f32_e32 v169,v215,v169
v_mul_f32_e32 v170,v215,v170
v_mul_f32_e32 v171,v215,v171
v_mul_f32_e32 v172,v215,v172
v_mul_f32_e32 v173,v215,v173
v_mul_f32_e32 v174,v215,v174
v_mul_f32_e32 v175,v215,v175
v_mul_f32_e32 v176,v215,v176
v_mul_f32_e32 v177,v215,v177
v_mul_f32_e32 v178,v215,v178
v_mul_f32_e32 v179,v215,v179
v_mul_f32_e32 v180,v215,v180
v_mul_f32_e32 v181,v215,v181
v_mul_f32_e32 v182,v215,v182
v_mul_f32_e32 v183,v215,v183
v_mul_f32_e32 v184,v215,v184
v_mul_f32_e32 v185,v215,v185
v_mul_f32_e32 v186,v215,v186
v_mul_f32_e32 v187,v215,v187
v_mul_f32_e32 v188,v215,v188
v_mul_f32_e32 v189,v215,v189
v_mul_f32_e32 v190,v215,v190
v_mul_f32_e32 v191,v215,v191
s_add_u32 s89,s95,4096
cv 160,160,161
cv 161,162,163
cv 162,164,165
cv 163,166,167
buffer_store_dwordx4 v[160:163],v244,s[8:11],s95 offen offset:0 sc0 sc1
cv 168,168,169
cv 169,170,171
cv 170,172,173
cv 171,174,175
buffer_store_dwordx4 v[168:171],v244,s[8:11],s95 offen offset:2048 sc0 sc1
cv 176,176,177
cv 177,178,179
cv 178,180,181
cv 179,182,183
buffer_store_dwordx4 v[176:179],v244,s[8:11],s89 offen offset:0 sc0 sc1
cv 184,184,185
cv 185,186,187
cv 186,188,189
cv 187,190,191
buffer_store_dwordx4 v[184:187],v244,s[8:11],s89 offen offset:2048 sc0 sc1
s_waitcnt vmcnt(0)
v_mov_b32_e32 v192,v160
v_accvgpr_read_b32 v160,a240
v_accvgpr_write_b32 a240,v192
v_mov_b32_e32 v193,v161
v_accvgpr_read_b32 v161,a241
v_accvgpr_write_b32 a241,v193
v_mov_b32_e32 v194,v162
v_accvgpr_read_b32 v162,a242
v_accvgpr_write_b32 a242,v194
v_mov_b32_e32 v195,v163
v_accvgpr_read_b32 v163,a243
v_accvgpr_write_b32 a243,v195
v_mov_b32_e32 v200,v168
v_accvgpr_read_b32 v168,a244
v_accvgpr_write_b32 a244,v200
v_mov_b32_e32 v201,v169
v_accvgpr_read_b32 v169,a245
v_accvgpr_write_b32 a245,v201
v_mov_b32_e32 v202,v170
v_accvgpr_read_b32 v170,a246
v_accvgpr_write_b32 a246,v202
v_mov_b32_e32 v203,v171
v_accvgpr_read_b32 v171,a247
v_accvgpr_write_b32 a247,v203
v_mov_b32_e32 v208,v176
v_accvgpr_read_b32 v176,a248
v_accvgpr_write_b32 a248,v208
v_mov_b32_e32 v209,v177
v_accvgpr_read_b32 v177,a249
v_accvgpr_write_b32 a249,v209
v_mov_b32_e32 v210,v178
v_accvgpr_read_b32 v178,a250
v_accvgpr_write_b32 a250,v210
v_mov_b32_e32 v211,v179
v_accvgpr_read_b32 v179,a251
v_accvgpr_write_b32 a251,v211
v_mov_b32_e32 v216,v184
v_accvgpr_read_b32 v184,a252
v_accvgpr_write_b32 a252,v216
v_mov_b32_e32 v217,v185
v_accvgpr_read_b32 v185,a253
v_accvgpr_write_b32 a253,v217
v_mov_b32_e32 v218,v186
v_accvgpr_read_b32 v186,a254
v_accvgpr_write_b32 a254,v218
v_mov_b32_e32 v219,v187
v_accvgpr_read_b32 v187,a255
v_accvgpr_write_b32 a255,v219
s_add_u32 s86,s86,1
s_cmp_lt_u32 s86,4
s_cbranch_scc1 L_rd_t
s_nop 15
s_nop 3
s_nop 1
s_cmp_eq_u32 s84,4
s_cbranch_scc1 L_redo_r4
s_cmp_lg_u32 s80,s81
s_branch L_redo_r5
L_k_ev:
L_v_ev:
buffer_load_dwordx4 a[252:255],v244,s[8:11],s35 offen sc1
s_branch L_k_evr
L_spbe:
s_mov_b32 s91,65536
L_splbe:
s_sleep 2
buffer_load_dword v253,v224,s[8:11],s53 offen sc1
s_waitcnt vmcnt(0)
v_cmp_ne_u32_e32 vcc,4,v253
s_nop 1
s_cbranch_vccz L_sprbe
s_sub_u32 s91,s91,1
s_cbranch_scc0 L_splbe
s_branch L_sprbe
L_spa3e:
s_mov_b32 s91,65536
L_spla3e:
s_sleep 2
buffer_load_dword v253,v242,s[8:11],s53 offen sc1
s_waitcnt vmcnt(0)
v_cmp_ne_u32_e32 vcc,4,v253
s_nop 1
s_cbranch_vccz L_spra3e
s_sub_u32 s91,s91,1
s_cbranch_scc0 L_spla3e
s_branch L_spra3e
L_spa4e:
s_mov_b32 s91,65536
L_spla4e:
s_sleep 2
buffer_load_dword v253,v242,s[8:11],s53 offen sc1
s_waitcnt vmcnt(0)
v_cmp_ne_u32_e32 vcc,4,v253
s_nop 1
s_cbranch_vccz L_spra4e
s_sub_u32 s91,s91,1
s_cbranch_scc0 L_spla4e
s_branch L_spra4e
L_spa5e:
s_mov_b32 s91,65536
L_spla5e:
s_sleep 2
buffer_load_dword v253,v242,s[8:11],s53 offen sc1
s_waitcnt vmcnt(0)
v_cmp_ne_u32_e32 vcc,4,v253
s_nop 1
s_cbranch_vccz L_spra5e
s_sub_u32 s91,s91,1
s_cbranch_scc0 L_spla5e
s_branch L_spra5e
L_spa6e:
s_mov_b32 s91,65536
L_spla6e:
s_sleep 2
buffer_load_dword v253,v242,s[8:11],s53 offen sc1
s_waitcnt vmcnt(0)
v_cmp_ne_u32_e32 vcc,4,v253
s_nop 1
s_cbranch_vccz L_spra6e
s_sub_u32 s91,s91,1
s_cbranch_scc0 L_spla6e
s_branch L_spra6e
L_spa7e:
s_mov_b32 s91,65536
L_spla7e:
s_sleep 2
buffer_load_dword v253,v242,s[8:11],s53 offen sc1
s_waitcnt vmcnt(0)
v_cmp_ne_u32_e32 vcc,4,v253
s_nop 1
s_cbranch_vccz L_spra7e
s_sub_u32 s91,s91,1
s_cbranch_scc0 L_spla7e
s_branch L_spra7e
L_spa8e:
s_mov_b32 s91,65536
L_spla8e:
s_sleep 2
buffer_load_dword v253,v242,s[8:11],s53 offen sc1
s_waitcnt vmcnt(0)
v_cmp_ne_u32_e32 vcc,4,v253
s_nop 1
s_cbranch_vccz L_spra8e
s_sub_u32 s91,s91,1
s_cbranch_scc0 L_spla8e
s_branch L_spra8e
L_spa9e:
s_mov_b32 s91,65536
L_spla9e:
s_sleep 2
buffer_load_dword v253,v242,s[8:11],s53 offen sc1
s_waitcnt vmcnt(0)
v_cmp_ne_u32_e32 vcc,4,v253
s_nop 1
s_cbranch_vccz L_spra9e
s_sub_u32 s91,s91,1
s_cbranch_scc0 L_spla9e
s_branch L_spra9e
L_spa10e:
s_mov_b32 s91,65536
L_spla10e:
s_sleep 2
buffer_load_dword v253,v242,s[8:11],s53 offen sc1
s_waitcnt vmcnt(0)
v_cmp_ne_u32_e32 vcc,4,v253
s_nop 1
s_cbranch_vccz L_spra10e
s_sub_u32 s91,s91,1
s_cbranch_scc0 L_spla10e
s_branch L_spra10e
L_sppe:
s_mov_b32 s91,65536
L_splpe:
s_sleep 2
buffer_load_dword v253,v242,s[8:11],s53 offen sc1
s_waitcnt vmcnt(0)
v_cmp_ne_u32_e32 vcc,4,v253
s_nop 1
s_cbranch_vccz L_sprpe
s_sub_u32 s91,s91,1
s_cbranch_scc0 L_splpe
s_branch L_sprpe
L_spx0e:
s_mov_b32 s91,65536
L_splx0e:
lv 16,244,82,0
lv 20,244,82,1024
lv 24,244,82,2048
lv 28,244,82,3072
s_waitcnt vmcnt(0)
v_max_u32_e32 v248,v16,v17
v_max3_u32 v248,v248,v18,v19
v_max3_u32 v248,v248,v20,v21
v_max3_u32 v248,v248,v22,v23
v_max3_u32 v248,v248,v24,v25
v_max3_u32 v248,v248,v26,v27
v_max3_u32 v248,v248,v28,v29
v_max3_u32 v248,v248,v30,v31
v_cmp_eq_u32_e32 vcc,-1,v248
s_nop 1
s_cbranch_vccz L_sprx0e
s_sub_u32 s91,s91,1
s_cbranch_scc0 L_splx0e
s_branch L_sprx0e
L_spx1e:
s_mov_b32 s91,65536
L_splx1e:
lv 48,244,83,0
lv 52,244,83,1024
lv 56,244,83,2048
lv 60,244,83,3072
s_waitcnt vmcnt(0)
v_max_u32_e32 v248,v48,v49
v_max3_u32 v248,v248,v50,v51
v_max3_u32 v248,v248,v52,v53
v_max3_u32 v248,v248,v54,v55
v_max3_u32 v248,v248,v56,v57
v_max3_u32 v248,v248,v58,v59
v_max3_u32 v248,v248,v60,v61
v_max3_u32 v248,v248,v62,v63
v_cmp_eq_u32_e32 vcc,-1,v248
s_nop 1
s_cbranch_vccz L_sprx1e
s_sub_u32 s91,s91,1
s_cbranch_scc0 L_splx1e
s_branch L_sprx1e
L_spx2e:
s_mov_b32 s91,65536
L_splx2e:
lv 80,244,84,0
lv 84,244,84,1024
lv 88,244,84,2048
lv 92,244,84,3072
s_waitcnt vmcnt(0)
v_max_u32_e32 v248,v80,v81
v_max3_u32 v248,v248,v82,v83
v_max3_u32 v248,v248,v84,v85
v_max3_u32 v248,v248,v86,v87
v_max3_u32 v248,v248,v88,v89
v_max3_u32 v248,v248,v90,v91
v_max3_u32 v248,v248,v92,v93
v_max3_u32 v248,v248,v94,v95
v_cmp_eq_u32_e32 vcc,-1,v248
s_nop 1
s_cbranch_vccz L_sprx2e
s_sub_u32 s91,s91,1
s_cbranch_scc0 L_splx2e
s_branch L_sprx2e
L_spbo:
s_mov_b32 s91,65536
L_splbo:
s_sleep 2
buffer_load_dword v253,v224,s[8:11],s53 offen sc1
s_waitcnt vmcnt(0)
v_cmp_ne_u32_e32 vcc,4,v253
s_nop 1
s_cbranch_vccz L_sprbo
s_sub_u32 s91,s91,1
s_cbranch_scc0 L_splbo
s_branch L_sprbo
L_spa3o:
s_mov_b32 s91,65536
L_spla3o:
s_sleep 2
buffer_load_dword v253,v242,s[8:11],s53 offen sc1
s_waitcnt vmcnt(0)
v_cmp_ne_u32_e32 vcc,4,v253
s_nop 1
s_cbranch_vccz L_spra3o
s_sub_u32 s91,s91,1
s_cbranch_scc0 L_spla3o
s_branch L_spra3o
L_spa4o:
s_mov_b32 s91,65536
L_spla4o:
s_sleep 2
buffer_load_dword v253,v242,s[8:11],s53 offen sc1
s_waitcnt vmcnt(0)
v_cmp_ne_u32_e32 vcc,4,v253
s_nop 1
s_cbranch_vccz L_spra4o
s_sub_u32 s91,s91,1
s_cbranch_scc0 L_spla4o
s_branch L_spra4o
L_spa5o:
s_mov_b32 s91,65536
L_spla5o:
s_sleep 2
buffer_load_dword v253,v242,s[8:11],s53 offen sc1
s_waitcnt vmcnt(0)
v_cmp_ne_u32_e32 vcc,4,v253
s_nop 1
s_cbranch_vccz L_spra5o
s_sub_u32 s91,s91,1
s_cbranch_scc0 L_spla5o
s_branch L_spra5o
L_spa6o:
s_mov_b32 s91,65536
L_spla6o:
s_sleep 2
buffer_load_dword v253,v242,s[8:11],s53 offen sc1
s_waitcnt vmcnt(0)
v_cmp_ne_u32_e32 vcc,4,v253
s_nop 1
s_cbranch_vccz L_spra6o
s_sub_u32 s91,s91,1
s_cbranch_scc0 L_spla6o
s_branch L_spra6o
L_spa7o:
s_mov_b32 s91,65536
L_spla7o:
s_sleep 2
buffer_load_dword v253,v242,s[8:11],s53 offen sc1
s_waitcnt vmcnt(0)
v_cmp_ne_u32_e32 vcc,4,v253
s_nop 1
s_cbranch_vccz L_spra7o
s_sub_u32 s91,s91,1
s_cbranch_scc0 L_spla7o
s_branch L_spra7o
L_spa8o:
s_mov_b32 s91,65536
L_spla8o:
s_sleep 2
buffer_load_dword v253,v242,s[8:11],s53 offen sc1
s_waitcnt vmcnt(0)
v_cmp_ne_u32_e32 vcc,4,v253
s_nop 1
s_cbranch_vccz L_spra8o
s_sub_u32 s91,s91,1
s_cbranch_scc0 L_spla8o
s_branch L_spra8o
L_sppo:
s_mov_b32 s91,65536
L_splpo:
s_sleep 2
buffer_load_dword v253,v242,s[8:11],s53 offen sc1
s_waitcnt vmcnt(0)
v_cmp_ne_u32_e32 vcc,4,v253
s_nop 1
s_cbranch_vccz L_sprpo
s_sub_u32 s91,s91,1
s_cbranch_scc0 L_splpo
s_branch L_sprpo
L_spx0o:
s_mov_b32 s91,65536
L_splx0o:
lv 16,244,82,0
lv 20,244,82,1024
lv 24,244,82,2048
lv 28,244,82,3072
s_waitcnt vmcnt(0)
v_max_u32_e32 v248,v16,v17
v_max3_u32 v248,v248,v18,v19
v_max3_u32 v248,v248,v20,v21
v_max3_u32 v248,v248,v22,v23
v_max3_u32 v248,v248,v24,v25
v_max3_u32 v248,v248,v26,v27
v_max3_u32 v248,v248,v28,v29
v_max3_u32 v248,v248,v30,v31
v_cmp_eq_u32_e32 vcc,-1,v248
s_nop 1
s_cbranch_vccz L_sprx0o
s_sub_u32 s91,s91,1
s_cbranch_scc0 L_splx0o
s_branch L_sprx0o
L_spx1o:
s_mov_b32 s91,65536
L_splx1o:
lv 48,244,83,0
lv 52,244,83,1024
lv 56,244,83,2048
lv 60,244,83,3072
s_waitcnt vmcnt(0)
v_max_u32_e32 v248,v48,v49
v_max3_u32 v248,v248,v50,v51
v_max3_u32 v248,v248,v52,v53
v_max3_u32 v248,v248,v54,v55
v_max3_u32 v248,v248,v56,v57
v_max3_u32 v248,v248,v58,v59
v_max3_u32 v248,v248,v60,v61
v_max3_u32 v248,v248,v62,v63
v_cmp_eq_u32_e32 vcc,-1,v248
s_nop 1
s_cbranch_vccz L_sprx1o
s_sub_u32 s91,s91,1
s_cbranch_scc0 L_splx1o
s_branch L_sprx1o
L_spx2o:
s_mov_b32 s91,65536
L_splx2o:
lv 80,244,84,0
lv 84,244,84,1024
lv 88,244,84,2048
lv 92,244,84,3072
s_waitcnt vmcnt(0)
v_max_u32_e32 v248,v80,v81
v_max3_u32 v248,v248,v82,v83
v_max3_u32 v248,v248,v84,v85
v_max3_u32 v248,v248,v86,v87
v_max3_u32 v248,v248,v88,v89
v_max3_u32 v248,v248,v90,v91
v_max3_u32 v248,v248,v92,v93
v_max3_u32 v248,v248,v94,v95
v_cmp_eq_u32_e32 vcc,-1,v248
s_nop 1
s_cbranch_vccz L_sprx2o
s_sub_u32 s91,s91,1
s_cbranch_scc0 L_splx2o
s_branch L_sprx2o
L_att_end:
s_endpgm
.p2align 12
.purgem cv
.purgem cvaa
.purgem cvvv
.purgem dl
.purgem ds
.purgem ex
.purgem la
.purgem lv
.purgem maaa
.purgem maav
.purgem mavv
.purgem mvaa
.purgem mvav
.purgem mvva
.purgem mvvv
.purgem ra
.purgem rv
.purgem ta
.purgem tv
.purgem zaaa
.purgem zaav
.purgem zvaa
.purgem zvav
.purgem zvva
.purgem zvvv
)"
        :: "s"(__builtin_amdgcn_kernarg_segment_ptr()), "s"(blockIdx.x), "v"(tid)
        : "v0", "v1", "v2", "v3", "v4", "v5", "v6", "v7", "v8", "v9", "v10", "v11", "v12", "v13", "v14", "v15", "v16", "v17", "v18", "v19", "v20", "v21", "v22", "v23", "v24", "v25", "v26", "v27", "v28", "v29", "v30", "v31", "v32", "v33", "v34", "v35", "v36", "v37", "v38", "v39", "v40", "v41", "v42", "v43", "v44", "v45", "v46", "v47", "v48", "v49", "v50", "v51", "v52", "v53", "v54", "v55", "v56", "v57", "v58", "v59", "v60", "v61", "v62", "v63", "v64", "v65", "v66", "v67", "v68", "v69", "v70", "v71", "v72", "v73", "v74", "v75", "v76", "v77", "v78", "v79", "v80", "v81", "v82", "v83", "v84", "v85", "v86", "v87", "v88", "v89", "v90", "v91", "v92", "v93", "v94", "v95", "v96", "v97", "v98", "v99", "v100", "v101", "v102", "v103", "v104", "v105", "v106", "v107", "v108", "v109", "v110", "v111", "v112", "v113", "v114", "v115", "v116", "v117", "v118", "v119", "v120", "v121", "v122", "v123", "v124", "v125", "v126", "v127", "v128", "v129", "v130", "v131", "v132", "v133", "v134", "v135", "v136", "v137", "v138", "v139", "v140", "v141", "v142", "v143", "v144", "v145", "v146", "v147", "v148", "v149", "v150", "v151", "v152", "v153", "v154", "v155", "v156", "v157", "v158", "v159", "v160", "v161", "v162", "v163", "v164", "v165", "v166", "v167", "v168", "v169", "v170", "v171", "v172", "v173", "v174", "v175", "v176", "v177", "v178", "v179", "v180", "v181", "v182", "v183", "v184", "v185", "v186", "v187", "v188", "v189", "v190", "v191", "v192", "v193", "v194", "v195", "v196", "v197", "v198", "v199", "v200", "v201", "v202", "v203", "v204", "v205", "v206", "v207", "v208", "v209", "v210", "v211", "v212", "v213", "v214", "v215", "v216", "v217", "v218", "v219", "v220", "v221", "v222", "v223", "v224", "v225", "v226", "v227", "v228", "v229", "v230", "v231", "v232", "v233", "v234", "v235", "v236", "v237", "v238", "v239", "v240", "v241", "v242", "v243", "v244", "v245", "v246", "v247", "v248", "v249", "v250", "v251", "v252", "v253", "v254", "a0", "a1", "a2", "a3", "a4", "a5", "a6", "a7", "a8", "a9", "a10", "a11", "a12", "a13", "a14", "a15", "a16", "a17", "a18", "a19", "a20", "a21", "a22", "a23", "a24", "a25", "a26", "a27", "a28", "a29", "a30", "a31", "a32", "a33", "a34", "a35", "a36", "a37", "a38", "a39", "a40", "a41", "a42", "a43", "a44", "a45", "a46", "a47", "a48", "a49", "a50", "a51", "a52", "a53", "a54", "a55", "a56", "a57", "a58", "a59", "a60", "a61", "a62", "a63", "a64", "a65", "a66", "a67", "a68", "a69", "a70", "a71", "a72", "a73", "a74", "a75", "a76", "a77", "a78", "a79", "a80", "a81", "a82", "a83", "a84", "a85", "a86", "a87", "a88", "a89", "a90", "a91", "a92", "a93", "a94", "a95", "a96", "a97", "a98", "a99", "a100", "a101", "a102", "a103", "a104", "a105", "a106", "a107", "a108", "a109", "a110", "a111", "a112", "a113", "a114", "a115", "a116", "a117", "a118", "a119", "a120", "a121", "a122", "a123", "a124", "a125", "a126", "a127", "a128", "a129", "a130", "a131", "a132", "a133", "a134", "a135", "a136", "a137", "a138", "a139", "a140", "a141", "a142", "a143", "a144", "a145", "a146", "a147", "a148", "a149", "a150", "a151", "a152", "a153", "a154", "a155", "a156", "a157", "a158", "a159", "a160", "a161", "a162", "a163", "a164", "a165", "a166", "a167", "a168", "a169", "a170", "a171", "a172", "a173", "a174", "a175", "a176", "a177", "a178", "a179", "a180", "a181", "a182", "a183", "a184", "a185", "a186", "a187", "a188", "a189", "a190", "a191", "a192", "a193", "a194", "a195", "a196", "a197", "a198", "a199", "a200", "a201", "a202", "a203", "a204", "a205", "a206", "a207", "a208", "a209", "a210", "a211", "a212", "a213", "a214", "a215", "a216", "a217", "a218", "a219", "a220", "a221", "a222", "a223", "a224", "a225", "a226", "a227", "a228", "a229", "a230", "a231", "a232", "a233", "a234", "a235", "a236", "a237", "a238", "a239", "a240", "a241", "a242", "a243", "a244", "a245", "a246", "a247", "a248", "a249", "a250", "a251", "a252", "a253", "a254", "a255", "s4", "s5", "s6", "s7", "s8", "s9", "s10", "s11", "s12", "s13", "s14", "s15", "s16", "s17", "s18", "s19", "s20", "s21", "s22", "s23", "s24", "s25", "s26", "s27", "s28", "s29", "s30", "s31", "s32", "s33", "s34", "s35", "s36", "s37", "s38", "s39", "s40", "s41", "s42", "s43", "s44", "s45", "s46", "s47", "s48", "s49", "s50", "s51", "s52", "s53", "s54", "s55", "s56", "s57", "s58", "s59", "s60", "s61", "s62", "s63", "s64", "s65", "s66", "s67", "s68", "s69", "s70", "s71", "s72", "s73", "s74", "s75", "s76", "s77", "s78", "s79", "s80", "s81", "s82", "s83", "s84", "s85", "s86", "s87", "s88", "s89", "s90", "s91", "s92", "s93", "s94", "s95", "s96", "s97", "s98", "s99", "s100", "s101", "vcc", "scc", "m0", "memory");
}

extern "C" void kernel_launch(void* const* d_in, const int* in_sizes, int n_in, void* d_out, int out_size, void* d_ws, size_t ws_size, hipStream_t stream) {
    (void)in_sizes; (void)n_in; (void)out_size; (void)ws_size;
    char* ws = (char*)d_ws + 0ull;
    Args a;
    a.p[0] = d_in[0]; a.p[1] = d_in[1]; a.p[2] = d_in[2]; a.p[3] = d_in[3]; a.p[4] = d_in[4]; a.p[5] = d_in[5]; a.p[6] = d_in[7]; a.p[7] = d_in[8]; a.p[8] = d_in[9]; a.p[9] = d_out; a.p[10] = ws;
    mega<<<256, 256, 0, stream>>>(a);
}
```

```cpp
#include <hip/hip_runtime.h>
#include <hip/hip_bf16.h>
#include <stdint.h>
#include <type_traits>

#define LAS __attribute__((address_space(3)))
struct Args { const void* p[11]; };
__global__ __launch_bounds__(256, 1)
void mega(Args args) {
    __shared__ __attribute__((aligned(1024))) char smem[163840];
    const unsigned tid = threadIdx.x + (unsigned)(uintptr_t)(LAS char*)smem;
    asm volatile(
R"(.macro cv a,b,c
v_cvt_pk_bf16_f32 v\a,v\b,v\c
.endm
.macro cvaa a,b,c,d
v_mfma_f32_16x16x32_bf16 v[\a:\a+3],a[\b:\b+3],a[\c:\c+3],v[\d:\d+3]
.endm
.macro cvvv a,b,c,d
v_mfma_f32_16x16x32_bf16 v[\a:\a+3],v[\b:\b+3],v[\c:\c+3],v[\d:\d+3]
.endm
.macro dl a,b,c,d
buffer_load_dwordx4 v\a,s[\b:\b+3],s\c offen offset:\d lds
.endm
.macro ds a,b,c,d
buffer_load_dwordx4 v\a,s[\b:\b+3],s\c offen offset:\d sc1 lds
.endm
.macro ex a
v_exp_f32_e32 v\a,v\a
.endm
.macro la a,b,c,d
buffer_load_dwordx4 a[\a:\a+3],v\b,s[8:11],s\c offen offset:\d sc1
.endm
.macro lv a,b,c,d
buffer_load_dwordx4 v[\a:\a+3],v\b,s[8:11],s\c offen offset:\d sc1
.endm
.macro maaa a,b,c
v_mfma_f32_16x16x32_bf16 a[\a:\a+3],a[\b:\b+3],a[\c:\c+3],a[\a:\a+3]
.endm
.macro maav a,b,c
v_mfma_f32_16x16x32_bf16 a[\a:\a+3],a[\b:\b+3],v[\c:\c+3],a[\a:\a+3]
.endm
.macro mavv a,b,c
v_mfma_f32_16x16x32_bf16 a[\a:\a+3],v[\b:\b+3],v[\c:\c+3],a[\a:\a+3]
.endm
.macro mvaa a,b,c
v_mfma_f32_16x16x32_bf16 v[\a:\a+3],a[\b:\b+3],a[\c:\c+3],v[\a:\a+3]
.endm
.macro mvav a,b,c
v_mfma_f32_16x16x32_bf16 v[\a:\a+3],a[\b:\b+3],v[\c:\c+3],v[\a:\a+3]
.endm
.macro mvva a,b,c
v_mfma_f32_16x16x32_bf16 v[\a:\a+3],v[\b:\b+3],a[\c:\c+3],v[\a:\a+3]
.endm
.macro mvvv a,b,c
v_mfma_f32_16x16x32_bf16 v[\a:\a+3],v[\b:\b+3],v[\c:\c+3],v[\a:\a+3]
.endm
.macro ra a,b,c
ds_read_b128 a[\a:\a+3],v\b offset:\c
.endm
.macro rv a,b,c
ds_read_b128 v[\a:\a+3],v\b offset:\c
.endm
.macro ta a,b,c
ds_read_b64_tr_b16 a[\a:\a+1],v\b offset:\c
.endm
.macro tv a,b,c
ds_read_b64_tr_b16 v[\a:\a+1],v\b offset:\c
.endm
.macro zaaa a,b,c
v_mfma_f32_16x16x32_bf16 a[\a:\a+3],a[\b:\b+3],a[\c:\c+3],0
.endm
.macro zaav a,b,c
v_mfma_f32_16x16x32_bf16 a[\a:\a+3],a[\b:\b+3],v[\c:\c+3],0
.endm
.macro zvaa a,b,c
v_mfma_f32_16x16x32_bf16 v[\a:\a+3],a[\b:\b+3],a[\c:\c+3],0
.endm
.macro zvav a,b,c
v_mfma_f32_16x16x32_bf16 v[\a:\a+3],a[\b:\b+3],v[\c:\c+3],0
.endm
.macro zvva a,b,c
v_mfma_f32_16x16x32_bf16 v[\a:\a+3],v[\b:\b+3],a[\c:\c+3],0
.endm
.macro zvvv a,b,c
v_mfma_f32_16x16x32_bf16 v[\a:\a+3],v[\b:\b+3],v[\c:\c+3],0
.endm
L_begin:
s_load_dwordx2 s[28:29],%0,72
s_load_dwordx16 s[68:83],%0,0
s_load_dwordx2 s[84:85],%0,64
s_load_dwordx2 s[8:9],%0,80
v_lshrrev_b32_e32 v246,6,%2
v_and_b32_e32 v247,63,%2
v_lshlrev_b32_e32 v244,4,v247
v_readfirstlane_b32 s12,v246
s_and_b32 s20,%1,7
s_lshr_b32 s21,%1,3
s_getpc_b64 s[96:97]
L_att_pc:
s_lshl_b32 s86,s21,2
s_add_u32 s86,s86,s12
s_mul_i32 s86,s86,512
v_lshl_add_u32 v246,v247,7,s86
v_min_u32_e32 v246,L_att_end-L_att_pc-4,v246
s_mov_b64 exec,15
global_load_dword a188,v246,s[96:97]
s_mov_b64 exec,-1
s_and_b32 s19,s21,7
s_lshr_b32 s86,s21,3
s_lshl_b32 s18,s20,2
s_add_u32 s18,s18,s86
v_lshrrev_b32_e32 v248,3,v247
v_and_b32_e32 v250,112,v244
v_and_b32_e32 v249,48,v247
v_xor_b32_e32 v249,v249,v250
v_and_b32_e32 v251,16,v247
v_lshlrev_b32_e32 v251,1,v251
v_xor_b32_e32 v250,v250,v251
v_lshl_add_u32 v224,v248,10,v249
v_xor_b32_e32 v225,64,v224
v_add_u32_e32 v227,21504,v225
v_add_u32_e32 v226,14336,v224
v_add_u32_e32 v225,7168,v225
v_lshl_add_u32 v228,v248,13,v250
v_xor_b32_e32 v229,64,v228
v_add_u32_e32 v231,193536,v229
v_add_u32_e32 v230,129024,v228
v_add_u32_e32 v229,64512,v229
s_lshl_b32 s86,s19,2
s_add_u32 s86,s86,s12
s_lshl_b32 s52,s86,16
s_bfe_u32 s87,s21,65539
s_lshl_b32 s88,s20,1
s_add_u32 s87,s87,s88
s_lshl_b32 s87,s87,2
s_add_u32 s87,s87,s12
s_lshl_b32 s53,s87,7
s_mov_b32 s54,s53
s_mov_b32 s39,131072
s_mov_b32 s43,131072
s_mov_b32 s47,131072
s_mov_b32 s51,131072
s_mov_b32 s38,2097152
s_mov_b32 s42,4194304
s_mov_b32 s46,4194304
s_mov_b32 s56,32768
s_mov_b32 s60,-32640
s_mov_b32 s57,262144
s_mov_b32 s58,262144
s_bfe_u32 s91,s21,65536
s_lshl_b32 s91,s91,18
s_add_u32 s53,s53,s91
s_mov_b32 s54,s53
s_lshl_b32 s91,s91,1
s_sub_u32 s57,s57,s91
s_add_u32 s58,s58,s91
s_lshl_b32 s98,s12,13
s_lshl_b32 s62,s12,12
s_add_u32 s62,s62,98304
s_lshl_b32 s35,%1,2
s_add_u32 s35,s35,83896320
v_lshrrev_b32_e32 v48,2,v247
v_and_b32_e32 v49,3,v247
v_lshlrev_b32_e32 v49,7,v49
v_lshl_add_u32 v48,v48,13,v49
s_lshl_b32 s89,s86,14
s_and_b32 s90,s53,7680
s_add_u32 s90,s90,s89
s_lshr_b32 s89,s21,3
s_lshl_b32 s89,s89,2
s_add_u32 s89,s89,s12
s_lshl_b32 s89,s89,14
s_lshl_b32 s91,s19,18
s_add_u32 s89,s89,s91
s_cmp_ge_u32 s21,16
s_waitcnt lgkmcnt(0)
s_mov_b64 s[36:37],s[70:71]
s_cselect_b64 s[40:41],s[76:77],s[74:75]
s_mov_b64 s[44:45],s[40:41]
s_bfm_b64 exec,8,0
buffer_load_dword a189,v48,s[40:43],s90 offen
s_mov_b64 exec,-1
s_and_b32 s9,s9,65535
s_mov_b32 s10,83951616
s_mov_b32 s11,131072
buffer_load_dword v44,off,s[8:11],s35 sc1
buffer_load_dword v45,off,s[8:11],s35 offset:1024 sc1
s_add_u32 m0,s62,0
s_nop 0
dl 228,40,53,0
dl 229,40,53,1024
s_add_u32 s53,s53,s57
dl 230,44,54,2048
dl 231,44,54,3072
s_add_u32 s54,s54,s57
s_add_u32 m0,s98,0
s_nop 0
dl 224,36,52,0
dl 225,36,52,1024
dl 226,36,52,2048
dl 227,36,52,3072
s_add_u32 s52,s52,s56
s_add_u32 m0,s98,4096
s_nop 0
dl 224,36,52,0
dl 225,36,52,1024
dl 226,36,52,2048
dl 227,36,52,3072
s_add_u32 s52,s52,s60
v_and_b32_e32 v246,15,v247
v_lshlrev_b32_e32 v248,3,v247
v_xor_b32_e32 v248,v248,v247
v_and_b32_e32 v248,112,v248
v_lshl_add_u32 v232,v246,7,v248
s_and_b32 s86,s98,16384
v_add_u32_e32 v232,s86,v232
s_bfe_u32 s86,s21,65536
s_lshl_b32 s86,s86,6
v_xor_b32_e32 v232,s86,v232
v_xor_b32_e32 v233,64,v232
v_and_b32_e32 v246,12,v247
v_lshlrev_b32_e32 v246,5,v246
v_and_b32_e32 v248,3,v247
v_lshl_add_u32 v246,v248,3,v246
v_and_b32_e32 v248,24,v247
v_lshl_add_u32 v246,v248,2,v246
s_and_b32 s87,s98,8192
s_add_u32 s87,s87,98304
v_add_u32_e32 v246,s87,v246
v_and_b32_e32 v248,48,v247
v_lshl_add_u32 v234,v248,6,v246
v_xor_b32_e32 v235,32,v234
v_xor_b32_e32 v236,64,v234
v_xor_b32_e32 v237,96,v234
s_mov_b64 exec,0
s_cmp_ge_u32 s21,16
s_cbranch_scc1 L_vbV
s_cmp_eq_u32 s12,0
s_cbranch_scc1 L_vs1
s_cmp_eq_u32 s12,1
s_cbranch_scc1 L_vs2
s_cmp_eq_u32 s12,2
s_cbranch_scc1 L_vs3
s_branch L_vs8
L_vbV:
s_cmp_eq_u32 s12,0
s_cbranch_scc1 L_vs4
s_cmp_eq_u32 s12,1
s_cbranch_scc1 L_vs5
s_cmp_eq_u32 s12,2
s_cbranch_scc1 L_vs6
s_branch L_vs7
L_vr0:
s_mov_b64 exec,-1
s_add_u32 m0,s62,16384
s_nop 0
dl 228,40,53,0
dl 229,40,53,1024
s_add_u32 s53,s53,s58
dl 230,44,54,2048
dl 231,44,54,3072
s_add_u32 s54,s54,s58
s_add_u32 m0,s98,32768
s_nop 0
dl 224,36,52,0
dl 225,36,52,1024
dl 226,36,52,2048
dl 227,36,52,3072
s_add_u32 s52,s52,s56
s_waitcnt vmcnt(20)
s_cmp_lg_u32 s12,0
s_cbranch_scc1 L_rqx
v_readfirstlane_b32 s32,v44
v_readfirstlane_b32 s86,v45
s_add_u32 s32,s32,1
s_cmp_eq_u32 s32,s86
s_addc_u32 s32,s32,0
v_mov_b32_e32 v44,s32
v_mov_b32_e32 v45,0
v_lshlrev_b32_e32 v46,3,v244
v_mov_b32_e32 v47,0
s_lshl_b32 s86,%1,7
s_add_u32 s87,s86,83914752
s_and_b32 s86,s86,16383
s_add_u32 s86,s86,83898368
s_mov_b64 exec,1
buffer_store_dword v44,off,s[8:11],s35 sc0 sc1
v_mov_b32_e32 v48,0
v_mov_b32_e32 v49,0
buffer_store_dwordx4 v[46:49],off,s[8:11],s86 sc0 sc1
buffer_store_dwordx4 v[46:49],off,s[8:11],s87 sc0 sc1
s_cmp_lg_u32 %1,0
s_cbranch_scc1 L_rqy
s_mov_b64 exec,65535
s_mov_b32 s86,83894272
buffer_store_dword v45,v46,s[8:11],s86 offen sc0 sc1
L_rqy:
s_mov_b64 exec,-1
L_rqx:
s_cmp_ge_u32 s21,16
s_cbranch_scc1 L_vtile
s_waitcnt vmcnt(8)
s_barrier
ta 64,234,0
ta 66,234,512
ra 0,232,0
ta 68,235,0
ta 70,235,512
ra 4,232,2048
ra 8,232,4096
ra 12,232,6144
ra 16,232,8192
ra 20,232,10240
ra 24,232,12288
ra 28,232,14336
s_add_u32 m0,s62,32768
v_mov_b32_e32 v216,0
dl 228,40,53,0
v_mov_b32_e32 v217,0
v_mov_b32_e32 v218,0
v_mov_b32_e32 v219,0
v_mov_b32_e32 v220,0
v_mov_b32_e32 v221,0
v_mov_b32_e32 v222,0
v_mov_b32_e32 v223,0
v_mov_b32_e32 v212,0
v_mov_b32_e32 v213,0
v_mov_b32_e32 v214,0
v_mov_b32_e32 v215,0
v_mov_b32_e32 v208,0
v_mov_b32_e32 v209,0
v_mov_b32_e32 v210,0
v_mov_b32_e32 v211,0
v_mov_b32_e32 v152,0
s_waitcnt lgkmcnt(9)
zaaa 128,64,0
s_lshl_b32 s4,%1,1
ta 72,236,0
v_mov_b32_e32 v153,0
s_waitcnt lgkmcnt(8)
zaaa 132,68,0
s_add_u32 s4,s4,s12
ta 74,236,512
v_mov_b32_e32 v154,0
s_waitcnt lgkmcnt(8)
zaaa 140,68,4
s_mul_i32 s4,s4,1408
ta 76,237,0
ta 78,237,512
zaaa 136,64,4
dl 229,40,53,1024
s_waitcnt lgkmcnt(4)
zvaa 32,64,8
s_add_u32 s53,s53,s57
v_mov_b32_e32 v155,0
v_mov_b32_e32 v156,0
zvaa 36,68,8
s_mov_b32 s55,s4
v_mov_b32_e32 v157,0
v_mov_b32_e32 v158,0
zvaa 44,68,12
s_mul_i32 s4,%1,2048
ta 80,234,4096
v_mov_b32_e32 v159,0
zvaa 40,64,12
dl 230,44,54,2048
zvaa 96,64,16
s_lshl_b32 s88,%1,2
ta 82,234,4608
v_mov_b32_e32 v148,0
zvaa 100,68,16
s_cmp_eq_u32 s12,2
s_cselect_b32 s55,s4,s55
ta 84,235,4096
v_mov_b32_e32 v149,0
zvaa 108,68,20
s_lshl_b32 s4,%1,14
ta 86,235,4608
v_mov_b32_e32 v150,0
zvaa 104,64,20
dl 231,44,54,3072
zvaa 160,64,24
s_add_u32 m0,s98,36864
v_mov_b32_e32 v151,0
v_mov_b32_e32 v144,0
zvaa 164,68,24
s_add_u32 s54,s54,s57
v_mov_b32_e32 v145,0
v_mov_b32_e32 v146,0
zvaa 172,68,28
s_add_u32 s88,s88,s12
v_mov_b32_e32 v147,0
v_mov_b32_e32 v88,0
zvaa 168,64,28
dl 224,36,52,0
s_waitcnt lgkmcnt(4)
zvaa 184,72,28
s_add_u32 s5,s4,16384
v_mov_b32_e32 v89,0
v_mov_b32_e32 v90,0
zvaa 188,76,28
s_cmp_eq_u32 s12,3
s_cselect_b32 s55,s4,s55
ra 32,233,0
v_mov_b32_e32 v91,0
zvaa 180,76,24
s_mov_b32 s4,11
v_mov_b32_e32 v92,0
v_mov_b32_e32 v93,0
zvaa 176,72,24
dl 225,36,52,1024
zvaa 120,72,20
s_lshl_b32 s88,s88,12
ra 36,233,2048
v_mov_b32_e32 v94,0
zvaa 124,76,20
s_xor_b32 s87,s19,15
ra 40,233,4096
v_mov_b32_e32 v95,0
zvaa 116,76,16
s_cmp_eq_u32 s12,2
s_cselect_b32 s4,16,s4
v_mov_b32_e32 v84,0
v_mov_b32_e32 v85,0
zvaa 112,72,16
dl 226,36,52,2048
zvaa 56,72,12
s_waitcnt vmcnt(11)
s_barrier
zvaa 60,76,12
s_add_u32 s30,s68,s88
s_addc_u32 s31,s69,0
ta 96,234,16384
v_mov_b32_e32 v86,0
zvaa 52,76,8
s_lshl_b32 s22,s87,7
ta 98,234,16896
v_mov_b32_e32 v87,0
zvaa 48,72,8
dl 227,36,52,3072
zaaa 152,72,4
s_add_u32 m0,s62,49152
ta 100,235,16384
v_mov_b32_e32 v80,0
zaaa 156,76,4
s_add_u32 s52,s52,s60
ta 102,235,16896
v_mov_b32_e32 v81,0
zaaa 148,76,0
s_lshl_b32 s87,s87,2
ra 44,233,6144
v_mov_b32_e32 v82,0
zaaa 144,72,0
dl 228,40,53,0
s_waitcnt lgkmcnt(5)
zvaa 0,80,0
s_lshl_b32 s88,s19,2
ra 48,233,8192
v_mov_b32_e32 v83,0
zvaa 4,84,0
s_cmp_eq_u32 s12,3
s_cselect_b32 s4,11,s4
ra 52,233,10240
v_mov_b32_e32 v24,0
zvaa 12,84,4
s_add_u32 s87,s87,s12
ra 56,233,12288
v_mov_b32_e32 v25,0
zvaa 8,80,4
dl 229,40,53,1024
zvaa 64,80,8
s_add_u32 s88,s88,s12
v_mov_b32_e32 v26,0
v_mov_b32_e32 v27,0
zvaa 68,84,8
s_lshl_b32 s90,s18,13
ra 60,233,14336
v_mov_b32_e32 v28,0
zvaa 76,84,12
s_add_u32 s90,s90,62914560
ta 104,236,16384
v_mov_b32_e32 v29,0
zvaa 72,80,12
dl 230,44,54,2048
zvaa 128,80,16
s_mul_i32 s14,s87,327680
ta 106,236,16896
v_mov_b32_e32 v30,0
zvaa 132,84,16
s_mul_i32 s13,s88,327680
ta 108,237,16384
v_mov_b32_e32 v31,0
zvaa 140,84,20
s_add_u32 s14,s14,s90
ta 110,237,16896
v_mov_b32_e32 v20,0
zvaa 136,80,20
dl 231,44,54,3072
zvaa 192,80,24
s_add_u32 m0,s98,65536
v_mov_b32_e32 v21,0
v_mov_b32_e32 v22,0
zvaa 196,84,24
s_add_u32 s13,s13,s90
v_mov_b32_e32 v23,0
v_mov_b32_e32 v16,0
zvaa 204,84,28
s_mul_i32 s90,s18,524288
v_mov_b32_e32 v17,0
v_mov_b32_e32 v18,0
zvaa 200,80,28
dl 224,36,52,0
s_waitcnt lgkmcnt(4)
maaa 128,96,32
s_lshl_b32 s91,s12,12
ta 208,236,4096
v_mov_b32_e32 v19,0
maaa 132,100,32
s_add_u32 s90,s90,s91
ta 210,236,4608
v_lshlrev_b32_e32 v241,7,v247
maaa 140,100,36
s_add_u32 s90,s90,16777216
ta 216,237,4096
v_mov_b32_e32 v242,134217728
maaa 136,96,36
dl 225,36,52,1024
mvaa 32,96,40
s_cmp_eq_u32 s12,3
s_cselect_b64 s[26:27],-1,0
ta 218,237,4608
v_lshrrev_b32_e32 v248,3,v247
mvaa 36,100,40
s_mov_b32 s50,8388608
v_and_b32_e32 v250,112,v244
v_and_b32_e32 v251,16,v247
mvaa 44,100,44
s_mov_b32 s59,720896
v_lshlrev_b32_e32 v251,1,v251
v_cmp_gt_u32_e64 s[24:25],s4,v247
mvaa 40,96,44
dl 226,36,52,2048
mvaa 96,96,48
s_lshl_b32 s86,s19,1
v_mul_u32_u24_e32 v246,6144,v248
v_add_u32_e32 v249,s90,v244
mvaa 100,100,48
s_mov_b64 s[48:49],s[72:73]
ta 112,234,20480
v_cndmask_b32_e64 v241,v242,v241,s[24:25]
mvaa 108,100,52
s_cmp_eq_u32 s12,2
s_cselect_b64 s[24:25],-1,0
ta 114,234,20992
v_add_u32_e32 v255,16777216,v249
mvaa 104,96,52
dl 227,36,52,3072
mvaa 160,96,56
s_add_u32 s52,s52,s56
s_add_u32 m0,s98,69632
ta 116,235,20480
mvaa 164,100,56
s_cmp_eq_u32 s12,2
s_cselect_b32 s50,6291456,s50
ta 118,235,20992
v_accvgpr_write_b32 a240,v249
mvaa 172,100,60
s_cmp_eq_u32 s12,2
s_cselect_b32 s59,524288,s59
v_and_b32_e32 v249,48,v247
v_accvgpr_write_b32 a241,v255
mvaa 168,96,60
dl 224,36,52,0
s_waitcnt lgkmcnt(4)
mvaa 184,104,60
s_add_u32 s86,s86,2
ta 212,236,20480
v_xor_b32_e32 v249,v249,v250
mvaa 188,108,60
s_cmp_eq_u32 s12,2
s_cselect_b64 s[48:49],s[80:81],s[48:49]
v_xor_b32_e32 v250,v250,v251
v_lshl_add_u32 v252,v248,11,v249
mvaa 180,108,56
s_cmp_eq_u32 s12,3
s_cselect_b32 s50,s5,s50
v_and_b32_e32 v251,48,v250
v_xor_b32_e32 v253,64,v252
mvaa 176,104,56
dl 225,36,52,1024
mvaa 120,104,52
s_cmp_eq_u32 s12,3
s_cselect_b32 s59,1408,s59
v_accvgpr_write_b32 a242,v252
v_add_u32_e32 v246,v246,v251
mvaa 124,108,52
s_lshl_b32 s15,1,s86
v_add_u32_e32 v242,15360,v253
v_and_b32_e32 v251,64,v250
mvaa 116,108,48
s_lshl_b32 s23,s19,7
v_lshl_add_u32 v248,v251,5,v246
v_xor_b32_e32 v249,64,v251
mvaa 112,104,48
dl 226,36,52,2048
mvaa 56,104,44
s_or_b32 s86,%1,s12
s_cselect_b32 s65,0,4
v_accvgpr_write_b32 a243,v242
v_mul_u32_u24_e32 v251,768,v251
mvaa 60,108,44
s_cmp_eq_u32 s12,3
s_cselect_b64 s[48:49],s[68:69],s[48:49]
v_add_u32_e32 v242,30720,v252
v_lshl_add_u32 v249,v249,5,v246
mvaa 52,108,40
s_mov_b32 s33,s50
v_add_u32_e32 v246,v246,v251
v_accvgpr_write_b32 a244,v242
mvaa 48,104,40
dl 227,36,52,3072
maaa 152,104,36
s_mov_b32 s34,s59
v_add_u32_e32 v242,46080,v253
v_accvgpr_write_b32 a245,v242
maaa 156,108,36
s_mov_b32 s50,0
v_add_u32_e32 v243,4096,v246
v_add_u32_e32 v242,0,v248
maaa 148,108,32
s_mov_b32 s59,0
ta 214,236,20992
v_cndmask_b32_e64 v242,v228,v242,s[24:25]
maaa 144,104,32
s_add_u32 s53,s53,s58
ta 220,237,20480
v_cndmask_b32_e64 v242,v242,v243,s[26:27]
s_waitcnt lgkmcnt(2)
mvaa 0,112,32
s_add_u32 s54,s54,s58
v_add_u32_e32 v243,101376,v246
v_and_b32_e32 v246,15,v247
mvaa 4,116,32
s_add_u32 s52,s52,s60
v_and_b32_e32 v246,12,v247
v_accvgpr_write_b32 a246,v242
mvaa 12,116,36
s_mov_b32 s63,32768
ta 222,237,20992
v_add_u32_e32 v242,48128,v249
mvaa 8,112,36
s_mov_b32 s61,s98
v_cndmask_b32_e64 v242,v229,v242,s[24:25]
v_lshlrev_b32_e32 v246,5,v246
mvaa 64,112,40
s_mov_b32 s64,1
v_cndmask_b32_e64 v242,v242,v243,s[26:27]
v_accvgpr_write_b32 a247,v242
mvaa 68,116,40
v_add_u32_e32 v242,96256,v248
v_and_b32_e32 v248,3,v247
mvaa 76,116,44
v_cndmask_b32_e64 v242,v230,v242,s[24:25]
v_lshl_add_u32 v246,v248,3,v246
mvaa 72,112,44
v_and_b32_e32 v248,24,v247
v_accvgpr_write_b32 a248,v242
mvaa 128,112,48
v_add_u32_e32 v242,144384,v249
v_lshl_add_u32 v246,v248,2,v246
mvaa 132,116,48
v_and_b32_e32 v248,32,v247
v_cndmask_b32_e64 v242,v231,v242,s[24:25]
mvaa 140,116,52
v_add_u32_e32 v246,98304,v246
v_accvgpr_write_b32 a249,v242
mvaa 136,112,52
v_lshl_add_u32 v238,v248,5,v246
v_xor_b32_e32 v239,32,v238
mvaa 192,112,56
v_add_u32_e32 v232,32768,v232
v_add_u32_e32 v233,32768,v233
mvaa 196,116,56
mvaa 204,116,60
mvaa 200,112,60
s_waitcnt vmcnt(8)
s_waitcnt lgkmcnt(0)
s_barrier
L_k_head:
mvaa 216,208,28
ra 64,232,0
mvaa 216,212,60
s_add_u32 m0,s62,0
ta 192,234,32768
mvaa 220,220,60
dl 228,40,53,0
mvaa 220,216,28
ta 194,234,33280
s_nop 3
mvaa 212,216,24
ra 96,233,0
mvaa 212,220,56
ta 196,234,49152
mvaa 208,212,56
ta 198,234,49664
mvaa 208,208,24
ta 204,235,49152
mvaa 152,208,20
dl 229,40,53,1024
mvaa 152,212,52
s_add_u32 s53,s53,s57
ta 206,235,49664
mvaa 156,220,52
ta 200,235,32768
mvaa 156,216,20
ta 202,235,33280
mvaa 148,216,16
ra 68,232,2048
mvaa 148,220,48
ra 100,233,2048
mvaa 144,212,48
ra 72,232,4096
mvaa 144,208,16
dl 230,44,54,2048
mvaa 88,208,12
ra 104,233,4096
mvaa 88,212,44
ra 76,232,6144
mvaa 92,220,44
ra 108,233,6144
mvaa 92,216,12
ra 80,232,8192
mvaa 84,216,8
ra 112,233,8192
mvaa 84,220,40
dl 231,44,54,3072
mvaa 80,212,40
s_add_u32 s54,s54,s57
ra 84,232,10240
mvaa 80,208,8
ra 116,233,10240
mvaa 24,208,4
ra 88,232,12288
mvaa 24,212,36
ra 120,233,12288
mvaa 28,220,36
ra 92,232,14336
mvaa 28,216,4
s_add_u32 m0,s62,16384
ra 124,233,14336
mvaa 20,216,0
dl 228,40,53,0
mvaa 20,220,32
L_vs1:
s_cbranch_execz L_vs9
mvaa 16,212,32
mvaa 16,208,0
s_waitcnt lgkmcnt(8)
maaa 128,192,64
maaa 128,196,96
maaa 132,204,96
dl 229,40,53,1024
maaa 132,200,64
s_add_u32 s53,s53,s58
maaa 140,200,68
maaa 140,204,100
maaa 136,196,100
maaa 136,192,68
mvaa 32,192,72
mvaa 32,196,104
dl 230,44,54,2048
mvaa 36,204,104
mvaa 36,200,72
s_waitcnt lgkmcnt(0)
mvaa 44,200,76
ta 208,236,32768
mvaa 44,204,108
ta 210,236,33280
mvaa 40,196,108
ta 212,236,49152
mvaa 40,192,76
dl 231,44,54,3072
mvaa 96,192,80
s_add_u32 s54,s54,s58
ta 214,236,49664
mvaa 96,196,112
ta 220,237,49152
mvaa 100,204,112
ta 222,237,49664
mvaa 100,200,80
ta 216,237,32768
mvaa 108,200,84
ta 218,237,33280
mvaa 108,204,116
mvaa 104,196,116
buffer_load_dword a184,v241,s[48:51],s55 offen
mvaa 104,192,84
s_add_u32 s55,s55,s59
mvaa 160,192,88
mvaa 160,196,120
mvaa 164,204,120
mvaa 164,200,88
mvaa 172,200,92
s_add_u32 m0,s61,0
mvaa 172,204,124
dl 224,36,52,0
mvaa 168,196,124
mvaa 168,192,92
s_waitcnt lgkmcnt(0)
mvaa 184,208,92
mvaa 184,212,124
mvaa 188,220,124
ta 192,234,36864
mvaa 188,216,92
ta 194,234,37376
mvaa 180,216,88
dl 225,36,52,1024
mvaa 180,220,120
ta 196,234,53248
mvaa 176,212,120
ta 198,234,53760
mvaa 176,208,88
ta 204,235,53248
mvaa 120,208,84
ta 206,235,53760
mvaa 120,212,116
ta 200,235,36864
mvaa 124,220,116
ta 202,235,37376
mvaa 124,216,84
dl 226,36,52,2048
mvaa 116,216,80
mvaa 116,220,112
mvaa 112,212,112
mvaa 112,208,80
mvaa 56,208,76
mvaa 56,212,108
mvaa 60,220,108
dl 227,36,52,3072
mvaa 60,216,76
s_add_u32 s52,s52,s56
mvaa 52,216,72
mvaa 52,220,104
mvaa 48,212,104
mvaa 48,208,72
maaa 152,208,68
maaa 152,212,100
buffer_load_dword a185,v241,s[48:51],s55 offen
maaa 156,220,100
s_add_u32 s55,s55,s59
maaa 156,216,68
s_mov_b32 s50,s33
s_mov_b32 s59,s34
maaa 148,216,64
maaa 148,220,96
maaa 144,212,96
s_waitcnt lgkmcnt(0)
maaa 144,208,64
s_add_u32 m0,s61,4096
mvaa 0,192,64
dl 224,36,52,0
mvaa 0,196,96
s_add_u32 s61,s63,s98
mvaa 4,204,96
ta 208,236,36864
mvaa 4,200,64
ta 210,236,37376
mvaa 12,200,68
ta 212,236,53248
mvaa 12,204,100
ta 214,236,53760
mvaa 8,196,100
ta 220,237,53248
mvaa 8,192,68
ta 222,237,53760
mvaa 64,192,72
dl 225,36,52,1024
mvaa 64,196,104
ta 216,237,36864
mvaa 68,204,104
ta 218,237,37376
mvaa 68,200,72
s_cmp_ge_u32 s63,65536
s_cselect_b32 s99,-2,1
s_lshl_b32 s99,s99,15
mvaa 76,200,76
s_add_u32 s63,s63,s99
v_add_u32_e32 v232,s99,v232
v_add_u32_e32 v233,s99,v233
mvaa 76,204,108
mvaa 72,196,108
mvaa 72,192,76
dl 226,36,52,2048
mvaa 128,192,80
mvaa 128,196,112
mvaa 132,204,112
mvaa 132,200,80
mvaa 140,200,84
mvaa 140,204,116
mvaa 136,196,116
dl 227,36,52,3072
mvaa 136,192,84
s_add_u32 s52,s52,s60
mvaa 192,192,88
mvaa 192,196,120
mvaa 196,204,120
mvaa 196,200,88
mvaa 204,200,92
mvaa 204,204,124
s_cmp_eq_u32 s64,3
mvaa 200,196,124
s_waitcnt vmcnt(10) lgkmcnt(0)
s_barrier
mvaa 200,192,92
s_cbranch_scc1 L_k_exit
mvaa 216,208,92
ra 0,232,0
mvaa 216,212,124
s_add_u32 m0,s62,32768
ta 192,234,0
mvaa 220,220,124
dl 228,40,53,0
mvaa 220,216,92
ta 194,234,512
s_nop 3
mvaa 212,216,88
ra 32,233,0
mvaa 212,220,120
ta 196,234,16384
mvaa 208,212,120
ta 198,234,16896
mvaa 208,208,88
ta 204,235,16384
mvaa 152,208,84
dl 229,40,53,1024
mvaa 152,212,116
s_add_u32 s53,s53,s57
ta 206,235,16896
mvaa 156,220,116
ta 200,235,0
mvaa 156,216,84
ta 202,235,512
mvaa 148,216,80
ra 4,232,2048
mvaa 148,220,112
ra 36,233,2048
mvaa 144,212,112
ra 8,232,4096
mvaa 144,208,80
dl 230,44,54,2048
mvaa 88,208,76
ra 40,233,4096
mvaa 88,212,108
ra 12,232,6144
mvaa 92,220,108
ra 44,233,6144
mvaa 92,216,76
ra 16,232,8192
mvaa 84,216,72
ra 48,233,8192
mvaa 84,220,104
dl 231,44,54,3072
mvaa 80,212,104
s_add_u32 s54,s54,s57
ra 20,232,10240
mvaa 80,208,72
ra 52,233,10240
mvaa 24,208,68
ra 24,232,12288
mvaa 24,212,100
ra 56,233,12288
mvaa 28,220,100
ra 28,232,14336
mvaa 28,216,68
s_add_u32 m0,s62,49152
ra 60,233,14336
mvaa 20,216,64
dl 228,40,53,0
mvaa 20,220,96
mvaa 16,212,96
mvaa 16,208,64
s_waitcnt lgkmcnt(8)
maaa 128,192,0
maaa 128,196,32
maaa 132,204,32
dl 229,40,53,1024
maaa 132,200,0
s_add_u32 s53,s53,s58
maaa 140,200,4
maaa 140,204,36
maaa 136,196,36
maaa 136,192,4
mvaa 32,192,8
mvaa 32,196,40
dl 230,44,54,2048
mvaa 36,204,40
mvaa 36,200,8
s_waitcnt lgkmcnt(0)
mvaa 44,200,12
ta 208,236,0
mvaa 44,204,44
ta 210,236,512
mvaa 40,196,44
ta 212,236,16384
mvaa 40,192,12
dl 231,44,54,3072
mvaa 96,192,16
s_add_u32 s54,s54,s58
ta 214,236,16896
mvaa 96,196,48
ta 220,237,16384
)"
R"(mvaa 100,204,48
ta 222,237,16896
mvaa 100,200,16
ta 216,237,0
mvaa 108,200,20
ta 218,237,512
mvaa 108,204,52
mvaa 104,196,52
buffer_load_dword a186,v241,s[48:51],s55 offen
mvaa 104,192,20
s_add_u32 s55,s55,s59
mvaa 160,192,24
s_lshl_b32 s86,s21,2
s_add_u32 s86,s86,s12
s_mul_i32 s86,s86,384
mvaa 160,196,56
s_add_u32 s86,s86,4096
v_lshl_add_u32 v253,v244,3,s86
mvaa 164,204,56
s_mov_b64 exec,7
global_load_dword a188,v253,s[96:97]
s_mov_b64 exec,-1
mvaa 164,200,24
mvaa 172,200,28
s_add_u32 m0,s61,0
mvaa 172,204,60
dl 224,36,52,0
mvaa 168,196,60
mvaa 168,192,28
s_waitcnt lgkmcnt(0)
mvaa 184,208,28
mvaa 184,212,60
mvaa 188,220,60
ta 192,234,4096
mvaa 188,216,28
ta 194,234,4608
mvaa 180,216,24
dl 225,36,52,1024
mvaa 180,220,56
ta 196,234,20480
mvaa 176,212,56
ta 198,234,20992
mvaa 176,208,24
ta 204,235,20480
mvaa 120,208,20
ta 206,235,20992
mvaa 120,212,52
ta 200,235,4096
mvaa 124,220,52
ta 202,235,4608
mvaa 124,216,20
dl 226,36,52,2048
mvaa 116,216,16
mvaa 116,220,48
mvaa 112,212,48
mvaa 112,208,16
mvaa 56,208,12
mvaa 56,212,44
s_bitcmp1_b32 s65,s64
s_cbranch_scc1 L_k_ev
L_k_evr:
mvaa 60,220,44
dl 227,36,52,3072
mvaa 60,216,12
s_add_u32 s52,s52,s56
mvaa 52,216,8
mvaa 52,220,40
mvaa 48,212,40
mvaa 48,208,8
maaa 152,208,4
maaa 152,212,36
buffer_load_dword a187,v241,s[48:51],s55 offen
maaa 156,220,36
s_add_u32 s55,s55,s59
maaa 156,216,4
maaa 148,216,0
maaa 148,220,32
maaa 144,212,32
s_waitcnt lgkmcnt(0)
maaa 144,208,0
s_add_u32 m0,s61,4096
mvaa 0,192,0
dl 224,36,52,0
mvaa 0,196,32
s_add_u32 s61,s63,s98
mvaa 4,204,32
ta 208,236,4096
mvaa 4,200,0
ta 210,236,4608
mvaa 12,200,4
ta 212,236,20480
mvaa 12,204,36
ta 214,236,20992
mvaa 8,196,36
ta 220,237,20480
mvaa 8,192,4
ta 222,237,20992
mvaa 64,192,8
dl 225,36,52,1024
mvaa 64,196,40
ta 216,237,4096
mvaa 68,204,40
ta 218,237,4608
mvaa 68,200,8
s_cmp_ge_u32 s63,65536
s_cselect_b32 s99,-2,1
s_lshl_b32 s99,s99,15
mvaa 76,200,12
s_add_u32 s63,s63,s99
v_add_u32_e32 v232,s99,v232
v_add_u32_e32 v233,s99,v233
mvaa 76,204,44
mvaa 72,196,44
mvaa 72,192,12
dl 226,36,52,2048
mvaa 128,192,16
mvaa 128,196,48
mvaa 132,204,48
mvaa 132,200,16
mvaa 140,200,20
mvaa 140,204,52
mvaa 136,196,52
dl 227,36,52,3072
mvaa 136,192,20
s_add_u32 s52,s52,s60
mvaa 192,192,24
mvaa 192,196,56
mvaa 196,204,56
s_add_u32 s64,s64,1
mvaa 196,200,24
mvaa 204,200,28
mvaa 204,204,60
mvaa 200,196,60
s_waitcnt vmcnt(10) lgkmcnt(0)
s_barrier
mvaa 200,192,28
s_branch L_k_head
L_k_exit:
global_load_dwordx4 a[160:163],v244,s[30:31] offset:0
s_add_u32 m0,s62,32768
mvaa 216,208,92
s_cmp_lt_u32 s12,2
s_cselect_b32 s86,s22,s23
ta 32,234,0
s_and_b32 s87,s12,1
mvaa 216,212,124
s_lshl_b32 s86,s86,11
ta 34,234,512
s_lshl_b32 s87,s87,17
mvaa 220,220,124
s_add_u32 s86,s86,s87
ra 0,232,0
s_mov_b64 s[36:37],s[68:69]
mvaa 220,216,92
s_mov_b32 s38,4194304
ta 36,235,0
s_mov_b32 s52,s86
mvaa 212,216,88
s_mov_b32 s56,65536
ta 38,235,512
v_accvgpr_read_b32 v224,a242
mvaa 212,220,120
dl 228,40,53,0
mvaa 208,212,120
s_lshl_b32 s88,s18,1
ra 4,232,2048
v_accvgpr_read_b32 v225,a243
mvaa 208,208,88
s_add_u32 s88,s88,s12
ra 8,232,4096
v_accvgpr_read_b32 v226,a244
mvaa 152,208,84
s_mov_b32 s91,196608
ra 12,232,6144
v_accvgpr_read_b32 v227,a245
mvaa 152,212,116
s_lshl_b32 s88,s88,7
ra 16,232,8192
s_lshl_b32 s89,s18,6
mvaa 156,220,116
s_mov_b32 s90,6291456
ra 20,232,10240
s_mov_b32 s60,-65408
mvaa 156,216,84
dl 229,40,53,1024
mvaa 148,216,80
s_add_u32 s53,s53,s57
ra 24,232,12288
s_lshl_b32 s87,s20,2
mvaa 148,220,112
s_mov_b32 s26,32768
ra 28,232,14336
s_mov_b32 s27,131072
mvaa 144,212,112
s_add_u32 s61,s98,32768
ta 40,236,0
s_mov_b32 s63,65536
mvaa 144,208,80
s_cmp_ge_u32 s63,65536
s_cselect_b32 s99,-2,1
ta 42,236,512
s_lshl_b32 s99,s99,15
mvaa 88,208,76
s_mov_b32 s67,2048
ta 44,237,0
s_mov_b32 s66,8
mvaa 88,212,108
dl 230,44,54,2048
mvaa 92,220,108
s_mov_b32 s64,1
ta 46,237,512
mvaa 92,216,76
s_bitcmp1_b32 s12,0
s_cselect_b64 vcc,-1,0
ta 48,234,4096
mvaa 84,216,72
ta 50,234,4608
mvaa 84,220,104
ta 52,235,4096
mvaa 80,212,104
ta 54,235,4608
mvaa 80,208,72
dl 231,44,54,3072
s_add_u32 m0,s62,49152
mvaa 24,208,68
s_add_u32 s54,s54,s57
ta 56,236,4096
mvaa 24,212,100
s_cmp_lt_u32 s12,2
s_cselect_b32 s57,262144,s91
ta 58,236,4608
mvaa 28,220,100
ta 60,237,4096
mvaa 28,216,68
ta 62,237,4608
mvaa 20,216,64
mvaa 20,220,96
dl 228,40,53,0
mvaa 16,212,96
v_accvgpr_read_b32 v228,a246
mvaa 16,208,64
s_waitcnt lgkmcnt(5)
maaa 128,32,0
maaa 132,36,0
maaa 140,36,4
dl 229,40,53,1024
maaa 136,32,4
s_waitcnt lgkmcnt(0)
s_barrier
mvaa 32,32,8
s_add_u32 s53,s53,s58
ta 96,234,16384
v_accvgpr_read_b32 v229,a247
mvaa 36,36,8
s_cmp_lt_u32 s12,2
s_cselect_b32 s42,8388608,s90
ta 98,234,16896
mvaa 44,36,12
s_cmp_lt_u32 s12,2
s_cselect_b32 s53,s88,s89
ra 64,233,0
s_and_b32 s89,s98,8192
mvaa 40,32,12
dl 230,44,54,2048
mvaa 96,32,16
s_bitcmp1_b32 s21,0
s_cselect_b32 s90,s57,0
ta 100,235,16384
s_add_u32 s53,s53,s90
mvaa 100,36,16
s_cmp_lt_u32 s12,2
s_cselect_b64 s[40:41],s[72:73],s[80:81]
ta 102,235,16896
v_accvgpr_read_b32 v230,a248
mvaa 108,36,20
s_lshl_b32 s90,s90,1
ra 68,233,2048
s_add_u32 s91,s89,65536
mvaa 104,32,20
s_bfe_u32 s88,s21,65539
ra 72,233,4096
s_lshl_b32 s88,s88,1
mvaa 160,32,24
dl 231,44,54,3072
mvaa 164,36,24
s_add_u32 m0,s98,65536
ra 76,233,6144
s_add_u32 s54,s54,s58
mvaa 172,36,28
s_cmp_eq_u32 s12,3
s_cselect_b64 s[44:45],s[68:69],s[40:41]
ra 80,233,8192
v_accvgpr_read_b32 v231,a249
mvaa 168,32,28
s_cmp_eq_u32 s12,3
s_cselect_b32 s46,0,s42
ra 84,233,10240
s_add_u32 s58,s57,s90
mvaa 184,40,28
s_cmp_eq_u32 s12,3
s_cselect_b32 s54,0,s53
ra 88,233,12288
s_sub_u32 s57,s57,s90
mvaa 188,44,28
global_load_dwordx4 a[168:171],v244,s[30:31] offset:2048
mvaa 180,44,24
L_vs2:
s_cbranch_execz L_vs10
s_add_u32 s90,s89,65536
ta 104,236,16384
s_add_u32 s88,s88,s87
mvaa 176,40,24
s_and_b32 s87,s12,1
ra 92,233,14336
s_add_u32 s88,s88,s87
mvaa 120,40,20
s_mul_i32 s88,s88,524288
ta 106,236,16896
s_and_b32 s87,s21,7
mvaa 124,44,20
s_add_u32 s86,s88,16777216
ta 108,237,16384
s_lshl_b32 s87,s87,16
mvaa 116,44,16
global_load_dwordx4 a[176:179],v244,s[84:85] offset:0
mvaa 112,40,16
s_add_u32 s86,s86,s87
ta 110,237,16896
s_lshr_b32 s87,s12,1
mvaa 56,40,12
s_lshl_b32 s87,s87,15
ta 112,234,20480
s_add_u32 s86,s86,s87
mvaa 60,44,12
s_add_u32 s24,s8,s86
s_addc_u32 s25,s9,0
ta 114,234,20992
s_lshl_b32 s86,%1,5
mvaa 52,44,8
s_lshl_b32 s87,s12,3
ta 116,235,20480
s_add_u32 s86,s86,s87
mvaa 48,40,8
global_load_dwordx4 a[164:167],v244,s[30:31] offset:1024
maaa 152,40,4
s_add_u32 s86,s86,83886080
ta 118,235,20992
s_mov_b32 s88,4096
maaa 156,44,4
s_cmp_lg_u32 s65,0
s_cbranch_scc1 L_k_ec1
L_k_ecr1:
ta 120,236,20480
maaa 148,44,0
s_cmp_eq_u32 s12,0
s_cselect_b32 s65,96,0
ta 122,236,20992
s_or_b32 s65,s65,128
maaa 144,40,0
ta 124,237,20480
mvaa 0,48,0
global_load_dwordx4 a[172:175],v244,s[30:31] offset:3072
mvaa 4,52,0
s_mov_b32 s30,1048577
ta 126,237,20992
s_mov_b32 s31,268435712
mvaa 12,52,4
mvaa 8,48,4
mvaa 64,48,8
mvaa 68,52,8
global_load_dwordx4 a[180:183],v244,s[84:85] offset:1024
mvaa 76,52,12
mvaa 72,48,12
mvaa 128,48,16
mvaa 132,52,16
mvaa 140,52,20
dl 224,36,52,0
mvaa 136,48,20
mvaa 192,48,24
mvaa 196,52,24
mvaa 204,52,28
mvaa 200,48,28
dl 225,36,52,1024
mvaa 216,56,28
mvaa 220,60,28
mvaa 212,60,24
mvaa 208,56,24
mvaa 152,56,20
dl 226,36,52,2048
mvaa 156,60,20
mvaa 148,60,16
mvaa 144,56,16
mvaa 88,56,12
s_waitcnt vmcnt(12) lgkmcnt(0)
s_barrier
mvaa 92,60,12
dl 227,36,52,3072
mvaa 84,60,8
s_add_u32 s52,s52,s56
s_add_u32 m0,s98,69632
ta 32,234,32768
mvaa 80,56,8
ta 34,234,33280
mvaa 24,56,4
ta 36,235,32768
mvaa 28,60,4
ta 38,235,33280
mvaa 20,60,0
dl 224,36,52,0
mvaa 16,56,0
ta 40,236,32768
maaa 128,96,64
ta 42,236,33280
maaa 132,100,64
ta 44,237,32768
maaa 140,100,68
ta 46,237,33280
maaa 136,96,68
dl 225,36,52,1024
mvaa 32,96,72
ta 48,234,36864
mvaa 36,100,72
ta 50,234,37376
mvaa 44,100,76
ta 52,235,36864
mvaa 40,96,76
ta 54,235,37376
mvaa 96,96,80
dl 226,36,52,2048
mvaa 100,100,80
ra 20,232,43008
mvaa 108,100,84
ra 24,232,45056
mvaa 104,96,84
ra 0,232,32768
mvaa 160,96,88
ra 28,232,47104
mvaa 164,100,88
dl 227,36,52,3072
mvaa 172,100,92
s_add_u32 m0,s62,0
ra 4,232,34816
s_add_u32 s52,s52,s60
mvaa 168,96,92
ra 16,232,40960
mvaa 184,104,92
ra 8,232,36864
mvaa 188,108,92
ra 12,232,38912
v_add_u32_e32 v232,s90,v232
mvaa 180,108,88
dl 228,40,53,0
mvaa 176,104,88
ta 60,237,36864
mvaa 120,104,84
ta 62,237,37376
mvaa 124,108,84
ta 56,236,36864
mvaa 116,108,80
ta 58,236,37376
mvaa 112,104,80
dl 229,40,53,1024
mvaa 56,104,76
s_add_u32 s53,s53,s57
mvaa 60,108,76
mvaa 52,108,72
mvaa 48,104,72
maaa 152,104,68
dl 230,44,54,2048
maaa 156,108,68
maaa 148,108,64
maaa 144,104,64
mvaa 0,112,64
mvaa 4,116,64
dl 231,44,54,3072
mvaa 12,116,68
s_add_u32 m0,s62,16384
s_add_u32 s54,s54,s57
mvaa 8,112,68
mvaa 64,112,72
mvaa 68,116,72
mvaa 76,116,76
dl 228,40,53,0
mvaa 72,112,76
mvaa 128,112,80
mvaa 132,116,80
mvaa 140,116,84
mvaa 136,112,84
dl 229,40,53,1024
mvaa 192,112,88
s_add_u32 s53,s53,s58
mvaa 196,116,88
mvaa 204,116,92
mvaa 200,112,92
mvaa 216,120,92
dl 230,44,54,2048
mvaa 220,124,92
mvaa 212,124,88
s_waitcnt vmcnt(20) lgkmcnt(0)
s_barrier
mvaa 208,120,88
ra 92,233,47104
mvaa 152,120,84
ta 96,234,49152
mvaa 156,124,84
dl 231,44,54,3072
mvaa 148,124,80
s_add_u32 m0,s98,0
ra 88,233,45056
s_add_u32 s54,s54,s58
mvaa 144,120,80
ta 98,234,49664
mvaa 88,120,76
ta 112,234,53248
mvaa 92,124,76
ta 116,235,53248
mvaa 84,124,72
dl 224,36,52,0
mvaa 80,120,72
ta 118,235,53760
mvaa 24,120,68
ta 114,234,53760
v_subrev_u32_e32 v234,s89,v234
mvaa 28,124,68
ta 100,235,49152
mvaa 20,124,64
ta 102,235,49664
v_subrev_u32_e32 v235,s89,v235
mvaa 16,120,64
dl 225,36,52,1024
maaa 128,32,0
ta 124,237,53248
maaa 132,36,0
ta 126,237,53760
maaa 140,36,4
ta 120,236,53248
maaa 136,32,4
ta 122,236,53760
mvaa 32,32,8
dl 226,36,52,2048
mvaa 36,36,8
ta 104,236,49152
mvaa 44,36,12
ta 106,236,49664
v_subrev_u32_e32 v236,s89,v236
mvaa 40,32,12
ta 108,237,49152
mvaa 56,40,12
ta 110,237,49664
v_subrev_u32_e32 v237,s89,v237
mvaa 60,44,12
dl 227,36,52,3072
mvaa 52,44,8
s_add_u32 s52,s52,s56
s_add_u32 m0,s98,4096
ra 84,233,43008
mvaa 48,40,8
ra 80,233,40960
maaa 152,40,4
ra 76,233,38912
maaa 156,44,4
ra 68,233,34816
maaa 148,44,0
dl 224,36,52,0
maaa 144,40,0
ra 72,233,36864
mvaa 0,48,0
ra 64,233,32768
v_add_u32_e32 v233,s91,v233
mvaa 4,52,0
mvaa 12,52,4
mvaa 8,48,4
dl 225,36,52,1024
mvaa 24,56,4
mvaa 28,60,4
mvaa 20,60,0
mvaa 16,56,0
mvaa 220,60,28
dl 226,36,52,2048
s_waitcnt lgkmcnt(2)
mvaa 220,124,92
mvaa 216,120,92
mvaa 216,56,28
mvaa 212,60,24
mvaa 212,124,88
dl 227,36,52,3072
mvaa 208,120,88
s_add_u32 m0,s62,32768
s_add_u32 s52,s52,s60
s_waitcnt vmcnt(25)
zvaa 246,160,176
mvaa 208,56,24
cv 223,222,223
cv 222,220,221
mvaa 204,52,28
cv 221,218,219
zvaa 250,168,176
mvaa 204,116,92
cv 220,216,217
cv 219,214,215
mvaa 200,112,92
dl 228,40,53,0
s_waitcnt vmcnt(25)
mvaa 246,164,180
mvaa 200,48,28
cv 218,212,213
mvaa 250,172,180
mvaa 196,52,24
cv 217,210,211
cv 216,208,209
mvaa 196,116,88
cv 215,206,207
v_cndmask_b32_e64 v246,0,v246,s[30:31]
mvaa 192,112,88
cv 214,204,205
v_cndmask_b32_e64 v250,0,v250,s[30:31]
mvaa 192,48,24
dl 229,40,53,1024
s_lshl_b64 s[30:31],s[30:31],1
v_cndmask_b32_e64 v246,v246,v247,s[30:31]
v_cndmask_b32_e64 v250,v250,v251,s[30:31]
mvaa 188,44,28
s_lshl_b64 s[30:31],s[30:31],1
s_add_u32 s53,s53,s57
v_cndmask_b32_e64 v246,v246,v248,s[30:31]
v_cndmask_b32_e64 v250,v250,v252,s[30:31]
mvaa 188,108,92
s_lshl_b64 s[30:31],s[30:31],1
cv 213,202,203
v_cndmask_b32_e64 v246,v246,v249,s[30:31]
mvaa 184,104,92
v_cndmask_b32_e64 v250,v250,v253,s[30:31]
v_add_f32_dpp v246,v246,v246 quad_perm:[1,0,3,2] row_mask:0xf bank_mask:0xf
mvaa 184,40,28
v_add_f32_dpp v250,v250,v250 quad_perm:[1,0,3,2] row_mask:0xf bank_mask:0xf
v_add_f32_dpp v246,v246,v246 quad_perm:[2,3,0,1] row_mask:0xf bank_mask:0xf
mvaa 180,44,24
dl 230,44,54,2048
v_add_f32_dpp v250,v250,v250 quad_perm:[2,3,0,1] row_mask:0xf bank_mask:0xf
mvaa 180,108,88
v_add_f32_dpp v246,v246,v246 row_half_mirror row_mask:0xf bank_mask:0xf
v_add_f32_dpp v250,v250,v250 row_half_mirror row_mask:0xf bank_mask:0xf
mvaa 176,104,88
v_add_f32_dpp v246,v246,v246 row_mirror row_mask:0xf bank_mask:0xf
v_add_f32_dpp v250,v250,v250 row_mirror row_mask:0xf bank_mask:0xf
mvaa 176,40,24
cv 212,200,201
cv 211,198,199
mvaa 172,36,28
v_permlane16_swap_b32_e32 v246,v250
mvaa 172,100,92
dl 231,44,54,3072
v_add_f32_e32 v246,v246,v250
v_mov_b32_e32 v250,v246
mvaa 168,96,92
s_add_u32 s54,s54,s57
v_permlane32_swap_b32_e32 v246,v250
mvaa 168,32,28
v_add_f32_e32 v246,v246,v250
v_lshrrev_b32_e32 v250,6,v244
mvaa 164,36,24
v_mul_f32_e32 v246,0xbfb8aa3b,v246
cv 210,196,197
mvaa 164,100,88
ex 246
mvaa 160,96,88
buffer_load_dword a242,v241,s[48:51],s55 offen
mvaa 160,32,24
s_add_u32 s55,s55,s59
v_add_f32_e32 v246,1.0,v246
cv 209,194,195
mvaa 156,60,20
v_rcp_f32_e32 v246,v246
mvaa 156,124,84
s_waitcnt vmcnt(17) lgkmcnt(0)
s_barrier
s_add_u32 m0,s62,49152
mvaa 152,120,84
cv 208,192,193
cv 207,190,191
mvaa 152,56,20
dl 228,40,53,0
mvaa 148,60,16
cv 206,188,189
cv 205,186,187
mvaa 148,124,80
cv 204,184,185
cv 203,182,183
mvaa 144,120,80
cv 202,180,181
cv 201,178,179
mvaa 144,56,16
cv 200,176,177
cv 199,174,175
mvaa 140,52,20
dl 229,40,53,1024
mvaa 140,116,84
s_add_u32 s53,s53,s58
cv 198,172,173
cv 197,170,171
mvaa 136,112,84
ra 4,232,0
cv 196,168,169
mvaa 136,48,20
ta 92,239,12288
cv 195,166,167
mvaa 132,52,16
ta 94,239,12800
cv 194,164,165
mvaa 132,116,80
dl 230,44,54,2048
mvaa 128,112,80
ta 88,238,12288
cv 193,162,163
mvaa 128,48,16
ta 90,238,12800
cv 192,160,161
mvaa 124,44,20
cv 191,158,159
cv 190,156,157
mvaa 124,108,84
cv 189,154,155
cv 188,152,153
mvaa 120,104,84
dl 231,44,54,3072
mvaa 120,40,20
s_add_u32 m0,s98,32768
cv 187,150,151
cv 186,148,149
mvaa 116,44,16
s_add_u32 s54,s54,s58
cv 185,146,147
cv 184,144,145
mvaa 116,108,80
cv 183,142,143
cv 182,140,141
mvaa 112,104,80
cv 181,138,139
cv 180,136,137
mvaa 112,40,16
dl 224,36,52,0
mvaa 108,36,20
cv 179,134,135
cv 178,132,133
mvaa 108,100,84
cv 177,130,131
cv 176,128,129
mvaa 104,96,84
cv 175,126,127
cv 174,124,125
mvaa 104,32,20
cv 173,122,123
cv 172,120,121
mvaa 100,36,16
dl 225,36,52,1024
mvaa 100,100,80
ta 44,235,0
cv 171,118,119
mvaa 96,96,80
ta 46,235,512
cv 170,116,117
mvaa 96,32,16
ta 40,234,0
cv 169,114,115
mvaa 92,60,12
ta 42,234,512
cv 168,112,113
mvaa 92,124,76
dl 226,36,52,2048
mvaa 88,120,76
cv 167,110,111
cv 166,108,109
mvaa 88,56,12
ta 84,237,8192
cv 165,106,107
mvaa 84,60,8
ta 86,237,8704
cv 164,104,105
mvaa 84,124,72
ta 80,236,8192
cv 163,102,103
mvaa 80,120,72
dl 227,36,52,3072
mvaa 80,56,8
s_add_u32 m0,s62,0
ta 82,236,8704
cv 162,100,101
mvaa 76,52,12
s_add_u32 s52,s52,s56
ra 16,232,6144
cv 161,98,99
mvaa 76,116,76
ta 60,235,4096
cv 160,96,97
mvaa 72,112,76
ta 56,234,4096
cv 159,94,95
mvaa 72,48,12
buffer_load_dword a243,v241,s[48:51],s55 offen
mvaa 68,52,8
s_add_u32 s55,s55,s59
ta 62,235,4608
cv 151,78,79
mvaa 68,116,72
ta 58,234,4608
cv 150,76,77
mvaa 64,112,72
ra 12,232,4096
cv 158,92,93
mvaa 64,48,8
ta 52,237,0
cv 157,90,91
mvaa 60,108,76
buffer_load_dword a244,v241,s[48:51],s55 offen
mvaa 56,104,76
s_add_u32 s55,s55,s59
ta 54,237,512
cv 147,70,71
mvaa 52,108,72
ra 8,232,2048
cv 146,68,69
mvaa 48,104,72
ta 48,236,0
cv 145,66,67
mvaa 44,100,76
ta 50,236,512
cv 144,64,65
mvaa 40,96,76
buffer_load_dword a245,v241,s[48:51],s55 offen
mvaa 36,100,72
s_add_u32 s55,s55,s59
cv 143,62,63
cv 142,60,61
mvaa 32,96,72
cv 141,58,59
cv 140,56,57
mvaa 28,124,68
cv 139,54,55
cv 138,52,53
mvaa 24,120,68
cv 137,50,51
cv 136,48,49
mvaa 20,124,64
s_mov_b64 exec,65537
buffer_store_dword v246,v250,s[8:11],s86 offen sc0 sc1
s_mov_b64 exec,-1
mvaa 16,120,64
cv 135,46,47
cv 134,44,45
mvaa 12,116,68
ta 76,235,8192
cv 133,42,43
mvaa 8,112,68
L_vs3:
s_cbranch_execz L_vs11
ta 78,235,8704
cv 132,40,41
mvaa 4,116,64
cv 131,38,39
cv 130,36,37
mvaa 0,112,64
ta 72,234,8192
cv 129,34,35
maaa 156,108,68
ta 74,234,8704
cv 128,32,33
maaa 152,104,68
cv 127,30,31
cv 126,28,29
maaa 148,108,64
cv 125,26,27
cv 124,24,25
maaa 144,104,64
v_accvgpr_read_b32 v63,a159
v_accvgpr_read_b32 v62,a158
maaa 140,100,68
v_accvgpr_read_b32 v61,a157
v_accvgpr_read_b32 v60,a156
maaa 136,96,68
v_accvgpr_read_b32 v59,a155
v_accvgpr_read_b32 v58,a154
maaa 132,100,64
v_accvgpr_read_b32 v57,a153
v_accvgpr_read_b32 v56,a152
maaa 128,96,64
ta 68,237,4096
ta 70,237,4608
s_waitcnt lgkmcnt(4)
zaaa 236,40,4
cv 56,56,57
cv 57,58,59
zaaa 220,44,4
ta 64,236,4096
ta 66,236,4608
zaaa 216,44,8
cv 58,60,61
cv 59,62,63
zaaa 232,40,8
buffer_store_dwordx4 v[56:59],v244,s[24:27],0 offen offset:3072 sc0 sc1
zaaa 228,40,12
v_accvgpr_read_b32 v55,a151
v_accvgpr_read_b32 v54,a150
zaaa 212,44,12
v_accvgpr_read_b32 v53,a149
v_accvgpr_read_b32 v52,a148
zaaa 208,44,16
v_accvgpr_read_b32 v51,a147
v_accvgpr_read_b32 v50,a146
zaaa 224,40,16
v_accvgpr_read_b32 v49,a145
v_accvgpr_read_b32 v48,a144
zaaa 192,48,16
cv 48,48,49
cv 49,50,51
zaaa 176,52,16
cv 50,52,53
cv 51,54,55
zaaa 180,52,12
buffer_store_dwordx4 v[48:51],v244,s[24:27],0 offen offset:2048 sc0 sc1
zaaa 196,48,12
s_waitcnt vmcnt(27) lgkmcnt(0)
s_barrier
ra 24,233,0
v_accvgpr_read_b32 v47,a143
zaaa 200,48,8
ra 28,233,2048
v_accvgpr_read_b32 v46,a142
v_accvgpr_read_b32 v45,a141
zaaa 184,52,8
ra 32,233,4096
ra 36,233,6144
zaaa 188,52,4
v_accvgpr_read_b32 v43,a139
v_accvgpr_read_b32 v42,a138
zaaa 204,48,4
dl 228,40,53,0
ta 96,236,24576
zaaa 172,56,4
ta 98,236,25088
v_accvgpr_read_b32 v41,a137
zaaa 156,60,4
ta 100,237,24576
v_accvgpr_read_b32 v39,a135
zaaa 152,60,8
ta 102,237,25088
ta 104,238,28672
zaaa 168,56,8
v_accvgpr_read_b32 v38,a134
v_accvgpr_read_b32 v35,a131
zaaa 164,56,12
dl 229,40,53,1024
ta 106,238,29184
zaaa 148,60,12
s_add_u32 s53,s53,s57
ta 108,239,28672
v_accvgpr_read_b32 v34,a130
zaaa 144,60,16
ta 110,239,29184
v_accvgpr_read_b32 v33,a129
zaaa 160,56,16
ta 44,235,16384
ta 46,235,16896
ta 40,234,16384
ta 42,234,16896
ta 52,237,16384
ta 54,237,16896
ta 48,236,16384
ta 50,236,16896
ta 60,235,20480
ta 62,235,20992
ta 56,234,20480
ta 58,234,20992
v_accvgpr_read_b32 v32,a128
zaaa 128,64,16
v_accvgpr_read_b32 v37,a133
v_accvgpr_read_b32 v36,a132
zaaa 112,68,16
dl 230,44,54,2048
zaaa 116,68,12
v_accvgpr_read_b32 v40,a136
v_accvgpr_read_b32 v44,a140
zaaa 132,64,12
cv 251,42,43
cv 250,40,41
zaaa 136,64,8
cv 253,46,47
cv 252,44,45
zaaa 120,68,8
cv 249,38,39
cv 247,34,35
zaaa 124,68,4
dl 231,44,54,3072
zaaa 140,64,4
s_add_u32 m0,s61,4096
cv 246,32,33
cv 248,36,37
zvaa 40,72,4
s_add_u32 s54,s54,s57
ta 68,237,20480
cv 123,22,23
)"
R"(zvaa 44,76,4
s_add_u32 s61,s63,s98
ta 70,237,20992
cv 121,18,19
zvaa 52,76,8
s_add_u32 s63,s63,s99
ta 64,236,20480
cv 120,16,17
zvaa 48,72,8
dl 224,36,52,0
zvaa 56,72,12
ta 66,236,20992
cv 122,20,21
zvaa 60,76,12
cv 119,14,15
cv 118,12,13
zvaa 68,76,16
cv 117,10,11
cv 116,8,9
zvaa 64,72,16
cv 156,88,89
cv 153,82,83
zvaa 32,80,16
dl 225,36,52,1024
ta 76,235,24576
ta 78,235,25088
zvaa 36,84,16
ta 72,234,24576
ta 74,234,25088
zvaa 28,84,12
cv 152,80,81
cv 155,86,87
zvaa 24,80,12
cv 154,84,85
cv 149,74,75
zvaa 16,80,8
cv 148,72,73
v_add_u32_e32 v232,s99,v232
zvaa 20,84,8
dl 226,36,52,2048
zvaa 12,84,4
cv 115,6,7
cv 114,4,5
zvaa 8,80,4
cv 113,2,3
cv 112,0,1
zvaa 76,4,88
zvaa 96,4,92
zvaa 100,8,92
dl 227,36,52,3072
zvaa 80,8,88
s_add_u32 s52,s52,s60
zvaa 84,12,88
zvaa 104,12,92
zvaa 108,16,92
s_waitcnt vmcnt(23) lgkmcnt(0)
s_barrier
zvaa 88,16,88
buffer_store_dwordx4 v[250:253],v244,s[24:27],0 offen offset:1024 sc0 sc1
buffer_store_dwordx4 v[246:249],v244,s[24:27],0 offen offset:0 sc0 sc1
s_branch L_kvx
L_vtile:
L_vs4:
s_cbranch_execz L_vs8
s_waitcnt vmcnt(8)
s_barrier
ta 64,234,0
ta 66,234,512
ra 0,232,0
ta 68,235,0
ta 70,235,512
ra 4,232,2048
ra 8,232,4096
ra 12,232,6144
ra 16,232,8192
ra 20,232,10240
ra 24,232,12288
ra 28,232,14336
s_add_u32 m0,s62,32768
v_mov_b32_e32 v212,0
dl 228,40,53,0
v_mov_b32_e32 v213,0
v_mov_b32_e32 v214,0
v_mov_b32_e32 v215,0
v_mov_b32_e32 v220,0
v_mov_b32_e32 v221,0
v_mov_b32_e32 v222,0
v_mov_b32_e32 v223,0
v_mov_b32_e32 v216,0
v_mov_b32_e32 v217,0
v_mov_b32_e32 v218,0
v_mov_b32_e32 v219,0
v_mov_b32_e32 v208,0
v_mov_b32_e32 v209,0
v_mov_b32_e32 v210,0
v_mov_b32_e32 v211,0
v_mov_b32_e32 v148,0
s_waitcnt lgkmcnt(9)
zaaa 128,0,64
s_lshl_b32 s4,%1,1
ta 72,236,0
v_mov_b32_e32 v149,0
s_waitcnt lgkmcnt(8)
zaaa 136,0,68
s_add_u32 s4,s4,s12
ta 74,236,512
v_mov_b32_e32 v150,0
s_waitcnt lgkmcnt(8)
zaaa 140,4,68
s_mul_i32 s4,s4,1408
ta 76,237,0
ta 78,237,512
zaaa 132,4,64
dl 229,40,53,1024
s_waitcnt lgkmcnt(4)
zvaa 32,8,64
s_add_u32 s53,s53,s57
v_mov_b32_e32 v151,0
v_mov_b32_e32 v156,0
zvaa 40,8,68
s_mov_b32 s55,s4
v_mov_b32_e32 v157,0
v_mov_b32_e32 v158,0
zvaa 44,12,68
s_mul_i32 s4,%1,2048
ta 80,234,4096
v_mov_b32_e32 v159,0
zvaa 36,12,64
dl 230,44,54,2048
zvaa 96,16,64
s_lshl_b32 s88,%1,2
ta 82,234,4608
v_mov_b32_e32 v152,0
zvaa 104,16,68
s_cmp_eq_u32 s12,2
s_cselect_b32 s55,s4,s55
ta 84,235,4096
v_mov_b32_e32 v153,0
zvaa 108,20,68
s_lshl_b32 s4,%1,14
ta 86,235,4608
v_mov_b32_e32 v154,0
zvaa 100,20,64
dl 231,44,54,3072
zvaa 160,24,64
s_add_u32 m0,s98,36864
v_mov_b32_e32 v155,0
v_mov_b32_e32 v144,0
zvaa 168,24,68
s_add_u32 s54,s54,s57
v_mov_b32_e32 v145,0
v_mov_b32_e32 v146,0
zvaa 172,28,68
s_add_u32 s88,s88,s12
v_mov_b32_e32 v147,0
v_mov_b32_e32 v84,0
zvaa 164,28,64
dl 224,36,52,0
s_waitcnt lgkmcnt(4)
zvaa 180,28,72
s_add_u32 s5,s4,16384
v_mov_b32_e32 v85,0
v_mov_b32_e32 v86,0
zvaa 188,28,76
s_cmp_eq_u32 s12,3
s_cselect_b32 s55,s4,s55
ra 32,233,0
v_mov_b32_e32 v87,0
zvaa 184,24,76
s_mov_b32 s4,11
v_mov_b32_e32 v92,0
v_mov_b32_e32 v93,0
zvaa 176,24,72
dl 225,36,52,1024
zvaa 116,20,72
s_lshl_b32 s88,s88,12
ra 36,233,2048
v_mov_b32_e32 v94,0
zvaa 124,20,76
s_xor_b32 s87,s19,15
ra 40,233,4096
v_mov_b32_e32 v95,0
zvaa 120,16,76
s_cmp_eq_u32 s12,2
s_cselect_b32 s4,16,s4
v_mov_b32_e32 v88,0
v_mov_b32_e32 v89,0
zvaa 112,16,72
dl 226,36,52,2048
zvaa 52,12,72
s_waitcnt vmcnt(11)
s_barrier
zvaa 60,12,76
s_add_u32 s30,s68,s88
s_addc_u32 s31,s69,0
ta 96,234,16384
v_mov_b32_e32 v90,0
zvaa 56,8,76
s_lshl_b32 s22,s87,7
ta 98,234,16896
v_mov_b32_e32 v91,0
zvaa 48,8,72
dl 227,36,52,3072
zaaa 148,4,72
s_add_u32 m0,s62,49152
ta 100,235,16384
v_mov_b32_e32 v80,0
zaaa 156,4,76
s_add_u32 s52,s52,s60
ta 102,235,16896
v_mov_b32_e32 v81,0
zaaa 152,0,76
s_lshl_b32 s87,s87,2
ra 44,233,6144
v_mov_b32_e32 v82,0
zaaa 144,0,72
dl 228,40,53,0
s_waitcnt lgkmcnt(5)
zvaa 0,0,80
s_lshl_b32 s88,s19,2
ra 48,233,8192
v_mov_b32_e32 v83,0
zvaa 8,0,84
s_cmp_eq_u32 s12,3
s_cselect_b32 s4,11,s4
ra 52,233,10240
v_mov_b32_e32 v20,0
zvaa 12,4,84
s_add_u32 s87,s87,s12
ra 56,233,12288
v_mov_b32_e32 v21,0
zvaa 4,4,80
dl 229,40,53,1024
zvaa 64,8,80
s_add_u32 s88,s88,s12
v_mov_b32_e32 v22,0
v_mov_b32_e32 v23,0
zvaa 72,8,84
s_lshl_b32 s90,s18,13
ra 60,233,14336
v_mov_b32_e32 v28,0
zvaa 76,12,84
s_add_u32 s90,s90,62914560
ta 104,236,16384
v_mov_b32_e32 v29,0
zvaa 68,12,80
dl 230,44,54,2048
zvaa 128,16,80
s_mul_i32 s14,s87,327680
ta 106,236,16896
v_mov_b32_e32 v30,0
zvaa 136,16,84
s_mul_i32 s13,s88,327680
ta 108,237,16384
v_mov_b32_e32 v31,0
zvaa 140,20,84
s_add_u32 s14,s14,s90
ta 110,237,16896
v_mov_b32_e32 v24,0
zvaa 132,20,80
dl 231,44,54,3072
zvaa 192,24,80
s_add_u32 m0,s98,65536
v_mov_b32_e32 v25,0
v_mov_b32_e32 v26,0
zvaa 200,24,84
s_add_u32 s13,s13,s90
v_mov_b32_e32 v27,0
v_mov_b32_e32 v16,0
zvaa 204,28,84
s_mul_i32 s90,s18,524288
v_mov_b32_e32 v17,0
v_mov_b32_e32 v18,0
zvaa 196,28,80
dl 224,36,52,0
s_waitcnt lgkmcnt(4)
maaa 128,32,96
s_lshl_b32 s91,s12,12
ta 208,236,4096
v_mov_b32_e32 v19,0
maaa 136,32,100
s_add_u32 s90,s90,s91
ta 210,236,4608
v_lshlrev_b32_e32 v241,7,v247
maaa 140,36,100
s_add_u32 s90,s90,16777216
ta 216,237,4096
v_mov_b32_e32 v242,134217728
maaa 132,36,96
dl 225,36,52,1024
mvaa 32,40,96
s_cmp_eq_u32 s12,3
s_cselect_b64 s[26:27],-1,0
ta 218,237,4608
v_lshrrev_b32_e32 v248,3,v247
mvaa 40,40,100
s_mov_b32 s50,8388608
v_and_b32_e32 v250,112,v244
v_and_b32_e32 v251,16,v247
mvaa 44,44,100
s_mov_b32 s59,720896
v_lshlrev_b32_e32 v251,1,v251
v_cmp_gt_u32_e64 s[24:25],s4,v247
mvaa 36,44,96
dl 226,36,52,2048
mvaa 96,48,96
s_lshl_b32 s86,s19,1
v_mul_u32_u24_e32 v246,6144,v248
v_add_u32_e32 v249,s90,v244
mvaa 104,48,100
s_mov_b64 s[48:49],s[72:73]
ta 112,234,20480
v_cndmask_b32_e64 v241,v242,v241,s[24:25]
mvaa 108,52,100
s_cmp_eq_u32 s12,2
s_cselect_b64 s[24:25],-1,0
ta 114,234,20992
v_add_u32_e32 v255,16777216,v249
mvaa 100,52,96
dl 227,36,52,3072
mvaa 160,56,96
s_add_u32 s52,s52,s56
s_add_u32 m0,s98,69632
ta 116,235,20480
mvaa 168,56,100
s_cmp_eq_u32 s12,2
s_cselect_b32 s50,6291456,s50
ta 118,235,20992
v_accvgpr_write_b32 a240,v249
mvaa 172,60,100
s_cmp_eq_u32 s12,2
s_cselect_b32 s59,524288,s59
v_and_b32_e32 v249,48,v247
v_accvgpr_write_b32 a241,v255
mvaa 164,60,96
dl 224,36,52,0
s_waitcnt lgkmcnt(4)
mvaa 180,60,104
s_add_u32 s86,s86,2
ta 212,236,20480
v_xor_b32_e32 v249,v249,v250
mvaa 188,60,108
s_cmp_eq_u32 s12,2
s_cselect_b64 s[48:49],s[80:81],s[48:49]
v_xor_b32_e32 v250,v250,v251
v_lshl_add_u32 v252,v248,11,v249
mvaa 184,56,108
s_cmp_eq_u32 s12,3
s_cselect_b32 s50,s5,s50
v_and_b32_e32 v251,48,v250
v_xor_b32_e32 v253,64,v252
mvaa 176,56,104
dl 225,36,52,1024
mvaa 116,52,104
s_cmp_eq_u32 s12,3
s_cselect_b32 s59,1408,s59
v_accvgpr_write_b32 a242,v252
v_add_u32_e32 v246,v246,v251
mvaa 124,52,108
s_lshl_b32 s15,1,s86
v_add_u32_e32 v242,15360,v253
v_and_b32_e32 v251,64,v250
mvaa 120,48,108
s_lshl_b32 s23,s19,7
v_lshl_add_u32 v248,v251,5,v246
v_xor_b32_e32 v249,64,v251
mvaa 112,48,104
dl 226,36,52,2048
mvaa 52,44,104
s_or_b32 s86,%1,s12
s_cselect_b32 s65,0,4
v_accvgpr_write_b32 a243,v242
v_mul_u32_u24_e32 v251,768,v251
mvaa 60,44,108
s_cmp_eq_u32 s12,3
s_cselect_b64 s[48:49],s[68:69],s[48:49]
v_add_u32_e32 v242,30720,v252
v_lshl_add_u32 v249,v249,5,v246
mvaa 56,40,108
s_mov_b32 s33,s50
v_add_u32_e32 v246,v246,v251
v_accvgpr_write_b32 a244,v242
mvaa 48,40,104
dl 227,36,52,3072
maaa 148,36,104
s_mov_b32 s34,s59
v_add_u32_e32 v242,46080,v253
v_accvgpr_write_b32 a245,v242
maaa 156,36,108
s_mov_b32 s50,0
v_add_u32_e32 v243,4096,v246
v_add_u32_e32 v242,0,v248
maaa 152,32,108
s_mov_b32 s59,0
ta 214,236,20992
v_cndmask_b32_e64 v242,v228,v242,s[24:25]
maaa 144,32,104
s_add_u32 s53,s53,s58
ta 220,237,20480
v_cndmask_b32_e64 v242,v242,v243,s[26:27]
s_waitcnt lgkmcnt(2)
mvaa 0,32,112
s_add_u32 s54,s54,s58
v_add_u32_e32 v243,101376,v246
v_and_b32_e32 v246,15,v247
mvaa 8,32,116
s_add_u32 s52,s52,s60
v_and_b32_e32 v246,12,v247
v_accvgpr_write_b32 a246,v242
mvaa 12,36,116
s_mov_b32 s63,32768
ta 222,237,20992
v_add_u32_e32 v242,48128,v249
mvaa 4,36,112
s_mov_b32 s61,s98
v_cndmask_b32_e64 v242,v229,v242,s[24:25]
v_lshlrev_b32_e32 v246,5,v246
mvaa 64,40,112
s_mov_b32 s64,1
v_cndmask_b32_e64 v242,v242,v243,s[26:27]
v_accvgpr_write_b32 a247,v242
mvaa 72,40,116
v_add_u32_e32 v242,96256,v248
v_and_b32_e32 v248,3,v247
mvaa 76,44,116
v_cndmask_b32_e64 v242,v230,v242,s[24:25]
v_lshl_add_u32 v246,v248,3,v246
mvaa 68,44,112
v_and_b32_e32 v248,24,v247
v_accvgpr_write_b32 a248,v242
mvaa 128,48,112
v_add_u32_e32 v242,144384,v249
v_lshl_add_u32 v246,v248,2,v246
mvaa 136,48,116
v_and_b32_e32 v248,32,v247
v_cndmask_b32_e64 v242,v231,v242,s[24:25]
mvaa 140,52,116
v_add_u32_e32 v246,98304,v246
v_accvgpr_write_b32 a249,v242
mvaa 132,52,112
v_lshl_add_u32 v238,v248,5,v246
v_xor_b32_e32 v239,32,v238
mvaa 192,56,112
v_add_u32_e32 v232,32768,v232
v_add_u32_e32 v233,32768,v233
mvaa 200,56,116
mvaa 204,60,116
mvaa 196,60,112
s_waitcnt vmcnt(8)
s_waitcnt lgkmcnt(0)
s_barrier
L_v_head:
mvaa 212,28,208
ra 64,232,0
mvaa 212,60,212
s_add_u32 m0,s62,0
ta 192,234,32768
mvaa 220,60,220
L_vs5:
s_cbranch_execz L_vs9
dl 228,40,53,0
mvaa 220,28,216
ta 194,234,33280
s_nop 3
mvaa 216,24,216
ra 96,233,0
mvaa 216,56,220
ta 196,234,49152
mvaa 208,56,212
ta 198,234,49664
mvaa 208,24,208
ta 204,235,49152
mvaa 148,20,208
dl 229,40,53,1024
mvaa 148,52,212
s_add_u32 s53,s53,s57
ta 206,235,49664
mvaa 156,52,220
ta 200,235,32768
mvaa 156,20,216
ta 202,235,33280
mvaa 152,16,216
ra 68,232,2048
mvaa 152,48,220
ra 100,233,2048
mvaa 144,48,212
ra 72,232,4096
mvaa 144,16,208
dl 230,44,54,2048
mvaa 84,12,208
ra 104,233,4096
mvaa 84,44,212
ra 76,232,6144
mvaa 92,44,220
ra 108,233,6144
mvaa 92,12,216
ra 80,232,8192
mvaa 88,8,216
ra 112,233,8192
mvaa 88,40,220
dl 231,44,54,3072
mvaa 80,40,212
s_add_u32 s54,s54,s57
ra 84,232,10240
mvaa 80,8,208
ra 116,233,10240
mvaa 20,4,208
ra 88,232,12288
mvaa 20,36,212
ra 120,233,12288
mvaa 28,36,220
ra 92,232,14336
mvaa 28,4,216
s_add_u32 m0,s62,16384
ra 124,233,14336
mvaa 24,0,216
dl 228,40,53,0
mvaa 24,32,220
mvaa 16,32,212
mvaa 16,0,208
s_waitcnt lgkmcnt(8)
maaa 128,64,192
maaa 128,96,196
maaa 136,96,204
dl 229,40,53,1024
maaa 136,64,200
s_add_u32 s53,s53,s58
maaa 140,68,200
maaa 140,100,204
maaa 132,100,196
maaa 132,68,192
mvaa 32,72,192
mvaa 32,104,196
dl 230,44,54,2048
mvaa 40,104,204
mvaa 40,72,200
s_waitcnt lgkmcnt(0)
mvaa 44,76,200
ta 208,236,32768
mvaa 44,108,204
ta 210,236,33280
mvaa 36,108,196
ta 212,236,49152
mvaa 36,76,192
dl 231,44,54,3072
mvaa 96,80,192
s_add_u32 s54,s54,s58
ta 214,236,49664
mvaa 96,112,196
ta 220,237,49152
mvaa 104,112,204
ta 222,237,49664
mvaa 104,80,200
ta 216,237,32768
mvaa 108,84,200
ta 218,237,33280
mvaa 108,116,204
mvaa 100,116,196
buffer_load_dword a184,v241,s[48:51],s55 offen
mvaa 100,84,192
s_add_u32 s55,s55,s59
mvaa 160,88,192
mvaa 160,120,196
mvaa 168,120,204
mvaa 168,88,200
mvaa 172,92,200
s_add_u32 m0,s61,0
mvaa 172,124,204
dl 224,36,52,0
mvaa 164,124,196
mvaa 164,92,192
s_waitcnt lgkmcnt(0)
mvaa 180,92,208
mvaa 180,124,212
mvaa 188,124,220
ta 192,234,36864
mvaa 188,92,216
ta 194,234,37376
mvaa 184,88,216
dl 225,36,52,1024
mvaa 184,120,220
ta 196,234,53248
mvaa 176,120,212
ta 198,234,53760
mvaa 176,88,208
ta 204,235,53248
mvaa 116,84,208
ta 206,235,53760
mvaa 116,116,212
ta 200,235,36864
mvaa 124,116,220
ta 202,235,37376
mvaa 124,84,216
dl 226,36,52,2048
mvaa 120,80,216
mvaa 120,112,220
mvaa 112,112,212
mvaa 112,80,208
mvaa 52,76,208
mvaa 52,108,212
mvaa 60,108,220
dl 227,36,52,3072
mvaa 60,76,216
s_add_u32 s52,s52,s56
mvaa 56,72,216
mvaa 56,104,220
mvaa 48,104,212
mvaa 48,72,208
maaa 148,68,208
maaa 148,100,212
buffer_load_dword a185,v241,s[48:51],s55 offen
maaa 156,100,220
s_add_u32 s55,s55,s59
maaa 156,68,216
s_mov_b32 s50,s33
s_mov_b32 s59,s34
maaa 152,64,216
maaa 152,96,220
maaa 144,96,212
s_waitcnt lgkmcnt(0)
maaa 144,64,208
s_add_u32 m0,s61,4096
mvaa 0,64,192
dl 224,36,52,0
mvaa 0,96,196
s_add_u32 s61,s63,s98
mvaa 8,96,204
ta 208,236,36864
mvaa 8,64,200
ta 210,236,37376
mvaa 12,68,200
ta 212,236,53248
mvaa 12,100,204
ta 214,236,53760
mvaa 4,100,196
ta 220,237,53248
mvaa 4,68,192
ta 222,237,53760
mvaa 64,72,192
dl 225,36,52,1024
mvaa 64,104,196
ta 216,237,36864
mvaa 72,104,204
ta 218,237,37376
mvaa 72,72,200
s_cmp_ge_u32 s63,65536
s_cselect_b32 s99,-2,1
s_lshl_b32 s99,s99,15
mvaa 76,76,200
s_add_u32 s63,s63,s99
v_add_u32_e32 v232,s99,v232
v_add_u32_e32 v233,s99,v233
mvaa 76,108,204
mvaa 68,108,196
mvaa 68,76,192
dl 226,36,52,2048
mvaa 128,80,192
mvaa 128,112,196
mvaa 136,112,204
mvaa 136,80,200
mvaa 140,84,200
mvaa 140,116,204
mvaa 132,116,196
dl 227,36,52,3072
mvaa 132,84,192
s_add_u32 s52,s52,s60
mvaa 192,88,192
mvaa 192,120,196
mvaa 200,120,204
mvaa 200,88,200
mvaa 204,92,200
mvaa 204,124,204
s_cmp_eq_u32 s64,3
mvaa 196,124,196
s_waitcnt vmcnt(10) lgkmcnt(0)
s_barrier
mvaa 196,92,192
s_cbranch_scc1 L_v_exit
mvaa 212,92,208
ra 0,232,0
mvaa 212,124,212
s_add_u32 m0,s62,32768
ta 192,234,0
mvaa 220,124,220
dl 228,40,53,0
mvaa 220,92,216
ta 194,234,512
s_nop 3
mvaa 216,88,216
ra 32,233,0
mvaa 216,120,220
ta 196,234,16384
mvaa 208,120,212
ta 198,234,16896
mvaa 208,88,208
ta 204,235,16384
mvaa 148,84,208
dl 229,40,53,1024
mvaa 148,116,212
s_add_u32 s53,s53,s57
ta 206,235,16896
mvaa 156,116,220
ta 200,235,0
mvaa 156,84,216
ta 202,235,512
mvaa 152,80,216
ra 4,232,2048
mvaa 152,112,220
ra 36,233,2048
mvaa 144,112,212
ra 8,232,4096
mvaa 144,80,208
dl 230,44,54,2048
mvaa 84,76,208
ra 40,233,4096
mvaa 84,108,212
ra 12,232,6144
mvaa 92,108,220
ra 44,233,6144
mvaa 92,76,216
ra 16,232,8192
mvaa 88,72,216
ra 48,233,8192
mvaa 88,104,220
dl 231,44,54,3072
mvaa 80,104,212
s_add_u32 s54,s54,s57
ra 20,232,10240
mvaa 80,72,208
ra 52,233,10240
mvaa 20,68,208
ra 24,232,12288
mvaa 20,100,212
ra 56,233,12288
mvaa 28,100,220
ra 28,232,14336
mvaa 28,68,216
s_add_u32 m0,s62,49152
ra 60,233,14336
mvaa 24,64,216
dl 228,40,53,0
mvaa 24,96,220
mvaa 16,96,212
mvaa 16,64,208
s_waitcnt lgkmcnt(8)
maaa 128,0,192
maaa 128,32,196
maaa 136,32,204
dl 229,40,53,1024
maaa 136,0,200
s_add_u32 s53,s53,s58
maaa 140,4,200
maaa 140,36,204
maaa 132,36,196
maaa 132,4,192
mvaa 32,8,192
mvaa 32,40,196
dl 230,44,54,2048
mvaa 40,40,204
mvaa 40,8,200
s_waitcnt lgkmcnt(0)
mvaa 44,12,200
ta 208,236,0
mvaa 44,44,204
ta 210,236,512
mvaa 36,44,196
ta 212,236,16384
mvaa 36,12,192
dl 231,44,54,3072
mvaa 96,16,192
s_add_u32 s54,s54,s58
ta 214,236,16896
mvaa 96,48,196
ta 220,237,16384
mvaa 104,48,204
ta 222,237,16896
mvaa 104,16,200
ta 216,237,0
mvaa 108,20,200
ta 218,237,512
mvaa 108,52,204
mvaa 100,52,196
buffer_load_dword a186,v241,s[48:51],s55 offen
mvaa 100,20,192
s_add_u32 s55,s55,s59
mvaa 160,24,192
s_lshl_b32 s86,s21,2
s_add_u32 s86,s86,s12
s_mul_i32 s86,s86,384
mvaa 160,56,196
s_add_u32 s86,s86,4096
v_lshl_add_u32 v253,v244,3,s86
mvaa 168,56,204
s_mov_b64 exec,7
global_load_dword a188,v253,s[96:97]
s_mov_b64 exec,-1
mvaa 168,24,200
mvaa 172,28,200
s_add_u32 m0,s61,0
mvaa 172,60,204
dl 224,36,52,0
mvaa 164,60,196
mvaa 164,28,192
s_waitcnt lgkmcnt(0)
mvaa 180,28,208
mvaa 180,60,212
mvaa 188,60,220
ta 192,234,4096
mvaa 188,28,216
ta 194,234,4608
mvaa 184,24,216
dl 225,36,52,1024
mvaa 184,56,220
ta 196,234,20480
mvaa 176,56,212
ta 198,234,20992
mvaa 176,24,208
ta 204,235,20480
mvaa 116,20,208
ta 206,235,20992
mvaa 116,52,212
ta 200,235,4096
mvaa 124,52,220
ta 202,235,4608
mvaa 124,20,216
dl 226,36,52,2048
mvaa 120,16,216
mvaa 120,48,220
mvaa 112,48,212
mvaa 112,16,208
mvaa 52,12,208
mvaa 52,44,212
s_bitcmp1_b32 s65,s64
s_cbranch_scc1 L_v_ev
L_v_evr:
mvaa 60,44,220
dl 227,36,52,3072
mvaa 60,12,216
s_add_u32 s52,s52,s56
mvaa 56,8,216
mvaa 56,40,220
mvaa 48,40,212
mvaa 48,8,208
maaa 148,4,208
maaa 148,36,212
buffer_load_dword a187,v241,s[48:51],s55 offen
maaa 156,36,220
s_add_u32 s55,s55,s59
maaa 156,4,216
maaa 152,0,216
maaa 152,32,220
maaa 144,32,212
s_waitcnt lgkmcnt(0)
maaa 144,0,208
s_add_u32 m0,s61,4096
mvaa 0,0,192
dl 224,36,52,0
mvaa 0,32,196
s_add_u32 s61,s63,s98
mvaa 8,32,204
ta 208,236,4096
mvaa 8,0,200
ta 210,236,4608
mvaa 12,4,200
ta 212,236,20480
mvaa 12,36,204
ta 214,236,20992
mvaa 4,36,196
ta 220,237,20480
mvaa 4,4,192
ta 222,237,20992
mvaa 64,8,192
dl 225,36,52,1024
mvaa 64,40,196
ta 216,237,4096
mvaa 72,40,204
ta 218,237,4608
mvaa 72,8,200
s_cmp_ge_u32 s63,65536
s_cselect_b32 s99,-2,1
s_lshl_b32 s99,s99,15
mvaa 76,12,200
s_add_u32 s63,s63,s99
v_add_u32_e32 v232,s99,v232
v_add_u32_e32 v233,s99,v233
mvaa 76,44,204
mvaa 68,44,196
mvaa 68,12,192
dl 226,36,52,2048
mvaa 128,16,192
mvaa 128,48,196
mvaa 136,48,204
mvaa 136,16,200
mvaa 140,20,200
mvaa 140,52,204
mvaa 132,52,196
dl 227,36,52,3072
mvaa 132,20,192
s_add_u32 s52,s52,s60
mvaa 192,24,192
mvaa 192,56,196
mvaa 200,56,204
s_add_u32 s64,s64,1
mvaa 200,24,200
mvaa 204,28,200
mvaa 204,60,204
mvaa 196,60,196
s_waitcnt vmcnt(10) lgkmcnt(0)
s_barrier
mvaa 196,28,192
s_branch L_v_head
L_v_exit:
global_load_dwordx4 a[160:163],v244,s[30:31] offset:0
s_add_u32 m0,s62,32768
mvaa 212,92,208
s_cmp_lt_u32 s12,2
s_cselect_b32 s86,s22,s23
ta 32,234,0
s_and_b32 s87,s12,1
mvaa 212,124,212
s_lshl_b32 s86,s86,11
ta 34,234,512
s_lshl_b32 s87,s87,17
mvaa 220,124,220
s_add_u32 s86,s86,s87
ra 0,232,0
s_mov_b64 s[36:37],s[68:69]
mvaa 220,92,216
s_mov_b32 s38,4194304
ta 36,235,0
s_mov_b32 s52,s86
mvaa 216,88,216
s_mov_b32 s56,65536
ta 38,235,512
v_accvgpr_read_b32 v224,a242
mvaa 216,120,220
dl 228,40,53,0
mvaa 208,120,212
s_lshl_b32 s88,s18,1
ra 4,232,2048
v_accvgpr_read_b32 v225,a243
mvaa 208,88,208
s_add_u32 s88,s88,s12
ra 8,232,4096
v_accvgpr_read_b32 v226,a244
mvaa 148,84,208
s_mov_b32 s91,196608
ra 12,232,6144
v_accvgpr_read_b32 v227,a245
mvaa 148,116,212
s_lshl_b32 s88,s88,7
ra 16,232,8192
s_lshl_b32 s89,s18,6
mvaa 156,116,220
s_mov_b32 s90,6291456
ra 20,232,10240
s_mov_b32 s60,-65408
mvaa 156,84,216
dl 229,40,53,1024
mvaa 152,80,216
s_add_u32 s53,s53,s57
ra 24,232,12288
s_lshl_b32 s87,s20,2
mvaa 152,112,220
s_mov_b32 s26,32768
ra 28,232,14336
s_mov_b32 s27,131072
mvaa 144,112,212
s_add_u32 s61,s98,32768
ta 40,236,0
s_mov_b32 s63,65536
mvaa 144,80,208
s_cmp_ge_u32 s63,65536
s_cselect_b32 s99,-2,1
ta 42,236,512
s_lshl_b32 s99,s99,15
mvaa 84,76,208
s_cmp_eq_u32 s12,0
s_cselect_b32 s65,96,0
ta 44,237,0
s_mov_b32 s67,2048
mvaa 84,108,212
dl 230,44,54,2048
mvaa 92,108,220
s_mov_b32 s66,8
ta 46,237,512
s_mov_b32 s64,1
mvaa 92,76,216
s_or_b32 s65,s65,128
ta 48,234,4096
mvaa 88,72,216
s_bitcmp1_b32 s12,0
s_cselect_b64 vcc,-1,0
ta 50,234,4608
mvaa 88,104,220
ta 52,235,4096
mvaa 80,104,212
ta 54,235,4608
mvaa 80,72,208
dl 231,44,54,3072
)"
R"(s_add_u32 m0,s62,49152
mvaa 20,68,208
s_add_u32 s54,s54,s57
ta 56,236,4096
mvaa 20,100,212
s_cmp_lt_u32 s12,2
s_cselect_b32 s57,262144,s91
ta 58,236,4608
mvaa 28,100,220
ta 60,237,4096
mvaa 28,68,216
ta 62,237,4608
mvaa 24,64,216
L_vs6:
s_cbranch_execz L_vs10
mvaa 24,96,220
dl 228,40,53,0
mvaa 16,96,212
v_accvgpr_read_b32 v228,a246
mvaa 16,64,208
s_waitcnt lgkmcnt(5)
maaa 128,0,32
maaa 136,0,36
maaa 140,4,36
dl 229,40,53,1024
maaa 132,4,32
s_waitcnt lgkmcnt(0)
s_barrier
mvaa 32,8,32
s_add_u32 s53,s53,s58
ta 96,234,16384
v_accvgpr_read_b32 v229,a247
mvaa 40,8,36
s_cmp_lt_u32 s12,2
s_cselect_b32 s42,8388608,s90
ta 98,234,16896
mvaa 44,12,36
s_cmp_lt_u32 s12,2
s_cselect_b32 s53,s88,s89
ra 64,233,0
s_and_b32 s89,s98,8192
mvaa 36,12,32
dl 230,44,54,2048
mvaa 96,16,32
s_bitcmp1_b32 s21,0
s_cselect_b32 s90,s57,0
ta 100,235,16384
s_add_u32 s53,s53,s90
mvaa 104,16,36
s_cmp_lt_u32 s12,2
s_cselect_b64 s[40:41],s[72:73],s[80:81]
ta 102,235,16896
v_accvgpr_read_b32 v230,a248
mvaa 108,20,36
s_lshl_b32 s90,s90,1
ra 68,233,2048
s_add_u32 s91,s89,65536
mvaa 100,20,32
s_bfe_u32 s88,s21,65539
ra 72,233,4096
s_lshl_b32 s88,s88,1
mvaa 160,24,32
dl 231,44,54,3072
mvaa 168,24,36
s_add_u32 m0,s98,65536
ra 76,233,6144
s_add_u32 s54,s54,s58
mvaa 172,28,36
s_cmp_eq_u32 s12,3
s_cselect_b64 s[44:45],s[68:69],s[40:41]
ra 80,233,8192
v_accvgpr_read_b32 v231,a249
mvaa 164,28,32
s_cmp_eq_u32 s12,3
s_cselect_b32 s46,0,s42
ra 84,233,10240
s_add_u32 s58,s57,s90
mvaa 180,28,40
s_cmp_eq_u32 s12,3
s_cselect_b32 s54,0,s53
ra 88,233,12288
s_sub_u32 s57,s57,s90
mvaa 188,28,44
global_load_dwordx4 a[168:171],v244,s[30:31] offset:2048
mvaa 184,24,44
s_add_u32 s90,s89,65536
ta 104,236,16384
s_add_u32 s88,s88,s87
mvaa 176,24,40
s_and_b32 s87,s12,1
ra 92,233,14336
s_add_u32 s88,s88,s87
mvaa 116,20,40
s_mul_i32 s88,s88,524288
ta 106,236,16896
s_and_b32 s87,s21,7
mvaa 124,20,44
s_add_u32 s86,s88,33554432
ta 108,237,16384
s_lshl_b32 s87,s87,16
mvaa 120,16,44
global_load_dwordx4 a[176:179],v244,s[84:85] offset:0
mvaa 112,16,40
s_add_u32 s86,s86,s87
ta 110,237,16896
s_lshr_b32 s87,s12,1
mvaa 52,12,40
s_lshl_b32 s87,s87,15
ta 112,234,20480
s_add_u32 s86,s86,s87
mvaa 60,12,44
s_add_u32 s24,s8,s86
s_addc_u32 s25,s9,0
ta 114,234,20992
s_lshl_b32 s86,%1,5
mvaa 56,8,44
s_lshl_b32 s87,s12,3
ta 116,235,20480
s_add_u32 s86,s86,s87
mvaa 48,8,40
global_load_dwordx4 a[164:167],v244,s[30:31] offset:1024
maaa 148,4,40
s_add_u32 s86,s86,83886080
ta 118,235,20992
s_mov_b32 s88,4096
maaa 156,4,44
ta 120,236,20480
maaa 152,0,44
ta 122,236,20992
maaa 144,0,40
ta 124,237,20480
mvaa 0,0,48
global_load_dwordx4 a[172:175],v244,s[30:31] offset:3072
mvaa 8,0,52
s_mov_b32 s30,1048577
ta 126,237,20992
s_mov_b32 s31,268435712
mvaa 12,4,52
mvaa 4,4,48
mvaa 64,8,48
mvaa 72,8,52
global_load_dwordx4 a[180:183],v244,s[84:85] offset:1024
mvaa 76,12,52
mvaa 68,12,48
mvaa 128,16,48
mvaa 136,16,52
mvaa 140,20,52
dl 224,36,52,0
mvaa 132,20,48
mvaa 192,24,48
mvaa 200,24,52
mvaa 204,28,52
mvaa 196,28,48
dl 225,36,52,1024
mvaa 212,28,56
mvaa 220,28,60
mvaa 216,24,60
mvaa 208,24,56
mvaa 148,20,56
dl 226,36,52,2048
mvaa 156,20,60
mvaa 152,16,60
mvaa 144,16,56
mvaa 84,12,56
s_waitcnt vmcnt(12) lgkmcnt(0)
s_barrier
mvaa 92,12,60
dl 227,36,52,3072
mvaa 88,8,60
s_add_u32 s52,s52,s56
s_add_u32 m0,s98,69632
ta 32,234,32768
mvaa 80,8,56
ta 34,234,33280
mvaa 20,4,56
ta 36,235,32768
mvaa 28,4,60
ta 38,235,33280
mvaa 24,0,60
dl 224,36,52,0
mvaa 16,0,56
ta 40,236,32768
maaa 128,64,96
ta 42,236,33280
maaa 136,64,100
ta 44,237,32768
maaa 140,68,100
ta 46,237,33280
maaa 132,68,96
dl 225,36,52,1024
mvaa 32,72,96
ta 48,234,36864
mvaa 40,72,100
ta 50,234,37376
mvaa 44,76,100
ta 52,235,36864
mvaa 36,76,96
ta 54,235,37376
mvaa 96,80,96
dl 226,36,52,2048
mvaa 104,80,100
ra 20,232,43008
mvaa 108,84,100
ra 0,232,32768
mvaa 100,84,96
ra 24,232,45056
mvaa 160,88,96
ra 4,232,34816
mvaa 168,88,100
dl 227,36,52,3072
mvaa 172,92,100
s_add_u32 m0,s62,0
ra 28,232,47104
s_add_u32 s52,s52,s60
mvaa 164,92,96
ra 8,232,36864
mvaa 180,92,104
ra 12,232,38912
mvaa 188,92,108
ra 16,232,40960
v_add_u32_e32 v232,s90,v232
mvaa 184,88,108
dl 228,40,53,0
mvaa 176,88,104
ta 60,237,36864
mvaa 116,84,104
ta 62,237,37376
mvaa 124,84,108
ta 56,236,36864
mvaa 120,80,108
ta 58,236,37376
mvaa 112,80,104
dl 229,40,53,1024
mvaa 52,76,104
s_add_u32 s53,s53,s57
mvaa 60,76,108
mvaa 56,72,108
mvaa 48,72,104
maaa 148,68,104
dl 230,44,54,2048
maaa 156,68,108
maaa 152,64,108
maaa 144,64,104
mvaa 0,64,112
mvaa 8,64,116
dl 231,44,54,3072
mvaa 12,68,116
s_add_u32 m0,s62,16384
s_add_u32 s54,s54,s57
mvaa 4,68,112
mvaa 64,72,112
mvaa 72,72,116
mvaa 76,76,116
dl 228,40,53,0
mvaa 68,76,112
mvaa 128,80,112
mvaa 136,80,116
mvaa 140,84,116
mvaa 132,84,112
dl 229,40,53,1024
mvaa 192,88,112
s_add_u32 s53,s53,s58
mvaa 200,88,116
mvaa 204,92,116
mvaa 196,92,112
mvaa 212,92,120
dl 230,44,54,2048
mvaa 220,92,124
mvaa 216,88,124
s_waitcnt vmcnt(20) lgkmcnt(0)
s_barrier
mvaa 208,88,120
ra 92,233,47104
mvaa 148,84,120
ta 96,234,49152
mvaa 156,84,124
dl 231,44,54,3072
mvaa 152,80,124
s_add_u32 m0,s98,0
ra 88,233,45056
s_add_u32 s54,s54,s58
mvaa 144,80,120
ta 98,234,49664
mvaa 84,76,120
ta 100,235,49152
mvaa 92,76,124
ta 116,235,53248
mvaa 88,72,124
dl 224,36,52,0
mvaa 80,72,120
ta 118,235,53760
mvaa 20,68,120
ta 102,235,49664
v_subrev_u32_e32 v235,s89,v235
mvaa 28,68,124
ta 104,236,49152
mvaa 24,64,124
ta 112,234,53248
mvaa 16,64,120
dl 225,36,52,1024
maaa 128,0,32
ta 124,237,53248
maaa 136,0,36
ta 126,237,53760
maaa 140,4,36
ta 120,236,53248
maaa 132,4,32
ta 122,236,53760
mvaa 32,8,32
dl 226,36,52,2048
mvaa 40,8,36
ta 114,234,53760
v_subrev_u32_e32 v234,s89,v234
mvaa 44,12,36
ta 106,236,49664
v_subrev_u32_e32 v236,s89,v236
mvaa 36,12,32
ta 108,237,49152
mvaa 52,12,40
ta 110,237,49664
v_subrev_u32_e32 v237,s89,v237
mvaa 60,12,44
dl 227,36,52,3072
mvaa 56,8,44
s_add_u32 s52,s52,s56
s_add_u32 m0,s98,4096
ra 84,233,43008
mvaa 48,8,40
ra 80,233,40960
maaa 148,4,40
ra 76,233,38912
maaa 156,4,44
ra 68,233,34816
maaa 152,0,44
dl 224,36,52,0
maaa 144,0,40
ra 72,233,36864
mvaa 0,0,48
ra 64,233,32768
v_add_u32_e32 v233,s91,v233
mvaa 8,0,52
mvaa 12,4,52
mvaa 4,4,48
dl 225,36,52,1024
mvaa 20,4,56
mvaa 28,4,60
mvaa 24,0,60
mvaa 16,0,56
mvaa 220,28,60
dl 226,36,52,2048
s_waitcnt lgkmcnt(2)
mvaa 220,92,124
mvaa 216,88,124
mvaa 216,24,60
mvaa 212,28,56
mvaa 212,92,120
dl 227,36,52,3072
mvaa 208,88,120
s_add_u32 m0,s62,32768
s_add_u32 s52,s52,s60
s_waitcnt vmcnt(25)
zvaa 246,160,176
mvaa 208,24,56
cv 223,222,223
cv 222,220,221
mvaa 204,28,52
cv 221,218,219
zvaa 250,168,176
mvaa 204,92,116
cv 220,216,217
cv 219,214,215
mvaa 200,88,116
dl 228,40,53,0
s_waitcnt vmcnt(25)
mvaa 246,164,180
mvaa 200,24,52
cv 218,212,213
mvaa 250,172,180
mvaa 196,28,48
cv 217,210,211
cv 216,208,209
mvaa 196,92,112
cv 215,206,207
v_cndmask_b32_e64 v246,0,v246,s[30:31]
mvaa 192,88,112
cv 214,204,205
v_cndmask_b32_e64 v250,0,v250,s[30:31]
mvaa 192,24,48
dl 229,40,53,1024
s_lshl_b64 s[30:31],s[30:31],1
v_cndmask_b32_e64 v246,v246,v247,s[30:31]
v_cndmask_b32_e64 v250,v250,v251,s[30:31]
mvaa 188,28,44
s_lshl_b64 s[30:31],s[30:31],1
s_add_u32 s53,s53,s57
v_cndmask_b32_e64 v246,v246,v248,s[30:31]
v_cndmask_b32_e64 v250,v250,v252,s[30:31]
mvaa 188,92,108
s_lshl_b64 s[30:31],s[30:31],1
cv 213,202,203
v_cndmask_b32_e64 v246,v246,v249,s[30:31]
mvaa 184,88,108
v_cndmask_b32_e64 v250,v250,v253,s[30:31]
v_add_f32_dpp v246,v246,v246 quad_perm:[1,0,3,2] row_mask:0xf bank_mask:0xf
mvaa 184,24,44
v_add_f32_dpp v250,v250,v250 quad_perm:[1,0,3,2] row_mask:0xf bank_mask:0xf
v_add_f32_dpp v246,v246,v246 quad_perm:[2,3,0,1] row_mask:0xf bank_mask:0xf
mvaa 180,28,40
dl 230,44,54,2048
v_add_f32_dpp v250,v250,v250 quad_perm:[2,3,0,1] row_mask:0xf bank_mask:0xf
mvaa 180,92,104
v_add_f32_dpp v246,v246,v246 row_half_mirror row_mask:0xf bank_mask:0xf
v_add_f32_dpp v250,v250,v250 row_half_mirror row_mask:0xf bank_mask:0xf
mvaa 176,88,104
v_add_f32_dpp v246,v246,v246 row_mirror row_mask:0xf bank_mask:0xf
v_add_f32_dpp v250,v250,v250 row_mirror row_mask:0xf bank_mask:0xf
mvaa 176,24,40
cv 212,200,201
cv 211,198,199
mvaa 172,28,36
v_permlane16_swap_b32_e32 v246,v250
mvaa 172,92,100
dl 231,44,54,3072
v_add_f32_e32 v246,v246,v250
v_mov_b32_e32 v250,v246
mvaa 168,88,100
s_add_u32 s54,s54,s57
v_permlane32_swap_b32_e32 v246,v250
mvaa 168,24,36
v_add_f32_e32 v246,v246,v250
v_lshrrev_b32_e32 v250,6,v244
mvaa 164,28,32
v_mul_f32_e32 v246,0xbfb8aa3b,v246
cv 210,196,197
mvaa 164,92,96
ex 246
mvaa 160,88,96
buffer_load_dword a242,v241,s[48:51],s55 offen
mvaa 160,24,32
s_add_u32 s55,s55,s59
v_add_f32_e32 v246,1.0,v246
cv 209,194,195
mvaa 156,20,60
v_rcp_f32_e32 v246,v246
mvaa 156,84,124
s_waitcnt vmcnt(17) lgkmcnt(0)
s_barrier
s_add_u32 m0,s62,49152
mvaa 152,80,124
cv 208,192,193
cv 207,190,191
mvaa 152,16,60
dl 228,40,53,0
mvaa 148,20,56
cv 206,188,189
cv 205,186,187
mvaa 148,84,120
cv 204,184,185
cv 203,182,183
mvaa 144,80,120
cv 202,180,181
cv 201,178,179
mvaa 144,16,56
cv 200,176,177
cv 199,174,175
mvaa 140,20,52
dl 229,40,53,1024
mvaa 140,84,116
s_add_u32 s53,s53,s58
cv 198,172,173
cv 197,170,171
mvaa 136,80,116
ra 4,232,0
cv 196,168,169
mvaa 136,16,52
ta 92,239,12288
cv 195,166,167
mvaa 132,20,48
ta 94,239,12800
cv 194,164,165
mvaa 132,84,112
dl 230,44,54,2048
mvaa 128,80,112
ta 88,238,12288
cv 193,162,163
mvaa 128,16,48
ta 90,238,12800
cv 192,160,161
mvaa 124,20,44
cv 191,158,159
cv 190,156,157
mvaa 124,84,108
cv 189,154,155
cv 188,152,153
mvaa 120,80,108
dl 231,44,54,3072
mvaa 120,16,44
s_add_u32 m0,s98,32768
cv 187,150,151
cv 186,148,149
mvaa 116,20,40
s_add_u32 s54,s54,s58
cv 185,146,147
cv 184,144,145
mvaa 116,84,104
cv 183,142,143
cv 182,140,141
mvaa 112,80,104
cv 181,138,139
cv 180,136,137
mvaa 112,16,40
dl 224,36,52,0
mvaa 108,20,36
cv 179,134,135
cv 178,132,133
mvaa 108,84,100
cv 177,130,131
cv 176,128,129
mvaa 104,80,100
cv 175,126,127
cv 174,124,125
mvaa 104,16,36
ta 44,235,0
cv 173,122,123
mvaa 100,20,32
dl 225,36,52,1024
mvaa 100,84,96
ta 46,235,512
cv 172,120,121
mvaa 96,80,96
cv 171,118,119
cv 170,116,117
mvaa 96,16,32
ta 40,234,0
cv 169,114,115
mvaa 92,12,60
ta 42,234,512
cv 168,112,113
mvaa 92,76,124
dl 226,36,52,2048
mvaa 88,72,124
cv 167,110,111
cv 166,108,109
mvaa 88,8,60
cv 165,106,107
cv 164,104,105
mvaa 84,12,56
ta 84,237,8192
cv 163,102,103
mvaa 84,76,120
ta 80,236,8192
cv 162,100,101
mvaa 80,72,120
dl 227,36,52,3072
mvaa 80,8,56
s_add_u32 m0,s62,0
ta 86,237,8704
cv 161,98,99
mvaa 76,12,52
s_add_u32 s52,s52,s56
ta 82,236,8704
cv 160,96,97
mvaa 76,76,116
ra 16,232,6144
cv 159,94,95
mvaa 72,72,116
ta 60,235,4096
cv 158,92,93
mvaa 72,8,52
L_vs7:
s_cbranch_execz L_vs11
buffer_load_dword a243,v241,s[48:51],s55 offen
mvaa 68,12,48
s_add_u32 s55,s55,s59
ta 56,234,4096
cv 151,78,79
mvaa 68,76,112
ta 62,235,4608
cv 150,76,77
mvaa 64,72,112
ta 58,234,4608
cv 157,90,91
mvaa 64,8,48
ta 52,237,0
cv 156,88,89
mvaa 60,76,108
buffer_load_dword a244,v241,s[48:51],s55 offen
mvaa 56,72,108
s_add_u32 s55,s55,s59
ra 12,232,4096
cv 147,70,71
mvaa 52,76,104
ta 54,237,512
cv 146,68,69
mvaa 48,72,104
ra 8,232,2048
cv 145,66,67
mvaa 44,76,100
ta 48,236,0
cv 144,64,65
mvaa 40,72,100
buffer_load_dword a245,v241,s[48:51],s55 offen
mvaa 36,76,96
s_add_u32 s55,s55,s59
ta 50,236,512
cv 143,62,63
mvaa 32,72,96
cv 142,60,61
cv 141,58,59
mvaa 28,68,124
cv 140,56,57
cv 139,54,55
mvaa 24,64,124
cv 138,52,53
cv 137,50,51
mvaa 20,68,120
s_mov_b64 exec,65537
buffer_store_dword v246,v250,s[8:11],s86 offen sc0 sc1
s_mov_b64 exec,-1
mvaa 16,64,120
cv 136,48,49
cv 135,46,47
mvaa 12,68,116
cv 134,44,45
cv 133,42,43
mvaa 8,64,116
ta 76,235,8192
cv 132,40,41
mvaa 4,68,112
ta 78,235,8704
cv 131,38,39
mvaa 0,64,112
ta 72,234,8192
cv 130,36,37
maaa 156,68,108
cv 129,34,35
cv 128,32,33
maaa 152,64,108
ta 74,234,8704
cv 127,30,31
maaa 148,68,104
cv 126,28,29
cv 125,26,27
maaa 144,64,104
v_accvgpr_read_b32 v63,a159
v_accvgpr_read_b32 v62,a158
maaa 140,68,100
v_accvgpr_read_b32 v61,a157
v_accvgpr_read_b32 v60,a156
maaa 136,64,100
v_accvgpr_read_b32 v59,a155
v_accvgpr_read_b32 v58,a154
maaa 132,68,96
v_accvgpr_read_b32 v57,a153
v_accvgpr_read_b32 v56,a152
maaa 128,64,96
cv 56,56,57
cv 57,58,59
s_waitcnt lgkmcnt(3)
zaaa 236,40,4
ta 68,237,4096
ta 70,237,4608
zaaa 220,44,4
ta 64,236,4096
ta 66,236,4608
zaaa 216,44,8
cv 58,60,61
cv 59,62,63
zaaa 232,40,8
buffer_store_dwordx4 v[56:59],v244,s[24:27],0 offen offset:3072 sc0 sc1
zaaa 228,40,12
v_accvgpr_read_b32 v55,a151
v_accvgpr_read_b32 v54,a150
zaaa 212,44,12
v_accvgpr_read_b32 v53,a149
v_accvgpr_read_b32 v52,a148
zaaa 208,44,16
v_accvgpr_read_b32 v51,a147
v_accvgpr_read_b32 v50,a146
zaaa 224,40,16
v_accvgpr_read_b32 v49,a145
v_accvgpr_read_b32 v48,a144
zaaa 192,48,16
cv 48,48,49
cv 49,50,51
zaaa 176,52,16
cv 50,52,53
cv 51,54,55
zaaa 180,52,12
buffer_store_dwordx4 v[48:51],v244,s[24:27],0 offen offset:2048 sc0 sc1
zaaa 196,48,12
s_waitcnt vmcnt(27) lgkmcnt(0)
s_barrier
ra 24,233,0
v_accvgpr_read_b32 v47,a143
zaaa 200,48,8
ra 28,233,2048
v_accvgpr_read_b32 v46,a142
v_accvgpr_read_b32 v45,a141
zaaa 184,52,8
ra 32,233,4096
ra 36,233,6144
zaaa 188,52,4
v_accvgpr_read_b32 v43,a139
v_accvgpr_read_b32 v42,a138
zaaa 204,48,4
dl 228,40,53,0
ta 96,236,24576
zaaa 172,56,4
ta 98,236,25088
v_accvgpr_read_b32 v41,a137
zaaa 156,60,4
ta 100,237,24576
v_accvgpr_read_b32 v39,a135
zaaa 152,60,8
ta 102,237,25088
ta 104,238,28672
zaaa 168,56,8
v_accvgpr_read_b32 v38,a134
v_accvgpr_read_b32 v35,a131
zaaa 164,56,12
dl 229,40,53,1024
ta 106,238,29184
zaaa 148,60,12
s_add_u32 s53,s53,s57
ta 108,239,28672
v_accvgpr_read_b32 v34,a130
zaaa 144,60,16
ta 110,239,29184
v_accvgpr_read_b32 v33,a129
zaaa 160,56,16
ta 44,235,16384
ta 46,235,16896
ta 40,234,16384
ta 42,234,16896
ta 52,237,16384
ta 54,237,16896
ta 48,236,16384
ta 50,236,16896
ta 60,235,20480
ta 62,235,20992
ta 56,234,20480
ta 58,234,20992
v_accvgpr_read_b32 v32,a128
zaaa 128,64,16
v_accvgpr_read_b32 v37,a133
v_accvgpr_read_b32 v36,a132
zaaa 112,68,16
dl 230,44,54,2048
zaaa 116,68,12
v_accvgpr_read_b32 v40,a136
v_accvgpr_read_b32 v44,a140
zaaa 132,64,12
cv 251,42,43
cv 250,40,41
zaaa 136,64,8
cv 253,46,47
cv 252,44,45
zaaa 120,68,8
cv 249,38,39
cv 247,34,35
zaaa 124,68,4
dl 231,44,54,3072
zaaa 140,64,4
s_add_u32 m0,s61,4096
cv 246,32,33
cv 248,36,37
zvaa 40,72,4
s_add_u32 s54,s54,s57
ta 68,237,20480
cv 124,24,25
zvaa 44,76,4
s_add_u32 s61,s63,s98
ta 70,237,20992
cv 123,22,23
zvaa 52,76,8
s_add_u32 s63,s63,s99
ta 64,236,20480
cv 121,18,19
zvaa 48,72,8
dl 224,36,52,0
zvaa 56,72,12
ta 66,236,20992
cv 120,16,17
zvaa 60,76,12
cv 122,20,21
cv 119,14,15
zvaa 68,76,16
cv 118,12,13
cv 117,10,11
zvaa 64,72,16
cv 116,8,9
cv 153,82,83
zvaa 32,80,16
dl 225,36,52,1024
ta 76,235,24576
ta 78,235,25088
zvaa 36,84,16
ta 72,234,24576
ta 74,234,25088
zvaa 28,84,12
cv 152,80,81
cv 155,86,87
zvaa 24,80,12
cv 154,84,85
cv 149,74,75
zvaa 16,80,8
cv 148,72,73
v_add_u32_e32 v232,s99,v232
zvaa 20,84,8
dl 226,36,52,2048
zvaa 12,84,4
cv 115,6,7
cv 114,4,5
zvaa 8,80,4
cv 113,2,3
cv 112,0,1
zvaa 76,4,88
zvaa 96,4,92
zvaa 100,8,92
dl 227,36,52,3072
zvaa 80,8,88
s_add_u32 s52,s52,s60
zvaa 84,12,88
zvaa 104,12,92
zvaa 108,16,92
s_waitcnt vmcnt(23) lgkmcnt(0)
s_barrier
zvaa 88,16,88
buffer_store_dwordx4 v[250:253],v244,s[24:27],0 offen offset:1024 sc0 sc1
buffer_store_dwordx4 v[246:249],v244,s[24:27],0 offen offset:0 sc0 sc1
L_kvx:
L_vs8:
s_cbranch_execz L_vs12
L_p_exit:
s_add_u32 m0,s62,16384
ra 4,232,0
s_lshl_b32 s86,s21,2
maaa 236,40,24
dl 228,40,53,0
maaa 220,44,24
s_add_u32 s86,s86,s12
ra 8,232,2048
s_lshl_b32 s86,s86,8
maaa 216,44,28
s_add_u32 s86,s86,L_p_exit-L_att_pc+2048
ra 12,232,4096
v_lshl_add_u32 v253,v244,3,s86
maaa 232,40,28
s_mov_b32 s67,4096
ra 16,232,6144
v_add_u32_e32 v233,0xffff0000,v233
maaa 228,40,32
s_cmp_lt_u32 s12,2
s_cselect_b32 s94,s22,s23
ra 80,233,0
v_mov_b32_e32 v248,-1
maaa 212,44,32
dl 229,40,53,1024
maaa 208,44,36
s_lshr_b32 s94,s94,6
ra 84,233,2048
v_mov_b32_e32 v249,-1
maaa 224,40,36
s_and_b32 s87,s12,1
ra 88,233,4096
v_mov_b32_e32 v250,-1
maaa 192,48,36
s_add_u32 s94,s94,s87
ra 92,233,6144
v_mov_b32_e32 v251,-1
maaa 176,52,36
s_lshl_b32 s87,s18,5
ta 0,234,32768
s_add_u32 s94,s94,s87
maaa 180,52,32
dl 230,44,54,2048
maaa 196,48,32
s_lshl_b32 s94,s94,11
ta 2,234,33280
s_add_u32 s94,s94,12582912
maaa 200,48,28
s_add_u32 s53,s53,s58
ta 40,235,32768
s_bfe_u32 s88,s21,65539
maaa 184,52,28
s_lshl_b32 s87,s20,1
ta 42,235,33280
s_add_u32 s88,s88,s87
maaa 188,52,24
s_lshl_b32 s88,s88,7
v_mov_b32_e32 v247,1
s_add_u32 s85,s88,83894272
maaa 204,48,24
dl 231,44,54,3072
maaa 172,56,24
s_add_u32 m0,s98,65536
ta 52,236,32768
s_add_u32 s54,s54,s58
maaa 156,60,24
s_lshl_b32 s91,s12,4
ta 54,236,33280
v_bfe_u32 v252,v244,4,4
maaa 152,60,28
s_cmp_lt_u32 s12,2
s_cselect_b32 s90,s22,s23
v_bfe_u32 v241,v244,6,4
v_and_b32_e32 v241,12,v241
maaa 168,56,28
s_mul_i32 s33,s90,10240
v_sub_u32_e32 v252,v252,v241
v_add_u32_e32 v252,s91,v252
maaa 164,56,32
s_mov_b64 exec,3
global_load_dword a250,v253,s[96:97]
s_mov_b64 exec,-1
maaa 148,60,32
s_cmp_lt_u32 s12,2
s_cselect_b32 s91,32768,0
v_add_u32_e32 v253,10240,v253
s_add_u32 s91,s91,98304
maaa 144,60,36
s_mov_b32 s64,0x55505555
v_accvgpr_read_b32 v255,a241
v_mov_b32_e32 v241,0xf149f2ca
maaa 160,56,36
s_mov_b32 s65,0x50005500
s_lshl_b32 s95,s12,12
s_mov_b32 s66,0x50000
maaa 128,64,36
s_add_u32 s70,s95,32768
s_add_u32 s72,s95,65536
s_lshl_b32 s4,s21,2
maaa 112,68,36
dl 224,36,52,0
maaa 116,68,32
s_add_u32 s4,s4,s12
ta 56,237,32768
s_lshl_b32 s4,s4,8
maaa 132,64,32
s_bitcmp1_b32 170,s20
s_cselect_b32 s5,L_odd-L_last,0
ta 58,237,33280
s_add_u32 s4,s4,s5
maaa 136,64,28
s_add_u32 s4,s4,L_last-L_att_pc
s_mov_b64 s[30:31],s[28:29]
s_mov_b64 s[28:29],s[78:79]
maaa 120,68,28
s_add_u32 s68,s95,0
v_cvt_f32_i32_e32 v252,v252
v_mul_f32_e32 v252,0x7149f2ca,v252
maaa 124,68,24
dl 225,36,52,1024
maaa 140,64,24
s_add_u32 s69,s95,16384
s_add_u32 s74,s95,98304
s_add_u32 s71,s95,49152
mvaa 40,72,24
s_waitcnt vmcnt(17) lgkmcnt(2)
s_barrier
mvaa 44,76,24
s_add_u32 s73,s95,81920
ta 20,234,49152
v_and_b32_e32 v245,48,v244
mvaa 52,76,28
s_lshl_b32 s59,%1,2
ta 22,234,49664
v_lshlrev_b32_e32 v245,3,v245
mvaa 48,72,28
s_lshl_b32 s84,%1,2
ta 44,235,49152
s_add_u32 s59,s59,s12
mvaa 56,72,32
dl 226,36,52,2048
mvaa 60,76,32
s_add_u32 s75,s95,114688
ta 46,235,49664
s_add_u32 s84,s84,s12
mvaa 68,76,36
s_mul_i32 s84,s84,12288
ta 48,236,49152
s_sub_u32 s55,s14,s13
mvaa 64,72,36
s_mov_b32 s48,0x38000000
ta 50,236,49664
s_mov_b32 s81,1
mvaa 32,96,36
s_mov_b32 s84,0
ta 60,237,49152
mvaa 36,100,36
dl 227,36,52,3072
mvaa 28,100,32
s_add_u32 s52,s52,s56
s_add_u32 m0,s98,69632
ta 62,237,49664
mvaa 24,96,32
ta 68,234,36864
mvaa 16,96,28
ta 70,234,37376
)"
R"(mvaa 20,100,28
ta 64,234,53248
mvaa 12,100,24
dl 224,36,52,0
mvaa 8,96,24
ta 66,234,53760
mvaa 76,24,104
ta 76,235,53248
mvaa 96,24,108
ta 78,235,53760
mvaa 100,28,108
ta 72,235,36864
mvaa 80,28,104
dl 225,36,52,1024
mvaa 84,32,104
ta 74,235,37376
mvaa 104,32,108
mvaa 108,36,108
mvaa 88,36,104
maaa 236,0,4
s_waitcnt lgkmcnt(5)
maaa 236,20,80
dl 226,36,52,2048
maaa 220,44,80
maaa 220,40,4
maaa 216,40,8
maaa 216,44,84
maaa 232,20,84
dl 227,36,52,3072
maaa 232,0,8
s_add_u32 s52,s52,s60
s_add_u32 m0,s98,0
maaa 228,0,12
maaa 228,20,88
maaa 212,44,88
maaa 212,40,12
dl 224,36,52,0
maaa 208,40,16
maaa 208,44,92
maaa 224,20,92
ta 40,236,36864
maaa 224,0,16
ta 42,236,37376
maaa 192,52,16
dl 225,36,52,1024
maaa 192,48,92
ta 44,236,53248
maaa 176,60,92
ta 46,236,53760
maaa 176,56,16
ta 20,237,53248
maaa 180,56,12
ta 22,237,53760
maaa 180,60,88
dl 226,36,52,2048
maaa 196,48,88
ta 0,237,36864
maaa 196,52,12
ta 2,237,37376
maaa 200,52,8
s_waitcnt vmcnt(18)
ra 24,232,32768
maaa 200,48,84
ra 28,232,34816
maaa 184,60,84
ra 32,232,36864
maaa 184,56,8
buffer_store_dwordx4 v[112:115],v244,s[24:27],s67 offen offset:0 sc0 sc1
maaa 188,56,4
ra 36,232,38912
v_add_u32_e32 v232,0x10000,v232
maaa 188,60,80
ra 96,233,32768
v_cndmask_b32_e64 v112,v241,0,s[64:65]
maaa 204,48,80
s_mov_b32 s64,0xaaa0aaaa
ra 100,233,34816
s_mov_b32 s65,0xa000aa00
maaa 204,52,4
ra 104,233,36864
v_cndmask_b32_e64 v113,v241,0,s[64:65]
maaa 172,68,4
dl 227,36,52,3072
s_waitcnt lgkmcnt(8)
maaa 172,64,80
s_add_u32 s52,s52,s56
s_add_u32 m0,s98,4096
ra 108,233,38912
maaa 156,76,80
s_mov_b32 s64,0x55405554
ta 56,234,40960
v_add_u32_e32 v233,0x10000,v233
maaa 156,72,4
s_mov_b32 s65,0x40005400
ta 58,234,41472
v_cndmask_b32_e64 v114,v241,0,s[64:65]
maaa 152,72,8
s_mov_b32 s64,0xaa80aaa8
ta 60,234,57344
s_mov_b32 s65,0x8000a800
maaa 152,76,84
dl 224,36,52,0
maaa 168,64,84
ta 62,234,57856
v_cndmask_b32_e64 v115,v241,0,s[64:65]
maaa 168,68,8
ta 48,235,57344
maaa 164,68,12
ta 50,235,57856
maaa 164,64,88
ta 52,235,40960
maaa 148,76,88
buffer_store_dwordx4 v[248:251],v244,s[8:11],s94 offen offset:0 sc0 sc1
maaa 148,72,12
ta 54,235,41472
maaa 144,72,16
maaa 144,76,92
maaa 160,64,92
ta 72,236,40960
maaa 160,68,16
L_vs9:
s_cbranch_execz L_vs13
ta 74,236,41472
maaa 128,40,16
dl 225,36,52,1024
maaa 128,44,92
ta 76,236,57344
maaa 112,20,92
ta 78,236,57856
s_waitcnt lgkmcnt(6)
maaa 112,0,16
ta 64,237,57344
maaa 116,0,12
ta 66,237,57856
maaa 116,20,88
dl 226,36,52,2048
maaa 132,44,88
ta 68,237,40960
maaa 132,40,12
ta 70,237,41472
maaa 136,40,8
maaa 136,44,84
maaa 120,20,84
dl 227,36,52,3072
maaa 120,0,8
s_add_u32 s52,s52,s60
maaa 124,0,4
maaa 124,20,80
maaa 140,44,80
ta 0,238,45056
ta 2,238,45568
maaa 140,40,4
buffer_store_dwordx4 v[248:251],v244,s[8:11],s94 offen offset:1024 sc0 sc1
ta 20,238,61440
ta 22,238,61952
mvaa 40,56,4
ta 44,239,61440
ta 46,239,61952
mvaa 40,60,80
ta 40,239,45056
ta 42,239,45568
mvaa 44,48,80
v_and_b32_e32 v250,240,v244
v_accvgpr_read_b32 v249,a240
s_waitcnt lgkmcnt(8)
mvaa 44,52,4
mvaa 52,52,8
mvaa 52,48,84
mvaa 48,60,84
mvaa 48,56,8
mvaa 56,56,12
mvaa 56,60,88
mvaa 60,48,88
mvaa 60,52,12
mvaa 68,52,16
mvaa 68,48,92
mvaa 64,60,92
mvaa 64,56,16
mvaa 32,72,16
s_waitcnt vmcnt(11) lgkmcnt(0)
s_barrier
s_add_u32 m0,s62,32768
mvaa 32,76,92
dl 228,40,53,0
mvaa 36,64,92
ta 52,234,0
mvaa 36,68,16
ta 54,234,512
mvaa 28,68,12
ta 48,234,16384
mvaa 28,64,88
ta 50,234,16896
mvaa 24,76,88
ta 60,235,16384
mvaa 24,72,12
dl 229,40,53,1024
mvaa 16,72,8
s_add_u32 s53,s53,s57
ta 62,235,16896
mvaa 16,76,84
ta 56,235,0
mvaa 20,64,84
ta 58,235,512
mvaa 20,68,8
mvaa 12,68,4
dl 230,44,54,2048
mvaa 12,64,80
mvaa 8,76,80
ta 68,236,0
mvaa 8,72,4
ta 70,236,512
mvaa 76,4,0
ta 64,236,16384
mvaa 76,80,20
dl 231,44,54,3072
mvaa 96,80,44
s_add_u32 m0,s62,49152
s_add_u32 s54,s54,s57
ta 66,236,16896
mvaa 96,4,40
ta 76,237,16384
mvaa 100,8,40
ta 78,237,16896
mvaa 100,84,44
ta 72,237,0
mvaa 80,84,20
ta 74,237,512
mvaa 80,8,0
dl 228,40,53,0
mvaa 84,12,0
ra 80,233,0
mvaa 84,88,20
ra 4,232,0
mvaa 104,88,44
ra 84,233,2048
mvaa 104,12,40
ra 8,232,2048
mvaa 108,16,40
dl 229,40,53,1024
mvaa 108,92,44
s_add_u32 s53,s53,s58
ra 88,233,4096
mvaa 88,92,20
ta 40,234,4096
mvaa 88,16,0
ta 42,234,4608
s_waitcnt lgkmcnt(8)
maaa 236,52,24
ta 44,234,20480
maaa 236,48,96
dl 230,44,54,2048
maaa 220,60,96
ta 46,234,20992
maaa 220,56,24
ta 20,235,20480
maaa 216,56,28
ta 22,235,20992
maaa 216,60,100
ta 0,235,4096
maaa 232,48,100
dl 231,44,54,3072
maaa 232,52,28
s_add_u32 m0,s98,32768
ta 2,235,4608
s_add_u32 s54,s54,s58
maaa 228,52,32
ra 12,232,4096
maaa 228,48,104
ra 16,232,6144
v_add_u32_e32 v232,0xffff0000,v232
maaa 212,60,104
ra 92,233,6144
v_add_u32_e32 v233,0xffff0000,v233
maaa 212,56,32
maaa 208,56,36
dl 224,36,52,0
maaa 208,60,108
maaa 224,48,108
ta 56,236,4096
maaa 224,52,36
ta 58,236,4608
maaa 192,68,36
ta 60,236,20480
maaa 192,64,108
dl 225,36,52,1024
maaa 176,76,108
ta 62,236,20992
s_waitcnt lgkmcnt(6)
maaa 176,72,36
ta 48,237,20480
maaa 180,72,32
ta 50,237,20992
maaa 180,76,104
ta 52,237,4096
maaa 196,64,104
buffer_store_dwordx4 v[116:119],v244,s[24:27],s67 offen offset:1024 sc0 sc1
maaa 196,68,32
ta 54,237,4608
maaa 200,68,28
maaa 200,64,100
maaa 184,76,100
maaa 184,72,28
dl 226,36,52,2048
maaa 188,72,24
maaa 188,76,96
maaa 204,64,96
ta 72,234,8192
maaa 204,68,24
ta 74,234,8704
maaa 172,40,24
ta 76,234,24576
maaa 172,44,96
dl 227,36,52,3072
maaa 156,20,96
s_add_u32 s52,s52,s56
s_add_u32 m0,s98,36864
ta 78,234,25088
maaa 156,0,24
ta 64,235,24576
maaa 152,0,28
ta 66,235,25088
maaa 152,20,100
ta 68,235,8192
maaa 168,44,100
dl 224,36,52,0
maaa 168,40,28
ta 70,235,8704
maaa 164,40,32
maaa 164,44,104
maaa 148,20,104
maaa 148,0,32
buffer_store_dwordx4 v[120:123],v244,s[24:27],s67 offen offset:2048 sc0 sc1
maaa 144,0,36
maaa 144,20,108
maaa 160,44,108
ta 0,236,8192
maaa 160,40,36
ta 2,236,8704
s_waitcnt lgkmcnt(3)
maaa 128,56,36
dl 225,36,52,1024
maaa 128,60,108
ta 20,236,24576
maaa 112,48,108
ta 22,236,25088
maaa 112,52,36
ta 44,237,24576
maaa 116,52,32
ta 46,237,25088
maaa 116,48,104
dl 226,36,52,2048
maaa 132,60,104
ta 40,237,8192
maaa 132,56,32
ta 42,237,8704
maaa 136,56,28
maaa 136,60,100
maaa 120,48,100
maaa 120,52,28
buffer_load_dword v246,off,s[8:11],s35 offset:1024 sc1
maaa 124,52,24
maaa 124,48,96
maaa 140,60,96
ta 52,238,12288
ta 54,238,12800
maaa 140,56,24
ta 48,238,28672
ta 50,238,29184
mvaa 40,72,24
buffer_store_dwordx4 v[124:127],v244,s[24:27],s67 offen offset:3072 sc0 sc1
ta 60,239,28672
mvaa 40,76,96
s_mov_b32 s67,8192
ta 62,239,29184
mvaa 44,64,96
ta 56,239,12288
ta 58,239,12800
s_waitcnt lgkmcnt(8)
mvaa 44,68,24
mvaa 52,68,28
mvaa 52,64,100
dl 227,36,52,3072
mvaa 48,76,100
s_add_u32 s52,s52,s60
mvaa 48,72,28
mvaa 56,72,32
mvaa 56,76,104
mvaa 60,64,104
mvaa 60,68,32
mvaa 68,68,36
mvaa 68,64,108
mvaa 64,76,108
mvaa 64,72,36
mvaa 32,0,36
s_waitcnt vmcnt(12) lgkmcnt(0)
s_barrier
s_add_u32 m0,s62,0
mvaa 32,20,108
dl 228,40,53,0
mvaa 36,44,108
ta 68,234,32768
mvaa 36,40,36
ta 70,234,33280
mvaa 28,40,32
ta 64,234,49152
mvaa 28,44,104
ta 66,234,49664
mvaa 24,20,104
ta 76,235,49152
mvaa 24,0,32
dl 229,40,53,1024
mvaa 16,0,28
s_add_u32 s53,s53,s57
ta 78,235,49664
mvaa 16,20,100
ta 72,235,32768
mvaa 20,44,100
ta 74,235,33280
mvaa 20,40,28
mvaa 12,40,24
mvaa 12,44,96
dl 230,44,54,2048
mvaa 8,20,96
ta 40,236,32768
mvaa 8,0,24
ta 42,236,33280
mvaa 76,24,52
ta 44,236,49152
mvaa 76,96,48
ta 46,236,49664
mvaa 96,96,60
dl 231,44,54,3072
mvaa 96,24,56
s_add_u32 m0,s62,16384
s_add_u32 s54,s54,s57
ta 20,237,49152
mvaa 100,28,56
ta 22,237,49664
mvaa 100,100,60
ta 0,237,32768
mvaa 80,100,48
ta 2,237,33280
mvaa 80,28,52
dl 228,40,53,0
mvaa 84,32,52
ra 96,233,0
mvaa 84,104,48
ra 24,232,0
mvaa 104,104,60
ra 100,233,2048
mvaa 104,32,56
ra 28,232,2048
mvaa 108,36,56
dl 229,40,53,1024
mvaa 108,108,60
s_add_u32 s53,s53,s58
ra 104,233,4096
mvaa 88,108,48
ta 56,234,36864
mvaa 88,36,52
ta 58,234,37376
s_waitcnt lgkmcnt(8)
maaa 236,68,4
ta 60,234,53248
maaa 236,64,80
ta 62,234,53760
maaa 220,76,80
dl 230,44,54,2048
maaa 220,72,4
ta 48,235,53248
maaa 216,72,8
ta 50,235,53760
maaa 216,76,84
ta 52,235,36864
maaa 232,64,84
ta 54,235,37376
maaa 232,68,8
dl 231,44,54,3072
maaa 228,68,12
s_add_u32 m0,s98,65536
ra 32,232,4096
s_add_u32 s54,s54,s58
maaa 228,64,88
ra 36,232,6144
maaa 212,76,88
ra 108,233,6144
maaa 212,72,12
s_waitcnt vmcnt(10)
v_readfirstlane_b32 s87,v246
maaa 208,72,16
dl 224,36,52,0
maaa 208,76,92
s_cmp_eq_u32 s12,0
s_cselect_b32 s89,s32,s87
s_cmp_lg_u32 s87,s89
s_cbranch_scc1 L_q_ack
L_q_acked:
maaa 224,64,92
s_and_b32 s87,s12,1
ta 72,236,36864
s_lshl_b32 s88,s87,6
maaa 224,68,16
s_add_u32 s90,s90,s88
ta 74,236,37376
s_lshl_b32 s88,s87,10
maaa 192,40,16
s_lshl_b32 s87,s87,14
ta 76,236,53248
s_add_u32 s91,s91,s87
maaa 192,44,92
dl 225,36,52,1024
maaa 176,20,92
s_lshr_b32 s87,s18,1
ta 78,236,53760
s_lshl_b32 s87,s87,4
s_waitcnt lgkmcnt(5)
maaa 176,0,16
s_add_u32 s33,s33,s88
ta 64,237,53248
v_cmp_eq_u32_e64 s[100:101],s87,v250
maaa 180,0,12
s_lshl_b32 s88,s18,11
ta 66,237,53760
v_lshlrev_b32_e32 v250,3,v250
maaa 180,20,88
s_add_u32 s88,s33,s88
ta 68,237,36864
v_add_u32_e32 v250,83894272,v250
maaa 196,44,88
s_add_u32 s33,s88,63176704
ta 70,237,37376
v_add_u32_e32 v251,s91,v244
maaa 196,40,12
buffer_store_dwordx4 v[128:131],v244,s[24:27],s67 offen offset:0 sc0 sc1
maaa 200,40,8
s_add_u32 s34,s88,63504384
v_cndmask_b32_e64 v242,31,-1,s[100:101]
v_and_b32_e32 v243,528,v242
maaa 200,44,84
s_add_u32 s50,s88,63832064
s_add_u32 s51,s88,64159744
s_lshr_b32 s91,s21,3
maaa 184,20,84
s_and_b32 s88,s20,4
s_and_b32 s89,s20,3
s_add_u32 s91,s91,s88
maaa 184,0,8
s_lshl_b32 s88,s89,6
s_lshl_b32 s86,s21,2
s_add_u32 s86,s86,s12
maaa 188,0,4
dl 226,36,52,2048
maaa 188,20,80
s_cmp_eq_u32 s90,0
s_cselect_b64 s[64:65],-1,0
s_and_b32 s89,s86,1
ta 0,234,40960
maaa 204,44,80
s_lshl_b32 s89,s89,9
ta 2,234,41472
s_lshr_b32 s49,s86,3
maaa 204,40,4
s_lshl_b32 s49,s49,11
ta 20,234,57344
v_add_f32_e32 v128,0x70c9f2ca,v252
maaa 172,56,4
ta 22,234,57856
v_add_f32_e32 v129,0xf0c9f2ca,v252
maaa 172,60,80
dl 227,36,52,3072
maaa 156,48,80
s_add_u32 s52,s52,s56
s_add_u32 m0,s98,69632
ta 44,235,57344
maaa 156,52,4
ta 46,235,57856
v_add_f32_e32 v130,0xf1977617,v252
maaa 152,52,8
ta 40,235,40960
v_add_f32_e32 v131,0xf1fc6f7c,v252
maaa 152,48,84
ta 42,235,41472
v_min_f32_e32 v128,0xc1a00000,v128
maaa 168,60,84
dl 224,36,52,0
maaa 168,56,8
v_min_f32_e32 v129,0xc1a00000,v129
v_min_f32_e32 v130,0xc1a00000,v130
maaa 164,56,12
v_min_f32_e32 v131,0xc1a00000,v131
v_lshrrev_b32_e32 v246,6,v244
maaa 164,60,88
v_lshl_add_u32 v245,v246,22,v245
v_lshlrev_b32_e32 v246,16,v244
maaa 148,48,88
maaa 148,52,12
maaa 144,52,16
buffer_store_dwordx4 v[132:135],v244,s[24:27],s67 offen offset:1024 sc0 sc1
maaa 144,48,92
maaa 160,60,92
ta 52,236,40960
v_add_f32_e32 v132,0xf343a333,v252
maaa 160,56,16
ta 54,236,41472
v_add_f32_e32 v133,0xf3504260,v252
s_waitcnt lgkmcnt(2)
maaa 128,72,16
ta 48,236,57344
v_add_f32_e32 v134,0xf35ce18d,v252
maaa 128,76,92
dl 225,36,52,1024
maaa 112,64,92
ta 50,236,57856
v_min_f32_e32 v132,0xc1a00000,v132
maaa 112,68,16
ta 60,237,57344
v_add_f32_e32 v135,0xf36980b9,v252
maaa 116,68,12
ta 62,237,57856
v_min_f32_e32 v133,0xc1a00000,v133
maaa 116,64,88
ta 56,237,40960
v_min_f32_e32 v134,0xc1a00000,v134
maaa 132,76,88
dl 226,36,52,2048
maaa 132,72,12
ta 58,237,41472
v_min_f32_e32 v135,0xc1a00000,v135
maaa 136,72,8
maaa 136,76,84
maaa 120,64,84
maaa 120,68,8
dl 227,36,52,3072
maaa 124,68,4
s_add_u32 s52,s52,s60
maaa 124,64,80
maaa 140,76,80
ta 68,238,45056
ta 70,238,45568
maaa 140,72,4
ta 64,238,61440
ta 66,238,61952
mvaa 40,0,4
ta 76,239,61440
mvaa 40,20,80
buffer_store_dwordx4 v[136:139],v244,s[24:27],s67 offen offset:2048 sc0 sc1
ta 78,239,61952
mvaa 44,44,80
ta 72,239,45056
ta 74,239,45568
mvaa 44,40,4
v_add_f32_e32 v136,0xf3c6caff,v252
v_add_f32_e32 v137,0xf3cd1a95,v252
mvaa 52,40,8
v_add_f32_e32 v138,0xf3d36a2b,v252
v_add_f32_e32 v139,0xf3d9b9c2,v252
mvaa 52,44,84
v_min_f32_e32 v136,0xc1a00000,v136
v_min_f32_e32 v137,0xc1a00000,v137
mvaa 48,20,84
v_min_f32_e32 v138,0xc1a00000,v138
v_min_f32_e32 v139,0xc1a00000,v139
mvaa 48,0,8
mvaa 56,0,12
mvaa 56,20,88
L_vs10:
s_cbranch_execz L_vs14
mvaa 60,44,88
mvaa 60,40,12
mvaa 68,40,16
mvaa 68,44,92
mvaa 64,20,92
mvaa 64,0,16
s_waitcnt lgkmcnt(0)
mvaa 32,52,16
s_waitcnt vmcnt(11)
s_barrier
mvaa 32,48,92
s_cmp_eq_u32 s12,0
s_cselect_b64 exec,1,0
buffer_atomic_add v247,off,s[8:11],s85 sc1
s_mov_b64 exec,-1
mvaa 36,60,92
s_add_u32 m0,s62,32768
ta 40,234,0
v_mov_b32_e32 v247,32
mvaa 36,56,16
ta 42,234,512
mvaa 28,56,12
ta 44,234,16384
mvaa 28,60,88
ta 46,234,16896
mvaa 24,48,88
dl 228,40,53,0
mvaa 24,52,12
ta 20,235,16384
mvaa 16,52,8
ta 22,235,16896
mvaa 16,48,84
ta 0,235,0
mvaa 20,60,84
ta 2,235,512
mvaa 20,56,8
dl 229,40,53,1024
mvaa 12,56,4
s_add_u32 s53,s53,s57
mvaa 12,60,80
mvaa 8,48,80
ta 56,236,0
mvaa 8,52,4
ta 58,236,512
mvaa 76,4,68
dl 230,44,54,2048
mvaa 76,80,64
ta 60,236,16384
mvaa 96,80,76
ta 62,236,16896
mvaa 96,4,72
ta 48,237,16384
mvaa 100,8,72
ta 50,237,16896
mvaa 100,84,76
dl 231,44,54,3072
mvaa 80,84,64
s_add_u32 m0,s62,49152
s_add_u32 s54,s54,s57
ta 52,237,0
mvaa 80,8,68
ta 54,237,512
mvaa 84,12,68
ra 80,233,32768
mvaa 84,88,64
ra 4,232,32768
mvaa 104,88,76
dl 228,40,53,0
mvaa 104,12,72
ra 84,233,34816
mvaa 108,16,72
ra 8,232,34816
mvaa 108,92,76
ra 12,232,36864
mvaa 88,92,64
ta 72,234,4096
mvaa 88,16,68
ta 74,234,4608
s_waitcnt lgkmcnt(9)
maaa 236,40,24
dl 229,40,53,1024
maaa 236,44,96
s_add_u32 s53,s53,s58
ta 76,234,20480
maaa 220,20,96
ta 78,234,20992
maaa 220,0,24
ta 64,235,20480
maaa 216,0,28
ta 66,235,20992
maaa 216,20,100
dl 230,44,54,2048
maaa 232,44,100
ta 68,235,4096
maaa 232,40,28
ta 70,235,4608
maaa 228,40,32
ra 88,233,36864
maaa 228,44,104
ra 92,233,38912
v_add_u32_e32 v233,0x10000,v233
maaa 212,20,104
dl 231,44,54,3072
maaa 212,0,32
s_add_u32 m0,s98,0
ra 16,232,38912
s_add_u32 s54,s54,s58
maaa 208,0,36
v_add_u32_e32 v232,0x10000,v232
maaa 208,20,108
maaa 224,44,108
ta 0,236,4096
maaa 224,40,36
dl 224,36,52,0
maaa 192,56,36
ta 2,236,4608
maaa 192,60,108
ta 20,236,20480
maaa 176,48,108
ta 22,236,20992
s_waitcnt lgkmcnt(6)
maaa 176,52,36
ta 44,237,20480
maaa 180,52,32
ta 46,237,20992
maaa 180,48,104
dl 225,36,52,1024
maaa 196,60,104
ta 40,237,4096
maaa 196,56,32
ta 42,237,4608
maaa 200,56,28
maaa 200,60,100
maaa 184,48,100
buffer_store_dwordx4 v[140:143],v244,s[24:27],s67 offen offset:3072 sc0 sc1
maaa 184,52,28
s_mov_b32 s67,12288
v_add_f32_e32 v140,0xf415e232,v252
v_add_f32_e32 v141,0xf41909fd,v252
maaa 188,52,24
v_add_f32_e32 v142,0xf41c31c8,v252
v_min_f32_e32 v140,0xc1a00000,v140
maaa 188,48,96
v_add_f32_e32 v143,0xf41f5993,v252
v_bfe_i32 v252,v244,4,1
maaa 204,60,96
ta 52,234,8192
v_min_f32_e32 v141,0xc1a00000,v141
maaa 204,56,24
dl 226,36,52,2048
maaa 172,72,24
ta 54,234,8704
v_min_f32_e32 v142,0xc1a00000,v142
maaa 172,76,96
ta 48,234,24576
v_min_f32_e32 v143,0xc1a00000,v143
maaa 156,64,96
ta 50,234,25088
v_xor_b32_e32 v252,65535,v252
maaa 156,68,24
ta 60,235,24576
maaa 152,68,28
dl 227,36,52,3072
maaa 152,64,100
s_add_u32 s52,s52,s56
s_add_u32 m0,s98,4096
ta 62,235,25088
maaa 168,76,100
ta 56,235,8192
maaa 168,72,28
ta 58,235,8704
maaa 164,72,32
maaa 164,76,104
maaa 148,64,104
dl 224,36,52,0
maaa 148,68,32
maaa 144,68,36
maaa 144,64,108
maaa 160,76,108
ta 68,236,8192
maaa 160,72,36
buffer_store_dwordx4 v[144:147],v244,s[24:27],s67 offen offset:0 sc0 sc1
s_waitcnt lgkmcnt(3)
maaa 128,0,36
ta 70,236,8704
maaa 128,20,108
ta 64,236,24576
maaa 112,44,108
ta 66,236,25088
maaa 112,40,36
ta 76,237,24576
maaa 116,40,32
dl 225,36,52,1024
maaa 116,44,104
ta 78,237,25088
maaa 132,20,104
ta 72,237,8192
maaa 132,0,32
ta 74,237,8704
maaa 136,0,28
maaa 136,20,100
dl 226,36,52,2048
maaa 120,44,100
maaa 120,40,28
maaa 124,40,24
maaa 124,44,96
maaa 140,20,96
buffer_store_dwordx4 v[148:151],v244,s[24:27],s67 offen offset:1024 sc0 sc1
ta 40,238,12288
ta 42,238,12800
maaa 140,0,24
ta 44,238,28672
ta 46,238,29184
mvaa 40,52,24
ta 20,239,28672
mvaa 40,48,96
ta 22,239,29184
mvaa 44,60,96
ta 0,239,12288
ta 2,239,12800
s_waitcnt lgkmcnt(8)
mvaa 44,56,24
mvaa 52,56,28
dl 227,36,52,3072
mvaa 52,60,100
s_add_u32 s52,s52,s60
mvaa 48,48,100
mvaa 48,52,28
mvaa 56,52,32
mvaa 56,48,104
mvaa 60,60,104
mvaa 60,56,32
mvaa 68,56,36
mvaa 68,60,108
mvaa 64,48,108
mvaa 64,52,36
mvaa 32,68,36
s_waitcnt vmcnt(11) lgkmcnt(0)
s_barrier
s_add_u32 m0,s62,0
mvaa 32,64,108
dl 228,40,53,0
mvaa 36,76,108
ta 56,234,32768
mvaa 36,72,36
ta 58,234,33280
mvaa 28,72,32
ta 60,234,49152
mvaa 28,76,104
ta 62,234,49664
mvaa 24,64,104
dl 229,40,53,1024
mvaa 24,68,32
s_add_u32 s53,s53,s57
ta 48,235,49152
mvaa 16,68,28
ta 50,235,49664
mvaa 16,64,100
ta 52,235,32768
mvaa 20,76,100
ta 54,235,33280
mvaa 20,72,28
dl 230,44,54,2048
mvaa 12,72,24
mvaa 12,76,96
mvaa 8,64,96
ta 72,236,32768
mvaa 8,68,24
ta 74,236,33280
mvaa 76,24,40
ta 76,236,49152
mvaa 76,96,44
dl 231,44,54,3072
mvaa 96,96,20
s_add_u32 m0,s62,16384
s_add_u32 s54,s54,s57
ta 78,236,49664
mvaa 96,24,0
ta 64,237,49152
mvaa 100,28,0
ta 66,237,49664
mvaa 100,100,20
ta 68,237,32768
mvaa 80,100,44
dl 228,40,53,0
mvaa 80,28,40
ta 70,237,33280
mvaa 84,32,40
ra 96,233,0
mvaa 84,104,44
ra 24,232,0
mvaa 104,104,20
ra 100,233,2048
mvaa 104,32,0
dl 229,40,53,1024
mvaa 108,36,0
s_add_u32 s53,s53,s58
ra 28,232,2048
mvaa 108,108,20
ra 104,233,4096
mvaa 88,108,44
ta 0,234,36864
mvaa 88,36,40
ta 2,234,37376
s_waitcnt lgkmcnt(8)
maaa 236,56,4
dl 230,44,54,2048
maaa 236,60,80
ta 20,234,53248
maaa 220,48,80
ta 22,234,53760
maaa 220,52,4
ta 44,235,53248
maaa 216,52,8
ta 46,235,53760
maaa 216,48,84
dl 231,44,54,3072
maaa 232,60,84
s_add_u32 m0,s98,32768
ta 40,235,36864
s_add_u32 s54,s54,s58
maaa 232,56,8
ta 42,235,37376
maaa 228,56,12
ra 32,232,4096
maaa 228,60,88
ra 108,233,6144
v_add_u32_e32 v233,0xffff0000,v233
maaa 212,48,88
ra 36,232,6144
v_add_u32_e32 v232,0xffff0000,v232
maaa 212,52,12
dl 224,36,52,0
)"
R"(maaa 208,52,16
maaa 208,48,92
maaa 224,60,92
ta 52,236,36864
maaa 224,56,16
ta 54,236,37376
maaa 192,72,16
dl 225,36,52,1024
maaa 192,76,92
ta 48,236,53248
maaa 176,64,92
ta 50,236,53760
s_waitcnt lgkmcnt(6)
maaa 176,68,16
ta 60,237,53248
maaa 180,68,12
ta 62,237,53760
maaa 180,64,88
dl 226,36,52,2048
maaa 196,76,88
ta 56,237,36864
maaa 196,72,12
ta 58,237,37376
maaa 200,72,8
maaa 200,76,84
maaa 184,64,84
buffer_store_dwordx4 v[152:155],v244,s[24:27],s67 offen offset:2048 sc0 sc1
maaa 184,68,8
maaa 188,68,4
maaa 188,64,80
maaa 204,76,80
ta 68,234,40960
maaa 204,72,4
ta 70,234,41472
maaa 172,0,4
dl 227,36,52,3072
maaa 172,20,80
s_add_u32 s52,s52,s56
s_add_u32 m0,s98,36864
ta 64,234,57344
maaa 156,44,80
ta 66,234,57856
maaa 156,40,4
ta 76,235,57344
maaa 152,40,8
ta 78,235,57856
maaa 152,44,84
dl 224,36,52,0
maaa 168,20,84
ta 72,235,40960
maaa 168,0,8
ta 74,235,41472
maaa 164,0,12
maaa 164,20,88
maaa 148,44,88
buffer_store_dwordx4 v[156:159],v244,s[24:27],s67 offen offset:3072 sc0 sc1
maaa 148,40,12
s_mov_b32 s67,16384
maaa 144,40,16
maaa 144,44,92
maaa 160,20,92
ta 40,236,40960
maaa 160,0,16
dl 225,36,52,1024
s_waitcnt lgkmcnt(3)
maaa 128,52,16
ta 42,236,41472
maaa 128,48,92
ta 44,236,57344
maaa 112,60,92
ta 46,236,57856
maaa 112,56,16
ta 20,237,57344
maaa 116,56,12
ta 22,237,57856
maaa 116,60,88
dl 226,36,52,2048
maaa 132,48,88
ta 0,237,40960
maaa 132,52,12
ta 2,237,41472
maaa 136,52,8
maaa 136,48,84
maaa 120,60,84
dl 227,36,52,3072
maaa 120,56,8
s_add_u32 s52,s52,s60
maaa 124,56,4
maaa 124,60,80
maaa 140,48,80
ta 56,238,45056
ta 58,238,45568
maaa 140,52,4
buffer_store_dwordx4 v[160:163],v244,s[24:27],s67 offen offset:0 sc0 sc1
ta 60,238,61440
ta 62,238,61952
mvaa 40,68,4
ta 48,239,61440
mvaa 40,64,80
ta 50,239,61952
mvaa 44,76,80
ta 52,239,45056
ta 54,239,45568
s_waitcnt lgkmcnt(8)
mvaa 44,72,4
mvaa 52,72,8
mvaa 52,76,84
mvaa 48,64,84
mvaa 48,68,8
mvaa 56,68,12
mvaa 56,64,88
mvaa 60,76,88
mvaa 60,72,12
mvaa 68,72,16
mvaa 68,76,92
mvaa 64,64,92
mvaa 64,68,16
mvaa 32,40,16
s_waitcnt vmcnt(11) lgkmcnt(0)
s_barrier
s_add_u32 m0,s62,32768
mvaa 32,44,92
dl 228,40,53,0
mvaa 36,20,92
ta 72,234,0
mvaa 36,0,16
ta 74,234,512
mvaa 28,0,12
ta 76,234,16384
mvaa 28,20,88
ta 78,234,16896
mvaa 24,44,88
ta 64,235,16384
mvaa 24,40,12
dl 229,40,53,1024
mvaa 16,40,8
s_add_u32 s53,s53,s57
ta 66,235,16896
mvaa 16,44,84
ta 68,235,0
mvaa 20,20,84
ta 70,235,512
mvaa 20,0,8
mvaa 12,0,4
dl 230,44,54,2048
mvaa 12,20,80
mvaa 8,44,80
ta 0,236,0
mvaa 8,40,4
ta 2,236,512
mvaa 76,4,56
ta 20,236,16384
mvaa 76,80,60
dl 231,44,54,3072
mvaa 96,80,48
s_add_u32 m0,s62,49152
s_add_u32 s54,s54,s57
ta 22,236,16896
mvaa 96,4,52
ta 44,237,16384
mvaa 100,8,52
ta 46,237,16896
mvaa 100,84,48
ta 40,237,0
mvaa 80,84,60
dl 228,40,53,0
mvaa 80,8,56
ta 42,237,512
mvaa 84,12,56
ra 80,233,0
mvaa 84,88,60
ra 4,232,0
mvaa 104,88,48
ra 84,233,2048
mvaa 104,12,52
ra 8,232,2048
mvaa 108,16,52
dl 229,40,53,1024
mvaa 108,92,48
s_add_u32 s53,s53,s58
ra 88,233,4096
mvaa 88,92,60
ta 52,234,4096
mvaa 88,16,56
ta 54,234,4608
s_waitcnt lgkmcnt(8)
maaa 236,72,24
ta 48,234,20480
maaa 236,76,96
dl 230,44,54,2048
maaa 220,64,96
ta 50,234,20992
maaa 220,68,24
ta 60,235,20480
maaa 216,68,28
ta 62,235,20992
maaa 216,64,100
L_vs11:
s_cbranch_execz L_vs15
ta 56,235,4096
v_accvgpr_read_b32 v0,a236
maaa 232,76,100
dl 231,44,54,3072
maaa 232,72,28
s_add_u32 m0,s98,65536
ta 58,235,4608
s_add_u32 s54,s54,s58
maaa 228,72,32
ra 12,232,4096
v_accvgpr_read_b32 v1,a237
maaa 228,76,104
ra 92,233,6144
v_accvgpr_read_b32 v2,a238
maaa 212,64,104
ra 16,232,6144
v_accvgpr_read_b32 v3,a239
maaa 212,68,32
dl 224,36,52,0
maaa 208,68,36
v_accvgpr_read_b32 v4,a220
v_accvgpr_read_b32 v5,a221
maaa 208,64,108
v_accvgpr_read_b32 v6,a222
v_accvgpr_read_b32 v7,a223
maaa 224,76,108
ta 68,236,4096
v_accvgpr_read_b32 v92,a216
maaa 224,72,36
ta 70,236,4608
v_accvgpr_read_b32 v93,a217
maaa 192,0,36
dl 225,36,52,1024
maaa 192,20,108
ta 64,236,20480
v_accvgpr_read_b32 v94,a218
maaa 176,44,108
ta 66,236,20992
v_accvgpr_read_b32 v95,a219
s_waitcnt lgkmcnt(6)
maaa 176,40,36
ta 76,237,20480
v_accvgpr_read_b32 v72,a232
maaa 180,40,32
ta 78,237,20992
v_accvgpr_read_b32 v73,a233
maaa 180,44,104
ta 72,237,4096
v_accvgpr_read_b32 v74,a234
maaa 196,20,104
s_mov_b64 exec,3
global_load_dword a250,v253,s[96:97]
s_mov_b64 exec,-1
maaa 196,0,32
ta 74,237,4608
v_accvgpr_read_b32 v75,a235
maaa 200,0,28
maaa 200,20,100
maaa 184,44,100
maaa 184,40,28
buffer_store_dwordx4 v[164:167],v244,s[24:27],s67 offen offset:1024 sc0 sc1
maaa 188,40,24
maaa 188,44,96
maaa 204,20,96
ta 40,234,8192
maaa 204,0,24
ta 42,234,8704
maaa 172,52,24
dl 226,36,52,2048
maaa 172,48,96
ta 44,234,24576
maaa 156,60,96
ta 46,234,25088
maaa 156,56,24
ta 20,235,24576
maaa 152,56,28
ta 22,235,25088
maaa 152,60,100
dl 227,36,52,3072
maaa 168,48,100
s_add_u32 s52,s52,s56
s_add_u32 m0,s98,69632
ta 0,235,8192
maaa 168,52,28
ta 2,235,8704
maaa 164,52,32
maaa 164,48,104
maaa 148,60,104
maaa 148,56,32
buffer_store_dwordx4 v[168:171],v244,s[24:27],s67 offen offset:2048 sc0 sc1
maaa 144,56,36
maaa 144,60,108
maaa 160,48,108
ta 56,236,8192
maaa 160,52,36
ta 58,236,8704
s_waitcnt lgkmcnt(4)
maaa 128,68,36
dl 224,36,52,0
maaa 128,64,108
ta 60,236,24576
maaa 112,76,108
ta 62,236,25088
maaa 112,72,36
ta 48,237,24576
maaa 116,72,32
ta 50,237,25088
maaa 116,76,104
dl 225,36,52,1024
maaa 132,64,104
ta 52,237,8192
maaa 132,68,32
ta 54,237,8704
maaa 136,68,28
maaa 136,64,100
maaa 120,76,100
dl 226,36,52,2048
maaa 120,72,28
maaa 124,72,24
maaa 124,76,96
maaa 140,64,96
ta 72,238,12288
ta 74,238,12800
maaa 140,68,24
ta 76,238,28672
ta 78,238,29184
mvaa 40,40,24
buffer_store_dwordx4 v[172:175],v244,s[24:27],s67 offen offset:3072 sc0 sc1
ta 64,239,28672
mvaa 40,44,96
s_mov_b32 s67,20480
ta 66,239,29184
mvaa 44,20,96
ta 68,239,12288
ta 70,239,12800
s_waitcnt lgkmcnt(8)
mvaa 44,0,24
mvaa 52,0,28
mvaa 52,20,100
dl 227,36,52,3072
mvaa 48,44,100
s_add_u32 s52,s52,s60
mvaa 48,40,28
mvaa 56,40,32
mvaa 56,44,104
mvaa 60,20,104
mvaa 60,0,32
mvaa 68,0,36
mvaa 68,20,108
mvaa 64,44,108
mvaa 64,40,36
mvaa 32,56,36
s_waitcnt vmcnt(12) lgkmcnt(0)
s_barrier
s_add_u32 m0,s62,0
mvaa 32,60,108
dl 228,40,53,0
mvaa 36,48,108
ta 0,234,32768
mvaa 36,52,36
ta 2,234,33280
mvaa 28,52,32
ta 20,234,49152
mvaa 28,48,104
ta 22,234,49664
mvaa 24,60,104
ta 44,235,49152
mvaa 24,56,32
dl 229,40,53,1024
mvaa 16,56,28
s_add_u32 s53,s53,s57
ta 46,235,49664
mvaa 16,60,100
ta 40,235,32768
mvaa 20,48,100
ta 42,235,33280
mvaa 20,52,28
ta 236,236,36864
mvaa 12,52,24
dl 230,44,54,2048
mvaa 12,48,96
ta 238,236,37376
mvaa 8,60,96
ta 52,236,32768
mvaa 8,56,24
ta 54,236,33280
mvaa 76,24,72
ta 48,236,49152
mvaa 76,96,76
ta 50,236,49664
mvaa 96,96,64
dl 231,44,54,3072
mvaa 96,24,68
s_add_u32 m0,s62,16384
s_add_u32 s54,s54,s57
ta 60,237,49152
mvaa 100,28,68
ta 62,237,49664
mvaa 100,100,64
ta 56,237,32768
mvaa 80,100,76
ta 58,237,33280
mvaa 80,28,72
dl 228,40,53,0
mvaa 84,32,72
ta 220,236,53248
mvaa 84,104,76
ta 222,236,53760
mvaa 104,104,64
ta 232,237,53248
mvaa 104,32,68
ta 234,237,53760
mvaa 108,36,68
dl 229,40,53,1024
mvaa 108,108,64
s_add_u32 s53,s53,s58
ta 216,237,36864
mvaa 88,108,76
ta 68,234,36864
mvaa 88,36,72
ta 70,234,37376
s_waitcnt lgkmcnt(8)
mvaa 0,0,4
ta 64,234,53248
mvaa 0,20,80
dl 230,44,54,2048
mvaa 4,44,80
ta 66,234,53760
mvaa 4,40,4
ta 76,235,53248
mvaa 92,40,8
ta 78,235,53760
mvaa 92,44,84
ta 72,235,36864
mvaa 72,20,84
ta 74,235,37376
mvaa 72,0,8
dl 231,44,54,3072
maaa 228,0,12
s_add_u32 m0,s98,0
ta 218,237,37376
s_add_u32 s54,s54,s58
maaa 228,20,88
ra 96,233,32768
maaa 212,44,88
ra 24,232,32768
maaa 212,40,12
ra 100,233,34816
maaa 208,40,16
dl 224,36,52,0
maaa 208,44,92
ra 28,232,34816
v_accvgpr_read_b32 v144,a228
maaa 224,20,92
ra 104,233,36864
v_accvgpr_read_b32 v145,a229
maaa 224,0,16
ra 32,232,36864
v_accvgpr_read_b32 v146,a230
maaa 192,52,16
ra 36,232,38912
v_accvgpr_read_b32 v147,a231
maaa 192,48,92
dl 225,36,52,1024
maaa 176,60,92
ra 108,233,38912
v_accvgpr_read_b32 v148,a212
s_waitcnt lgkmcnt(8)
maaa 176,56,16
ta 40,234,40960
v_accvgpr_read_b32 v149,a213
maaa 180,56,12
ta 42,234,41472
v_accvgpr_read_b32 v150,a214
maaa 180,60,88
ta 44,234,57344
v_accvgpr_read_b32 v151,a215
maaa 196,48,88
buffer_store_dwordx4 v[176:179],v244,s[24:27],s67 offen offset:0 sc0 sc1
maaa 196,52,12
ta 46,234,57856
v_accvgpr_read_b32 v156,a208
maaa 200,52,8
ta 20,235,57344
v_accvgpr_read_b32 v157,a209
maaa 200,48,84
ta 22,235,57856
v_accvgpr_read_b32 v158,a210
maaa 184,60,84
ta 0,235,40960
v_accvgpr_read_b32 v159,a211
maaa 184,56,8
ta 2,235,41472
v_accvgpr_read_b32 v152,a224
maaa 188,56,4
dl 226,36,52,2048
maaa 188,60,80
v_accvgpr_read_b32 v153,a225
v_accvgpr_read_b32 v154,a226
maaa 204,48,80
ta 56,236,40960
v_accvgpr_read_b32 v155,a227
maaa 204,52,4
ta 58,236,41472
v_add_u32_e32 v233,0x10000,v233
maaa 172,68,4
ta 60,236,57344
v_add_u32_e32 v232,0x10000,v232
maaa 172,64,80
dl 227,36,52,3072
maaa 156,76,80
s_add_u32 s52,s52,s56
s_add_u32 m0,s98,4096
ta 62,236,57856
maaa 156,72,4
ta 48,237,57344
maaa 152,72,8
ta 50,237,57856
maaa 152,76,84
ta 52,237,40960
maaa 168,64,84
dl 224,36,52,0
maaa 168,68,8
ta 54,237,41472
maaa 164,68,12
maaa 164,64,88
maaa 148,76,88
maaa 148,72,12
buffer_store_dwordx4 v[180:183],v244,s[24:27],s67 offen offset:1024 sc0 sc1
maaa 144,72,16
maaa 144,76,92
maaa 160,64,92
ta 72,238,45056
maaa 160,68,16
ta 74,238,45568
maaa 128,236,16
dl 225,36,52,1024
maaa 128,220,92
ta 76,238,61440
maaa 112,232,92
ta 78,238,61952
maaa 112,216,16
ta 64,239,61440
maaa 116,216,12
ta 66,239,61952
maaa 116,232,88
ta 68,239,45056
maaa 132,220,88
dl 226,36,52,2048
maaa 132,236,12
ta 70,239,45568
maaa 136,236,8
maaa 136,220,84
maaa 120,232,84
maaa 120,216,8
dl 227,36,52,3072
maaa 124,216,4
s_add_u32 s52,s52,s60
maaa 124,232,80
maaa 140,220,80
maaa 140,236,4
s_waitcnt lgkmcnt(1)
mvaa 40,40,4
buffer_store_dwordx4 v[184:187],v244,s[24:27],s67 offen offset:2048 sc0 sc1
mvaa 40,44,80
mvaa 44,20,80
mvaa 44,0,4
mvaa 52,0,8
mvaa 52,20,84
mvaa 48,44,84
mvaa 48,40,8
mvaa 56,40,12
mvaa 56,44,88
mvaa 60,20,88
mvaa 60,0,12
mvaa 68,0,16
mvaa 68,20,92
mvaa 64,44,92
mvaa 64,40,16
mvaa 32,56,16
s_waitcnt vmcnt(11) lgkmcnt(0)
s_barrier
s_add_u32 m0,s62,32768
mvaa 32,60,92
dl 228,40,53,0
mvaa 36,48,92
ta 216,234,0
mvaa 36,52,16
ta 218,234,512
mvaa 28,52,12
ta 232,234,16384
mvaa 28,48,88
ta 234,234,16896
mvaa 24,60,88
dl 229,40,53,1024
mvaa 24,56,12
s_add_u32 s53,s53,s57
ta 220,235,16384
mvaa 16,56,8
ta 222,235,16896
mvaa 16,60,84
ta 236,235,0
mvaa 20,48,84
ta 238,235,512
mvaa 20,52,8
dl 230,44,54,2048
mvaa 12,52,4
ta 0,236,0
mvaa 12,48,80
ta 2,236,512
mvaa 8,60,80
ta 20,236,16384
mvaa 8,56,4
ta 22,236,16896
mvaa 76,4,72
dl 231,44,54,3072
mvaa 76,80,76
s_add_u32 m0,s62,49152
s_add_u32 s54,s54,s57
ta 44,237,16384
mvaa 96,80,64
ta 46,237,16896
mvaa 96,4,68
ta 40,237,0
mvaa 100,8,68
ta 42,237,512
mvaa 100,84,64
dl 228,40,53,0
mvaa 80,84,76
ta 52,234,4096
mvaa 80,8,72
ta 54,234,4608
mvaa 84,12,72
ta 48,234,20480
mvaa 84,88,76
ta 50,234,20992
mvaa 104,88,64
dl 229,40,53,1024
mvaa 104,12,68
s_add_u32 s53,s53,s58
ta 60,235,20480
mvaa 108,16,68
ta 62,235,20992
mvaa 108,92,64
ta 56,235,4096
mvaa 88,92,76
ta 58,235,4608
mvaa 88,16,72
dl 230,44,54,2048
s_waitcnt lgkmcnt(8)
mvaa 0,216,24
ta 228,234,8192
mvaa 0,232,96
ta 68,236,4096
mvaa 4,220,96
ta 70,236,4608
mvaa 4,236,24
ta 64,236,20480
mvaa 92,236,28
ta 66,236,20992
mvaa 92,220,100
dl 231,44,54,3072
mvaa 72,232,100
s_add_u32 m0,s98,32768
ta 76,237,20480
s_add_u32 s54,s54,s58
mvaa 72,216,28
ta 78,237,20992
mvaa 144,216,32
ta 72,237,4096
mvaa 144,232,104
ta 74,237,4608
mvaa 148,220,104
dl 224,36,52,0
mvaa 148,236,32
ta 230,234,8704
mvaa 156,236,36
ta 212,234,24576
mvaa 156,220,108
ta 214,234,25088
mvaa 152,232,108
ta 224,235,24576
mvaa 152,216,36
dl 225,36,52,1024
maaa 192,0,36
ta 226,235,25088
maaa 192,20,108
ta 208,235,8192
maaa 176,44,108
ra 80,233,0
maaa 176,40,36
ta 210,235,8704
maaa 180,40,32
dl 226,36,52,2048
maaa 180,44,104
ra 4,232,0
v_accvgpr_read_b32 v160,a192
maaa 196,20,104
ra 84,233,2048
v_accvgpr_read_b32 v161,a193
maaa 196,0,32
ra 8,232,2048
v_accvgpr_read_b32 v162,a194
maaa 200,0,28
ra 12,232,4096
v_accvgpr_read_b32 v163,a195
maaa 200,20,100
buffer_store_dwordx4 v[188:191],v244,s[24:27],s67 offen offset:3072 sc0 sc1
maaa 184,44,100
s_mov_b32 s67,24576
ra 88,233,4096
v_accvgpr_read_b32 v164,a176
maaa 184,40,28
ra 92,233,6144
v_accvgpr_read_b32 v165,a177
maaa 188,40,24
ra 16,232,6144
v_accvgpr_read_b32 v166,a178
maaa 188,44,96
ta 236,236,8192
v_accvgpr_read_b32 v167,a179
maaa 204,20,96
ta 238,236,8704
v_accvgpr_read_b32 v172,a180
maaa 204,0,24
dl 227,36,52,3072
s_waitcnt lgkmcnt(8)
maaa 172,52,24
L_vs12:
s_cbranch_execz L_vs16
s_add_u32 s52,s52,s56
s_add_u32 m0,s98,36864
ta 220,236,24576
maaa 172,48,96
ta 222,236,25088
v_accvgpr_read_b32 v173,a181
maaa 156,60,96
ta 232,237,24576
v_accvgpr_read_b32 v174,a182
maaa 156,56,24
ta 234,237,25088
v_accvgpr_read_b32 v175,a183
maaa 152,56,28
dl 224,36,52,0
maaa 152,60,100
ta 216,237,8192
v_accvgpr_read_b32 v168,a196
maaa 168,48,100
ta 218,237,8704
v_accvgpr_read_b32 v169,a197
maaa 168,52,28
ta 40,238,12288
v_accvgpr_read_b32 v170,a198
maaa 164,52,32
ta 42,238,12800
v_accvgpr_read_b32 v171,a199
maaa 164,48,104
dl 225,36,52,1024
maaa 148,60,104
ta 44,238,28672
v_add_u32_e32 v233,0xffff0000,v233
maaa 148,56,32
ta 46,238,29184
v_add_u32_e32 v232,0xffff0000,v232
maaa 144,56,36
ta 20,239,28672
maaa 144,60,108
ta 22,239,29184
maaa 160,48,108
buffer_store_dwordx4 v[192:195],v244,s[24:27],s67 offen offset:0 sc0 sc1
maaa 160,52,36
ta 0,239,12288
maaa 128,68,36
ta 2,239,12800
maaa 128,64,108
maaa 112,76,108
maaa 112,72,36
maaa 116,72,32
dl 226,36,52,2048
maaa 116,76,104
maaa 132,64,104
maaa 132,68,32
maaa 136,68,28
maaa 136,64,100
dl 227,36,52,3072
maaa 120,76,100
s_add_u32 s52,s52,s60
maaa 120,72,28
maaa 124,72,24
maaa 124,76,96
maaa 140,64,96
buffer_store_dwordx4 v[196:199],v244,s[24:27],s67 offen offset:1024 sc0 sc1
maaa 140,68,24
mvaa 40,228,24
mvaa 40,212,96
mvaa 44,224,96
mvaa 44,208,24
mvaa 52,208,28
mvaa 52,224,100
mvaa 48,212,100
mvaa 48,228,28
mvaa 56,228,32
mvaa 56,212,104
mvaa 60,224,104
mvaa 60,208,32
mvaa 68,208,36
mvaa 68,224,108
mvaa 64,212,108
mvaa 64,228,36
s_waitcnt lgkmcnt(0)
mvaa 32,236,36
s_waitcnt vmcnt(11)
s_barrier
s_add_u32 m0,s62,0
mvaa 32,220,108
dl 228,40,53,0
mvaa 36,232,108
ta 56,234,32768
mvaa 36,216,36
ta 58,234,33280
mvaa 28,216,32
ta 60,234,49152
mvaa 28,232,104
ta 62,234,49664
mvaa 24,220,104
dl 229,40,53,1024
mvaa 24,236,32
s_add_u32 s53,s53,s57
ta 48,235,49152
mvaa 16,236,28
ta 50,235,49664
mvaa 16,220,100
ta 52,235,32768
mvaa 20,232,100
ta 54,235,33280
mvaa 20,216,28
dl 230,44,54,2048
mvaa 12,216,24
ta 72,236,32768
mvaa 12,232,96
ta 74,236,33280
mvaa 8,220,96
ta 76,236,49152
mvaa 8,236,24
ta 78,236,49664
mvaa 76,24,40
dl 231,44,54,3072
mvaa 76,96,44
s_add_u32 m0,s62,16384
s_add_u32 s54,s54,s57
ta 64,237,49152
mvaa 96,96,20
ta 66,237,49664
mvaa 96,24,0
ta 68,237,32768
mvaa 100,28,0
ta 70,237,33280
mvaa 100,100,20
dl 228,40,53,0
mvaa 80,100,44
ta 208,234,36864
mvaa 80,28,40
ta 210,234,37376
mvaa 84,32,40
ta 224,234,53248
mvaa 84,104,44
ta 226,234,53760
mvaa 104,104,20
ta 212,235,53248
mvaa 104,32,0
dl 229,40,53,1024
mvaa 108,36,0
s_add_u32 s53,s53,s58
ta 214,235,53760
mvaa 108,108,20
ta 228,235,36864
mvaa 88,108,44
ta 230,235,37376
mvaa 88,36,40
ta 192,236,40960
s_waitcnt lgkmcnt(9)
mvaa 0,56,4
dl 230,44,54,2048
mvaa 0,60,80
ta 216,236,36864
mvaa 4,48,80
ta 218,236,37376
mvaa 4,52,4
ta 232,236,53248
mvaa 92,52,8
ta 234,236,53760
mvaa 92,48,84
dl 231,44,54,3072
mvaa 72,60,84
s_add_u32 m0,s98,65536
ta 220,237,53248
s_add_u32 s54,s54,s58
mvaa 72,56,8
ta 222,237,53760
mvaa 144,56,12
ta 236,237,36864
mvaa 144,60,88
ta 238,237,37376
mvaa 148,48,88
dl 224,36,52,0
mvaa 148,52,12
ta 194,236,41472
mvaa 156,52,16
ta 176,236,57344
mvaa 156,48,92
ta 178,236,57856
mvaa 152,60,92
ta 196,237,57344
mvaa 152,56,16
ta 198,237,57856
mvaa 160,72,16
dl 225,36,52,1024
mvaa 160,76,92
ta 180,237,40960
mvaa 164,64,92
ta 182,237,41472
mvaa 164,68,16
ra 96,233,0
mvaa 172,68,12
ta 0,234,40960
mvaa 172,64,88
dl 226,36,52,2048
mvaa 168,76,88
ta 2,234,41472
mvaa 168,72,12
ta 20,234,57344
maaa 200,72,8
ta 22,234,57856
maaa 200,76,84
ta 44,235,57344
maaa 184,64,84
buffer_store_dwordx4 v[200:203],v244,s[24:27],s67 offen offset:2048 sc0 sc1
maaa 184,68,8
ta 46,235,57856
maaa 188,68,4
ta 40,235,40960
maaa 188,64,80
ta 42,235,41472
v_accvgpr_read_b32 v176,a200
maaa 204,76,80
ra 24,232,0
v_accvgpr_read_b32 v177,a201
maaa 204,72,4
dl 227,36,52,3072
s_waitcnt lgkmcnt(8)
maaa 172,208,4
s_add_u32 s52,s52,s56
s_add_u32 m0,s98,69632
ra 100,233,2048
maaa 172,224,80
ra 28,232,2048
v_accvgpr_read_b32 v178,a202
maaa 156,212,80
ra 32,232,4096
v_accvgpr_read_b32 v179,a203
maaa 156,228,4
ra 104,233,4096
v_accvgpr_read_b32 v180,a184
maaa 152,228,8
ra 108,233,6144
v_accvgpr_read_b32 v181,a185
maaa 152,212,84
dl 224,36,52,0
maaa 168,224,84
ra 36,232,6144
v_accvgpr_read_b32 v182,a186
maaa 168,208,8
ta 52,238,45056
v_accvgpr_read_b32 v183,a187
maaa 164,208,12
ta 54,238,45568
v_accvgpr_read_b32 v188,a188
maaa 164,224,88
ta 48,238,61440
v_accvgpr_read_b32 v189,a189
maaa 148,212,88
buffer_store_dwordx4 v[204:207],v244,s[24:27],s67 offen offset:3072 sc0 sc1
maaa 148,228,12
s_mov_b32 s67,28672
ta 50,238,61952
v_accvgpr_read_b32 v190,a190
maaa 144,228,16
ta 60,239,61440
v_accvgpr_read_b32 v184,a204
maaa 144,212,92
ta 62,239,61952
v_accvgpr_read_b32 v191,a191
maaa 160,224,92
ta 56,239,45056
v_accvgpr_read_b32 v185,a205
maaa 160,208,16
dl 225,36,52,1024
maaa 128,216,16
ta 58,239,45568
v_accvgpr_read_b32 v186,a206
maaa 128,232,92
)"
R"(v_accvgpr_read_b32 v187,a207
maaa 112,220,92
maaa 112,236,16
maaa 116,236,12
dl 226,36,52,2048
maaa 116,220,88
maaa 132,232,88
maaa 132,216,12
maaa 136,216,8
maaa 136,232,84
dl 227,36,52,3072
maaa 120,220,84
s_add_u32 s52,s52,s60
maaa 120,236,8
maaa 124,236,4
maaa 124,220,80
maaa 140,232,80
maaa 140,216,4
buffer_store_dwordx4 v[208:211],v244,s[24:27],s67 offen offset:0 sc0 sc1
s_waitcnt lgkmcnt(0)
mvaa 40,0,4
mvaa 40,20,80
mvaa 44,44,80
mvaa 44,40,4
mvaa 52,40,8
mvaa 52,44,84
mvaa 48,20,84
mvaa 48,0,8
mvaa 56,0,12
mvaa 56,20,88
mvaa 60,44,88
mvaa 60,40,12
mvaa 68,40,16
mvaa 68,44,92
mvaa 64,20,92
mvaa 64,0,16
mvaa 32,192,16
s_waitcnt vmcnt(11)
s_barrier
s_add_u32 m0,s62,32768
mvaa 32,176,92
dl 228,40,53,0
mvaa 36,196,92
ta 68,234,0
mvaa 36,180,16
ta 70,234,512
mvaa 28,180,12
ta 64,234,16384
mvaa 28,196,88
ta 66,234,16896
mvaa 24,176,88
dl 229,40,53,1024
mvaa 24,192,12
s_add_u32 s53,s53,s57
ta 76,235,16384
mvaa 16,192,8
ta 78,235,16896
mvaa 16,176,84
ta 72,235,0
mvaa 20,196,84
ta 74,235,512
mvaa 20,180,8
ta 228,236,0
mvaa 12,180,4
dl 230,44,54,2048
mvaa 12,196,80
ta 230,236,512
mvaa 8,176,80
ta 212,236,16384
mvaa 8,192,4
ta 214,236,16896
mvaa 76,4,52
ta 224,237,16384
mvaa 76,80,48
dl 231,44,54,3072
mvaa 96,80,60
s_add_u32 m0,s62,49152
s_add_u32 s54,s54,s57
ta 226,237,16896
mvaa 96,4,56
ta 208,237,0
mvaa 100,8,56
ta 210,237,512
mvaa 100,84,60
ta 236,234,4096
mvaa 80,84,48
dl 228,40,53,0
mvaa 80,8,52
ta 238,234,4608
mvaa 84,12,52
ta 220,234,20480
mvaa 84,88,48
ta 222,234,20992
mvaa 104,88,60
ta 232,235,20480
mvaa 104,12,56
dl 229,40,53,1024
mvaa 108,16,56
s_add_u32 s53,s53,s58
ta 234,235,20992
mvaa 108,92,60
ta 216,235,4096
mvaa 88,92,48
ta 218,235,4608
mvaa 88,16,52
ta 40,236,4096
s_waitcnt lgkmcnt(8)
mvaa 0,68,24
dl 230,44,54,2048
mvaa 0,64,96
ta 42,236,4608
mvaa 4,76,96
ta 44,236,20480
mvaa 4,72,24
ta 46,236,20992
mvaa 92,72,28
ta 20,237,20480
mvaa 92,76,100
ta 22,237,20992
mvaa 72,64,100
dl 231,44,54,3072
mvaa 72,68,28
s_add_u32 m0,s98,0
ta 200,238,12288
s_add_u32 s54,s54,s58
mvaa 144,68,32
ta 202,238,12800
mvaa 144,64,104
ta 0,237,4096
mvaa 148,76,104
ta 2,237,4608
mvaa 148,72,32
dl 224,36,52,0
mvaa 156,72,36
ta 184,238,28672
mvaa 156,76,108
ta 186,238,29184
mvaa 152,64,108
ta 204,239,28672
mvaa 152,68,36
ta 206,239,29184
mvaa 160,228,36
dl 225,36,52,1024
mvaa 160,212,108
ta 188,239,12288
mvaa 164,224,108
ta 190,239,12800
mvaa 164,208,36
ta 180,234,8192
mvaa 172,208,32
ta 182,234,8704
mvaa 172,224,104
dl 226,36,52,2048
mvaa 168,212,104
ta 196,234,24576
mvaa 168,228,32
ta 198,234,25088
mvaa 176,228,28
ta 176,235,24576
mvaa 176,212,100
ta 178,235,25088
mvaa 180,224,100
ta 192,235,8192
mvaa 180,208,28
buffer_store_dwordx4 v[212:215],v244,s[24:27],s67 offen offset:1024 sc0 sc1
mvaa 188,208,24
ta 194,235,8704
mvaa 188,224,96
ra 4,232,32768
mvaa 184,212,96
ra 80,233,32768
mvaa 184,228,24
ra 84,233,34816
s_waitcnt lgkmcnt(9)
maaa 172,236,24
dl 227,36,52,3072
maaa 172,220,96
s_add_u32 s52,s52,s56
s_add_u32 m0,s98,4096
ra 8,232,34816
maaa 156,232,96
ra 88,233,36864
maaa 156,216,24
ta 56,236,8192
maaa 152,216,28
ra 12,232,36864
v_accvgpr_read_b32 v192,a172
maaa 152,232,100
dl 224,36,52,0
maaa 168,220,100
ta 58,236,8704
v_accvgpr_read_b32 v193,a173
maaa 168,236,28
ta 60,236,24576
v_accvgpr_read_b32 v194,a174
maaa 164,236,32
ta 62,236,25088
v_accvgpr_read_b32 v195,a175
maaa 164,220,104
ta 48,237,24576
v_accvgpr_read_b32 v196,a156
maaa 148,232,104
buffer_store_dwordx4 v[216:219],v244,s[24:27],s67 offen offset:2048 sc0 sc1
maaa 148,216,32
ta 50,237,25088
v_accvgpr_read_b32 v197,a157
maaa 144,216,36
ra 92,233,38912
v_accvgpr_read_b32 v198,a158
maaa 144,232,108
ra 16,232,38912
v_accvgpr_read_b32 v199,a159
maaa 160,220,108
ta 52,237,8192
v_accvgpr_read_b32 v204,a152
maaa 160,236,36
ta 54,237,8704
v_accvgpr_read_b32 v205,a153
maaa 128,40,36
dl 225,36,52,1024
maaa 128,44,108
v_accvgpr_read_b32 v206,a154
v_accvgpr_read_b32 v207,a155
maaa 112,20,108
v_accvgpr_read_b32 v200,a168
v_accvgpr_read_b32 v201,a169
maaa 112,0,36
v_accvgpr_read_b32 v202,a170
v_accvgpr_read_b32 v203,a171
maaa 116,0,32
v_add_u32_e32 v233,0x10000,v233
v_add_u32_e32 v232,0x10000,v232
maaa 116,20,104
dl 226,36,52,2048
maaa 132,44,104
maaa 132,40,32
maaa 136,40,28
maaa 136,44,100
maaa 120,20,100
dl 227,36,52,3072
maaa 120,0,28
s_add_u32 s52,s52,s60
maaa 124,0,24
maaa 124,20,96
maaa 140,44,96
maaa 140,40,24
buffer_store_dwordx4 v[220:223],v244,s[24:27],s67 offen offset:3072 sc0 sc1
mvaa 40,180,24
s_mov_b32 s67,0x5550055
s_mov_b32 s26,2097152
v_cndmask_b32_e64 v116,v241,0,s[66:67]
s_waitcnt lgkmcnt(0)
mvaa 40,196,96
L_vs13:
s_cbranch_execz L_vr0
s_mov_b32 s66,0xa0000
s_mov_b32 s67,0xaaa00aa
v_cndmask_b32_e64 v117,v241,0,s[66:67]
mvaa 44,176,96
s_mov_b32 s66,0x150001
s_mov_b32 s67,0x15550155
v_cndmask_b32_e64 v118,v241,0,s[66:67]
mvaa 44,192,24
s_mov_b32 s66,0x2a0002
s_mov_b32 s67,0x2aaa02aa
v_cndmask_b32_e64 v119,v241,0,s[66:67]
mvaa 52,192,28
s_lshr_b32 s66,s18,2
s_lshl_b32 s67,s18,13
s_lshl_b32 s66,s66,13
mvaa 52,176,100
s_mov_b32 s27,131072
v_cndmask_b32_e64 v120,v116,v241,s[64:65]
v_cndmask_b32_e64 v121,v117,v241,s[64:65]
mvaa 48,196,100
s_mov_b64 s[24:25],s[82:83]
v_cndmask_b32_e64 v122,v118,v241,s[64:65]
v_cndmask_b32_e64 v123,v119,v241,s[64:65]
mvaa 48,180,28
s_mov_b32 s83,0
s_or_b32 s82,s15,16384
mvaa 56,180,32
mvaa 56,196,104
mvaa 60,176,104
mvaa 60,192,32
mvaa 68,192,36
mvaa 68,176,108
mvaa 64,196,108
mvaa 64,180,36
mvaa 32,56,36
s_waitcnt vmcnt(11)
s_barrier
s_add_u32 m0,s62,0
mvaa 32,60,108
dl 228,40,53,0
mvaa 36,48,108
ta 72,234,32768
mvaa 36,52,36
ta 74,234,33280
mvaa 28,52,32
ta 76,234,49152
mvaa 28,48,104
ta 78,234,49664
mvaa 24,60,104
ta 64,235,49152
mvaa 24,56,32
dl 229,40,53,1024
mvaa 16,56,28
s_add_u32 s53,s53,s57
ta 66,235,49664
mvaa 16,60,100
ta 68,235,32768
mvaa 20,48,100
ta 70,235,33280
mvaa 20,52,28
ta 208,236,32768
mvaa 12,52,24
dl 230,44,54,2048
mvaa 12,48,96
ta 210,236,33280
mvaa 8,60,96
ta 224,236,49152
mvaa 8,56,24
ta 226,236,49664
mvaa 76,24,200
ta 212,237,49152
mvaa 76,96,184
dl 231,44,54,3072
mvaa 96,96,204
s_add_u32 m0,s62,16384
s_add_u32 s54,s54,s57
ta 214,237,49664
mvaa 96,24,188
ta 228,237,32768
mvaa 100,28,188
ta 230,237,33280
mvaa 100,100,204
ta 216,234,36864
mvaa 80,100,184
ta 218,234,37376
mvaa 80,28,200
dl 228,40,53,0
mvaa 84,32,200
ta 232,234,53248
mvaa 84,104,184
ta 234,234,53760
mvaa 104,104,204
ta 220,235,53248
mvaa 104,32,188
ta 222,235,53760
mvaa 108,36,188
dl 229,40,53,1024
mvaa 108,108,204
s_add_u32 s53,s53,s58
ta 236,235,36864
mvaa 88,108,184
ta 238,235,37376
mvaa 88,36,200
ta 0,236,36864
s_waitcnt lgkmcnt(8)
mvaa 0,72,4
ta 2,236,37376
mvaa 0,76,80
dl 230,44,54,2048
mvaa 4,64,80
ta 20,236,53248
mvaa 4,68,4
ta 22,236,53760
mvaa 92,68,8
ta 44,237,53248
mvaa 92,64,84
ta 46,237,53760
mvaa 72,76,84
dl 231,44,54,3072
mvaa 72,72,8
s_add_u32 m0,s98,32768
ta 40,237,36864
s_add_u32 s54,s54,s58
mvaa 144,72,12
ta 42,237,37376
mvaa 144,76,88
ta 192,234,40960
mvaa 148,64,88
ta 194,234,41472
mvaa 148,68,12
ta 176,234,57344
mvaa 156,68,16
dl 224,36,52,0
mvaa 156,64,92
ta 178,234,57856
mvaa 152,76,92
ta 196,235,57344
mvaa 152,72,16
ta 198,235,57856
mvaa 160,208,16
ta 180,235,40960
mvaa 160,224,92
dl 225,36,52,1024
mvaa 164,212,92
ta 182,235,41472
mvaa 164,228,16
ta 52,236,40960
mvaa 172,228,12
ta 54,236,41472
mvaa 172,212,88
ta 48,236,57344
mvaa 168,224,88
s_mov_b64 exec,3
global_load_dword a250,v253,s[96:97]
s_mov_b64 exec,-1
mvaa 168,208,12
ta 50,236,57856
v_lshrrev_b32_e32 v253,1,v244
mvaa 176,208,8
ta 60,237,57344
mvaa 176,224,84
ta 62,237,57856
mvaa 180,212,84
ta 56,237,40960
mvaa 180,228,8
dl 226,36,52,2048
mvaa 188,228,4
ta 58,237,41472
mvaa 188,212,80
ra 96,233,0
mvaa 184,224,80
ra 24,232,0
mvaa 184,208,4
ra 100,233,2048
s_waitcnt lgkmcnt(8)
mvaa 192,216,4
dl 227,36,52,3072
mvaa 192,232,80
s_add_u32 s52,s52,s56
s_add_u32 m0,s98,36864
ra 28,232,2048
mvaa 196,220,80
ra 104,233,4096
mvaa 196,236,4
ra 32,232,4096
mvaa 204,236,8
ta 188,238,45056
mvaa 204,220,84
ta 190,238,45568
mvaa 200,232,84
dl 224,36,52,0
mvaa 200,216,8
ta 204,238,61440
maaa 164,216,12
ta 206,238,61952
maaa 164,232,88
ta 184,239,61440
maaa 148,220,88
ta 186,239,61952
maaa 148,236,12
dl 225,36,52,1024
maaa 144,236,16
ra 108,233,6144
v_add_u32_e32 v233,0xffff0000,v233
maaa 144,220,92
ra 36,232,6144
v_add_u32_e32 v232,0xffff0000,v232
maaa 160,232,92
ta 200,239,45056
v_accvgpr_read_b32 v208,a164
maaa 160,216,16
ta 202,239,45568
v_accvgpr_read_b32 v209,a165
maaa 128,0,16
dl 226,36,52,2048
maaa 128,20,92
v_accvgpr_read_b32 v210,a166
v_accvgpr_read_b32 v211,a167
maaa 112,44,92
v_accvgpr_read_b32 v212,a148
v_accvgpr_read_b32 v213,a149
maaa 112,40,16
v_accvgpr_read_b32 v214,a150
v_accvgpr_read_b32 v215,a151
maaa 116,40,12
v_accvgpr_read_b32 v220,a144
v_accvgpr_read_b32 v221,a145
maaa 116,44,88
dl 227,36,52,3072
maaa 132,20,88
s_add_u32 s52,s52,s60
v_accvgpr_read_b32 v222,a146
v_accvgpr_read_b32 v223,a147
maaa 132,0,12
s_lshl_b32 s60,%1,7
v_accvgpr_read_b32 v216,a160
v_accvgpr_read_b32 v217,a161
maaa 136,0,8
s_and_b32 s87,s60,7552
v_accvgpr_read_b32 v218,a162
v_accvgpr_read_b32 v219,a163
maaa 136,20,84
s_add_u32 s61,s60,83914752
maaa 120,44,84
maaa 120,40,8
maaa 124,40,4
maaa 124,44,80
maaa 140,20,80
maaa 140,0,4
mvaa 40,192,4
mvaa 40,176,80
mvaa 44,196,80
mvaa 44,180,4
mvaa 52,180,8
mvaa 52,196,84
mvaa 48,176,84
mvaa 48,192,8
mvaa 56,192,12
s_waitcnt vmcnt(9) lgkmcnt(0)
s_barrier
mvaa 56,176,88
s_cmp_eq_u32 s12,0
s_cselect_b64 exec,1,0
buffer_atomic_add v247,off,s[8:11],s85 sc1
s_mov_b64 exec,-1
mvaa 60,196,88
s_add_u32 m0,s62,32768
ta 172,234,0
s_lshr_b32 s85,s86,1
mvaa 60,180,12
s_lshl_b32 s85,s85,11
ta 174,234,512
v_bfe_u32 v247,v244,2,6
mvaa 68,180,16
ta 156,234,16384
mvaa 68,196,92
ta 158,234,16896
mvaa 64,176,92
dl 228,40,53,0
mvaa 64,192,16
ta 168,235,16384
mvaa 32,52,16
ta 170,235,16896
mvaa 32,48,92
ta 152,235,0
mvaa 36,60,92
ta 154,235,512
mvaa 36,56,16
dl 229,40,53,1024
mvaa 28,56,12
s_add_u32 s53,s53,s57
ta 68,236,0
mvaa 28,60,88
ta 70,236,512
mvaa 24,48,88
ta 64,236,16384
mvaa 24,52,12
ta 66,236,16896
mvaa 16,52,8
dl 230,44,54,2048
mvaa 16,48,84
ta 76,237,16384
mvaa 20,60,84
ta 78,237,16896
mvaa 20,56,8
ta 72,237,0
mvaa 12,56,4
ta 74,237,512
mvaa 12,60,80
dl 231,44,54,3072
mvaa 8,48,80
s_add_u32 m0,s62,49152
s_add_u32 s54,s54,s57
ta 228,234,4096
mvaa 8,52,4
ta 230,234,4608
mvaa 76,4,188
ta 212,234,20480
mvaa 76,80,204
ta 214,234,20992
mvaa 96,80,184
dl 228,40,53,0
mvaa 96,4,200
ta 224,235,20480
mvaa 100,8,200
ta 226,235,20992
mvaa 100,84,184
ta 208,235,4096
mvaa 80,84,204
ta 210,235,4608
mvaa 80,8,188
dl 229,40,53,1024
mvaa 84,12,188
s_add_u32 s53,s53,s58
ta 236,236,4096
mvaa 84,88,204
ta 238,236,4608
mvaa 104,88,184
ta 220,236,20480
mvaa 104,12,200
ta 222,236,20992
mvaa 108,16,200
dl 230,44,54,2048
mvaa 108,92,184
ta 232,237,20480
mvaa 88,92,204
ta 234,237,20992
mvaa 88,16,188
ta 216,237,4096
s_waitcnt lgkmcnt(8)
mvaa 0,172,24
ta 218,237,4608
mvaa 0,156,96
dl 231,44,54,3072
mvaa 4,168,96
s_add_u32 m0,s98,65536
ta 40,234,8192
s_add_u32 s54,s54,s58
mvaa 4,152,24
ta 42,234,8704
mvaa 92,152,28
ta 44,234,24576
mvaa 92,168,100
ta 46,234,25088
mvaa 72,156,100
dl 224,36,52,0
mvaa 72,172,28
ta 20,235,24576
mvaa 144,172,32
ta 22,235,25088
mvaa 144,156,104
ta 0,235,8192
mvaa 148,168,104
ta 2,235,8704
mvaa 148,152,32
dl 225,36,52,1024
mvaa 156,152,36
ta 180,236,8192
mvaa 156,168,108
ta 182,236,8704
mvaa 152,156,108
ta 196,236,24576
mvaa 152,172,36
ta 198,236,25088
mvaa 160,68,36
dl 226,36,52,2048
mvaa 160,64,108
ta 176,237,24576
mvaa 164,76,108
ta 178,237,25088
mvaa 164,72,36
ta 192,237,8192
mvaa 172,72,32
ta 194,237,8704
mvaa 172,76,104
dl 227,36,52,3072
mvaa 168,64,104
s_add_u32 s52,s52,s56
s_add_u32 m0,s98,69632
ta 56,238,12288
mvaa 168,68,32
s_mov_b32 s98,s94
ta 58,238,12800
s_mov_b32 s99,s98
mvaa 176,68,28
s_and_b32 s94,s12,1
ta 60,238,28672
s_lshl_b32 s94,s94,14
mvaa 176,64,100
s_mul_i32 s56,s59,12288
ta 62,238,29184
mvaa 180,76,100
dl 224,36,52,0
mvaa 180,72,28
ta 48,239,28672
v_mov_b32_e32 v224,v244
mvaa 188,72,24
ta 50,239,29184
mvaa 188,76,96
ta 52,239,12288
mvaa 184,64,96
ta 54,239,12800
mvaa 184,68,24
dl 225,36,52,1024
mvaa 192,228,24
ra 80,233,0
v_add_u32_e32 v225,65536,v224
mvaa 192,212,96
ra 4,232,0
mvaa 196,224,96
ra 84,233,2048
s_waitcnt lgkmcnt(8)
mvaa 196,208,24
ra 8,232,2048
mvaa 204,208,28
dl 226,36,52,2048
mvaa 204,224,100
ra 88,233,4096
v_mov_b32_e32 v226,1065369472
mvaa 200,212,100
ra 12,232,4096
mvaa 200,228,28
ra 16,232,6144
v_add_u32_e32 v232,0x8000,v232
mvaa 208,228,32
ra 92,233,6144
v_add_u32_e32 v233,0x8000,v233
mvaa 208,212,104
dl 227,36,52,3072
mvaa 212,224,104
s_mov_b64 s[36:37],s[28:29]
s_mov_b32 s38,8388608
s_mov_b32 s39,131072
mvaa 212,208,32
s_lshl_b32 s52,s12,12
mvaa 220,208,36
mvaa 220,224,108
mvaa 216,212,108
mvaa 216,228,36
maaa 128,236,36
maaa 128,220,108
maaa 112,232,108
maaa 112,216,36
maaa 116,216,32
maaa 116,232,104
maaa 132,220,104
maaa 132,236,32
maaa 136,236,28
maaa 136,220,100
maaa 120,232,100
maaa 120,216,28
maaa 124,216,24
maaa 124,232,96
maaa 140,220,96
s_waitcnt vmcnt(8) lgkmcnt(0)
s_barrier
s_add_u32 m0,s62,0
maaa 140,236,24
dl 228,40,53,0
mvaa 40,40,24
ta 200,234,32768
mvaa 40,44,96
ta 202,234,33280
mvaa 44,20,96
ta 184,234,49152
mvaa 44,0,24
ta 186,234,49664
mvaa 52,0,28
dl 229,40,53,1024
mvaa 52,20,100
s_add_u32 s53,s53,s57
ta 204,235,49152
mvaa 48,44,100
ta 206,235,49664
mvaa 48,40,28
ta 188,235,32768
mvaa 56,40,32
ta 190,235,33280
mvaa 56,44,104
dl 230,44,54,2048
mvaa 60,20,104
ta 164,236,32768
mvaa 60,0,32
ta 166,236,33280
mvaa 68,0,36
ta 148,236,49152
mvaa 68,20,108
ta 150,236,49664
mvaa 64,44,108
dl 231,44,54,3072
mvaa 64,40,36
s_add_u32 m0,s62,16384
s_add_u32 s54,s54,s57
ta 160,237,49152
mvaa 32,180,36
ta 162,237,49664
mvaa 32,196,108
ta 144,237,32768
mvaa 36,176,108
ta 146,237,33280
mvaa 36,192,36
dl 228,40,53,0
mvaa 28,192,32
ta 152,234,36864
mvaa 28,176,104
ta 154,234,37376
mvaa 24,196,104
ta 168,234,53248
mvaa 24,180,32
ta 170,234,53760
mvaa 16,180,28
dl 229,40,53,1024
mvaa 16,196,100
s_add_u32 s53,s53,s58
ta 156,235,53248
mvaa 20,176,100
L_vs14:
s_cbranch_execz L_vr0
ta 158,235,53760
mvaa 20,192,28
ta 172,235,36864
mvaa 12,192,24
ta 174,235,37376
mvaa 12,176,96
dl 230,44,54,2048
mvaa 8,196,96
ta 208,236,36864
mvaa 8,180,24
ta 210,236,37376
mvaa 76,24,56
ta 224,236,53248
mvaa 76,96,60
ta 226,236,53760
mvaa 96,96,48
dl 231,44,54,3072
mvaa 96,24,52
s_add_u32 s54,s54,s58
ta 212,237,53248
s_add_u32 s58,s56,8192
mvaa 100,28,52
ta 214,237,53760
mvaa 100,100,48
ta 228,237,36864
mvaa 80,100,60
ta 230,237,37376
mvaa 80,28,56
ta 216,234,40960
mvaa 84,32,56
ta 218,234,41472
mvaa 84,104,60
ta 232,234,57344
mvaa 104,104,48
ta 234,234,57856
mvaa 104,32,52
ta 220,235,57344
mvaa 108,36,52
ta 222,235,57856
mvaa 108,108,48
ta 236,235,40960
mvaa 88,108,60
ta 238,235,41472
mvaa 88,36,56
ta 0,236,40960
s_waitcnt lgkmcnt(10)
mvaa 0,200,4
ta 2,236,41472
mvaa 0,184,80
ta 20,236,57344
mvaa 4,204,80
ta 22,236,57856
mvaa 4,188,4
ta 192,237,57344
mvaa 92,188,8
ta 194,237,57856
mvaa 92,204,84
ta 176,237,40960
mvaa 72,184,84
ta 178,237,41472
mvaa 72,200,8
ta 196,238,45056
mvaa 144,200,12
ta 198,238,45568
mvaa 144,184,88
ta 180,238,61440
mvaa 148,204,88
ta 182,238,61952
mvaa 148,188,12
ra 24,232,0
mvaa 156,188,16
ra 28,232,2048
mvaa 156,204,92
ra 32,232,4096
mvaa 152,184,92
ra 36,232,6144
mvaa 152,200,16
ta 188,239,61440
mvaa 160,164,16
ta 190,239,61952
mvaa 160,148,92
ta 204,239,45056
mvaa 164,160,92
ta 206,239,45568
mvaa 164,144,16
mvaa 172,144,12
mvaa 172,160,88
mvaa 168,148,88
mvaa 168,164,12
mvaa 176,164,8
mvaa 176,148,84
mvaa 180,160,84
mvaa 180,144,8
mvaa 188,144,4
mvaa 188,160,80
mvaa 184,148,80
mvaa 184,164,4
mvaa 192,152,4
mvaa 192,168,80
mvaa 196,156,80
mvaa 196,172,4
mvaa 204,172,8
mvaa 204,156,84
mvaa 200,168,84
mvaa 200,152,8
mvaa 208,152,12
mvaa 208,168,88
mvaa 212,156,88
mvaa 212,172,12
mvaa 220,172,16
mvaa 220,156,92
mvaa 216,168,92
mvaa 216,152,16
maaa 128,208,16
maaa 128,224,92
maaa 112,212,92
s_waitcnt vmcnt(0) lgkmcnt(0)
s_barrier
s_add_u32 m0,s62,32768
maaa 112,228,16
dl 228,40,53,0
maaa 116,228,12
ta 40,234,0
maaa 116,212,88
ta 42,234,512
maaa 132,224,88
ta 44,235,0
maaa 132,208,12
ta 46,235,512
maaa 136,208,8
dl 229,40,53,1024
maaa 136,224,84
s_add_u32 s53,s53,s57
ta 48,236,0
maaa 120,212,84
ta 50,236,512
maaa 120,228,8
ta 52,237,0
maaa 124,228,4
ta 54,237,512
maaa 124,212,80
dl 230,44,54,2048
maaa 140,224,80
ta 56,234,4096
maaa 140,208,4
ta 58,234,4608
mvaa 40,216,4
ta 60,235,4096
mvaa 40,232,80
ta 62,235,4608
mvaa 44,220,80
dl 231,44,54,3072
mvaa 44,236,4
s_add_u32 m0,s62,49152
ta 64,236,4096
s_add_u32 s54,s54,s57
mvaa 52,236,8
s_add_u32 s57,s56,4096
ta 66,236,4608
mvaa 52,220,84
ta 68,237,4096
mvaa 48,232,84
ta 70,237,4608
mvaa 48,216,8
buffer_load_dword v248,v250,s[8:11],0 offen sc1
mvaa 56,216,12
ta 72,234,8192
mvaa 56,232,88
ta 74,234,8704
mvaa 60,220,88
ta 76,235,8192
mvaa 60,236,12
ta 78,235,8704
mvaa 68,236,16
dl 228,40,53,0
mvaa 68,220,92
ta 96,236,8192
mvaa 64,232,92
ta 98,236,8704
mvaa 64,216,16
ta 100,237,8192
mvaa 32,0,16
ta 102,237,8704
mvaa 32,20,92
dl 229,40,53,1024
mvaa 36,192,92
s_lshl_b32 s43,s19,15
ta 104,238,12288
s_add_u32 s42,s43,16384
mvaa 36,176,16
s_add_u32 s53,s87,83898368
ta 106,238,12800
s_lshl_b32 s87,s91,8
mvaa 28,176,12
s_lshl_b32 s91,s23,2
ta 108,239,12288
s_mov_b32 s40,858993459
mvaa 28,192,88
s_mov_b32 s41,858993459
ta 110,239,12800
v_cndmask_b32_e64 v228,0,v226,s[40:41]
mvaa 24,20,88
dl 230,44,54,2048
mvaa 24,0,12
ta 144,234,16384
v_cndmask_b32_e64 v229,0,v226,s[40:41]
mvaa 16,0,8
)"
R"(ta 146,234,16896
v_cndmask_b32_e64 v230,0,v226,s[40:41]
mvaa 16,20,84
ta 164,235,16384
mvaa 20,192,84
ta 166,235,16896
mvaa 20,176,8
dl 231,44,54,3072
mvaa 12,176,4
s_add_u32 s54,s87,s88
ta 168,236,16384
s_lshl_b32 s87,s12,16
mvaa 12,192,80
s_add_u32 s87,s87,s54
ta 170,236,16896
s_andn2_b32 s87,s87,64
mvaa 8,20,80
s_lshl_b32 s88,s66,5
ta 244,235,24576
s_add_u32 s63,s87,s88
mvaa 8,0,4
s_lshl_b32 s88,s67,5
ta 246,235,25088
s_add_u32 s65,s87,s88
mvaa 76,4,196
s_and_b32 s87,s20,4
ta 212,235,20480
s_lshl_b32 s87,s87,8
mvaa 76,80,180
s_and_b32 s88,s20,3
ta 214,235,20992
s_add_u32 s85,s85,s87
mvaa 96,80,188
s_add_u32 s85,s85,s89
ta 236,237,20480
s_lshl_b32 s89,s88,20
mvaa 96,4,204
s_add_u32 s85,s85,s89
ta 238,237,20992
s_and_b32 s89,s86,7
mvaa 100,8,204
s_add_u32 s49,s49,s87
ta 216,236,20480
s_lshl_b32 s89,s89,7
mvaa 100,84,188
s_add_u32 s49,s49,s89
ta 218,236,20992
s_lshl_b32 s89,s88,18
mvaa 80,84,180
s_add_u32 s49,s49,s89
ta 192,234,20480
s_lshl_b32 s89,s12,6
mvaa 80,8,196
s_add_u32 s89,s89,83886080
ta 194,234,20992
s_add_u32 s91,s91,s89
mvaa 84,12,196
s_mov_b32 s46,16384
ra 20,233,0
s_mov_b32 s86,268435456
mvaa 84,88,180
s_bfe_u32 s87,s98,327691
s_cselect_b32 s87,2048,0
ra 0,232,32768
v_cndmask_b32_e64 v231,0,v226,s[40:41]
mvaa 104,88,188
s_mov_b32 s40,0xf0000
ra 80,233,4096
s_mov_b32 s41,0xfff00ff
mvaa 104,12,204
s_sub_u32 s98,s98,s87
ra 4,233,32768
v_cndmask_b32_e64 v226,0,v252,s[40:41]
mvaa 108,16,204
s_mov_b32 s40,0x3f0003
ra 84,232,36864
s_mov_b32 s41,0x3fff03ff
mvaa 108,92,188
s_sub_u32 s47,32768,s43
ra 8,233,2048
v_cndmask_b32_e64 v227,0,v252,s[40:41]
mvaa 88,92,180
s_mov_b32 s40,0xfff0ffff
ra 88,233,36864
s_mov_b32 s41,0xf000ff00
mvaa 88,16,196
s_add_u32 s47,s47,32768
ra 12,232,34816
s_lshr_b32 s45,s15,1
s_waitcnt lgkmcnt(10)
mvaa 0,40,24
s_xor_b32 s62,s63,128
ta 188,237,16384
s_xor_b32 s64,s65,128
mvaa 4,44,24
s_ff1_i32_b32 s80,s45
ta 190,237,16896
s_sub_u32 s44,s56,2048
mvaa 92,44,28
ra 92,233,6144
mvaa 72,40,28
ra 16,233,34816
mvaa 144,40,32
mvaa 148,44,32
mvaa 156,44,36
mvaa 152,40,36
mvaa 160,48,36
ra 44,232,38912
v_mov_b32_e32 v232,0xc1a00000
mvaa 164,52,36
ra 40,233,38912
v_mov_b32_e32 v233,0xc1a00000
mvaa 172,52,32
mvaa 168,48,32
mvaa 176,48,28
mvaa 180,52,28
mvaa 188,52,24
mvaa 184,48,24
mvaa 192,56,24
ta 52,239,28672
mvaa 196,60,24
ta 54,239,29184
mvaa 204,60,28
mvaa 200,56,28
mvaa 208,56,32
mvaa 212,60,32
mvaa 220,60,36
mvaa 216,56,36
maaa 128,64,36
maaa 112,68,36
maaa 116,68,32
maaa 132,64,32
maaa 136,64,28
maaa 120,68,28
maaa 124,68,24
maaa 140,64,24
mvaa 40,72,24
mvaa 44,76,24
ta 64,238,28672
ta 66,238,29184
mvaa 52,76,28
s_waitcnt vmcnt(4)
v_and_b32_e32 v248,v242,v248
v_cmp_ne_u32_e64 s[100:101],v243,v248
mvaa 48,72,28
s_waitcnt vmcnt(0)
s_barrier
ta 148,234,32768
ta 150,234,33280
ta 152,234,49152
ta 154,234,49664
ta 156,235,49152
mvaa 56,72,32
s_cmp_lg_u64 s[100:101],0
s_cbranch_scc1 L_q_spin
L_q_ok:
ta 158,235,49664
ta 160,235,32768
ta 162,235,33280
ta 172,236,32768
ta 174,236,33280
ta 176,236,49152
ta 178,236,49664
mvaa 60,76,32
buffer_load_dword v242,v247,s[8:11],s91 offen offset:0 sc1
ta 180,237,49152
ta 182,237,49664
ta 184,237,32768
ta 186,237,33280
ta 196,234,36864
mvaa 68,76,36
s_mov_b32 m0,s95
ta 198,234,37376
ta 200,234,53248
ta 202,234,53760
ta 204,235,53248
ta 206,235,53760
ta 208,235,36864
ta 210,235,37376
mvaa 64,72,36
s_lshl_b32 s35,s90,2
ta 220,236,36864
ta 222,236,37376
ta 224,236,53248
mvaa 32,96,36
s_lshl_b32 s90,s22,2
ta 226,236,53760
ta 228,237,53248
mvaa 36,100,36
s_add_u32 s90,s90,s89
ta 230,237,53760
ta 232,237,36864
mvaa 28,100,32
ds 249,8,42,0
ta 234,237,37376
ta 240,235,40960
mvaa 24,96,32
s_add_u32 s35,s35,83886080
ta 242,235,41472
ta 48,239,45056
mvaa 16,96,28
ta 50,239,45568
ta 60,239,61440
mvaa 20,100,28
ta 62,239,61952
ta 56,238,61440
mvaa 12,100,24
ta 58,238,61952
ta 68,238,45056
mvaa 8,96,24
ds 249,8,42,1024
ta 70,238,45568
ta 76,237,24576
mvaa 76,24,104
ta 78,237,25088
ta 72,237,40960
mvaa 96,24,108
ta 74,237,41472
ta 100,237,57344
mvaa 100,28,108
ta 102,237,57856
ta 96,236,57344
mvaa 80,28,104
ta 98,236,57856
ta 24,236,40960
mvaa 84,32,104
ds 249,8,42,2048
ta 26,236,41472
mvaa 104,32,108
ta 28,236,24576
ta 30,236,25088
mvaa 108,36,108
v_lshl_add_u32 v238,v244,3,s4
v_min_u32_e32 v238,L_att_end-L_att_pc-4,v238
mvaa 88,36,104
ta 32,234,24576
ta 34,234,25088
s_waitcnt lgkmcnt(15)
mvaa 0,144,20
ta 108,234,40960
ta 110,234,41472
mvaa 0,148,0
ds 249,8,42,3072
ta 36,234,57344
ta 38,234,57856
ta 104,235,57344
ta 106,235,57856
mvaa 0,152,4
v_mov_b32_e32 v234,0xc1a00000
v_mov_b32_e32 v235,0xc1a00000
mvaa 4,156,4
v_cndmask_b32_e64 v250,0,v252,s[40:41]
s_mov_b32 s40,0xffc0fffc
mvaa 4,160,0
s_mov_b32 s41,0xc000fc00
v_bfe_i32 v248,v244,4,1
v_mul_f32_e32 v0,0x3e0293ee,v0
mvaa 4,164,20
v_mul_f32_e32 v1,0x3e0293ee,v1
v_mul_f32_e32 v2,0x3e0293ee,v2
mvaa 92,164,8
buffer_load_dword v240,v247,s[8:11],s90 offen offset:0 sc1
mvaa 92,160,12
v_mul_f32_e32 v3,0x3e0293ee,v3
cv 0,0,1
mvaa 92,156,16
v_mul_f32_e32 v4,0x3e0293ee,v4
cv 1,2,3
mvaa 72,152,16
v_mul_f32_e32 v5,0x3e0293ee,v5
s_waitcnt lgkmcnt(0)
s_barrier
mvaa 72,148,12
s_mov_b32 m0,s70
v_mul_f32_e32 v6,0x3e0293ee,v6
v_mul_f32_e32 v7,0x3e0293ee,v7
mvaa 72,144,8
buffer_load_dword v241,v247,s[8:11],s90 offen offset:256 sc1
mvaa 144,144,80
cv 2,4,5
cv 3,6,7
mvaa 144,148,84
v_mul_f32_e32 v92,0x3e0293ee,v92
v_mul_f32_e32 v93,0x3e0293ee,v93
mvaa 144,152,88
v_mul_f32_e32 v94,0x3e0293ee,v94
v_mul_f32_e32 v72,0x3e0293ee,v72
mvaa 148,156,88
v_mul_f32_e32 v95,0x3e0293ee,v95
v_mul_f32_e32 v73,0x3e0293ee,v73
mvaa 148,160,84
ds 249,8,43,0
mvaa 148,164,80
v_mul_f32_e32 v74,0x3e0293ee,v74
v_mul_f32_e32 v75,0x3e0293ee,v75
mvaa 156,164,92
cv 72,72,73
cv 73,74,75
mvaa 156,160,44
cv 74,92,93
cv 75,94,95
mvaa 156,156,40
v_mul_f32_e32 v144,0x3e0293ee,v144
v_mul_f32_e32 v145,0x3e0293ee,v145
mvaa 152,152,40
ds 249,8,43,1024
mvaa 152,148,44
v_mul_f32_e32 v146,0x3e0293ee,v146
v_mul_f32_e32 v147,0x3e0293ee,v147
mvaa 152,144,92
cv 144,144,145
v_mul_f32_e32 v148,0x3e0293ee,v148
mvaa 160,168,92
cv 145,146,147
v_mul_f32_e32 v149,0x3e0293ee,v149
mvaa 160,172,44
v_mul_f32_e32 v150,0x3e0293ee,v150
v_mul_f32_e32 v151,0x3e0293ee,v151
mvaa 160,176,40
ds 249,8,43,2048
mvaa 164,180,40
cv 146,148,149
cv 147,150,151
mvaa 164,184,44
v_mul_f32_e32 v156,0x3e0293ee,v156
v_mul_f32_e32 v157,0x3e0293ee,v157
mvaa 164,188,92
v_mul_f32_e32 v152,0x3e0293ee,v152
v_mul_f32_e32 v158,0x3e0293ee,v158
mvaa 172,188,80
L_vs15:
s_cbranch_execz L_vr0
v_mul_f32_e32 v159,0x3e0293ee,v159
v_mul_f32_e32 v153,0x3e0293ee,v153
mvaa 172,184,84
ds 249,8,43,3072
s_mov_b32 m0,s72
mvaa 172,180,88
v_mul_f32_e32 v154,0x3e0293ee,v154
v_mul_f32_e32 v155,0x3e0293ee,v155
mvaa 168,176,88
cv 152,152,153
cv 153,154,155
mvaa 168,172,84
ds_write_b128 v251,v[0:3] offset:0
mvaa 168,168,80
cv 154,156,157
cv 155,158,159
mvaa 176,168,8
ds 255,8,42,0
mvaa 176,172,12
v_mul_f32_e32 v160,0x3e0293ee,v160
v_mul_f32_e32 v161,0x3e0293ee,v161
mvaa 176,176,16
v_mul_f32_e32 v162,0x3e0293ee,v162
v_mul_f32_e32 v163,0x3e0293ee,v163
mvaa 180,180,16
cv 160,160,161
v_mul_f32_e32 v164,0x3e0293ee,v164
mvaa 180,184,12
v_mul_f32_e32 v165,0x3e0293ee,v165
cv 161,162,163
mvaa 180,188,8
ds 255,8,42,1024
ds_write_b128 v251,v[72:75] offset:4096
mvaa 188,188,20
v_mul_f32_e32 v166,0x3e0293ee,v166
v_mul_f32_e32 v167,0x3e0293ee,v167
mvaa 188,184,0
cv 162,164,165
cv 163,166,167
mvaa 188,180,4
v_mul_f32_e32 v172,0x3e0293ee,v172
v_mul_f32_e32 v173,0x3e0293ee,v173
mvaa 184,176,4
v_mul_f32_e32 v174,0x3e0293ee,v174
v_mul_f32_e32 v175,0x3e0293ee,v175
mvaa 184,172,0
ds 255,8,42,2048
mvaa 184,168,20
v_mul_f32_e32 v168,0x3e0293ee,v168
v_mul_f32_e32 v169,0x3e0293ee,v169
mvaa 192,192,20
ds_write_b128 v251,v[144:147] offset:8192
mvaa 192,196,0
v_mul_f32_e32 v170,0x3e0293ee,v170
v_mul_f32_e32 v171,0x3e0293ee,v171
mvaa 192,200,4
cv 168,168,169
cv 169,170,171
mvaa 196,204,4
ds 255,8,42,3072
mvaa 196,208,0
cv 170,172,173
cv 171,174,175
mvaa 196,212,20
v_mul_f32_e32 v176,0x3e0293ee,v176
v_mul_f32_e32 v177,0x3e0293ee,v177
mvaa 204,212,8
v_mul_f32_e32 v178,0x3e0293ee,v178
v_mul_f32_e32 v179,0x3e0293ee,v179
mvaa 204,208,12
ds_write_b128 v251,v[152:155] offset:12288
mvaa 204,204,16
buffer_load_dword v243,v247,s[8:11],s91 offen offset:256 sc1
mvaa 200,200,16
cv 176,176,177
cv 177,178,179
mvaa 200,196,12
v_mul_f32_e32 v180,0x3e0293ee,v180
v_mul_f32_e32 v181,0x3e0293ee,v181
mvaa 200,192,8
v_mul_f32_e32 v182,0x3e0293ee,v182
v_mul_f32_e32 v183,0x3e0293ee,v183
mvaa 208,192,80
cv 178,180,181
cv 179,182,183
mvaa 208,196,84
v_mul_f32_e32 v188,0x3e0293ee,v188
v_mul_f32_e32 v189,0x3e0293ee,v189
mvaa 208,200,88
ds_write_b128 v251,v[160:163] offset:13312
mvaa 212,204,88
v_mul_f32_e32 v190,0x3e0293ee,v190
v_mul_f32_e32 v191,0x3e0293ee,v191
mvaa 212,208,84
v_mul_f32_e32 v184,0x3e0293ee,v184
v_mul_f32_e32 v185,0x3e0293ee,v185
mvaa 212,212,80
v_mul_f32_e32 v186,0x3e0293ee,v186
v_mul_f32_e32 v187,0x3e0293ee,v187
mvaa 220,212,92
cv 184,184,185
cv 185,186,187
mvaa 220,208,44
cv 186,188,189
cv 187,190,191
mvaa 220,204,40
v_mul_f32_e32 v192,0x3e0293ee,v192
v_mul_f32_e32 v193,0x3e0293ee,v193
mvaa 216,200,40
ds_write_b128 v251,v[168:171] offset:9216
mvaa 216,196,44
v_mul_f32_e32 v194,0x3e0293ee,v194
v_mul_f32_e32 v195,0x3e0293ee,v195
mvaa 216,192,92
cv 192,192,193
cv 193,194,195
maaa 128,216,92
v_mul_f32_e32 v196,0x3e0293ee,v196
v_mul_f32_e32 v197,0x3e0293ee,v197
maaa 128,220,44
v_mul_f32_e32 v198,0x3e0293ee,v198
v_mul_f32_e32 v199,0x3e0293ee,v199
maaa 128,224,40
cv 194,196,197
cv 195,198,199
maaa 112,228,40
v_mul_f32_e32 v204,0x3e0293ee,v204
v_mul_f32_e32 v205,0x3e0293ee,v205
maaa 112,232,44
ds_write_b128 v251,v[176:179] offset:5120
maaa 112,236,92
v_mul_f32_e32 v206,0x3e0293ee,v206
v_mul_f32_e32 v207,0x3e0293ee,v207
maaa 116,236,80
v_mul_f32_e32 v200,0x3e0293ee,v200
v_mul_f32_e32 v201,0x3e0293ee,v201
maaa 116,232,84
v_mul_f32_e32 v202,0x3e0293ee,v202
v_mul_f32_e32 v203,0x3e0293ee,v203
maaa 116,228,88
cv 200,200,201
cv 201,202,203
maaa 132,224,88
cv 202,204,205
cv 203,206,207
maaa 132,220,84
v_mul_f32_e32 v208,0x3e0293ee,v208
v_mul_f32_e32 v209,0x3e0293ee,v209
maaa 132,216,80
ds_write_b128 v251,v[184:187] offset:1024
maaa 136,216,8
v_mul_f32_e32 v210,0x3e0293ee,v210
v_mul_f32_e32 v211,0x3e0293ee,v211
maaa 136,220,12
cv 208,208,209
cv 209,210,211
maaa 136,224,16
v_mul_f32_e32 v212,0x3e0293ee,v212
v_mul_f32_e32 v213,0x3e0293ee,v213
maaa 120,228,16
s_waitcnt vmcnt(11)
s_barrier
maaa 120,232,12
v_mul_f32_e32 v214,0x3e0293ee,v214
v_mul_f32_e32 v215,0x3e0293ee,v215
maaa 120,236,8
cv 210,212,213
cv 211,214,215
maaa 124,236,20
ds_write_b128 v251,v[192:195] offset:2048
maaa 124,232,0
v_mul_f32_e32 v220,0x3e0293ee,v220
v_mul_f32_e32 v221,0x3e0293ee,v221
maaa 124,228,4
v_mul_f32_e32 v216,0x3e0293ee,v216
v_mul_f32_e32 v217,0x3e0293ee,v217
maaa 140,224,4
v_mul_f32_e32 v222,0x3e0293ee,v222
v_mul_f32_e32 v223,0x3e0293ee,v223
maaa 140,220,0
v_mul_f32_e32 v218,0x3e0293ee,v218
v_mul_f32_e32 v219,0x3e0293ee,v219
maaa 140,216,20
cv 216,216,217
cv 217,218,219
mvaa 88,92,64
cv 218,220,221
cv 219,222,223
mvaa 88,44,68
ds_write_b128 v251,v[200:203] offset:6144
mvaa 88,40,56
v_accvgpr_read_b32 v0,a128
v_accvgpr_read_b32 v1,a129
mvaa 108,40,60
v_accvgpr_read_b32 v2,a130
v_accvgpr_read_b32 v3,a131
mvaa 108,44,48
v_mul_f32_e32 v0,0x3e0293ee,v0
v_mul_f32_e32 v1,0x3e0293ee,v1
cv 88,88,89
cv 89,90,91
mvaa 108,92,52
buffer_store_dwordx2 v[88:89],v253,s[8:11],s99 offen offset:1024 sc0 sc1
mvaa 36,76,92
v_mul_f32_e32 v2,0x3e0293ee,v2
v_mul_f32_e32 v3,0x3e0293ee,v3
mvaa 36,72,44
cv 0,0,1
cv 1,2,3
mvaa 36,100,40
ds_write_b128 v251,v[208:211] offset:10240
mvaa 32,96,40
cv 108,108,109
cv 109,110,111
mvaa 32,24,44
buffer_store_dwordx2 v[108:109],v253,s[8:11],s99 offen offset:1536 sc0 sc1
mvaa 32,28,92
v_accvgpr_read_b32 v4,a112
v_accvgpr_read_b32 v5,a113
mvaa 64,32,92
v_accvgpr_read_b32 v6,a114
v_mul_f32_e32 v4,0x3e0293ee,v4
mvaa 64,108,44
v_mul_f32_e32 v5,0x3e0293ee,v5
v_mul_f32_e32 v6,0x3e0293ee,v6
mvaa 64,36,40
cv 32,32,33
cv 33,34,35
cv 34,36,37
cv 35,38,39
mvaa 68,104,40
buffer_store_dwordx4 v[32:35],v244,s[8:11],s99 offen sc0 sc1
ds_write_b128 v251,v[216:219] offset:14336
mvaa 68,240,44
v_accvgpr_read_b32 v7,a115
v_mul_f32_e32 v7,0x3e0293ee,v7
mvaa 68,244,92
cv 2,4,5
cv 3,6,7
mvaa 60,244,80
ra 192,224,0
v_accvgpr_read_b32 v92,a116
mvaa 60,240,84
ra 196,224,1024
v_accvgpr_read_b32 v93,a117
mvaa 60,104,88
ra 200,224,2048
v_mul_f32_e32 v92,0x3e0293ee,v92
mvaa 56,36,88
ra 204,224,3072
v_mul_f32_e32 v93,0x3e0293ee,v93
mvaa 56,108,84
ds_write_b128 v251,v[0:3] offset:15360
mvaa 56,32,80
v_accvgpr_read_b32 v94,a118
v_accvgpr_read_b32 v95,a119
mvaa 24,28,80
v_accvgpr_read_b32 v72,a132
v_accvgpr_read_b32 v73,a133
mvaa 24,24,84
v_accvgpr_read_b32 v74,a134
v_mul_f32_e32 v94,0x3e0293ee,v94
mvaa 24,96,88
v_mul_f32_e32 v95,0x3e0293ee,v95
v_mul_f32_e32 v72,0x3e0293ee,v72
mvaa 28,100,88
v_mul_f32_e32 v73,0x3e0293ee,v73
v_mul_f32_e32 v74,0x3e0293ee,v74
v_accvgpr_read_b32 v75,a135
mvaa 28,72,84
v_mul_f32_e32 v75,0x3e0293ee,v75
cv 72,72,73
cv 73,74,75
cv 74,92,93
mvaa 28,76,80
ra 208,224,4096
cv 75,94,95
mvaa 20,76,8
ds_write_b128 v251,v[72:75] offset:11264
mvaa 20,72,12
v_accvgpr_read_b32 v144,a136
v_accvgpr_read_b32 v145,a137
mvaa 20,100,16
s_mov_b64 exec,3
global_load_dword v239,v238,s[96:97]
s_mov_b64 exec,-1
ra 212,224,5120
ra 248,224,14336
ra 252,224,15360
mvaa 16,96,16
v_accvgpr_read_b32 v146,a138
v_mul_f32_e32 v144,0x3e0293ee,v144
v_mul_f32_e32 v145,0x3e0293ee,v145
v_mul_f32_e32 v146,0x3e0293ee,v146
v_accvgpr_read_b32 v147,a139
v_mul_f32_e32 v147,0x3e0293ee,v147
cv 144,144,145
mvaa 16,24,12
ra 236,224,11264
v_accvgpr_read_b32 v148,a120
v_accvgpr_read_b32 v149,a121
v_accvgpr_read_b32 v150,a122
cv 145,146,147
mvaa 16,28,8
v_mul_f32_e32 v148,0x3e0293ee,v148
v_mul_f32_e32 v149,0x3e0293ee,v149
v_mul_f32_e32 v150,0x3e0293ee,v150
v_accvgpr_read_b32 v151,a123
v_mul_f32_e32 v151,0x3e0293ee,v151
cv 146,148,149
mvaa 48,32,8
ra 228,224,9216
cv 147,150,151
mvaa 48,108,12
ds_write_b128 v251,v[144:147] offset:7168
mvaa 48,36,16
v_accvgpr_read_b32 v156,a124
v_accvgpr_read_b32 v157,a125
mvaa 52,104,16
ra 232,224,10240
ra 216,224,6144
ra 220,224,7168
ra 224,224,8192
v_accvgpr_read_b32 v158,a126
v_accvgpr_read_b32 v159,a127
mvaa 52,240,12
v_accvgpr_read_b32 v152,a140
v_mul_f32_e32 v156,0x3e0293ee,v156
v_mul_f32_e32 v157,0x3e0293ee,v157
v_mul_f32_e32 v158,0x3e0293ee,v158
v_mul_f32_e32 v159,0x3e0293ee,v159
mvaa 52,244,8
v_mul_f32_e32 v152,0x3e0293ee,v152
v_accvgpr_read_b32 v153,a141
v_mul_f32_e32 v153,0x3e0293ee,v153
v_accvgpr_read_b32 v154,a142
v_accvgpr_read_b32 v155,a143
mvaa 44,244,20
v_mul_f32_e32 v154,0x3e0293ee,v154
v_mul_f32_e32 v155,0x3e0293ee,v155
cv 152,152,153
cv 153,154,155
cv 154,156,157
mvaa 44,240,0
ra 244,224,13312
cv 155,158,159
mvaa 44,104,4
ds_write_b128 v251,v[152:155] offset:3072
mvaa 40,36,4
cv 64,64,65
cv 56,56,57
mvaa 40,108,0
cv 57,58,59
cv 58,60,61
mvaa 40,32,20
ra 240,224,12288
cv 59,62,63
mvaa 8,28,20
cv 16,16,17
cv 17,18,19
mvaa 8,24,0
cv 48,48,49
cv 194,44,45
mvaa 8,96,4
cv 195,46,47
cv 192,40,41
mvaa 12,100,4
cv 193,42,43
cv 49,50,51
mvaa 12,72,0
s_cmp_eq_u32 s19,0
s_cselect_b32 s76,32768,0
s_cselect_b32 s77,49152,s86
s_cselect_b32 s78,65536,s46
s_cselect_b32 s79,81920,s86
mvaa 12,76,20
s_add_u32 s86,s95,98304
cv 8,8,9
v_add_u32_e32 v236,s86,v244
mvaa 76,20,64
s_add_u32 s46,s46,16384
v_xor_b32_e32 v237,4096,v236
s_waitcnt lgkmcnt(0)
s_barrier
s_mov_b32 m0,s68
mvaa 76,0,68
ds 249,8,76,0
ra 128,237,32768
ra 132,237,33792
mvaa 76,4,56
s_add_u32 s95,s47,32768
ra 136,237,34816
cv 9,10,11
mvaa 96,4,60
ra 140,237,35840
ra 144,237,49152
mvaa 96,0,48
cv 10,12,13
cv 11,14,15
mvaa 96,20,52
ra 148,237,50176
cv 50,52,53
mvaa 100,8,52
ds 249,8,76,1024
ra 152,237,51200
ra 156,237,52224
mvaa 100,12,48
ra 176,236,16384
cv 51,54,55
mvaa 100,16,60
ra 180,236,17408
cv 18,20,21
mvaa 80,16,56
ra 184,236,18432
cv 19,22,23
mvaa 80,12,68
ra 188,236,19456
cv 24,24,25
mvaa 80,8,64
ds 249,8,76,2048
ra 160,236,0
mvaa 84,80,64
ra 164,236,1024
cv 25,26,27
mvaa 84,84,68
ra 168,236,2048
ra 172,236,3072
mvaa 84,88,56
cv 26,28,29
cv 27,30,31
mvaa 104,88,60
cv 65,66,67
cv 66,68,69
mvaa 104,84,48
ds 249,8,76,3072
mvaa 104,80,52
s_mov_b32 m0,s69
cv 67,70,71
v_cndmask_b32_e64 v252,0,v252,s[40:41]
cvvv 164,8,192,112
cv 70,76,77
cv 71,78,79
cvvv 168,8,48,116
ds 249,8,77,0
cvvv 172,16,48,112
cv 94,96,97
v_mov_b32_e32 v76,v88
cvvv 176,16,56,116
cv 95,98,99
s_waitcnt vmcnt(9) lgkmcnt(0)
s_barrier
cvvv 180,24,56,112
ds 249,8,77,1024
cvvv 184,24,64,116
ra 0,224,32768
cv 96,100,101
cvvv 188,32,64,112
ra 4,224,33792
cv 97,102,103
cvaa 0,192,128,232
buffer_load_dword v17,v247,s[8:11],s35 offen offset:64 sc1
mvaa 0,200,132
ra 8,224,34816
cv 72,80,81
mvaa 0,208,136
ra 12,224,35840
cv 73,82,83
mvaa 0,216,140
ds 249,8,77,2048
cvaa 4,220,140,232
ra 16,224,36864
cv 74,84,85
mvaa 4,212,136
ra 20,224,37888
v_max3_f32 v172,v168,v169,v172
mvaa 4,204,132
ds 249,8,77,3072
mvaa 4,196,128
s_mov_b32 m0,s74
ra 24,224,38912
v_max_f32_e32 v172,v172,v173
cvaa 32,224,128,232
ra 28,224,39936
v_max3_f32 v174,v170,v171,v174
mvaa 32,232,132
buffer_load_dword v19,v247,s[8:11],s35 offen offset:192 sc1
mvaa 32,240,136
ra 32,224,40960
v_max_f32_e32 v174,v174,v175
mvaa 32,248,140
ra 36,224,41984
ra 40,224,43008
cvaa 36,252,140,232
buffer_load_dword v18,v247,s[8:11],s35 offen offset:128 sc1
ra 44,224,44032
mvaa 36,244,136
ra 48,224,45056
v_max_f32_e32 v148,v172,v174
mvaa 36,236,132
ra 56,224,47104
ra 60,224,48128
mvaa 36,228,128
buffer_load_dword v16,v247,s[8:11],s35 offen offset:0 sc1
ra 52,224,46080
cvaa 8,192,144,232
v_mov_b32_e32 v149,v148
cv 75,86,87
mvaa 8,200,148
v_permlane16_swap_b32_e32 v148,v149
mvaa 8,208,152
ds 255,8,43,0
mvaa 8,216,156
v_max_f32_e32 v148,v148,v149
v_mov_b32_e32 v149,v148
cvaa 12,220,156,232
cv 98,104,105
v_max3_f32 v180,v176,v177,v180
mvaa 12,212,152
ds 255,8,43,1024
mvaa 12,204,148
v_permlane32_swap_b32_e32 v148,v149
mvaa 12,196,144
L_vs16:
s_cbranch_execz L_vr0
v_max_f32_e32 v148,v148,v149
s_waitcnt lgkmcnt(0)
s_barrier
cvaa 40,224,144,232
ds 255,8,43,2048
mvaa 40,232,148
ra 64,225,0
v_mul_f32_e32 v148,0xbe8293ee,v148
mvaa 40,240,152
ra 68,225,1024
v_fmamk_f32 v172,v172,0x3e8293ee,v148
mvaa 40,248,156
ds 255,8,43,3072
cvaa 44,252,156,232
s_mov_b32 m0,s70
ra 72,225,2048
v_fmamk_f32 v174,v174,0x3e8293ee,v148
mvaa 44,244,152
ra 76,225,3072
v_max3_f32 v182,v178,v179,v182
mvaa 44,236,148
ds 249,8,78,0
mvaa 44,228,144
ra 80,225,4096
v_max_f32_e32 v180,v180,v181
cvaa 24,192,176,128
ra 84,225,5120
v_max_f32_e32 v182,v182,v183
mvaa 24,200,180
ds 249,8,78,1024
mvaa 24,208,184
ra 88,225,6144
v_max_f32_e32 v152,v180,v182
mvaa 24,216,188
ra 92,225,7168
v_mov_b32_e32 v153,v152
cvaa 28,220,188,132
ds 249,8,78,2048
mvaa 28,212,184
ra 96,225,8192
v_max3_f32 v188,v184,v185,v188
mvaa 28,204,180
ra 100,225,9216
ra 104,225,10240
mvaa 28,196,176
ds 249,8,78,3072
ra 108,225,11264
cvaa 56,224,176,136
s_mov_b32 m0,s71
ra 112,225,12288
v_max3_f32 v190,v186,v187,v190
mvaa 56,232,180
ra 116,225,13312
ra 120,225,14336
mvaa 56,240,184
ds 249,8,79,0
ra 124,225,15360
mvaa 56,248,188
ex 172
cvaa 60,252,188,140
ex 174
mvaa 60,244,184
ds 249,8,79,1024
mvaa 60,236,180
v_permlane16_swap_b32_e32 v152,v153
mvaa 60,228,176
cv 172,172,172
cv 174,174,174
cvaa 80,0,160,128
ds 249,8,79,2048
mvaa 80,8,164
v_and_b32_e32 v168,v226,v172
v_max_f32_e32 v152,v152,v153
mvaa 80,16,168
v_and_b32_e32 v169,v227,v174
v_and_b32_e32 v170,v250,v172
v_and_b32_e32 v171,v252,v174
mvaa 80,24,172
ds 249,8,79,3072
v_mov_b32_e32 v153,v152
cvaa 84,28,172,132
v_max_f32_e32 v188,v188,v189
v_permlane32_swap_b32_e32 v152,v153
v_max_f32_e32 v152,v152,v153
mvaa 84,20,168
v_mul_f32_e32 v152,0xbe8293ee,v152
v_fmamk_f32 v180,v180,0x3e8293ee,v152
mvaa 84,12,164
v_fmamk_f32 v182,v182,0x3e8293ee,v152
v_max_f32_e32 v190,v190,v191
mvaa 84,4,160
ex 180
cvaa 112,32,160,136
ex 182
v_max_f32_e32 v156,v188,v190
mvaa 112,40,164
v_mov_b32_e32 v157,v156
cv 180,180,180
mvaa 112,48,168
v_permlane16_swap_b32_e32 v156,v157
mvaa 112,56,172
cv 182,182,182
v_and_b32_e32 v176,v226,v180
v_max_f32_e32 v156,v156,v157
s_waitcnt vmcnt(15) lgkmcnt(0)
s_barrier
s_mov_b32 m0,s72
cvaa 116,60,172,140
ds 255,8,76,0
v_and_b32_e32 v177,v227,v182
mvaa 116,52,168
v_and_b32_e32 v178,v250,v180
v_and_b32_e32 v179,v252,v182
v_mov_b32_e32 v157,v156
mvaa 116,44,164
cv 99,106,107
v_permlane32_swap_b32_e32 v156,v157
v_max_f32_e32 v156,v156,v157
mvaa 116,36,160
ds 255,8,76,1024
v_mul_f32_e32 v156,0xbe8293ee,v156
zvvv 148,228,168
v_fmamk_f32 v188,v188,0x3e8293ee,v156
ex 188
zvvv 200,70,168
v_fmamk_f32 v190,v190,0x3e8293ee,v156
ex 190
zvvv 204,94,168
ds 255,8,76,2048
zvvv 152,228,176
cv 188,188,188
cv 190,190,190
v_and_b32_e32 v184,v226,v188
zvvv 208,72,176
v_and_b32_e32 v185,v227,v190
v_and_b32_e32 v186,v250,v188
v_and_b32_e32 v187,v252,v190
zvvv 212,96,176
ds 255,8,76,3072
s_mov_b32 m0,s73
v_mov_b32_e32 v77,v89
zvvv 156,228,184
v_mov_b32_e32 v100,v108
v_mov_b32_e32 v101,v109
)"
R"(zvvv 216,74,184
ex 0
zvvv 220,98,184
ds 255,8,77,0
cvaa 64,0,128,232
ex 1
mvaa 64,8,132
ex 2
mvaa 64,16,136
ds 255,8,77,1024
mvaa 64,24,140
ds 255,8,77,2048
v_mov_b32_e32 v170,v70
v_mov_b32_e32 v171,v71
cvaa 68,28,140,232
ex 3
mvaa 68,20,136
ex 4
mvaa 68,12,132
ds 255,8,77,3072
mvaa 68,4,128
ex 5
v_and_b32_e32 v248,2863311530,v248
cvaa 96,32,128,232
ex 6
mvaa 96,40,132
v_sub_f32_e32 v17,1.0,v17
v_rcp_f32_e32 v148,v148
mvaa 96,48,136
v_mul_f32_e32 v148,v148,v17
s_waitcnt vmcnt(21)
v_sub_f32_e32 v18,1.0,v18
v_sub_f32_e32 v19,1.0,v19
mvaa 96,56,140
v_mul_f32_e32 v200,v148,v200
v_mul_f32_e32 v201,v148,v201
v_mul_f32_e32 v202,v148,v202
cvaa 100,60,140,232
v_mul_f32_e32 v203,v148,v203
cv 200,200,201
mvaa 100,52,136
v_mul_f32_e32 v204,v148,v204
v_mul_f32_e32 v205,v148,v205
cv 201,202,203
mvaa 100,44,132
v_mul_f32_e32 v206,v148,v206
v_mul_f32_e32 v207,v148,v207
cv 202,204,205
mvaa 100,36,128
buffer_load_dwordx4 v[176:179],v244,s[8:11],s98 offen sc1
cv 203,206,207
cvaa 72,0,144,232
v_rcp_f32_e32 v152,v152
mvaa 72,8,148
v_mul_f32_e32 v152,v152,v18
v_mul_f32_e32 v208,v152,v208
v_mul_f32_e32 v209,v152,v209
mvaa 72,16,152
buffer_load_dwordx2 v[168:169],v253,s[8:11],s98 offen offset:1024 sc1
v_mul_f32_e32 v210,v152,v210
mvaa 72,24,156
v_mul_f32_e32 v211,v152,v211
cv 208,208,209
cv 209,210,211
cvaa 76,28,156,232
v_mul_f32_e32 v212,v152,v212
v_mul_f32_e32 v213,v152,v213
mvaa 76,20,152
buffer_load_dwordx2 v[172:173],v253,s[8:11],s98 offen offset:1536 sc1
v_mul_f32_e32 v214,v152,v214
mvaa 76,12,148
v_mul_f32_e32 v215,v152,v215
v_rcp_f32_e32 v156,v156
mvaa 76,4,144
cv 210,212,213
cv 211,214,215
v_mul_f32_e32 v156,v156,v19
cvaa 104,32,144,232
buffer_store_dwordx4 v[200:203],v244,s[8:11],s34 offen sc0 sc1
mvaa 104,40,148
v_mul_f32_e32 v216,v156,v216
v_mul_f32_e32 v217,v156,v217
v_mul_f32_e32 v218,v156,v218
mvaa 104,48,152
v_mul_f32_e32 v219,v156,v219
cv 216,216,217
cv 217,218,219
mvaa 104,56,156
buffer_store_dwordx4 v[208:211],v244,s[8:11],s50 offen sc0 sc1
v_mul_f32_e32 v220,v156,v220
cvaa 108,60,156,232
v_mul_f32_e32 v221,v156,v221
v_mul_f32_e32 v222,v156,v222
mvaa 108,52,152
v_mul_f32_e32 v223,v156,v223
cv 218,220,221
cv 219,222,223
mvaa 108,44,148
buffer_store_dwordx4 v[216:219],v244,s[8:11],s51 offen sc0 sc1
v_mov_b32_e32 v160,v120
mvaa 108,36,144
v_mov_b32_e32 v161,v121
ex 7
cvaa 88,0,176,232
ex 32
mvaa 88,8,180
v_mov_b32_e32 v162,v122
ex 33
mvaa 88,16,184
v_mov_b32_e32 v175,v95
ex 34
mvaa 88,24,188
s_cmp_ge_i32 s47,s46
s_cselect_b32 s76,s95,s46
s_cselect_b32 s77,s95,268435456
v_mov_b32_e32 v174,v94
ex 35
cvaa 92,28,188,232
s_add_u32 s77,s77,16384
ex 36
mvaa 92,20,184
v_mov_b32_e32 v163,v123
ex 37
mvaa 92,12,180
cv 0,0,1
ex 38
mvaa 92,4,176
cv 1,2,3
ex 39
cvaa 120,32,176,232
buffer_load_dword v247,v246,s[24:27],s49 offen
mvaa 120,40,180
s_add_u32 s49,s49,32768
cv 2,4,5
ex 8
mvaa 120,48,184
cv 3,6,7
ex 9
mvaa 120,56,188
buffer_load_dword v247,v245,s[36:39],s85 offen
cv 4,32,33
cvaa 124,60,188,232
s_add_u32 s85,s85,131072
ex 10
mvaa 124,52,184
cv 5,34,35
ex 11
mvaa 124,44,180
buffer_load_dword v247,v246,s[24:27],s49 offen
cv 6,36,37
mvaa 124,36,176
s_cmp_lg_u32 s19,0
s_cbranch_scc1 L_j0
s_nop 1
s_lshl_b32 s86,s12,12
s_add_u32 s86,s86,131072
v_add_u32_e32 v236,s86,v244
ra 160,236,0
ra 164,236,1024
ra 168,236,2048
ra 172,236,3072
ra 176,236,16384
ra 180,236,17408
ra 184,236,18432
ra 188,236,19456
v_add_u32_e32 v224,s94,v224
s_waitcnt lgkmcnt(0)
L_j0:
cv 7,38,39
ex 12
zaav 0,64,0
s_add_u32 s49,s49,32768
ex 13
maav 0,96,4
ex 14
v_or_b32_e32 v248,1145324612,v248
zaav 4,68,0
ex 15
v_mov_b64_e32 v[144:145],0
maav 4,100,4
cv 8,8,9
ex 40
zaav 8,72,0
ex 41
maav 8,104,4
cv 9,10,11
ex 42
zaav 12,76,0
cv 10,12,13
ex 43
maav 12,108,4
cv 11,14,15
ex 44
v_smfmac_f32_16x16x64_bf16 v[144:147],v[228:231],v[0:7],v248
ex 45
zaav 16,80,0
cv 12,40,41
ex 46
maav 16,112,4
s_waitcnt vmcnt(8)
v_max3_u32 v180,v176,v177,v178
cv 13,42,43
zaav 20,84,0
s_waitcnt vmcnt(7)
v_max3_u32 v180,v180,v179,v168
s_waitcnt vmcnt(6)
v_max3_u32 v180,v180,v169,v172
v_max_u32_e32 v180,v180,v173
v_cmp_eq_u32_e64 s[40:41],-1,v180
maav 20,116,4
s_cmp_lg_u64 s[40:41],0
s_cbranch_scc1 L_bx
L_bxr:
cv 14,44,45
ex 47
mvvv 160,176,192
zaav 24,88,0
cv 15,46,47
v_sub_f32_e32 v16,1.0,v16
maav 24,120,4
ex 24
zaav 28,92,0
ex 25
maav 28,124,4
v_max3_f32 v164,v160,v161,v164
v_max_f32_e32 v164,v164,v165
v_max3_f32 v166,v162,v163,v166
v_max_f32_e32 v166,v166,v167
v_max_f32_e32 v180,v164,v166
v_mov_b32_e32 v181,v180
zaav 32,64,8
ex 26
v_permlane16_swap_b32_e32 v180,v181
v_max_f32_e32 v180,v180,v181
v_mov_b32_e32 v181,v180
maav 32,96,12
ex 27
v_permlane32_swap_b32_e32 v180,v181
v_max_f32_e32 v180,v180,v181
v_mul_f32_e32 v180,0xbe8293ee,v180
v_fmamk_f32 v164,v164,0x3e8293ee,v180
zaav 36,68,8
ex 164
v_fmamk_f32 v166,v166,0x3e8293ee,v180
ex 166
cv 164,164,164
cv 166,166,166
maav 36,100,12
v_and_b32_e32 v160,v226,v164
v_and_b32_e32 v161,v227,v166
v_and_b32_e32 v162,v250,v164
v_and_b32_e32 v163,v252,v166
zaav 40,72,8
v_mov_b64_e32 v[148:149],0
zvvv 180,228,160
maav 40,104,12
ra 192,224,0
zvvv 184,168,160
zaav 44,76,8
ra 196,224,1024
zvvv 188,172,160
maav 44,108,12
ra 200,224,2048
v_rcp_f32_e32 v180,v180
cv 24,24,25
v_mul_f32_e32 v180,v180,v16
v_mul_f32_e32 v184,v180,v184
v_mul_f32_e32 v185,v180,v185
v_smfmac_f32_16x16x64_bf16 v[148:151],v[228:231],v[8:15],v248
v_mul_f32_e32 v186,v180,v186
v_mul_f32_e32 v187,v180,v187
v_mul_f32_e32 v188,v180,v188
v_mul_f32_e32 v189,v180,v189
v_mul_f32_e32 v190,v180,v190
v_mul_f32_e32 v191,v180,v191
zaav 48,80,8
cv 184,184,185
cv 185,186,187
cv 186,188,189
cv 187,190,191
cv 25,26,27
ex 28
maav 48,112,12
buffer_store_dwordx4 v[184:187],v244,s[8:11],s33 offen sc0 sc1
ex 29
ex 30
cv 26,28,29
zaav 52,84,8
ex 31
ex 56
cv 27,30,31
ex 57
maav 52,116,12
ex 58
ex 59
ex 60
zaav 56,88,8
ex 61
maav 56,120,12
cv 28,56,57
ex 62
zaav 60,92,8
ra 204,224,3072
ra 208,224,4096
ex 63
maav 60,124,12
ra 212,224,5120
cv 29,58,59
cv 30,60,61
cv 31,62,63
zvav 192,64,24
ra 216,224,6144
ra 220,224,7168
ex 64
mvav 192,96,28
ra 224,224,8192
v_mov_b32_e32 v251,-1
ex 65
zvav 196,68,24
ra 228,224,9216
ra 232,224,10240
ex 66
mvav 196,100,28
ra 236,224,11264
v_mov_b64_e32 v[152:153],0
ex 67
zvav 200,72,24
ra 240,224,12288
ra 244,224,13312
v_mov_b64_e32 v[156:157],0
cv 64,64,65
mvav 200,104,28
ra 248,224,14336
ra 252,224,15360
ex 68
zvav 204,76,24
ex 69
ex 70
mvav 204,108,28
cv 65,66,67
ex 71
cv 66,68,69
v_smfmac_f32_16x16x64_bf16 v[156:159],v[228:231],v[24:31],v248
ex 96
ex 97
zvav 208,80,24
ex 98
ex 99
mvav 208,112,28
cv 67,70,71
cv 68,96,97
ex 100
zvav 212,84,24
ex 101
ex 102
mvav 212,116,28
cv 69,98,99
ex 103
cv 70,100,101
zvav 216,88,24
ex 72
ex 73
mvav 216,120,28
ex 74
ex 75
zvav 220,92,24
ex 76
ex 77
mvav 220,124,28
v_mov_b32_e32 v250,-1
v_mov_b32_e32 v252,-1
v_mov_b32_e32 v253,-1
cv 71,102,103
s_mov_b64 exec,0
s_bitcmp1_b32 170,s20
s_cbranch_scc1 L_vbO
s_cmp_eq_u32 s12,0
s_cbranch_scc1 L_vs18
s_cmp_eq_u32 s12,1
s_cbranch_scc1 L_vs19
s_cmp_eq_u32 s12,2
s_cbranch_scc1 L_vs20
s_branch L_vs21
L_vbO:
s_cmp_eq_u32 s12,0
s_cbranch_scc1 L_vs25
s_cmp_eq_u32 s12,1
s_cbranch_scc1 L_vs26
s_cmp_eq_u32 s12,2
s_cbranch_scc1 L_vs27
s_branch L_vs28
L_vr1:
s_mov_b64 exec,-1
s_waitcnt lgkmcnt(0)
s_barrier
cvaa 0,192,128,232
mvaa 0,200,132
s_add_u32 s81,s81,1
ex 78
mvaa 0,208,136
s_add_u32 s44,s44,1024
ex 79
mvaa 0,216,140
cv 72,72,73
cv 73,74,75
cvaa 4,220,140,232
s_mov_b32 m0,s68
ex 104
mvaa 4,212,136
ds 249,8,76,0
mvaa 4,204,132
ex 105
mvaa 4,196,128
ex 106
cvaa 32,224,128,232
ra 64,225,32768
cv 74,76,77
mvaa 32,232,132
ex 107
mvaa 32,240,136
ra 68,225,33792
cv 75,78,79
mvaa 32,248,140
ex 108
cvaa 36,252,140,232
ds 249,8,76,1024
mvaa 36,244,136
ex 109
mvaa 36,236,132
ra 72,225,34816
cv 76,104,105
mvaa 36,228,128
ex 110
cvaa 8,192,144,232
ra 76,225,35840
cv 77,106,107
mvaa 8,200,148
ex 111
mvaa 8,208,152
ex 80
mvaa 8,216,156
ds 249,8,76,2048
cvaa 12,220,156,232
ra 80,225,36864
cv 78,108,109
mvaa 12,212,152
ex 81
mvaa 12,204,148
ra 84,225,37888
cv 79,110,111
mvaa 12,196,144
ex 82
cvaa 40,224,144,232
ex 83
mvaa 40,232,148
ra 88,225,38912
cv 80,80,81
mvaa 40,240,152
ds 249,8,76,3072
mvaa 40,248,156
ex 84
cvaa 44,252,156,232
ra 92,225,39936
cv 81,82,83
mvaa 44,244,152
ex 85
mvaa 44,236,148
ex 86
mvaa 44,228,144
ra 96,225,40960
cv 82,84,85
cvaa 16,192,160,232
s_mov_b32 m0,s69
ex 87
mvaa 16,200,164
ra 100,225,41984
cv 83,86,87
mvaa 16,208,168
ds 249,8,77,0
mvaa 16,216,172
ex 112
cvaa 20,220,172,232
ex 113
mvaa 20,212,168
ra 104,225,43008
cv 84,112,113
mvaa 20,204,164
ex 114
mvaa 20,196,160
ra 108,225,44032
cvaa 48,224,160,232
ex 115
mvaa 48,232,164
ds 249,8,77,1024
mvaa 48,240,168
ex 116
mvaa 48,248,172
ra 112,225,45056
cv 85,114,115
cvaa 52,252,172,232
L_vs17:
s_cbranch_execz L_vr1
ex 117
mvaa 52,244,168
ra 116,225,46080
cv 86,116,117
mvaa 52,236,164
ex 118
mvaa 52,228,160
ex 119
cvaa 24,192,176,232
ds 249,8,77,2048
mvaa 24,200,180
ra 120,225,47104
cv 87,118,119
mvaa 24,208,184
ex 88
mvaa 24,216,188
ra 124,225,48128
cvaa 28,220,188,232
mvaa 28,212,184
ex 89
mvaa 28,204,180
ex 90
mvaa 28,196,176
ds 249,8,77,3072
cvaa 56,224,176,232
ex 91
mvaa 56,232,180
ex 92
mvaa 56,240,184
ex 93
mvaa 56,248,188
ex 94
cvaa 60,252,188,232
mvaa 60,244,184
ex 95
mvaa 60,236,180
cv 88,88,89
cv 89,90,91
mvaa 60,228,176
s_waitcnt vmcnt(8) lgkmcnt(0)
s_barrier
maav 0,64,64
ex 120
maav 0,96,68
s_mov_b32 m0,s74
ex 121
maav 4,68,64
ds 255,8,78,0
maav 4,100,68
ex 122
maav 8,72,64
ex 123
maav 8,104,68
cv 90,92,93
cv 91,94,95
maav 12,76,64
ex 124
maav 12,108,68
ex 125
v_smfmac_f32_16x16x64_bf16 v[144:147],v[228:231],v[64:71],v248
cv 92,120,121
maav 16,80,64
ex 126
maav 16,112,68
buffer_load_dword v247,v245,s[36:39],s85 offen
maav 20,84,64
ex 127
maav 20,116,68
cv 93,122,123
cv 94,124,125
maav 24,88,64
cv 95,126,127
maav 24,120,68
ra 192,224,32768
maav 28,92,64
ds 255,8,78,1024
maav 28,124,68
s_lshl_b32 s86,s19,3
s_lshr_b32 s87,s18,2
s_add_u32 s86,s86,s87
maav 32,64,72
ra 196,224,33792
maav 32,96,76
s_add_u32 s85,s85,131072
maav 36,68,72
ra 200,224,34816
maav 36,100,76
s_lshl_b32 s86,s86,7
s_add_u32 s86,s86,83898368
v_mov_b32_e32 v238,1
maav 40,72,72
ds 255,8,78,2048
maav 40,104,76
s_cmp_eq_u32 s12,0
s_cselect_b64 exec,1,0
buffer_atomic_add v238,off,s[8:11],s86 sc1
s_mov_b64 exec,-1
maav 44,76,72
ra 204,224,35840
maav 44,108,76
v_smfmac_f32_16x16x64_bf16 v[148:151],v[228:231],v[72:79],v248
ra 208,224,36864
maav 48,80,72
maav 48,112,76
maav 52,84,72
ds 255,8,78,3072
maav 52,116,76
ra 212,224,37888
maav 56,88,72
ex 0
maav 56,120,76
ra 216,224,38912
maav 60,92,72
ex 1
maav 60,124,76
s_mov_b32 m0,s75
ex 2
zvav 160,64,80
ra 220,224,39936
cv 0,0,1
mvav 160,96,84
ds 255,8,79,0
zvav 164,68,80
ex 3
mvav 164,100,84
ra 224,224,40960
cv 1,2,3
zvav 168,72,80
ex 4
mvav 168,104,84
ra 228,224,41984
zvav 172,76,80
ds 255,8,79,1024
mvav 172,108,84
s_add_u32 s46,s46,16384
ex 5
v_smfmac_f32_16x16x64_bf16 v[152:155],v[228:231],v[80:87],v248
ra 232,224,43008
zvav 176,80,80
s_add_u32 s47,s47,32768
ex 6
mvav 176,112,84
s_add_u32 s95,s47,32768
ex 7
zvav 180,84,80
ra 236,224,44032
cv 2,4,5
mvav 180,116,84
ex 32
zvav 184,88,80
ds 255,8,79,2048
mvav 184,120,84
ex 33
zvav 188,92,80
ra 240,224,45056
cv 3,6,7
mvav 188,124,84
ex 34
mvav 192,64,88
ra 244,224,46080
cv 4,32,33
mvav 192,96,92
ex 35
mvav 196,68,88
ex 36
mvav 196,100,92
ds 255,8,79,3072
mvav 200,72,88
ra 248,224,47104
cv 5,34,35
mvav 200,104,92
s_cmp_ge_i32 s47,s46
ex 37
mvav 204,76,88
s_cselect_b32 s78,s95,s46
ex 38
mvav 204,108,92
s_cselect_b32 s79,s95,268435456
ex 39
v_smfmac_f32_16x16x64_bf16 v[156:159],v[228:231],v[88:95],v248
ra 252,224,48128
mvav 208,80,88
s_add_u32 s79,s79,16384
ex 8
mvav 208,112,92
ex 9
mvav 212,84,88
ex 10
mvav 212,116,92
ex 11
mvav 216,88,88
cv 6,36,37
cv 7,38,39
mvav 216,120,92
ex 12
mvav 220,92,88
ex 13
mvav 220,124,92
s_waitcnt vmcnt(16) lgkmcnt(0)
s_barrier
cvaa 64,192,128,232
s_bitcmp0_b32 s82,s81
s_cbranch_scc1 L_loop
s_bitcmp1_b32 s15,s81
s_cbranch_scc1 L_short
.p2align 6
L_loop:
mvaa 64,200,132
s_add_u32 s81,s81,1
ex 14
mvaa 64,208,136
s_add_u32 s44,s44,1024
ex 15
mvaa 64,216,140
cv 8,8,9
cv 9,10,11
cvaa 68,220,140,232
s_mov_b32 m0,s70
ex 40
mvaa 68,212,136
ds 249,8,78,0
mvaa 68,204,132
ex 41
mvaa 68,196,128
ex 42
cvaa 96,224,128,232
ra 64,225,0
cv 10,12,13
mvaa 96,232,132
ex 43
mvaa 96,240,136
ra 68,225,1024
cv 11,14,15
mvaa 96,248,140
ex 44
cvaa 100,252,140,232
ds 249,8,78,1024
mvaa 100,244,136
ex 45
mvaa 100,236,132
ra 72,225,2048
cv 12,40,41
mvaa 100,228,128
ex 46
cvaa 72,192,144,232
ra 76,225,3072
cv 13,42,43
mvaa 72,200,148
ex 47
mvaa 72,208,152
ex 16
mvaa 72,216,156
ds 249,8,78,2048
cvaa 76,220,156,232
ra 80,225,4096
cv 14,44,45
mvaa 76,212,152
ex 17
mvaa 76,204,148
ra 84,225,5120
cv 15,46,47
mvaa 76,196,144
ex 18
cvaa 104,224,144,232
ex 19
mvaa 104,232,148
ra 88,225,6144
cv 16,16,17
mvaa 104,240,152
ds 249,8,78,3072
mvaa 104,248,156
ex 20
cvaa 108,252,156,232
ra 92,225,7168
cv 17,18,19
mvaa 108,244,152
ex 21
mvaa 108,236,148
ex 22
mvaa 108,228,144
ra 96,225,8192
cv 18,20,21
cvaa 80,192,160,232
s_mov_b32 m0,s71
ex 23
mvaa 80,200,164
ra 100,225,9216
cv 19,22,23
mvaa 80,208,168
ds 249,8,79,0
mvaa 80,216,172
ex 48
cvaa 84,220,172,232
ex 49
mvaa 84,212,168
ra 104,225,10240
cv 20,48,49
mvaa 84,204,164
ex 50
mvaa 84,196,160
ra 108,225,11264
cvaa 112,224,160,232
ex 51
mvaa 112,232,164
ds 249,8,79,1024
mvaa 112,240,168
ex 52
mvaa 112,248,172
ra 112,225,12288
cv 21,50,51
cvaa 116,252,172,232
ex 53
mvaa 116,244,168
ra 116,225,13312
cv 22,52,53
mvaa 116,236,164
ex 54
mvaa 116,228,160
ex 55
cvaa 88,192,176,232
ds 249,8,79,2048
mvaa 88,200,180
ra 120,225,14336
cv 23,54,55
mvaa 88,208,184
ex 24
mvaa 88,216,188
ra 124,225,15360
cvaa 92,220,188,232
buffer_store_dwordx4 v[250:253],v244,s[8:11],s44 offen offset:1024 sc0 sc1
mvaa 92,212,184
ex 25
mvaa 92,204,180
ex 26
mvaa 92,196,176
ds 249,8,79,3072
cvaa 120,224,176,232
ex 27
mvaa 120,232,180
ex 28
mvaa 120,240,184
ex 29
mvaa 120,248,188
ex 30
cvaa 124,252,188,232
buffer_store_dwordx4 v[250:253],v244,s[8:11],s44 offen sc0 sc1
mvaa 124,244,184
ex 31
mvaa 124,236,180
cv 24,24,25
cv 25,26,27
mvaa 124,228,176
s_waitcnt vmcnt(19) lgkmcnt(0)
s_barrier
maav 0,64,0
ex 56
maav 0,96,4
s_mov_b32 m0,s72
ex 57
maav 4,68,0
ds 255,8,76,0
maav 4,100,4
ex 58
maav 8,72,0
ex 59
maav 8,104,4
cv 26,28,29
cv 27,30,31
maav 12,76,0
s_cmp_lg_u32 s80,s81
ex 60
maav 12,108,4
ex 61
v_smfmac_f32_16x16x64_bf16 v[144:147],v[228:231],v[0:7],v248
cv 28,56,57
maav 16,80,0
ex 62
maav 16,112,4
buffer_load_dword v247,v246,s[24:27],s49 offen
maav 20,84,0
ex 63
maav 20,116,4
cv 29,58,59
cv 30,60,61
maav 24,88,0
s_cbranch_scc1 L_evb_r
v_add_u32_e32 v224,s94,v224
s_lshl_b32 s86,s12,12
s_add_u32 s86,s86,131072
v_add_u32_e32 v236,s86,v244
ra 160,236,0
ra 164,236,1024
ra 168,236,2048
ra 172,236,3072
ra 176,236,16384
ra 180,236,17408
ra 184,236,18432
ra 188,236,19456
L_evb_r:
cv 31,62,63
maav 24,120,4
ra 192,224,0
maav 28,92,0
ds 255,8,76,1024
maav 28,124,4
maav 32,64,8
ra 196,224,1024
maav 32,96,12
s_add_u32 s49,s49,32768
maav 36,68,8
ra 200,224,2048
maav 36,100,12
maav 40,72,8
ds 255,8,76,2048
maav 40,104,12
maav 44,76,8
ra 204,224,3072
maav 44,108,12
v_smfmac_f32_16x16x64_bf16 v[148:151],v[228:231],v[8:15],v248
ra 208,224,4096
maav 48,80,8
maav 48,112,12
maav 52,84,8
ds 255,8,76,3072
maav 52,116,12
ra 212,224,5120
maav 56,88,8
ex 64
maav 56,120,12
ra 216,224,6144
maav 60,92,8
ex 65
maav 60,124,12
s_mov_b32 m0,s73
ex 66
mvav 160,64,16
ra 220,224,7168
cv 64,64,65
mvav 160,96,20
ds 255,8,77,0
mvav 164,68,16
ex 67
mvav 164,100,20
ra 224,224,8192
cv 65,66,67
mvav 168,72,16
ex 68
mvav 168,104,20
ra 228,224,9216
mvav 172,76,16
ds 255,8,77,1024
mvav 172,108,20
s_add_u32 s46,s46,16384
ex 69
v_smfmac_f32_16x16x64_bf16 v[152:155],v[228:231],v[16:23],v248
ra 232,224,10240
mvav 176,80,16
s_add_u32 s47,s47,32768
ex 70
mvav 176,112,20
s_add_u32 s95,s47,32768
ex 71
mvav 180,84,16
ra 236,224,11264
cv 66,68,69
mvav 180,116,20
ex 96
mvav 184,88,16
ds 255,8,77,2048
mvav 184,120,20
ex 97
mvav 188,92,16
ra 240,224,12288
cv 67,70,71
mvav 188,124,20
ex 98
mvav 192,64,24
ra 244,224,13312
cv 68,96,97
mvav 192,96,28
ex 99
mvav 196,68,24
ex 100
mvav 196,100,28
ds 255,8,77,3072
mvav 200,72,24
ra 248,224,14336
cv 69,98,99
mvav 200,104,28
s_cmp_ge_i32 s47,s46
ex 101
mvav 204,76,24
s_cselect_b32 s76,s95,s46
ex 102
mvav 204,108,28
s_cselect_b32 s77,s95,268435456
ex 103
v_smfmac_f32_16x16x64_bf16 v[156:159],v[228:231],v[24:31],v248
)"
R"(ra 252,224,15360
mvav 208,80,24
s_add_u32 s77,s77,16384
ex 72
mvav 208,112,28
ex 73
mvav 212,84,24
ex 74
mvav 212,116,28
ex 75
mvav 216,88,24
cv 70,100,101
cv 71,102,103
mvav 216,120,28
ex 76
mvav 220,92,24
ex 77
mvav 220,124,28
s_waitcnt vmcnt(19) lgkmcnt(0)
s_barrier
cvaa 0,192,128,232
mvaa 0,200,132
s_add_u32 s81,s81,1
ex 78
mvaa 0,208,136
s_add_u32 s44,s44,1024
ex 79
mvaa 0,216,140
cv 72,72,73
cv 73,74,75
cvaa 4,220,140,232
s_mov_b32 m0,s68
ex 104
mvaa 4,212,136
ds 249,8,76,0
mvaa 4,204,132
ex 105
mvaa 4,196,128
ex 106
cvaa 32,224,128,232
ra 64,225,32768
cv 74,76,77
mvaa 32,232,132
ex 107
mvaa 32,240,136
ra 68,225,33792
cv 75,78,79
mvaa 32,248,140
ex 108
cvaa 36,252,140,232
ds 249,8,76,1024
mvaa 36,244,136
ex 109
mvaa 36,236,132
ra 72,225,34816
cv 76,104,105
mvaa 36,228,128
ex 110
cvaa 8,192,144,232
ra 76,225,35840
cv 77,106,107
mvaa 8,200,148
ex 111
mvaa 8,208,152
ex 80
mvaa 8,216,156
ds 249,8,76,2048
cvaa 12,220,156,232
ra 80,225,36864
cv 78,108,109
mvaa 12,212,152
ex 81
mvaa 12,204,148
ra 84,225,37888
cv 79,110,111
mvaa 12,196,144
ex 82
cvaa 40,224,144,232
ex 83
mvaa 40,232,148
ra 88,225,38912
cv 80,80,81
mvaa 40,240,152
ds 249,8,76,3072
mvaa 40,248,156
ex 84
cvaa 44,252,156,232
ra 92,225,39936
cv 81,82,83
mvaa 44,244,152
ex 85
mvaa 44,236,148
ex 86
mvaa 44,228,144
ra 96,225,40960
cv 82,84,85
cvaa 16,192,160,232
s_mov_b32 m0,s69
ex 87
mvaa 16,200,164
ra 100,225,41984
cv 83,86,87
mvaa 16,208,168
ds 249,8,77,0
mvaa 16,216,172
ex 112
cvaa 20,220,172,232
ex 113
mvaa 20,212,168
ra 104,225,43008
cv 84,112,113
mvaa 20,204,164
ex 114
mvaa 20,196,160
ra 108,225,44032
cvaa 48,224,160,232
ex 115
mvaa 48,232,164
ds 249,8,77,1024
mvaa 48,240,168
ex 116
mvaa 48,248,172
ra 112,225,45056
cv 85,114,115
cvaa 52,252,172,232
ex 117
mvaa 52,244,168
ra 116,225,46080
cv 86,116,117
mvaa 52,236,164
ex 118
mvaa 52,228,160
ex 119
cvaa 24,192,176,232
ds 249,8,77,2048
mvaa 24,200,180
ra 120,225,47104
cv 87,118,119
mvaa 24,208,184
ex 88
mvaa 24,216,188
ra 124,225,48128
cvaa 28,220,188,232
mvaa 28,212,184
ex 89
mvaa 28,204,180
ex 90
mvaa 28,196,176
ds 249,8,77,3072
cvaa 56,224,176,232
ex 91
mvaa 56,232,180
ex 92
mvaa 56,240,184
ex 93
mvaa 56,248,188
ex 94
cvaa 60,252,188,232
mvaa 60,244,184
ex 95
mvaa 60,236,180
cv 88,88,89
cv 89,90,91
mvaa 60,228,176
s_waitcnt vmcnt(18) lgkmcnt(0)
s_barrier
maav 0,64,64
ex 120
maav 0,96,68
s_mov_b32 m0,s74
ex 121
maav 4,68,64
ds 255,8,78,0
maav 4,100,68
ex 122
maav 8,72,64
ex 123
maav 8,104,68
cv 90,92,93
cv 91,94,95
maav 12,76,64
ex 124
maav 12,108,68
ex 125
v_smfmac_f32_16x16x64_bf16 v[144:147],v[228:231],v[64:71],v248
cv 92,120,121
maav 16,80,64
ex 126
maav 16,112,68
buffer_load_dword v247,v245,s[36:39],s85 offen
maav 20,84,64
ex 127
maav 20,116,68
cv 93,122,123
cv 94,124,125
maav 24,88,64
cv 95,126,127
maav 24,120,68
ra 192,224,32768
maav 28,92,64
ds 255,8,78,1024
maav 28,124,68
maav 32,64,72
ra 196,224,33792
maav 32,96,76
s_add_u32 s85,s85,131072
maav 36,68,72
ra 200,224,34816
maav 36,100,76
maav 40,72,72
ds 255,8,78,2048
maav 40,104,76
maav 44,76,72
ra 204,224,35840
maav 44,108,76
v_smfmac_f32_16x16x64_bf16 v[148:151],v[228:231],v[72:79],v248
ra 208,224,36864
maav 48,80,72
maav 48,112,76
maav 52,84,72
ds 255,8,78,3072
maav 52,116,76
ra 212,224,37888
maav 56,88,72
ex 0
maav 56,120,76
ra 216,224,38912
maav 60,92,72
ex 1
maav 60,124,76
s_mov_b32 m0,s75
ex 2
mvav 160,64,80
ra 220,224,39936
cv 0,0,1
mvav 160,96,84
ds 255,8,79,0
mvav 164,68,80
ex 3
mvav 164,100,84
ra 224,224,40960
cv 1,2,3
mvav 168,72,80
ex 4
mvav 168,104,84
ra 228,224,41984
mvav 172,76,80
ds 255,8,79,1024
mvav 172,108,84
s_add_u32 s46,s46,16384
ex 5
v_smfmac_f32_16x16x64_bf16 v[152:155],v[228:231],v[80:87],v248
ra 232,224,43008
mvav 176,80,80
s_add_u32 s47,s47,32768
ex 6
mvav 176,112,84
s_add_u32 s95,s47,32768
ex 7
mvav 180,84,80
ra 236,224,44032
cv 2,4,5
mvav 180,116,84
ex 32
mvav 184,88,80
ds 255,8,79,2048
mvav 184,120,84
ex 33
mvav 188,92,80
ra 240,224,45056
cv 3,6,7
mvav 188,124,84
ex 34
mvav 192,64,88
ra 244,224,46080
cv 4,32,33
mvav 192,96,92
ex 35
mvav 196,68,88
ex 36
mvav 196,100,92
ds 255,8,79,3072
mvav 200,72,88
ra 248,224,47104
cv 5,34,35
mvav 200,104,92
s_cmp_ge_i32 s47,s46
ex 37
mvav 204,76,88
s_cselect_b32 s78,s95,s46
ex 38
mvav 204,108,92
s_cselect_b32 s79,s95,268435456
ex 39
v_smfmac_f32_16x16x64_bf16 v[156:159],v[228:231],v[88:95],v248
ra 252,224,48128
mvav 208,80,88
s_add_u32 s79,s79,16384
ex 8
mvav 208,112,92
ex 9
mvav 212,84,88
ex 10
mvav 212,116,92
ex 11
mvav 216,88,88
cv 6,36,37
cv 7,38,39
mvav 216,120,92
ex 12
mvav 220,92,88
ex 13
mvav 220,124,92
s_waitcnt vmcnt(17) lgkmcnt(0)
s_barrier
cvaa 64,192,128,232
s_bitcmp0_b32 s82,s81
s_cbranch_scc1 L_loop
s_bitcmp0_b32 s15,s81
s_cbranch_scc1 L_eva_r
L_short:
v_add_f32_e32 v226,v152,v153
v_add_f32_e32 v227,v156,v157
v_subrev_u32_e32 v236,0x23800000,v226
v_subrev_u32_e32 v237,0x23800000,v227
v_max_u32_e32 v236,v236,v237
v_cmp_lt_u32_e64 s[40:41],s48,v236
v_rcp_f32_e32 v226,v226
v_rcp_f32_e32 v227,v227
s_add_u32 s86,s13,4096
v_mov_b32_e32 v236,0
v_mov_b32_e32 v237,0
v_mov_b32_e32 v238,0
v_mov_b32_e32 v239,0
v_mov_b64_e32 v[152:153],0
v_mov_b64_e32 v[156:157],0
v_mul_f32_e32 v226,v242,v226
v_mul_f32_e32 v227,v243,v227
v_add_u32_e32 v225,s94,v225
v_pk_mul_f32 v[160:161],v[160:161],v[226:227] op_sel:[0,0] op_sel_hi:[1,0]
v_pk_mul_f32 v[162:163],v[162:163],v[226:227] op_sel:[0,0] op_sel_hi:[1,0]
v_pk_mul_f32 v[164:165],v[164:165],v[226:227] op_sel:[0,0] op_sel_hi:[1,0]
v_pk_mul_f32 v[166:167],v[166:167],v[226:227] op_sel:[0,0] op_sel_hi:[1,0]
cv 160,160,161
cv 161,162,163
cv 162,164,165
cv 163,166,167
buffer_store_dwordx4 v[160:163],v244,s[8:11],s13 offen offset:0 sc0 sc1
v_pk_mul_f32 v[168:169],v[168:169],v[226:227] op_sel:[0,0] op_sel_hi:[1,0]
v_pk_mul_f32 v[170:171],v[170:171],v[226:227] op_sel:[0,0] op_sel_hi:[1,0]
v_pk_mul_f32 v[172:173],v[172:173],v[226:227] op_sel:[0,0] op_sel_hi:[1,0]
v_pk_mul_f32 v[174:175],v[174:175],v[226:227] op_sel:[0,0] op_sel_hi:[1,0]
cv 168,168,169
cv 169,170,171
cv 170,172,173
cv 171,174,175
buffer_store_dwordx4 v[168:171],v244,s[8:11],s13 offen offset:2048 sc0 sc1
v_pk_mul_f32 v[176:177],v[176:177],v[226:227] op_sel:[0,0] op_sel_hi:[1,0]
v_pk_mul_f32 v[178:179],v[178:179],v[226:227] op_sel:[0,0] op_sel_hi:[1,0]
v_pk_mul_f32 v[180:181],v[180:181],v[226:227] op_sel:[0,0] op_sel_hi:[1,0]
v_pk_mul_f32 v[182:183],v[182:183],v[226:227] op_sel:[0,0] op_sel_hi:[1,0]
cv 176,176,177
cv 177,178,179
cv 178,180,181
v_mfma_f32_32x32x16_bf16 v[160:175],v[236:239],v[236:239],0
cv 179,182,183
buffer_store_dwordx4 v[176:179],v244,s[8:11],s86 offen offset:0 sc0 sc1
v_pk_mul_f32 v[184:185],v[184:185],v[226:227] op_sel:[0,0] op_sel_hi:[1,0]
v_pk_mul_f32 v[186:187],v[186:187],v[226:227] op_sel:[0,0] op_sel_hi:[1,0]
v_pk_mul_f32 v[188:189],v[188:189],v[226:227] op_sel:[0,0] op_sel_hi:[1,0]
v_pk_mul_f32 v[190:191],v[190:191],v[226:227] op_sel:[0,0] op_sel_hi:[1,0]
cv 184,184,185
cv 185,186,187
cv 186,188,189
cv 187,190,191
buffer_store_dwordx4 v[184:187],v244,s[8:11],s86 offen offset:2048 sc0 sc1
v_pk_mul_f32 v[192:193],v[192:193],v[226:227] op_sel:[0,1] op_sel_hi:[1,1]
v_pk_mul_f32 v[194:195],v[194:195],v[226:227] op_sel:[0,1] op_sel_hi:[1,1]
v_pk_mul_f32 v[196:197],v[196:197],v[226:227] op_sel:[0,1] op_sel_hi:[1,1]
v_pk_mul_f32 v[198:199],v[198:199],v[226:227] op_sel:[0,1] op_sel_hi:[1,1]
cv 192,192,193
cv 193,194,195
cv 194,196,197
v_mfma_f32_32x32x16_bf16 v[176:191],v[236:239],v[236:239],0
cv 195,198,199
buffer_store_dwordx4 v[192:195],v244,s[8:11],s13 offen offset:1024 sc0 sc1
v_pk_mul_f32 v[200:201],v[200:201],v[226:227] op_sel:[0,1] op_sel_hi:[1,1]
v_pk_mul_f32 v[202:203],v[202:203],v[226:227] op_sel:[0,1] op_sel_hi:[1,1]
v_pk_mul_f32 v[204:205],v[204:205],v[226:227] op_sel:[0,1] op_sel_hi:[1,1]
v_pk_mul_f32 v[206:207],v[206:207],v[226:227] op_sel:[0,1] op_sel_hi:[1,1]
cv 200,200,201
cv 201,202,203
cv 202,204,205
cv 203,206,207
buffer_store_dwordx4 v[200:203],v244,s[8:11],s13 offen offset:3072 sc0 sc1
v_pk_mul_f32 v[208:209],v[208:209],v[226:227] op_sel:[0,1] op_sel_hi:[1,1]
v_pk_mul_f32 v[210:211],v[210:211],v[226:227] op_sel:[0,1] op_sel_hi:[1,1]
v_pk_mul_f32 v[212:213],v[212:213],v[226:227] op_sel:[0,1] op_sel_hi:[1,1]
v_pk_mul_f32 v[214:215],v[214:215],v[226:227] op_sel:[0,1] op_sel_hi:[1,1]
cv 208,208,209
cv 209,210,211
cv 210,212,213
v_mfma_f32_32x32x16_bf16 v[192:207],v[236:239],v[236:239],0
cv 211,214,215
buffer_store_dwordx4 v[208:211],v244,s[8:11],s86 offen offset:1024 sc0 sc1
v_pk_mul_f32 v[216:217],v[216:217],v[226:227] op_sel:[0,1] op_sel_hi:[1,1]
v_pk_mul_f32 v[218:219],v[218:219],v[226:227] op_sel:[0,1] op_sel_hi:[1,1]
v_pk_mul_f32 v[220:221],v[220:221],v[226:227] op_sel:[0,1] op_sel_hi:[1,1]
v_pk_mul_f32 v[222:223],v[222:223],v[226:227] op_sel:[0,1] op_sel_hi:[1,1]
cv 216,216,217
cv 217,218,219
cv 218,220,221
cv 219,222,223
buffer_store_dwordx4 v[216:219],v244,s[8:11],s86 offen offset:3072 sc0 sc1
s_nop 1
v_mfma_f32_32x32x16_bf16 v[208:223],v[236:239],v[236:239],0
L_eva_r:
s_cmp_lg_u32 s81,14
s_cbranch_scc1 L_loop
s_add_u32 s81,s81,1
s_bitcmp1_b32 170,s20
s_cbranch_scc1 L_odd
L_last:
s_lshl_b32 s86,s21,2
s_sub_u32 s87,s13,s67
s_add_u32 s86,s86,s12
mvaa 64,200,132
s_lshl_b32 s86,s86,8
v_add_u32_e32 v252,s87,v244
s_add_u32 s86,s86,L_last-L_att_pc+1024
v_lshl_add_u32 v226,v244,3,s86
s_lshl_b32 s86,s12,1
s_bfe_u32 s87,s21,65539
mvaa 64,208,136
s_add_u32 s86,s86,s87
v_min_u32_e32 v226,L_att_end-L_att_pc-4,v226
s_lshl_b32 s86,s86,7
s_add_u32 s86,s86,L_pend2-L_att_pc
v_writelane_b32 v226,s86,2
mvaa 64,216,140
s_mov_b64 exec,7
global_load_dword v253,v226,s[96:97]
s_mov_b64 exec,-1
cvaa 68,220,140,232
s_add_u32 m0,s52,32768
v_and_b32_e32 v224,16,v244
v_lshlrev_b32_e32 v224,5,v224
mvaa 68,212,136
s_xor_b32 s51,s62,0
v_lshrrev_b32_e32 v239,4,v244
v_lshrrev_b32_e32 v236,3,v239
mvaa 68,204,132
s_lshl_b32 s87,s12,13
v_and_b32_e32 v237,112,v244
v_and_b32_e32 v238,48,v239
mvaa 68,196,128
s_xor_b32 s87,s87,8192
ra 64,225,0
v_lshlrev_b32_e32 v238,1,v238
cvaa 96,224,128,232
buffer_load_dword v253,v224,s[8:11],s53 offen sc1
mvaa 96,232,132
s_mov_b32 s84,6
ra 68,225,1024
v_xor_b32_e32 v237,v237,v238
mvaa 96,240,136
s_add_u32 s81,s81,1
ra 72,225,2048
v_lshl_add_u32 v245,v236,11,v237
mvaa 96,248,140
s_mov_b32 s84,4
v_add_u32_e32 v246,15360,v245
v_add_u32_e32 v249,30720,v245
cvaa 100,252,140,232
s_andn2_b32 s69,64,s54
ra 76,225,3072
s_waitcnt vmcnt(9)
v_add_u32_e32 v247,46080,v245
mvaa 100,244,136
dl 245,24,51,0
mvaa 100,236,132
s_add_u32 s88,s14,4096
ra 80,225,4096
cv 8,8,9
mvaa 100,228,128
s_lshl_b32 s35,s19,1
ra 84,225,5120
cv 9,10,11
cvaa 72,192,144,232
s_lshr_b32 s33,40569,s35
ra 88,225,6144
cv 10,12,13
mvaa 72,200,148
s_and_b32 s33,s33,3
ra 92,225,7168
v_add_u32_e32 v250,4096,v252
mvaa 72,208,152
dl 246,24,51,1024
mvaa 72,216,156
s_lshl_b32 s33,s33,13
ra 96,225,8192
v_mov_b32_e32 v251,4
cvaa 76,220,156,232
s_lshr_b32 s34,59294,s35
ra 100,225,9216
s_and_b32 s34,s34,3
mvaa 76,212,152
s_lshl_b32 s34,s34,13
ra 104,225,10240
s_xor_b32 s35,s33,s34
mvaa 76,204,148
s_and_b32 s98,s19,3
ra 108,225,11264
s_lshl_b32 s98,s98,13
mvaa 76,196,144
dl 249,24,51,2048
cvaa 104,224,144,232
s_mov_b32 s74,0x300003
ra 112,225,12288
s_mov_b32 s75,0x30000300
mvaa 104,232,148
s_lshl_b32 s71,s12,15
ra 116,225,13312
s_add_u32 s71,s71,s54
mvaa 104,240,152
s_mov_b64 s[44:45],s[30:31]
ra 120,225,14336
s_mov_b32 s46,4194304
mvaa 104,248,156
s_mov_b32 s47,131072
ex 14
cvaa 108,252,156,232
dl 247,24,51,3072
mvaa 108,244,152
s_add_u32 m0,s52,49152
s_xor_b32 s51,s63,0
ra 124,225,15360
mvaa 108,236,148
s_lshl_b32 s70,s23,11
ex 15
mvaa 108,228,144
s_add_u32 s70,s70,s71
ex 40
cvaa 80,192,160,232
ex 41
mvaa 80,200,164
dl 245,24,51,0
mvaa 80,208,168
ex 42
mvaa 80,216,172
ex 43
cvaa 84,220,172,232
ex 44
mvaa 84,212,168
ex 45
mvaa 84,204,164
dl 246,24,51,1024
mvaa 84,196,160
ex 46
cvaa 112,224,160,232
ex 47
mvaa 112,232,164
ex 16
mvaa 112,240,168
ex 17
mvaa 112,248,172
dl 249,24,51,2048
cvaa 116,252,172,232
ex 18
mvaa 116,244,168
ex 19
mvaa 116,236,164
ex 20
mvaa 116,228,160
ex 21
cvaa 88,192,176,232
dl 247,24,51,3072
s_waitcnt vmcnt(8)
v_cmp_ne_u32_e32 vcc,4,v253
mvaa 88,200,180
s_cbranch_vccnz L_spbe
L_sprbe:
ex 22
mvaa 88,208,184
s_xor_b32 s50,s66,262144
ex 23
mvaa 88,216,188
ex 48
cvaa 92,220,188,232
ex 49
mvaa 92,212,184
la 128,252,50,0
mvaa 92,204,180
s_waitcnt lgkmcnt(0)
s_barrier
mvaa 92,196,176
s_mov_b32 m0,s72
ex 50
cvaa 120,224,176,232
s_lshl_b32 s72,s22,11
ex 51
mvaa 120,232,180
s_add_u32 s72,s72,s71
ex 52
mvaa 120,240,184
ds 255,8,76,0
mvaa 120,248,188
s_add_u32 s71,s70,131072
ex 53
cvaa 124,252,188,232
ex 54
mvaa 124,244,184
ex 55
mvaa 124,236,180
ex 24
mvaa 124,228,176
ds 255,8,76,1024
maav 0,64,0
s_cmp_lg_u32 s19,7
s_cbranch_scc1 L_j7e
s_nop 1
s_lshl_b32 s86,s12,12
s_add_u32 s86,s86,131072
v_add_u32_e32 v236,s86,v244
ra 160,236,0
ra 164,236,1024
ra 168,236,2048
ra 172,236,3072
ra 176,236,16384
ra 180,236,17408
ra 184,236,18432
ra 188,236,19456
v_add_u32_e32 v224,s94,v224
s_waitcnt lgkmcnt(0)
L_j7e:
ex 25
maav 0,96,4
ex 26
maav 4,68,0
ex 27
maav 4,100,4
cv 11,14,15
cv 12,40,41
maav 8,72,0
ds 255,8,76,2048
maav 8,104,4
cv 13,42,43
cv 14,44,45
maav 12,76,0
cv 15,46,47
cv 16,16,17
maav 12,108,4
ex 28
v_smfmac_f32_16x16x64_bf16 v[144:147],v[228:231],v[0:7],v248
cv 17,18,19
maav 16,80,0
ds 255,8,76,3072
maav 16,112,4
s_mov_b32 m0,s73
ex 29
maav 20,84,0
s_add_u32 s73,s72,131072
ex 30
maav 20,116,4
ex 31
maav 24,88,0
ex 56
maav 24,120,4
ds 255,8,77,0
maav 28,92,0
ex 57
maav 28,124,4
ex 58
maav 32,64,8
ex 59
maav 32,96,12
ex 60
maav 36,68,8
ds 255,8,77,1024
maav 36,100,12
cv 18,20,21
cv 19,22,23
maav 40,72,8
cv 20,48,49
cv 21,50,51
maav 40,104,12
cv 22,52,53
cv 23,54,55
maav 44,76,8
ex 61
maav 44,108,12
ds 255,8,77,2048
v_smfmac_f32_16x16x64_bf16 v[148:151],v[228:231],v[8:15],v248
cv 24,24,25
maav 48,80,8
L_vs18:
s_cbranch_execz L_vs22
ex 62
maav 48,112,12
ex 63
maav 52,84,8
ra 192,244,0
cv 25,26,27
maav 52,116,12
ds 255,8,77,3072
maav 56,88,8
cv 26,28,29
cv 27,30,31
maav 56,120,12
cv 28,56,57
cv 29,58,59
maav 60,92,8
cv 30,60,61
cv 31,62,63
maav 60,124,12
ex 64
mvav 160,64,16
la 132,252,50,2048
mvav 160,96,20
ex 65
mvav 164,68,16
ex 66
mvav 164,100,20
ex 67
mvav 168,72,16
ex 68
mvav 168,104,20
la 136,250,50,0
mvav 172,76,16
ex 69
mvav 172,108,20
ex 70
v_smfmac_f32_16x16x64_bf16 v[152:155],v[228:231],v[16:23],v248
ra 200,244,2048
mvav 176,80,16
ra 208,244,4096
cv 64,64,65
mvav 176,112,20
la 140,250,50,2048
mvav 180,84,16
ra 216,244,6144
cv 65,66,67
mvav 180,116,20
ex 71
mvav 184,88,16
ex 96
mvav 184,120,20
s_barrier
ex 97
mvav 188,92,16
la 156,250,50,3072
mvav 188,124,20
s_add_u32 m0,s52,131072
ra 196,244,1024
s_xor_b32 s51,s62,1048576
mvav 192,64,24
ra 204,244,3072
cv 66,68,69
mvav 192,96,28
ra 212,244,5120
cv 67,70,71
mvav 196,68,24
ra 220,244,7168
cv 68,96,97
mvav 196,100,28
la 152,250,50,1024
mvav 200,72,24
ex 98
mvav 200,104,28
ex 99
mvav 204,76,24
ra 224,244,8192
cv 69,98,99
mvav 204,108,28
ra 232,244,10240
v_add_u32_e32 v6,s87,v244
v_smfmac_f32_16x16x64_bf16 v[156:159],v[228:231],v[24:31],v248
ra 240,244,12288
mvav 208,80,24
la 148,252,50,3072
mvav 208,112,28
s_xor_b32 s87,s52,4096
ra 248,244,14336
v_add_u32_e32 v7,98304,v6
mvav 212,84,24
s_add_u32 s87,s87,150528
ra 252,244,15360
v_add_u32_e32 v5,65536,v244
mvav 212,116,28
ra 244,244,13312
v_add_u32_e32 v32,s87,v244
mvav 216,88,24
ra 228,244,9216
v_and_b32_e32 v224,960,v244
mvav 216,120,28
la 144,252,50,1024
mvav 220,92,24
s_add_u32 s50,s50,s55
ra 236,244,11264
mvav 220,124,28
ra 64,225,32768
s_waitcnt lgkmcnt(8)
cvaa 24,192,176,232
dl 245,24,51,0
mvaa 24,200,180
ra 96,225,40960
mvaa 24,208,184
ra 68,225,33792
mvaa 24,216,188
dl 246,24,51,1024
cvaa 16,192,160,128
ra 100,225,41984
mvaa 16,200,164
ra 72,225,34816
mvaa 16,208,168
dl 249,24,51,2048
mvaa 16,216,172
ra 104,225,43008
cvaa 28,196,176,232
ra 76,225,35840
mvaa 28,204,180
dl 247,24,51,3072
mvaa 28,212,184
s_add_u32 m0,s52,147456
s_xor_b32 s51,s63,1048576
ra 108,225,44032
mvaa 28,220,188
ra 80,225,36864
cvaa 20,196,160,132
dl 245,24,51,0
mvaa 20,204,164
ra 112,225,45056
mvaa 20,212,168
ra 192,244,16384
mvaa 20,220,172
dl 246,24,51,1024
cvaa 56,224,176,232
ra 200,244,18432
s_waitcnt lgkmcnt(7)
mvaa 56,232,180
ra 208,244,20480
mvaa 56,240,184
dl 249,24,51,2048
mvaa 56,248,188
ra 216,244,22528
cvaa 48,224,160,136
ra 196,244,17408
mvaa 48,232,164
ra 204,244,19456
mvaa 48,240,168
ra 212,244,21504
mvaa 48,248,172
ra 220,244,23552
cvaa 60,228,176,232
ra 224,244,24576
mvaa 60,236,180
ra 232,244,26624
mvaa 60,244,184
ra 240,244,28672
mvaa 60,252,188
ra 248,244,30720
cvaa 52,228,160,140
ra 84,225,37888
mvaa 52,236,164
ra 116,225,46080
mvaa 52,244,168
la 160,252,50,0
ra 228,244,25600
mvaa 52,252,172
ra 236,244,27648
s_waitcnt lgkmcnt(10)
cvaa 8,192,176,128
ra 244,244,29696
mvaa 8,200,180
la 164,252,50,2048
ra 252,244,31744
mvaa 8,208,184
ra 88,225,38912
mvaa 8,216,188
ra 120,225,47104
cvaa 12,196,176,132
la 168,250,50,0
mvaa 12,204,180
ra 92,225,39936
ra 124,225,48128
s_waitcnt lgkmcnt(9)
mvaa 12,212,184
ex 100
mvaa 12,220,188
la 172,250,50,2048
cvaa 40,224,176,136
ex 101
mvaa 40,232,180
ex 102
mvaa 40,240,184
ex 103
mvaa 40,248,188
cv 70,100,101
cv 71,102,103
s_waitcnt lgkmcnt(4)
cvaa 44,228,176,140
ex 72
mvaa 44,236,180
s_waitcnt vmcnt(18) lgkmcnt(0)
s_barrier
ex 73
mvaa 44,244,184
la 176,252,50,1024
mvaa 44,252,188
ex 74
maav 0,64,64
ex 75
maav 0,96,68
la 188,250,50,3072
maav 4,68,64
ra 192,5,0
cv 72,72,73
maav 4,100,68
ra 196,5,1024
cv 73,74,75
maav 8,72,64
la 184,250,50,1024
ds_write_b128 v6,a[0:3] offset:0
maav 8,104,68
ex 76
maav 12,76,64
ex 77
maav 12,108,68
la 180,252,50,3072
ds_write_b128 v6,a[4:7] offset:1024
v_smfmac_f32_16x16x64_bf16 v[144:147],v[228:231],v[64:71],v248
cv 74,76,77
maav 16,80,64
ex 78
maav 16,112,68
ds_write_b128 v6,a[8:11] offset:2048
ex 79
maav 20,84,64
ex 104
maav 20,116,68
ds_write_b128 v6,a[12:15] offset:3072
ex 105
maav 24,88,64
ex 106
ex 107
ex 108
maav 24,120,68
ds_write_b128 v6,a[16:19] offset:4096
cv 75,78,79
maav 28,92,64
ex 109
ex 110
ex 111
maav 28,124,68
cv 76,104,105
cv 77,106,107
cv 78,108,109
cv 79,110,111
maav 32,64,72
ds_write_b128 v6,a[20:23] offset:5120
maav 32,96,76
ex 80
maav 36,68,72
ex 81
maav 36,100,76
ds_write_b128 v6,a[24:27] offset:6144
maav 40,72,72
ex 82
maav 40,104,76
ex 83
maav 44,76,72
ds_write_b128 v6,a[28:31] offset:7168
maav 44,108,76
cv 80,80,81
cv 81,82,83
v_smfmac_f32_16x16x64_bf16 v[148:151],v[228:231],v[72:79],v248
ex 84
maav 48,80,72
ds_write_b128 v7,a[32:35] offset:0
maav 48,112,76
ex 85
ex 86
ex 87
maav 52,84,72
ex 112
ex 113
maav 52,116,76
ds_write_b128 v7,a[36:39] offset:1024
cv 82,84,85
maav 56,88,72
ex 114
ex 115
ex 116
maav 56,120,76
ex 117
ex 118
ex 119
maav 60,92,72
ds_write_b128 v7,a[40:43] offset:2048
cv 83,86,87
)"
R"(maav 60,124,76
cv 84,112,113
cv 85,114,115
cv 86,116,117
cv 87,118,119
mvav 160,64,80
ex 88
mvav 160,96,84
ds_write_b128 v7,a[44:47] offset:3072
mvav 164,68,80
v_add_f32_e32 v130,v144,v145
v_add_f32_e32 v131,v148,v149
mvav 164,100,84
ex 89
mvav 168,72,80
ds_write_b128 v7,a[48:51] offset:4096
mvav 168,104,84
ex 90
mvav 172,76,80
ex 91
mvav 172,108,84
ds_write_b128 v32,v[128:131]
v_smfmac_f32_16x16x64_bf16 v[152:155],v[228:231],v[80:87],v248
ex 92
ex 93
mvav 176,80,80
ds_write_b128 v7,a[52:55] offset:5120
ex 94
mvav 176,112,84
ra 200,5,2048
ra 204,5,3072
ex 95
cv 88,88,89
mvav 180,84,80
ra 208,5,4096
ra 212,5,5120
cv 89,90,91
cv 90,92,93
ex 120
mvav 180,116,84
ds_write_b128 v7,a[56:59] offset:6144
ex 121
mvav 184,88,80
ex 122
ex 123
cv 91,94,95
mvav 184,120,84
ex 124
ex 125
mvav 188,92,80
ds_write_b128 v7,a[60:63] offset:7168
ex 126
mvav 188,124,84
ra 216,5,6144
ra 220,5,7168
ra 224,5,8192
ra 232,5,10240
ex 127
cv 92,120,121
mvav 192,64,88
ra 240,5,12288
ra 248,5,14336
ra 228,5,9216
ra 236,5,11264
ra 244,5,13312
ra 252,5,15360
cv 93,122,123
cv 94,124,125
cv 95,126,127
v_xor_b32_e32 v6,8192,v6
s_nop 0
mvav 192,96,92
ex 16
mvav 196,68,88
s_waitcnt lgkmcnt(10)
s_xor_b32 s87,s87,0
mvav 196,100,92
s_xor_b32 s50,s66,294912
s_barrier
v_xor_b32_e32 v7,8192,v7
mvav 200,72,88
la 4,252,50,0
ra 64,5,16384
ra 96,5,24576
rv 64,6,0
rv 68,6,1024
rv 72,6,2048
mvav 200,104,92
rv 76,6,3072
rv 80,6,4096
rv 84,6,5120
ex 17
ex 18
mvav 204,76,88
rv 96,7,0
rv 100,7,1024
ex 19
mvav 204,108,92
la 12,252,50,2048
rv 104,7,2048
rv 108,7,3072
cv 16,16,17
v_smfmac_f32_16x16x64_bf16 v[156:159],v[228:231],v[88:95],v248
rv 112,7,4096
rv 116,7,5120
ex 20
ex 21
mvav 208,80,88
rv 120,7,6144
rv 124,7,7168
ex 22
ex 23
mvav 208,112,92
la 20,250,50,0
ra 68,5,17408
ra 100,5,25600
cv 17,18,19
cv 18,20,21
mvav 212,84,88
ra 72,5,18432
ra 104,5,26624
ex 48
ex 49
mvav 212,116,92
ra 76,5,19456
ra 108,5,27648
ex 50
ex 51
mvav 216,88,88
la 28,250,50,2048
ra 80,5,20480
ra 112,5,28672
ra 84,5,21504
mvav 216,120,92
ra 116,5,29696
cv 19,22,23
ex 52
ex 53
mvav 220,92,88
ra 88,5,22528
cv 20,48,49
ex 54
ex 55
mvav 220,124,92
la 60,250,50,3072
ra 120,5,30720
cv 21,50,51
cv 22,52,53
cv 23,54,55
s_bitcmp1_b32 s82,s81
s_cbranch_scc1 L_pend2
L_ret_eb4:
mvav 160,192,16
ra 92,5,23552
rv 88,6,6144
ra 124,5,31744
rv 92,6,7168
ex 24
s_waitcnt lgkmcnt(10)
mvav 160,224,20
ex 25
ex 26
v_smfmac_f32_16x16x64_bf16 v[152:155],v[228:231],v[16:23],v248
ex 27
v_xor_b32_e32 v32,4096,v32
mvav 164,196,16
la 52,250,50,1024
ds_read_b128 v[236:239],v32
cv 24,24,25
ex 28
mvav 164,228,20
ex 29
cv 25,26,27
ex 30
ex 31
mvav 168,200,16
ex 56
ex 57
mvav 168,232,20
la 44,252,50,3072
cv 26,28,29
ex 58
ex 59
mvav 172,204,16
ex 60
ex 61
ex 62
mvav 172,236,20
s_waitcnt lgkmcnt(0)
s_barrier
cv 27,30,31
ex 63
mvav 176,208,16
la 36,252,50,1024
cv 28,56,57
cv 29,58,59
cv 30,60,61
cv 31,62,63
mvav 176,240,20
s_add_u32 m0,s52,147456
ex 8
s_xor_b32 s51,s63,1048576
ex 9
ex 10
mvav 180,212,16
s_add_u32 s50,s50,s55
ex 11
cv 8,8,9
cv 9,10,11
ex 12
mvav 180,244,20
dl 247,24,51,3072
ex 13
ex 14
mvav 184,216,16
s_add_u32 m0,s52,0
ex 15
ex 40
ex 41
mvav 184,248,20
s_xor_b32 s51,s64,0
ex 42
ex 43
ex 44
mvav 188,220,16
la 0,252,50,0
ex 45
ex 46
mvav 188,252,20
ex 47
cv 10,12,13
cv 11,14,15
cv 12,40,41
cv 13,42,43
cv 14,44,45
v_smfmac_f32_16x16x64_bf16 v[156:159],v[228:231],v[24:31],v248
cv 15,46,47
v_bfe_u32 v225,v244,1,5
s_nop 0
v_smfmac_f32_16x16x64_bf16 v[156:159],v[228:231],v[8:15],v248
v_lshl_add_u32 v224,v224,1,v225
v_bfe_u32 v225,v244,7,2
v_lshl_add_u32 v224,v225,5,v224
mvav 192,192,24
la 8,252,50,2048
v_xor_b32_e32 v224,s69,v224
v_add_f32_e32 v152,v152,v153
v_add_f32_e32 v137,v152,v238
v_subrev_u32_e32 v236,0x23800000,v137
mvav 192,224,28
v_rcp_f32_e32 v137,v137
mvav 192,64,8
tv 128,224,32768
v_mul_f32_e32 v232,v240,v137
v_mul_f32_e32 v64,v232,v64
v_mul_f32_e32 v65,v232,v65
v_mul_f32_e32 v66,v232,v66
mvav 192,96,12
la 16,250,50,0
v_mul_f32_e32 v67,v232,v67
v_mul_f32_e32 v68,v232,v68
v_mul_f32_e32 v69,v232,v69
v_mul_f32_e32 v70,v232,v70
mvav 196,196,24
v_fmac_f32_e32 v64,v232,v160
v_fmac_f32_e32 v65,v232,v161
v_mul_f32_e32 v71,v232,v71
mvav 196,228,28
v_fmac_f32_e32 v66,v232,v162
v_fmac_f32_e32 v67,v232,v163
v_fmac_f32_e32 v68,v232,v164
v_fmac_f32_e32 v69,v232,v165
v_fmac_f32_e32 v70,v232,v166
mvav 196,68,8
la 24,250,50,2048
v_fmac_f32_e32 v71,v232,v167
cv 160,64,65
cv 161,66,67
cv 162,68,69
cv 163,70,71
mvav 196,100,12
tv 130,224,34816
tv 132,224,36864
mvav 200,200,24
tv 134,224,38912
tv 144,224,40960
v_mul_f32_e32 v72,v232,v72
v_mul_f32_e32 v73,v232,v73
v_mul_f32_e32 v74,v232,v74
mvav 200,232,28
L_vs19:
s_cbranch_execz L_vs23
buffer_store_dwordx4 v[160:163],v244,s[8:11],s14 offen offset:0 sc0 sc1
v_mul_f32_e32 v75,v232,v75
v_mul_f32_e32 v76,v232,v76
v_mul_f32_e32 v77,v232,v77
v_mul_f32_e32 v78,v232,v78
mvav 200,72,8
v_fmac_f32_e32 v72,v232,v168
v_fmac_f32_e32 v73,v232,v169
v_mul_f32_e32 v79,v232,v79
mvav 200,104,12
v_fmac_f32_e32 v74,v232,v170
v_fmac_f32_e32 v75,v232,v171
v_fmac_f32_e32 v76,v232,v172
v_fmac_f32_e32 v77,v232,v173
v_fmac_f32_e32 v78,v232,v174
mvav 204,204,24
la 56,250,50,3072
v_fmac_f32_e32 v79,v232,v175
cv 168,72,73
cv 169,74,75
cv 170,76,77
cv 171,78,79
mvav 204,236,28
tv 146,224,43008
tv 148,224,45056
mvav 204,76,8
tv 150,224,47104
v_mul_f32_e32 v80,v232,v80
v_mul_f32_e32 v81,v232,v81
v_mul_f32_e32 v82,v232,v82
mvav 204,108,12
buffer_store_dwordx4 v[168:171],v244,s[8:11],s14 offen offset:2048 sc0 sc1
v_mul_f32_e32 v83,v232,v83
v_mul_f32_e32 v84,v232,v84
v_mul_f32_e32 v85,v232,v85
v_mul_f32_e32 v86,v232,v86
v_mul_f32_e32 v87,v232,v87
mvav 208,208,24
v_fmac_f32_e32 v80,v232,v176
v_fmac_f32_e32 v81,v232,v177
v_fmac_f32_e32 v82,v232,v178
mvav 208,240,28
v_fmac_f32_e32 v83,v232,v179
v_fmac_f32_e32 v84,v232,v180
v_fmac_f32_e32 v85,v232,v181
cv 176,80,81
cv 177,82,83
mvav 208,80,8
la 48,250,50,1024
v_fmac_f32_e32 v86,v232,v182
v_fmac_f32_e32 v87,v232,v183
cv 178,84,85
cv 179,86,87
mvav 208,112,12
v_mul_f32_e32 v88,v232,v88
v_mul_f32_e32 v89,v232,v89
v_mul_f32_e32 v90,v232,v90
mvav 212,212,24
v_mul_f32_e32 v91,v232,v91
v_mul_f32_e32 v92,v232,v92
v_mul_f32_e32 v93,v232,v93
v_mul_f32_e32 v94,v232,v94
v_mul_f32_e32 v95,v232,v95
mvav 212,244,28
buffer_store_dwordx4 v[176:179],v244,s[8:11],s88 offen offset:0 sc0 sc1
v_fmac_f32_e32 v88,v232,v184
v_fmac_f32_e32 v89,v232,v185
v_fmac_f32_e32 v90,v232,v186
v_fmac_f32_e32 v91,v232,v187
cv 184,88,89
mvav 212,84,8
v_fmac_f32_e32 v92,v232,v188
cv 185,90,91
mvav 212,116,12
v_fmac_f32_e32 v93,v232,v189
v_fmac_f32_e32 v94,v232,v190
v_fmac_f32_e32 v95,v232,v191
cv 186,92,93
cv 187,94,95
mvav 216,216,24
la 40,252,50,3072
v_add_f32_e32 v156,v156,v157
v_add_f32_e32 v141,v156,v239
v_subrev_u32_e32 v237,0x23800000,v141
v_rcp_f32_e32 v141,v141
mvav 216,248,28
v_mul_f32_e32 v233,v241,v141
v_mul_f32_e32 v96,v233,v96
mvav 216,88,8
v_mul_f32_e32 v97,v233,v97
v_mul_f32_e32 v98,v233,v98
v_mul_f32_e32 v99,v233,v99
v_mul_f32_e32 v100,v233,v100
mvav 216,120,12
buffer_store_dwordx4 v[184:187],v244,s[8:11],s88 offen offset:2048 sc0 sc1
v_fmac_f32_e32 v96,v233,v192
v_fmac_f32_e32 v97,v233,v193
v_mul_f32_e32 v101,v233,v101
v_mul_f32_e32 v102,v233,v102
v_mul_f32_e32 v103,v233,v103
mvav 220,220,24
v_fmac_f32_e32 v98,v233,v194
v_fmac_f32_e32 v99,v233,v195
mvav 220,252,28
v_fmac_f32_e32 v100,v233,v196
v_fmac_f32_e32 v101,v233,v197
cv 192,96,97
cv 193,98,99
mvav 220,92,8
la 32,252,50,1024
v_fmac_f32_e32 v102,v233,v198
v_fmac_f32_e32 v103,v233,v199
cv 194,100,101
cv 195,102,103
mvav 220,124,12
v_xor_b32_e32 v225,32,v224
v_xor_b32_e32 v226,64,v224
s_waitcnt lgkmcnt(0)
zvva 0,128,128
tv 138,225,34816
tv 142,225,38912
tv 152,225,40960
s_waitcnt vmcnt(36)
mvva 0,132,132
tv 136,225,32768
tv 154,225,43008
mvva 0,144,136
tv 140,225,36864
tv 158,225,47104
mvva 0,148,140
tv 156,225,45056
v_mul_f32_e32 v104,v233,v104
v_mul_f32_e32 v105,v233,v105
v_mul_f32_e32 v106,v233,v106
zvva 4,148,156
buffer_store_dwordx4 v[192:195],v244,s[8:11],s14 offen offset:1024 sc0 sc1
v_mul_f32_e32 v107,v233,v107
v_mul_f32_e32 v108,v233,v108
v_mul_f32_e32 v109,v233,v109
v_mul_f32_e32 v110,v233,v110
mvva 4,144,152
v_fmac_f32_e32 v104,v233,v200
v_fmac_f32_e32 v105,v233,v201
v_fmac_f32_e32 v106,v233,v202
v_mul_f32_e32 v111,v233,v111
mvva 4,132,148
v_fmac_f32_e32 v107,v233,v203
v_fmac_f32_e32 v108,v233,v204
v_fmac_f32_e32 v109,v233,v205
cv 200,104,105
cv 201,106,107
mvva 4,128,144
v_fmac_f32_e32 v110,v233,v206
v_fmac_f32_e32 v111,v233,v207
cv 202,108,109
cv 203,110,111
s_waitcnt vmcnt(16)
zvva 8,128,160
v_cmp_lt_u32_e64 s[42:43],s48,v236
v_mul_f32_e32 v112,v233,v112
s_or_b64 s[40:41],s[40:41],s[42:43]
v_cmp_lt_u32_e64 s[42:43],s48,v237
mvva 8,132,164
s_or_b64 s[40:41],s[40:41],s[42:43]
v_mul_f32_e32 v113,v233,v113
v_mul_f32_e32 v114,v233,v114
v_mul_f32_e32 v115,v233,v115
mvva 8,144,168
dl 245,36,51,0
v_mul_f32_e32 v116,v233,v116
v_mul_f32_e32 v117,v233,v117
v_mul_f32_e32 v118,v233,v118
v_mul_f32_e32 v119,v233,v119
v_mul_f32_e32 v120,v233,v120
mvva 8,148,172
v_fmac_f32_e32 v112,v233,v208
v_fmac_f32_e32 v113,v233,v209
v_fmac_f32_e32 v114,v233,v210
v_fmac_f32_e32 v115,v233,v211
zvva 12,148,188
v_fmac_f32_e32 v116,v233,v212
v_fmac_f32_e32 v117,v233,v213
cv 208,112,113
cv 209,114,115
mvva 12,144,184
v_fmac_f32_e32 v118,v233,v214
v_fmac_f32_e32 v119,v233,v215
cv 210,116,117
cv 211,118,119
mvva 12,132,180
v_mul_f32_e32 v121,v233,v121
v_mul_f32_e32 v122,v233,v122
v_mul_f32_e32 v123,v233,v123
mvva 12,128,176
v_mul_f32_e32 v124,v233,v124
v_mul_f32_e32 v125,v233,v125
v_mul_f32_e32 v126,v233,v126
v_mul_f32_e32 v127,v233,v127
s_waitcnt lgkmcnt(0)
zvva 28,136,176
buffer_store_dwordx4 v[200:203],v244,s[8:11],s14 offen offset:3072 sc0 sc1
tv 144,226,40960
v_fmac_f32_e32 v120,v233,v216
v_fmac_f32_e32 v121,v233,v217
v_fmac_f32_e32 v122,v233,v218
v_fmac_f32_e32 v123,v233,v219
mvva 28,140,180
tv 128,226,32768
tv 130,226,34816
cv 216,120,121
cv 217,122,123
mvva 28,152,184
tv 132,226,36864
tv 134,226,38912
v_fmac_f32_e32 v124,v233,v220
v_fmac_f32_e32 v125,v233,v221
v_fmac_f32_e32 v126,v233,v222
mvva 28,156,188
tv 146,226,43008
v_fmac_f32_e32 v127,v233,v223
cv 218,124,125
cv 219,126,127
zvva 24,156,172
tv 148,226,45056
tv 150,226,47104
mvva 24,152,168
v_xor_b32_e32 v227,96,v224
v_add_u32_e32 v232,131072,v224
mvva 24,140,164
buffer_store_dwordx4 v[208:211],v244,s[8:11],s88 offen offset:1024 sc0 sc1
mvva 24,136,160
v_add_u32_e32 v233,131072,v225
v_add_u32_e32 v234,131072,v226
zvva 20,136,144
v_add_u32_e32 v235,131072,v227
v_mov_b32_e32 v242,83914752
mvva 20,140,148
v_add_u32_e32 v228,65536,v224
v_add_u32_e32 v229,65536,v225
mvva 20,152,152
v_add_u32_e32 v230,65536,v226
v_add_u32_e32 v231,65536,v227
mvva 20,156,156
v_and_b32_e32 v243,240,v244
v_lshlrev_b32_e32 v243,7,v243
zvva 16,156,140
buffer_store_dwordx4 v[216:219],v244,s[8:11],s88 offen offset:3072 sc0 sc1
s_cmp_lg_u64 s[40:41],0
s_cbranch_scc1 L_redo
L_redo_r4:
mvva 16,152,136
tv 156,227,45056
v_mov_b32_e32 v236,16256
mvva 16,140,132
tv 158,227,47104
v_and_b32_e32 v248,256,v244
mvva 16,136,128
tv 152,227,40960
v_lshrrev_b32_e32 v248,3,v248
s_waitcnt lgkmcnt(3)
zvva 32,128,128
tv 140,227,36864
v_add_u32_e32 v243,v243,v248
mvva 32,132,132
tv 136,227,32768
tv 138,227,34816
mvva 32,144,136
dl 246,36,51,1024
tv 142,227,38912
mvva 32,148,140
tv 154,227,43008
v_and_b32_e32 v248,512,v244
zvva 36,148,156
v_lshrrev_b32_e32 v248,5,v248
v_add_u32_e32 v243,v243,v248
mvva 36,144,152
v_and_b32_e32 v248,16,v244
v_lshlrev_b32_e32 v248,v248,v236
mvva 36,132,148
v_cndmask_b32_e64 v238,0,v248,s[74:75]
s_mov_b32 s74,0xc0000c
mvva 36,128,144
s_mov_b32 s75,0xc0000c00
v_mov_b32_e32 v240,0
v_cndmask_b32_e64 v239,0,v248,s[74:75]
zvva 40,128,160
dl 249,36,51,2048
mvva 40,132,164
v_mov_b32_e32 v241,0
v_mov_b32_e32 v236,0
mvva 40,144,168
v_mov_b32_e32 v237,0
mvva 40,148,172
zvva 44,148,188
mvva 44,144,184
mvva 44,132,180
dl 247,36,51,3072
s_add_u32 m0,s52,16384
s_xor_b32 s51,s65,0
mvva 44,128,176
tv 144,224,57344
s_waitcnt lgkmcnt(1)
zvva 60,136,176
tv 132,224,53248
tv 134,224,55296
mvva 60,140,180
tv 128,224,49152
tv 130,224,51200
mvva 60,152,184
mvva 60,156,188
tv 146,224,59392
zvva 56,156,172
dl 245,36,51,0
tv 148,224,61440
tv 150,224,63488
mvva 56,152,168
mvva 56,140,164
mvva 56,136,160
zvva 52,136,144
mvva 52,140,148
mvva 52,152,152
dl 246,36,51,1024
mvva 52,156,156
zvva 48,156,140
mvva 48,152,136
mvva 48,140,132
tv 156,225,61440
mvva 48,136,128
tv 152,225,57344
s_waitcnt lgkmcnt(2)
zvva 64,128,128
dl 249,36,51,2048
tv 140,225,53248
mvva 64,132,132
tv 136,225,49152
tv 138,225,51200
mvva 64,144,136
tv 142,225,55296
mvva 64,148,140
tv 154,225,59392
zvva 68,148,156
tv 158,225,63488
mvva 68,144,152
mvva 68,132,148
dl 247,36,51,3072
s_lshl_b32 s51,s33,5
s_add_u32 m0,s52,98304
s_xor_b32 s51,s64,s51
mvva 68,128,144
zvva 72,128,160
mvva 72,132,164
s_waitcnt vmcnt(18)
s_mov_b32 s90,0
mvva 72,144,168
s_xor_b32 s50,s67,0
mvva 72,148,172
zvva 76,148,188
dl 245,36,51,0
mvva 76,144,184
mvva 76,132,180
tv 148,226,61440
mvva 76,128,176
tv 144,226,57344
s_waitcnt lgkmcnt(2)
zvva 92,136,176
tv 132,226,53248
mvva 92,140,180
tv 128,226,49152
tv 130,226,51200
mvva 92,152,184
dl 246,36,51,1024
tv 134,226,55296
mvva 92,156,188
tv 146,226,59392
zvva 88,156,172
tv 150,226,63488
mvva 88,152,168
mvva 88,140,164
mvva 88,136,160
zvva 84,136,144
dl 249,36,51,2048
mvva 84,140,148
mvva 84,152,152
mvva 84,156,156
zvva 80,156,140
mvva 80,152,136
mvva 80,140,132
dl 247,36,51,3072
mvva 80,136,128
s_lshl_b32 s51,s33,5
tv 152,227,57344
s_add_u32 m0,s52,114688
s_waitcnt lgkmcnt(1)
zvva 96,128,128
s_xor_b32 s51,s65,s51
tv 140,227,53248
tv 142,227,55296
mvva 96,132,132
tv 136,227,49152
tv 138,227,51200
mvva 96,144,136
mvva 96,148,140
tv 154,227,59392
zvva 100,148,156
lv 164,252,50,0
tv 156,227,61440
tv 158,227,63488
mvva 100,144,152
mvva 100,132,148
mvva 100,128,144
zvva 104,128,160
mvva 104,132,164
mvva 104,144,168
lv 172,252,50,2048
mvva 104,148,172
zvva 108,148,188
mvva 108,144,184
mvva 108,132,180
mvva 108,128,176
s_barrier
s_waitcnt lgkmcnt(0)
zvva 124,136,176
lv 180,250,50,0
tv 132,232,4096
tv 144,232,8192
tv 146,232,10240
mvva 124,140,180
tv 128,232,0
tv 130,232,2048
mvva 124,152,184
tv 134,232,6144
mvva 124,156,188
tv 148,232,12288
tv 150,232,14336
zvva 120,156,172
mvva 120,152,168
mvva 120,140,164
lv 188,250,50,2048
mvva 120,136,160
zvva 116,136,144
mvva 116,140,148
mvva 116,152,152
mvva 116,156,156
zvva 112,156,140
lv 220,250,50,3072
mvva 112,152,136
mvva 112,140,132
tv 156,233,12288
mvva 112,136,128
tv 152,233,8192
s_waitcnt lgkmcnt(2)
mvva 0,128,4
tv 140,233,4096
mvva 0,132,12
tv 136,233,0
tv 138,233,2048
mvva 0,144,20
lv 212,250,50,1024
tv 142,233,6144
mvva 0,148,28
tv 154,233,10240
mvva 4,148,60
tv 158,233,14336
mvva 4,144,52
mvva 4,132,44
mvva 4,128,36
mvva 8,128,0
lv 204,252,50,3072
mvva 8,132,8
mvva 8,144,16
mvva 8,148,24
mvva 12,148,56
s_waitcnt vmcnt(27)
mvva 12,144,48
s_waitcnt vmcnt(25)
mvva 12,132,40
lv 196,252,50,1024
s_waitcnt vmcnt(24)
mvva 12,128,32
tv 144,234,8192
s_waitcnt lgkmcnt(1)
mvva 28,136,32
tv 132,234,4096
tv 134,234,6144
mvva 28,140,40
tv 128,234,0
tv 130,234,2048
mvva 28,152,48
mvva 28,156,56
tv 146,234,10240
mvva 24,156,24
dl 245,36,51,0
s_waitcnt vmcnt(20)
s_barrier
tv 148,234,12288
tv 150,234,14336
mvva 24,152,16
mvva 24,140,8
mvva 24,136,0
mvva 20,136,36
mvva 20,140,44
mvva 20,152,52
s_cmp_eq_u32 s12,0
s_cselect_b64 exec,1,0
buffer_store_dword v251,off,s[8:11],s61 sc0 sc1
s_mov_b64 exec,-1
mvva 20,156,60
mvva 16,156,28
mvva 16,152,20
mvva 16,140,12
tv 156,235,12288
mvva 16,136,4
tv 152,235,8192
s_waitcnt lgkmcnt(2)
mvva 32,128,4
dl 246,36,51,1024
tv 140,235,4096
mvva 32,132,12
tv 136,235,0
tv 138,235,2048
mvva 32,144,20
tv 142,235,6144
mvva 32,148,28
tv 154,235,10240
mvva 36,148,60
tv 158,235,14336
mvva 36,144,52
mvva 36,132,44
dl 249,36,51,2048
mvva 36,128,36
mvva 40,128,0
mvva 40,132,8
mvva 40,144,16
mvva 40,148,24
mvva 44,148,56
dl 247,36,51,3072
s_lshl_b32 s51,s34,5
s_add_u32 m0,s52,65536
s_xor_b32 s51,s64,s51
mvva 44,144,48
tv 148,232,28672
mvva 44,132,40
s_xor_b32 s53,s60,s33
tv 150,232,30720
mvva 44,128,32
tv 144,232,24576
s_waitcnt lgkmcnt(3)
mvva 60,136,32
tv 132,232,20480
mvva 60,140,40
tv 128,232,16384
tv 130,232,18432
mvva 60,152,48
dl 245,36,51,0
tv 134,232,22528
mvva 60,156,56
tv 146,232,26624
mvva 56,156,24
mvva 56,152,16
mvva 56,140,8
mvva 56,136,0
mvva 52,136,36
buffer_load_dword v253,v242,s[8:11],s53 offen sc1
mvva 52,140,44
mvva 52,152,52
mvva 52,156,60
mvva 48,156,28
mvva 48,152,20
mvva 48,140,12
dl 246,36,51,1024
mvva 48,136,4
tv 152,233,24576
s_waitcnt lgkmcnt(1)
mvva 64,128,4
tv 140,233,20480
tv 142,233,22528
mvva 64,132,12
tv 136,233,16384
tv 138,233,18432
mvva 64,144,20
mvva 64,148,28
tv 154,233,26624
mvva 68,148,60
dl 249,36,51,2048
tv 156,233,28672
tv 158,233,30720
mvva 68,144,52
mvva 68,132,44
mvva 68,128,36
mvva 72,128,0
mvva 72,132,8
mvva 72,144,16
dl 247,36,51,3072
s_lshl_b32 s51,s34,5
s_add_u32 m0,s52,81920
s_xor_b32 s51,s65,s51
mvva 72,148,24
L_vs20:
s_cbranch_execz L_vs24
mvva 76,148,56
mvva 76,144,48
mvva 76,132,40
tv 148,234,28672
mvva 76,128,32
tv 144,234,24576
s_waitcnt lgkmcnt(2)
mvva 92,136,32
dl 245,36,51,0
tv 132,234,20480
mvva 92,140,40
tv 128,234,16384
tv 130,234,18432
mvva 92,152,48
tv 134,234,22528
mvva 92,156,56
tv 146,234,26624
mvva 88,156,24
tv 150,234,30720
mvva 88,152,16
mvva 88,140,8
dl 246,36,51,1024
mvva 88,136,0
mvva 84,136,36
mvva 84,140,44
mvva 84,152,52
mvva 84,156,60
mvva 80,156,28
dl 249,36,51,2048
mvva 80,152,20
mvva 80,140,12
tv 156,235,28672
mvva 80,136,4
tv 152,235,24576
s_waitcnt lgkmcnt(2)
mvva 96,128,4
tv 140,235,20480
mvva 96,132,12
tv 136,235,16384
tv 138,235,18432
mvva 96,144,20
dl 247,36,51,3072
s_lshl_b32 s51,s35,5
s_add_u32 m0,s52,32768
s_xor_b32 s51,s64,s51
tv 142,235,22528
mvva 96,148,28
tv 154,235,26624
mvva 100,148,60
tv 158,235,30720
mvva 100,144,52
mvva 100,132,44
mvva 100,128,36
mvva 104,128,0
dl 245,36,51,0
mvva 104,132,8
mvva 104,144,16
mvva 104,148,24
mvva 108,148,56
mvva 108,144,48
mvva 108,132,40
dl 246,36,51,1024
mvva 108,128,32
s_waitcnt vmcnt(27)
s_barrier
s_waitcnt lgkmcnt(0)
mvva 124,136,32
tv 132,224,4096
tv 134,224,6144
tv 144,224,8192
mvva 124,140,40
tv 128,224,0
tv 130,224,2048
mvva 124,152,48
mvva 124,156,56
tv 146,224,10240
mvva 120,156,24
dl 249,36,51,2048
tv 148,224,12288
tv 150,224,14336
mvva 120,152,16
mvva 120,140,8
mvva 120,136,0
mvva 116,136,36
mvva 116,140,44
mvva 116,152,52
dl 247,36,51,3072
s_lshl_b32 s51,s35,5
s_add_u32 m0,s52,49152
s_xor_b32 s51,s65,s51
mvva 116,156,60
mvva 112,156,28
mvva 112,152,20
s_waitcnt vmcnt(11)
v_cmp_ne_u32_e32 vcc,4,v253
mvva 112,140,12
dl 245,36,51,0
s_cbranch_vccnz L_spa3e
L_spra3e:
mvva 112,136,4
s_xor_b32 s50,s67,s33
tv 152,225,8192
s_waitcnt lgkmcnt(1)
mvvv 0,128,164
tv 140,225,4096
tv 142,225,6144
mvvv 0,132,172
tv 136,225,0
tv 138,225,2048
)"
R"(mvvv 0,144,180
mvvv 0,148,188
dl 246,36,51,1024
tv 154,225,10240
mvvv 4,148,220
tv 156,225,12288
tv 158,225,14336
mvvv 4,144,212
mvvv 4,132,204
mvvv 4,128,196
mvvv 8,128,160
la 196,252,50,0
s_xor_b32 s53,s60,s34
mvvv 8,132,168
mvvv 8,144,176
mvvv 8,148,184
mvvv 12,148,216
mvvv 12,144,208
la 204,252,50,2048
mvvv 12,132,200
tv 148,226,12288
mvvv 12,128,192
tv 144,226,8192
s_waitcnt lgkmcnt(2)
mvvv 28,136,192
tv 132,226,4096
mvvv 28,140,200
tv 128,226,0
tv 130,226,2048
mvvv 28,152,208
buffer_load_dword v253,v242,s[8:11],s53 offen sc1
tv 134,226,6144
mvvv 28,156,216
tv 146,226,10240
mvvv 24,156,184
tv 150,226,14336
mvvv 24,152,176
mvvv 24,140,168
mvvv 24,136,160
la 212,250,50,0
mvvv 20,136,196
mvvv 20,140,204
mvvv 20,152,212
mvvv 20,156,220
la 220,250,50,2048
mvvv 16,156,188
mvvv 16,152,180
mvvv 16,140,172
tv 156,227,12288
mvvv 16,136,164
la 252,250,50,3072
tv 152,227,8192
s_waitcnt lgkmcnt(2)
mvvv 32,128,164
tv 140,227,4096
mvvv 32,132,172
tv 136,227,0
tv 138,227,2048
mvvv 32,144,180
tv 142,227,6144
mvvv 32,148,188
la 244,250,50,1024
tv 154,227,10240
mvvv 36,148,220
tv 158,227,14336
mvvv 36,144,212
mvvv 36,132,204
mvvv 36,128,196
la 236,252,50,3072
mvvv 40,128,160
mvvv 40,132,168
mvvv 40,144,176
mvvv 40,148,184
mvvv 44,148,216
la 228,252,50,1024
s_add_u32 s50,s50,s55
mvvv 44,144,208
mvvv 44,132,200
tv 148,224,28672
tv 150,224,30720
mvvv 44,128,192
s_waitcnt lgkmcnt(2)
mvvv 60,136,192
la 192,252,50,0
tv 132,224,20480
mvvv 60,140,200
tv 128,224,16384
tv 130,224,18432
mvvv 60,152,208
tv 134,224,22528
mvvv 60,156,216
tv 144,224,24576
tv 146,224,26624
mvvv 56,156,184
la 200,252,50,2048
mvvv 56,152,176
mvvv 56,140,168
mvvv 56,136,160
mvvv 52,136,196
la 208,250,50,0
mvvv 52,140,204
mvvv 52,152,212
mvvv 52,156,220
mvvv 48,156,188
la 216,250,50,2048
mvvv 48,152,180
mvvv 48,140,172
tv 156,225,28672
mvvv 48,136,164
tv 152,225,24576
s_waitcnt lgkmcnt(2)
mvvv 64,128,164
tv 140,225,20480
mvvv 64,132,172
la 248,250,50,3072
tv 136,225,16384
tv 138,225,18432
mvvv 64,144,180
tv 142,225,22528
mvvv 64,148,188
tv 154,225,26624
mvvv 68,148,220
tv 158,225,30720
mvvv 68,144,212
la 240,250,50,1024
mvvv 68,132,204
mvvv 68,128,196
mvvv 72,128,160
mvvv 72,132,168
la 232,252,50,3072
mvvv 72,144,176
mvvv 72,148,184
mvvv 76,148,216
mvvv 76,144,208
mvvv 76,132,200
la 224,252,50,1024
mvvv 76,128,192
tv 144,226,24576
s_waitcnt lgkmcnt(1)
mvvv 92,136,192
tv 132,226,20480
tv 134,226,22528
mvvv 92,140,200
tv 128,226,16384
tv 130,226,18432
mvvv 92,152,208
dl 249,36,51,2048
mvvv 92,156,216
tv 146,226,26624
mvvv 88,156,184
tv 148,226,28672
tv 150,226,30720
mvvv 88,152,176
mvvv 88,140,168
dl 247,36,51,3072
s_lshl_b32 s51,s98,5
s_xor_b32 s51,s64,s51
s_add_u32 m0,s52,131072
s_xor_b32 s51,s51,4194304
mvvv 88,136,160
s_waitcnt vmcnt(16)
v_cmp_ne_u32_e32 vcc,4,v253
mvvv 84,136,196
s_cbranch_vccnz L_spa4e
L_spra4e:
mvvv 84,140,204
s_xor_b32 s50,s67,s34
mvvv 84,152,212
dl 245,36,51,0
mvvv 84,156,220
mvvv 80,156,188
mvvv 80,152,180
mvvv 80,140,172
la 68,252,50,0
mvvv 80,136,164
tv 152,227,24576
s_waitcnt lgkmcnt(1)
mvvv 96,128,164
tv 140,227,20480
tv 142,227,22528
mvvv 96,132,172
tv 136,227,16384
tv 138,227,18432
mvvv 96,144,180
s_xor_b32 s53,s60,s35
mvvv 96,148,188
la 76,252,50,2048
tv 154,227,26624
mvvv 100,148,220
tv 156,227,28672
tv 158,227,30720
mvvv 100,144,212
mvvv 100,132,204
mvvv 100,128,196
la 84,250,50,0
mvvv 104,128,160
mvvv 104,132,168
mvvv 104,144,176
mvvv 104,148,184
buffer_load_dword v253,v242,s[8:11],s53 offen sc1
mvvv 108,148,216
mvvv 108,144,208
mvvv 108,132,200
mvvv 108,128,192
s_barrier
s_waitcnt lgkmcnt(0)
mvvv 124,136,192
la 92,250,50,2048
tv 132,228,36864
tv 144,228,40960
tv 146,228,43008
mvvv 124,140,200
tv 128,228,32768
tv 130,228,34816
mvvv 124,152,208
tv 134,228,38912
mvvv 124,156,216
tv 148,228,45056
tv 150,228,47104
mvvv 120,156,184
la 124,250,50,3072
mvvv 120,152,176
mvvv 120,140,168
mvvv 120,136,160
mvvv 116,136,196
la 116,250,50,1024
mvvv 116,140,204
mvvv 116,152,212
mvvv 116,156,220
mvvv 112,156,188
la 108,252,50,3072
mvvv 112,152,180
mvvv 112,140,172
tv 156,229,45056
tv 158,229,47104
mvvv 112,136,164
s_waitcnt lgkmcnt(2)
mvva 0,128,196
la 100,252,50,1024
s_add_u32 s50,s50,s55
tv 140,229,36864
mvva 0,132,204
tv 136,229,32768
tv 138,229,34816
s_waitcnt vmcnt(21)
mvva 0,144,212
tv 142,229,38912
mvva 0,148,220
tv 152,229,40960
tv 154,229,43008
mvva 4,148,252
mvva 4,144,244
la 64,252,50,0
mvva 4,132,236
s_waitcnt vmcnt(21)
mvva 4,128,228
s_waitcnt vmcnt(20)
mvva 8,128,192
s_waitcnt vmcnt(19)
mvva 8,132,200
la 72,252,50,2048
s_waitcnt vmcnt(19)
mvva 8,144,208
s_waitcnt vmcnt(18)
mvva 8,148,216
s_waitcnt vmcnt(17)
mvva 12,148,248
s_waitcnt vmcnt(16)
mvva 12,144,240
la 80,250,50,0
s_waitcnt vmcnt(16)
mvva 12,132,232
tv 148,230,45056
s_waitcnt vmcnt(15)
mvva 12,128,224
tv 144,230,40960
s_waitcnt lgkmcnt(2)
mvva 28,136,224
tv 132,230,36864
mvva 28,140,232
la 88,250,50,2048
tv 128,230,32768
tv 130,230,34816
mvva 28,152,240
tv 134,230,38912
mvva 28,156,248
tv 146,230,43008
mvva 24,156,216
tv 150,230,47104
mvva 24,152,208
la 120,250,50,3072
mvva 24,140,200
mvva 24,136,192
mvva 20,136,228
mvva 20,140,236
mvva 20,152,244
la 112,250,50,1024
mvva 20,156,252
mvva 16,156,220
mvva 16,152,212
mvva 16,140,204
la 104,252,50,3072
mvva 16,136,196
tv 152,231,40960
s_waitcnt lgkmcnt(1)
mvva 32,128,196
tv 140,231,36864
tv 142,231,38912
mvva 32,132,204
tv 136,231,32768
tv 138,231,34816
mvva 32,144,212
la 96,252,50,1024
mvva 32,148,220
tv 154,231,43008
mvva 36,148,252
tv 156,231,45056
tv 158,231,47104
mvva 36,144,244
mvva 36,132,236
mvva 36,128,228
dl 246,36,51,1024
mvva 40,128,192
mvva 40,132,200
mvva 40,144,208
mvva 40,148,216
dl 249,36,51,2048
mvva 44,148,248
mvva 44,144,240
mvva 44,132,232
tv 148,228,61440
mvva 44,128,224
dl 247,36,51,3072
s_lshl_b32 s51,s98,5
s_xor_b32 s51,s65,s51
s_add_u32 m0,s52,147456
s_xor_b32 s51,s51,4194304
tv 144,228,57344
s_waitcnt lgkmcnt(2)
mvva 60,136,224
tv 132,228,53248
mvva 60,140,232
tv 128,228,49152
tv 130,228,51200
s_waitcnt vmcnt(16)
v_cmp_ne_u32_e32 vcc,4,v253
mvva 60,152,240
s_cbranch_vccnz L_spa5e
L_spra5e:
tv 134,228,55296
s_xor_b32 s50,s67,s35
mvva 60,156,248
dl 245,36,51,0
tv 146,228,59392
mvva 56,156,216
tv 150,228,63488
mvva 56,152,208
mvva 56,140,200
mvva 56,136,192
la 132,252,50,0
mvva 52,136,228
mvva 52,140,236
mvva 52,152,244
mvva 52,156,252
mvva 48,156,220
la 140,252,50,2048
s_xor_b32 s53,s60,s98
s_xor_b32 s53,s53,512
mvva 48,152,212
tv 156,229,61440
mvva 48,140,204
tv 158,229,63488
mvva 48,136,196
tv 152,229,57344
s_waitcnt lgkmcnt(3)
mvva 64,128,196
la 148,250,50,0
tv 140,229,53248
mvva 64,132,204
tv 136,229,49152
tv 138,229,51200
mvva 64,144,212
tv 142,229,55296
mvva 64,148,220
tv 154,229,59392
mvva 68,148,252
buffer_load_dword v253,v242,s[8:11],s53 offen sc1
mvva 68,144,244
mvva 68,132,236
mvva 68,128,228
mvva 72,128,192
mvva 72,132,200
la 156,250,50,2048
mvva 72,144,208
mvva 72,148,216
mvva 76,148,248
mvva 76,144,240
la 188,250,50,3072
mvva 76,132,232
tv 148,230,61440
mvva 76,128,224
tv 144,230,57344
s_waitcnt lgkmcnt(2)
mvva 92,136,224
tv 132,230,53248
mvva 92,140,232
la 180,250,50,1024
tv 128,230,49152
tv 130,230,51200
mvva 92,152,240
tv 134,230,55296
mvva 92,156,248
tv 146,230,59392
mvva 88,156,216
tv 150,230,63488
mvva 88,152,208
la 172,252,50,3072
mvva 88,140,200
mvva 88,136,192
mvva 84,136,228
mvva 84,140,236
la 164,252,50,1024
s_add_u32 s50,s50,s55
mvva 84,152,244
mvva 84,156,252
mvva 80,156,220
mvva 80,152,212
mvva 80,140,204
la 128,252,50,0
mvva 80,136,196
tv 152,231,57344
s_waitcnt lgkmcnt(1)
mvva 96,128,196
L_vs21:
s_cbranch_execz L_vs17
tv 140,231,53248
tv 142,231,55296
mvva 96,132,204
tv 136,231,49152
tv 138,231,51200
mvva 96,144,212
la 136,252,50,2048
mvva 96,148,220
tv 154,231,59392
mvva 100,148,252
tv 156,231,61440
tv 158,231,63488
mvva 100,144,244
mvva 100,132,236
la 144,250,50,0
mvva 100,128,228
mvva 104,128,192
mvva 104,132,200
mvva 104,144,208
la 152,250,50,2048
mvva 104,148,216
mvva 108,148,248
mvva 108,144,240
mvva 108,132,232
la 184,250,50,3072
mvva 108,128,224
s_barrier
s_waitcnt lgkmcnt(0)
mvva 124,136,224
tv 132,228,4096
tv 134,228,6144
tv 144,228,8192
mvva 124,140,232
tv 128,228,0
tv 130,228,2048
mvva 124,152,240
mvva 124,156,248
la 176,250,50,1024
tv 146,228,10240
mvva 120,156,216
tv 148,228,12288
tv 150,228,14336
mvva 120,152,208
mvva 120,140,200
mvva 120,136,192
la 168,252,50,3072
mvva 116,136,228
mvva 116,140,236
mvva 116,152,244
mvva 116,156,252
la 160,252,50,1024
mvva 112,156,220
mvva 112,152,212
mvva 112,140,204
tv 156,229,12288
tv 158,229,14336
mvva 112,136,196
s_waitcnt lgkmcnt(2)
mvva 0,128,68
dl 246,36,51,1024
tv 140,229,4096
mvva 0,132,76
tv 136,229,0
tv 138,229,2048
mvva 0,144,84
tv 142,229,6144
s_waitcnt vmcnt(22)
mvva 0,148,92
tv 152,229,8192
tv 154,229,10240
mvva 4,148,124
dl 249,36,51,2048
mvva 4,144,116
mvva 4,132,108
mvva 4,128,100
mvva 8,128,64
dl 247,36,51,3072
s_lshl_b32 s51,s98,5
s_xor_b32 s51,s64,s51
s_add_u32 m0,s52,0
s_xor_b32 s51,s51,4456448
mvva 8,132,72
mvva 8,144,80
mvva 8,148,88
s_waitcnt vmcnt(16)
v_cmp_ne_u32_e32 vcc,4,v253
mvva 12,148,120
dl 245,36,51,0
s_cbranch_vccnz L_spa6e
L_spra6e:
mvva 12,144,112
s_xor_b32 s50,s67,s98
mvva 12,132,104
s_xor_b32 s50,s50,131072
tv 148,230,12288
tv 150,230,14336
mvva 12,128,96
s_waitcnt lgkmcnt(2)
mvva 28,136,96
dl 246,36,51,1024
tv 132,230,4096
mvva 28,140,104
tv 128,230,0
tv 130,230,2048
mvva 28,152,112
tv 134,230,6144
mvva 28,156,120
tv 144,230,8192
tv 146,230,10240
mvva 24,156,88
mvva 24,152,80
dl 249,36,51,2048
mvva 24,140,72
s_xor_b32 s53,s60,s98
mvva 24,136,64
s_xor_b32 s53,s53,8704
mvva 20,136,100
mvva 20,140,108
dl 247,36,51,3072
mvva 20,152,116
s_lshl_b32 s51,s98,5
s_add_u32 m0,s52,16384
s_xor_b32 s51,s65,s51
mvva 20,156,124
s_xor_b32 s51,s51,4456448
mvva 16,156,92
mvva 16,152,84
buffer_load_dword v253,v242,s[8:11],s53 offen sc1
mvva 16,140,76
tv 156,231,12288
mvva 16,136,68
tv 152,231,8192
s_waitcnt lgkmcnt(2)
mvva 32,128,68
tv 140,231,4096
mvva 32,132,76
la 4,252,50,0
tv 136,231,0
tv 138,231,2048
mvva 32,144,84
tv 142,231,6144
mvva 32,148,92
tv 154,231,10240
mvva 36,148,124
tv 158,231,14336
mvva 36,144,116
la 12,252,50,2048
mvva 36,132,108
mvva 36,128,100
mvva 40,128,64
mvva 40,132,72
mvva 40,144,80
la 20,250,50,0
mvva 40,148,88
mvva 44,148,120
mvva 44,144,112
mvva 44,132,104
la 28,250,50,2048
mvva 44,128,96
tv 144,228,24576
s_waitcnt lgkmcnt(1)
mvva 60,136,96
tv 132,228,20480
tv 134,228,22528
mvva 60,140,104
tv 128,228,16384
tv 130,228,18432
mvva 60,152,112
la 60,250,50,3072
mvva 60,156,120
tv 146,228,26624
mvva 56,156,88
tv 148,228,28672
tv 150,228,30720
mvva 56,152,80
mvva 56,140,72
mvva 56,136,64
la 52,250,50,1024
mvva 52,136,100
mvva 52,140,108
mvva 52,152,116
mvva 52,156,124
la 44,252,50,3072
mvva 48,156,92
mvva 48,152,84
mvva 48,140,76
tv 156,229,28672
mvva 48,136,68
la 36,252,50,1024
s_add_u32 s50,s50,s55
tv 152,229,24576
s_waitcnt lgkmcnt(2)
mvva 64,128,68
tv 140,229,20480
mvva 64,132,76
tv 136,229,16384
tv 138,229,18432
mvva 64,144,84
tv 142,229,22528
mvva 64,148,92
la 0,252,50,0
tv 154,229,26624
mvva 68,148,124
tv 158,229,30720
mvva 68,144,116
mvva 68,132,108
mvva 68,128,100
la 8,252,50,2048
mvva 72,128,64
mvva 72,132,72
mvva 72,144,80
mvva 72,148,88
mvva 76,148,120
la 16,250,50,0
mvva 76,144,112
mvva 76,132,104
tv 148,230,28672
tv 150,230,30720
mvva 76,128,96
s_waitcnt lgkmcnt(2)
mvva 92,136,96
la 24,250,50,2048
tv 132,230,20480
mvva 92,140,104
tv 128,230,16384
tv 130,230,18432
mvva 92,152,112
tv 134,230,22528
mvva 92,156,120
tv 144,230,24576
tv 146,230,26624
mvva 88,156,88
la 56,250,50,3072
mvva 88,152,80
mvva 88,140,72
mvva 88,136,64
mvva 84,136,100
mvva 84,140,108
la 48,250,50,1024
mvva 84,152,116
mvva 84,156,124
mvva 80,156,92
mvva 80,152,84
la 40,252,50,3072
mvva 80,140,76
tv 156,231,28672
mvva 80,136,68
tv 152,231,24576
s_waitcnt lgkmcnt(2)
mvva 96,128,68
tv 140,231,20480
mvva 96,132,76
la 32,252,50,1024
tv 136,231,16384
tv 138,231,18432
mvva 96,144,84
tv 142,231,22528
mvva 96,148,92
tv 154,231,26624
mvva 100,148,124
tv 158,231,30720
mvva 100,144,116
dl 245,36,51,0
s_waitcnt vmcnt(17)
v_cmp_ne_u32_e32 vcc,4,v253
mvva 100,132,108
s_cbranch_vccnz L_spa7e
L_spra7e:
mvva 100,128,100
s_xor_b32 s50,s67,s98
mvva 104,128,64
s_xor_b32 s50,s50,139264
mvva 104,132,72
dl 246,36,51,1024
mvva 104,144,80
mvva 104,148,88
mvva 108,148,120
mvva 108,144,112
mvva 108,132,104
lv 164,252,50,0
mvva 108,128,96
s_barrier
s_waitcnt lgkmcnt(0)
mvva 124,136,96
s_xor_b32 s53,s60,s98
tv 132,224,36864
tv 134,224,38912
tv 144,224,40960
s_xor_b32 s53,s53,16896
mvva 124,140,104
tv 128,224,32768
tv 130,224,34816
mvva 124,152,112
lv 172,252,50,2048
mvva 124,156,120
tv 146,224,43008
mvva 120,156,88
tv 148,224,45056
tv 150,224,47104
mvva 120,152,80
mvva 120,140,72
buffer_load_dword v253,v242,s[8:11],s53 offen sc1
mvva 120,136,64
mvva 116,136,100
mvva 116,140,108
mvva 116,152,116
lv 180,250,50,0
mvva 116,156,124
mvva 112,156,92
mvva 112,152,84
mvva 112,140,76
lv 188,250,50,2048
mvva 112,136,68
tv 152,225,40960
s_waitcnt lgkmcnt(1)
mvva 0,128,132
tv 140,225,36864
tv 142,225,38912
mvva 0,132,140
tv 136,225,32768
tv 138,225,34816
mvva 0,144,148
mvva 0,148,156
lv 220,250,50,3072
tv 154,225,43008
mvva 4,148,188
tv 156,225,45056
tv 158,225,47104
mvva 4,144,180
mvva 4,132,172
mvva 4,128,164
lv 212,250,50,1024
mvva 8,128,128
mvva 8,132,136
mvva 8,144,144
mvva 8,148,152
lv 204,252,50,3072
mvva 12,148,184
mvva 12,144,176
mvva 12,132,168
tv 148,226,45056
tv 150,226,47104
mvva 12,128,160
s_waitcnt lgkmcnt(2)
mvva 28,136,160
lv 196,252,50,1024
s_add_u32 s50,s50,s55
tv 132,226,36864
mvva 28,140,168
tv 128,226,32768
tv 130,226,34816
mvva 28,152,176
tv 134,226,38912
mvva 28,156,184
tv 144,226,40960
tv 146,226,43008
mvva 24,156,152
lv 160,252,50,0
mvva 24,152,144
mvva 24,140,136
mvva 24,136,128
mvva 20,136,164
lv 168,252,50,2048
mvva 20,140,172
mvva 20,152,180
mvva 20,156,188
mvva 16,156,156
lv 176,250,50,0
mvva 16,152,148
mvva 16,140,140
tv 156,227,45056
tv 158,227,47104
mvva 16,136,132
s_waitcnt lgkmcnt(2)
mvva 32,128,132
lv 184,250,50,2048
tv 140,227,36864
mvva 32,132,140
tv 136,227,32768
tv 138,227,34816
mvva 32,144,148
tv 142,227,38912
mvva 32,148,156
tv 152,227,40960
tv 154,227,43008
mvva 36,148,188
mvva 36,144,180
lv 216,250,50,3072
mvva 36,132,172
mvva 36,128,164
mvva 40,128,128
mvva 40,132,136
lv 208,250,50,1024
mvva 40,144,144
mvva 40,148,152
mvva 44,148,184
mvva 44,144,176
lv 200,252,50,3072
mvva 44,132,168
tv 148,224,61440
mvva 44,128,160
tv 144,224,57344
s_waitcnt lgkmcnt(2)
mvva 60,136,160
tv 132,224,53248
mvva 60,140,168
tv 128,224,49152
tv 130,224,51200
mvva 60,152,176
lv 192,252,50,1024
tv 134,224,55296
mvva 60,156,184
tv 146,224,59392
mvva 56,156,152
tv 150,224,63488
mvva 56,152,144
mvva 56,140,136
dl 249,36,51,2048
mvva 56,136,128
mvva 52,136,164
mvva 52,140,172
mvva 52,152,180
dl 247,36,51,3072
s_lshl_b32 s51,s98,5
s_xor_b32 s51,s64,s51
s_add_u32 m0,s52,98304
s_xor_b32 s51,s51,4718592
mvva 52,156,188
mvva 48,156,156
mvva 48,152,148
mvva 48,140,140
dl 245,36,51,0
mvva 48,136,132
tv 152,225,57344
s_waitcnt vmcnt(17)
v_cmp_ne_u32_e32 vcc,4,v253
s_waitcnt lgkmcnt(1)
mvva 64,128,132
s_cbranch_vccnz L_spa8e
L_spra8e:
tv 140,225,53248
tv 142,225,55296
mvva 64,132,140
s_xor_b32 s50,s67,s98
tv 136,225,49152
tv 138,225,51200
mvva 64,144,148
dl 246,36,51,1024
mvva 64,148,156
s_xor_b32 s50,s50,147456
tv 154,225,59392
mvva 68,148,188
tv 156,225,61440
tv 158,225,63488
mvva 68,144,180
mvva 68,132,172
mvva 68,128,164
dl 249,36,51,2048
mvva 72,128,128
mvva 72,132,136
mvva 72,144,144
s_xor_b32 s53,s60,s98
mvva 72,148,152
dl 247,36,51,3072
s_xor_b32 s53,s53,25088
mvva 76,148,184
s_lshl_b32 s51,s98,5
s_add_u32 m0,s52,114688
s_xor_b32 s51,s65,s51
mvva 76,144,176
s_xor_b32 s51,s51,4718592
mvva 76,132,168
tv 148,226,61440
mvva 76,128,160
buffer_load_dword v253,v242,s[8:11],s53 offen sc1
tv 144,226,57344
s_waitcnt lgkmcnt(2)
mvva 92,136,160
tv 132,226,53248
mvva 92,140,168
tv 128,226,49152
tv 130,226,51200
mvva 92,152,176
tv 134,226,55296
mvva 92,156,184
la 196,252,50,0
tv 146,226,59392
mvva 88,156,152
tv 150,226,63488
mvva 88,152,144
mvva 88,140,136
mvva 88,136,128
la 204,252,50,2048
mvva 84,136,164
mvva 84,140,172
mvva 84,152,180
mvva 84,156,188
mvva 80,156,156
la 212,250,50,0
mvva 80,152,148
mvva 80,140,140
tv 156,227,61440
tv 158,227,63488
mvva 80,136,132
s_waitcnt lgkmcnt(2)
mvva 96,128,132
la 220,250,50,2048
tv 140,227,53248
mvva 96,132,140
tv 136,227,49152
tv 138,227,51200
mvva 96,144,148
tv 142,227,55296
mvva 96,148,156
tv 152,227,57344
tv 154,227,59392
mvva 100,148,188
la 252,250,50,3072
mvva 100,144,180
mvva 100,132,172
mvva 100,128,164
mvva 104,128,128
mvva 104,132,136
la 244,250,50,1024
mvva 104,144,144
mvva 104,148,152
mvva 108,148,184
mvva 108,144,176
la 236,252,50,3072
mvva 108,132,168
mvva 108,128,160
s_barrier
s_waitcnt lgkmcnt(0)
mvva 124,136,160
tv 132,232,4096
tv 144,232,8192
mvva 124,140,168
L_vs22:
s_cbranch_execz L_vr1
la 228,252,50,1024
s_add_u32 s50,s50,s55
tv 128,232,0
tv 130,232,2048
mvva 124,152,176
tv 134,232,6144
mvva 124,156,184
tv 146,232,10240
mvva 120,156,152
tv 148,232,12288
tv 150,232,14336
mvva 120,152,144
la 192,252,50,0
mvva 120,140,136
mvva 120,136,128
mvva 116,136,164
mvva 116,140,172
la 200,252,50,2048
mvva 116,152,180
mvva 116,156,188
mvva 112,156,156
mvva 112,152,148
mvva 112,140,140
la 208,250,50,0
mvva 112,136,132
tv 152,233,8192
)"
R"(s_waitcnt lgkmcnt(1)
mvva 0,128,4
tv 140,233,4096
tv 142,233,6144
mvva 0,132,12
tv 136,233,0
tv 138,233,2048
mvva 0,144,20
la 216,250,50,2048
mvva 0,148,28
tv 154,233,10240
mvva 4,148,60
tv 156,233,12288
tv 158,233,14336
mvva 4,144,52
mvva 4,132,44
la 248,250,50,3072
mvva 4,128,36
mvva 8,128,0
mvva 8,132,8
mvva 8,144,16
la 240,250,50,1024
mvva 8,148,24
mvva 12,148,56
mvva 12,144,48
mvva 12,132,40
la 232,252,50,3072
mvva 12,128,32
tv 144,234,8192
s_waitcnt lgkmcnt(1)
mvva 28,136,32
tv 132,234,4096
tv 134,234,6144
mvva 28,140,40
tv 128,234,0
tv 130,234,2048
mvva 28,152,48
mvva 28,156,56
la 224,252,50,1024
tv 146,234,10240
mvva 24,156,24
tv 148,234,12288
tv 150,234,14336
mvva 24,152,16
mvva 24,140,8
mvva 24,136,0
dl 245,36,51,0
mvva 20,136,36
mvva 20,140,44
s_waitcnt vmcnt(17)
v_cmp_ne_u32_e32 vcc,4,v253
mvva 20,152,52
s_cbranch_vccnz L_spa9e
L_spra9e:
mvva 20,156,60
dl 246,36,51,1024
mvva 16,156,28
s_xor_b32 s50,s67,s98
mvva 16,152,20
s_xor_b32 s50,s50,155648
mvva 16,140,12
tv 156,235,12288
tv 158,235,14336
mvva 16,136,4
s_waitcnt lgkmcnt(2)
mvva 32,128,4
dl 249,36,51,2048
tv 140,235,4096
mvva 32,132,12
tv 136,235,0
tv 138,235,2048
mvva 32,144,20
tv 142,235,6144
mvva 32,148,28
tv 152,235,8192
tv 154,235,10240
mvva 36,148,60
dl 247,36,51,3072
s_lshl_b32 s51,s98,5
s_xor_b32 s51,s64,s51
s_add_u32 m0,s52,65536
s_xor_b32 s51,s51,4980736
s_xor_b32 s53,s60,s98
mvva 36,144,52
s_xor_b32 s53,s53,25216
mvva 36,132,44
mvva 36,128,36
mvva 40,128,0
dl 245,36,51,0
mvva 40,132,8
mvva 40,144,16
mvva 40,148,24
mvva 44,148,56
buffer_load_dword v253,v242,s[8:11],s53 offen sc1
mvva 44,144,48
mvva 44,132,40
tv 148,232,28672
tv 150,232,30720
mvva 44,128,32
s_waitcnt lgkmcnt(2)
mvva 60,136,32
dl 246,36,51,1024
tv 132,232,20480
mvva 60,140,40
tv 128,232,16384
tv 130,232,18432
mvva 60,152,48
tv 134,232,22528
mvva 60,156,56
tv 144,232,24576
tv 146,232,26624
mvva 56,156,24
mvva 56,152,16
dl 249,36,51,2048
mvva 56,140,8
mvva 56,136,0
mvva 52,136,36
mvva 52,140,44
dl 247,36,51,3072
s_lshl_b32 s51,s98,5
s_xor_b32 s51,s64,s51
s_add_u32 m0,s52,81920
s_xor_b32 s51,s51,6029312
mvva 52,152,52
mvva 52,156,60
mvva 48,156,28
mvva 48,152,20
dl 245,36,51,0
mvva 48,140,12
tv 156,233,28672
mvva 48,136,4
tv 152,233,24576
s_waitcnt lgkmcnt(2)
mvva 64,128,4
tv 140,233,20480
mvva 64,132,12
tv 136,233,16384
tv 138,233,18432
mvva 64,144,20
dl 246,36,51,1024
tv 142,233,22528
mvva 64,148,28
tv 154,233,26624
mvva 68,148,60
tv 158,233,30720
mvva 68,144,52
mvva 68,132,44
dl 249,36,51,2048
mvva 68,128,36
mvva 72,128,0
mvva 72,132,8
mvva 72,144,16
dl 247,36,51,3072
mvva 72,148,24
s_lshl_b32 s51,s98,5
s_add_u32 m0,s52,32768
s_xor_b32 s51,s65,s51
mvva 76,148,56
s_xor_b32 s51,s51,4980736
mvva 76,144,48
mvva 76,132,40
la 68,252,50,0
mvva 76,128,32
tv 144,224,8192
s_waitcnt lgkmcnt(1)
mvva 92,136,32
tv 132,224,4096
tv 134,224,6144
mvva 92,140,40
tv 128,224,0
tv 130,224,2048
mvva 92,152,48
la 76,252,50,2048
mvva 92,156,56
tv 146,224,10240
mvva 88,156,24
tv 148,224,12288
tv 150,224,14336
mvva 88,152,16
mvva 88,140,8
mvva 88,136,0
la 84,250,50,0
mvva 84,136,36
mvva 84,140,44
mvva 84,152,52
mvva 84,156,60
la 92,250,50,2048
mvva 80,156,28
mvva 80,152,20
mvva 80,140,12
tv 156,225,12288
mvva 80,136,4
la 124,250,50,3072
tv 152,225,8192
s_waitcnt lgkmcnt(2)
mvvv 0,128,164
tv 140,225,4096
mvvv 0,132,172
tv 136,225,0
tv 138,225,2048
mvvv 0,144,180
tv 142,225,6144
mvvv 0,148,188
la 116,250,50,1024
tv 154,225,10240
mvvv 4,148,220
tv 158,225,14336
mvvv 4,144,212
mvvv 4,132,204
mvvv 4,128,196
la 108,252,50,3072
mvvv 8,128,160
mvvv 8,132,168
mvvv 8,144,176
mvvv 8,148,184
mvvv 12,148,216
la 100,252,50,1024
s_add_u32 s50,s50,s55
mvvv 12,144,208
mvvv 12,132,200
tv 148,226,12288
tv 150,226,14336
mvvv 12,128,192
s_barrier
tv 144,226,8192
s_waitcnt lgkmcnt(3)
mvvv 28,136,192
la 64,252,50,0
mvvv 28,140,200
tv 128,226,0
tv 130,226,2048
mvvv 28,152,208
tv 132,226,4096
tv 134,226,6144
mvvv 28,156,216
tv 146,226,10240
s_waitcnt vmcnt(16)
v_cmp_ne_u32_e32 vcc,4,v253
mvvv 24,156,184
la 72,252,50,2048
s_cbranch_vccnz L_spa10e
L_spra10e:
mvvv 24,152,176
v_and_b32_e32 v242,48,v244
v_lshlrev_b32_e32 v242,3,v242
mvvv 24,140,168
mvvv 24,136,160
mvvv 20,136,196
mvvv 20,140,204
la 80,250,50,0
mvvv 20,152,212
mvvv 20,156,220
mvvv 16,156,188
s_andn2_b32 s53,s61,384
mvvv 16,152,180
la 88,250,50,2048
mvvv 16,140,172
tv 156,227,12288
mvvv 16,136,164
tv 152,227,8192
s_waitcnt lgkmcnt(2)
mvvv 32,128,164
tv 140,227,4096
mvvv 32,132,172
buffer_load_dword v253,v242,s[8:11],s53 offen sc1
tv 136,227,0
tv 138,227,2048
mvvv 32,144,180
tv 142,227,6144
mvvv 32,148,188
tv 154,227,10240
mvvv 36,148,220
tv 158,227,14336
mvvv 36,144,212
la 120,250,50,3072
mvvv 36,132,204
mvvv 36,128,196
mvvv 40,128,160
mvvv 40,132,168
la 112,250,50,1024
mvvv 40,144,176
mvvv 40,148,184
mvvv 44,148,216
mvvv 44,144,208
mvvv 44,132,200
la 104,252,50,3072
mvvv 44,128,192
tv 144,224,24576
s_waitcnt lgkmcnt(1)
mvvv 60,136,192
tv 132,224,20480
tv 134,224,22528
mvvv 60,140,200
tv 128,224,16384
tv 130,224,18432
mvvv 60,152,208
la 96,252,50,1024
s_xor_b32 s50,s67,s98
s_xor_b32 s50,s50,188416
mvvv 60,156,216
tv 146,224,26624
mvvv 56,156,184
tv 148,224,28672
tv 150,224,30720
mvvv 56,152,176
mvvv 56,140,168
la 132,252,50,0
mvvv 56,136,160
mvvv 52,136,196
mvvv 52,140,204
mvvv 52,152,212
la 140,252,50,2048
mvvv 52,156,220
mvvv 48,156,188
mvvv 48,152,180
mvvv 48,140,172
la 148,250,50,0
mvvv 48,136,164
tv 152,225,24576
s_waitcnt lgkmcnt(1)
mvvv 64,128,164
tv 140,225,20480
tv 142,225,22528
mvvv 64,132,172
tv 136,225,16384
tv 138,225,18432
mvvv 64,144,180
mvvv 64,148,188
la 156,250,50,2048
tv 154,225,26624
mvvv 68,148,220
tv 156,225,28672
tv 158,225,30720
mvvv 68,144,212
mvvv 68,132,204
mvvv 68,128,196
la 188,250,50,3072
mvvv 72,128,160
mvvv 72,132,168
mvvv 72,144,176
mvvv 72,148,184
la 180,250,50,1024
mvvv 76,148,216
mvvv 76,144,208
mvvv 76,132,200
tv 148,228,45056
tv 150,228,47104
mvvv 76,128,192
s_waitcnt lgkmcnt(2)
mvvv 92,136,192
la 172,252,50,3072
tv 132,228,36864
mvvv 92,140,200
tv 128,228,32768
tv 130,228,34816
mvvv 92,152,208
tv 134,228,38912
mvvv 92,156,216
tv 144,228,40960
tv 146,228,43008
mvvv 88,156,184
la 164,252,50,1024
s_add_u32 s50,s50,s55
mvvv 88,152,176
mvvv 88,140,168
mvvv 88,136,160
mvvv 84,136,196
la 128,252,50,0
mvvv 84,140,204
mvvv 84,152,212
mvvv 84,156,220
mvvv 80,156,188
la 136,252,50,2048
mvvv 80,152,180
mvvv 80,140,172
tv 156,229,45056
tv 158,229,47104
mvvv 80,136,164
s_waitcnt lgkmcnt(2)
mvva 0,128,196
la 144,250,50,0
tv 140,229,36864
mvva 0,132,204
tv 136,229,32768
tv 138,229,34816
mvva 0,144,212
tv 142,229,38912
mvva 0,148,220
tv 152,229,40960
tv 154,229,43008
mvva 4,148,252
mvva 4,144,244
la 152,250,50,2048
mvva 4,132,236
mvva 4,128,228
mvva 8,128,192
mvva 8,132,200
la 184,250,50,3072
mvva 8,144,208
mvva 8,148,216
s_waitcnt vmcnt(17)
v_cmp_ne_u32_e32 vcc,4,v253
mvva 12,148,248
s_cbranch_vccnz L_sppe
L_sprpe:
mvva 12,144,240
la 176,250,50,1024
mvva 12,132,232
tv 148,230,45056
mvva 12,128,224
tv 144,230,40960
s_waitcnt lgkmcnt(2)
mvva 28,136,224
tv 132,230,36864
mvva 28,140,232
tv 128,230,32768
tv 130,230,34816
mvva 28,152,240
la 168,252,50,3072
tv 134,230,38912
mvva 28,156,248
tv 146,230,43008
mvva 24,156,216
tv 150,230,47104
mvva 24,152,208
mvva 24,140,200
la 160,252,50,1024
mvva 24,136,192
mvva 20,136,228
mvva 20,140,236
mvva 20,152,244
dl 245,36,51,0
mvva 20,156,252
mvva 16,156,220
mvva 16,152,212
mvva 16,140,204
dl 246,36,51,1024
mvva 16,136,196
tv 152,231,40960
s_waitcnt lgkmcnt(1)
mvva 32,128,196
tv 140,231,36864
tv 142,231,38912
mvva 32,132,204
tv 136,231,32768
tv 138,231,34816
mvva 32,144,212
dl 249,36,51,2048
mvva 32,148,220
tv 154,231,43008
mvva 36,148,252
tv 156,231,45056
tv 158,231,47104
mvva 36,144,244
mvva 36,132,236
mvva 36,128,228
dl 247,36,51,3072
s_lshl_b32 s51,s98,5
s_xor_b32 s51,s65,s51
s_add_u32 m0,s52,49152
s_xor_b32 s51,s51,6029312
mvva 40,128,192
mvva 40,132,200
mvva 40,144,208
mvva 40,148,216
dl 245,36,51,0
mvva 44,148,248
mvva 44,144,240
mvva 44,132,232
mvva 44,128,224
dl 246,36,51,1024
s_waitcnt lgkmcnt(0)
mvva 60,136,224
s_barrier
mvva 60,140,232
tv 144,228,57344
tv 146,228,59392
tv 128,228,49152
tv 130,228,51200
mvva 60,152,240
tv 132,228,53248
tv 134,228,55296
mvva 60,156,248
dl 249,36,51,2048
mvva 56,156,216
tv 148,228,61440
tv 150,228,63488
mvva 56,152,208
mvva 56,140,200
mvva 56,136,192
dl 247,36,51,3072
mvva 52,136,228
mvva 52,140,236
mvva 52,152,244
mvva 52,156,252
mvva 48,156,220
mvva 48,152,212
mvva 48,140,204
tv 156,229,61440
mvva 48,136,196
tv 152,229,57344
s_waitcnt lgkmcnt(2)
mvva 64,128,196
tv 140,229,53248
mvva 64,132,204
tv 136,229,49152
tv 138,229,51200
mvva 64,144,212
tv 142,229,55296
mvva 64,148,220
L_vs23:
s_cbranch_execz L_vr1
tv 154,229,59392
mvva 68,148,252
tv 158,229,63488
mvva 68,144,244
mvva 68,132,236
mvva 68,128,228
mvva 72,128,192
mvva 72,132,200
mvva 72,144,208
mvva 72,148,216
mvva 76,148,248
mvva 76,144,240
mvva 76,132,232
tv 148,228,12288
mvva 76,128,224
tv 144,228,8192
s_waitcnt lgkmcnt(2)
mvva 92,136,224
tv 132,228,4096
mvva 92,140,232
tv 128,228,0
tv 130,228,2048
mvva 92,152,240
tv 134,228,6144
mvva 92,156,248
tv 146,228,10240
mvva 88,156,216
tv 150,228,14336
mvva 88,152,208
mvva 88,140,200
mvva 88,136,192
mvva 84,136,228
mvva 84,140,236
mvva 84,152,244
mvva 84,156,252
mvva 80,156,220
mvva 80,152,212
mvva 80,140,204
tv 156,229,12288
mvva 80,136,196
tv 152,229,8192
s_waitcnt lgkmcnt(2)
mvva 0,128,68
tv 140,229,4096
mvva 0,132,76
tv 136,229,0
tv 138,229,2048
mvva 0,144,84
tv 142,229,6144
mvva 0,148,92
tv 154,229,10240
mvva 4,148,124
tv 158,229,14336
mvva 4,144,116
mvva 4,132,108
mvva 4,128,100
mvva 8,128,64
mvva 8,132,72
mvva 8,144,80
mvva 8,148,88
s_waitcnt vmcnt(10)
mvva 12,148,120
mvva 12,144,112
mvva 12,132,104
tv 148,228,28672
mvva 12,128,96
tv 144,228,24576
s_waitcnt lgkmcnt(2)
mvva 28,136,96
tv 132,228,20480
mvva 28,140,104
tv 128,228,16384
tv 130,228,18432
mvva 28,152,112
tv 134,228,22528
mvva 28,156,120
tv 146,228,26624
mvva 24,156,88
tv 150,228,30720
mvva 24,152,80
mvva 24,140,72
mvva 24,136,64
mvva 20,136,100
mvva 20,140,108
mvva 20,152,116
mvva 20,156,124
mvva 16,156,92
mvva 16,152,84
mvva 16,140,76
tv 156,229,28672
mvva 16,136,68
tv 152,229,24576
s_waitcnt lgkmcnt(2)
mvva 0,128,132
tv 140,229,20480
mvva 0,132,140
tv 136,229,16384
tv 138,229,18432
mvva 0,144,148
tv 142,229,22528
mvva 0,148,156
tv 154,229,26624
mvva 4,148,188
tv 158,229,30720
mvva 4,144,180
mvva 4,132,172
mvva 4,128,164
mvva 8,128,128
mvva 8,132,136
cv 0,0,1
cv 1,2,3
mvva 8,144,144
mvva 8,148,152
mvva 12,148,184
mvva 12,144,176
cv 4,4,5
cv 5,6,7
s_waitcnt vmcnt(8)
mvva 12,132,168
tv 148,230,12288
mvva 12,128,160
tv 144,230,8192
cv 8,8,9
s_waitcnt lgkmcnt(2)
mvva 28,136,160
tv 132,230,4096
cv 9,10,11
mvva 28,140,168
tv 128,230,0
tv 130,230,2048
mvva 28,152,176
tv 134,230,6144
cv 12,12,13
mvva 28,156,184
tv 146,230,10240
cv 13,14,15
mvva 24,156,152
tv 150,230,14336
mvva 24,152,144
mvva 24,140,136
mvva 24,136,128
mvva 20,136,164
cv 14,28,29
cv 15,30,31
mvva 20,140,172
buffer_store_dwordx4 v[12:15],v244,s[8:11],s56 offen offset:3072 sc0 sc1
mvva 20,152,180
mvva 20,156,188
mvva 16,156,156
cv 10,24,25
cv 11,26,27
mvva 16,152,148
buffer_store_dwordx4 v[8:11],v244,s[8:11],s56 offen offset:2048 sc0 sc1
mvva 16,140,140
tv 156,231,12288
mvva 16,136,132
cv 6,20,21
cv 7,22,23
s_waitcnt lgkmcnt(1)
mvva 32,128,68
tv 140,231,4096
mvva 32,132,76
tv 136,231,0
tv 138,231,2048
mvva 32,144,84
buffer_store_dwordx4 v[4:7],v244,s[8:11],s56 offen offset:1024 sc0 sc1
tv 142,231,6144
mvva 32,148,92
tv 152,231,8192
tv 154,231,10240
mvva 36,148,124
tv 158,231,14336
cv 2,16,17
cv 3,18,19
mvva 36,144,116
mvva 36,132,108
buffer_store_dwordx4 v[0:3],v244,s[8:11],s56 offen offset:0 sc0 sc1
mvva 36,128,100
mvva 40,128,64
mvva 40,132,72
mvva 40,144,80
mvva 40,148,88
mvva 44,148,120
mvva 44,144,112
mvva 44,132,104
tv 148,230,28672
mvva 44,128,96
s_waitcnt vmcnt(4)
s_barrier
tv 144,230,24576
s_waitcnt lgkmcnt(2)
mvva 60,136,96
tv 132,230,20480
mvva 60,140,104
tv 128,230,16384
tv 130,230,18432
mvva 60,152,112
tv 134,230,22528
mvva 60,156,120
tv 146,230,26624
mvva 56,156,88
tv 150,230,30720
mvva 56,152,80
mvva 56,140,72
mvva 56,136,64
mvva 52,136,100
mvva 52,140,108
mvva 52,152,116
mvva 52,156,124
mvva 48,156,92
mvva 48,152,84
mvva 48,140,76
tv 156,231,28672
mvva 48,136,68
tv 152,231,24576
s_waitcnt lgkmcnt(2)
mvva 32,128,132
tv 140,231,20480
mvva 32,132,140
tv 136,231,16384
tv 138,231,18432
mvva 32,144,148
tv 142,231,22528
mvva 32,148,156
tv 154,231,26624
mvva 36,148,188
tv 158,231,30720
mvva 36,144,180
mvva 36,132,172
mvva 36,128,164
mvva 40,128,128
mvva 40,132,136
cv 32,32,33
cv 33,34,35
mvva 40,144,144
mvva 40,148,152
mvva 44,148,184
mvva 44,144,176
cv 36,36,37
cv 37,38,39
mvva 44,132,168
tv 148,224,45056
mvva 44,128,160
tv 144,224,40960
cv 40,40,41
s_waitcnt lgkmcnt(2)
mvva 60,136,160
tv 132,224,36864
cv 41,42,43
mvva 60,140,168
tv 128,224,32768
tv 130,224,34816
mvva 60,152,176
tv 134,224,38912
cv 44,44,45
mvva 60,156,184
tv 146,224,43008
cv 45,46,47
mvva 56,156,152
tv 150,224,47104
mvva 56,152,144
mvva 56,140,136
mvva 56,136,128
mvva 52,136,164
cv 46,60,61
cv 47,62,63
mvva 52,140,172
buffer_store_dwordx4 v[44:47],v244,s[8:11],s57 offen offset:3072 sc0 sc1
mvva 52,152,180
mvva 52,156,188
mvva 48,156,156
cv 42,56,57
cv 43,58,59
mvva 48,152,148
buffer_store_dwordx4 v[40:43],v244,s[8:11],s57 offen offset:2048 sc0 sc1
mvva 48,140,140
tv 156,225,45056
mvva 48,136,132
cv 38,52,53
cv 39,54,55
s_waitcnt lgkmcnt(1)
mvva 64,128,68
tv 140,225,36864
mvva 64,132,76
buffer_store_dwordx4 v[36:39],v244,s[8:11],s57 offen offset:1024 sc0 sc1
tv 136,225,32768
tv 138,225,34816
mvva 64,144,84
tv 142,225,38912
cv 34,48,49
mvva 64,148,92
tv 152,225,40960
tv 154,225,43008
mvva 68,148,124
tv 158,225,47104
cv 35,50,51
mvva 68,144,116
buffer_store_dwordx4 v[32:35],v244,s[8:11],s57 offen offset:0 sc0 sc1
mvva 68,132,108
mvva 68,128,100
mvva 72,128,64
mvva 72,132,72
mvva 72,144,80
mvva 72,148,88
mvva 76,148,120
mvva 76,144,112
mvva 76,132,104
tv 148,224,61440
mvva 76,128,96
tv 144,224,57344
s_waitcnt lgkmcnt(2)
mvva 92,136,96
tv 132,224,53248
mvva 92,140,104
tv 128,224,49152
tv 130,224,51200
mvva 92,152,112
tv 134,224,55296
mvva 92,156,120
tv 146,224,59392
mvva 88,156,88
tv 150,224,63488
mvva 88,152,80
mvva 88,140,72
mvva 88,136,64
mvva 84,136,100
mvva 84,140,108
mvva 84,152,116
mvva 84,156,124
mvva 80,156,92
mvva 80,152,84
mvva 80,140,76
tv 156,225,61440
mvva 80,136,68
tv 152,225,57344
s_waitcnt lgkmcnt(2)
mvva 64,128,132
tv 140,225,53248
mvva 64,132,140
tv 136,225,49152
tv 138,225,51200
mvva 64,144,148
tv 142,225,55296
mvva 64,148,156
tv 154,225,59392
mvva 68,148,188
tv 158,225,63488
mvva 68,144,180
mvva 68,132,172
mvva 68,128,164
s_xor_b32 s82,s59,12
mvva 72,128,128
s_mul_i32 s82,s82,12288
cv 64,64,65
s_add_u32 s82,s82,0
mvva 72,132,136
lv 16,244,82,0
mvva 72,144,144
cv 65,66,67
cv 68,68,69
mvva 72,148,152
cv 69,70,71
mvva 76,148,184
mvva 76,144,176
mvva 76,132,168
lv 20,244,82,1024
mvva 76,128,160
tv 144,234,24576
s_waitcnt lgkmcnt(1)
mvva 92,136,160
tv 132,234,20480
tv 134,234,22528
mvva 92,140,168
tv 128,234,16384
tv 130,234,18432
mvva 92,152,176
cv 72,72,73
cv 73,74,75
mvva 92,156,184
lv 24,244,82,2048
tv 146,234,26624
mvva 88,156,152
tv 148,234,28672
tv 150,234,30720
mvva 88,152,144
cv 76,76,77
cv 77,78,79
mvva 88,140,136
cv 78,92,93
cv 79,94,95
mvva 88,136,128
mvva 84,136,164
lv 28,244,82,3072
mvva 84,140,172
mvva 84,152,180
mvva 84,156,188
mvva 80,156,156
buffer_store_dwordx4 v[76:79],v244,s[8:11],s58 offen offset:3072 sc0 sc1
mvva 80,152,148
cv 74,88,89
cv 75,90,91
mvva 80,140,140
tv 156,235,28672
mvva 80,136,132
cv 70,84,85
cv 71,86,87
s_waitcnt lgkmcnt(1)
mvva 96,128,4
buffer_store_dwordx4 v[72:75],v244,s[8:11],s58 offen offset:2048 sc0 sc1
tv 140,235,20480
mvva 96,132,12
tv 136,235,16384
tv 138,235,18432
mvva 96,144,20
tv 142,235,22528
cv 66,80,81
mvva 96,148,28
tv 152,235,24576
tv 154,235,26624
mvva 100,148,60
tv 158,235,30720
cv 67,82,83
mvva 100,144,52
buffer_store_dwordx4 v[68:71],v244,s[8:11],s58 offen offset:1024 sc0 sc1
mvva 100,132,44
mvva 100,128,36
mvva 104,128,0
mvva 104,132,8
buffer_store_dwordx4 v[64:67],v244,s[8:11],s58 offen offset:0 sc0 sc1
mvva 104,144,16
mvva 104,148,24
mvva 108,148,56
mvva 108,144,48
mvva 108,132,40
tv 148,226,28672
mvva 108,128,32
tv 144,226,24576
s_waitcnt lgkmcnt(2)
mvva 124,136,32
tv 132,226,20480
mvva 124,140,40
tv 128,226,16384
tv 130,226,18432
mvva 124,152,48
tv 134,226,22528
mvva 124,156,56
tv 146,226,26624
mvva 120,156,24
tv 150,226,30720
mvva 120,152,16
mvva 120,140,8
mvva 120,136,0
mvva 116,136,36
mvva 116,140,44
s_waitcnt vmcnt(7)
v_max_u32_e32 v248,v16,v17
mvva 116,152,52
v_max3_u32 v248,v248,v18,v19
s_waitcnt vmcnt(6)
v_max3_u32 v248,v248,v20,v21
v_max3_u32 v248,v248,v22,v23
mvva 116,156,60
s_waitcnt vmcnt(5)
v_max3_u32 v248,v248,v24,v25
v_max3_u32 v248,v248,v26,v27
s_waitcnt vmcnt(4)
v_max3_u32 v248,v248,v28,v29
mvva 112,156,28
v_max3_u32 v248,v248,v30,v31
v_cmp_eq_u32_e32 vcc,-1,v248
mvva 112,152,20
s_cbranch_vccnz L_spx0e
L_sprx0e:
mvva 112,140,12
tv 156,227,28672
tv 158,227,30720
v_lshlrev_b32_e32 v251,16,v18
v_and_b32_e32 v255,4294901760,v18
mvva 112,136,4
tv 152,227,24576
tv 154,227,26624
s_waitcnt lgkmcnt(4)
mvvv 96,128,164
v_lshlrev_b32_e32 v253,16,v19
v_and_b32_e32 v242,4294901760,v19
mvvv 96,132,172
tv 136,227,16384
tv 138,227,18432
)"
R"(mvvv 96,144,180
tv 140,227,20480
tv 142,227,22528
v_add_f32_e32 v112,v112,v251
mvvv 96,148,188
v_add_f32_e32 v113,v113,v255
v_add_f32_e32 v114,v114,v253
mvvv 100,148,220
v_add_f32_e32 v115,v115,v242
v_lshlrev_b32_e32 v251,16,v22
mvvv 100,144,212
v_and_b32_e32 v255,4294901760,v22
v_lshlrev_b32_e32 v253,16,v23
mvvv 100,132,204
v_and_b32_e32 v242,4294901760,v23
v_add_f32_e32 v116,v116,v251
v_add_f32_e32 v117,v117,v255
mvvv 100,128,196
v_add_f32_e32 v118,v118,v253
v_add_f32_e32 v119,v119,v242
mvvv 104,128,160
v_lshlrev_b32_e32 v251,16,v26
v_and_b32_e32 v255,4294901760,v26
mvvv 104,132,168
v_lshlrev_b32_e32 v253,16,v27
v_and_b32_e32 v242,4294901760,v27
mvvv 104,144,176
v_add_f32_e32 v120,v120,v251
v_add_f32_e32 v121,v121,v255
v_add_f32_e32 v122,v122,v253
mvvv 104,148,184
v_add_f32_e32 v123,v123,v242
v_lshlrev_b32_e32 v251,16,v30
mvvv 108,148,216
v_and_b32_e32 v255,4294901760,v30
v_lshlrev_b32_e32 v253,16,v31
mvvv 108,144,208
v_and_b32_e32 v242,4294901760,v31
v_add_f32_e32 v124,v124,v251
mvvv 108,132,200
v_add_f32_e32 v125,v125,v255
v_add_f32_e32 v126,v126,v253
v_add_f32_e32 v127,v127,v242
mvvv 108,128,192
tv 144,230,57344
v_lshlrev_b32_e32 v251,16,v16
s_waitcnt lgkmcnt(1)
mvvv 124,136,192
tv 132,230,53248
v_and_b32_e32 v255,4294901760,v16
mvvv 124,140,200
tv 128,230,49152
tv 130,230,51200
mvvv 124,152,208
L_vs24:
s_cbranch_execz L_vr1
tv 134,230,55296
v_lshlrev_b32_e32 v253,16,v17
mvvv 124,156,216
tv 146,230,59392
v_and_b32_e32 v242,4294901760,v17
mvvv 120,156,184
tv 148,230,61440
tv 150,230,63488
mvvv 120,152,176
v_add_f32_e32 v96,v96,v251
v_add_f32_e32 v97,v97,v255
mvvv 120,140,168
v_add_f32_e32 v98,v98,v253
v_add_f32_e32 v99,v99,v242
mvvv 120,136,160
v_lshlrev_b32_e32 v251,16,v20
v_and_b32_e32 v255,4294901760,v20
mvvv 116,136,196
v_lshlrev_b32_e32 v253,16,v21
v_and_b32_e32 v242,4294901760,v21
mvvv 116,140,204
v_add_f32_e32 v100,v100,v251
v_add_f32_e32 v101,v101,v255
mvvv 116,152,212
v_add_f32_e32 v102,v102,v253
v_add_f32_e32 v103,v103,v242
mvvv 116,156,220
v_lshlrev_b32_e32 v251,16,v24
v_and_b32_e32 v255,4294901760,v24
mvvv 112,156,188
v_lshlrev_b32_e32 v253,16,v25
v_and_b32_e32 v242,4294901760,v25
mvvv 112,152,180
v_add_f32_e32 v104,v104,v251
v_add_f32_e32 v105,v105,v255
mvvv 112,140,172
tv 156,231,61440
v_add_f32_e32 v106,v106,v253
mvvv 112,136,164
tv 152,231,57344
v_add_f32_e32 v107,v107,v242
s_waitcnt lgkmcnt(2)
mvva 96,128,196
tv 140,231,53248
v_lshlrev_b32_e32 v251,16,v28
mvva 96,132,204
tv 136,231,49152
tv 138,231,51200
mvva 96,144,212
tv 142,231,55296
v_and_b32_e32 v255,4294901760,v28
mvva 96,148,220
tv 154,231,59392
v_lshlrev_b32_e32 v253,16,v29
mvva 100,148,252
tv 158,231,63488
v_and_b32_e32 v242,4294901760,v29
mvva 100,144,244
v_add_f32_e32 v108,v108,v251
v_add_f32_e32 v109,v109,v255
mvva 100,132,236
v_add_f32_e32 v110,v110,v253
v_add_f32_e32 v111,v111,v242
mvva 100,128,228
mvva 104,128,192
mvva 104,132,200
mvva 104,144,208
mvva 104,148,216
mvva 108,148,248
mvva 108,144,240
mvva 108,132,232
tv 148,226,45056
mvva 108,128,224
tv 144,226,40960
s_waitcnt lgkmcnt(2)
mvva 124,136,224
s_xor_b32 s83,s59,8
tv 132,226,36864
s_mul_i32 s83,s83,12288
mvva 124,140,232
s_add_u32 s83,s83,4096
tv 128,226,32768
tv 130,226,34816
mvva 124,152,240
lv 48,244,83,0
tv 134,226,38912
mvva 124,156,248
tv 146,226,43008
mvva 120,156,216
lv 52,244,83,1024
tv 150,226,47104
mvva 120,152,208
mvva 120,140,200
lv 56,244,83,2048
mvva 120,136,192
mvva 116,136,228
lv 60,244,83,3072
mvva 116,140,236
mvva 116,152,244
s_xor_b32 s84,s59,4
mvva 116,156,252
s_mul_i32 s84,s84,12288
mvva 112,156,220
s_add_u32 s84,s84,8192
mvva 112,152,212
lv 80,244,84,0
mvva 112,140,204
tv 156,227,45056
mvva 112,136,196
lv 84,244,84,1024
tv 152,227,40960
s_waitcnt lgkmcnt(2)
mvva 96,128,68
tv 140,227,36864
mvva 96,132,76
lv 88,244,84,2048
tv 136,227,32768
tv 138,227,34816
mvva 96,144,84
tv 142,227,38912
mvva 96,148,92
lv 92,244,84,3072
tv 154,227,43008
mvva 100,148,124
tv 158,227,47104
mvva 100,144,116
mvva 100,132,108
mvva 100,128,100
mvva 104,128,64
mvva 104,132,72
mvva 104,144,80
mvva 104,148,88
mvva 108,148,120
mvva 108,144,112
mvva 108,132,104
tv 148,226,61440
mvva 108,128,96
tv 144,226,57344
s_waitcnt lgkmcnt(2)
mvva 124,136,96
tv 132,226,53248
mvva 124,140,104
tv 128,226,49152
tv 130,226,51200
mvva 124,152,112
tv 134,226,55296
mvva 124,156,120
tv 146,226,59392
mvva 120,156,88
tv 150,226,63488
mvva 120,152,80
mvva 120,140,72
mvva 120,136,64
mvva 116,136,100
mvva 116,140,108
mvva 116,152,116
mvva 116,156,124
mvva 112,156,92
mvva 112,152,84
mvva 112,140,76
tv 156,227,61440
mvva 112,136,68
tv 152,227,57344
s_waitcnt lgkmcnt(2)
mvva 96,128,132
tv 140,227,53248
mvva 96,132,140
tv 136,227,49152
tv 138,227,51200
mvva 96,144,148
tv 142,227,55296
mvva 96,148,156
tv 154,227,59392
mvva 100,148,188
tv 158,227,63488
mvva 100,144,180
mvva 100,132,172
mvva 100,128,164
mvva 104,128,128
mvva 104,132,136
mvva 104,144,144
mvva 104,148,152
s_waitcnt vmcnt(7)
v_max_u32_e32 v248,v48,v49
mvva 108,148,184
v_max3_u32 v248,v248,v50,v51
s_waitcnt vmcnt(6)
v_max3_u32 v248,v248,v52,v53
mvva 108,144,176
v_max3_u32 v248,v248,v54,v55
s_waitcnt vmcnt(5)
v_max3_u32 v248,v248,v56,v57
mvva 108,132,168
v_max3_u32 v248,v248,v58,v59
s_waitcnt vmcnt(4)
v_max3_u32 v248,v248,v60,v61
mvva 108,128,160
v_max3_u32 v248,v248,v62,v63
v_cmp_eq_u32_e32 vcc,-1,v248
s_waitcnt lgkmcnt(0)
mvva 124,136,160
s_cbranch_vccnz L_spx1e
L_sprx1e:
mvva 124,140,168
mvva 124,152,176
mvva 124,156,184
s_waitcnt vmcnt(3)
v_max_u32_e32 v248,v80,v81
mvva 120,156,152
v_max3_u32 v248,v248,v82,v83
s_waitcnt vmcnt(2)
v_max3_u32 v248,v248,v84,v85
mvva 120,152,144
v_max3_u32 v248,v248,v86,v87
s_waitcnt vmcnt(1)
v_max3_u32 v248,v248,v88,v89
mvva 120,140,136
v_max3_u32 v248,v248,v90,v91
s_waitcnt vmcnt(0)
v_max3_u32 v248,v248,v92,v93
mvva 120,136,128
v_max3_u32 v248,v248,v94,v95
v_cmp_eq_u32_e32 vcc,-1,v248
mvva 116,136,164
s_cbranch_vccnz L_spx2e
L_sprx2e:
mvva 116,140,172
mvva 116,152,180
mvva 116,156,188
mvva 112,156,156
mvva 112,152,148
mvva 112,140,140
mvva 112,136,132
mvvv 108,238,60
mvvv 108,238,92
mvvv 124,236,60
mvvv 124,236,92
mvvv 104,238,56
mvvv 104,238,88
mvvv 120,236,56
mvvv 120,236,88
mvvv 100,238,52
mvvv 100,238,84
cv 108,108,109
cv 109,110,111
cv 110,124,125
cv 111,126,127
cv 104,104,105
v_permlane16_swap_b32_e32 v108,v110
mvvv 116,236,52
v_permlane16_swap_b32_e32 v109,v111
cv 105,106,107
cv 106,120,121
cv 107,122,123
mvvv 116,236,84
buffer_store_dwordx4 v[108:111],v243,s[44:47],s73 offen sc0 sc1
v_permlane16_swap_b32_e32 v104,v106
v_permlane16_swap_b32_e32 v105,v107
mvvv 96,238,48
cv 100,100,101
cv 101,102,103
mvvv 96,238,80
buffer_store_dwordx4 v[104:107],v243,s[44:47],s72 offen sc0 sc1
cv 102,116,117
cv 103,118,119
mvvv 112,236,48
v_permlane16_swap_b32_e32 v100,v102
v_permlane16_swap_b32_e32 v101,v103
mvvv 112,236,80
buffer_store_dwordx4 v[100:103],v243,s[44:47],s71 offen sc0 sc1
cv 96,96,97
cv 97,98,99
s_nop 4
cv 98,112,113
cv 99,114,115
s_nop 0
v_permlane16_swap_b32_e32 v96,v98
v_permlane16_swap_b32_e32 v97,v99
buffer_store_dwordx4 v[96:99],v243,s[44:47],s70 offen sc0 sc1
s_endpgm
L_odd:
L_vs25:
s_cbranch_execz L_vs29
s_lshl_b32 s86,s21,2
s_sub_u32 s87,s13,s67
s_add_u32 s86,s86,s12
mvaa 64,200,132
s_lshl_b32 s86,s86,8
v_add_u32_e32 v252,s87,v244
s_add_u32 s86,s86,L_odd-L_att_pc+1024
v_lshl_add_u32 v226,v244,3,s86
s_lshl_b32 s86,s12,1
s_bfe_u32 s87,s21,65539
mvaa 64,208,136
s_add_u32 s86,s86,s87
v_min_u32_e32 v226,L_att_end-L_att_pc-4,v226
s_lshl_b32 s86,s86,7
s_add_u32 s86,s86,L_pend2-L_att_pc
v_writelane_b32 v226,s86,2
mvaa 64,216,140
s_mov_b64 exec,7
global_load_dword v253,v226,s[96:97]
s_mov_b64 exec,-1
cvaa 68,220,140,232
s_add_u32 m0,s52,32768
v_and_b32_e32 v224,16,v244
v_lshlrev_b32_e32 v224,5,v224
mvaa 68,212,136
s_xor_b32 s51,s62,0
v_lshrrev_b32_e32 v239,4,v244
v_lshrrev_b32_e32 v236,3,v239
mvaa 68,204,132
s_lshl_b32 s87,s12,13
v_and_b32_e32 v237,112,v244
v_and_b32_e32 v238,48,v239
mvaa 68,196,128
s_xor_b32 s87,s87,8192
ra 64,225,0
v_lshlrev_b32_e32 v238,1,v238
cvaa 96,224,128,232
buffer_load_dword v253,v224,s[8:11],s53 offen sc1
mvaa 96,232,132
s_mov_b32 s84,7
ra 68,225,1024
v_xor_b32_e32 v237,v237,v238
mvaa 96,240,136
s_add_u32 s81,s81,1
ra 72,225,2048
v_lshl_add_u32 v245,v236,11,v237
mvaa 96,248,140
s_mov_b32 s84,5
v_add_u32_e32 v246,15360,v245
v_add_u32_e32 v249,30720,v245
cvaa 100,252,140,232
s_andn2_b32 s69,64,s54
ra 76,225,3072
s_waitcnt vmcnt(9)
v_add_u32_e32 v247,46080,v245
mvaa 100,244,136
dl 245,24,51,0
mvaa 100,236,132
s_add_u32 s88,s14,4096
ra 80,225,4096
cv 8,8,9
mvaa 100,228,128
s_lshl_b32 s35,s19,1
ra 84,225,5120
cv 9,10,11
cvaa 72,192,144,232
s_lshr_b32 s33,40569,s35
ra 88,225,6144
cv 10,12,13
mvaa 72,200,148
s_and_b32 s33,s33,3
ra 92,225,7168
v_add_u32_e32 v250,4096,v252
mvaa 72,208,152
dl 246,24,51,1024
mvaa 72,216,156
s_lshl_b32 s33,s33,13
ra 96,225,8192
v_mov_b32_e32 v251,4
cvaa 76,220,156,232
s_lshr_b32 s34,59294,s35
ra 100,225,9216
s_and_b32 s34,s34,3
mvaa 76,212,152
s_lshl_b32 s34,s34,13
ra 104,225,10240
s_xor_b32 s35,s33,s34
mvaa 76,204,148
s_and_b32 s98,s19,3
ra 108,225,11264
s_lshl_b32 s98,s98,13
mvaa 76,196,144
dl 249,24,51,2048
cvaa 104,224,144,232
s_mov_b32 s74,0x300003
ra 112,225,12288
s_mov_b32 s75,0x30000300
mvaa 104,232,148
s_lshl_b32 s71,s12,15
ra 116,225,13312
s_add_u32 s71,s71,s54
mvaa 104,240,152
s_mov_b64 s[44:45],s[30:31]
ra 120,225,14336
s_mov_b32 s46,4194304
mvaa 104,248,156
s_mov_b32 s47,131072
ex 14
cvaa 108,252,156,232
dl 247,24,51,3072
mvaa 108,244,152
s_add_u32 m0,s52,49152
s_xor_b32 s51,s63,0
ra 124,225,15360
mvaa 108,236,148
s_lshl_b32 s70,s23,11
ex 15
mvaa 108,228,144
s_add_u32 s70,s70,s71
ex 40
cvaa 80,192,160,232
ex 41
mvaa 80,200,164
dl 245,24,51,0
mvaa 80,208,168
ex 42
mvaa 80,216,172
ex 43
cvaa 84,220,172,232
ex 44
mvaa 84,212,168
ex 45
mvaa 84,204,164
dl 246,24,51,1024
mvaa 84,196,160
ex 46
cvaa 112,224,160,232
ex 47
mvaa 112,232,164
ex 16
mvaa 112,240,168
ex 17
mvaa 112,248,172
dl 249,24,51,2048
cvaa 116,252,172,232
ex 18
mvaa 116,244,168
ex 19
mvaa 116,236,164
ex 20
mvaa 116,228,160
ex 21
cvaa 88,192,176,232
dl 247,24,51,3072
s_waitcnt vmcnt(8)
v_cmp_ne_u32_e32 vcc,4,v253
mvaa 88,200,180
s_cbranch_vccnz L_spbo
L_sprbo:
ex 22
mvaa 88,208,184
s_xor_b32 s50,s66,262144
ex 23
mvaa 88,216,188
ex 48
cvaa 92,220,188,232
ex 49
mvaa 92,212,184
la 128,252,50,0
mvaa 92,204,180
s_waitcnt lgkmcnt(0)
s_barrier
mvaa 92,196,176
s_mov_b32 m0,s72
ex 50
cvaa 120,224,176,232
s_lshl_b32 s72,s22,11
ex 51
mvaa 120,232,180
s_add_u32 s72,s72,s71
ex 52
mvaa 120,240,184
ds 255,8,76,0
mvaa 120,248,188
s_add_u32 s71,s70,131072
ex 53
cvaa 124,252,188,232
ex 54
mvaa 124,244,184
ex 55
mvaa 124,236,180
ex 24
mvaa 124,228,176
ds 255,8,76,1024
maav 0,64,0
s_cmp_lg_u32 s19,7
s_cbranch_scc1 L_j7o
s_nop 1
s_lshl_b32 s86,s12,12
s_add_u32 s86,s86,131072
v_add_u32_e32 v236,s86,v244
ra 160,236,0
ra 164,236,1024
ra 168,236,2048
ra 172,236,3072
ra 176,236,16384
ra 180,236,17408
ra 184,236,18432
ra 188,236,19456
v_add_u32_e32 v224,s94,v224
s_waitcnt lgkmcnt(0)
L_j7o:
ex 25
maav 0,96,4
ex 26
maav 4,68,0
ex 27
maav 4,100,4
cv 11,14,15
cv 12,40,41
maav 8,72,0
ds 255,8,76,2048
maav 8,104,4
cv 13,42,43
cv 14,44,45
maav 12,76,0
cv 15,46,47
cv 16,16,17
maav 12,108,4
ex 28
v_smfmac_f32_16x16x64_bf16 v[144:147],v[228:231],v[0:7],v248
cv 17,18,19
maav 16,80,0
ds 255,8,76,3072
maav 16,112,4
s_mov_b32 m0,s73
ex 29
maav 20,84,0
s_add_u32 s73,s72,131072
ex 30
maav 20,116,4
ex 31
maav 24,88,0
ex 56
maav 24,120,4
ds 255,8,77,0
maav 28,92,0
ex 57
maav 28,124,4
ex 58
maav 32,64,8
ex 59
maav 32,96,12
ex 60
maav 36,68,8
ds 255,8,77,1024
maav 36,100,12
cv 18,20,21
cv 19,22,23
maav 40,72,8
cv 20,48,49
cv 21,50,51
maav 40,104,12
cv 22,52,53
cv 23,54,55
maav 44,76,8
ex 61
maav 44,108,12
ds 255,8,77,2048
v_smfmac_f32_16x16x64_bf16 v[148:151],v[228:231],v[8:15],v248
cv 24,24,25
maav 48,80,8
ex 62
maav 48,112,12
ex 63
maav 52,84,8
ra 192,244,0
cv 25,26,27
maav 52,116,12
ds 255,8,77,3072
maav 56,88,8
cv 26,28,29
cv 27,30,31
maav 56,120,12
cv 28,56,57
cv 29,58,59
maav 60,92,8
cv 30,60,61
cv 31,62,63
maav 60,124,12
ex 64
mvav 160,64,16
la 132,252,50,2048
mvav 160,96,20
ex 65
mvav 164,68,16
ex 66
mvav 164,100,20
ex 67
mvav 168,72,16
ex 68
mvav 168,104,20
la 136,250,50,0
mvav 172,76,16
ex 69
mvav 172,108,20
ex 70
v_smfmac_f32_16x16x64_bf16 v[152:155],v[228:231],v[16:23],v248
ra 200,244,2048
mvav 176,80,16
ra 208,244,4096
cv 64,64,65
mvav 176,112,20
L_vs26:
s_cbranch_execz L_vs30
la 140,250,50,2048
mvav 180,84,16
ra 216,244,6144
cv 65,66,67
mvav 180,116,20
ex 71
mvav 184,88,16
ex 96
mvav 184,120,20
s_barrier
ex 97
mvav 188,92,16
la 156,250,50,3072
mvav 188,124,20
s_add_u32 m0,s52,131072
ra 196,244,1024
s_xor_b32 s51,s62,1048576
mvav 192,64,24
ra 204,244,3072
cv 66,68,69
mvav 192,96,28
ra 212,244,5120
cv 67,70,71
mvav 196,68,24
ra 220,244,7168
cv 68,96,97
mvav 196,100,28
la 152,250,50,1024
mvav 200,72,24
ex 98
mvav 200,104,28
ex 99
mvav 204,76,24
ra 224,244,8192
cv 69,98,99
mvav 204,108,28
ra 232,244,10240
v_add_u32_e32 v6,s87,v244
v_smfmac_f32_16x16x64_bf16 v[156:159],v[228:231],v[24:31],v248
ra 240,244,12288
mvav 208,80,24
la 148,252,50,3072
mvav 208,112,28
s_xor_b32 s87,s52,4096
ra 248,244,14336
v_add_u32_e32 v7,98304,v6
mvav 212,84,24
s_add_u32 s87,s87,150528
ra 252,244,15360
v_add_u32_e32 v5,65536,v244
mvav 212,116,28
ra 244,244,13312
v_add_u32_e32 v32,s87,v244
mvav 216,88,24
ra 228,244,9216
v_and_b32_e32 v224,960,v244
mvav 216,120,28
la 144,252,50,1024
mvav 220,92,24
s_add_u32 s50,s50,s55
ra 236,244,11264
mvav 220,124,28
ra 64,225,32768
s_waitcnt lgkmcnt(8)
cvaa 24,192,176,232
dl 245,24,51,0
mvaa 24,200,180
ra 96,225,40960
mvaa 24,208,184
ra 68,225,33792
mvaa 24,216,188
dl 246,24,51,1024
cvaa 16,192,160,128
ra 100,225,41984
mvaa 16,200,164
ra 72,225,34816
mvaa 16,208,168
dl 249,24,51,2048
mvaa 16,216,172
ra 104,225,43008
cvaa 28,196,176,232
ra 76,225,35840
mvaa 28,204,180
dl 247,24,51,3072
mvaa 28,212,184
s_add_u32 m0,s52,147456
s_xor_b32 s51,s63,1048576
ra 108,225,44032
mvaa 28,220,188
ra 80,225,36864
cvaa 20,196,160,132
dl 245,24,51,0
mvaa 20,204,164
ra 112,225,45056
mvaa 20,212,168
ra 192,244,16384
mvaa 20,220,172
dl 246,24,51,1024
cvaa 56,224,176,232
ra 200,244,18432
s_waitcnt lgkmcnt(7)
mvaa 56,232,180
ra 208,244,20480
mvaa 56,240,184
dl 249,24,51,2048
mvaa 56,248,188
ra 216,244,22528
cvaa 48,224,160,136
ra 196,244,17408
mvaa 48,232,164
ra 204,244,19456
mvaa 48,240,168
ra 212,244,21504
mvaa 48,248,172
ra 220,244,23552
cvaa 60,228,176,232
ra 224,244,24576
mvaa 60,236,180
ra 232,244,26624
mvaa 60,244,184
ra 240,244,28672
mvaa 60,252,188
ra 248,244,30720
cvaa 52,228,160,140
ra 84,225,37888
mvaa 52,236,164
ra 116,225,46080
mvaa 52,244,168
la 160,252,50,0
ra 228,244,25600
mvaa 52,252,172
ra 236,244,27648
s_waitcnt lgkmcnt(10)
cvaa 8,192,176,128
ra 244,244,29696
mvaa 8,200,180
la 164,252,50,2048
ra 252,244,31744
mvaa 8,208,184
ra 88,225,38912
mvaa 8,216,188
ra 120,225,47104
cvaa 12,196,176,132
la 168,250,50,0
mvaa 12,204,180
ra 92,225,39936
ra 124,225,48128
s_waitcnt lgkmcnt(9)
mvaa 12,212,184
ex 100
mvaa 12,220,188
la 172,250,50,2048
cvaa 40,224,176,136
ex 101
mvaa 40,232,180
ex 102
mvaa 40,240,184
ex 103
mvaa 40,248,188
cv 70,100,101
cv 71,102,103
s_waitcnt lgkmcnt(4)
cvaa 44,228,176,140
ex 72
mvaa 44,236,180
s_waitcnt vmcnt(18) lgkmcnt(0)
s_barrier
ex 73
mvaa 44,244,184
la 176,252,50,1024
mvaa 44,252,188
ex 74
maav 0,64,64
ex 75
maav 0,96,68
la 188,250,50,3072
maav 4,68,64
ra 192,5,0
cv 72,72,73
maav 4,100,68
ra 196,5,1024
cv 73,74,75
maav 8,72,64
la 184,250,50,1024
ds_write_b128 v6,a[0:3] offset:0
maav 8,104,68
ex 76
maav 12,76,64
ex 77
maav 12,108,68
la 180,252,50,3072
ds_write_b128 v6,a[4:7] offset:1024
v_smfmac_f32_16x16x64_bf16 v[144:147],v[228:231],v[64:71],v248
cv 74,76,77
maav 16,80,64
ex 78
maav 16,112,68
ds_write_b128 v6,a[8:11] offset:2048
ex 79
maav 20,84,64
ex 104
maav 20,116,68
ds_write_b128 v6,a[12:15] offset:3072
ex 105
maav 24,88,64
ex 106
ex 107
ex 108
maav 24,120,68
ds_write_b128 v6,a[16:19] offset:4096
cv 75,78,79
maav 28,92,64
ex 109
ex 110
ex 111
maav 28,124,68
cv 76,104,105
cv 77,106,107
cv 78,108,109
cv 79,110,111
maav 32,64,72
ds_write_b128 v6,a[20:23] offset:5120
maav 32,96,76
ex 80
maav 36,68,72
ex 81
maav 36,100,76
ds_write_b128 v6,a[24:27] offset:6144
maav 40,72,72
ex 82
maav 40,104,76
ex 83
maav 44,76,72
ds_write_b128 v6,a[28:31] offset:7168
maav 44,108,76
cv 80,80,81
cv 81,82,83
v_smfmac_f32_16x16x64_bf16 v[148:151],v[228:231],v[72:79],v248
ex 84
maav 48,80,72
ds_write_b128 v7,a[32:35] offset:0
maav 48,112,76
ex 85
ex 86
ex 87
maav 52,84,72
ex 112
ex 113
maav 52,116,76
ds_write_b128 v7,a[36:39] offset:1024
cv 82,84,85
maav 56,88,72
ex 114
ex 115
ex 116
maav 56,120,76
ex 117
ex 118
ex 119
maav 60,92,72
ds_write_b128 v7,a[40:43] offset:2048
cv 83,86,87
maav 60,124,76
cv 84,112,113
cv 85,114,115
cv 86,116,117
cv 87,118,119
mvav 160,64,80
ex 88
mvav 160,96,84
ds_write_b128 v7,a[44:47] offset:3072
mvav 164,68,80
v_add_f32_e32 v130,v144,v145
v_add_f32_e32 v131,v148,v149
mvav 164,100,84
ex 89
mvav 168,72,80
ds_write_b128 v7,a[48:51] offset:4096
mvav 168,104,84
ex 90
mvav 172,76,80
ex 91
mvav 172,108,84
ds_write_b128 v32,v[128:131]
v_smfmac_f32_16x16x64_bf16 v[152:155],v[228:231],v[80:87],v248
ex 92
ex 93
mvav 176,80,80
ds_write_b128 v7,a[52:55] offset:5120
ex 94
mvav 176,112,84
ra 200,5,2048
ra 204,5,3072
ex 95
cv 88,88,89
mvav 180,84,80
ra 208,5,4096
ra 212,5,5120
cv 89,90,91
cv 90,92,93
ex 120
mvav 180,116,84
ds_write_b128 v7,a[56:59] offset:6144
ex 121
mvav 184,88,80
ex 122
ex 123
cv 91,94,95
mvav 184,120,84
ex 124
ex 125
mvav 188,92,80
ds_write_b128 v7,a[60:63] offset:7168
ex 126
mvav 188,124,84
ra 216,5,6144
ra 220,5,7168
ra 224,5,8192
ra 232,5,10240
ex 127
cv 92,120,121
mvav 192,64,88
ra 240,5,12288
ra 248,5,14336
ra 228,5,9216
ra 236,5,11264
ra 244,5,13312
ra 252,5,15360
cv 93,122,123
cv 94,124,125
cv 95,126,127
v_xor_b32_e32 v6,8192,v6
s_nop 0
mvav 192,96,92
ex 16
mvav 196,68,88
s_waitcnt lgkmcnt(10)
s_xor_b32 s87,s87,0
mvav 196,100,92
s_xor_b32 s50,s66,294912
s_barrier
v_xor_b32_e32 v7,8192,v7
mvav 200,72,88
la 4,252,50,0
ra 64,5,16384
ra 96,5,24576
)"
R"(rv 64,6,0
rv 68,6,1024
rv 72,6,2048
mvav 200,104,92
rv 76,6,3072
rv 80,6,4096
rv 84,6,5120
ex 17
ex 18
mvav 204,76,88
rv 96,7,0
rv 100,7,1024
ex 19
mvav 204,108,92
la 12,252,50,2048
rv 104,7,2048
rv 108,7,3072
cv 16,16,17
v_smfmac_f32_16x16x64_bf16 v[156:159],v[228:231],v[88:95],v248
rv 112,7,4096
rv 116,7,5120
ex 20
ex 21
mvav 208,80,88
rv 120,7,6144
rv 124,7,7168
ex 22
ex 23
mvav 208,112,92
la 20,250,50,0
ra 68,5,17408
ra 100,5,25600
cv 17,18,19
cv 18,20,21
mvav 212,84,88
ra 72,5,18432
ra 104,5,26624
ex 48
ex 49
mvav 212,116,92
ra 76,5,19456
ra 108,5,27648
ex 50
ex 51
mvav 216,88,88
la 28,250,50,2048
ra 80,5,20480
ra 112,5,28672
ra 84,5,21504
mvav 216,120,92
ra 116,5,29696
cv 19,22,23
ex 52
ex 53
mvav 220,92,88
ra 88,5,22528
cv 20,48,49
ex 54
ex 55
mvav 220,124,92
la 60,250,50,3072
ra 120,5,30720
cv 21,50,51
cv 22,52,53
cv 23,54,55
s_bitcmp1_b32 s82,s81
s_cbranch_scc1 L_pend2
L_ret_eb5:
mvav 160,192,16
ra 92,5,23552
rv 88,6,6144
ra 124,5,31744
rv 92,6,7168
ex 24
s_waitcnt lgkmcnt(10)
mvav 160,224,20
ex 25
ex 26
v_smfmac_f32_16x16x64_bf16 v[152:155],v[228:231],v[16:23],v248
ex 27
v_xor_b32_e32 v32,4096,v32
mvav 164,196,16
la 52,250,50,1024
ds_read_b128 v[236:239],v32
cv 24,24,25
ex 28
mvav 164,228,20
ex 29
cv 25,26,27
ex 30
ex 31
mvav 168,200,16
ex 56
ex 57
mvav 168,232,20
la 44,252,50,3072
cv 26,28,29
ex 58
ex 59
mvav 172,204,16
ex 60
ex 61
ex 62
mvav 172,236,20
s_waitcnt lgkmcnt(0)
s_barrier
cv 27,30,31
ex 63
mvav 176,208,16
la 36,252,50,1024
cv 28,56,57
cv 29,58,59
cv 30,60,61
cv 31,62,63
mvav 176,240,20
s_add_u32 m0,s52,147456
ex 8
s_xor_b32 s51,s63,1048576
ex 9
ex 10
mvav 180,212,16
s_add_u32 s50,s50,s55
ex 11
cv 8,8,9
cv 9,10,11
ex 12
mvav 180,244,20
dl 247,24,51,3072
ex 13
ex 14
mvav 184,216,16
s_add_u32 m0,s52,0
ex 15
ex 40
ex 41
mvav 184,248,20
s_xor_b32 s51,s64,0
ex 42
ex 43
ex 44
mvav 188,220,16
la 0,252,50,0
ex 45
ex 46
mvav 188,252,20
ex 47
cv 10,12,13
cv 11,14,15
cv 12,40,41
cv 13,42,43
cv 14,44,45
v_smfmac_f32_16x16x64_bf16 v[156:159],v[228:231],v[24:31],v248
cv 15,46,47
v_bfe_u32 v225,v244,1,5
s_nop 0
v_smfmac_f32_16x16x64_bf16 v[156:159],v[228:231],v[8:15],v248
v_lshl_add_u32 v224,v224,1,v225
v_bfe_u32 v225,v244,7,2
v_lshl_add_u32 v224,v225,5,v224
mvav 192,192,24
la 8,252,50,2048
v_xor_b32_e32 v224,s69,v224
v_add_f32_e32 v152,v152,v153
v_add_f32_e32 v137,v152,v238
v_subrev_u32_e32 v236,0x23800000,v137
mvav 192,224,28
v_rcp_f32_e32 v137,v137
mvav 192,64,8
tv 128,224,32768
v_mul_f32_e32 v232,v240,v137
v_mul_f32_e32 v64,v232,v64
v_mul_f32_e32 v65,v232,v65
v_mul_f32_e32 v66,v232,v66
mvav 192,96,12
la 16,250,50,0
v_mul_f32_e32 v67,v232,v67
v_mul_f32_e32 v68,v232,v68
v_mul_f32_e32 v69,v232,v69
v_mul_f32_e32 v70,v232,v70
mvav 196,196,24
v_fmac_f32_e32 v64,v232,v160
v_fmac_f32_e32 v65,v232,v161
v_mul_f32_e32 v71,v232,v71
mvav 196,228,28
v_fmac_f32_e32 v66,v232,v162
v_fmac_f32_e32 v67,v232,v163
v_fmac_f32_e32 v68,v232,v164
v_fmac_f32_e32 v69,v232,v165
v_fmac_f32_e32 v70,v232,v166
mvav 196,68,8
la 24,250,50,2048
v_fmac_f32_e32 v71,v232,v167
cv 160,64,65
cv 161,66,67
cv 162,68,69
cv 163,70,71
mvav 196,100,12
tv 130,224,34816
tv 132,224,36864
mvav 200,200,24
tv 134,224,38912
tv 144,224,40960
v_mul_f32_e32 v72,v232,v72
v_mul_f32_e32 v73,v232,v73
v_mul_f32_e32 v74,v232,v74
mvav 200,232,28
buffer_store_dwordx4 v[160:163],v244,s[8:11],s14 offen offset:0 sc0 sc1
v_mul_f32_e32 v75,v232,v75
v_mul_f32_e32 v76,v232,v76
v_mul_f32_e32 v77,v232,v77
v_mul_f32_e32 v78,v232,v78
mvav 200,72,8
v_fmac_f32_e32 v72,v232,v168
v_fmac_f32_e32 v73,v232,v169
v_mul_f32_e32 v79,v232,v79
mvav 200,104,12
v_fmac_f32_e32 v74,v232,v170
v_fmac_f32_e32 v75,v232,v171
v_fmac_f32_e32 v76,v232,v172
v_fmac_f32_e32 v77,v232,v173
v_fmac_f32_e32 v78,v232,v174
mvav 204,204,24
la 56,250,50,3072
v_fmac_f32_e32 v79,v232,v175
cv 168,72,73
cv 169,74,75
cv 170,76,77
cv 171,78,79
mvav 204,236,28
tv 146,224,43008
tv 148,224,45056
mvav 204,76,8
tv 150,224,47104
v_mul_f32_e32 v80,v232,v80
v_mul_f32_e32 v81,v232,v81
v_mul_f32_e32 v82,v232,v82
mvav 204,108,12
buffer_store_dwordx4 v[168:171],v244,s[8:11],s14 offen offset:2048 sc0 sc1
v_mul_f32_e32 v83,v232,v83
v_mul_f32_e32 v84,v232,v84
v_mul_f32_e32 v85,v232,v85
v_mul_f32_e32 v86,v232,v86
v_mul_f32_e32 v87,v232,v87
mvav 208,208,24
v_fmac_f32_e32 v80,v232,v176
v_fmac_f32_e32 v81,v232,v177
v_fmac_f32_e32 v82,v232,v178
mvav 208,240,28
v_fmac_f32_e32 v83,v232,v179
v_fmac_f32_e32 v84,v232,v180
v_fmac_f32_e32 v85,v232,v181
cv 176,80,81
cv 177,82,83
mvav 208,80,8
la 48,250,50,1024
v_fmac_f32_e32 v86,v232,v182
v_fmac_f32_e32 v87,v232,v183
cv 178,84,85
cv 179,86,87
mvav 208,112,12
L_vs27:
s_cbranch_execz L_vs31
v_mul_f32_e32 v88,v232,v88
v_mul_f32_e32 v89,v232,v89
v_mul_f32_e32 v90,v232,v90
mvav 212,212,24
v_mul_f32_e32 v91,v232,v91
v_mul_f32_e32 v92,v232,v92
v_mul_f32_e32 v93,v232,v93
v_mul_f32_e32 v94,v232,v94
v_mul_f32_e32 v95,v232,v95
mvav 212,244,28
buffer_store_dwordx4 v[176:179],v244,s[8:11],s88 offen offset:0 sc0 sc1
v_fmac_f32_e32 v88,v232,v184
v_fmac_f32_e32 v89,v232,v185
v_fmac_f32_e32 v90,v232,v186
v_fmac_f32_e32 v91,v232,v187
cv 184,88,89
mvav 212,84,8
v_fmac_f32_e32 v92,v232,v188
cv 185,90,91
mvav 212,116,12
v_fmac_f32_e32 v93,v232,v189
v_fmac_f32_e32 v94,v232,v190
v_fmac_f32_e32 v95,v232,v191
cv 186,92,93
cv 187,94,95
mvav 216,216,24
la 40,252,50,3072
v_add_f32_e32 v156,v156,v157
v_add_f32_e32 v141,v156,v239
v_subrev_u32_e32 v237,0x23800000,v141
v_rcp_f32_e32 v141,v141
mvav 216,248,28
v_mul_f32_e32 v233,v241,v141
v_mul_f32_e32 v96,v233,v96
mvav 216,88,8
v_mul_f32_e32 v97,v233,v97
v_mul_f32_e32 v98,v233,v98
v_mul_f32_e32 v99,v233,v99
v_mul_f32_e32 v100,v233,v100
mvav 216,120,12
buffer_store_dwordx4 v[184:187],v244,s[8:11],s88 offen offset:2048 sc0 sc1
v_fmac_f32_e32 v96,v233,v192
v_fmac_f32_e32 v97,v233,v193
v_mul_f32_e32 v101,v233,v101
v_mul_f32_e32 v102,v233,v102
v_mul_f32_e32 v103,v233,v103
mvav 220,220,24
v_fmac_f32_e32 v98,v233,v194
v_fmac_f32_e32 v99,v233,v195
mvav 220,252,28
v_fmac_f32_e32 v100,v233,v196
v_fmac_f32_e32 v101,v233,v197
cv 192,96,97
cv 193,98,99
mvav 220,92,8
la 32,252,50,1024
v_fmac_f32_e32 v102,v233,v198
v_fmac_f32_e32 v103,v233,v199
cv 194,100,101
cv 195,102,103
mvav 220,124,12
v_xor_b32_e32 v225,32,v224
v_xor_b32_e32 v226,64,v224
s_waitcnt lgkmcnt(0)
zvva 0,128,128
tv 138,225,34816
tv 142,225,38912
tv 152,225,40960
s_waitcnt vmcnt(36)
mvva 0,132,132
tv 136,225,32768
tv 154,225,43008
mvva 0,144,136
tv 140,225,36864
tv 158,225,47104
mvva 0,148,140
tv 156,225,45056
v_mul_f32_e32 v104,v233,v104
v_mul_f32_e32 v105,v233,v105
v_mul_f32_e32 v106,v233,v106
zvva 4,148,156
buffer_store_dwordx4 v[192:195],v244,s[8:11],s14 offen offset:1024 sc0 sc1
v_mul_f32_e32 v107,v233,v107
v_mul_f32_e32 v108,v233,v108
v_mul_f32_e32 v109,v233,v109
v_mul_f32_e32 v110,v233,v110
mvva 4,144,152
v_fmac_f32_e32 v104,v233,v200
v_fmac_f32_e32 v105,v233,v201
v_fmac_f32_e32 v106,v233,v202
v_mul_f32_e32 v111,v233,v111
mvva 4,132,148
v_fmac_f32_e32 v107,v233,v203
v_fmac_f32_e32 v108,v233,v204
v_fmac_f32_e32 v109,v233,v205
cv 200,104,105
cv 201,106,107
mvva 4,128,144
v_fmac_f32_e32 v110,v233,v206
v_fmac_f32_e32 v111,v233,v207
cv 202,108,109
cv 203,110,111
s_waitcnt vmcnt(16)
zvva 8,128,160
v_cmp_lt_u32_e64 s[42:43],s48,v236
v_mul_f32_e32 v112,v233,v112
s_or_b64 s[40:41],s[40:41],s[42:43]
v_cmp_lt_u32_e64 s[42:43],s48,v237
mvva 8,132,164
s_or_b64 s[40:41],s[40:41],s[42:43]
v_mul_f32_e32 v113,v233,v113
v_mul_f32_e32 v114,v233,v114
v_mul_f32_e32 v115,v233,v115
mvva 8,144,168
dl 245,36,51,0
v_mul_f32_e32 v116,v233,v116
v_mul_f32_e32 v117,v233,v117
v_mul_f32_e32 v118,v233,v118
v_mul_f32_e32 v119,v233,v119
v_mul_f32_e32 v120,v233,v120
mvva 8,148,172
v_fmac_f32_e32 v112,v233,v208
v_fmac_f32_e32 v113,v233,v209
v_fmac_f32_e32 v114,v233,v210
v_fmac_f32_e32 v115,v233,v211
zvva 12,148,188
v_fmac_f32_e32 v116,v233,v212
v_fmac_f32_e32 v117,v233,v213
cv 208,112,113
cv 209,114,115
mvva 12,144,184
v_fmac_f32_e32 v118,v233,v214
v_fmac_f32_e32 v119,v233,v215
cv 210,116,117
cv 211,118,119
mvva 12,132,180
v_mul_f32_e32 v121,v233,v121
v_mul_f32_e32 v122,v233,v122
v_mul_f32_e32 v123,v233,v123
mvva 12,128,176
v_mul_f32_e32 v124,v233,v124
v_mul_f32_e32 v125,v233,v125
v_mul_f32_e32 v126,v233,v126
v_mul_f32_e32 v127,v233,v127
s_waitcnt lgkmcnt(0)
zvva 28,136,176
buffer_store_dwordx4 v[200:203],v244,s[8:11],s14 offen offset:3072 sc0 sc1
tv 144,226,40960
v_fmac_f32_e32 v120,v233,v216
v_fmac_f32_e32 v121,v233,v217
v_fmac_f32_e32 v122,v233,v218
v_fmac_f32_e32 v123,v233,v219
mvva 28,140,180
tv 128,226,32768
tv 130,226,34816
cv 216,120,121
cv 217,122,123
mvva 28,152,184
tv 132,226,36864
tv 134,226,38912
v_fmac_f32_e32 v124,v233,v220
v_fmac_f32_e32 v125,v233,v221
v_fmac_f32_e32 v126,v233,v222
mvva 28,156,188
tv 146,226,43008
v_fmac_f32_e32 v127,v233,v223
cv 218,124,125
cv 219,126,127
zvva 24,156,172
tv 148,226,45056
tv 150,226,47104
mvva 24,152,168
v_xor_b32_e32 v227,96,v224
v_add_u32_e32 v232,131072,v224
mvva 24,140,164
buffer_store_dwordx4 v[208:211],v244,s[8:11],s88 offen offset:1024 sc0 sc1
mvva 24,136,160
v_add_u32_e32 v233,131072,v225
v_add_u32_e32 v234,131072,v226
zvva 20,136,144
v_add_u32_e32 v235,131072,v227
v_mov_b32_e32 v242,83914752
mvva 20,140,148
v_add_u32_e32 v228,65536,v224
v_add_u32_e32 v229,65536,v225
mvva 20,152,152
v_add_u32_e32 v230,65536,v226
v_add_u32_e32 v231,65536,v227
mvva 20,156,156
v_and_b32_e32 v243,240,v244
v_lshlrev_b32_e32 v243,7,v243
zvva 16,156,140
buffer_store_dwordx4 v[216:219],v244,s[8:11],s88 offen offset:3072 sc0 sc1
s_cmp_lg_u64 s[40:41],0
s_cbranch_scc1 L_redo
L_redo_r5:
mvva 16,152,136
tv 156,227,45056
v_mov_b32_e32 v236,16256
mvva 16,140,132
tv 158,227,47104
v_and_b32_e32 v248,256,v244
mvva 16,136,128
tv 152,227,40960
v_lshrrev_b32_e32 v248,3,v248
s_waitcnt lgkmcnt(3)
zvva 32,128,128
tv 140,227,36864
v_add_u32_e32 v243,v243,v248
mvva 32,132,132
tv 136,227,32768
tv 138,227,34816
mvva 32,144,136
dl 246,36,51,1024
tv 142,227,38912
mvva 32,148,140
tv 154,227,43008
v_and_b32_e32 v248,512,v244
zvva 36,148,156
v_lshrrev_b32_e32 v248,5,v248
v_add_u32_e32 v243,v243,v248
mvva 36,144,152
v_and_b32_e32 v248,16,v244
v_lshlrev_b32_e32 v248,v248,v236
mvva 36,132,148
v_cndmask_b32_e64 v238,0,v248,s[74:75]
s_mov_b32 s74,0xc0000c
mvva 36,128,144
s_mov_b32 s75,0xc0000c00
v_mov_b32_e32 v240,0
v_cndmask_b32_e64 v239,0,v248,s[74:75]
zvva 40,128,160
dl 249,36,51,2048
mvva 40,132,164
v_mov_b32_e32 v241,0
v_mov_b32_e32 v236,0
mvva 40,144,168
v_mov_b32_e32 v237,0
mvva 40,148,172
zvva 44,148,188
mvva 44,144,184
mvva 44,132,180
dl 247,36,51,3072
s_add_u32 m0,s52,16384
s_xor_b32 s51,s65,0
mvva 44,128,176
tv 144,224,57344
s_waitcnt lgkmcnt(1)
zvva 60,136,176
tv 132,224,53248
tv 134,224,55296
mvva 60,140,180
tv 128,224,49152
tv 130,224,51200
mvva 60,152,184
mvva 60,156,188
tv 146,224,59392
zvva 56,156,172
dl 245,36,51,0
tv 148,224,61440
tv 150,224,63488
mvva 56,152,168
mvva 56,140,164
mvva 56,136,160
zvva 52,136,144
mvva 52,140,148
mvva 52,152,152
dl 246,36,51,1024
mvva 52,156,156
zvva 48,156,140
mvva 48,152,136
mvva 48,140,132
tv 156,225,61440
mvva 48,136,128
tv 152,225,57344
s_waitcnt lgkmcnt(2)
zvva 64,128,128
dl 249,36,51,2048
tv 140,225,53248
mvva 64,132,132
tv 136,225,49152
tv 138,225,51200
mvva 64,144,136
tv 142,225,55296
mvva 64,148,140
tv 154,225,59392
zvva 68,148,156
tv 158,225,63488
mvva 68,144,152
mvva 68,132,148
dl 247,36,51,3072
s_lshl_b32 s51,s33,5
s_add_u32 m0,s52,98304
s_xor_b32 s51,s64,s51
mvva 68,128,144
zvva 72,128,160
mvva 72,132,164
s_waitcnt vmcnt(18)
s_mov_b32 s90,0
mvva 72,144,168
s_xor_b32 s50,s67,0
mvva 72,148,172
zvva 76,148,188
dl 245,36,51,0
mvva 76,144,184
mvva 76,132,180
tv 148,226,61440
mvva 76,128,176
tv 144,226,57344
s_waitcnt lgkmcnt(2)
zvva 92,136,176
tv 132,226,53248
mvva 92,140,180
tv 128,226,49152
tv 130,226,51200
mvva 92,152,184
dl 246,36,51,1024
tv 134,226,55296
mvva 92,156,188
tv 146,226,59392
zvva 88,156,172
tv 150,226,63488
mvva 88,152,168
mvva 88,140,164
mvva 88,136,160
zvva 84,136,144
dl 249,36,51,2048
mvva 84,140,148
mvva 84,152,152
mvva 84,156,156
zvva 80,156,140
mvva 80,152,136
mvva 80,140,132
dl 247,36,51,3072
mvva 80,136,128
s_lshl_b32 s51,s33,5
tv 152,227,57344
s_add_u32 m0,s52,114688
s_waitcnt lgkmcnt(1)
zvva 96,128,128
s_xor_b32 s51,s65,s51
tv 140,227,53248
tv 142,227,55296
mvva 96,132,132
tv 136,227,49152
tv 138,227,51200
mvva 96,144,136
mvva 96,148,140
tv 154,227,59392
zvva 100,148,156
lv 164,252,50,0
tv 156,227,61440
tv 158,227,63488
mvva 100,144,152
mvva 100,132,148
mvva 100,128,144
zvva 104,128,160
mvva 104,132,164
mvva 104,144,168
lv 172,252,50,2048
mvva 104,148,172
zvva 108,148,188
mvva 108,144,184
mvva 108,132,180
mvva 108,128,176
s_barrier
s_waitcnt lgkmcnt(0)
zvva 124,136,176
lv 180,250,50,0
tv 132,232,4096
tv 144,232,8192
tv 146,232,10240
mvva 124,140,180
tv 128,232,0
tv 130,232,2048
mvva 124,152,184
tv 134,232,6144
mvva 124,156,188
tv 148,232,12288
tv 150,232,14336
zvva 120,156,172
mvva 120,152,168
mvva 120,140,164
lv 188,250,50,2048
mvva 120,136,160
zvva 116,136,144
mvva 116,140,148
mvva 116,152,152
mvva 116,156,156
zvva 112,156,140
lv 220,250,50,3072
mvva 112,152,136
mvva 112,140,132
tv 156,233,12288
mvva 112,136,128
tv 152,233,8192
s_waitcnt lgkmcnt(2)
mvva 0,128,4
tv 140,233,4096
mvva 0,132,12
tv 136,233,0
tv 138,233,2048
mvva 0,144,20
lv 212,250,50,1024
tv 142,233,6144
mvva 0,148,28
tv 154,233,10240
mvva 4,148,60
tv 158,233,14336
mvva 4,144,52
mvva 4,132,44
mvva 4,128,36
mvva 8,128,0
lv 204,252,50,3072
mvva 8,132,8
mvva 8,144,16
mvva 8,148,24
mvva 12,148,56
s_waitcnt vmcnt(27)
mvva 12,144,48
s_waitcnt vmcnt(25)
mvva 12,132,40
lv 196,252,50,1024
s_waitcnt vmcnt(24)
mvva 12,128,32
tv 144,234,8192
s_waitcnt lgkmcnt(1)
mvva 28,136,32
tv 132,234,4096
tv 134,234,6144
mvva 28,140,40
tv 128,234,0
tv 130,234,2048
mvva 28,152,48
mvva 28,156,56
tv 146,234,10240
mvva 24,156,24
dl 245,36,51,0
s_waitcnt vmcnt(20)
s_barrier
tv 148,234,12288
tv 150,234,14336
mvva 24,152,16
mvva 24,140,8
mvva 24,136,0
mvva 20,136,36
mvva 20,140,44
mvva 20,152,52
s_cmp_eq_u32 s12,0
s_cselect_b64 exec,1,0
buffer_store_dword v251,off,s[8:11],s61 sc0 sc1
s_mov_b64 exec,-1
mvva 20,156,60
mvva 16,156,28
mvva 16,152,20
mvva 16,140,12
tv 156,235,12288
mvva 16,136,4
tv 152,235,8192
s_waitcnt lgkmcnt(2)
mvva 32,128,4
dl 246,36,51,1024
tv 140,235,4096
mvva 32,132,12
tv 136,235,0
tv 138,235,2048
mvva 32,144,20
tv 142,235,6144
mvva 32,148,28
tv 154,235,10240
mvva 36,148,60
tv 158,235,14336
mvva 36,144,52
mvva 36,132,44
dl 249,36,51,2048
mvva 36,128,36
mvva 40,128,0
mvva 40,132,8
mvva 40,144,16
mvva 40,148,24
mvva 44,148,56
dl 247,36,51,3072
s_lshl_b32 s51,s34,5
s_add_u32 m0,s52,65536
s_xor_b32 s51,s64,s51
mvva 44,144,48
tv 148,232,28672
mvva 44,132,40
s_xor_b32 s53,s60,s33
tv 150,232,30720
mvva 44,128,32
tv 144,232,24576
s_waitcnt lgkmcnt(3)
mvva 60,136,32
tv 132,232,20480
mvva 60,140,40
tv 128,232,16384
tv 130,232,18432
mvva 60,152,48
dl 245,36,51,0
tv 134,232,22528
mvva 60,156,56
tv 146,232,26624
mvva 56,156,24
mvva 56,152,16
mvva 56,140,8
mvva 56,136,0
mvva 52,136,36
buffer_load_dword v253,v242,s[8:11],s53 offen sc1
mvva 52,140,44
mvva 52,152,52
mvva 52,156,60
mvva 48,156,28
mvva 48,152,20
mvva 48,140,12
dl 246,36,51,1024
mvva 48,136,4
tv 152,233,24576
s_waitcnt lgkmcnt(1)
mvva 64,128,4
tv 140,233,20480
tv 142,233,22528
mvva 64,132,12
tv 136,233,16384
tv 138,233,18432
mvva 64,144,20
mvva 64,148,28
tv 154,233,26624
mvva 68,148,60
dl 249,36,51,2048
tv 156,233,28672
tv 158,233,30720
mvva 68,144,52
mvva 68,132,44
mvva 68,128,36
mvva 72,128,0
mvva 72,132,8
mvva 72,144,16
dl 247,36,51,3072
s_lshl_b32 s51,s34,5
s_add_u32 m0,s52,81920
s_xor_b32 s51,s65,s51
mvva 72,148,24
mvva 76,148,56
mvva 76,144,48
mvva 76,132,40
tv 148,234,28672
mvva 76,128,32
tv 144,234,24576
s_waitcnt lgkmcnt(2)
mvva 92,136,32
dl 245,36,51,0
tv 132,234,20480
mvva 92,140,40
tv 128,234,16384
tv 130,234,18432
mvva 92,152,48
tv 134,234,22528
mvva 92,156,56
tv 146,234,26624
mvva 88,156,24
tv 150,234,30720
mvva 88,152,16
mvva 88,140,8
dl 246,36,51,1024
mvva 88,136,0
mvva 84,136,36
mvva 84,140,44
mvva 84,152,52
mvva 84,156,60
mvva 80,156,28
dl 249,36,51,2048
mvva 80,152,20
mvva 80,140,12
tv 156,235,28672
mvva 80,136,4
tv 152,235,24576
s_waitcnt lgkmcnt(2)
mvva 96,128,4
tv 140,235,20480
mvva 96,132,12
L_vs28:
s_cbranch_execz L_vs17
tv 136,235,16384
tv 138,235,18432
mvva 96,144,20
dl 247,36,51,3072
s_lshl_b32 s51,s35,5
s_add_u32 m0,s52,32768
s_xor_b32 s51,s64,s51
tv 142,235,22528
mvva 96,148,28
tv 154,235,26624
mvva 100,148,60
tv 158,235,30720
mvva 100,144,52
mvva 100,132,44
mvva 100,128,36
mvva 104,128,0
dl 245,36,51,0
mvva 104,132,8
mvva 104,144,16
mvva 104,148,24
mvva 108,148,56
mvva 108,144,48
mvva 108,132,40
dl 246,36,51,1024
mvva 108,128,32
s_waitcnt vmcnt(27)
s_barrier
s_waitcnt lgkmcnt(0)
mvva 124,136,32
tv 132,224,4096
tv 134,224,6144
tv 144,224,8192
mvva 124,140,40
tv 128,224,0
tv 130,224,2048
mvva 124,152,48
mvva 124,156,56
tv 146,224,10240
mvva 120,156,24
dl 249,36,51,2048
tv 148,224,12288
tv 150,224,14336
mvva 120,152,16
mvva 120,140,8
mvva 120,136,0
mvva 116,136,36
mvva 116,140,44
mvva 116,152,52
dl 247,36,51,3072
s_lshl_b32 s51,s35,5
s_add_u32 m0,s52,49152
s_xor_b32 s51,s65,s51
mvva 116,156,60
mvva 112,156,28
mvva 112,152,20
s_waitcnt vmcnt(11)
v_cmp_ne_u32_e32 vcc,4,v253
mvva 112,140,12
dl 245,36,51,0
s_cbranch_vccnz L_spa3o
L_spra3o:
mvva 112,136,4
s_xor_b32 s50,s67,s33
tv 152,225,8192
s_waitcnt lgkmcnt(1)
mvvv 0,128,164
tv 140,225,4096
tv 142,225,6144
mvvv 0,132,172
tv 136,225,0
tv 138,225,2048
mvvv 0,144,180
dl 246,36,51,1024
mvvv 0,148,188
tv 154,225,10240
mvvv 4,148,220
tv 156,225,12288
tv 158,225,14336
mvvv 4,144,212
mvvv 4,132,204
la 196,252,50,0
mvvv 4,128,196
mvvv 8,128,160
s_xor_b32 s53,s60,s34
mvvv 8,132,168
mvvv 8,144,176
la 204,252,50,2048
mvvv 8,148,184
mvvv 12,148,216
mvvv 12,144,208
mvvv 12,132,200
buffer_load_dword v253,v242,s[8:11],s53 offen sc1
mvvv 12,128,192
tv 144,226,8192
s_waitcnt lgkmcnt(1)
mvvv 28,136,192
tv 132,226,4096
tv 134,226,6144
mvvv 28,140,200
tv 128,226,0
tv 130,226,2048
mvvv 28,152,208
la 212,250,50,0
mvvv 28,156,216
tv 146,226,10240
mvvv 24,156,184
tv 148,226,12288
tv 150,226,14336
mvvv 24,152,176
mvvv 24,140,168
mvvv 24,136,160
la 220,250,50,2048
mvvv 20,136,196
mvvv 20,140,204
mvvv 20,152,212
mvvv 20,156,220
la 252,250,50,3072
mvvv 16,156,188
mvvv 16,152,180
mvvv 16,140,172
tv 156,227,12288
mvvv 16,136,164
la 244,250,50,1024
tv 152,227,8192
s_waitcnt lgkmcnt(2)
mvvv 32,128,164
tv 140,227,4096
mvvv 32,132,172
tv 136,227,0
tv 138,227,2048
mvvv 32,144,180
tv 142,227,6144
mvvv 32,148,188
la 236,252,50,3072
tv 154,227,10240
mvvv 36,148,220
tv 158,227,14336
mvvv 36,144,212
mvvv 36,132,204
mvvv 36,128,196
la 228,252,50,1024
s_add_u32 s50,s50,s55
mvvv 40,128,160
mvvv 40,132,168
mvvv 40,144,176
mvvv 40,148,184
mvvv 44,148,216
la 192,252,50,0
mvvv 44,144,208
mvvv 44,132,200
tv 148,224,28672
tv 150,224,30720
mvvv 44,128,192
s_waitcnt lgkmcnt(2)
mvvv 60,136,192
)"
R"(la 200,252,50,2048
tv 132,224,20480
mvvv 60,140,200
tv 128,224,16384
tv 130,224,18432
mvvv 60,152,208
tv 134,224,22528
mvvv 60,156,216
tv 144,224,24576
tv 146,224,26624
mvvv 56,156,184
la 208,250,50,0
mvvv 56,152,176
mvvv 56,140,168
mvvv 56,136,160
mvvv 52,136,196
la 216,250,50,2048
mvvv 52,140,204
mvvv 52,152,212
mvvv 52,156,220
mvvv 48,156,188
la 248,250,50,3072
mvvv 48,152,180
mvvv 48,140,172
tv 156,225,28672
mvvv 48,136,164
tv 152,225,24576
s_waitcnt lgkmcnt(2)
mvvv 64,128,164
tv 140,225,20480
mvvv 64,132,172
la 240,250,50,1024
tv 136,225,16384
tv 138,225,18432
mvvv 64,144,180
tv 142,225,22528
mvvv 64,148,188
tv 154,225,26624
mvvv 68,148,220
tv 158,225,30720
mvvv 68,144,212
la 232,252,50,3072
mvvv 68,132,204
mvvv 68,128,196
mvvv 72,128,160
mvvv 72,132,168
la 224,252,50,1024
mvvv 72,144,176
mvvv 72,148,184
mvvv 76,148,216
mvvv 76,144,208
mvvv 76,132,200
dl 249,36,51,2048
mvvv 76,128,192
tv 144,226,24576
s_waitcnt lgkmcnt(1)
mvvv 92,136,192
tv 132,226,20480
tv 134,226,22528
mvvv 92,140,200
tv 128,226,16384
tv 130,226,18432
mvvv 92,152,208
dl 247,36,51,3072
s_lshl_b32 s51,s98,5
s_xor_b32 s51,s64,s51
s_add_u32 m0,s52,131072
s_xor_b32 s51,s51,4194304
mvvv 92,156,216
tv 146,226,26624
mvvv 88,156,184
tv 148,226,28672
tv 150,226,30720
mvvv 88,152,176
mvvv 88,140,168
dl 245,36,51,0
mvvv 88,136,160
s_waitcnt vmcnt(17)
v_cmp_ne_u32_e32 vcc,4,v253
mvvv 84,136,196
s_cbranch_vccnz L_spa4o
L_spra4o:
mvvv 84,140,204
s_xor_b32 s50,s67,s34
mvvv 84,152,212
dl 246,36,51,1024
mvvv 84,156,220
mvvv 80,156,188
mvvv 80,152,180
mvvv 80,140,172
la 68,252,50,0
mvvv 80,136,164
tv 152,227,24576
s_waitcnt lgkmcnt(1)
mvvv 96,128,164
tv 140,227,20480
tv 142,227,22528
mvvv 96,132,172
tv 136,227,16384
tv 138,227,18432
mvvv 96,144,180
s_xor_b32 s53,s60,s35
mvvv 96,148,188
la 76,252,50,2048
tv 154,227,26624
mvvv 100,148,220
tv 156,227,28672
tv 158,227,30720
mvvv 100,144,212
mvvv 100,132,204
mvvv 100,128,196
la 84,250,50,0
mvvv 104,128,160
mvvv 104,132,168
mvvv 104,144,176
mvvv 104,148,184
buffer_load_dword v253,v242,s[8:11],s53 offen sc1
mvvv 108,148,216
mvvv 108,144,208
mvvv 108,132,200
mvvv 108,128,192
s_barrier
s_waitcnt lgkmcnt(0)
mvvv 124,136,192
la 92,250,50,2048
tv 132,228,36864
tv 144,228,40960
tv 146,228,43008
mvvv 124,140,200
tv 128,228,32768
tv 130,228,34816
mvvv 124,152,208
tv 134,228,38912
mvvv 124,156,216
tv 148,228,45056
tv 150,228,47104
mvvv 120,156,184
la 124,250,50,3072
mvvv 120,152,176
mvvv 120,140,168
mvvv 120,136,160
mvvv 116,136,196
la 116,250,50,1024
mvvv 116,140,204
mvvv 116,152,212
mvvv 116,156,220
mvvv 112,156,188
la 108,252,50,3072
mvvv 112,152,180
mvvv 112,140,172
tv 156,229,45056
tv 158,229,47104
mvvv 112,136,164
s_waitcnt lgkmcnt(2)
mvva 0,128,196
la 100,252,50,1024
s_add_u32 s50,s50,s55
tv 140,229,36864
mvva 0,132,204
tv 136,229,32768
tv 138,229,34816
s_waitcnt vmcnt(21)
mvva 0,144,212
tv 142,229,38912
mvva 0,148,220
tv 152,229,40960
tv 154,229,43008
mvva 4,148,252
mvva 4,144,244
la 64,252,50,0
mvva 4,132,236
mvva 4,128,228
s_waitcnt vmcnt(21)
mvva 8,128,192
s_waitcnt vmcnt(20)
mvva 8,132,200
la 72,252,50,2048
s_waitcnt vmcnt(20)
mvva 8,144,208
s_waitcnt vmcnt(19)
mvva 8,148,216
s_waitcnt vmcnt(18)
mvva 12,148,248
s_waitcnt vmcnt(17)
mvva 12,144,240
la 80,250,50,0
s_waitcnt vmcnt(17)
mvva 12,132,232
tv 148,230,45056
s_waitcnt vmcnt(16)
mvva 12,128,224
tv 144,230,40960
s_waitcnt lgkmcnt(2)
mvva 28,136,224
tv 132,230,36864
mvva 28,140,232
la 88,250,50,2048
tv 128,230,32768
tv 130,230,34816
mvva 28,152,240
tv 134,230,38912
mvva 28,156,248
tv 146,230,43008
mvva 24,156,216
tv 150,230,47104
mvva 24,152,208
la 120,250,50,3072
mvva 24,140,200
mvva 24,136,192
mvva 20,136,228
mvva 20,140,236
mvva 20,152,244
la 112,250,50,1024
mvva 20,156,252
mvva 16,156,220
mvva 16,152,212
mvva 16,140,204
la 104,252,50,3072
mvva 16,136,196
tv 152,231,40960
s_waitcnt lgkmcnt(1)
mvva 32,128,196
tv 140,231,36864
tv 142,231,38912
mvva 32,132,204
tv 136,231,32768
tv 138,231,34816
mvva 32,144,212
la 96,252,50,1024
mvva 32,148,220
tv 154,231,43008
mvva 36,148,252
tv 156,231,45056
tv 158,231,47104
mvva 36,144,244
mvva 36,132,236
mvva 36,128,228
dl 249,36,51,2048
mvva 40,128,192
mvva 40,132,200
mvva 40,144,208
mvva 40,148,216
dl 247,36,51,3072
s_lshl_b32 s51,s98,5
s_xor_b32 s51,s65,s51
s_add_u32 m0,s52,147456
s_xor_b32 s51,s51,4194304
mvva 44,148,248
mvva 44,144,240
mvva 44,132,232
tv 148,228,61440
mvva 44,128,224
dl 245,36,51,0
tv 144,228,57344
s_waitcnt lgkmcnt(2)
mvva 60,136,224
tv 132,228,53248
mvva 60,140,232
tv 128,228,49152
tv 130,228,51200
s_waitcnt vmcnt(16)
v_cmp_ne_u32_e32 vcc,4,v253
mvva 60,152,240
s_cbranch_vccnz L_spa5o
L_spra5o:
tv 134,228,55296
s_xor_b32 s50,s67,s35
mvva 60,156,248
dl 246,36,51,1024
tv 146,228,59392
mvva 56,156,216
tv 150,228,63488
mvva 56,152,208
mvva 56,140,200
mvva 56,136,192
la 132,252,50,0
mvva 52,136,228
mvva 52,140,236
mvva 52,152,244
mvva 52,156,252
mvva 48,156,220
la 140,252,50,2048
s_xor_b32 s53,s60,s98
s_xor_b32 s53,s53,512
mvva 48,152,212
tv 156,229,61440
mvva 48,140,204
tv 158,229,63488
mvva 48,136,196
tv 152,229,57344
s_waitcnt lgkmcnt(3)
mvva 64,128,196
la 148,250,50,0
tv 140,229,53248
mvva 64,132,204
tv 136,229,49152
tv 138,229,51200
mvva 64,144,212
tv 142,229,55296
mvva 64,148,220
tv 154,229,59392
mvva 68,148,252
buffer_load_dword v253,v242,s[8:11],s53 offen sc1
mvva 68,144,244
mvva 68,132,236
mvva 68,128,228
mvva 72,128,192
mvva 72,132,200
la 156,250,50,2048
mvva 72,144,208
mvva 72,148,216
mvva 76,148,248
mvva 76,144,240
la 188,250,50,3072
mvva 76,132,232
tv 148,230,61440
mvva 76,128,224
tv 144,230,57344
s_waitcnt lgkmcnt(2)
mvva 92,136,224
tv 132,230,53248
mvva 92,140,232
la 180,250,50,1024
tv 128,230,49152
tv 130,230,51200
mvva 92,152,240
tv 134,230,55296
mvva 92,156,248
tv 146,230,59392
mvva 88,156,216
tv 150,230,63488
mvva 88,152,208
la 172,252,50,3072
mvva 88,140,200
mvva 88,136,192
mvva 84,136,228
mvva 84,140,236
la 164,252,50,1024
s_add_u32 s50,s50,s55
mvva 84,152,244
mvva 84,156,252
mvva 80,156,220
mvva 80,152,212
mvva 80,140,204
la 128,252,50,0
mvva 80,136,196
tv 152,231,57344
s_waitcnt lgkmcnt(1)
mvva 96,128,196
tv 140,231,53248
tv 142,231,55296
mvva 96,132,204
tv 136,231,49152
tv 138,231,51200
mvva 96,144,212
la 136,252,50,2048
mvva 96,148,220
tv 154,231,59392
mvva 100,148,252
tv 156,231,61440
tv 158,231,63488
mvva 100,144,244
mvva 100,132,236
la 144,250,50,0
mvva 100,128,228
mvva 104,128,192
mvva 104,132,200
mvva 104,144,208
la 152,250,50,2048
mvva 104,148,216
mvva 108,148,248
mvva 108,144,240
mvva 108,132,232
la 184,250,50,3072
mvva 108,128,224
s_barrier
s_waitcnt lgkmcnt(0)
mvva 124,136,224
tv 132,228,4096
tv 134,228,6144
tv 144,228,8192
mvva 124,140,232
tv 128,228,0
tv 130,228,2048
mvva 124,152,240
mvva 124,156,248
la 176,250,50,1024
tv 146,228,10240
mvva 120,156,216
L_vs29:
s_cbranch_execz L_vr1
tv 148,228,12288
tv 150,228,14336
mvva 120,152,208
mvva 120,140,200
mvva 120,136,192
la 168,252,50,3072
mvva 116,136,228
mvva 116,140,236
mvva 116,152,244
mvva 116,156,252
la 160,252,50,1024
mvva 112,156,220
mvva 112,152,212
mvva 112,140,204
tv 156,229,12288
tv 158,229,14336
mvva 112,136,196
s_waitcnt lgkmcnt(2)
mvva 0,128,68
dl 249,36,51,2048
tv 140,229,4096
mvva 0,132,76
tv 136,229,0
tv 138,229,2048
mvva 0,144,84
tv 142,229,6144
s_waitcnt vmcnt(22)
mvva 0,148,92
tv 152,229,8192
tv 154,229,10240
mvva 4,148,124
dl 247,36,51,3072
s_lshl_b32 s51,s98,5
s_xor_b32 s51,s64,s51
s_add_u32 m0,s52,0
s_xor_b32 s51,s51,4456448
mvva 4,144,116
mvva 4,132,108
mvva 4,128,100
mvva 8,128,64
dl 245,36,51,0
mvva 8,132,72
mvva 8,144,80
mvva 8,148,88
s_waitcnt vmcnt(16)
v_cmp_ne_u32_e32 vcc,4,v253
mvva 12,148,120
dl 246,36,51,1024
s_cbranch_vccnz L_spa6o
L_spra6o:
mvva 12,144,112
s_xor_b32 s50,s67,s98
mvva 12,132,104
s_xor_b32 s50,s50,131072
tv 148,230,12288
tv 150,230,14336
mvva 12,128,96
s_waitcnt lgkmcnt(2)
mvva 28,136,96
dl 249,36,51,2048
tv 132,230,4096
mvva 28,140,104
tv 128,230,0
tv 130,230,2048
mvva 28,152,112
tv 134,230,6144
mvva 28,156,120
tv 144,230,8192
tv 146,230,10240
mvva 24,156,88
mvva 24,152,80
la 4,252,50,0
mvva 24,140,72
s_xor_b32 s53,s60,s98
mvva 24,136,64
s_xor_b32 s53,s53,8704
mvva 20,136,100
mvva 20,140,108
la 12,252,50,2048
mvva 20,152,116
mvva 20,156,124
mvva 16,156,92
mvva 16,152,84
buffer_load_dword v253,v242,s[8:11],s53 offen sc1
mvva 16,140,76
tv 156,231,12288
mvva 16,136,68
tv 152,231,8192
s_waitcnt lgkmcnt(2)
mvva 32,128,68
tv 140,231,4096
mvva 32,132,76
la 20,250,50,0
tv 136,231,0
tv 138,231,2048
mvva 32,144,84
tv 142,231,6144
mvva 32,148,92
tv 154,231,10240
mvva 36,148,124
tv 158,231,14336
mvva 36,144,116
la 28,250,50,2048
mvva 36,132,108
mvva 36,128,100
mvva 40,128,64
mvva 40,132,72
mvva 40,144,80
la 60,250,50,3072
mvva 40,148,88
mvva 44,148,120
mvva 44,144,112
mvva 44,132,104
la 52,250,50,1024
mvva 44,128,96
tv 144,228,24576
s_waitcnt lgkmcnt(1)
mvva 60,136,96
tv 132,228,20480
tv 134,228,22528
mvva 60,140,104
tv 128,228,16384
tv 130,228,18432
mvva 60,152,112
la 44,252,50,3072
mvva 60,156,120
tv 146,228,26624
mvva 56,156,88
tv 148,228,28672
tv 150,228,30720
mvva 56,152,80
mvva 56,140,72
mvva 56,136,64
la 36,252,50,1024
s_add_u32 s50,s50,s55
mvva 52,136,100
mvva 52,140,108
mvva 52,152,116
mvva 52,156,124
la 0,252,50,0
mvva 48,156,92
mvva 48,152,84
mvva 48,140,76
tv 156,229,28672
mvva 48,136,68
la 8,252,50,2048
tv 152,229,24576
s_waitcnt lgkmcnt(2)
mvva 64,128,68
tv 140,229,20480
mvva 64,132,76
tv 136,229,16384
tv 138,229,18432
mvva 64,144,84
tv 142,229,22528
mvva 64,148,92
la 16,250,50,0
tv 154,229,26624
mvva 68,148,124
tv 158,229,30720
mvva 68,144,116
mvva 68,132,108
mvva 68,128,100
la 24,250,50,2048
mvva 72,128,64
mvva 72,132,72
mvva 72,144,80
mvva 72,148,88
mvva 76,148,120
la 56,250,50,3072
mvva 76,144,112
mvva 76,132,104
tv 148,224,45056
tv 150,224,47104
mvva 76,128,96
s_waitcnt lgkmcnt(2)
mvva 92,136,96
la 48,250,50,1024
tv 132,224,36864
mvva 92,140,104
tv 128,224,32768
tv 130,224,34816
mvva 92,152,112
tv 134,224,38912
mvva 92,156,120
tv 144,224,40960
tv 146,224,43008
mvva 88,156,88
la 40,252,50,3072
mvva 88,152,80
mvva 88,140,72
mvva 88,136,64
mvva 84,136,100
mvva 84,140,108
la 32,252,50,1024
mvva 84,152,116
mvva 84,156,124
mvva 80,156,92
mvva 80,152,84
dl 247,36,51,3072
s_lshl_b32 s51,s98,5
s_xor_b32 s51,s64,s51
s_add_u32 m0,s52,16384
s_xor_b32 s51,s51,4718592
mvva 80,140,76
tv 152,225,40960
mvva 80,136,68
tv 154,225,43008
s_waitcnt lgkmcnt(2)
mvva 0,128,132
tv 140,225,36864
mvva 0,132,140
dl 245,36,51,0
tv 136,225,32768
tv 138,225,34816
mvva 0,144,148
tv 142,225,38912
s_waitcnt vmcnt(24)
mvva 0,148,156
mvva 4,148,188
tv 156,225,45056
tv 158,225,47104
mvva 4,144,180
dl 246,36,51,1024
s_waitcnt vmcnt(17)
v_cmp_ne_u32_e32 vcc,4,v253
mvva 4,132,172
s_cbranch_vccnz L_spa7o
L_spra7o:
mvva 4,128,164
s_xor_b32 s50,s67,s98
mvva 8,128,128
s_xor_b32 s50,s50,139264
mvva 8,132,136
dl 249,36,51,2048
mvva 8,144,144
mvva 8,148,152
mvva 12,148,184
mvva 12,144,176
mvva 12,132,168
dl 247,36,51,3072
mvva 12,128,160
s_barrier
tv 144,226,40960
s_lshl_b32 s51,s98,5
s_waitcnt lgkmcnt(1)
mvva 28,136,160
s_xor_b32 s53,s60,s98
tv 132,226,36864
tv 134,226,38912
s_xor_b32 s53,s53,16896
mvva 28,140,168
s_xor_b32 s51,s65,s51
tv 128,226,32768
tv 130,226,34816
mvva 28,152,176
lv 164,252,50,0
mvva 28,156,184
s_add_u32 m0,s52,98304
tv 146,226,43008
s_xor_b32 s51,s51,4456448
mvva 24,156,152
tv 148,226,45056
tv 150,226,47104
mvva 24,152,144
mvva 24,140,136
buffer_load_dword v253,v242,s[8:11],s53 offen sc1
mvva 24,136,128
mvva 20,136,164
mvva 20,140,172
mvva 20,152,180
lv 172,252,50,2048
mvva 20,156,188
mvva 16,156,156
mvva 16,152,148
mvva 16,140,140
lv 180,250,50,0
mvva 16,136,132
tv 152,227,40960
s_waitcnt lgkmcnt(1)
mvva 32,128,132
tv 140,227,36864
tv 142,227,38912
mvva 32,132,140
tv 136,227,32768
tv 138,227,34816
mvva 32,144,148
mvva 32,148,156
lv 188,250,50,2048
tv 154,227,43008
mvva 36,148,188
tv 156,227,45056
tv 158,227,47104
mvva 36,144,180
mvva 36,132,172
mvva 36,128,164
lv 220,250,50,3072
mvva 40,128,128
mvva 40,132,136
mvva 40,144,144
mvva 40,148,152
lv 212,250,50,1024
mvva 44,148,184
mvva 44,144,176
mvva 44,132,168
tv 148,224,61440
tv 150,224,63488
mvva 44,128,160
s_waitcnt lgkmcnt(2)
mvva 60,136,160
lv 204,252,50,3072
tv 132,224,53248
mvva 60,140,168
tv 128,224,49152
tv 130,224,51200
mvva 60,152,176
tv 134,224,55296
mvva 60,156,184
tv 144,224,57344
tv 146,224,59392
mvva 56,156,152
lv 196,252,50,1024
s_add_u32 s50,s50,s55
mvva 56,152,144
mvva 56,140,136
mvva 56,136,128
mvva 52,136,164
lv 160,252,50,0
mvva 52,140,172
mvva 52,152,180
mvva 52,156,188
mvva 48,156,156
lv 168,252,50,2048
mvva 48,152,148
mvva 48,140,140
tv 156,225,61440
tv 158,225,63488
mvva 48,136,132
s_waitcnt lgkmcnt(2)
mvva 64,128,132
lv 176,250,50,0
tv 140,225,53248
mvva 64,132,140
tv 136,225,49152
tv 138,225,51200
mvva 64,144,148
tv 142,225,55296
mvva 64,148,156
tv 152,225,57344
tv 154,225,59392
mvva 68,148,188
mvva 68,144,180
lv 184,250,50,2048
mvva 68,132,172
mvva 68,128,164
mvva 72,128,128
mvva 72,132,136
lv 216,250,50,3072
mvva 72,144,144
mvva 72,148,152
mvva 76,148,184
mvva 76,144,176
lv 208,250,50,1024
mvva 76,132,168
tv 148,232,12288
mvva 76,128,160
tv 144,232,8192
s_waitcnt lgkmcnt(2)
mvva 92,136,160
tv 132,232,4096
mvva 92,140,168
tv 128,232,0
tv 130,232,2048
mvva 92,152,176
lv 200,252,50,3072
tv 134,232,6144
mvva 92,156,184
tv 146,232,10240
mvva 88,156,152
tv 150,232,14336
mvva 88,152,144
mvva 88,140,136
lv 192,252,50,1024
mvva 88,136,128
mvva 84,136,164
mvva 84,140,172
mvva 84,152,180
dl 245,36,51,0
mvva 84,156,188
mvva 80,156,156
mvva 80,152,148
mvva 80,140,140
dl 246,36,51,1024
mvva 80,136,132
tv 152,233,8192
s_waitcnt vmcnt(17)
v_cmp_ne_u32_e32 vcc,4,v253
s_waitcnt lgkmcnt(1)
mvva 0,128,4
s_cbranch_vccnz L_spa8o
L_spra8o:
tv 140,233,4096
tv 142,233,6144
mvva 0,132,12
s_xor_b32 s50,s67,s98
tv 136,233,0
s_xor_b32 s50,s50,147456
tv 138,233,2048
mvva 0,144,20
dl 249,36,51,2048
mvva 0,148,28
tv 154,233,10240
v_and_b32_e32 v242,48,v244
mvva 4,148,60
tv 156,233,12288
tv 158,233,14336
mvva 4,144,52
v_lshlrev_b32_e32 v242,3,v242
mvva 4,132,44
mvva 4,128,36
la 196,252,50,0
mvva 8,128,0
s_andn2_b32 s53,s61,384
mvva 8,132,8
mvva 8,144,16
mvva 8,148,24
la 204,252,50,2048
mvva 12,148,56
mvva 12,144,48
mvva 12,132,40
tv 148,234,12288
mvva 12,128,32
buffer_load_dword v253,v242,s[8:11],s53 offen sc1
tv 144,234,8192
s_waitcnt lgkmcnt(2)
mvva 28,136,32
tv 132,234,4096
mvva 28,140,40
tv 128,234,0
tv 130,234,2048
mvva 28,152,48
tv 134,234,6144
mvva 28,156,56
la 212,250,50,0
tv 146,234,10240
mvva 24,156,24
tv 150,234,14336
mvva 24,152,16
mvva 24,140,8
mvva 24,136,0
la 220,250,50,2048
mvva 20,136,36
mvva 20,140,44
mvva 20,152,52
mvva 20,156,60
mvva 16,156,28
la 252,250,50,3072
mvva 16,152,20
mvva 16,140,12
tv 156,235,12288
tv 158,235,14336
mvva 16,136,4
s_waitcnt lgkmcnt(2)
mvva 32,128,4
la 244,250,50,1024
tv 140,235,4096
mvva 32,132,12
tv 136,235,0
tv 138,235,2048
mvva 32,144,20
tv 142,235,6144
mvva 32,148,28
tv 152,235,8192
tv 154,235,10240
mvva 36,148,60
la 236,252,50,3072
mvva 36,144,52
mvva 36,132,44
mvva 36,128,36
mvva 40,128,0
mvva 40,132,8
la 228,252,50,1024
s_add_u32 s50,s50,s55
mvva 40,144,16
mvva 40,148,24
mvva 44,148,56
mvva 44,144,48
la 192,252,50,0
mvva 44,132,40
mvva 44,128,32
s_barrier
s_waitcnt lgkmcnt(0)
mvva 60,136,32
tv 132,232,20480
tv 144,232,24576
mvva 60,140,40
la 200,252,50,2048
tv 128,232,16384
tv 130,232,18432
mvva 60,152,48
tv 134,232,22528
mvva 60,156,56
tv 146,232,26624
mvva 56,156,24
tv 148,232,28672
tv 150,232,30720
mvva 56,152,16
la 208,250,50,0
mvva 56,140,8
mvva 56,136,0
mvva 52,136,36
mvva 52,140,44
la 216,250,50,2048
mvva 52,152,52
mvva 52,156,60
mvva 48,156,28
mvva 48,152,20
mvva 48,140,12
la 248,250,50,3072
mvva 48,136,4
tv 152,233,24576
s_waitcnt lgkmcnt(1)
mvva 64,128,4
tv 140,233,20480
tv 142,233,22528
mvva 64,132,12
tv 136,233,16384
tv 138,233,18432
mvva 64,144,20
la 240,250,50,1024
mvva 64,148,28
tv 154,233,26624
mvva 68,148,60
tv 156,233,28672
tv 158,233,30720
mvva 68,144,52
L_vs30:
s_cbranch_execz L_vr1
mvva 68,132,44
la 232,252,50,3072
mvva 68,128,36
mvva 72,128,0
mvva 72,132,8
mvva 72,144,16
la 224,252,50,1024
mvva 72,148,24
mvva 76,148,56
mvva 76,144,48
mvva 76,132,40
dl 247,36,51,3072
s_lshl_b32 s51,s98,5
s_xor_b32 s51,s65,s51
s_add_u32 m0,s52,114688
s_xor_b32 s51,s51,4718592
mvva 76,128,32
tv 144,224,8192
s_waitcnt lgkmcnt(1)
mvva 92,136,32
tv 132,224,4096
tv 134,224,6144
mvva 92,140,40
tv 128,224,0
tv 130,224,2048
mvva 92,152,48
mvva 92,156,56
dl 245,36,51,0
tv 146,224,10240
mvva 88,156,24
tv 148,224,12288
tv 150,224,14336
mvva 88,152,16
mvva 88,140,8
mvva 88,136,0
dl 246,36,51,1024
s_waitcnt vmcnt(17)
v_cmp_ne_u32_e32 vcc,4,v253
mvva 84,136,36
s_cbranch_vccnz L_sppo
L_sprpo:
mvva 84,140,44
mvva 84,152,52
mvva 84,156,60
dl 249,36,51,2048
mvva 80,156,28
mvva 80,152,20
mvva 80,140,12
tv 156,225,12288
mvva 80,136,4
tv 152,225,8192
s_waitcnt lgkmcnt(2)
mvvv 0,128,164
dl 247,36,51,3072
tv 140,225,4096
mvvv 0,132,172
tv 136,225,0
tv 138,225,2048
mvvv 0,144,180
tv 142,225,6144
mvvv 0,148,188
tv 154,225,10240
mvvv 4,148,220
tv 158,225,14336
mvvv 4,144,212
mvvv 4,132,204
mvvv 4,128,196
mvvv 8,128,160
mvvv 8,132,168
mvvv 8,144,176
mvvv 8,148,184
)"
R"(mvvv 12,148,216
mvvv 12,144,208
mvvv 12,132,200
tv 148,224,28672
mvvv 12,128,192
tv 144,224,24576
s_waitcnt lgkmcnt(2)
mvvv 28,136,192
tv 132,224,20480
mvvv 28,140,200
tv 128,224,16384
tv 130,224,18432
mvvv 28,152,208
tv 134,224,22528
mvvv 28,156,216
tv 146,224,26624
mvvv 24,156,184
tv 150,224,30720
mvvv 24,152,176
mvvv 24,140,168
mvvv 24,136,160
mvvv 20,136,196
mvvv 20,140,204
mvvv 20,152,212
mvvv 20,156,220
mvvv 16,156,188
mvvv 16,152,180
mvvv 16,140,172
tv 156,225,28672
mvvv 16,136,164
tv 152,225,24576
s_waitcnt lgkmcnt(2)
mvva 0,128,196
tv 140,225,20480
mvva 0,132,204
tv 136,225,16384
tv 138,225,18432
s_waitcnt vmcnt(14)
mvva 0,144,212
tv 142,225,22528
mvva 0,148,220
tv 154,225,26624
mvva 4,148,252
tv 158,225,30720
mvva 4,144,244
mvva 4,132,236
s_waitcnt vmcnt(13)
mvva 4,128,228
s_waitcnt vmcnt(12)
mvva 8,128,192
cv 0,0,1
cv 1,2,3
s_waitcnt vmcnt(11)
mvva 8,132,200
s_waitcnt vmcnt(10)
mvva 8,144,208
cv 4,4,5
cv 5,6,7
s_waitcnt vmcnt(9)
mvva 8,148,216
s_waitcnt vmcnt(8)
mvva 12,148,248
s_waitcnt vmcnt(7)
mvva 12,144,240
s_waitcnt vmcnt(6)
mvva 12,132,232
tv 148,226,12288
s_waitcnt vmcnt(5)
mvva 12,128,224
tv 144,226,8192
cv 8,8,9
s_waitcnt lgkmcnt(2)
mvva 28,136,224
tv 132,226,4096
cv 9,10,11
mvva 28,140,232
tv 128,226,0
tv 130,226,2048
mvva 28,152,240
tv 134,226,6144
cv 12,12,13
mvva 28,156,248
tv 146,226,10240
cv 13,14,15
mvva 24,156,216
tv 150,226,14336
mvva 24,152,208
mvva 24,140,200
mvva 24,136,192
mvva 20,136,228
cv 14,28,29
cv 15,30,31
mvva 20,140,236
buffer_store_dwordx4 v[12:15],v244,s[8:11],s56 offen offset:3072 sc0 sc1
mvva 20,152,244
mvva 20,156,252
mvva 16,156,220
cv 10,24,25
cv 11,26,27
mvva 16,152,212
buffer_store_dwordx4 v[8:11],v244,s[8:11],s56 offen offset:2048 sc0 sc1
mvva 16,140,204
tv 156,227,12288
mvva 16,136,196
cv 6,20,21
cv 7,22,23
s_waitcnt lgkmcnt(1)
mvvv 32,128,164
tv 140,227,4096
mvvv 32,132,172
buffer_store_dwordx4 v[4:7],v244,s[8:11],s56 offen offset:1024 sc0 sc1
tv 136,227,0
tv 138,227,2048
mvvv 32,144,180
tv 142,227,6144
cv 2,16,17
mvvv 32,148,188
tv 152,227,8192
tv 154,227,10240
mvvv 36,148,220
tv 158,227,14336
cv 3,18,19
mvvv 36,144,212
buffer_store_dwordx4 v[0:3],v244,s[8:11],s56 offen offset:0 sc0 sc1
mvvv 36,132,204
mvvv 36,128,196
mvvv 40,128,160
mvvv 40,132,168
mvvv 40,144,176
mvvv 40,148,184
mvvv 44,148,216
mvvv 44,144,208
mvvv 44,132,200
tv 148,226,28672
mvvv 44,128,192
s_waitcnt vmcnt(4)
s_barrier
tv 144,226,24576
s_waitcnt lgkmcnt(2)
mvvv 60,136,192
tv 132,226,20480
mvvv 60,140,200
tv 128,226,16384
tv 130,226,18432
mvvv 60,152,208
tv 134,226,22528
mvvv 60,156,216
tv 146,226,26624
mvvv 56,156,184
tv 150,226,30720
mvvv 56,152,176
mvvv 56,140,168
mvvv 56,136,160
mvvv 52,136,196
mvvv 52,140,204
mvvv 52,152,212
mvvv 52,156,220
mvvv 48,156,188
mvvv 48,152,180
mvvv 48,140,172
tv 156,227,28672
mvvv 48,136,164
tv 152,227,24576
s_waitcnt lgkmcnt(2)
mvva 32,128,196
tv 140,227,20480
mvva 32,132,204
tv 136,227,16384
tv 138,227,18432
mvva 32,144,212
tv 142,227,22528
mvva 32,148,220
tv 154,227,26624
mvva 36,148,252
tv 158,227,30720
mvva 36,144,244
mvva 36,132,236
mvva 36,128,228
mvva 40,128,192
mvva 40,132,200
cv 32,32,33
cv 33,34,35
mvva 40,144,208
mvva 40,148,216
mvva 44,148,248
mvva 44,144,240
cv 36,36,37
cv 37,38,39
mvva 44,132,232
tv 148,228,45056
mvva 44,128,224
tv 144,228,40960
cv 40,40,41
s_waitcnt lgkmcnt(2)
mvva 60,136,224
tv 132,228,36864
cv 41,42,43
mvva 60,140,232
tv 128,228,32768
tv 130,228,34816
mvva 60,152,240
tv 134,228,38912
cv 44,44,45
mvva 60,156,248
tv 146,228,43008
cv 45,46,47
mvva 56,156,216
tv 150,228,47104
mvva 56,152,208
mvva 56,140,200
mvva 56,136,192
mvva 52,136,228
cv 46,60,61
cv 47,62,63
mvva 52,140,236
buffer_store_dwordx4 v[44:47],v244,s[8:11],s57 offen offset:3072 sc0 sc1
mvva 52,152,244
mvva 52,156,252
mvva 48,156,220
cv 42,56,57
cv 43,58,59
mvva 48,152,212
buffer_store_dwordx4 v[40:43],v244,s[8:11],s57 offen offset:2048 sc0 sc1
mvva 48,140,204
tv 156,229,45056
mvva 48,136,196
cv 38,52,53
cv 39,54,55
s_waitcnt lgkmcnt(1)
mvvv 64,128,164
tv 140,229,36864
mvvv 64,132,172
buffer_store_dwordx4 v[36:39],v244,s[8:11],s57 offen offset:1024 sc0 sc1
tv 136,229,32768
tv 138,229,34816
mvvv 64,144,180
tv 142,229,38912
cv 34,48,49
mvvv 64,148,188
tv 152,229,40960
tv 154,229,43008
mvvv 68,148,220
tv 158,229,47104
cv 35,50,51
mvvv 68,144,212
buffer_store_dwordx4 v[32:35],v244,s[8:11],s57 offen offset:0 sc0 sc1
mvvv 68,132,204
mvvv 68,128,196
mvvv 72,128,160
mvvv 72,132,168
mvvv 72,144,176
mvvv 72,148,184
mvvv 76,148,216
mvvv 76,144,208
mvvv 76,132,200
tv 148,228,61440
mvvv 76,128,192
tv 144,228,57344
s_waitcnt lgkmcnt(2)
mvvv 92,136,192
tv 132,228,53248
mvvv 92,140,200
tv 128,228,49152
tv 130,228,51200
mvvv 92,152,208
tv 134,228,55296
mvvv 92,156,216
tv 146,228,59392
mvvv 88,156,184
tv 150,228,63488
mvvv 88,152,176
mvvv 88,140,168
mvvv 88,136,160
mvvv 84,136,196
mvvv 84,140,204
mvvv 84,152,212
mvvv 84,156,220
mvvv 80,156,188
mvvv 80,152,180
mvvv 80,140,172
tv 156,229,61440
mvvv 80,136,164
tv 152,229,57344
s_waitcnt lgkmcnt(2)
mvva 64,128,196
tv 140,229,53248
mvva 64,132,204
tv 136,229,49152
tv 138,229,51200
mvva 64,144,212
tv 142,229,55296
mvva 64,148,220
tv 154,229,59392
mvva 68,148,252
tv 158,229,63488
mvva 68,144,244
mvva 68,132,236
mvva 68,128,228
s_xor_b32 s82,s59,12
mvva 72,128,192
s_mul_i32 s82,s82,12288
cv 64,64,65
s_add_u32 s82,s82,0
mvva 72,132,200
lv 16,244,82,0
mvva 72,144,208
cv 65,66,67
cv 68,68,69
mvva 72,148,216
cv 69,70,71
mvva 76,148,248
mvva 76,144,240
lv 20,244,82,1024
mvva 76,132,232
tv 148,230,28672
mvva 76,128,224
tv 144,230,24576
cv 72,72,73
s_waitcnt lgkmcnt(2)
mvva 92,136,224
tv 132,230,20480
cv 73,74,75
mvva 92,140,232
tv 128,230,16384
tv 130,230,18432
mvva 92,152,240
lv 24,244,82,2048
tv 134,230,22528
mvva 92,156,248
tv 146,230,26624
cv 76,76,77
mvva 88,156,216
tv 150,230,30720
cv 77,78,79
mvva 88,152,208
mvva 88,140,200
lv 28,244,82,3072
mvva 88,136,192
cv 78,92,93
cv 79,94,95
mvva 84,136,228
mvva 84,140,236
mvva 84,152,244
buffer_store_dwordx4 v[76:79],v244,s[8:11],s58 offen offset:3072 sc0 sc1
mvva 84,156,252
mvva 80,156,220
cv 74,88,89
cv 75,90,91
mvva 80,152,212
mvva 80,140,204
buffer_store_dwordx4 v[72:75],v244,s[8:11],s58 offen offset:2048 sc0 sc1
tv 156,231,28672
mvva 80,136,196
cv 70,84,85
cv 71,86,87
s_waitcnt lgkmcnt(1)
mvva 96,128,68
tv 140,231,20480
mvva 96,132,76
tv 136,231,16384
tv 138,231,18432
mvva 96,144,84
buffer_store_dwordx4 v[68:71],v244,s[8:11],s58 offen offset:1024 sc0 sc1
tv 142,231,22528
mvva 96,148,92
tv 152,231,24576
tv 154,231,26624
mvva 100,148,124
tv 158,231,30720
cv 66,80,81
cv 67,82,83
mvva 100,144,116
mvva 100,132,108
mvva 100,128,100
buffer_store_dwordx4 v[64:67],v244,s[8:11],s58 offen offset:0 sc0 sc1
mvva 104,128,64
mvva 104,132,72
mvva 104,144,80
mvva 104,148,88
mvva 108,148,120
mvva 108,144,112
mvva 108,132,104
tv 148,226,61440
mvva 108,128,96
tv 144,226,57344
s_waitcnt lgkmcnt(2)
mvva 124,136,96
tv 132,226,53248
mvva 124,140,104
tv 128,226,49152
tv 130,226,51200
mvva 124,152,112
tv 134,226,55296
mvva 124,156,120
tv 146,226,59392
mvva 120,156,88
tv 150,226,63488
mvva 120,152,80
mvva 120,140,72
mvva 120,136,64
mvva 116,136,100
mvva 116,140,108
s_waitcnt vmcnt(7)
v_max_u32_e32 v248,v16,v17
mvva 116,152,116
v_max3_u32 v248,v248,v18,v19
s_waitcnt vmcnt(6)
v_max3_u32 v248,v248,v20,v21
v_max3_u32 v248,v248,v22,v23
mvva 116,156,124
s_waitcnt vmcnt(5)
v_max3_u32 v248,v248,v24,v25
v_max3_u32 v248,v248,v26,v27
s_waitcnt vmcnt(4)
v_max3_u32 v248,v248,v28,v29
mvva 112,156,92
v_max3_u32 v248,v248,v30,v31
v_cmp_eq_u32_e32 vcc,-1,v248
mvva 112,152,84
s_cbranch_vccnz L_spx0o
L_sprx0o:
mvva 112,140,76
tv 156,227,61440
tv 158,227,63488
v_lshlrev_b32_e32 v251,16,v18
v_and_b32_e32 v255,4294901760,v18
mvva 112,136,68
tv 152,227,57344
tv 154,227,59392
s_waitcnt lgkmcnt(4)
mvva 96,128,132
v_lshlrev_b32_e32 v253,16,v19
v_and_b32_e32 v242,4294901760,v19
mvva 96,132,140
tv 136,227,49152
tv 138,227,51200
mvva 96,144,148
tv 140,227,53248
tv 142,227,55296
v_add_f32_e32 v112,v112,v251
mvva 96,148,156
v_add_f32_e32 v113,v113,v255
v_add_f32_e32 v114,v114,v253
mvva 100,148,188
v_add_f32_e32 v115,v115,v242
v_lshlrev_b32_e32 v251,16,v22
mvva 100,144,180
v_and_b32_e32 v255,4294901760,v22
v_lshlrev_b32_e32 v253,16,v23
mvva 100,132,172
v_and_b32_e32 v242,4294901760,v23
v_add_f32_e32 v116,v116,v251
v_add_f32_e32 v117,v117,v255
mvva 100,128,164
v_add_f32_e32 v118,v118,v253
v_add_f32_e32 v119,v119,v242
mvva 104,128,128
v_lshlrev_b32_e32 v251,16,v26
v_and_b32_e32 v255,4294901760,v26
mvva 104,132,136
v_lshlrev_b32_e32 v253,16,v27
v_and_b32_e32 v242,4294901760,v27
mvva 104,144,144
v_add_f32_e32 v120,v120,v251
v_add_f32_e32 v121,v121,v255
v_add_f32_e32 v122,v122,v253
mvva 104,148,152
v_add_f32_e32 v123,v123,v242
v_lshlrev_b32_e32 v251,16,v30
mvva 108,148,184
v_and_b32_e32 v255,4294901760,v30
v_lshlrev_b32_e32 v253,16,v31
mvva 108,144,176
v_and_b32_e32 v242,4294901760,v31
v_add_f32_e32 v124,v124,v251
mvva 108,132,168
L_vs31:
s_cbranch_execz L_vr1
v_add_f32_e32 v125,v125,v255
v_add_f32_e32 v126,v126,v253
v_add_f32_e32 v127,v127,v242
mvva 108,128,160
tv 144,234,24576
v_lshlrev_b32_e32 v251,16,v16
s_waitcnt lgkmcnt(1)
mvva 124,136,160
tv 132,234,20480
v_and_b32_e32 v255,4294901760,v16
mvva 124,140,168
tv 128,234,16384
tv 130,234,18432
mvva 124,152,176
tv 134,234,22528
v_lshlrev_b32_e32 v253,16,v17
mvva 124,156,184
tv 146,234,26624
v_and_b32_e32 v242,4294901760,v17
mvva 120,156,152
tv 148,234,28672
tv 150,234,30720
mvva 120,152,144
v_add_f32_e32 v96,v96,v251
v_add_f32_e32 v97,v97,v255
mvva 120,140,136
v_add_f32_e32 v98,v98,v253
v_add_f32_e32 v99,v99,v242
mvva 120,136,128
v_lshlrev_b32_e32 v251,16,v20
v_and_b32_e32 v255,4294901760,v20
mvva 116,136,164
v_lshlrev_b32_e32 v253,16,v21
v_and_b32_e32 v242,4294901760,v21
mvva 116,140,172
v_add_f32_e32 v100,v100,v251
v_add_f32_e32 v101,v101,v255
mvva 116,152,180
v_add_f32_e32 v102,v102,v253
v_add_f32_e32 v103,v103,v242
mvva 116,156,188
v_lshlrev_b32_e32 v251,16,v24
v_and_b32_e32 v255,4294901760,v24
mvva 112,156,156
v_lshlrev_b32_e32 v253,16,v25
v_and_b32_e32 v242,4294901760,v25
mvva 112,152,148
v_add_f32_e32 v104,v104,v251
v_add_f32_e32 v105,v105,v255
mvva 112,140,140
tv 156,235,28672
v_add_f32_e32 v106,v106,v253
mvva 112,136,132
tv 152,235,24576
v_add_f32_e32 v107,v107,v242
s_waitcnt lgkmcnt(2)
mvva 96,128,4
tv 140,235,20480
v_lshlrev_b32_e32 v251,16,v28
mvva 96,132,12
tv 136,235,16384
tv 138,235,18432
mvva 96,144,20
tv 142,235,22528
v_and_b32_e32 v255,4294901760,v28
mvva 96,148,28
tv 154,235,26624
v_lshlrev_b32_e32 v253,16,v29
mvva 100,148,60
tv 158,235,30720
v_and_b32_e32 v242,4294901760,v29
mvva 100,144,52
v_add_f32_e32 v108,v108,v251
v_add_f32_e32 v109,v109,v255
mvva 100,132,44
v_add_f32_e32 v110,v110,v253
v_add_f32_e32 v111,v111,v242
mvva 100,128,36
mvva 104,128,0
mvva 104,132,8
mvva 104,144,16
mvva 104,148,24
mvva 108,148,56
mvva 108,144,48
mvva 108,132,40
tv 148,230,45056
mvva 108,128,32
tv 144,230,40960
s_waitcnt lgkmcnt(2)
mvva 124,136,32
s_xor_b32 s83,s59,8
tv 132,230,36864
s_mul_i32 s83,s83,12288
mvva 124,140,40
s_add_u32 s83,s83,4096
tv 128,230,32768
tv 130,230,34816
mvva 124,152,48
lv 48,244,83,0
tv 134,230,38912
mvva 124,156,56
tv 146,230,43008
mvva 120,156,24
lv 52,244,83,1024
tv 150,230,47104
mvva 120,152,16
mvva 120,140,8
lv 56,244,83,2048
mvva 120,136,0
mvva 116,136,36
lv 60,244,83,3072
mvva 116,140,44
mvva 116,152,52
s_xor_b32 s84,s59,4
mvva 116,156,60
s_mul_i32 s84,s84,12288
mvva 112,156,28
s_add_u32 s84,s84,8192
mvva 112,152,20
lv 80,244,84,0
mvva 112,140,12
tv 156,231,45056
mvva 112,136,4
lv 84,244,84,1024
tv 152,231,40960
s_waitcnt lgkmcnt(2)
mvvv 96,128,164
tv 140,231,36864
mvvv 96,132,172
lv 88,244,84,2048
tv 136,231,32768
tv 138,231,34816
mvvv 96,144,180
tv 142,231,38912
mvvv 96,148,188
lv 92,244,84,3072
tv 154,231,43008
mvvv 100,148,220
tv 158,231,47104
mvvv 100,144,212
mvvv 100,132,204
mvvv 100,128,196
mvvv 104,128,160
mvvv 104,132,168
mvvv 104,144,176
mvvv 104,148,184
mvvv 108,148,216
mvvv 108,144,208
mvvv 108,132,200
tv 148,230,61440
mvvv 108,128,192
tv 144,230,57344
s_waitcnt lgkmcnt(2)
mvvv 124,136,192
tv 132,230,53248
mvvv 124,140,200
tv 128,230,49152
tv 130,230,51200
mvvv 124,152,208
tv 134,230,55296
mvvv 124,156,216
tv 146,230,59392
mvvv 120,156,184
tv 150,230,63488
mvvv 120,152,176
mvvv 120,140,168
mvvv 120,136,160
mvvv 116,136,196
mvvv 116,140,204
mvvv 116,152,212
mvvv 116,156,220
mvvv 112,156,188
mvvv 112,152,180
mvvv 112,140,172
tv 156,231,61440
mvvv 112,136,164
tv 152,231,57344
s_waitcnt lgkmcnt(2)
mvva 96,128,196
tv 140,231,53248
mvva 96,132,204
tv 136,231,49152
tv 138,231,51200
mvva 96,144,212
tv 142,231,55296
mvva 96,148,220
tv 154,231,59392
mvva 100,148,252
tv 158,231,63488
mvva 100,144,244
mvva 100,132,236
mvva 100,128,228
mvva 104,128,192
mvva 104,132,200
mvva 104,144,208
mvva 104,148,216
s_waitcnt vmcnt(7)
v_max_u32_e32 v248,v48,v49
mvva 108,148,248
v_max3_u32 v248,v248,v50,v51
s_waitcnt vmcnt(6)
v_max3_u32 v248,v248,v52,v53
mvva 108,144,240
v_max3_u32 v248,v248,v54,v55
s_waitcnt vmcnt(5)
v_max3_u32 v248,v248,v56,v57
mvva 108,132,232
v_max3_u32 v248,v248,v58,v59
s_waitcnt vmcnt(4)
v_max3_u32 v248,v248,v60,v61
mvva 108,128,224
v_max3_u32 v248,v248,v62,v63
v_cmp_eq_u32_e32 vcc,-1,v248
s_waitcnt lgkmcnt(0)
mvva 124,136,224
s_cbranch_vccnz L_spx1o
L_sprx1o:
mvva 124,140,232
mvva 124,152,240
mvva 124,156,248
s_waitcnt vmcnt(3)
v_max_u32_e32 v248,v80,v81
mvva 120,156,216
v_max3_u32 v248,v248,v82,v83
s_waitcnt vmcnt(2)
v_max3_u32 v248,v248,v84,v85
mvva 120,152,208
v_max3_u32 v248,v248,v86,v87
s_waitcnt vmcnt(1)
v_max3_u32 v248,v248,v88,v89
mvva 120,140,200
v_max3_u32 v248,v248,v90,v91
s_waitcnt vmcnt(0)
v_max3_u32 v248,v248,v92,v93
mvva 120,136,192
v_max3_u32 v248,v248,v94,v95
v_cmp_eq_u32_e32 vcc,-1,v248
mvva 116,136,228
s_cbranch_vccnz L_spx2o
L_sprx2o:
mvva 116,140,236
mvva 116,152,244
mvva 116,156,252
mvva 112,156,220
mvva 112,152,212
mvva 112,140,204
mvva 112,136,196
mvvv 108,238,60
mvvv 108,238,92
mvvv 124,236,60
mvvv 124,236,92
mvvv 104,238,56
mvvv 104,238,88
mvvv 120,236,56
mvvv 120,236,88
mvvv 100,238,52
mvvv 100,238,84
cv 108,108,109
cv 109,110,111
cv 110,124,125
cv 111,126,127
cv 104,104,105
v_permlane16_swap_b32_e32 v108,v110
mvvv 116,236,52
v_permlane16_swap_b32_e32 v109,v111
cv 105,106,107
cv 106,120,121
cv 107,122,123
mvvv 116,236,84
buffer_store_dwordx4 v[108:111],v243,s[44:47],s73 offen sc0 sc1
v_permlane16_swap_b32_e32 v104,v106
v_permlane16_swap_b32_e32 v105,v107
mvvv 96,238,48
cv 100,100,101
cv 101,102,103
mvvv 96,238,80
buffer_store_dwordx4 v[104:107],v243,s[44:47],s72 offen sc0 sc1
cv 102,116,117
cv 103,118,119
mvvv 112,236,48
v_permlane16_swap_b32_e32 v100,v102
v_permlane16_swap_b32_e32 v101,v103
mvvv 112,236,80
buffer_store_dwordx4 v[100:103],v243,s[44:47],s71 offen sc0 sc1
cv 96,96,97
cv 97,98,99
s_nop 4
cv 98,112,113
cv 99,114,115
s_nop 0
v_permlane16_swap_b32_e32 v96,v98
v_permlane16_swap_b32_e32 v97,v99
buffer_store_dwordx4 v[96:99],v243,s[44:47],s70 offen sc0 sc1
L_oddx:
s_endpgm
L_k_ec1:
buffer_store_dwordx4 a[252:255],v244,s[8:11],s35 offen offset:1024 sc0 sc1
s_branch L_k_ecr1
L_q_ack:
s_mov_b32 s86,65536
L_q_ack2:
s_sleep 8
buffer_load_dword v246,off,s[8:11],s35 offset:1024 sc1
s_waitcnt vmcnt(0)
v_readfirstlane_b32 s87,v246
s_nop 0
s_cmp_eq_u32 s87,s32
s_cbranch_scc1 L_q_acked
s_sub_u32 s86,s86,1
s_cbranch_scc0 L_q_ack2
s_branch L_q_acked
L_q_spin:
s_mov_b32 s6,65536
L_q_spin1:
s_sleep 8
buffer_load_dword v248,v250,s[8:11],0 offen sc1
s_or_b32 s7,%1,s12
s_cbranch_scc1 L_q_spin2
buffer_load_dwordx4 a[252:255],v244,s[8:11],s35 offen sc1
s_waitcnt vmcnt(0)
buffer_store_dwordx4 a[252:255],v244,s[8:11],s35 offen offset:1024 sc0 sc1
L_q_spin2:
s_waitcnt vmcnt(0)
v_and_b32_e32 v248,v242,v248
v_cmp_ne_u32_e64 s[100:101],v243,v248
s_nop 1
s_cmp_eq_u64 s[100:101],0
s_cbranch_scc1 L_q_ok
s_sub_u32 s6,s6,1
s_cbranch_scc0 L_q_spin1
s_branch L_q_ok
L_bx:
s_mov_b32 s87,65536
L_bx1:
s_sleep 2
buffer_load_dwordx4 v[176:179],v244,s[8:11],s98 offen sc1
buffer_load_dwordx2 v[168:169],v253,s[8:11],s98 offen offset:1024 sc1
buffer_load_dwordx2 v[172:173],v253,s[8:11],s98 offen offset:1536 sc1
s_waitcnt vmcnt(0)
v_max3_u32 v180,v176,v177,v178
v_max3_u32 v180,v180,v179,v168
v_max3_u32 v180,v180,v169,v172
v_max_u32_e32 v180,v180,v173
v_cmp_eq_u32_e64 s[40:41],-1,v180
s_nop 1
s_cmp_eq_u64 s[40:41],0
s_cbranch_scc1 L_bxr
s_sub_u32 s87,s87,1
s_cbranch_scc0 L_bx1
s_branch L_bxr
L_pend2:
s_nop 7
v_add_f32_e32 v226,v152,v153
v_add_f32_e32 v227,v156,v157
v_subrev_u32_e32 v236,0x23800000,v226
v_subrev_u32_e32 v237,0x23800000,v227
v_max_u32_e32 v236,v236,v237
v_cmp_lt_u32_e64 s[40:41],s48,v236
v_rcp_f32_e32 v226,v226
v_rcp_f32_e32 v227,v227
s_add_u32 s86,s13,4096
v_mov_b32_e32 v236,0
v_mov_b32_e32 v237,0
v_mov_b32_e32 v238,0
v_mov_b32_e32 v239,0
v_mov_b64_e32 v[152:153],0
v_mov_b64_e32 v[156:157],0
v_mul_f32_e32 v226,v242,v226
v_mul_f32_e32 v227,v243,v227
v_add_u32_e32 v225,s94,v225
v_pk_mul_f32 v[160:161],v[160:161],v[226:227] op_sel:[0,0] op_sel_hi:[1,0]
v_pk_mul_f32 v[162:163],v[162:163],v[226:227] op_sel:[0,0] op_sel_hi:[1,0]
v_pk_mul_f32 v[164:165],v[164:165],v[226:227] op_sel:[0,0] op_sel_hi:[1,0]
v_pk_mul_f32 v[166:167],v[166:167],v[226:227] op_sel:[0,0] op_sel_hi:[1,0]
cv 160,160,161
cv 161,162,163
cv 162,164,165
cv 163,166,167
buffer_store_dwordx4 v[160:163],v244,s[8:11],s13 offen offset:0 sc0 sc1
v_pk_mul_f32 v[168:169],v[168:169],v[226:227] op_sel:[0,0] op_sel_hi:[1,0]
v_pk_mul_f32 v[170:171],v[170:171],v[226:227] op_sel:[0,0] op_sel_hi:[1,0]
v_pk_mul_f32 v[172:173],v[172:173],v[226:227] op_sel:[0,0] op_sel_hi:[1,0]
v_pk_mul_f32 v[174:175],v[174:175],v[226:227] op_sel:[0,0] op_sel_hi:[1,0]
cv 168,168,169
cv 169,170,171
cv 170,172,173
cv 171,174,175
buffer_store_dwordx4 v[168:171],v244,s[8:11],s13 offen offset:2048 sc0 sc1
v_pk_mul_f32 v[176:177],v[176:177],v[226:227] op_sel:[0,0] op_sel_hi:[1,0]
v_pk_mul_f32 v[178:179],v[178:179],v[226:227] op_sel:[0,0] op_sel_hi:[1,0]
v_pk_mul_f32 v[180:181],v[180:181],v[226:227] op_sel:[0,0] op_sel_hi:[1,0]
v_pk_mul_f32 v[182:183],v[182:183],v[226:227] op_sel:[0,0] op_sel_hi:[1,0]
cv 176,176,177
cv 177,178,179
cv 178,180,181
v_mfma_f32_32x32x16_bf16 v[160:175],v[236:239],v[236:239],0
cv 179,182,183
buffer_store_dwordx4 v[176:179],v244,s[8:11],s86 offen offset:0 sc0 sc1
v_pk_mul_f32 v[184:185],v[184:185],v[226:227] op_sel:[0,0] op_sel_hi:[1,0]
v_pk_mul_f32 v[186:187],v[186:187],v[226:227] op_sel:[0,0] op_sel_hi:[1,0]
v_pk_mul_f32 v[188:189],v[188:189],v[226:227] op_sel:[0,0] op_sel_hi:[1,0]
v_pk_mul_f32 v[190:191],v[190:191],v[226:227] op_sel:[0,0] op_sel_hi:[1,0]
cv 184,184,185
cv 185,186,187
cv 186,188,189
cv 187,190,191
buffer_store_dwordx4 v[184:187],v244,s[8:11],s86 offen offset:2048 sc0 sc1
v_pk_mul_f32 v[192:193],v[192:193],v[226:227] op_sel:[0,1] op_sel_hi:[1,1]
v_pk_mul_f32 v[194:195],v[194:195],v[226:227] op_sel:[0,1] op_sel_hi:[1,1]
v_pk_mul_f32 v[196:197],v[196:197],v[226:227] op_sel:[0,1] op_sel_hi:[1,1]
v_pk_mul_f32 v[198:199],v[198:199],v[226:227] op_sel:[0,1] op_sel_hi:[1,1]
cv 192,192,193
cv 193,194,195
cv 194,196,197
v_mfma_f32_32x32x16_bf16 v[176:191],v[236:239],v[236:239],0
cv 195,198,199
buffer_store_dwordx4 v[192:195],v244,s[8:11],s13 offen offset:1024 sc0 sc1
v_pk_mul_f32 v[200:201],v[200:201],v[226:227] op_sel:[0,1] op_sel_hi:[1,1]
v_pk_mul_f32 v[202:203],v[202:203],v[226:227] op_sel:[0,1] op_sel_hi:[1,1]
v_pk_mul_f32 v[204:205],v[204:205],v[226:227] op_sel:[0,1] op_sel_hi:[1,1]
v_pk_mul_f32 v[206:207],v[206:207],v[226:227] op_sel:[0,1] op_sel_hi:[1,1]
cv 200,200,201
cv 201,202,203
cv 202,204,205
cv 203,206,207
buffer_store_dwordx4 v[200:203],v244,s[8:11],s13 offen offset:3072 sc0 sc1
v_pk_mul_f32 v[208:209],v[208:209],v[226:227] op_sel:[0,1] op_sel_hi:[1,1]
v_pk_mul_f32 v[210:211],v[210:211],v[226:227] op_sel:[0,1] op_sel_hi:[1,1]
v_pk_mul_f32 v[212:213],v[212:213],v[226:227] op_sel:[0,1] op_sel_hi:[1,1]
v_pk_mul_f32 v[214:215],v[214:215],v[226:227] op_sel:[0,1] op_sel_hi:[1,1]
cv 208,208,209
cv 209,210,211
cv 210,212,213
v_mfma_f32_32x32x16_bf16 v[192:207],v[236:239],v[236:239],0
cv 211,214,215
buffer_store_dwordx4 v[208:211],v244,s[8:11],s86 offen offset:1024 sc0 sc1
v_pk_mul_f32 v[216:217],v[216:217],v[226:227] op_sel:[0,1] op_sel_hi:[1,1]
v_pk_mul_f32 v[218:219],v[218:219],v[226:227] op_sel:[0,1] op_sel_hi:[1,1]
v_pk_mul_f32 v[220:221],v[220:221],v[226:227] op_sel:[0,1] op_sel_hi:[1,1]
v_pk_mul_f32 v[222:223],v[222:223],v[226:227] op_sel:[0,1] op_sel_hi:[1,1]
cv 216,216,217
cv 217,218,219
cv 218,220,221
cv 219,222,223
buffer_store_dwordx4 v[216:219],v244,s[8:11],s86 offen offset:3072 sc0 sc1
s_nop 1
v_mfma_f32_32x32x16_bf16 v[208:223],v[236:239],v[236:239],0
s_nop 7
s_nop 1
s_cmp_eq_u32 s84,4
s_cbranch_scc1 L_ret_eb4
s_cmp_lg_u32 s80,s81
s_branch L_ret_eb5
L_redo:
s_waitcnt vmcnt(0) lgkmcnt(0)
s_nop 15
s_nop 3
s_load_dwordx2 s[76:77],%0,0
s_load_dwordx2 s[80:81],%0,16
s_mov_b32 s78,4194304
s_mov_b32 s79,131072
s_mov_b32 s82,8388608
s_mov_b32 s83,131072
s_mov_b32 s86,0
s_waitcnt lgkmcnt(0)
L_rd_t:
s_nop 15
s_nop 3
s_cmp_lt_u32 s86,2
s_cselect_b32 s88,s23,s22
s_cselect_b32 s95,s13,s14
s_and_b32 s89,s86,1
s_lshl_b32 s90,s89,10
s_add_u32 s95,s95,s90
s_lshl_b32 s89,s89,6
s_add_u32 s88,s88,s89
s_lshl_b32 s89,s12,4
s_add_u32 s88,s88,s89
v_bfe_u32 v213,v244,4,4
v_lshrrev_b32_e32 v214,8,v244
v_add_u32_e32 v210,s88,v213
v_lshlrev_b32_e32 v208,11,v210
v_lshl_add_u32 v208,v214,4,v208
v_lshlrev_b32_e32 v209,16,v214
v_lshl_add_u32 v209,v213,1,v209
s_lshl_b32 s89,s18,8
v_add_u32_e32 v209,s89,v209
v_lshlrev_b32_e32 v214,2,v214
v_sub_u32_e32 v210,v210,v214
v_accvgpr_write_b32 a192,0
v_accvgpr_write_b32 a193,0
v_accvgpr_write_b32 a194,0
v_accvgpr_write_b32 a195,0
v_accvgpr_write_b32 a196,0
v_accvgpr_write_b32 a197,0
v_accvgpr_write_b32 a198,0
v_accvgpr_write_b32 a199,0
v_accvgpr_write_b32 a200,0
v_accvgpr_write_b32 a201,0
v_accvgpr_write_b32 a202,0
v_accvgpr_write_b32 a203,0
v_accvgpr_write_b32 a204,0
v_accvgpr_write_b32 a205,0
v_accvgpr_write_b32 a206,0
v_accvgpr_write_b32 a207,0
v_accvgpr_write_b32 a208,0
v_accvgpr_write_b32 a209,0
v_accvgpr_write_b32 a210,0
v_accvgpr_write_b32 a211,0
v_accvgpr_write_b32 a212,0
v_accvgpr_write_b32 a213,0
v_accvgpr_write_b32 a214,0
v_accvgpr_write_b32 a215,0
v_accvgpr_write_b32 a216,0
v_accvgpr_write_b32 a217,0
v_accvgpr_write_b32 a218,0
v_accvgpr_write_b32 a219,0
v_accvgpr_write_b32 a220,0
v_accvgpr_write_b32 a221,0
v_accvgpr_write_b32 a222,0
v_accvgpr_write_b32 a223,0
s_mov_b32 s87,0
)"
R"(L_rd_q:
s_nop 15
s_nop 3
s_lshl_b32 s89,s87,6
s_lshl_b32 s90,s87,18
buffer_load_dwordx4 v[200:203],v208,s[76:79],s89 offen
buffer_load_ushort v160,v209,s[80:83],s90 offen offset:0
buffer_load_ushort v164,v209,s[80:83],s90 offen offset:32
buffer_load_ushort v168,v209,s[80:83],s90 offen offset:64
buffer_load_ushort v172,v209,s[80:83],s90 offen offset:96
buffer_load_ushort v176,v209,s[80:83],s90 offen offset:128
buffer_load_ushort v180,v209,s[80:83],s90 offen offset:160
buffer_load_ushort v184,v209,s[80:83],s90 offen offset:192
buffer_load_ushort v188,v209,s[80:83],s90 offen offset:224
s_add_u32 s90,s90,8192
buffer_load_ushort v192,v209,s[80:83],s90 offen offset:0
buffer_load_ushort v193,v209,s[80:83],s90 offen offset:32
buffer_load_ushort v194,v209,s[80:83],s90 offen offset:64
buffer_load_ushort v195,v209,s[80:83],s90 offen offset:96
buffer_load_ushort v196,v209,s[80:83],s90 offen offset:128
buffer_load_ushort v197,v209,s[80:83],s90 offen offset:160
buffer_load_ushort v198,v209,s[80:83],s90 offen offset:192
buffer_load_ushort v199,v209,s[80:83],s90 offen offset:224
s_add_u32 s90,s90,8192
s_waitcnt vmcnt(0)
v_lshl_or_b32 v160,v192,16,v160
v_lshl_or_b32 v164,v193,16,v164
v_lshl_or_b32 v168,v194,16,v168
v_lshl_or_b32 v172,v195,16,v172
v_lshl_or_b32 v176,v196,16,v176
v_lshl_or_b32 v180,v197,16,v180
v_lshl_or_b32 v184,v198,16,v184
v_lshl_or_b32 v188,v199,16,v188
buffer_load_ushort v161,v209,s[80:83],s90 offen offset:0
buffer_load_ushort v165,v209,s[80:83],s90 offen offset:32
buffer_load_ushort v169,v209,s[80:83],s90 offen offset:64
buffer_load_ushort v173,v209,s[80:83],s90 offen offset:96
buffer_load_ushort v177,v209,s[80:83],s90 offen offset:128
buffer_load_ushort v181,v209,s[80:83],s90 offen offset:160
buffer_load_ushort v185,v209,s[80:83],s90 offen offset:192
buffer_load_ushort v189,v209,s[80:83],s90 offen offset:224
s_add_u32 s90,s90,8192
buffer_load_ushort v192,v209,s[80:83],s90 offen offset:0
buffer_load_ushort v193,v209,s[80:83],s90 offen offset:32
buffer_load_ushort v194,v209,s[80:83],s90 offen offset:64
buffer_load_ushort v195,v209,s[80:83],s90 offen offset:96
buffer_load_ushort v196,v209,s[80:83],s90 offen offset:128
buffer_load_ushort v197,v209,s[80:83],s90 offen offset:160
buffer_load_ushort v198,v209,s[80:83],s90 offen offset:192
buffer_load_ushort v199,v209,s[80:83],s90 offen offset:224
s_add_u32 s90,s90,8192
s_waitcnt vmcnt(0)
v_lshl_or_b32 v161,v192,16,v161
v_lshl_or_b32 v165,v193,16,v165
v_lshl_or_b32 v169,v194,16,v169
v_lshl_or_b32 v173,v195,16,v173
v_lshl_or_b32 v177,v196,16,v177
v_lshl_or_b32 v181,v197,16,v181
v_lshl_or_b32 v185,v198,16,v185
v_lshl_or_b32 v189,v199,16,v189
buffer_load_ushort v162,v209,s[80:83],s90 offen offset:0
buffer_load_ushort v166,v209,s[80:83],s90 offen offset:32
buffer_load_ushort v170,v209,s[80:83],s90 offen offset:64
buffer_load_ushort v174,v209,s[80:83],s90 offen offset:96
buffer_load_ushort v178,v209,s[80:83],s90 offen offset:128
buffer_load_ushort v182,v209,s[80:83],s90 offen offset:160
buffer_load_ushort v186,v209,s[80:83],s90 offen offset:192
buffer_load_ushort v190,v209,s[80:83],s90 offen offset:224
s_add_u32 s90,s90,8192
buffer_load_ushort v192,v209,s[80:83],s90 offen offset:0
buffer_load_ushort v193,v209,s[80:83],s90 offen offset:32
buffer_load_ushort v194,v209,s[80:83],s90 offen offset:64
buffer_load_ushort v195,v209,s[80:83],s90 offen offset:96
buffer_load_ushort v196,v209,s[80:83],s90 offen offset:128
buffer_load_ushort v197,v209,s[80:83],s90 offen offset:160
buffer_load_ushort v198,v209,s[80:83],s90 offen offset:192
buffer_load_ushort v199,v209,s[80:83],s90 offen offset:224
s_add_u32 s90,s90,8192
s_waitcnt vmcnt(0)
v_lshl_or_b32 v162,v192,16,v162
v_lshl_or_b32 v166,v193,16,v166
v_lshl_or_b32 v170,v194,16,v170
v_lshl_or_b32 v174,v195,16,v174
v_lshl_or_b32 v178,v196,16,v178
v_lshl_or_b32 v182,v197,16,v182
v_lshl_or_b32 v186,v198,16,v186
v_lshl_or_b32 v190,v199,16,v190
buffer_load_ushort v163,v209,s[80:83],s90 offen offset:0
buffer_load_ushort v167,v209,s[80:83],s90 offen offset:32
buffer_load_ushort v171,v209,s[80:83],s90 offen offset:64
buffer_load_ushort v175,v209,s[80:83],s90 offen offset:96
buffer_load_ushort v179,v209,s[80:83],s90 offen offset:128
buffer_load_ushort v183,v209,s[80:83],s90 offen offset:160
buffer_load_ushort v187,v209,s[80:83],s90 offen offset:192
buffer_load_ushort v191,v209,s[80:83],s90 offen offset:224
s_add_u32 s90,s90,8192
buffer_load_ushort v192,v209,s[80:83],s90 offen offset:0
buffer_load_ushort v193,v209,s[80:83],s90 offen offset:32
buffer_load_ushort v194,v209,s[80:83],s90 offen offset:64
buffer_load_ushort v195,v209,s[80:83],s90 offen offset:96
buffer_load_ushort v196,v209,s[80:83],s90 offen offset:128
buffer_load_ushort v197,v209,s[80:83],s90 offen offset:160
buffer_load_ushort v198,v209,s[80:83],s90 offen offset:192
buffer_load_ushort v199,v209,s[80:83],s90 offen offset:224
s_add_u32 s90,s90,8192
s_waitcnt vmcnt(0)
v_lshl_or_b32 v163,v192,16,v163
v_lshl_or_b32 v167,v193,16,v167
v_lshl_or_b32 v171,v194,16,v171
v_lshl_or_b32 v175,v195,16,v175
v_lshl_or_b32 v179,v196,16,v179
v_lshl_or_b32 v183,v197,16,v183
v_lshl_or_b32 v187,v198,16,v187
v_lshl_or_b32 v191,v199,16,v191
mavv 192,160,200
mavv 196,164,200
mavv 200,168,200
mavv 204,172,200
mavv 208,176,200
mavv 212,180,200
mavv 216,184,200
mavv 220,188,200
s_add_u32 s87,s87,1
s_cmp_lt_u32 s87,32
s_cbranch_scc1 L_rd_q
s_nop 15
s_nop 3
v_accvgpr_read_b32 v192,a192
v_accvgpr_read_b32 v193,a193
v_accvgpr_read_b32 v194,a194
v_accvgpr_read_b32 v195,a195
v_accvgpr_read_b32 v196,a196
v_accvgpr_read_b32 v197,a197
v_accvgpr_read_b32 v198,a198
v_accvgpr_read_b32 v199,a199
v_mul_f32_e32 v192,0x3e0293ee,v192
v_mul_f32_e32 v193,0x3e0293ee,v193
v_mul_f32_e32 v194,0x3e0293ee,v194
v_mul_f32_e32 v195,0x3e0293ee,v195
v_mul_f32_e32 v196,0x3e0293ee,v196
v_mul_f32_e32 v197,0x3e0293ee,v197
v_mul_f32_e32 v198,0x3e0293ee,v198
v_mul_f32_e32 v199,0x3e0293ee,v199
cv 192,192,193
cv 193,194,195
cv 194,196,197
cv 195,198,199
v_accvgpr_write_b32 a224,v192
v_accvgpr_write_b32 a225,v193
v_accvgpr_write_b32 a226,v194
v_accvgpr_write_b32 a227,v195
v_accvgpr_read_b32 v192,a200
v_accvgpr_read_b32 v193,a201
v_accvgpr_read_b32 v194,a202
v_accvgpr_read_b32 v195,a203
v_accvgpr_read_b32 v196,a204
v_accvgpr_read_b32 v197,a205
v_accvgpr_read_b32 v198,a206
v_accvgpr_read_b32 v199,a207
v_mul_f32_e32 v192,0x3e0293ee,v192
v_mul_f32_e32 v193,0x3e0293ee,v193
v_mul_f32_e32 v194,0x3e0293ee,v194
v_mul_f32_e32 v195,0x3e0293ee,v195
v_mul_f32_e32 v196,0x3e0293ee,v196
v_mul_f32_e32 v197,0x3e0293ee,v197
v_mul_f32_e32 v198,0x3e0293ee,v198
v_mul_f32_e32 v199,0x3e0293ee,v199
cv 192,192,193
cv 193,194,195
cv 194,196,197
cv 195,198,199
v_accvgpr_write_b32 a228,v192
v_accvgpr_write_b32 a229,v193
v_accvgpr_write_b32 a230,v194
v_accvgpr_write_b32 a231,v195
v_accvgpr_read_b32 v192,a208
v_accvgpr_read_b32 v193,a209
v_accvgpr_read_b32 v194,a210
v_accvgpr_read_b32 v195,a211
v_accvgpr_read_b32 v196,a212
v_accvgpr_read_b32 v197,a213
v_accvgpr_read_b32 v198,a214
v_accvgpr_read_b32 v199,a215
v_mul_f32_e32 v192,0x3e0293ee,v192
v_mul_f32_e32 v193,0x3e0293ee,v193
v_mul_f32_e32 v194,0x3e0293ee,v194
v_mul_f32_e32 v195,0x3e0293ee,v195
v_mul_f32_e32 v196,0x3e0293ee,v196
v_mul_f32_e32 v197,0x3e0293ee,v197
v_mul_f32_e32 v198,0x3e0293ee,v198
v_mul_f32_e32 v199,0x3e0293ee,v199
cv 192,192,193
cv 193,194,195
cv 194,196,197
cv 195,198,199
v_accvgpr_write_b32 a232,v192
v_accvgpr_write_b32 a233,v193
v_accvgpr_write_b32 a234,v194
v_accvgpr_write_b32 a235,v195
v_accvgpr_read_b32 v192,a216
v_accvgpr_read_b32 v193,a217
v_accvgpr_read_b32 v194,a218
v_accvgpr_read_b32 v195,a219
v_accvgpr_read_b32 v196,a220
v_accvgpr_read_b32 v197,a221
v_accvgpr_read_b32 v198,a222
v_accvgpr_read_b32 v199,a223
v_mul_f32_e32 v192,0x3e0293ee,v192
v_mul_f32_e32 v193,0x3e0293ee,v193
v_mul_f32_e32 v194,0x3e0293ee,v194
v_mul_f32_e32 v195,0x3e0293ee,v195
v_mul_f32_e32 v196,0x3e0293ee,v196
v_mul_f32_e32 v197,0x3e0293ee,v197
v_mul_f32_e32 v198,0x3e0293ee,v198
v_mul_f32_e32 v199,0x3e0293ee,v199
cv 192,192,193
cv 193,194,195
cv 194,196,197
cv 195,198,199
v_accvgpr_write_b32 a236,v192
v_accvgpr_write_b32 a237,v193
v_accvgpr_write_b32 a238,v194
v_accvgpr_write_b32 a239,v195
v_mov_b32_e32 v160,0
v_mov_b32_e32 v161,0
v_mov_b32_e32 v162,0
v_mov_b32_e32 v163,0
v_mov_b32_e32 v164,0
v_mov_b32_e32 v165,0
v_mov_b32_e32 v166,0
v_mov_b32_e32 v167,0
v_mov_b32_e32 v168,0
v_mov_b32_e32 v169,0
v_mov_b32_e32 v170,0
v_mov_b32_e32 v171,0
v_mov_b32_e32 v172,0
v_mov_b32_e32 v173,0
v_mov_b32_e32 v174,0
v_mov_b32_e32 v175,0
v_mov_b32_e32 v176,0
v_mov_b32_e32 v177,0
v_mov_b32_e32 v178,0
v_mov_b32_e32 v179,0
v_mov_b32_e32 v180,0
v_mov_b32_e32 v181,0
v_mov_b32_e32 v182,0
v_mov_b32_e32 v183,0
v_mov_b32_e32 v184,0
v_mov_b32_e32 v185,0
v_mov_b32_e32 v186,0
v_mov_b32_e32 v187,0
v_mov_b32_e32 v188,0
v_mov_b32_e32 v189,0
v_mov_b32_e32 v190,0
v_mov_b32_e32 v191,0
v_mov_b32_e32 v216,0xf149f2ca
v_mov_b32_e32 v211,v216
v_mov_b32_e32 v212,0
s_lshr_b32 s91,s88,5
s_mov_b32 s87,0
L_rd_a:
s_nop 15
s_nop 3
s_mul_i32 s89,s18,524288
s_lshl_b32 s90,s87,13
s_add_u32 s89,s89,s90
s_add_u32 s90,s89,16777216
la 64,244,90,0
la 68,244,90,1024
la 72,244,90,2048
la 76,244,90,3072
s_add_u32 s90,s89,16781312
la 80,244,90,0
la 84,244,90,1024
la 88,244,90,2048
la 92,244,90,3072
s_add_u32 s90,s89,33554432
la 96,244,90,0
la 100,244,90,1024
la 104,244,90,2048
la 108,244,90,3072
s_add_u32 s90,s89,33558528
la 112,244,90,0
la 116,244,90,1024
la 120,244,90,2048
la 124,244,90,3072
s_waitcnt vmcnt(0)
zvaa 192,64,224
mvaa 192,72,228
mvaa 192,80,232
mvaa 192,88,236
zvaa 196,68,224
mvaa 196,76,228
mvaa 196,84,232
mvaa 196,92,236
v_cmp_gt_i32_e32 vcc,0,v210
s_nop 2
v_cndmask_b32_e32 v192,v192,v216,vcc
v_cmp_gt_i32_e32 vcc,1,v210
s_nop 1
v_cndmask_b32_e32 v193,v193,v216,vcc
v_cmp_gt_i32_e32 vcc,2,v210
s_nop 1
v_cndmask_b32_e32 v194,v194,v216,vcc
v_cmp_gt_i32_e32 vcc,3,v210
s_nop 1
v_cndmask_b32_e32 v195,v195,v216,vcc
v_cmp_gt_i32_e32 vcc,16,v210
s_nop 1
v_cndmask_b32_e32 v196,v196,v216,vcc
v_cmp_gt_i32_e32 vcc,17,v210
s_nop 1
v_cndmask_b32_e32 v197,v197,v216,vcc
v_cmp_gt_i32_e32 vcc,18,v210
s_nop 1
v_cndmask_b32_e32 v198,v198,v216,vcc
v_cmp_gt_i32_e32 vcc,19,v210
s_nop 1
v_cndmask_b32_e32 v199,v199,v216,vcc
v_max3_f32 v213,v192,v193,v194
v_max3_f32 v214,v195,v196,v197
v_max3_f32 v213,v213,v198,v199
v_max_f32_e32 v213,v213,v214
v_mov_b32_e32 v214,v213
s_nop 1
v_permlane16_swap_b32_e32 v213,v214
v_max_f32_e32 v213,v213,v214
v_mov_b32_e32 v214,v213
s_nop 1
v_permlane32_swap_b32_e32 v213,v214
v_max_f32_e32 v213,v213,v214
v_max_f32_e32 v213,v213,v211
v_sub_f32_e32 v215,v211,v213
ex 215
v_mov_b32_e32 v211,v213
v_sub_f32_e32 v192,v192,v211
v_sub_f32_e32 v193,v193,v211
v_sub_f32_e32 v194,v194,v211
v_sub_f32_e32 v195,v195,v211
v_sub_f32_e32 v196,v196,v211
v_sub_f32_e32 v197,v197,v211
v_sub_f32_e32 v198,v198,v211
v_sub_f32_e32 v199,v199,v211
ex 192
ex 193
ex 194
ex 195
ex 196
ex 197
ex 198
ex 199
v_mul_f32_e32 v212,v215,v212
v_add_f32_e32 v212,v212,v192
v_add_f32_e32 v212,v212,v193
v_add_f32_e32 v212,v212,v194
v_add_f32_e32 v212,v212,v195
v_add_f32_e32 v212,v212,v196
v_add_f32_e32 v212,v212,v197
v_add_f32_e32 v212,v212,v198
v_add_f32_e32 v212,v212,v199
cv 192,192,193
cv 193,194,195
cv 194,196,197
cv 195,198,199
v_mul_f32_e32 v160,v215,v160
v_mul_f32_e32 v161,v215,v161
v_mul_f32_e32 v162,v215,v162
v_mul_f32_e32 v163,v215,v163
v_mul_f32_e32 v164,v215,v164
v_mul_f32_e32 v165,v215,v165
v_mul_f32_e32 v166,v215,v166
v_mul_f32_e32 v167,v215,v167
v_mul_f32_e32 v168,v215,v168
v_mul_f32_e32 v169,v215,v169
v_mul_f32_e32 v170,v215,v170
v_mul_f32_e32 v171,v215,v171
v_mul_f32_e32 v172,v215,v172
v_mul_f32_e32 v173,v215,v173
v_mul_f32_e32 v174,v215,v174
v_mul_f32_e32 v175,v215,v175
v_mul_f32_e32 v176,v215,v176
v_mul_f32_e32 v177,v215,v177
v_mul_f32_e32 v178,v215,v178
v_mul_f32_e32 v179,v215,v179
v_mul_f32_e32 v180,v215,v180
v_mul_f32_e32 v181,v215,v181
v_mul_f32_e32 v182,v215,v182
v_mul_f32_e32 v183,v215,v183
v_mul_f32_e32 v184,v215,v184
v_mul_f32_e32 v185,v215,v185
v_mul_f32_e32 v186,v215,v186
v_mul_f32_e32 v187,v215,v187
v_mul_f32_e32 v188,v215,v188
v_mul_f32_e32 v189,v215,v189
v_mul_f32_e32 v190,v215,v190
v_mul_f32_e32 v191,v215,v191
mvav 160,96,192
mvav 164,100,192
mvav 168,104,192
mvav 172,108,192
mvav 176,112,192
mvav 180,116,192
mvav 184,120,192
mvav 188,124,192
v_subrev_u32_e32 v210,32,v210
s_add_u32 s87,s87,1
s_cmp_le_u32 s87,s91
s_cbranch_scc1 L_rd_a
s_nop 15
s_nop 3
s_lshl_b32 s89,s88,2
s_add_u32 s89,s89,83886080
v_bfe_u32 v213,v244,2,6
buffer_load_dword v215,v213,s[8:11],s89 offen sc1
v_mov_b32_e32 v214,v212
s_nop 1
v_permlane16_swap_b32_e32 v212,v214
v_add_f32_e32 v212,v212,v214
v_mov_b32_e32 v214,v212
s_nop 1
v_permlane32_swap_b32_e32 v212,v214
v_add_f32_e32 v212,v212,v214
v_rcp_f32_e32 v212,v212
s_waitcnt vmcnt(0)
v_mul_f32_e32 v215,v215,v212
v_mul_f32_e32 v160,v215,v160
v_mul_f32_e32 v161,v215,v161
v_mul_f32_e32 v162,v215,v162
v_mul_f32_e32 v163,v215,v163
v_mul_f32_e32 v164,v215,v164
v_mul_f32_e32 v165,v215,v165
v_mul_f32_e32 v166,v215,v166
v_mul_f32_e32 v167,v215,v167
v_mul_f32_e32 v168,v215,v168
v_mul_f32_e32 v169,v215,v169
v_mul_f32_e32 v170,v215,v170
v_mul_f32_e32 v171,v215,v171
v_mul_f32_e32 v172,v215,v172
v_mul_f32_e32 v173,v215,v173
v_mul_f32_e32 v174,v215,v174
v_mul_f32_e32 v175,v215,v175
v_mul_f32_e32 v176,v215,v176
v_mul_f32_e32 v177,v215,v177
v_mul_f32_e32 v178,v215,v178
v_mul_f32_e32 v179,v215,v179
v_mul_f32_e32 v180,v215,v180
v_mul_f32_e32 v181,v215,v181
v_mul_f32_e32 v182,v215,v182
v_mul_f32_e32 v183,v215,v183
v_mul_f32_e32 v184,v215,v184
v_mul_f32_e32 v185,v215,v185
v_mul_f32_e32 v186,v215,v186
v_mul_f32_e32 v187,v215,v187
v_mul_f32_e32 v188,v215,v188
v_mul_f32_e32 v189,v215,v189
v_mul_f32_e32 v190,v215,v190
v_mul_f32_e32 v191,v215,v191
s_add_u32 s89,s95,4096
cv 160,160,161
cv 161,162,163
cv 162,164,165
cv 163,166,167
buffer_store_dwordx4 v[160:163],v244,s[8:11],s95 offen offset:0 sc0 sc1
cv 168,168,169
cv 169,170,171
cv 170,172,173
cv 171,174,175
buffer_store_dwordx4 v[168:171],v244,s[8:11],s95 offen offset:2048 sc0 sc1
cv 176,176,177
cv 177,178,179
cv 178,180,181
cv 179,182,183
buffer_store_dwordx4 v[176:179],v244,s[8:11],s89 offen offset:0 sc0 sc1
cv 184,184,185
cv 185,186,187
cv 186,188,189
cv 187,190,191
buffer_store_dwordx4 v[184:187],v244,s[8:11],s89 offen offset:2048 sc0 sc1
s_waitcnt vmcnt(0)
v_mov_b32_e32 v192,v160
v_accvgpr_read_b32 v160,a240
v_accvgpr_write_b32 a240,v192
v_mov_b32_e32 v193,v161
v_accvgpr_read_b32 v161,a241
v_accvgpr_write_b32 a241,v193
v_mov_b32_e32 v194,v162
v_accvgpr_read_b32 v162,a242
v_accvgpr_write_b32 a242,v194
v_mov_b32_e32 v195,v163
v_accvgpr_read_b32 v163,a243
v_accvgpr_write_b32 a243,v195
v_mov_b32_e32 v200,v168
v_accvgpr_read_b32 v168,a244
v_accvgpr_write_b32 a244,v200
v_mov_b32_e32 v201,v169
v_accvgpr_read_b32 v169,a245
v_accvgpr_write_b32 a245,v201
v_mov_b32_e32 v202,v170
v_accvgpr_read_b32 v170,a246
v_accvgpr_write_b32 a246,v202
v_mov_b32_e32 v203,v171
v_accvgpr_read_b32 v171,a247
v_accvgpr_write_b32 a247,v203
v_mov_b32_e32 v208,v176
v_accvgpr_read_b32 v176,a248
v_accvgpr_write_b32 a248,v208
v_mov_b32_e32 v209,v177
v_accvgpr_read_b32 v177,a249
v_accvgpr_write_b32 a249,v209
v_mov_b32_e32 v210,v178
v_accvgpr_read_b32 v178,a250
v_accvgpr_write_b32 a250,v210
v_mov_b32_e32 v211,v179
v_accvgpr_read_b32 v179,a251
v_accvgpr_write_b32 a251,v211
v_mov_b32_e32 v216,v184
v_accvgpr_read_b32 v184,a252
v_accvgpr_write_b32 a252,v216
v_mov_b32_e32 v217,v185
v_accvgpr_read_b32 v185,a253
v_accvgpr_write_b32 a253,v217
v_mov_b32_e32 v218,v186
v_accvgpr_read_b32 v186,a254
v_accvgpr_write_b32 a254,v218
v_mov_b32_e32 v219,v187
v_accvgpr_read_b32 v187,a255
v_accvgpr_write_b32 a255,v219
s_add_u32 s86,s86,1
s_cmp_lt_u32 s86,4
s_cbranch_scc1 L_rd_t
s_nop 15
s_nop 3
s_nop 1
s_cmp_eq_u32 s84,4
s_cbranch_scc1 L_redo_r4
s_cmp_lg_u32 s80,s81
s_branch L_redo_r5
L_k_ev:
L_v_ev:
buffer_load_dwordx4 a[252:255],v244,s[8:11],s35 offen sc1
s_branch L_k_evr
L_spbe:
s_mov_b32 s91,65536
L_splbe:
s_sleep 2
buffer_load_dword v253,v224,s[8:11],s53 offen sc1
s_waitcnt vmcnt(0)
v_cmp_ne_u32_e32 vcc,4,v253
s_nop 1
s_cbranch_vccz L_sprbe
s_sub_u32 s91,s91,1
s_cbranch_scc0 L_splbe
s_branch L_sprbe
L_spa3e:
s_mov_b32 s91,65536
L_spla3e:
s_sleep 2
buffer_load_dword v253,v242,s[8:11],s53 offen sc1
s_waitcnt vmcnt(0)
v_cmp_ne_u32_e32 vcc,4,v253
s_nop 1
s_cbranch_vccz L_spra3e
s_sub_u32 s91,s91,1
s_cbranch_scc0 L_spla3e
s_branch L_spra3e
L_spa4e:
s_mov_b32 s91,65536
L_spla4e:
s_sleep 2
buffer_load_dword v253,v242,s[8:11],s53 offen sc1
s_waitcnt vmcnt(0)
v_cmp_ne_u32_e32 vcc,4,v253
s_nop 1
s_cbranch_vccz L_spra4e
s_sub_u32 s91,s91,1
s_cbranch_scc0 L_spla4e
s_branch L_spra4e
L_spa5e:
s_mov_b32 s91,65536
L_spla5e:
s_sleep 2
buffer_load_dword v253,v242,s[8:11],s53 offen sc1
s_waitcnt vmcnt(0)
v_cmp_ne_u32_e32 vcc,4,v253
s_nop 1
s_cbranch_vccz L_spra5e
s_sub_u32 s91,s91,1
s_cbranch_scc0 L_spla5e
s_branch L_spra5e
L_spa6e:
s_mov_b32 s91,65536
L_spla6e:
s_sleep 2
buffer_load_dword v253,v242,s[8:11],s53 offen sc1
s_waitcnt vmcnt(0)
v_cmp_ne_u32_e32 vcc,4,v253
s_nop 1
s_cbranch_vccz L_spra6e
s_sub_u32 s91,s91,1
s_cbranch_scc0 L_spla6e
s_branch L_spra6e
L_spa7e:
s_mov_b32 s91,65536
L_spla7e:
s_sleep 2
buffer_load_dword v253,v242,s[8:11],s53 offen sc1
s_waitcnt vmcnt(0)
v_cmp_ne_u32_e32 vcc,4,v253
s_nop 1
s_cbranch_vccz L_spra7e
s_sub_u32 s91,s91,1
s_cbranch_scc0 L_spla7e
s_branch L_spra7e
L_spa8e:
s_mov_b32 s91,65536
L_spla8e:
s_sleep 2
buffer_load_dword v253,v242,s[8:11],s53 offen sc1
s_waitcnt vmcnt(0)
v_cmp_ne_u32_e32 vcc,4,v253
s_nop 1
s_cbranch_vccz L_spra8e
s_sub_u32 s91,s91,1
s_cbranch_scc0 L_spla8e
s_branch L_spra8e
L_spa9e:
s_mov_b32 s91,65536
L_spla9e:
s_sleep 2
buffer_load_dword v253,v242,s[8:11],s53 offen sc1
s_waitcnt vmcnt(0)
v_cmp_ne_u32_e32 vcc,4,v253
s_nop 1
s_cbranch_vccz L_spra9e
s_sub_u32 s91,s91,1
s_cbranch_scc0 L_spla9e
s_branch L_spra9e
L_spa10e:
s_mov_b32 s91,65536
L_spla10e:
s_sleep 2
buffer_load_dword v253,v242,s[8:11],s53 offen sc1
s_waitcnt vmcnt(0)
v_cmp_ne_u32_e32 vcc,4,v253
s_nop 1
s_cbranch_vccz L_spra10e
s_sub_u32 s91,s91,1
s_cbranch_scc0 L_spla10e
s_branch L_spra10e
L_sppe:
s_mov_b32 s91,65536
L_splpe:
s_sleep 2
buffer_load_dword v253,v242,s[8:11],s53 offen sc1
s_waitcnt vmcnt(0)
v_cmp_ne_u32_e32 vcc,4,v253
s_nop 1
s_cbranch_vccz L_sprpe
s_sub_u32 s91,s91,1
s_cbranch_scc0 L_splpe
s_branch L_sprpe
L_spx0e:
s_mov_b32 s91,65536
L_splx0e:
lv 16,244,82,0
lv 20,244,82,1024
lv 24,244,82,2048
lv 28,244,82,3072
s_waitcnt vmcnt(0)
v_max_u32_e32 v248,v16,v17
v_max3_u32 v248,v248,v18,v19
v_max3_u32 v248,v248,v20,v21
v_max3_u32 v248,v248,v22,v23
v_max3_u32 v248,v248,v24,v25
v_max3_u32 v248,v248,v26,v27
v_max3_u32 v248,v248,v28,v29
v_max3_u32 v248,v248,v30,v31
v_cmp_eq_u32_e32 vcc,-1,v248
s_nop 1
s_cbranch_vccz L_sprx0e
s_sub_u32 s91,s91,1
s_cbranch_scc0 L_splx0e
s_branch L_sprx0e
L_spx1e:
s_mov_b32 s91,65536
L_splx1e:
lv 48,244,83,0
lv 52,244,83,1024
lv 56,244,83,2048
lv 60,244,83,3072
s_waitcnt vmcnt(0)
v_max_u32_e32 v248,v48,v49
v_max3_u32 v248,v248,v50,v51
v_max3_u32 v248,v248,v52,v53
v_max3_u32 v248,v248,v54,v55
v_max3_u32 v248,v248,v56,v57
v_max3_u32 v248,v248,v58,v59
v_max3_u32 v248,v248,v60,v61
v_max3_u32 v248,v248,v62,v63
v_cmp_eq_u32_e32 vcc,-1,v248
s_nop 1
s_cbranch_vccz L_sprx1e
s_sub_u32 s91,s91,1
s_cbranch_scc0 L_splx1e
s_branch L_sprx1e
L_spx2e:
s_mov_b32 s91,65536
L_splx2e:
lv 80,244,84,0
lv 84,244,84,1024
lv 88,244,84,2048
lv 92,244,84,3072
s_waitcnt vmcnt(0)
v_max_u32_e32 v248,v80,v81
v_max3_u32 v248,v248,v82,v83
v_max3_u32 v248,v248,v84,v85
v_max3_u32 v248,v248,v86,v87
v_max3_u32 v248,v248,v88,v89
v_max3_u32 v248,v248,v90,v91
v_max3_u32 v248,v248,v92,v93
v_max3_u32 v248,v248,v94,v95
v_cmp_eq_u32_e32 vcc,-1,v248
s_nop 1
s_cbranch_vccz L_sprx2e
s_sub_u32 s91,s91,1
s_cbranch_scc0 L_splx2e
s_branch L_sprx2e
L_spbo:
s_mov_b32 s91,65536
L_splbo:
s_sleep 2
buffer_load_dword v253,v224,s[8:11],s53 offen sc1
s_waitcnt vmcnt(0)
v_cmp_ne_u32_e32 vcc,4,v253
s_nop 1
s_cbranch_vccz L_sprbo
s_sub_u32 s91,s91,1
s_cbranch_scc0 L_splbo
s_branch L_sprbo
L_spa3o:
s_mov_b32 s91,65536
L_spla3o:
s_sleep 2
buffer_load_dword v253,v242,s[8:11],s53 offen sc1
s_waitcnt vmcnt(0)
v_cmp_ne_u32_e32 vcc,4,v253
s_nop 1
s_cbranch_vccz L_spra3o
s_sub_u32 s91,s91,1
s_cbranch_scc0 L_spla3o
s_branch L_spra3o
L_spa4o:
s_mov_b32 s91,65536
L_spla4o:
s_sleep 2
buffer_load_dword v253,v242,s[8:11],s53 offen sc1
s_waitcnt vmcnt(0)
v_cmp_ne_u32_e32 vcc,4,v253
s_nop 1
s_cbranch_vccz L_spra4o
s_sub_u32 s91,s91,1
s_cbranch_scc0 L_spla4o
s_branch L_spra4o
L_spa5o:
s_mov_b32 s91,65536
L_spla5o:
s_sleep 2
buffer_load_dword v253,v242,s[8:11],s53 offen sc1
s_waitcnt vmcnt(0)
v_cmp_ne_u32_e32 vcc,4,v253
s_nop 1
s_cbranch_vccz L_spra5o
s_sub_u32 s91,s91,1
s_cbranch_scc0 L_spla5o
s_branch L_spra5o
L_spa6o:
s_mov_b32 s91,65536
L_spla6o:
s_sleep 2
buffer_load_dword v253,v242,s[8:11],s53 offen sc1
s_waitcnt vmcnt(0)
v_cmp_ne_u32_e32 vcc,4,v253
s_nop 1
s_cbranch_vccz L_spra6o
s_sub_u32 s91,s91,1
s_cbranch_scc0 L_spla6o
s_branch L_spra6o
L_spa7o:
s_mov_b32 s91,65536
L_spla7o:
s_sleep 2
buffer_load_dword v253,v242,s[8:11],s53 offen sc1
s_waitcnt vmcnt(0)
v_cmp_ne_u32_e32 vcc,4,v253
s_nop 1
s_cbranch_vccz L_spra7o
s_sub_u32 s91,s91,1
s_cbranch_scc0 L_spla7o
s_branch L_spra7o
L_spa8o:
s_mov_b32 s91,65536
L_spla8o:
s_sleep 2
buffer_load_dword v253,v242,s[8:11],s53 offen sc1
s_waitcnt vmcnt(0)
v_cmp_ne_u32_e32 vcc,4,v253
s_nop 1
s_cbranch_vccz L_spra8o
s_sub_u32 s91,s91,1
s_cbranch_scc0 L_spla8o
s_branch L_spra8o
L_sppo:
s_mov_b32 s91,65536
L_splpo:
s_sleep 2
buffer_load_dword v253,v242,s[8:11],s53 offen sc1
s_waitcnt vmcnt(0)
v_cmp_ne_u32_e32 vcc,4,v253
s_nop 1
s_cbranch_vccz L_sprpo
s_sub_u32 s91,s91,1
s_cbranch_scc0 L_splpo
s_branch L_sprpo
L_spx0o:
s_mov_b32 s91,65536
L_splx0o:
lv 16,244,82,0
lv 20,244,82,1024
lv 24,244,82,2048
lv 28,244,82,3072
s_waitcnt vmcnt(0)
v_max_u32_e32 v248,v16,v17
v_max3_u32 v248,v248,v18,v19
v_max3_u32 v248,v248,v20,v21
v_max3_u32 v248,v248,v22,v23
v_max3_u32 v248,v248,v24,v25
v_max3_u32 v248,v248,v26,v27
v_max3_u32 v248,v248,v28,v29
v_max3_u32 v248,v248,v30,v31
v_cmp_eq_u32_e32 vcc,-1,v248
s_nop 1
s_cbranch_vccz L_sprx0o
s_sub_u32 s91,s91,1
s_cbranch_scc0 L_splx0o
s_branch L_sprx0o
L_spx1o:
s_mov_b32 s91,65536
L_splx1o:
lv 48,244,83,0
lv 52,244,83,1024
lv 56,244,83,2048
lv 60,244,83,3072
s_waitcnt vmcnt(0)
v_max_u32_e32 v248,v48,v49
v_max3_u32 v248,v248,v50,v51
v_max3_u32 v248,v248,v52,v53
v_max3_u32 v248,v248,v54,v55
v_max3_u32 v248,v248,v56,v57
v_max3_u32 v248,v248,v58,v59
v_max3_u32 v248,v248,v60,v61
v_max3_u32 v248,v248,v62,v63
v_cmp_eq_u32_e32 vcc,-1,v248
s_nop 1
s_cbranch_vccz L_sprx1o
s_sub_u32 s91,s91,1
s_cbranch_scc0 L_splx1o
s_branch L_sprx1o
L_spx2o:
s_mov_b32 s91,65536
L_splx2o:
lv 80,244,84,0
lv 84,244,84,1024
lv 88,244,84,2048
lv 92,244,84,3072
s_waitcnt vmcnt(0)
v_max_u32_e32 v248,v80,v81
v_max3_u32 v248,v248,v82,v83
v_max3_u32 v248,v248,v84,v85
v_max3_u32 v248,v248,v86,v87
v_max3_u32 v248,v248,v88,v89
v_max3_u32 v248,v248,v90,v91
v_max3_u32 v248,v248,v92,v93
v_max3_u32 v248,v248,v94,v95
v_cmp_eq_u32_e32 vcc,-1,v248
s_nop 1
s_cbranch_vccz L_sprx2o
s_sub_u32 s91,s91,1
s_cbranch_scc0 L_splx2o
s_branch L_sprx2o
L_att_end:
s_endpgm
.p2align 12
.purgem cv
.purgem cvaa
.purgem cvvv
.purgem dl
.purgem ds
.purgem ex
.purgem la
.purgem lv
.purgem maaa
.purgem maav
.purgem mavv
.purgem mvaa
.purgem mvav
.purgem mvva
.purgem mvvv
.purgem ra
.purgem rv
.purgem ta
.purgem tv
.purgem zaaa
.purgem zaav
.purgem zvaa
.purgem zvav
.purgem zvva
.purgem zvvv
)"
        :: "s"(__builtin_amdgcn_kernarg_segment_ptr()), "s"(blockIdx.x), "v"(tid)
        : "v0", "v1", "v2", "v3", "v4", "v5", "v6", "v7", "v8", "v9", "v10", "v11", "v12", "v13", "v14", "v15", "v16", "v17", "v18", "v19", "v20", "v21", "v22", "v23", "v24", "v25", "v26", "v27", "v28", "v29", "v30", "v31", "v32", "v33", "v34", "v35", "v36", "v37", "v38", "v39", "v40", "v41", "v42", "v43", "v44", "v45", "v46", "v47", "v48", "v49", "v50", "v51", "v52", "v53", "v54", "v55", "v56", "v57", "v58", "v59", "v60", "v61", "v62", "v63", "v64", "v65", "v66", "v67", "v68", "v69", "v70", "v71", "v72", "v73", "v74", "v75", "v76", "v77", "v78", "v79", "v80", "v81", "v82", "v83", "v84", "v85", "v86", "v87", "v88", "v89", "v90", "v91", "v92", "v93", "v94", "v95", "v96", "v97", "v98", "v99", "v100", "v101", "v102", "v103", "v104", "v105", "v106", "v107", "v108", "v109", "v110", "v111", "v112", "v113", "v114", "v115", "v116", "v117", "v118", "v119", "v120", "v121", "v122", "v123", "v124", "v125", "v126", "v127", "v128", "v129", "v130", "v131", "v132", "v133", "v134", "v135", "v136", "v137", "v138", "v139", "v140", "v141", "v142", "v143", "v144", "v145", "v146", "v147", "v148", "v149", "v150", "v151", "v152", "v153", "v154", "v155", "v156", "v157", "v158", "v159", "v160", "v161", "v162", "v163", "v164", "v165", "v166", "v167", "v168", "v169", "v170", "v171", "v172", "v173", "v174", "v175", "v176", "v177", "v178", "v179", "v180", "v181", "v182", "v183", "v184", "v185", "v186", "v187", "v188", "v189", "v190", "v191", "v192", "v193", "v194", "v195", "v196", "v197", "v198", "v199", "v200", "v201", "v202", "v203", "v204", "v205", "v206", "v207", "v208", "v209", "v210", "v211", "v212", "v213", "v214", "v215", "v216", "v217", "v218", "v219", "v220", "v221", "v222", "v223", "v224", "v225", "v226", "v227", "v228", "v229", "v230", "v231", "v232", "v233", "v234", "v235", "v236", "v237", "v238", "v239", "v240", "v241", "v242", "v243", "v244", "v245", "v246", "v247", "v248", "v249", "v250", "v251", "v252", "v253", "v254", "a0", "a1", "a2", "a3", "a4", "a5", "a6", "a7", "a8", "a9", "a10", "a11", "a12", "a13", "a14", "a15", "a16", "a17", "a18", "a19", "a20", "a21", "a22", "a23", "a24", "a25", "a26", "a27", "a28", "a29", "a30", "a31", "a32", "a33", "a34", "a35", "a36", "a37", "a38", "a39", "a40", "a41", "a42", "a43", "a44", "a45", "a46", "a47", "a48", "a49", "a50", "a51", "a52", "a53", "a54", "a55", "a56", "a57", "a58", "a59", "a60", "a61", "a62", "a63", "a64", "a65", "a66", "a67", "a68", "a69", "a70", "a71", "a72", "a73", "a74", "a75", "a76", "a77", "a78", "a79", "a80", "a81", "a82", "a83", "a84", "a85", "a86", "a87", "a88", "a89", "a90", "a91", "a92", "a93", "a94", "a95", "a96", "a97", "a98", "a99", "a100", "a101", "a102", "a103", "a104", "a105", "a106", "a107", "a108", "a109", "a110", "a111", "a112", "a113", "a114", "a115", "a116", "a117", "a118", "a119", "a120", "a121", "a122", "a123", "a124", "a125", "a126", "a127", "a128", "a129", "a130", "a131", "a132", "a133", "a134", "a135", "a136", "a137", "a138", "a139", "a140", "a141", "a142", "a143", "a144", "a145", "a146", "a147", "a148", "a149", "a150", "a151", "a152", "a153", "a154", "a155", "a156", "a157", "a158", "a159", "a160", "a161", "a162", "a163", "a164", "a165", "a166", "a167", "a168", "a169", "a170", "a171", "a172", "a173", "a174", "a175", "a176", "a177", "a178", "a179", "a180", "a181", "a182", "a183", "a184", "a185", "a186", "a187", "a188", "a189", "a190", "a191", "a192", "a193", "a194", "a195", "a196", "a197", "a198", "a199", "a200", "a201", "a202", "a203", "a204", "a205", "a206", "a207", "a208", "a209", "a210", "a211", "a212", "a213", "a214", "a215", "a216", "a217", "a218", "a219", "a220", "a221", "a222", "a223", "a224", "a225", "a226", "a227", "a228", "a229", "a230", "a231", "a232", "a233", "a234", "a235", "a236", "a237", "a238", "a239", "a240", "a241", "a242", "a243", "a244", "a245", "a246", "a247", "a248", "a249", "a250", "a251", "a252", "a253", "a254", "a255", "s4", "s5", "s6", "s7", "s8", "s9", "s10", "s11", "s12", "s13", "s14", "s15", "s16", "s17", "s18", "s19", "s20", "s21", "s22", "s23", "s24", "s25", "s26", "s27", "s28", "s29", "s30", "s31", "s32", "s33", "s34", "s35", "s36", "s37", "s38", "s39", "s40", "s41", "s42", "s43", "s44", "s45", "s46", "s47", "s48", "s49", "s50", "s51", "s52", "s53", "s54", "s55", "s56", "s57", "s58", "s59", "s60", "s61", "s62", "s63", "s64", "s65", "s66", "s67", "s68", "s69", "s70", "s71", "s72", "s73", "s74", "s75", "s76", "s77", "s78", "s79", "s80", "s81", "s82", "s83", "s84", "s85", "s86", "s87", "s88", "s89", "s90", "s91", "s92", "s93", "s94", "s95", "s96", "s97", "s98", "s99", "s100", "s101", "vcc", "scc", "m0", "memory");
}

extern "C" void kernel_launch(void* const* d_in, const int* in_sizes, int n_in, void* d_out, int out_size, void* d_ws, size_t ws_size, hipStream_t stream) {
    (void)in_sizes; (void)n_in; (void)out_size; (void)ws_size;
    char* ws = (char*)d_ws + 0ull;
    Args a;
    a.p[0] = d_in[0]; a.p[1] = d_in[1]; a.p[2] = d_in[2]; a.p[3] = d_in[3]; a.p[4] = d_in[4]; a.p[5] = d_in[5]; a.p[6] = d_in[7]; a.p[7] = d_in[8]; a.p[8] = d_in[9]; a.p[9] = d_out; a.p[10] = ws;
    mega<<<256, 256, 0, stream>>>(a);
}
```

```cpp
#include <hip/hip_runtime.h>
#include <hip/hip_bf16.h>
#include <stdint.h>
#include <type_traits>

#define LAS __attribute__((address_space(3)))
struct Args { const void* p[11]; };
__global__ __launch_bounds__(256, 1)
void mega(Args args) {
    __shared__ __attribute__((aligned(1024))) char smem[163840];
    const unsigned tid = threadIdx.x + (unsigned)(uintptr_t)(LAS char*)smem;
    asm volatile(
R"(.macro cv a,b,c
v_cvt_pk_bf16_f32 v\a,v\b,v\c
.endm
.macro cvaa a,b,c,d
v_mfma_f32_16x16x32_bf16 v[\a:\a+3],a[\b:\b+3],a[\c:\c+3],v[\d:\d+3]
.endm
.macro cvvv a,b,c,d
v_mfma_f32_16x16x32_bf16 v[\a:\a+3],v[\b:\b+3],v[\c:\c+3],v[\d:\d+3]
.endm
.macro dl a,b,c,d
buffer_load_dwordx4 v\a,s[\b:\b+3],s\c offen offset:\d lds
.endm
.macro ds a,b,c,d
buffer_load_dwordx4 v\a,s[\b:\b+3],s\c offen offset:\d sc1 lds
.endm
.macro ex a
v_exp_f32_e32 v\a,v\a
.endm
.macro la a,b,c,d
buffer_load_dwordx4 a[\a:\a+3],v\b,s[8:11],s\c offen offset:\d sc1
.endm
.macro lv a,b,c,d
buffer_load_dwordx4 v[\a:\a+3],v\b,s[8:11],s\c offen offset:\d sc1
.endm
.macro maaa a,b,c
v_mfma_f32_16x16x32_bf16 a[\a:\a+3],a[\b:\b+3],a[\c:\c+3],a[\a:\a+3]
.endm
.macro maav a,b,c
v_mfma_f32_16x16x32_bf16 a[\a:\a+3],a[\b:\b+3],v[\c:\c+3],a[\a:\a+3]
.endm
.macro mavv a,b,c
v_mfma_f32_16x16x32_bf16 a[\a:\a+3],v[\b:\b+3],v[\c:\c+3],a[\a:\a+3]
.endm
.macro mvaa a,b,c
v_mfma_f32_16x16x32_bf16 v[\a:\a+3],a[\b:\b+3],a[\c:\c+3],v[\a:\a+3]
.endm
.macro mvav a,b,c
v_mfma_f32_16x16x32_bf16 v[\a:\a+3],a[\b:\b+3],v[\c:\c+3],v[\a:\a+3]
.endm
.macro mvva a,b,c
v_mfma_f32_16x16x32_bf16 v[\a:\a+3],v[\b:\b+3],a[\c:\c+3],v[\a:\a+3]
.endm
.macro mvvv a,b,c
v_mfma_f32_16x16x32_bf16 v[\a:\a+3],v[\b:\b+3],v[\c:\c+3],v[\a:\a+3]
.endm
.macro ra a,b,c
ds_read_b128 a[\a:\a+3],v\b offset:\c
.endm
.macro rv a,b,c
ds_read_b128 v[\a:\a+3],v\b offset:\c
.endm
.macro ta a,b,c
ds_read_b64_tr_b16 a[\a:\a+1],v\b offset:\c
.endm
.macro tv a,b,c
ds_read_b64_tr_b16 v[\a:\a+1],v\b offset:\c
.endm
.macro zaaa a,b,c
v_mfma_f32_16x16x32_bf16 a[\a:\a+3],a[\b:\b+3],a[\c:\c+3],0
.endm
.macro zaav a,b,c
v_mfma_f32_16x16x32_bf16 a[\a:\a+3],a[\b:\b+3],v[\c:\c+3],0
.endm
.macro zvaa a,b,c
v_mfma_f32_16x16x32_bf16 v[\a:\a+3],a[\b:\b+3],a[\c:\c+3],0
.endm
.macro zvav a,b,c
v_mfma_f32_16x16x32_bf16 v[\a:\a+3],a[\b:\b+3],v[\c:\c+3],0
.endm
.macro zvva a,b,c
v_mfma_f32_16x16x32_bf16 v[\a:\a+3],v[\b:\b+3],a[\c:\c+3],0
.endm
.macro zvvv a,b,c
v_mfma_f32_16x16x32_bf16 v[\a:\a+3],v[\b:\b+3],v[\c:\c+3],0
.endm
L_begin:
s_load_dwordx2 s[28:29],%0,72
s_load_dwordx16 s[68:83],%0,0
s_load_dwordx2 s[84:85],%0,64
s_load_dwordx2 s[8:9],%0,80
v_lshrrev_b32_e32 v246,6,%2
v_and_b32_e32 v247,63,%2
v_lshlrev_b32_e32 v244,4,v247
v_readfirstlane_b32 s12,v246
s_and_b32 s20,%1,7
s_lshr_b32 s21,%1,3
s_getpc_b64 s[96:97]
L_att_pc:
s_lshl_b32 s86,s21,2
s_add_u32 s86,s86,s12
s_mul_i32 s86,s86,512
v_lshl_add_u32 v246,v247,7,s86
v_min_u32_e32 v246,L_att_end-L_att_pc-4,v246
s_mov_b64 exec,15
global_load_dword a188,v246,s[96:97]
s_mov_b64 exec,-1
s_and_b32 s19,s21,7
s_lshr_b32 s86,s21,3
s_lshl_b32 s18,s20,2
s_add_u32 s18,s18,s86
v_lshrrev_b32_e32 v248,3,v247
v_and_b32_e32 v250,112,v244
v_and_b32_e32 v249,48,v247
v_xor_b32_e32 v249,v249,v250
v_and_b32_e32 v251,16,v247
v_lshlrev_b32_e32 v251,1,v251
v_xor_b32_e32 v250,v250,v251
v_lshl_add_u32 v224,v248,10,v249
v_xor_b32_e32 v225,64,v224
v_add_u32_e32 v227,21504,v225
v_add_u32_e32 v226,14336,v224
v_add_u32_e32 v225,7168,v225
v_lshl_add_u32 v228,v248,13,v250
v_xor_b32_e32 v229,64,v228
v_add_u32_e32 v231,193536,v229
v_add_u32_e32 v230,129024,v228
v_add_u32_e32 v229,64512,v229
s_lshl_b32 s86,s19,2
s_add_u32 s86,s86,s12
s_lshl_b32 s52,s86,16
s_bfe_u32 s87,s21,65539
s_lshl_b32 s88,s20,1
s_add_u32 s87,s87,s88
s_lshl_b32 s87,s87,2
s_add_u32 s87,s87,s12
s_lshl_b32 s53,s87,7
s_mov_b32 s54,s53
s_mov_b32 s39,131072
s_mov_b32 s43,131072
s_mov_b32 s47,131072
s_mov_b32 s51,131072
s_mov_b32 s38,2097152
s_mov_b32 s42,4194304
s_mov_b32 s46,4194304
s_mov_b32 s56,32768
s_mov_b32 s60,-32640
s_mov_b32 s57,262144
s_mov_b32 s58,262144
s_lshl_b32 s98,s12,13
s_lshl_b32 s62,s12,12
s_add_u32 s62,s62,98304
s_lshl_b32 s35,%1,2
s_add_u32 s35,s35,83896320
v_lshrrev_b32_e32 v48,2,v247
v_and_b32_e32 v49,3,v247
v_lshlrev_b32_e32 v49,7,v49
v_lshl_add_u32 v48,v48,13,v49
s_lshl_b32 s89,s86,14
s_and_b32 s90,s53,7680
s_add_u32 s90,s90,s89
s_lshr_b32 s89,s21,3
s_lshl_b32 s89,s89,2
s_add_u32 s89,s89,s12
s_lshl_b32 s89,s89,14
s_lshl_b32 s91,s19,18
s_add_u32 s89,s89,s91
s_cmp_ge_u32 s21,16
s_waitcnt lgkmcnt(0)
s_mov_b64 s[36:37],s[70:71]
s_cselect_b64 s[40:41],s[76:77],s[74:75]
s_mov_b64 s[44:45],s[40:41]
s_bfm_b64 exec,8,0
buffer_load_dword a189,v48,s[40:43],s90 offen
s_mov_b64 exec,-1
s_and_b32 s9,s9,65535
s_mov_b32 s10,83951616
s_mov_b32 s11,131072
buffer_load_dword v44,off,s[8:11],s35 sc1
buffer_load_dword v45,off,s[8:11],s35 offset:1024 sc1
s_add_u32 m0,s62,0
s_nop 0
dl 228,40,53,0
dl 229,40,53,1024
s_add_u32 s53,s53,s57
dl 230,44,54,2048
dl 231,44,54,3072
s_add_u32 s54,s54,s57
s_add_u32 m0,s98,0
s_nop 0
dl 224,36,52,0
dl 225,36,52,1024
dl 226,36,52,2048
dl 227,36,52,3072
s_add_u32 s52,s52,s56
s_add_u32 m0,s98,4096
s_nop 0
dl 224,36,52,0
dl 225,36,52,1024
dl 226,36,52,2048
dl 227,36,52,3072
s_add_u32 s52,s52,s60
v_and_b32_e32 v246,15,v247
v_lshlrev_b32_e32 v248,3,v247
v_xor_b32_e32 v248,v248,v247
v_and_b32_e32 v248,112,v248
v_lshl_add_u32 v232,v246,7,v248
s_and_b32 s86,s98,16384
v_add_u32_e32 v232,s86,v232
v_xor_b32_e32 v233,64,v232
v_and_b32_e32 v246,12,v247
v_lshlrev_b32_e32 v246,5,v246
v_and_b32_e32 v248,3,v247
v_lshl_add_u32 v246,v248,3,v246
v_and_b32_e32 v248,24,v247
v_lshl_add_u32 v246,v248,2,v246
s_and_b32 s87,s98,8192
s_add_u32 s87,s87,98304
v_add_u32_e32 v246,s87,v246
v_and_b32_e32 v248,48,v247
v_lshl_add_u32 v234,v248,6,v246
v_xor_b32_e32 v235,32,v234
v_xor_b32_e32 v236,64,v234
v_xor_b32_e32 v237,96,v234
s_mov_b64 exec,0
s_cmp_ge_u32 s21,16
s_cbranch_scc1 L_vbV
s_cmp_eq_u32 s12,0
s_cbranch_scc1 L_vs1
s_cmp_eq_u32 s12,1
s_cbranch_scc1 L_vs2
s_cmp_eq_u32 s12,2
s_cbranch_scc1 L_vs3
s_branch L_vs8
L_vbV:
s_cmp_eq_u32 s12,0
s_cbranch_scc1 L_vs4
s_cmp_eq_u32 s12,1
s_cbranch_scc1 L_vs5
s_cmp_eq_u32 s12,2
s_cbranch_scc1 L_vs6
s_branch L_vs7
L_vr0:
s_mov_b64 exec,-1
s_add_u32 m0,s62,16384
s_nop 0
dl 228,40,53,0
dl 229,40,53,1024
s_add_u32 s53,s53,s58
dl 230,44,54,2048
dl 231,44,54,3072
s_add_u32 s54,s54,s58
s_add_u32 m0,s98,32768
s_nop 0
dl 224,36,52,0
dl 225,36,52,1024
dl 226,36,52,2048
dl 227,36,52,3072
s_add_u32 s52,s52,s56
s_waitcnt vmcnt(20)
s_cmp_lg_u32 s12,0
s_cbranch_scc1 L_rqx
v_readfirstlane_b32 s32,v44
v_readfirstlane_b32 s86,v45
s_add_u32 s32,s32,1
s_cmp_eq_u32 s32,s86
s_addc_u32 s32,s32,0
v_mov_b32_e32 v44,s32
v_mov_b32_e32 v45,0
v_lshlrev_b32_e32 v46,3,v244
v_mov_b32_e32 v47,0
s_lshl_b32 s86,%1,7
s_add_u32 s87,s86,83914752
s_and_b32 s86,s86,16383
s_add_u32 s86,s86,83898368
s_mov_b64 exec,1
buffer_store_dword v44,off,s[8:11],s35 sc0 sc1
v_mov_b32_e32 v48,0
v_mov_b32_e32 v49,0
buffer_store_dwordx4 v[46:49],off,s[8:11],s86 sc0 sc1
buffer_store_dwordx4 v[46:49],off,s[8:11],s87 sc0 sc1
s_cmp_lg_u32 %1,0
s_cbranch_scc1 L_rqy
s_mov_b64 exec,65535
s_mov_b32 s86,83894272
buffer_store_dword v45,v46,s[8:11],s86 offen sc0 sc1
L_rqy:
s_mov_b64 exec,-1
L_rqx:
s_cmp_ge_u32 s21,16
s_cbranch_scc1 L_vtile
s_waitcnt vmcnt(8)
s_barrier
ta 64,234,0
ta 66,234,512
ra 0,232,0
ta 68,235,0
ta 70,235,512
ra 4,232,2048
ra 8,232,4096
ra 12,232,6144
ra 16,232,8192
ra 20,232,10240
ra 24,232,12288
ra 28,232,14336
s_add_u32 m0,s62,32768
v_mov_b32_e32 v216,0
dl 228,40,53,0
v_mov_b32_e32 v217,0
v_mov_b32_e32 v218,0
v_mov_b32_e32 v219,0
v_mov_b32_e32 v220,0
v_mov_b32_e32 v221,0
v_mov_b32_e32 v222,0
v_mov_b32_e32 v223,0
v_mov_b32_e32 v212,0
v_mov_b32_e32 v213,0
v_mov_b32_e32 v214,0
v_mov_b32_e32 v215,0
v_mov_b32_e32 v208,0
v_mov_b32_e32 v209,0
v_mov_b32_e32 v210,0
v_mov_b32_e32 v211,0
v_mov_b32_e32 v152,0
s_waitcnt lgkmcnt(9)
zaaa 128,64,0
s_lshl_b32 s4,%1,1
ta 72,236,0
v_mov_b32_e32 v153,0
s_waitcnt lgkmcnt(8)
zaaa 132,68,0
s_add_u32 s4,s4,s12
ta 74,236,512
v_mov_b32_e32 v154,0
s_waitcnt lgkmcnt(8)
zaaa 140,68,4
s_mul_i32 s4,s4,1280
ta 76,237,0
ta 78,237,512
zaaa 136,64,4
dl 229,40,53,1024
s_waitcnt lgkmcnt(4)
zvaa 32,64,8
s_add_u32 s53,s53,s57
v_mov_b32_e32 v155,0
v_mov_b32_e32 v156,0
zvaa 36,68,8
s_mov_b32 s55,s4
v_mov_b32_e32 v157,0
v_mov_b32_e32 v158,0
zvaa 44,68,12
s_mul_i32 s4,%1,1792
ta 80,234,4096
v_mov_b32_e32 v159,0
zvaa 40,64,12
dl 230,44,54,2048
zvaa 96,64,16
s_lshl_b32 s88,%1,2
ta 82,234,4608
v_mov_b32_e32 v148,0
zvaa 100,68,16
s_cmp_eq_u32 s12,2
s_cselect_b32 s55,s4,s55
ta 84,235,4096
v_mov_b32_e32 v149,0
zvaa 108,68,20
s_lshl_b32 s4,%1,14
ta 86,235,4608
v_mov_b32_e32 v150,0
zvaa 104,64,20
dl 231,44,54,3072
zvaa 160,64,24
s_add_u32 m0,s98,36864
v_mov_b32_e32 v151,0
v_mov_b32_e32 v144,0
zvaa 164,68,24
s_add_u32 s54,s54,s57
v_mov_b32_e32 v145,0
v_mov_b32_e32 v146,0
zvaa 172,68,28
s_add_u32 s88,s88,s12
v_mov_b32_e32 v147,0
v_mov_b32_e32 v88,0
zvaa 168,64,28
dl 224,36,52,0
s_waitcnt lgkmcnt(4)
zvaa 184,72,28
s_add_u32 s5,s4,16384
v_mov_b32_e32 v89,0
v_mov_b32_e32 v90,0
zvaa 188,76,28
s_cmp_eq_u32 s12,3
s_cselect_b32 s55,s4,s55
ra 32,233,0
v_mov_b32_e32 v91,0
zvaa 180,76,24
s_mov_b32 s4,10
v_mov_b32_e32 v92,0
v_mov_b32_e32 v93,0
zvaa 176,72,24
dl 225,36,52,1024
zvaa 120,72,20
s_lshl_b32 s88,s88,12
ra 36,233,2048
v_mov_b32_e32 v94,0
zvaa 124,76,20
s_xor_b32 s87,s19,15
ra 40,233,4096
v_mov_b32_e32 v95,0
zvaa 116,76,16
s_cmp_eq_u32 s12,2
s_cselect_b32 s4,14,s4
v_mov_b32_e32 v84,0
v_mov_b32_e32 v85,0
zvaa 112,72,16
dl 226,36,52,2048
zvaa 56,72,12
s_waitcnt vmcnt(11)
s_barrier
zvaa 60,76,12
s_add_u32 s30,s68,s88
s_addc_u32 s31,s69,0
ta 96,234,16384
v_mov_b32_e32 v86,0
zvaa 52,76,8
s_lshl_b32 s22,s87,7
ta 98,234,16896
v_mov_b32_e32 v87,0
zvaa 48,72,8
dl 227,36,52,3072
zaaa 152,72,4
s_add_u32 m0,s62,49152
ta 100,235,16384
v_mov_b32_e32 v80,0
zaaa 156,76,4
s_add_u32 s52,s52,s60
ta 102,235,16896
v_mov_b32_e32 v81,0
zaaa 148,76,0
s_lshl_b32 s87,s87,2
ra 44,233,6144
v_mov_b32_e32 v82,0
zaaa 144,72,0
dl 228,40,53,0
s_waitcnt lgkmcnt(5)
zvaa 0,80,0
s_lshl_b32 s88,s19,2
ra 48,233,8192
v_mov_b32_e32 v83,0
zvaa 4,84,0
s_cmp_eq_u32 s12,3
s_cselect_b32 s4,10,s4
ra 52,233,10240
v_mov_b32_e32 v24,0
zvaa 12,84,4
s_add_u32 s87,s87,s12
ra 56,233,12288
v_mov_b32_e32 v25,0
zvaa 8,80,4
dl 229,40,53,1024
zvaa 64,80,8
s_add_u32 s88,s88,s12
v_mov_b32_e32 v26,0
v_mov_b32_e32 v27,0
zvaa 68,84,8
s_lshl_b32 s90,s18,13
ra 60,233,14336
v_mov_b32_e32 v28,0
zvaa 76,84,12
s_add_u32 s90,s90,62914560
ta 104,236,16384
v_mov_b32_e32 v29,0
zvaa 72,80,12
dl 230,44,54,2048
zvaa 128,80,16
s_mul_i32 s14,s87,327680
ta 106,236,16896
v_mov_b32_e32 v30,0
zvaa 132,84,16
s_mul_i32 s13,s88,327680
ta 108,237,16384
v_mov_b32_e32 v31,0
zvaa 140,84,20
s_add_u32 s14,s14,s90
ta 110,237,16896
v_mov_b32_e32 v20,0
zvaa 136,80,20
dl 231,44,54,3072
zvaa 192,80,24
s_add_u32 m0,s98,65536
v_mov_b32_e32 v21,0
v_mov_b32_e32 v22,0
zvaa 196,84,24
s_add_u32 s13,s13,s90
v_mov_b32_e32 v23,0
v_mov_b32_e32 v16,0
zvaa 204,84,28
s_mul_i32 s90,s18,524288
v_mov_b32_e32 v17,0
v_mov_b32_e32 v18,0
zvaa 200,80,28
dl 224,36,52,0
s_waitcnt lgkmcnt(4)
maaa 128,96,32
s_lshl_b32 s91,s12,12
ta 208,236,4096
v_mov_b32_e32 v19,0
maaa 132,100,32
s_add_u32 s90,s90,s91
ta 210,236,4608
v_lshlrev_b32_e32 v241,7,v247
maaa 140,100,36
s_add_u32 s90,s90,16777216
ta 216,237,4096
v_mov_b32_e32 v242,134217728
maaa 136,96,36
dl 225,36,52,1024
mvaa 32,96,40
s_cmp_eq_u32 s12,3
s_cselect_b64 s[26:27],-1,0
ta 218,237,4608
v_lshrrev_b32_e32 v248,3,v247
mvaa 36,100,40
s_lshl_b32 s86,s19,1
v_and_b32_e32 v250,112,v244
v_and_b32_e32 v251,16,v247
mvaa 44,100,44
s_mov_b64 s[48:49],s[72:73]
v_lshlrev_b32_e32 v251,1,v251
v_cmp_gt_u32_e64 s[24:25],s4,v247
mvaa 40,96,44
dl 226,36,52,2048
mvaa 96,96,48
s_mov_b32 s50,8388608
v_mul_u32_u24_e32 v246,6144,v248
v_add_u32_e32 v249,s90,v244
mvaa 100,100,48
s_mov_b32 s59,655360
ta 112,234,20480
v_cndmask_b32_e64 v241,v242,v241,s[24:25]
mvaa 108,100,52
s_cmp_eq_u32 s12,2
s_cselect_b64 s[24:25],-1,0
ta 114,234,20992
v_add_u32_e32 v255,16777216,v249
mvaa 104,96,52
dl 227,36,52,3072
mvaa 160,96,56
s_add_u32 s52,s52,s56
s_add_u32 m0,s98,69632
ta 116,235,20480
mvaa 164,100,56
s_add_u32 s86,s86,2
ta 118,235,20992
v_accvgpr_write_b32 a240,v249
mvaa 172,100,60
s_cmp_eq_u32 s12,2
s_cselect_b64 s[48:49],s[80:81],s[48:49]
v_and_b32_e32 v249,48,v247
v_accvgpr_write_b32 a241,v255
mvaa 168,96,60
dl 224,36,52,0
s_waitcnt lgkmcnt(4)
mvaa 184,104,60
s_cmp_eq_u32 s12,2
s_cselect_b32 s50,6291456,s50
ta 212,236,20480
v_xor_b32_e32 v249,v249,v250
mvaa 188,108,60
s_cmp_eq_u32 s12,2
s_cselect_b32 s59,458752,s59
v_xor_b32_e32 v250,v250,v251
v_lshl_add_u32 v252,v248,11,v249
mvaa 180,108,56
s_lshl_b32 s15,1,s86
v_and_b32_e32 v251,48,v250
v_xor_b32_e32 v253,64,v252
mvaa 176,104,56
dl 225,36,52,1024
mvaa 120,104,52
s_lshl_b32 s23,s19,7
v_accvgpr_write_b32 a242,v252
v_add_u32_e32 v246,v246,v251
mvaa 124,108,52
s_or_b32 s86,%1,s12
s_cselect_b32 s65,0,4
v_add_u32_e32 v242,15360,v253
v_and_b32_e32 v251,64,v250
mvaa 116,108,48
s_cmp_eq_u32 s12,3
s_cselect_b64 s[48:49],s[68:69],s[48:49]
v_lshl_add_u32 v248,v251,5,v246
v_xor_b32_e32 v249,64,v251
mvaa 112,104,48
dl 226,36,52,2048
mvaa 56,104,44
s_cmp_eq_u32 s12,3
s_cselect_b32 s50,s5,s50
v_accvgpr_write_b32 a243,v242
v_mul_u32_u24_e32 v251,768,v251
mvaa 60,108,44
s_cmp_eq_u32 s12,3
s_cselect_b32 s59,1280,s59
v_add_u32_e32 v242,30720,v252
v_lshl_add_u32 v249,v249,5,v246
mvaa 52,108,40
s_add_u32 s53,s53,s58
v_add_u32_e32 v246,v246,v251
v_accvgpr_write_b32 a244,v242
mvaa 48,104,40
dl 227,36,52,3072
maaa 152,104,36
s_add_u32 s54,s54,s58
v_add_u32_e32 v242,46080,v253
v_accvgpr_write_b32 a245,v242
maaa 156,108,36
s_add_u32 s52,s52,s60
v_add_u32_e32 v243,4096,v246
v_add_u32_e32 v242,0,v248
maaa 148,108,32
s_mov_b32 s63,32768
ta 214,236,20992
v_cndmask_b32_e64 v242,v228,v242,s[24:25]
maaa 144,104,32
s_mov_b32 s61,s98
ta 220,237,20480
v_cndmask_b32_e64 v242,v242,v243,s[26:27]
s_waitcnt lgkmcnt(2)
mvaa 0,112,32
s_mov_b32 s64,1
v_add_u32_e32 v243,101376,v246
v_and_b32_e32 v246,15,v247
mvaa 4,116,32
v_and_b32_e32 v246,12,v247
v_accvgpr_write_b32 a246,v242
mvaa 12,116,36
ta 222,237,20992
v_add_u32_e32 v242,48128,v249
mvaa 8,112,36
v_cndmask_b32_e64 v242,v229,v242,s[24:25]
v_lshlrev_b32_e32 v246,5,v246
mvaa 64,112,40
v_cndmask_b32_e64 v242,v242,v243,s[26:27]
v_accvgpr_write_b32 a247,v242
mvaa 68,116,40
v_add_u32_e32 v242,96256,v248
v_and_b32_e32 v248,3,v247
mvaa 76,116,44
v_cndmask_b32_e64 v242,v230,v242,s[24:25]
v_lshl_add_u32 v246,v248,3,v246
mvaa 72,112,44
v_and_b32_e32 v248,24,v247
v_accvgpr_write_b32 a248,v242
mvaa 128,112,48
v_add_u32_e32 v242,144384,v249
v_lshl_add_u32 v246,v248,2,v246
mvaa 132,116,48
v_and_b32_e32 v248,32,v247
v_cndmask_b32_e64 v242,v231,v242,s[24:25]
mvaa 140,116,52
v_add_u32_e32 v246,98304,v246
v_accvgpr_write_b32 a249,v242
mvaa 136,112,52
v_lshl_add_u32 v238,v248,5,v246
v_xor_b32_e32 v239,32,v238
mvaa 192,112,56
v_add_u32_e32 v232,32768,v232
v_add_u32_e32 v233,32768,v233
mvaa 196,116,56
mvaa 204,116,60
mvaa 200,112,60
s_sleep 15
s_waitcnt vmcnt(8)
s_waitcnt lgkmcnt(0)
s_barrier
L_k_head:
mvaa 216,208,28
ra 64,232,0
mvaa 216,212,60
s_add_u32 m0,s62,0
ta 192,234,32768
mvaa 220,220,60
dl 228,40,53,0
mvaa 220,216,28
ta 194,234,33280
s_nop 3
mvaa 212,216,24
ra 96,233,0
mvaa 212,220,56
ta 196,234,49152
mvaa 208,212,56
ta 198,234,49664
mvaa 208,208,24
ta 204,235,49152
mvaa 152,208,20
dl 229,40,53,1024
mvaa 152,212,52
s_add_u32 s53,s53,s57
ta 206,235,49664
mvaa 156,220,52
ta 200,235,32768
mvaa 156,216,20
ta 202,235,33280
mvaa 148,216,16
ra 68,232,2048
mvaa 148,220,48
ra 100,233,2048
mvaa 144,212,48
ra 72,232,4096
mvaa 144,208,16
dl 230,44,54,2048
mvaa 88,208,12
ra 104,233,4096
mvaa 88,212,44
ra 76,232,6144
mvaa 92,220,44
ra 108,233,6144
mvaa 92,216,12
ra 80,232,8192
mvaa 84,216,8
ra 112,233,8192
mvaa 84,220,40
dl 231,44,54,3072
mvaa 80,212,40
s_add_u32 s54,s54,s57
ra 84,232,10240
mvaa 80,208,8
ra 116,233,10240
mvaa 24,208,4
ra 88,232,12288
mvaa 24,212,36
ra 120,233,12288
mvaa 28,220,36
ra 92,232,14336
mvaa 28,216,4
s_add_u32 m0,s62,16384
ra 124,233,14336
mvaa 20,216,0
dl 228,40,53,0
mvaa 20,220,32
mvaa 16,212,32
mvaa 16,208,0
s_waitcnt lgkmcnt(8)
maaa 128,192,64
maaa 128,196,96
maaa 132,204,96
dl 229,40,53,1024
maaa 132,200,64
L_vs1:
s_cbranch_execz L_vs9
s_add_u32 s53,s53,s58
maaa 140,200,68
maaa 140,204,100
maaa 136,196,100
maaa 136,192,68
mvaa 32,192,72
mvaa 32,196,104
dl 230,44,54,2048
mvaa 36,204,104
mvaa 36,200,72
s_waitcnt lgkmcnt(0)
mvaa 44,200,76
ta 208,236,32768
mvaa 44,204,108
ta 210,236,33280
mvaa 40,196,108
ta 212,236,49152
mvaa 40,192,76
dl 231,44,54,3072
mvaa 96,192,80
s_add_u32 s54,s54,s58
ta 214,236,49664
mvaa 96,196,112
ta 220,237,49152
mvaa 100,204,112
ta 222,237,49664
mvaa 100,200,80
ta 216,237,32768
mvaa 108,200,84
ta 218,237,33280
mvaa 108,204,116
mvaa 104,196,116
s_add_u32 m0,s61,0
mvaa 104,192,84
dl 224,36,52,0
mvaa 160,192,88
mvaa 160,196,120
mvaa 164,204,120
mvaa 164,200,88
mvaa 172,200,92
mvaa 172,204,124
mvaa 168,196,124
dl 225,36,52,1024
mvaa 168,192,92
s_waitcnt lgkmcnt(0)
mvaa 184,208,92
mvaa 184,212,124
mvaa 188,220,124
ta 192,234,36864
mvaa 188,216,92
ta 194,234,37376
mvaa 180,216,88
ta 196,234,53248
mvaa 180,220,120
dl 226,36,52,2048
mvaa 176,212,120
ta 198,234,53760
mvaa 176,208,88
ta 204,235,53248
mvaa 120,208,84
ta 206,235,53760
mvaa 120,212,116
ta 200,235,36864
mvaa 124,220,116
ta 202,235,37376
mvaa 124,216,84
mvaa 116,216,80
dl 227,36,52,3072
mvaa 116,220,112
s_add_u32 s52,s52,s56
mvaa 112,212,112
mvaa 112,208,80
mvaa 56,208,76
mvaa 56,212,108
mvaa 60,220,108
mvaa 60,216,76
mvaa 52,216,72
s_add_u32 m0,s61,4096
mvaa 52,220,104
dl 224,36,52,0
mvaa 48,212,104
s_add_u32 s61,s63,s98
mvaa 48,208,72
s_cmp_ge_u32 s63,65536
s_cselect_b32 s99,-2,1
s_lshl_b32 s99,s99,15
maaa 152,208,68
s_add_u32 s63,s63,s99
v_add_u32_e32 v232,s99,v232
v_add_u32_e32 v233,s99,v233
maaa 152,212,100
maaa 156,220,100
maaa 156,216,68
maaa 148,216,64
dl 225,36,52,1024
maaa 148,220,96
maaa 144,212,96
s_waitcnt lgkmcnt(0)
maaa 144,208,64
mvaa 0,192,64
mvaa 0,196,96
mvaa 4,204,96
ta 208,236,36864
mvaa 4,200,64
ta 210,236,37376
mvaa 12,200,68
dl 226,36,52,2048
mvaa 12,204,100
ta 212,236,53248
mvaa 8,196,100
ta 214,236,53760
mvaa 8,192,68
ta 220,237,53248
mvaa 64,192,72
ta 222,237,53760
mvaa 64,196,104
ta 216,237,36864
mvaa 68,204,104
ta 218,237,37376
mvaa 68,200,72
dl 227,36,52,3072
mvaa 76,200,76
s_add_u32 s52,s52,s60
mvaa 76,204,108
mvaa 72,196,108
mvaa 72,192,76
mvaa 128,192,80
mvaa 128,196,112
buffer_load_dword a184,v241,s[48:51],s55 offen
mvaa 132,204,112
s_add_u32 s55,s55,s59
mvaa 132,200,80
mvaa 140,200,84
mvaa 140,204,116
mvaa 136,196,116
mvaa 136,192,84
buffer_load_dword a185,v241,s[48:51],s55 offen
mvaa 192,192,88
s_add_u32 s55,s55,s59
mvaa 192,196,120
mvaa 196,204,120
mvaa 196,200,88
mvaa 204,200,92
mvaa 204,204,124
s_cmp_eq_u32 s64,3
mvaa 200,196,124
s_waitcnt vmcnt(10) lgkmcnt(0)
s_barrier
mvaa 200,192,92
s_cbranch_scc1 L_k_exit
mvaa 216,208,92
ra 0,232,0
mvaa 216,212,124
s_add_u32 m0,s62,32768
ta 192,234,0
mvaa 220,220,124
dl 228,40,53,0
mvaa 220,216,92
ta 194,234,512
s_nop 3
mvaa 212,216,88
ra 32,233,0
mvaa 212,220,120
ta 196,234,16384
mvaa 208,212,120
ta 198,234,16896
mvaa 208,208,88
ta 204,235,16384
mvaa 152,208,84
dl 229,40,53,1024
mvaa 152,212,116
s_add_u32 s53,s53,s57
ta 206,235,16896
mvaa 156,220,116
ta 200,235,0
mvaa 156,216,84
ta 202,235,512
mvaa 148,216,80
ra 4,232,2048
mvaa 148,220,112
ra 36,233,2048
mvaa 144,212,112
ra 8,232,4096
mvaa 144,208,80
dl 230,44,54,2048
mvaa 88,208,76
ra 40,233,4096
mvaa 88,212,108
ra 12,232,6144
mvaa 92,220,108
ra 44,233,6144
mvaa 92,216,76
ra 16,232,8192
mvaa 84,216,72
ra 48,233,8192
mvaa 84,220,104
dl 231,44,54,3072
mvaa 80,212,104
s_add_u32 s54,s54,s57
ra 20,232,10240
mvaa 80,208,72
ra 52,233,10240
mvaa 24,208,68
ra 24,232,12288
mvaa 24,212,100
ra 56,233,12288
mvaa 28,220,100
ra 28,232,14336
mvaa 28,216,68
s_add_u32 m0,s62,49152
ra 60,233,14336
mvaa 20,216,64
dl 228,40,53,0
mvaa 20,220,96
mvaa 16,212,96
mvaa 16,208,64
s_waitcnt lgkmcnt(8)
maaa 128,192,0
maaa 128,196,32
maaa 132,204,32
dl 229,40,53,1024
maaa 132,200,0
s_add_u32 s53,s53,s58
maaa 140,200,4
maaa 140,204,36
maaa 136,196,36
maaa 136,192,4
mvaa 32,192,8
mvaa 32,196,40
dl 230,44,54,2048
mvaa 36,204,40
mvaa 36,200,8
s_waitcnt lgkmcnt(0)
mvaa 44,200,12
ta 208,236,0
mvaa 44,204,44
ta 210,236,512
mvaa 40,196,44
ta 212,236,16384
mvaa 40,192,12
dl 231,44,54,3072
mvaa 96,192,16
s_add_u32 s54,s54,s58
ta 214,236,16896
mvaa 96,196,48
ta 220,237,16384
mvaa 100,204,48
ta 222,237,16896
mvaa 100,200,16
ta 216,237,0
mvaa 108,200,20
ta 218,237,512
mvaa 108,204,52
mvaa 104,196,52
s_add_u32 m0,s61,0
mvaa 104,192,20
dl 224,36,52,0
mvaa 160,192,24
s_lshl_b32 s86,s21,2
s_add_u32 s86,s86,s12
s_mul_i32 s86,s86,384
)"
R"(mvaa 160,196,56
s_add_u32 s86,s86,4096
v_lshl_add_u32 v253,v244,3,s86
mvaa 164,204,56
s_mov_b64 exec,7
global_load_dword a188,v253,s[96:97]
s_mov_b64 exec,-1
mvaa 164,200,24
mvaa 172,200,28
mvaa 172,204,60
mvaa 168,196,60
dl 225,36,52,1024
mvaa 168,192,28
s_waitcnt lgkmcnt(0)
mvaa 184,208,28
mvaa 184,212,60
mvaa 188,220,60
ta 192,234,4096
mvaa 188,216,28
ta 194,234,4608
mvaa 180,216,24
ta 196,234,20480
mvaa 180,220,56
dl 226,36,52,2048
mvaa 176,212,56
ta 198,234,20992
mvaa 176,208,24
ta 204,235,20480
mvaa 120,208,20
ta 206,235,20992
mvaa 120,212,52
ta 200,235,4096
mvaa 124,220,52
ta 202,235,4608
mvaa 124,216,20
mvaa 116,216,16
dl 227,36,52,3072
mvaa 116,220,48
s_add_u32 s52,s52,s56
mvaa 112,212,48
mvaa 112,208,16
mvaa 56,208,12
mvaa 56,212,44
s_bitcmp1_b32 s65,s64
s_cbranch_scc1 L_k_ev
L_k_evr:
mvaa 60,220,44
mvaa 60,216,12
mvaa 52,216,8
s_add_u32 m0,s61,4096
mvaa 52,220,40
dl 224,36,52,0
mvaa 48,212,40
s_add_u32 s61,s63,s98
mvaa 48,208,8
s_cmp_ge_u32 s63,65536
s_cselect_b32 s99,-2,1
s_lshl_b32 s99,s99,15
maaa 152,208,4
s_add_u32 s63,s63,s99
v_add_u32_e32 v232,s99,v232
v_add_u32_e32 v233,s99,v233
maaa 152,212,36
maaa 156,220,36
maaa 156,216,4
maaa 148,216,0
dl 225,36,52,1024
maaa 148,220,32
maaa 144,212,32
s_waitcnt lgkmcnt(0)
maaa 144,208,0
mvaa 0,192,0
mvaa 0,196,32
mvaa 4,204,32
ta 208,236,4096
mvaa 4,200,0
ta 210,236,4608
mvaa 12,200,4
dl 226,36,52,2048
mvaa 12,204,36
ta 212,236,20480
mvaa 8,196,36
ta 214,236,20992
mvaa 8,192,4
ta 220,237,20480
mvaa 64,192,8
ta 222,237,20992
mvaa 64,196,40
ta 216,237,4096
mvaa 68,204,40
ta 218,237,4608
mvaa 68,200,8
dl 227,36,52,3072
mvaa 76,200,12
s_add_u32 s52,s52,s60
mvaa 76,204,44
mvaa 72,196,44
mvaa 72,192,12
mvaa 128,192,16
mvaa 128,196,48
buffer_load_dword a187,v241,s[48:51],s55 offen
mvaa 132,204,48
s_add_u32 s55,s55,s59
mvaa 132,200,16
mvaa 140,200,20
mvaa 140,204,52
mvaa 136,196,52
mvaa 136,192,20
buffer_load_dword a190,v241,s[48:51],s55 offen
mvaa 192,192,24
s_add_u32 s55,s55,s59
mvaa 192,196,56
mvaa 196,204,56
s_add_u32 s64,s64,1
mvaa 196,200,24
mvaa 204,200,28
mvaa 204,204,60
mvaa 200,196,60
s_waitcnt vmcnt(10) lgkmcnt(0)
s_barrier
mvaa 200,192,28
s_branch L_k_head
L_k_exit:
global_load_dwordx4 a[160:163],v244,s[30:31] offset:0
s_add_u32 m0,s62,32768
mvaa 216,208,92
s_cmp_lt_u32 s12,2
s_cselect_b32 s86,s22,s23
ta 32,234,0
s_and_b32 s87,s12,1
mvaa 216,212,124
s_lshl_b32 s86,s86,11
ta 34,234,512
s_lshl_b32 s87,s87,17
mvaa 220,220,124
s_add_u32 s86,s86,s87
ra 0,232,0
s_mov_b64 s[36:37],s[68:69]
mvaa 220,216,92
s_mov_b32 s38,4194304
ta 36,235,0
s_mov_b32 s52,s86
mvaa 212,216,88
s_mov_b32 s56,65536
ta 38,235,512
v_accvgpr_read_b32 v224,a242
mvaa 212,220,120
dl 228,40,53,0
mvaa 208,212,120
s_lshl_b32 s88,s18,1
ra 4,232,2048
v_accvgpr_read_b32 v225,a243
mvaa 208,208,88
s_add_u32 s88,s88,s12
ra 8,232,4096
v_accvgpr_read_b32 v226,a244
mvaa 152,208,84
s_mov_b32 s91,196608
ra 12,232,6144
v_accvgpr_read_b32 v227,a245
mvaa 152,212,116
s_lshl_b32 s88,s88,7
ra 16,232,8192
s_lshl_b32 s89,s18,6
mvaa 156,220,116
s_mov_b32 s90,6291456
ra 20,232,10240
s_mov_b32 s60,-65408
mvaa 156,216,84
dl 229,40,53,1024
mvaa 148,216,80
s_add_u32 s53,s53,s57
ra 24,232,12288
s_bfe_u32 s4,s21,65536
mvaa 148,220,112
s_lshl_b32 s4,s4,6
ra 28,232,14336
s_lshl_b32 s87,s20,2
mvaa 144,212,112
s_mov_b32 s26,32768
ta 40,236,0
s_mov_b32 s27,131072
mvaa 144,208,80
s_add_u32 s61,s98,32768
ta 42,236,512
s_mov_b32 s63,65536
mvaa 88,208,76
s_cmp_ge_u32 s63,65536
s_cselect_b32 s99,-2,1
ta 44,237,0
s_lshl_b32 s99,s99,15
mvaa 88,212,108
dl 230,44,54,2048
mvaa 92,220,108
s_mov_b32 s67,2048
ta 46,237,512
s_mov_b32 s66,8
mvaa 92,216,76
s_mov_b32 s64,1
ta 48,234,4096
mvaa 84,216,72
s_bitcmp1_b32 s12,0
s_cselect_b64 vcc,-1,0
ta 50,234,4608
mvaa 84,220,104
ta 52,235,4096
mvaa 80,212,104
ta 54,235,4608
mvaa 80,208,72
dl 231,44,54,3072
s_add_u32 m0,s62,49152
mvaa 24,208,68
s_add_u32 s54,s54,s57
ta 56,236,4096
mvaa 24,212,100
s_cmp_lt_u32 s12,2
s_cselect_b32 s57,262144,s91
ta 58,236,4608
mvaa 28,220,100
ta 60,237,4096
mvaa 28,216,68
ta 62,237,4608
mvaa 20,216,64
mvaa 20,220,96
dl 228,40,53,0
mvaa 16,212,96
v_accvgpr_read_b32 v228,a246
mvaa 16,208,64
s_waitcnt lgkmcnt(5)
maaa 128,32,0
maaa 132,36,0
maaa 140,36,4
dl 229,40,53,1024
maaa 136,32,4
s_waitcnt lgkmcnt(0)
s_barrier
mvaa 32,32,8
s_add_u32 s53,s53,s58
ta 96,234,16384
v_accvgpr_read_b32 v229,a247
mvaa 36,36,8
s_cmp_lt_u32 s12,2
s_cselect_b32 s42,8388608,s90
ta 98,234,16896
mvaa 44,36,12
s_cmp_lt_u32 s12,2
s_cselect_b32 s53,s88,s89
ra 64,233,0
s_and_b32 s89,s98,8192
mvaa 40,32,12
dl 230,44,54,2048
mvaa 96,32,16
s_bitcmp1_b32 s21,0
s_cselect_b32 s90,s57,0
ta 100,235,16384
s_add_u32 s53,s53,s90
mvaa 100,36,16
s_cmp_lt_u32 s12,2
s_cselect_b64 s[40:41],s[72:73],s[80:81]
ta 102,235,16896
v_accvgpr_read_b32 v230,a248
mvaa 108,36,20
s_lshl_b32 s90,s90,1
ra 68,233,2048
s_add_u32 s91,s89,65536
mvaa 104,32,20
s_bfe_u32 s88,s21,65539
ra 72,233,4096
s_lshl_b32 s88,s88,1
mvaa 160,32,24
dl 231,44,54,3072
mvaa 164,36,24
s_add_u32 m0,s98,65536
ra 76,233,6144
s_add_u32 s54,s54,s58
mvaa 172,36,28
s_cmp_eq_u32 s12,3
s_cselect_b64 s[44:45],s[68:69],s[40:41]
ra 80,233,8192
v_accvgpr_read_b32 v231,a249
mvaa 168,32,28
s_cmp_eq_u32 s12,3
s_cselect_b32 s46,0,s42
ra 84,233,10240
s_add_u32 s58,s57,s90
mvaa 184,40,28
s_cmp_eq_u32 s12,3
s_cselect_b32 s54,0,s53
ra 88,233,12288
s_sub_u32 s57,s57,s90
mvaa 188,44,28
global_load_dwordx4 a[168:171],v244,s[30:31] offset:2048
mvaa 180,44,24
s_add_u32 s90,s89,65536
ta 104,236,16384
s_add_u32 s88,s88,s87
mvaa 176,40,24
s_and_b32 s87,s12,1
ra 92,233,14336
s_add_u32 s88,s88,s87
mvaa 120,40,20
L_vs2:
s_cbranch_execz L_vs10
s_mul_i32 s88,s88,524288
ta 106,236,16896
s_and_b32 s87,s21,7
mvaa 124,44,20
s_add_u32 s86,s88,16777216
ta 108,237,16384
s_lshl_b32 s87,s87,16
mvaa 116,44,16
global_load_dwordx4 a[176:179],v244,s[84:85] offset:0
mvaa 112,40,16
s_add_u32 s86,s86,s87
ta 110,237,16896
s_lshr_b32 s87,s12,1
mvaa 56,40,12
s_lshl_b32 s87,s87,15
ta 112,234,20480
s_add_u32 s86,s86,s87
mvaa 60,44,12
s_add_u32 s24,s8,s86
s_addc_u32 s25,s9,0
ta 114,234,20992
s_lshl_b32 s86,%1,5
mvaa 52,44,8
s_lshl_b32 s87,s12,3
ta 116,235,20480
s_add_u32 s86,s86,s87
mvaa 48,40,8
global_load_dwordx4 a[164:167],v244,s[30:31] offset:1024
maaa 152,40,4
s_add_u32 s86,s86,83886080
ta 118,235,20992
s_mov_b32 s88,4096
maaa 156,44,4
s_cmp_lg_u32 s65,0
s_cbranch_scc1 L_k_ec1
L_k_ecr1:
ta 120,236,20480
maaa 148,44,0
s_cmp_eq_u32 s12,0
s_cselect_b32 s65,96,0
ta 122,236,20992
s_or_b32 s65,s65,128
maaa 144,40,0
ta 124,237,20480
mvaa 0,48,0
global_load_dwordx4 a[172:175],v244,s[30:31] offset:3072
mvaa 4,52,0
s_mov_b32 s30,1048577
ta 126,237,20992
s_mov_b32 s31,268435712
mvaa 12,52,4
mvaa 8,48,4
mvaa 64,48,8
mvaa 68,52,8
global_load_dwordx4 a[180:183],v244,s[84:85] offset:1024
mvaa 76,52,12
mvaa 72,48,12
mvaa 128,48,16
mvaa 132,52,16
mvaa 140,52,20
dl 224,36,52,0
mvaa 136,48,20
mvaa 192,48,24
mvaa 196,52,24
mvaa 204,52,28
mvaa 200,48,28
dl 225,36,52,1024
mvaa 216,56,28
mvaa 220,60,28
mvaa 212,60,24
mvaa 208,56,24
mvaa 152,56,20
dl 226,36,52,2048
mvaa 156,60,20
mvaa 148,60,16
mvaa 144,56,16
mvaa 88,56,12
s_waitcnt vmcnt(12) lgkmcnt(0)
s_barrier
mvaa 92,60,12
dl 227,36,52,3072
mvaa 84,60,8
s_add_u32 s52,s52,s56
s_add_u32 m0,s98,69632
ta 32,234,32768
mvaa 80,56,8
ta 34,234,33280
mvaa 24,56,4
ta 36,235,32768
mvaa 28,60,4
ta 38,235,33280
mvaa 20,60,0
dl 224,36,52,0
mvaa 16,56,0
ta 40,236,32768
maaa 128,96,64
ta 42,236,33280
maaa 132,100,64
ta 44,237,32768
maaa 140,100,68
ta 46,237,33280
maaa 136,96,68
dl 225,36,52,1024
mvaa 32,96,72
ta 48,234,36864
mvaa 36,100,72
ta 50,234,37376
mvaa 44,100,76
ta 52,235,36864
mvaa 40,96,76
ta 54,235,37376
mvaa 96,96,80
dl 226,36,52,2048
mvaa 100,100,80
ra 20,232,43008
mvaa 108,100,84
ra 24,232,45056
mvaa 104,96,84
ra 0,232,32768
mvaa 160,96,88
ra 28,232,47104
mvaa 164,100,88
dl 227,36,52,3072
mvaa 172,100,92
s_add_u32 m0,s62,0
ra 4,232,34816
s_add_u32 s52,s52,s60
mvaa 168,96,92
ra 16,232,40960
mvaa 184,104,92
ra 8,232,36864
mvaa 188,108,92
ra 12,232,38912
v_add_u32_e32 v232,s90,v232
mvaa 180,108,88
dl 228,40,53,0
mvaa 176,104,88
ta 60,237,36864
v_xor_b32_e32 v232,s4,v232
mvaa 120,104,84
ta 62,237,37376
mvaa 124,108,84
ta 56,236,36864
mvaa 116,108,80
ta 58,236,37376
mvaa 112,104,80
dl 229,40,53,1024
mvaa 56,104,76
s_add_u32 s53,s53,s57
mvaa 60,108,76
mvaa 52,108,72
mvaa 48,104,72
maaa 152,104,68
dl 230,44,54,2048
maaa 156,108,68
maaa 148,108,64
maaa 144,104,64
mvaa 0,112,64
mvaa 4,116,64
dl 231,44,54,3072
mvaa 12,116,68
s_add_u32 m0,s62,16384
s_add_u32 s54,s54,s57
mvaa 8,112,68
mvaa 64,112,72
mvaa 68,116,72
mvaa 76,116,76
dl 228,40,53,0
mvaa 72,112,76
mvaa 128,112,80
mvaa 132,116,80
mvaa 140,116,84
mvaa 136,112,84
dl 229,40,53,1024
mvaa 192,112,88
s_add_u32 s53,s53,s58
mvaa 196,116,88
mvaa 204,116,92
mvaa 200,112,92
mvaa 216,120,92
dl 230,44,54,2048
mvaa 220,124,92
mvaa 212,124,88
s_waitcnt vmcnt(20) lgkmcnt(0)
s_barrier
mvaa 208,120,88
ra 92,233,47104
mvaa 152,120,84
ta 96,234,49152
mvaa 156,124,84
dl 231,44,54,3072
mvaa 148,124,80
s_add_u32 m0,s98,0
ra 88,233,45056
s_add_u32 s54,s54,s58
mvaa 144,120,80
ta 98,234,49664
mvaa 88,120,76
ta 112,234,53248
mvaa 92,124,76
ta 116,235,53248
mvaa 84,124,72
dl 224,36,52,0
mvaa 80,120,72
ta 118,235,53760
mvaa 24,120,68
ta 114,234,53760
v_subrev_u32_e32 v234,s89,v234
mvaa 28,124,68
ta 100,235,49152
mvaa 20,124,64
ta 102,235,49664
v_subrev_u32_e32 v235,s89,v235
mvaa 16,120,64
dl 225,36,52,1024
maaa 128,32,0
ta 124,237,53248
maaa 132,36,0
ta 126,237,53760
maaa 140,36,4
ta 120,236,53248
maaa 136,32,4
ta 122,236,53760
mvaa 32,32,8
dl 226,36,52,2048
mvaa 36,36,8
ta 104,236,49152
mvaa 44,36,12
ta 106,236,49664
v_subrev_u32_e32 v236,s89,v236
mvaa 40,32,12
ta 108,237,49152
mvaa 56,40,12
ta 110,237,49664
v_subrev_u32_e32 v237,s89,v237
mvaa 60,44,12
dl 227,36,52,3072
mvaa 52,44,8
s_add_u32 s52,s52,s56
s_add_u32 m0,s98,4096
ra 84,233,43008
mvaa 48,40,8
ra 80,233,40960
maaa 152,40,4
ra 72,233,36864
maaa 156,44,4
ra 76,233,38912
maaa 148,44,0
dl 224,36,52,0
maaa 144,40,0
ra 68,233,34816
mvaa 0,48,0
ra 64,233,32768
v_add_u32_e32 v233,s91,v233
mvaa 4,52,0
v_xor_b32_e32 v233,s4,v233
mvaa 12,52,4
mvaa 8,48,4
dl 225,36,52,1024
mvaa 24,56,4
mvaa 28,60,4
mvaa 20,60,0
mvaa 16,56,0
mvaa 220,60,28
dl 226,36,52,2048
s_waitcnt lgkmcnt(2)
mvaa 220,124,92
mvaa 216,120,92
mvaa 216,56,28
mvaa 212,60,24
mvaa 212,124,88
dl 227,36,52,3072
mvaa 208,120,88
s_add_u32 m0,s62,32768
s_add_u32 s52,s52,s60
s_waitcnt vmcnt(25)
zvaa 246,160,176
mvaa 208,56,24
cv 223,222,223
cv 222,220,221
mvaa 204,52,28
cv 221,218,219
zvaa 250,168,176
mvaa 204,116,92
cv 220,216,217
cv 219,214,215
mvaa 200,112,92
dl 228,40,53,0
s_waitcnt vmcnt(25)
mvaa 246,164,180
mvaa 200,48,28
cv 218,212,213
mvaa 250,172,180
mvaa 196,52,24
cv 217,210,211
cv 216,208,209
mvaa 196,116,88
cv 215,206,207
v_cndmask_b32_e64 v246,0,v246,s[30:31]
mvaa 192,112,88
cv 214,204,205
v_cndmask_b32_e64 v250,0,v250,s[30:31]
mvaa 192,48,24
dl 229,40,53,1024
s_lshl_b64 s[30:31],s[30:31],1
v_cndmask_b32_e64 v246,v246,v247,s[30:31]
v_cndmask_b32_e64 v250,v250,v251,s[30:31]
mvaa 188,44,28
s_lshl_b64 s[30:31],s[30:31],1
s_add_u32 s53,s53,s57
v_cndmask_b32_e64 v246,v246,v248,s[30:31]
v_cndmask_b32_e64 v250,v250,v252,s[30:31]
mvaa 188,108,92
s_lshl_b64 s[30:31],s[30:31],1
cv 213,202,203
v_cndmask_b32_e64 v246,v246,v249,s[30:31]
mvaa 184,104,92
v_cndmask_b32_e64 v250,v250,v253,s[30:31]
v_add_f32_dpp v246,v246,v246 quad_perm:[1,0,3,2] row_mask:0xf bank_mask:0xf
mvaa 184,40,28
v_add_f32_dpp v250,v250,v250 quad_perm:[1,0,3,2] row_mask:0xf bank_mask:0xf
v_add_f32_dpp v246,v246,v246 quad_perm:[2,3,0,1] row_mask:0xf bank_mask:0xf
mvaa 180,44,24
dl 230,44,54,2048
v_add_f32_dpp v250,v250,v250 quad_perm:[2,3,0,1] row_mask:0xf bank_mask:0xf
mvaa 180,108,88
v_add_f32_dpp v246,v246,v246 row_half_mirror row_mask:0xf bank_mask:0xf
v_add_f32_dpp v250,v250,v250 row_half_mirror row_mask:0xf bank_mask:0xf
mvaa 176,104,88
v_add_f32_dpp v246,v246,v246 row_mirror row_mask:0xf bank_mask:0xf
v_add_f32_dpp v250,v250,v250 row_mirror row_mask:0xf bank_mask:0xf
mvaa 176,40,24
cv 212,200,201
cv 211,198,199
mvaa 172,36,28
v_permlane16_swap_b32_e32 v246,v250
mvaa 172,100,92
dl 231,44,54,3072
v_add_f32_e32 v246,v246,v250
v_mov_b32_e32 v250,v246
mvaa 168,96,92
s_add_u32 s54,s54,s57
v_permlane32_swap_b32_e32 v246,v250
mvaa 168,32,28
v_add_f32_e32 v246,v246,v250
v_lshrrev_b32_e32 v250,6,v244
mvaa 164,36,24
v_mul_f32_e32 v246,0xbfb8aa3b,v246
cv 210,196,197
mvaa 164,100,88
ex 246
mvaa 160,96,88
buffer_load_dword a242,v241,s[48:51],s55 offen
mvaa 160,32,24
s_add_u32 s55,s55,s59
v_add_f32_e32 v246,1.0,v246
cv 209,194,195
mvaa 156,60,20
v_rcp_f32_e32 v246,v246
mvaa 156,124,84
s_waitcnt vmcnt(17) lgkmcnt(0)
s_barrier
s_add_u32 m0,s62,49152
mvaa 152,120,84
cv 208,192,193
cv 207,190,191
mvaa 152,56,20
dl 228,40,53,0
mvaa 148,60,16
cv 206,188,189
cv 205,186,187
mvaa 148,124,80
cv 204,184,185
cv 203,182,183
mvaa 144,120,80
cv 202,180,181
cv 201,178,179
mvaa 144,56,16
cv 200,176,177
cv 199,174,175
mvaa 140,52,20
dl 229,40,53,1024
mvaa 140,116,84
s_add_u32 s53,s53,s58
cv 198,172,173
cv 197,170,171
mvaa 136,112,84
ra 4,232,0
cv 196,168,169
mvaa 136,48,20
ta 92,239,12288
cv 195,166,167
mvaa 132,52,16
ta 94,239,12800
cv 194,164,165
mvaa 132,116,80
dl 230,44,54,2048
mvaa 128,112,80
ta 88,238,12288
cv 193,162,163
mvaa 128,48,16
ta 90,238,12800
cv 192,160,161
mvaa 124,44,20
cv 191,158,159
cv 190,156,157
mvaa 124,108,84
cv 189,154,155
cv 188,152,153
mvaa 120,104,84
dl 231,44,54,3072
mvaa 120,40,20
s_add_u32 m0,s98,32768
cv 187,150,151
cv 186,148,149
mvaa 116,44,16
s_add_u32 s54,s54,s58
cv 185,146,147
cv 184,144,145
mvaa 116,108,80
cv 183,142,143
cv 182,140,141
mvaa 112,104,80
cv 181,138,139
cv 180,136,137
mvaa 112,40,16
dl 224,36,52,0
mvaa 108,36,20
cv 179,134,135
cv 178,132,133
mvaa 108,100,84
cv 177,130,131
cv 176,128,129
mvaa 104,96,84
cv 175,126,127
cv 174,124,125
mvaa 104,32,20
cv 173,122,123
cv 172,120,121
mvaa 100,36,16
dl 225,36,52,1024
mvaa 100,100,80
ta 44,235,0
cv 171,118,119
mvaa 96,96,80
ta 46,235,512
cv 170,116,117
mvaa 96,32,16
ta 40,234,0
cv 169,114,115
mvaa 92,60,12
ta 42,234,512
cv 168,112,113
mvaa 92,124,76
dl 226,36,52,2048
mvaa 88,120,76
cv 167,110,111
cv 166,108,109
mvaa 88,56,12
ta 84,237,8192
cv 165,106,107
mvaa 84,60,8
ta 86,237,8704
cv 164,104,105
mvaa 84,124,72
ta 80,236,8192
cv 163,102,103
mvaa 80,120,72
dl 227,36,52,3072
mvaa 80,56,8
s_add_u32 m0,s62,0
ta 82,236,8704
cv 162,100,101
mvaa 76,52,12
s_add_u32 s52,s52,s56
ra 16,232,6144
cv 161,98,99
mvaa 76,116,76
ta 60,235,4096
cv 160,96,97
mvaa 72,112,76
ta 56,234,4096
cv 159,94,95
mvaa 72,48,12
buffer_load_dword a243,v241,s[48:51],s55 offen
mvaa 68,52,8
s_add_u32 s55,s55,s59
ta 62,235,4608
cv 151,78,79
mvaa 68,116,72
ta 58,234,4608
cv 150,76,77
mvaa 64,112,72
ra 12,232,4096
cv 158,92,93
mvaa 64,48,8
ta 52,237,0
cv 157,90,91
mvaa 60,108,76
buffer_load_dword a244,v241,s[48:51],s55 offen
mvaa 56,104,76
s_add_u32 s55,s55,s59
ta 54,237,512
cv 147,70,71
mvaa 52,108,72
ra 8,232,2048
cv 146,68,69
mvaa 48,104,72
ta 48,236,0
cv 145,66,67
mvaa 44,100,76
ta 50,236,512
cv 144,64,65
mvaa 40,96,76
buffer_load_dword a245,v241,s[48:51],s55 offen
mvaa 36,100,72
s_add_u32 s55,s55,s59
cv 143,62,63
cv 142,60,61
mvaa 32,96,72
cv 141,58,59
cv 140,56,57
mvaa 28,124,68
cv 139,54,55
cv 138,52,53
mvaa 24,120,68
cv 137,50,51
cv 136,48,49
mvaa 20,124,64
s_mov_b64 exec,65537
buffer_store_dword v246,v250,s[8:11],s86 offen sc0 sc1
s_mov_b64 exec,-1
mvaa 16,120,64
cv 135,46,47
cv 134,44,45
mvaa 12,116,68
ta 76,235,8192
cv 133,42,43
mvaa 8,112,68
ta 78,235,8704
cv 132,40,41
mvaa 4,116,64
cv 131,38,39
cv 130,36,37
mvaa 0,112,64
L_vs3:
s_cbranch_execz L_vs11
ta 72,234,8192
cv 129,34,35
maaa 156,108,68
ta 74,234,8704
cv 128,32,33
maaa 152,104,68
cv 127,30,31
cv 126,28,29
maaa 148,108,64
cv 125,26,27
cv 124,24,25
maaa 144,104,64
v_accvgpr_read_b32 v63,a159
v_accvgpr_read_b32 v62,a158
maaa 140,100,68
v_accvgpr_read_b32 v61,a157
v_accvgpr_read_b32 v60,a156
maaa 136,96,68
v_accvgpr_read_b32 v59,a155
v_accvgpr_read_b32 v58,a154
maaa 132,100,64
v_accvgpr_read_b32 v57,a153
v_accvgpr_read_b32 v56,a152
maaa 128,96,64
ta 68,237,4096
ta 70,237,4608
s_waitcnt lgkmcnt(4)
zaaa 236,40,4
cv 56,56,57
cv 57,58,59
zaaa 220,44,4
ta 64,236,4096
ta 66,236,4608
zaaa 216,44,8
cv 58,60,61
cv 59,62,63
zaaa 232,40,8
buffer_store_dwordx4 v[56:59],v244,s[24:27],0 offen offset:3072 sc0 sc1
zaaa 228,40,12
v_accvgpr_read_b32 v55,a151
v_accvgpr_read_b32 v54,a150
zaaa 212,44,12
v_accvgpr_read_b32 v53,a149
v_accvgpr_read_b32 v52,a148
zaaa 208,44,16
v_accvgpr_read_b32 v51,a147
v_accvgpr_read_b32 v50,a146
zaaa 224,40,16
v_accvgpr_read_b32 v49,a145
v_accvgpr_read_b32 v48,a144
zaaa 192,48,16
cv 48,48,49
cv 49,50,51
zaaa 176,52,16
cv 50,52,53
cv 51,54,55
zaaa 180,52,12
buffer_store_dwordx4 v[48:51],v244,s[24:27],0 offen offset:2048 sc0 sc1
zaaa 196,48,12
s_waitcnt vmcnt(27) lgkmcnt(0)
s_barrier
ra 24,233,0
v_accvgpr_read_b32 v47,a143
zaaa 200,48,8
ra 28,233,2048
v_accvgpr_read_b32 v46,a142
v_accvgpr_read_b32 v45,a141
zaaa 184,52,8
ra 32,233,4096
ra 36,233,6144
zaaa 188,52,4
v_accvgpr_read_b32 v43,a139
v_accvgpr_read_b32 v42,a138
zaaa 204,48,4
dl 228,40,53,0
ta 96,236,24576
zaaa 172,56,4
ta 98,236,25088
v_accvgpr_read_b32 v41,a137
zaaa 156,60,4
ta 100,237,24576
v_accvgpr_read_b32 v39,a135
zaaa 152,60,8
ta 102,237,25088
ta 104,238,28672
zaaa 168,56,8
v_accvgpr_read_b32 v38,a134
v_accvgpr_read_b32 v35,a131
zaaa 164,56,12
dl 229,40,53,1024
ta 106,238,29184
zaaa 148,60,12
s_add_u32 s53,s53,s57
ta 108,239,28672
v_accvgpr_read_b32 v34,a130
zaaa 144,60,16
ta 110,239,29184
v_accvgpr_read_b32 v33,a129
zaaa 160,56,16
ta 44,235,16384
ta 46,235,16896
ta 40,234,16384
ta 42,234,16896
ta 52,237,16384
ta 54,237,16896
ta 48,236,16384
ta 50,236,16896
ta 60,235,20480
ta 62,235,20992
ta 56,234,20480
ta 58,234,20992
v_accvgpr_read_b32 v32,a128
zaaa 128,64,16
v_accvgpr_read_b32 v37,a133
v_accvgpr_read_b32 v36,a132
zaaa 112,68,16
dl 230,44,54,2048
zaaa 116,68,12
v_accvgpr_read_b32 v40,a136
v_accvgpr_read_b32 v44,a140
zaaa 132,64,12
cv 251,42,43
cv 250,40,41
zaaa 136,64,8
cv 253,46,47
cv 252,44,45
zaaa 120,68,8
cv 249,38,39
cv 247,34,35
zaaa 124,68,4
dl 231,44,54,3072
zaaa 140,64,4
s_add_u32 m0,s61,4096
cv 246,32,33
cv 248,36,37
zvaa 40,72,4
s_add_u32 s54,s54,s57
ta 68,237,20480
cv 123,22,23
zvaa 44,76,4
s_add_u32 s61,s63,s98
ta 70,237,20992
cv 121,18,19
zvaa 52,76,8
s_add_u32 s63,s63,s99
ta 64,236,20480
cv 120,16,17
zvaa 48,72,8
dl 224,36,52,0
zvaa 56,72,12
)"
R"(ta 66,236,20992
cv 122,20,21
zvaa 60,76,12
cv 119,14,15
cv 118,12,13
zvaa 68,76,16
cv 117,10,11
cv 116,8,9
zvaa 64,72,16
cv 156,88,89
cv 153,82,83
zvaa 32,80,16
dl 225,36,52,1024
ta 76,235,24576
ta 78,235,25088
zvaa 36,84,16
ta 72,234,24576
ta 74,234,25088
zvaa 28,84,12
cv 152,80,81
cv 155,86,87
zvaa 24,80,12
cv 154,84,85
cv 149,74,75
zvaa 16,80,8
cv 148,72,73
v_add_u32_e32 v232,s99,v232
zvaa 20,84,8
dl 226,36,52,2048
zvaa 12,84,4
cv 115,6,7
cv 114,4,5
zvaa 8,80,4
cv 113,2,3
cv 112,0,1
zvaa 76,4,88
zvaa 96,4,92
zvaa 100,8,92
dl 227,36,52,3072
zvaa 80,8,88
s_add_u32 s52,s52,s60
zvaa 84,12,88
zvaa 104,12,92
zvaa 108,16,92
s_waitcnt vmcnt(23) lgkmcnt(0)
s_barrier
zvaa 88,16,88
buffer_store_dwordx4 v[250:253],v244,s[24:27],0 offen offset:1024 sc0 sc1
buffer_store_dwordx4 v[246:249],v244,s[24:27],0 offen offset:0 sc0 sc1
s_branch L_kvx
L_vtile:
L_vs4:
s_cbranch_execz L_vs8
s_waitcnt vmcnt(8)
s_barrier
ta 64,234,0
ta 66,234,512
ra 0,232,0
ta 68,235,0
ta 70,235,512
ra 4,232,2048
ra 8,232,4096
ra 12,232,6144
ra 16,232,8192
ra 20,232,10240
ra 24,232,12288
ra 28,232,14336
s_add_u32 m0,s62,32768
v_mov_b32_e32 v212,0
dl 228,40,53,0
v_mov_b32_e32 v213,0
v_mov_b32_e32 v214,0
v_mov_b32_e32 v215,0
v_mov_b32_e32 v220,0
v_mov_b32_e32 v221,0
v_mov_b32_e32 v222,0
v_mov_b32_e32 v223,0
v_mov_b32_e32 v216,0
v_mov_b32_e32 v217,0
v_mov_b32_e32 v218,0
v_mov_b32_e32 v219,0
v_mov_b32_e32 v208,0
v_mov_b32_e32 v209,0
v_mov_b32_e32 v210,0
v_mov_b32_e32 v211,0
v_mov_b32_e32 v148,0
s_waitcnt lgkmcnt(9)
zaaa 128,0,64
s_lshl_b32 s4,%1,1
ta 72,236,0
v_mov_b32_e32 v149,0
s_waitcnt lgkmcnt(8)
zaaa 136,0,68
s_add_u32 s4,s4,s12
ta 74,236,512
v_mov_b32_e32 v150,0
s_waitcnt lgkmcnt(8)
zaaa 140,4,68
s_mul_i32 s4,s4,1280
ta 76,237,0
ta 78,237,512
zaaa 132,4,64
dl 229,40,53,1024
s_waitcnt lgkmcnt(4)
zvaa 32,8,64
s_add_u32 s53,s53,s57
v_mov_b32_e32 v151,0
v_mov_b32_e32 v156,0
zvaa 40,8,68
s_mov_b32 s55,s4
v_mov_b32_e32 v157,0
v_mov_b32_e32 v158,0
zvaa 44,12,68
s_mul_i32 s4,%1,1792
ta 80,234,4096
v_mov_b32_e32 v159,0
zvaa 36,12,64
dl 230,44,54,2048
zvaa 96,16,64
s_lshl_b32 s88,%1,2
ta 82,234,4608
v_mov_b32_e32 v152,0
zvaa 104,16,68
s_cmp_eq_u32 s12,2
s_cselect_b32 s55,s4,s55
ta 84,235,4096
v_mov_b32_e32 v153,0
zvaa 108,20,68
s_lshl_b32 s4,%1,14
ta 86,235,4608
v_mov_b32_e32 v154,0
zvaa 100,20,64
dl 231,44,54,3072
zvaa 160,24,64
s_add_u32 m0,s98,36864
v_mov_b32_e32 v155,0
v_mov_b32_e32 v144,0
zvaa 168,24,68
s_add_u32 s54,s54,s57
v_mov_b32_e32 v145,0
v_mov_b32_e32 v146,0
zvaa 172,28,68
s_add_u32 s88,s88,s12
v_mov_b32_e32 v147,0
v_mov_b32_e32 v84,0
zvaa 164,28,64
dl 224,36,52,0
s_waitcnt lgkmcnt(4)
zvaa 180,28,72
s_add_u32 s5,s4,16384
v_mov_b32_e32 v85,0
v_mov_b32_e32 v86,0
zvaa 188,28,76
s_cmp_eq_u32 s12,3
s_cselect_b32 s55,s4,s55
ra 32,233,0
v_mov_b32_e32 v87,0
zvaa 184,24,76
s_mov_b32 s4,10
v_mov_b32_e32 v92,0
v_mov_b32_e32 v93,0
zvaa 176,24,72
dl 225,36,52,1024
zvaa 116,20,72
s_lshl_b32 s88,s88,12
ra 36,233,2048
v_mov_b32_e32 v94,0
zvaa 124,20,76
s_xor_b32 s87,s19,15
ra 40,233,4096
v_mov_b32_e32 v95,0
zvaa 120,16,76
s_cmp_eq_u32 s12,2
s_cselect_b32 s4,14,s4
v_mov_b32_e32 v88,0
v_mov_b32_e32 v89,0
zvaa 112,16,72
dl 226,36,52,2048
zvaa 52,12,72
s_waitcnt vmcnt(11)
s_barrier
zvaa 60,12,76
s_add_u32 s30,s68,s88
s_addc_u32 s31,s69,0
ta 96,234,16384
v_mov_b32_e32 v90,0
zvaa 56,8,76
s_lshl_b32 s22,s87,7
ta 98,234,16896
v_mov_b32_e32 v91,0
zvaa 48,8,72
dl 227,36,52,3072
zaaa 148,4,72
s_add_u32 m0,s62,49152
ta 100,235,16384
v_mov_b32_e32 v80,0
zaaa 156,4,76
s_add_u32 s52,s52,s60
ta 102,235,16896
v_mov_b32_e32 v81,0
zaaa 152,0,76
s_lshl_b32 s87,s87,2
ra 44,233,6144
v_mov_b32_e32 v82,0
zaaa 144,0,72
dl 228,40,53,0
s_waitcnt lgkmcnt(5)
zvaa 0,0,80
s_lshl_b32 s88,s19,2
ra 48,233,8192
v_mov_b32_e32 v83,0
zvaa 8,0,84
s_cmp_eq_u32 s12,3
s_cselect_b32 s4,10,s4
ra 52,233,10240
v_mov_b32_e32 v20,0
zvaa 12,4,84
s_add_u32 s87,s87,s12
ra 56,233,12288
v_mov_b32_e32 v21,0
zvaa 4,4,80
dl 229,40,53,1024
zvaa 64,8,80
s_add_u32 s88,s88,s12
v_mov_b32_e32 v22,0
v_mov_b32_e32 v23,0
zvaa 72,8,84
s_lshl_b32 s90,s18,13
ra 60,233,14336
v_mov_b32_e32 v28,0
zvaa 76,12,84
s_add_u32 s90,s90,62914560
ta 104,236,16384
v_mov_b32_e32 v29,0
zvaa 68,12,80
dl 230,44,54,2048
zvaa 128,16,80
s_mul_i32 s14,s87,327680
ta 106,236,16896
v_mov_b32_e32 v30,0
zvaa 136,16,84
s_mul_i32 s13,s88,327680
ta 108,237,16384
v_mov_b32_e32 v31,0
zvaa 140,20,84
s_add_u32 s14,s14,s90
ta 110,237,16896
v_mov_b32_e32 v24,0
zvaa 132,20,80
dl 231,44,54,3072
zvaa 192,24,80
s_add_u32 m0,s98,65536
v_mov_b32_e32 v25,0
v_mov_b32_e32 v26,0
zvaa 200,24,84
s_add_u32 s13,s13,s90
v_mov_b32_e32 v27,0
v_mov_b32_e32 v16,0
zvaa 204,28,84
s_mul_i32 s90,s18,524288
v_mov_b32_e32 v17,0
v_mov_b32_e32 v18,0
zvaa 196,28,80
dl 224,36,52,0
s_waitcnt lgkmcnt(4)
maaa 128,32,96
s_lshl_b32 s91,s12,12
ta 208,236,4096
v_mov_b32_e32 v19,0
maaa 136,32,100
s_add_u32 s90,s90,s91
ta 210,236,4608
v_lshlrev_b32_e32 v241,7,v247
maaa 140,36,100
s_add_u32 s90,s90,16777216
ta 216,237,4096
v_mov_b32_e32 v242,134217728
maaa 132,36,96
dl 225,36,52,1024
mvaa 32,40,96
s_cmp_eq_u32 s12,3
s_cselect_b64 s[26:27],-1,0
ta 218,237,4608
v_lshrrev_b32_e32 v248,3,v247
mvaa 40,40,100
s_lshl_b32 s86,s19,1
v_and_b32_e32 v250,112,v244
v_and_b32_e32 v251,16,v247
mvaa 44,44,100
s_mov_b64 s[48:49],s[72:73]
v_lshlrev_b32_e32 v251,1,v251
v_cmp_gt_u32_e64 s[24:25],s4,v247
mvaa 36,44,96
dl 226,36,52,2048
mvaa 96,48,96
s_mov_b32 s50,8388608
v_mul_u32_u24_e32 v246,6144,v248
v_add_u32_e32 v249,s90,v244
mvaa 104,48,100
s_mov_b32 s59,655360
ta 112,234,20480
v_cndmask_b32_e64 v241,v242,v241,s[24:25]
mvaa 108,52,100
s_cmp_eq_u32 s12,2
s_cselect_b64 s[24:25],-1,0
ta 114,234,20992
v_add_u32_e32 v255,16777216,v249
mvaa 100,52,96
dl 227,36,52,3072
mvaa 160,56,96
s_add_u32 s52,s52,s56
s_add_u32 m0,s98,69632
ta 116,235,20480
mvaa 168,56,100
s_add_u32 s86,s86,2
ta 118,235,20992
v_accvgpr_write_b32 a240,v249
mvaa 172,60,100
s_cmp_eq_u32 s12,2
s_cselect_b64 s[48:49],s[80:81],s[48:49]
v_and_b32_e32 v249,48,v247
v_accvgpr_write_b32 a241,v255
mvaa 164,60,96
dl 224,36,52,0
s_waitcnt lgkmcnt(4)
mvaa 180,60,104
s_cmp_eq_u32 s12,2
s_cselect_b32 s50,6291456,s50
ta 212,236,20480
v_xor_b32_e32 v249,v249,v250
mvaa 188,60,108
s_cmp_eq_u32 s12,2
s_cselect_b32 s59,458752,s59
v_xor_b32_e32 v250,v250,v251
v_lshl_add_u32 v252,v248,11,v249
mvaa 184,56,108
s_lshl_b32 s15,1,s86
v_and_b32_e32 v251,48,v250
v_xor_b32_e32 v253,64,v252
mvaa 176,56,104
dl 225,36,52,1024
mvaa 116,52,104
s_lshl_b32 s23,s19,7
v_accvgpr_write_b32 a242,v252
v_add_u32_e32 v246,v246,v251
mvaa 124,52,108
s_or_b32 s86,%1,s12
s_cselect_b32 s65,0,4
v_add_u32_e32 v242,15360,v253
v_and_b32_e32 v251,64,v250
mvaa 120,48,108
s_cmp_eq_u32 s12,3
s_cselect_b64 s[48:49],s[68:69],s[48:49]
v_lshl_add_u32 v248,v251,5,v246
v_xor_b32_e32 v249,64,v251
mvaa 112,48,104
dl 226,36,52,2048
mvaa 52,44,104
s_cmp_eq_u32 s12,3
s_cselect_b32 s50,s5,s50
v_accvgpr_write_b32 a243,v242
v_mul_u32_u24_e32 v251,768,v251
mvaa 60,44,108
s_cmp_eq_u32 s12,3
s_cselect_b32 s59,1280,s59
v_add_u32_e32 v242,30720,v252
v_lshl_add_u32 v249,v249,5,v246
mvaa 56,40,108
s_add_u32 s53,s53,s58
v_add_u32_e32 v246,v246,v251
v_accvgpr_write_b32 a244,v242
mvaa 48,40,104
dl 227,36,52,3072
maaa 148,36,104
s_add_u32 s54,s54,s58
v_add_u32_e32 v242,46080,v253
v_accvgpr_write_b32 a245,v242
maaa 156,36,108
s_add_u32 s52,s52,s60
v_add_u32_e32 v243,4096,v246
v_add_u32_e32 v242,0,v248
maaa 152,32,108
s_mov_b32 s63,32768
ta 214,236,20992
v_cndmask_b32_e64 v242,v228,v242,s[24:25]
maaa 144,32,104
s_mov_b32 s61,s98
ta 220,237,20480
v_cndmask_b32_e64 v242,v242,v243,s[26:27]
s_waitcnt lgkmcnt(2)
mvaa 0,32,112
s_mov_b32 s64,1
v_add_u32_e32 v243,101376,v246
v_and_b32_e32 v246,15,v247
mvaa 8,32,116
v_and_b32_e32 v246,12,v247
v_accvgpr_write_b32 a246,v242
mvaa 12,36,116
ta 222,237,20992
v_add_u32_e32 v242,48128,v249
mvaa 4,36,112
v_cndmask_b32_e64 v242,v229,v242,s[24:25]
v_lshlrev_b32_e32 v246,5,v246
mvaa 64,40,112
v_cndmask_b32_e64 v242,v242,v243,s[26:27]
v_accvgpr_write_b32 a247,v242
mvaa 72,40,116
v_add_u32_e32 v242,96256,v248
v_and_b32_e32 v248,3,v247
mvaa 76,44,116
v_cndmask_b32_e64 v242,v230,v242,s[24:25]
v_lshl_add_u32 v246,v248,3,v246
mvaa 68,44,112
v_and_b32_e32 v248,24,v247
v_accvgpr_write_b32 a248,v242
mvaa 128,48,112
v_add_u32_e32 v242,144384,v249
v_lshl_add_u32 v246,v248,2,v246
mvaa 136,48,116
v_and_b32_e32 v248,32,v247
v_cndmask_b32_e64 v242,v231,v242,s[24:25]
mvaa 140,52,116
v_add_u32_e32 v246,98304,v246
v_accvgpr_write_b32 a249,v242
mvaa 132,52,112
v_lshl_add_u32 v238,v248,5,v246
v_xor_b32_e32 v239,32,v238
mvaa 192,56,112
v_add_u32_e32 v232,32768,v232
v_add_u32_e32 v233,32768,v233
mvaa 200,56,116
mvaa 204,60,116
mvaa 196,60,112
s_sleep 15
s_waitcnt vmcnt(8)
s_waitcnt lgkmcnt(0)
s_barrier
L_v_head:
mvaa 212,28,208
ra 64,232,0
mvaa 212,60,212
s_add_u32 m0,s62,0
ta 192,234,32768
mvaa 220,60,220
dl 228,40,53,0
mvaa 220,28,216
ta 194,234,33280
s_nop 3
mvaa 216,24,216
ra 96,233,0
mvaa 216,56,220
ta 196,234,49152
mvaa 208,56,212
L_vs5:
s_cbranch_execz L_vs9
ta 198,234,49664
mvaa 208,24,208
ta 204,235,49152
mvaa 148,20,208
dl 229,40,53,1024
mvaa 148,52,212
s_add_u32 s53,s53,s57
ta 206,235,49664
mvaa 156,52,220
ta 200,235,32768
mvaa 156,20,216
ta 202,235,33280
mvaa 152,16,216
ra 68,232,2048
mvaa 152,48,220
ra 100,233,2048
mvaa 144,48,212
ra 72,232,4096
mvaa 144,16,208
dl 230,44,54,2048
mvaa 84,12,208
ra 104,233,4096
mvaa 84,44,212
ra 76,232,6144
mvaa 92,44,220
ra 108,233,6144
mvaa 92,12,216
ra 80,232,8192
mvaa 88,8,216
ra 112,233,8192
mvaa 88,40,220
dl 231,44,54,3072
mvaa 80,40,212
s_add_u32 s54,s54,s57
ra 84,232,10240
mvaa 80,8,208
ra 116,233,10240
mvaa 20,4,208
ra 88,232,12288
mvaa 20,36,212
ra 120,233,12288
mvaa 28,36,220
ra 92,232,14336
mvaa 28,4,216
s_add_u32 m0,s62,16384
ra 124,233,14336
mvaa 24,0,216
dl 228,40,53,0
mvaa 24,32,220
mvaa 16,32,212
mvaa 16,0,208
s_waitcnt lgkmcnt(8)
maaa 128,64,192
maaa 128,96,196
maaa 136,96,204
dl 229,40,53,1024
maaa 136,64,200
s_add_u32 s53,s53,s58
maaa 140,68,200
maaa 140,100,204
maaa 132,100,196
maaa 132,68,192
mvaa 32,72,192
mvaa 32,104,196
dl 230,44,54,2048
mvaa 40,104,204
mvaa 40,72,200
s_waitcnt lgkmcnt(0)
mvaa 44,76,200
ta 208,236,32768
mvaa 44,108,204
ta 210,236,33280
mvaa 36,108,196
ta 212,236,49152
mvaa 36,76,192
dl 231,44,54,3072
mvaa 96,80,192
s_add_u32 s54,s54,s58
ta 214,236,49664
mvaa 96,112,196
ta 220,237,49152
mvaa 104,112,204
ta 222,237,49664
mvaa 104,80,200
ta 216,237,32768
mvaa 108,84,200
ta 218,237,33280
mvaa 108,116,204
mvaa 100,116,196
s_add_u32 m0,s61,0
mvaa 100,84,192
dl 224,36,52,0
mvaa 160,88,192
mvaa 160,120,196
mvaa 168,120,204
mvaa 168,88,200
mvaa 172,92,200
mvaa 172,124,204
mvaa 164,124,196
dl 225,36,52,1024
mvaa 164,92,192
s_waitcnt lgkmcnt(0)
mvaa 180,92,208
mvaa 180,124,212
mvaa 188,124,220
ta 192,234,36864
mvaa 188,92,216
ta 194,234,37376
mvaa 184,88,216
ta 196,234,53248
mvaa 184,120,220
dl 226,36,52,2048
mvaa 176,120,212
ta 198,234,53760
mvaa 176,88,208
ta 204,235,53248
mvaa 116,84,208
ta 206,235,53760
mvaa 116,116,212
ta 200,235,36864
mvaa 124,116,220
ta 202,235,37376
mvaa 124,84,216
mvaa 120,80,216
dl 227,36,52,3072
mvaa 120,112,220
s_add_u32 s52,s52,s56
mvaa 112,112,212
mvaa 112,80,208
mvaa 52,76,208
mvaa 52,108,212
mvaa 60,108,220
mvaa 60,76,216
mvaa 56,72,216
s_add_u32 m0,s61,4096
mvaa 56,104,220
dl 224,36,52,0
mvaa 48,104,212
s_add_u32 s61,s63,s98
mvaa 48,72,208
s_cmp_ge_u32 s63,65536
s_cselect_b32 s99,-2,1
s_lshl_b32 s99,s99,15
maaa 148,68,208
s_add_u32 s63,s63,s99
v_add_u32_e32 v232,s99,v232
v_add_u32_e32 v233,s99,v233
maaa 148,100,212
maaa 156,100,220
maaa 156,68,216
maaa 152,64,216
dl 225,36,52,1024
maaa 152,96,220
maaa 144,96,212
s_waitcnt lgkmcnt(0)
maaa 144,64,208
mvaa 0,64,192
mvaa 0,96,196
mvaa 8,96,204
ta 208,236,36864
mvaa 8,64,200
ta 210,236,37376
mvaa 12,68,200
dl 226,36,52,2048
mvaa 12,100,204
ta 212,236,53248
mvaa 4,100,196
ta 214,236,53760
mvaa 4,68,192
ta 220,237,53248
mvaa 64,72,192
ta 222,237,53760
mvaa 64,104,196
ta 216,237,36864
mvaa 72,104,204
ta 218,237,37376
mvaa 72,72,200
dl 227,36,52,3072
mvaa 76,76,200
s_add_u32 s52,s52,s60
mvaa 76,108,204
mvaa 68,108,196
mvaa 68,76,192
mvaa 128,80,192
mvaa 128,112,196
buffer_load_dword a184,v241,s[48:51],s55 offen
mvaa 136,112,204
s_add_u32 s55,s55,s59
mvaa 136,80,200
mvaa 140,84,200
mvaa 140,116,204
mvaa 132,116,196
mvaa 132,84,192
buffer_load_dword a185,v241,s[48:51],s55 offen
mvaa 192,88,192
s_add_u32 s55,s55,s59
mvaa 192,120,196
mvaa 200,120,204
mvaa 200,88,200
mvaa 204,92,200
mvaa 204,124,204
s_cmp_eq_u32 s64,3
mvaa 196,124,196
s_waitcnt vmcnt(10) lgkmcnt(0)
s_barrier
mvaa 196,92,192
s_cbranch_scc1 L_v_exit
mvaa 212,92,208
ra 0,232,0
mvaa 212,124,212
s_add_u32 m0,s62,32768
ta 192,234,0
mvaa 220,124,220
dl 228,40,53,0
mvaa 220,92,216
ta 194,234,512
s_nop 3
mvaa 216,88,216
ra 32,233,0
mvaa 216,120,220
ta 196,234,16384
mvaa 208,120,212
ta 198,234,16896
mvaa 208,88,208
ta 204,235,16384
mvaa 148,84,208
dl 229,40,53,1024
mvaa 148,116,212
s_add_u32 s53,s53,s57
ta 206,235,16896
mvaa 156,116,220
ta 200,235,0
mvaa 156,84,216
ta 202,235,512
mvaa 152,80,216
ra 4,232,2048
mvaa 152,112,220
ra 36,233,2048
mvaa 144,112,212
ra 8,232,4096
mvaa 144,80,208
dl 230,44,54,2048
mvaa 84,76,208
ra 40,233,4096
mvaa 84,108,212
ra 12,232,6144
mvaa 92,108,220
ra 44,233,6144
mvaa 92,76,216
ra 16,232,8192
mvaa 88,72,216
ra 48,233,8192
mvaa 88,104,220
dl 231,44,54,3072
mvaa 80,104,212
s_add_u32 s54,s54,s57
ra 20,232,10240
mvaa 80,72,208
ra 52,233,10240
mvaa 20,68,208
ra 24,232,12288
mvaa 20,100,212
ra 56,233,12288
mvaa 28,100,220
ra 28,232,14336
mvaa 28,68,216
s_add_u32 m0,s62,49152
ra 60,233,14336
mvaa 24,64,216
dl 228,40,53,0
mvaa 24,96,220
mvaa 16,96,212
mvaa 16,64,208
s_waitcnt lgkmcnt(8)
maaa 128,0,192
maaa 128,32,196
maaa 136,32,204
dl 229,40,53,1024
maaa 136,0,200
s_add_u32 s53,s53,s58
maaa 140,4,200
maaa 140,36,204
maaa 132,36,196
maaa 132,4,192
mvaa 32,8,192
mvaa 32,40,196
dl 230,44,54,2048
mvaa 40,40,204
mvaa 40,8,200
s_waitcnt lgkmcnt(0)
mvaa 44,12,200
ta 208,236,0
mvaa 44,44,204
ta 210,236,512
mvaa 36,44,196
ta 212,236,16384
mvaa 36,12,192
dl 231,44,54,3072
mvaa 96,16,192
s_add_u32 s54,s54,s58
ta 214,236,16896
mvaa 96,48,196
ta 220,237,16384
mvaa 104,48,204
ta 222,237,16896
mvaa 104,16,200
ta 216,237,0
mvaa 108,20,200
ta 218,237,512
mvaa 108,52,204
mvaa 100,52,196
s_add_u32 m0,s61,0
mvaa 100,20,192
dl 224,36,52,0
mvaa 160,24,192
s_lshl_b32 s86,s21,2
s_add_u32 s86,s86,s12
s_mul_i32 s86,s86,384
mvaa 160,56,196
s_add_u32 s86,s86,4096
v_lshl_add_u32 v253,v244,3,s86
mvaa 168,56,204
s_mov_b64 exec,7
global_load_dword a188,v253,s[96:97]
s_mov_b64 exec,-1
mvaa 168,24,200
mvaa 172,28,200
mvaa 172,60,204
mvaa 164,60,196
dl 225,36,52,1024
mvaa 164,28,192
s_waitcnt lgkmcnt(0)
mvaa 180,28,208
mvaa 180,60,212
mvaa 188,60,220
ta 192,234,4096
mvaa 188,28,216
ta 194,234,4608
mvaa 184,24,216
ta 196,234,20480
mvaa 184,56,220
dl 226,36,52,2048
mvaa 176,56,212
ta 198,234,20992
mvaa 176,24,208
ta 204,235,20480
mvaa 116,20,208
ta 206,235,20992
mvaa 116,52,212
ta 200,235,4096
mvaa 124,52,220
ta 202,235,4608
mvaa 124,20,216
mvaa 120,16,216
dl 227,36,52,3072
mvaa 120,48,220
s_add_u32 s52,s52,s56
mvaa 112,48,212
mvaa 112,16,208
mvaa 52,12,208
mvaa 52,44,212
s_bitcmp1_b32 s65,s64
s_cbranch_scc1 L_v_ev
L_v_evr:
mvaa 60,44,220
mvaa 60,12,216
mvaa 56,8,216
s_add_u32 m0,s61,4096
mvaa 56,40,220
dl 224,36,52,0
mvaa 48,40,212
s_add_u32 s61,s63,s98
mvaa 48,8,208
s_cmp_ge_u32 s63,65536
s_cselect_b32 s99,-2,1
s_lshl_b32 s99,s99,15
maaa 148,4,208
s_add_u32 s63,s63,s99
v_add_u32_e32 v232,s99,v232
v_add_u32_e32 v233,s99,v233
maaa 148,36,212
maaa 156,36,220
maaa 156,4,216
maaa 152,0,216
dl 225,36,52,1024
maaa 152,32,220
maaa 144,32,212
s_waitcnt lgkmcnt(0)
maaa 144,0,208
mvaa 0,0,192
mvaa 0,32,196
mvaa 8,32,204
ta 208,236,4096
mvaa 8,0,200
ta 210,236,4608
mvaa 12,4,200
dl 226,36,52,2048
mvaa 12,36,204
ta 212,236,20480
mvaa 4,36,196
ta 214,236,20992
mvaa 4,4,192
ta 220,237,20480
mvaa 64,8,192
ta 222,237,20992
mvaa 64,40,196
ta 216,237,4096
mvaa 72,40,204
ta 218,237,4608
mvaa 72,8,200
dl 227,36,52,3072
mvaa 76,12,200
s_add_u32 s52,s52,s60
mvaa 76,44,204
mvaa 68,44,196
mvaa 68,12,192
mvaa 128,16,192
mvaa 128,48,196
buffer_load_dword a187,v241,s[48:51],s55 offen
mvaa 136,48,204
s_add_u32 s55,s55,s59
mvaa 136,16,200
mvaa 140,20,200
mvaa 140,52,204
mvaa 132,52,196
mvaa 132,20,192
buffer_load_dword a190,v241,s[48:51],s55 offen
mvaa 192,24,192
s_add_u32 s55,s55,s59
mvaa 192,56,196
mvaa 200,56,204
s_add_u32 s64,s64,1
mvaa 200,24,200
mvaa 204,28,200
mvaa 204,60,204
mvaa 196,60,196
s_waitcnt vmcnt(10) lgkmcnt(0)
s_barrier
mvaa 196,28,192
s_branch L_v_head
L_v_exit:
global_load_dwordx4 a[160:163],v244,s[30:31] offset:0
s_add_u32 m0,s62,32768
mvaa 212,92,208
s_cmp_lt_u32 s12,2
s_cselect_b32 s86,s22,s23
ta 32,234,0
s_and_b32 s87,s12,1
mvaa 212,124,212
s_lshl_b32 s86,s86,11
ta 34,234,512
s_lshl_b32 s87,s87,17
mvaa 220,124,220
s_add_u32 s86,s86,s87
ra 0,232,0
s_mov_b64 s[36:37],s[68:69]
mvaa 220,92,216
s_mov_b32 s38,4194304
ta 36,235,0
s_mov_b32 s52,s86
mvaa 216,88,216
s_mov_b32 s56,65536
ta 38,235,512
v_accvgpr_read_b32 v224,a242
mvaa 216,120,220
dl 228,40,53,0
mvaa 208,120,212
s_lshl_b32 s88,s18,1
ra 4,232,2048
v_accvgpr_read_b32 v225,a243
mvaa 208,88,208
s_add_u32 s88,s88,s12
ra 8,232,4096
v_accvgpr_read_b32 v226,a244
mvaa 148,84,208
s_mov_b32 s91,196608
ra 12,232,6144
v_accvgpr_read_b32 v227,a245
mvaa 148,116,212
s_lshl_b32 s88,s88,7
ra 16,232,8192
s_lshl_b32 s89,s18,6
mvaa 156,116,220
s_mov_b32 s90,6291456
ra 20,232,10240
s_mov_b32 s60,-65408
mvaa 156,84,216
dl 229,40,53,1024
mvaa 152,80,216
s_add_u32 s53,s53,s57
ra 24,232,12288
s_bfe_u32 s4,s21,65536
mvaa 152,112,220
s_lshl_b32 s4,s4,6
ra 28,232,14336
s_lshl_b32 s87,s20,2
mvaa 144,112,212
s_mov_b32 s26,32768
ta 40,236,0
s_mov_b32 s27,131072
mvaa 144,80,208
s_add_u32 s61,s98,32768
ta 42,236,512
s_mov_b32 s63,65536
mvaa 84,76,208
s_cmp_ge_u32 s63,65536
s_cselect_b32 s99,-2,1
ta 44,237,0
s_lshl_b32 s99,s99,15
mvaa 84,108,212
dl 230,44,54,2048
mvaa 92,108,220
s_cmp_eq_u32 s12,0
s_cselect_b32 s65,96,0
ta 46,237,512
s_mov_b32 s67,2048
mvaa 92,76,216
s_mov_b32 s66,8
ta 48,234,4096
s_mov_b32 s64,1
mvaa 88,72,216
s_or_b32 s65,s65,128
ta 50,234,4608
mvaa 88,104,220
s_bitcmp1_b32 s12,0
s_cselect_b64 vcc,-1,0
ta 52,235,4096
mvaa 80,104,212
ta 54,235,4608
mvaa 80,72,208
dl 231,44,54,3072
s_add_u32 m0,s62,49152
mvaa 20,68,208
s_add_u32 s54,s54,s57
ta 56,236,4096
mvaa 20,100,212
s_cmp_lt_u32 s12,2
s_cselect_b32 s57,262144,s91
ta 58,236,4608
mvaa 28,100,220
ta 60,237,4096
mvaa 28,68,216
ta 62,237,4608
mvaa 24,64,216
mvaa 24,96,220
)"
R"(dl 228,40,53,0
mvaa 16,96,212
v_accvgpr_read_b32 v228,a246
mvaa 16,64,208
s_waitcnt lgkmcnt(5)
maaa 128,0,32
maaa 136,0,36
L_vs6:
s_cbranch_execz L_vs10
maaa 140,4,36
dl 229,40,53,1024
maaa 132,4,32
s_waitcnt lgkmcnt(0)
s_barrier
mvaa 32,8,32
s_add_u32 s53,s53,s58
ta 96,234,16384
v_accvgpr_read_b32 v229,a247
mvaa 40,8,36
s_cmp_lt_u32 s12,2
s_cselect_b32 s42,8388608,s90
ta 98,234,16896
mvaa 44,12,36
s_cmp_lt_u32 s12,2
s_cselect_b32 s53,s88,s89
ra 64,233,0
s_and_b32 s89,s98,8192
mvaa 36,12,32
dl 230,44,54,2048
mvaa 96,16,32
s_bitcmp1_b32 s21,0
s_cselect_b32 s90,s57,0
ta 100,235,16384
s_add_u32 s53,s53,s90
mvaa 104,16,36
s_cmp_lt_u32 s12,2
s_cselect_b64 s[40:41],s[72:73],s[80:81]
ta 102,235,16896
v_accvgpr_read_b32 v230,a248
mvaa 108,20,36
s_lshl_b32 s90,s90,1
ra 68,233,2048
s_add_u32 s91,s89,65536
mvaa 100,20,32
s_bfe_u32 s88,s21,65539
ra 72,233,4096
s_lshl_b32 s88,s88,1
mvaa 160,24,32
dl 231,44,54,3072
mvaa 168,24,36
s_add_u32 m0,s98,65536
ra 76,233,6144
s_add_u32 s54,s54,s58
mvaa 172,28,36
s_cmp_eq_u32 s12,3
s_cselect_b64 s[44:45],s[68:69],s[40:41]
ra 80,233,8192
v_accvgpr_read_b32 v231,a249
mvaa 164,28,32
s_cmp_eq_u32 s12,3
s_cselect_b32 s46,0,s42
ra 84,233,10240
s_add_u32 s58,s57,s90
mvaa 180,28,40
s_cmp_eq_u32 s12,3
s_cselect_b32 s54,0,s53
ra 88,233,12288
s_sub_u32 s57,s57,s90
mvaa 188,28,44
global_load_dwordx4 a[168:171],v244,s[30:31] offset:2048
mvaa 184,24,44
s_add_u32 s90,s89,65536
ta 104,236,16384
s_add_u32 s88,s88,s87
mvaa 176,24,40
s_and_b32 s87,s12,1
ra 92,233,14336
s_add_u32 s88,s88,s87
mvaa 116,20,40
s_mul_i32 s88,s88,524288
ta 106,236,16896
s_and_b32 s87,s21,7
mvaa 124,20,44
s_add_u32 s86,s88,33554432
ta 108,237,16384
s_lshl_b32 s87,s87,16
mvaa 120,16,44
global_load_dwordx4 a[176:179],v244,s[84:85] offset:0
mvaa 112,16,40
s_add_u32 s86,s86,s87
ta 110,237,16896
s_lshr_b32 s87,s12,1
mvaa 52,12,40
s_lshl_b32 s87,s87,15
ta 112,234,20480
s_add_u32 s86,s86,s87
mvaa 60,12,44
s_add_u32 s24,s8,s86
s_addc_u32 s25,s9,0
ta 114,234,20992
s_lshl_b32 s86,%1,5
mvaa 56,8,44
s_lshl_b32 s87,s12,3
ta 116,235,20480
s_add_u32 s86,s86,s87
mvaa 48,8,40
global_load_dwordx4 a[164:167],v244,s[30:31] offset:1024
maaa 148,4,40
s_add_u32 s86,s86,83886080
ta 118,235,20992
s_mov_b32 s88,4096
maaa 156,4,44
ta 120,236,20480
maaa 152,0,44
ta 122,236,20992
maaa 144,0,40
ta 124,237,20480
mvaa 0,0,48
global_load_dwordx4 a[172:175],v244,s[30:31] offset:3072
mvaa 8,0,52
s_mov_b32 s30,1048577
ta 126,237,20992
s_mov_b32 s31,268435712
mvaa 12,4,52
mvaa 4,4,48
mvaa 64,8,48
mvaa 72,8,52
global_load_dwordx4 a[180:183],v244,s[84:85] offset:1024
mvaa 76,12,52
mvaa 68,12,48
mvaa 128,16,48
mvaa 136,16,52
mvaa 140,20,52
dl 224,36,52,0
mvaa 132,20,48
mvaa 192,24,48
mvaa 200,24,52
mvaa 204,28,52
mvaa 196,28,48
dl 225,36,52,1024
mvaa 212,28,56
mvaa 220,28,60
mvaa 216,24,60
mvaa 208,24,56
mvaa 148,20,56
dl 226,36,52,2048
mvaa 156,20,60
mvaa 152,16,60
mvaa 144,16,56
mvaa 84,12,56
s_waitcnt vmcnt(12) lgkmcnt(0)
s_barrier
mvaa 92,12,60
dl 227,36,52,3072
mvaa 88,8,60
s_add_u32 s52,s52,s56
s_add_u32 m0,s98,69632
ta 32,234,32768
mvaa 80,8,56
ta 34,234,33280
mvaa 20,4,56
ta 36,235,32768
mvaa 28,4,60
ta 38,235,33280
mvaa 24,0,60
dl 224,36,52,0
mvaa 16,0,56
ta 40,236,32768
maaa 128,64,96
ta 42,236,33280
maaa 136,64,100
ta 44,237,32768
maaa 140,68,100
ta 46,237,33280
maaa 132,68,96
dl 225,36,52,1024
mvaa 32,72,96
ta 48,234,36864
mvaa 40,72,100
ta 50,234,37376
mvaa 44,76,100
ta 52,235,36864
mvaa 36,76,96
ta 54,235,37376
mvaa 96,80,96
dl 226,36,52,2048
mvaa 104,80,100
ra 20,232,43008
mvaa 108,84,100
ra 0,232,32768
mvaa 100,84,96
ra 24,232,45056
mvaa 160,88,96
ra 4,232,34816
mvaa 168,88,100
dl 227,36,52,3072
mvaa 172,92,100
s_add_u32 m0,s62,0
ra 28,232,47104
s_add_u32 s52,s52,s60
mvaa 164,92,96
ra 8,232,36864
mvaa 180,92,104
ra 12,232,38912
mvaa 188,92,108
ra 16,232,40960
v_add_u32_e32 v232,s90,v232
mvaa 184,88,108
dl 228,40,53,0
mvaa 176,88,104
ta 60,237,36864
v_xor_b32_e32 v232,s4,v232
mvaa 116,84,104
ta 62,237,37376
mvaa 124,84,108
ta 56,236,36864
mvaa 120,80,108
ta 58,236,37376
mvaa 112,80,104
dl 229,40,53,1024
mvaa 52,76,104
s_add_u32 s53,s53,s57
mvaa 60,76,108
mvaa 56,72,108
mvaa 48,72,104
maaa 148,68,104
dl 230,44,54,2048
maaa 156,68,108
maaa 152,64,108
maaa 144,64,104
mvaa 0,64,112
mvaa 8,64,116
dl 231,44,54,3072
mvaa 12,68,116
s_add_u32 m0,s62,16384
s_add_u32 s54,s54,s57
mvaa 4,68,112
mvaa 64,72,112
mvaa 72,72,116
mvaa 76,76,116
dl 228,40,53,0
mvaa 68,76,112
mvaa 128,80,112
mvaa 136,80,116
mvaa 140,84,116
mvaa 132,84,112
dl 229,40,53,1024
mvaa 192,88,112
s_add_u32 s53,s53,s58
mvaa 200,88,116
mvaa 204,92,116
mvaa 196,92,112
mvaa 212,92,120
dl 230,44,54,2048
mvaa 220,92,124
mvaa 216,88,124
s_waitcnt vmcnt(20) lgkmcnt(0)
s_barrier
mvaa 208,88,120
ra 92,233,47104
mvaa 148,84,120
ta 96,234,49152
mvaa 156,84,124
dl 231,44,54,3072
mvaa 152,80,124
s_add_u32 m0,s98,0
ra 88,233,45056
s_add_u32 s54,s54,s58
mvaa 144,80,120
ta 98,234,49664
mvaa 84,76,120
ta 100,235,49152
mvaa 92,76,124
ta 116,235,53248
mvaa 88,72,124
dl 224,36,52,0
mvaa 80,72,120
ta 118,235,53760
mvaa 20,68,120
ta 102,235,49664
v_subrev_u32_e32 v235,s89,v235
mvaa 28,68,124
ta 104,236,49152
mvaa 24,64,124
ta 112,234,53248
mvaa 16,64,120
dl 225,36,52,1024
maaa 128,0,32
ta 124,237,53248
maaa 136,0,36
ta 126,237,53760
maaa 140,4,36
ta 120,236,53248
maaa 132,4,32
ta 122,236,53760
mvaa 32,8,32
dl 226,36,52,2048
mvaa 40,8,36
ta 114,234,53760
v_subrev_u32_e32 v234,s89,v234
mvaa 44,12,36
ta 106,236,49664
v_subrev_u32_e32 v236,s89,v236
mvaa 36,12,32
ta 108,237,49152
mvaa 52,12,40
ta 110,237,49664
v_subrev_u32_e32 v237,s89,v237
mvaa 60,12,44
dl 227,36,52,3072
mvaa 56,8,44
s_add_u32 s52,s52,s56
s_add_u32 m0,s98,4096
ra 84,233,43008
mvaa 48,8,40
ra 80,233,40960
maaa 148,4,40
ra 72,233,36864
maaa 156,4,44
ra 76,233,38912
maaa 152,0,44
dl 224,36,52,0
maaa 144,0,40
ra 68,233,34816
mvaa 0,0,48
ra 64,233,32768
v_add_u32_e32 v233,s91,v233
mvaa 8,0,52
v_xor_b32_e32 v233,s4,v233
mvaa 12,4,52
mvaa 4,4,48
dl 225,36,52,1024
mvaa 20,4,56
mvaa 28,4,60
mvaa 24,0,60
mvaa 16,0,56
mvaa 220,28,60
dl 226,36,52,2048
s_waitcnt lgkmcnt(2)
mvaa 220,92,124
mvaa 216,88,124
mvaa 216,24,60
mvaa 212,28,56
mvaa 212,92,120
dl 227,36,52,3072
mvaa 208,88,120
s_add_u32 m0,s62,32768
s_add_u32 s52,s52,s60
s_waitcnt vmcnt(25)
zvaa 246,160,176
mvaa 208,24,56
cv 223,222,223
cv 222,220,221
mvaa 204,28,52
cv 221,218,219
zvaa 250,168,176
mvaa 204,92,116
cv 220,216,217
cv 219,214,215
mvaa 200,88,116
dl 228,40,53,0
s_waitcnt vmcnt(25)
mvaa 246,164,180
mvaa 200,24,52
cv 218,212,213
mvaa 250,172,180
mvaa 196,28,48
cv 217,210,211
cv 216,208,209
mvaa 196,92,112
cv 215,206,207
v_cndmask_b32_e64 v246,0,v246,s[30:31]
mvaa 192,88,112
cv 214,204,205
v_cndmask_b32_e64 v250,0,v250,s[30:31]
mvaa 192,24,48
dl 229,40,53,1024
s_lshl_b64 s[30:31],s[30:31],1
v_cndmask_b32_e64 v246,v246,v247,s[30:31]
v_cndmask_b32_e64 v250,v250,v251,s[30:31]
mvaa 188,28,44
s_lshl_b64 s[30:31],s[30:31],1
s_add_u32 s53,s53,s57
v_cndmask_b32_e64 v246,v246,v248,s[30:31]
v_cndmask_b32_e64 v250,v250,v252,s[30:31]
mvaa 188,92,108
s_lshl_b64 s[30:31],s[30:31],1
cv 213,202,203
v_cndmask_b32_e64 v246,v246,v249,s[30:31]
mvaa 184,88,108
v_cndmask_b32_e64 v250,v250,v253,s[30:31]
v_add_f32_dpp v246,v246,v246 quad_perm:[1,0,3,2] row_mask:0xf bank_mask:0xf
mvaa 184,24,44
v_add_f32_dpp v250,v250,v250 quad_perm:[1,0,3,2] row_mask:0xf bank_mask:0xf
v_add_f32_dpp v246,v246,v246 quad_perm:[2,3,0,1] row_mask:0xf bank_mask:0xf
mvaa 180,28,40
dl 230,44,54,2048
v_add_f32_dpp v250,v250,v250 quad_perm:[2,3,0,1] row_mask:0xf bank_mask:0xf
mvaa 180,92,104
v_add_f32_dpp v246,v246,v246 row_half_mirror row_mask:0xf bank_mask:0xf
v_add_f32_dpp v250,v250,v250 row_half_mirror row_mask:0xf bank_mask:0xf
mvaa 176,88,104
v_add_f32_dpp v246,v246,v246 row_mirror row_mask:0xf bank_mask:0xf
v_add_f32_dpp v250,v250,v250 row_mirror row_mask:0xf bank_mask:0xf
mvaa 176,24,40
cv 212,200,201
cv 211,198,199
mvaa 172,28,36
v_permlane16_swap_b32_e32 v246,v250
mvaa 172,92,100
dl 231,44,54,3072
v_add_f32_e32 v246,v246,v250
v_mov_b32_e32 v250,v246
mvaa 168,88,100
s_add_u32 s54,s54,s57
v_permlane32_swap_b32_e32 v246,v250
mvaa 168,24,36
v_add_f32_e32 v246,v246,v250
v_lshrrev_b32_e32 v250,6,v244
mvaa 164,28,32
v_mul_f32_e32 v246,0xbfb8aa3b,v246
cv 210,196,197
mvaa 164,92,96
ex 246
mvaa 160,88,96
buffer_load_dword a242,v241,s[48:51],s55 offen
mvaa 160,24,32
s_add_u32 s55,s55,s59
v_add_f32_e32 v246,1.0,v246
cv 209,194,195
mvaa 156,20,60
v_rcp_f32_e32 v246,v246
mvaa 156,84,124
s_waitcnt vmcnt(17) lgkmcnt(0)
s_barrier
s_add_u32 m0,s62,49152
mvaa 152,80,124
cv 208,192,193
cv 207,190,191
mvaa 152,16,60
dl 228,40,53,0
mvaa 148,20,56
cv 206,188,189
cv 205,186,187
mvaa 148,84,120
cv 204,184,185
cv 203,182,183
mvaa 144,80,120
cv 202,180,181
cv 201,178,179
mvaa 144,16,56
cv 200,176,177
cv 199,174,175
mvaa 140,20,52
dl 229,40,53,1024
mvaa 140,84,116
s_add_u32 s53,s53,s58
cv 198,172,173
cv 197,170,171
mvaa 136,80,116
ra 4,232,0
cv 196,168,169
mvaa 136,16,52
ta 92,239,12288
cv 195,166,167
mvaa 132,20,48
ta 94,239,12800
cv 194,164,165
mvaa 132,84,112
dl 230,44,54,2048
mvaa 128,80,112
ta 88,238,12288
cv 193,162,163
mvaa 128,16,48
ta 90,238,12800
cv 192,160,161
mvaa 124,20,44
cv 191,158,159
cv 190,156,157
mvaa 124,84,108
cv 189,154,155
cv 188,152,153
mvaa 120,80,108
dl 231,44,54,3072
mvaa 120,16,44
s_add_u32 m0,s98,32768
cv 187,150,151
cv 186,148,149
mvaa 116,20,40
s_add_u32 s54,s54,s58
cv 185,146,147
cv 184,144,145
mvaa 116,84,104
cv 183,142,143
cv 182,140,141
mvaa 112,80,104
cv 181,138,139
cv 180,136,137
mvaa 112,16,40
dl 224,36,52,0
mvaa 108,20,36
cv 179,134,135
cv 178,132,133
mvaa 108,84,100
cv 177,130,131
cv 176,128,129
mvaa 104,80,100
cv 175,126,127
cv 174,124,125
mvaa 104,16,36
ta 44,235,0
cv 173,122,123
mvaa 100,20,32
dl 225,36,52,1024
mvaa 100,84,96
ta 46,235,512
cv 172,120,121
mvaa 96,80,96
cv 171,118,119
cv 170,116,117
mvaa 96,16,32
ta 40,234,0
cv 169,114,115
mvaa 92,12,60
ta 42,234,512
cv 168,112,113
mvaa 92,76,124
dl 226,36,52,2048
mvaa 88,72,124
cv 167,110,111
cv 166,108,109
mvaa 88,8,60
cv 165,106,107
cv 164,104,105
mvaa 84,12,56
ta 84,237,8192
cv 163,102,103
mvaa 84,76,120
ta 80,236,8192
cv 162,100,101
mvaa 80,72,120
dl 227,36,52,3072
mvaa 80,8,56
s_add_u32 m0,s62,0
ta 86,237,8704
cv 161,98,99
mvaa 76,12,52
s_add_u32 s52,s52,s56
ta 82,236,8704
cv 160,96,97
mvaa 76,76,116
ra 16,232,6144
cv 159,94,95
mvaa 72,72,116
ta 60,235,4096
cv 158,92,93
mvaa 72,8,52
buffer_load_dword a243,v241,s[48:51],s55 offen
mvaa 68,12,48
s_add_u32 s55,s55,s59
ta 56,234,4096
cv 151,78,79
mvaa 68,76,112
L_vs7:
s_cbranch_execz L_vs11
ta 62,235,4608
cv 150,76,77
mvaa 64,72,112
ta 58,234,4608
cv 157,90,91
mvaa 64,8,48
ta 52,237,0
cv 156,88,89
mvaa 60,76,108
buffer_load_dword a244,v241,s[48:51],s55 offen
mvaa 56,72,108
s_add_u32 s55,s55,s59
ra 12,232,4096
cv 147,70,71
mvaa 52,76,104
ta 54,237,512
cv 146,68,69
mvaa 48,72,104
ra 8,232,2048
cv 145,66,67
mvaa 44,76,100
ta 48,236,0
cv 144,64,65
mvaa 40,72,100
buffer_load_dword a245,v241,s[48:51],s55 offen
mvaa 36,76,96
s_add_u32 s55,s55,s59
ta 50,236,512
cv 143,62,63
mvaa 32,72,96
cv 142,60,61
cv 141,58,59
mvaa 28,68,124
cv 140,56,57
cv 139,54,55
mvaa 24,64,124
cv 138,52,53
cv 137,50,51
mvaa 20,68,120
s_mov_b64 exec,65537
buffer_store_dword v246,v250,s[8:11],s86 offen sc0 sc1
s_mov_b64 exec,-1
mvaa 16,64,120
cv 136,48,49
cv 135,46,47
mvaa 12,68,116
cv 134,44,45
cv 133,42,43
mvaa 8,64,116
ta 76,235,8192
cv 132,40,41
mvaa 4,68,112
ta 78,235,8704
cv 131,38,39
mvaa 0,64,112
ta 72,234,8192
cv 130,36,37
maaa 156,68,108
cv 129,34,35
cv 128,32,33
maaa 152,64,108
ta 74,234,8704
cv 127,30,31
maaa 148,68,104
cv 126,28,29
cv 125,26,27
maaa 144,64,104
v_accvgpr_read_b32 v63,a159
v_accvgpr_read_b32 v62,a158
maaa 140,68,100
v_accvgpr_read_b32 v61,a157
v_accvgpr_read_b32 v60,a156
maaa 136,64,100
v_accvgpr_read_b32 v59,a155
v_accvgpr_read_b32 v58,a154
maaa 132,68,96
v_accvgpr_read_b32 v57,a153
v_accvgpr_read_b32 v56,a152
maaa 128,64,96
cv 56,56,57
cv 57,58,59
s_waitcnt lgkmcnt(3)
zaaa 236,40,4
ta 68,237,4096
ta 70,237,4608
zaaa 220,44,4
ta 64,236,4096
ta 66,236,4608
zaaa 216,44,8
cv 58,60,61
cv 59,62,63
zaaa 232,40,8
buffer_store_dwordx4 v[56:59],v244,s[24:27],0 offen offset:3072 sc0 sc1
zaaa 228,40,12
v_accvgpr_read_b32 v55,a151
v_accvgpr_read_b32 v54,a150
zaaa 212,44,12
v_accvgpr_read_b32 v53,a149
v_accvgpr_read_b32 v52,a148
zaaa 208,44,16
v_accvgpr_read_b32 v51,a147
v_accvgpr_read_b32 v50,a146
zaaa 224,40,16
v_accvgpr_read_b32 v49,a145
v_accvgpr_read_b32 v48,a144
zaaa 192,48,16
cv 48,48,49
cv 49,50,51
zaaa 176,52,16
cv 50,52,53
cv 51,54,55
zaaa 180,52,12
buffer_store_dwordx4 v[48:51],v244,s[24:27],0 offen offset:2048 sc0 sc1
zaaa 196,48,12
s_waitcnt vmcnt(27) lgkmcnt(0)
s_barrier
ra 24,233,0
v_accvgpr_read_b32 v47,a143
zaaa 200,48,8
ra 28,233,2048
v_accvgpr_read_b32 v46,a142
v_accvgpr_read_b32 v45,a141
zaaa 184,52,8
ra 32,233,4096
ra 36,233,6144
zaaa 188,52,4
v_accvgpr_read_b32 v43,a139
v_accvgpr_read_b32 v42,a138
zaaa 204,48,4
dl 228,40,53,0
ta 96,236,24576
zaaa 172,56,4
ta 98,236,25088
v_accvgpr_read_b32 v41,a137
zaaa 156,60,4
ta 100,237,24576
v_accvgpr_read_b32 v39,a135
zaaa 152,60,8
ta 102,237,25088
ta 104,238,28672
zaaa 168,56,8
v_accvgpr_read_b32 v38,a134
v_accvgpr_read_b32 v35,a131
zaaa 164,56,12
dl 229,40,53,1024
ta 106,238,29184
zaaa 148,60,12
s_add_u32 s53,s53,s57
ta 108,239,28672
v_accvgpr_read_b32 v34,a130
zaaa 144,60,16
ta 110,239,29184
v_accvgpr_read_b32 v33,a129
zaaa 160,56,16
ta 44,235,16384
ta 46,235,16896
ta 40,234,16384
ta 42,234,16896
ta 52,237,16384
ta 54,237,16896
ta 48,236,16384
ta 50,236,16896
ta 60,235,20480
ta 62,235,20992
ta 56,234,20480
ta 58,234,20992
v_accvgpr_read_b32 v32,a128
zaaa 128,64,16
v_accvgpr_read_b32 v37,a133
v_accvgpr_read_b32 v36,a132
zaaa 112,68,16
dl 230,44,54,2048
zaaa 116,68,12
v_accvgpr_read_b32 v40,a136
v_accvgpr_read_b32 v44,a140
zaaa 132,64,12
cv 251,42,43
cv 250,40,41
zaaa 136,64,8
cv 253,46,47
cv 252,44,45
zaaa 120,68,8
cv 249,38,39
cv 247,34,35
zaaa 124,68,4
dl 231,44,54,3072
zaaa 140,64,4
s_add_u32 m0,s61,4096
cv 246,32,33
cv 248,36,37
zvaa 40,72,4
s_add_u32 s54,s54,s57
ta 68,237,20480
cv 124,24,25
zvaa 44,76,4
s_add_u32 s61,s63,s98
ta 70,237,20992
cv 123,22,23
zvaa 52,76,8
s_add_u32 s63,s63,s99
ta 64,236,20480
cv 121,18,19
zvaa 48,72,8
dl 224,36,52,0
zvaa 56,72,12
ta 66,236,20992
cv 120,16,17
zvaa 60,76,12
cv 122,20,21
cv 119,14,15
zvaa 68,76,16
cv 118,12,13
cv 117,10,11
zvaa 64,72,16
cv 116,8,9
cv 153,82,83
zvaa 32,80,16
dl 225,36,52,1024
ta 76,235,24576
ta 78,235,25088
zvaa 36,84,16
ta 72,234,24576
ta 74,234,25088
zvaa 28,84,12
cv 152,80,81
cv 155,86,87
zvaa 24,80,12
cv 154,84,85
cv 149,74,75
zvaa 16,80,8
cv 148,72,73
v_add_u32_e32 v232,s99,v232
zvaa 20,84,8
dl 226,36,52,2048
zvaa 12,84,4
cv 115,6,7
cv 114,4,5
zvaa 8,80,4
cv 113,2,3
cv 112,0,1
zvaa 76,4,88
zvaa 96,4,92
zvaa 100,8,92
dl 227,36,52,3072
zvaa 80,8,88
s_add_u32 s52,s52,s60
zvaa 84,12,88
zvaa 104,12,92
zvaa 108,16,92
s_waitcnt vmcnt(23) lgkmcnt(0)
s_barrier
zvaa 88,16,88
buffer_store_dwordx4 v[250:253],v244,s[24:27],0 offen offset:1024 sc0 sc1
buffer_store_dwordx4 v[246:249],v244,s[24:27],0 offen offset:0 sc0 sc1
L_kvx:
L_vs8:
s_cbranch_execz L_vs12
L_p_exit:
s_add_u32 m0,s62,16384
ra 4,232,0
s_lshl_b32 s86,s21,2
maaa 236,40,24
dl 228,40,53,0
maaa 220,44,24
s_add_u32 s86,s86,s12
ra 8,232,2048
s_lshl_b32 s86,s86,8
maaa 216,44,28
s_add_u32 s86,s86,L_p_exit-L_att_pc+2048
ra 12,232,4096
v_lshl_add_u32 v253,v244,3,s86
maaa 232,40,28
s_mov_b32 s67,4096
ra 16,232,6144
v_add_u32_e32 v233,0xffff0000,v233
maaa 228,40,32
s_cmp_lt_u32 s12,2
s_cselect_b32 s94,s22,s23
ra 80,233,0
v_mov_b32_e32 v248,-1
maaa 212,44,32
dl 229,40,53,1024
maaa 208,44,36
s_lshr_b32 s94,s94,6
ra 84,233,2048
v_mov_b32_e32 v249,-1
maaa 224,40,36
s_and_b32 s87,s12,1
ra 88,233,4096
v_mov_b32_e32 v250,-1
maaa 192,48,36
s_add_u32 s94,s94,s87
ra 92,233,6144
v_mov_b32_e32 v251,-1
maaa 176,52,36
s_lshl_b32 s87,s18,5
ta 0,234,32768
s_add_u32 s94,s94,s87
maaa 180,52,32
dl 230,44,54,2048
maaa 196,48,32
s_lshl_b32 s94,s94,11
ta 2,234,33280
s_add_u32 s94,s94,12582912
maaa 200,48,28
s_add_u32 s53,s53,s58
ta 40,235,32768
s_bfe_u32 s88,s21,65539
maaa 184,52,28
s_lshl_b32 s87,s20,1
ta 42,235,33280
s_add_u32 s88,s88,s87
maaa 188,52,24
s_lshl_b32 s88,s88,7
v_mov_b32_e32 v247,1
s_add_u32 s85,s88,83894272
maaa 204,48,24
dl 231,44,54,3072
maaa 172,56,24
s_add_u32 m0,s98,65536
ta 52,236,32768
s_add_u32 s54,s54,s58
maaa 156,60,24
s_lshl_b32 s91,s12,4
ta 54,236,33280
v_bfe_u32 v252,v244,4,4
maaa 152,60,28
s_cmp_lt_u32 s12,2
s_cselect_b32 s90,s22,s23
v_bfe_u32 v241,v244,6,4
v_and_b32_e32 v241,12,v241
maaa 168,56,28
s_mul_i32 s33,s90,10240
v_sub_u32_e32 v252,v252,v241
v_add_u32_e32 v252,s91,v252
maaa 164,56,32
s_mov_b64 exec,3
global_load_dword a250,v253,s[96:97]
s_mov_b64 exec,-1
maaa 148,60,32
s_cmp_lt_u32 s12,2
s_cselect_b32 s91,32768,0
v_add_u32_e32 v253,10240,v253
s_add_u32 s91,s91,98304
maaa 144,60,36
s_mov_b32 s64,0x55505555
v_accvgpr_read_b32 v255,a241
v_mov_b32_e32 v241,0xf149f2ca
maaa 160,56,36
s_mov_b32 s65,0x50005500
s_lshl_b32 s95,s12,12
s_mov_b32 s66,0x50000
maaa 128,64,36
s_add_u32 s70,s95,32768
s_add_u32 s72,s95,65536
s_lshl_b32 s4,s21,2
maaa 112,68,36
dl 224,36,52,0
maaa 116,68,32
s_add_u32 s4,s4,s12
ta 56,237,32768
s_lshl_b32 s4,s4,8
maaa 132,64,32
s_bitcmp1_b32 170,s20
s_cselect_b32 s5,L_odd-L_last,0
ta 58,237,33280
s_add_u32 s4,s4,s5
maaa 136,64,28
s_add_u32 s4,s4,L_last-L_att_pc
s_mov_b64 s[30:31],s[28:29]
s_mov_b64 s[28:29],s[78:79]
maaa 120,68,28
s_add_u32 s68,s95,0
v_cvt_f32_i32_e32 v252,v252
v_mul_f32_e32 v252,0x7149f2ca,v252
maaa 124,68,24
dl 225,36,52,1024
maaa 140,64,24
s_add_u32 s69,s95,16384
s_add_u32 s74,s95,98304
s_add_u32 s71,s95,49152
mvaa 40,72,24
s_waitcnt vmcnt(17) lgkmcnt(2)
s_barrier
mvaa 44,76,24
s_add_u32 s73,s95,81920
ta 20,234,49152
v_and_b32_e32 v245,48,v244
mvaa 52,76,28
s_lshl_b32 s59,%1,2
ta 22,234,49664
v_lshlrev_b32_e32 v245,3,v245
mvaa 48,72,28
s_lshl_b32 s84,%1,2
ta 44,235,49152
s_add_u32 s59,s59,s12
mvaa 56,72,32
dl 226,36,52,2048
mvaa 60,76,32
s_add_u32 s75,s95,114688
ta 46,235,49664
s_add_u32 s84,s84,s12
mvaa 68,76,36
s_mul_i32 s84,s84,12288
ta 48,236,49152
s_sub_u32 s55,s14,s13
mvaa 64,72,36
s_mov_b32 s48,0x38000000
ta 50,236,49664
s_mov_b32 s81,1
mvaa 32,96,36
s_mov_b32 s84,0
ta 60,237,49152
mvaa 36,100,36
dl 227,36,52,3072
mvaa 28,100,32
s_add_u32 s52,s52,s56
s_add_u32 m0,s98,69632
ta 62,237,49664
mvaa 24,96,32
ta 68,234,36864
mvaa 16,96,28
ta 70,234,37376
mvaa 20,100,28
ta 64,234,53248
mvaa 12,100,24
dl 224,36,52,0
mvaa 8,96,24
ta 66,234,53760
mvaa 76,24,104
ta 76,235,53248
mvaa 96,24,108
ta 78,235,53760
mvaa 100,28,108
ta 72,235,36864
)"
R"(mvaa 80,28,104
dl 225,36,52,1024
mvaa 84,32,104
ta 74,235,37376
mvaa 104,32,108
mvaa 108,36,108
mvaa 88,36,104
maaa 236,0,4
s_waitcnt lgkmcnt(5)
maaa 236,20,80
dl 226,36,52,2048
maaa 220,44,80
maaa 220,40,4
maaa 216,40,8
maaa 216,44,84
maaa 232,20,84
dl 227,36,52,3072
maaa 232,0,8
s_add_u32 s52,s52,s60
s_add_u32 m0,s98,0
maaa 228,0,12
maaa 228,20,88
maaa 212,44,88
maaa 212,40,12
dl 224,36,52,0
maaa 208,40,16
maaa 208,44,92
maaa 224,20,92
ta 40,236,36864
maaa 224,0,16
ta 42,236,37376
maaa 192,52,16
dl 225,36,52,1024
maaa 192,48,92
ta 44,236,53248
maaa 176,60,92
ta 46,236,53760
maaa 176,56,16
ta 20,237,53248
maaa 180,56,12
ta 22,237,53760
maaa 180,60,88
dl 226,36,52,2048
maaa 196,48,88
ta 0,237,36864
maaa 196,52,12
ta 2,237,37376
maaa 200,52,8
s_waitcnt vmcnt(18)
ra 24,232,32768
maaa 200,48,84
ra 28,232,34816
maaa 184,60,84
ra 32,232,36864
maaa 184,56,8
buffer_store_dwordx4 v[112:115],v244,s[24:27],s67 offen offset:0 sc0 sc1
maaa 188,56,4
ra 36,232,38912
v_add_u32_e32 v232,0x10000,v232
maaa 188,60,80
ra 96,233,32768
v_cndmask_b32_e64 v112,v241,0,s[64:65]
maaa 204,48,80
s_mov_b32 s64,0xaaa0aaaa
ra 100,233,34816
s_mov_b32 s65,0xa000aa00
maaa 204,52,4
ra 104,233,36864
v_cndmask_b32_e64 v113,v241,0,s[64:65]
maaa 172,68,4
dl 227,36,52,3072
s_waitcnt lgkmcnt(8)
maaa 172,64,80
s_add_u32 s52,s52,s56
s_add_u32 m0,s98,4096
ra 108,233,38912
maaa 156,76,80
s_mov_b32 s64,0x55405554
ta 56,234,40960
v_add_u32_e32 v233,0x10000,v233
maaa 156,72,4
s_mov_b32 s65,0x40005400
ta 58,234,41472
v_cndmask_b32_e64 v114,v241,0,s[64:65]
maaa 152,72,8
s_mov_b32 s64,0xaa80aaa8
ta 60,234,57344
s_mov_b32 s65,0x8000a800
maaa 152,76,84
dl 224,36,52,0
maaa 168,64,84
ta 62,234,57856
v_cndmask_b32_e64 v115,v241,0,s[64:65]
maaa 168,68,8
ta 48,235,57344
maaa 164,68,12
ta 50,235,57856
maaa 164,64,88
ta 52,235,40960
maaa 148,76,88
buffer_store_dwordx4 v[248:251],v244,s[8:11],s94 offen offset:0 sc0 sc1
maaa 148,72,12
ta 54,235,41472
maaa 144,72,16
maaa 144,76,92
maaa 160,64,92
ta 72,236,40960
maaa 160,68,16
ta 74,236,41472
maaa 128,40,16
dl 225,36,52,1024
maaa 128,44,92
ta 76,236,57344
maaa 112,20,92
L_vs9:
s_cbranch_execz L_vs13
ta 78,236,57856
s_waitcnt lgkmcnt(6)
maaa 112,0,16
ta 64,237,57344
maaa 116,0,12
ta 66,237,57856
maaa 116,20,88
dl 226,36,52,2048
maaa 132,44,88
ta 68,237,40960
maaa 132,40,12
ta 70,237,41472
maaa 136,40,8
maaa 136,44,84
maaa 120,20,84
dl 227,36,52,3072
maaa 120,0,8
s_add_u32 s52,s52,s60
maaa 124,0,4
maaa 124,20,80
maaa 140,44,80
ta 0,238,45056
ta 2,238,45568
maaa 140,40,4
buffer_store_dwordx4 v[248:251],v244,s[8:11],s94 offen offset:1024 sc0 sc1
ta 20,238,61440
ta 22,238,61952
mvaa 40,56,4
ta 44,239,61440
ta 46,239,61952
mvaa 40,60,80
ta 40,239,45056
ta 42,239,45568
mvaa 44,48,80
v_and_b32_e32 v250,240,v244
v_accvgpr_read_b32 v249,a240
s_waitcnt lgkmcnt(8)
mvaa 44,52,4
mvaa 52,52,8
mvaa 52,48,84
mvaa 48,60,84
mvaa 48,56,8
mvaa 56,56,12
mvaa 56,60,88
mvaa 60,48,88
mvaa 60,52,12
mvaa 68,52,16
mvaa 68,48,92
mvaa 64,60,92
mvaa 64,56,16
mvaa 32,72,16
s_waitcnt vmcnt(11) lgkmcnt(0)
s_barrier
s_add_u32 m0,s62,32768
mvaa 32,76,92
dl 228,40,53,0
mvaa 36,64,92
ta 52,234,0
mvaa 36,68,16
ta 54,234,512
mvaa 28,68,12
ta 48,234,16384
mvaa 28,64,88
ta 50,234,16896
mvaa 24,76,88
ta 60,235,16384
mvaa 24,72,12
dl 229,40,53,1024
mvaa 16,72,8
s_add_u32 s53,s53,s57
ta 62,235,16896
mvaa 16,76,84
ta 56,235,0
mvaa 20,64,84
ta 58,235,512
mvaa 20,68,8
mvaa 12,68,4
dl 230,44,54,2048
mvaa 12,64,80
mvaa 8,76,80
ta 68,236,0
mvaa 8,72,4
ta 70,236,512
mvaa 76,4,0
ta 64,236,16384
mvaa 76,80,20
dl 231,44,54,3072
mvaa 96,80,44
s_add_u32 m0,s62,49152
s_add_u32 s54,s54,s57
ta 66,236,16896
mvaa 96,4,40
ta 76,237,16384
mvaa 100,8,40
ta 78,237,16896
mvaa 100,84,44
ta 72,237,0
mvaa 80,84,20
ta 74,237,512
mvaa 80,8,0
dl 228,40,53,0
mvaa 84,12,0
ra 80,233,0
mvaa 84,88,20
ra 4,232,0
mvaa 104,88,44
ra 84,233,2048
mvaa 104,12,40
ra 8,232,2048
mvaa 108,16,40
dl 229,40,53,1024
mvaa 108,92,44
s_add_u32 s53,s53,s58
ra 88,233,4096
mvaa 88,92,20
ta 40,234,4096
mvaa 88,16,0
ta 42,234,4608
s_waitcnt lgkmcnt(8)
maaa 236,52,24
ta 44,234,20480
maaa 236,48,96
dl 230,44,54,2048
maaa 220,60,96
ta 46,234,20992
maaa 220,56,24
ta 20,235,20480
maaa 216,56,28
ta 22,235,20992
maaa 216,60,100
ta 0,235,4096
maaa 232,48,100
dl 231,44,54,3072
maaa 232,52,28
s_add_u32 m0,s98,32768
ta 2,235,4608
s_add_u32 s54,s54,s58
maaa 228,52,32
ra 12,232,4096
maaa 228,48,104
ra 16,232,6144
v_add_u32_e32 v232,0xffff0000,v232
maaa 212,60,104
ra 92,233,6144
v_add_u32_e32 v233,0xffff0000,v233
maaa 212,56,32
maaa 208,56,36
dl 224,36,52,0
maaa 208,60,108
maaa 224,48,108
ta 56,236,4096
maaa 224,52,36
ta 58,236,4608
maaa 192,68,36
ta 60,236,20480
maaa 192,64,108
dl 225,36,52,1024
maaa 176,76,108
ta 62,236,20992
s_waitcnt lgkmcnt(6)
maaa 176,72,36
ta 48,237,20480
maaa 180,72,32
ta 50,237,20992
maaa 180,76,104
ta 52,237,4096
maaa 196,64,104
buffer_store_dwordx4 v[116:119],v244,s[24:27],s67 offen offset:1024 sc0 sc1
maaa 196,68,32
ta 54,237,4608
maaa 200,68,28
maaa 200,64,100
maaa 184,76,100
maaa 184,72,28
dl 226,36,52,2048
maaa 188,72,24
maaa 188,76,96
maaa 204,64,96
ta 72,234,8192
maaa 204,68,24
ta 74,234,8704
maaa 172,40,24
ta 76,234,24576
maaa 172,44,96
dl 227,36,52,3072
maaa 156,20,96
s_add_u32 s52,s52,s56
s_add_u32 m0,s98,36864
ta 78,234,25088
maaa 156,0,24
ta 64,235,24576
maaa 152,0,28
ta 66,235,25088
maaa 152,20,100
ta 68,235,8192
maaa 168,44,100
dl 224,36,52,0
maaa 168,40,28
ta 70,235,8704
maaa 164,40,32
maaa 164,44,104
maaa 148,20,104
maaa 148,0,32
buffer_store_dwordx4 v[120:123],v244,s[24:27],s67 offen offset:2048 sc0 sc1
maaa 144,0,36
maaa 144,20,108
maaa 160,44,108
ta 0,236,8192
maaa 160,40,36
ta 2,236,8704
s_waitcnt lgkmcnt(3)
maaa 128,56,36
dl 225,36,52,1024
maaa 128,60,108
ta 20,236,24576
maaa 112,48,108
ta 22,236,25088
maaa 112,52,36
ta 44,237,24576
maaa 116,52,32
ta 46,237,25088
maaa 116,48,104
dl 226,36,52,2048
maaa 132,60,104
ta 40,237,8192
maaa 132,56,32
ta 42,237,8704
maaa 136,56,28
maaa 136,60,100
maaa 120,48,100
maaa 120,52,28
buffer_load_dword v246,off,s[8:11],s35 offset:1024 sc1
maaa 124,52,24
maaa 124,48,96
maaa 140,60,96
ta 52,238,12288
ta 54,238,12800
maaa 140,56,24
ta 48,238,28672
ta 50,238,29184
mvaa 40,72,24
buffer_store_dwordx4 v[124:127],v244,s[24:27],s67 offen offset:3072 sc0 sc1
ta 60,239,28672
mvaa 40,76,96
s_mov_b32 s67,8192
ta 62,239,29184
mvaa 44,64,96
ta 56,239,12288
ta 58,239,12800
s_waitcnt lgkmcnt(8)
mvaa 44,68,24
mvaa 52,68,28
mvaa 52,64,100
dl 227,36,52,3072
mvaa 48,76,100
s_add_u32 s52,s52,s60
mvaa 48,72,28
mvaa 56,72,32
mvaa 56,76,104
mvaa 60,64,104
mvaa 60,68,32
mvaa 68,68,36
mvaa 68,64,108
mvaa 64,76,108
mvaa 64,72,36
mvaa 32,0,36
s_waitcnt vmcnt(12) lgkmcnt(0)
s_barrier
s_add_u32 m0,s62,0
mvaa 32,20,108
dl 228,40,53,0
mvaa 36,44,108
ta 68,234,32768
mvaa 36,40,36
ta 70,234,33280
mvaa 28,40,32
ta 64,234,49152
mvaa 28,44,104
ta 66,234,49664
mvaa 24,20,104
ta 76,235,49152
mvaa 24,0,32
dl 229,40,53,1024
mvaa 16,0,28
s_add_u32 s53,s53,s57
ta 78,235,49664
mvaa 16,20,100
ta 72,235,32768
mvaa 20,44,100
ta 74,235,33280
mvaa 20,40,28
mvaa 12,40,24
mvaa 12,44,96
dl 230,44,54,2048
mvaa 8,20,96
ta 40,236,32768
mvaa 8,0,24
ta 42,236,33280
mvaa 76,24,52
ta 44,236,49152
mvaa 76,96,48
ta 46,236,49664
mvaa 96,96,60
dl 231,44,54,3072
mvaa 96,24,56
s_add_u32 m0,s62,16384
s_add_u32 s54,s54,s57
ta 20,237,49152
mvaa 100,28,56
ta 22,237,49664
mvaa 100,100,60
ta 0,237,32768
mvaa 80,100,48
ta 2,237,33280
mvaa 80,28,52
dl 228,40,53,0
mvaa 84,32,52
ra 96,233,0
mvaa 84,104,48
ra 24,232,0
mvaa 104,104,60
ra 100,233,2048
mvaa 104,32,56
ra 28,232,2048
mvaa 108,36,56
dl 229,40,53,1024
mvaa 108,108,60
s_add_u32 s53,s53,s58
ra 104,233,4096
mvaa 88,108,48
ta 56,234,36864
mvaa 88,36,52
ta 58,234,37376
s_waitcnt lgkmcnt(8)
maaa 236,68,4
ta 60,234,53248
maaa 236,64,80
ta 62,234,53760
maaa 220,76,80
dl 230,44,54,2048
maaa 220,72,4
ta 48,235,53248
maaa 216,72,8
ta 50,235,53760
maaa 216,76,84
ta 52,235,36864
maaa 232,64,84
ta 54,235,37376
maaa 232,68,8
dl 231,44,54,3072
maaa 228,68,12
s_add_u32 m0,s98,65536
ra 32,232,4096
s_add_u32 s54,s54,s58
maaa 228,64,88
ra 36,232,6144
maaa 212,76,88
ra 108,233,6144
maaa 212,72,12
s_waitcnt vmcnt(10)
v_readfirstlane_b32 s87,v246
maaa 208,72,16
dl 224,36,52,0
maaa 208,76,92
s_cmp_eq_u32 s12,0
s_cselect_b32 s89,s32,s87
s_cmp_lg_u32 s87,s89
s_cbranch_scc1 L_q_ack
L_q_acked:
maaa 224,64,92
s_and_b32 s87,s12,1
ta 72,236,36864
s_lshl_b32 s88,s87,6
maaa 224,68,16
s_add_u32 s90,s90,s88
ta 74,236,37376
s_lshl_b32 s88,s87,10
maaa 192,40,16
s_lshl_b32 s87,s87,14
ta 76,236,53248
s_add_u32 s91,s91,s87
maaa 192,44,92
dl 225,36,52,1024
maaa 176,20,92
s_lshr_b32 s87,s18,1
ta 78,236,53760
s_lshl_b32 s87,s87,4
s_waitcnt lgkmcnt(5)
maaa 176,0,16
s_add_u32 s33,s33,s88
ta 64,237,53248
v_cmp_eq_u32_e64 s[100:101],s87,v250
maaa 180,0,12
s_lshl_b32 s88,s18,11
ta 66,237,53760
v_lshlrev_b32_e32 v250,3,v250
maaa 180,20,88
s_add_u32 s88,s33,s88
ta 68,237,36864
v_add_u32_e32 v250,83894272,v250
maaa 196,44,88
s_add_u32 s33,s88,63176704
ta 70,237,37376
v_add_u32_e32 v251,s91,v244
maaa 196,40,12
buffer_store_dwordx4 v[128:131],v244,s[24:27],s67 offen offset:0 sc0 sc1
maaa 200,40,8
s_add_u32 s34,s88,63504384
v_cndmask_b32_e64 v242,31,-1,s[100:101]
v_and_b32_e32 v243,528,v242
maaa 200,44,84
s_add_u32 s50,s88,63832064
s_add_u32 s51,s88,64159744
s_lshr_b32 s91,s21,3
maaa 184,20,84
s_and_b32 s88,s20,4
s_and_b32 s89,s20,3
s_add_u32 s91,s91,s88
maaa 184,0,8
s_lshl_b32 s88,s89,6
s_lshl_b32 s86,s21,2
s_add_u32 s86,s86,s12
maaa 188,0,4
dl 226,36,52,2048
maaa 188,20,80
s_cmp_eq_u32 s90,0
s_cselect_b64 s[64:65],-1,0
s_and_b32 s89,s86,1
ta 0,234,40960
maaa 204,44,80
s_lshl_b32 s89,s89,9
ta 2,234,41472
s_lshr_b32 s49,s86,3
maaa 204,40,4
s_lshl_b32 s49,s49,11
ta 20,234,57344
v_add_f32_e32 v128,0x70c9f2ca,v252
maaa 172,56,4
ta 22,234,57856
v_add_f32_e32 v129,0xf0c9f2ca,v252
maaa 172,60,80
dl 227,36,52,3072
maaa 156,48,80
s_add_u32 s52,s52,s56
s_add_u32 m0,s98,69632
ta 44,235,57344
maaa 156,52,4
ta 46,235,57856
v_add_f32_e32 v130,0xf1977617,v252
maaa 152,52,8
ta 40,235,40960
v_add_f32_e32 v131,0xf1fc6f7c,v252
maaa 152,48,84
ta 42,235,41472
v_min_f32_e32 v128,0xc1a00000,v128
maaa 168,60,84
dl 224,36,52,0
maaa 168,56,8
v_min_f32_e32 v129,0xc1a00000,v129
v_min_f32_e32 v130,0xc1a00000,v130
maaa 164,56,12
v_min_f32_e32 v131,0xc1a00000,v131
v_lshrrev_b32_e32 v246,6,v244
maaa 164,60,88
v_lshl_add_u32 v245,v246,22,v245
v_lshlrev_b32_e32 v246,16,v244
maaa 148,48,88
maaa 148,52,12
maaa 144,52,16
buffer_store_dwordx4 v[132:135],v244,s[24:27],s67 offen offset:1024 sc0 sc1
maaa 144,48,92
maaa 160,60,92
ta 52,236,40960
v_add_f32_e32 v132,0xf343a333,v252
maaa 160,56,16
ta 54,236,41472
v_add_f32_e32 v133,0xf3504260,v252
s_waitcnt lgkmcnt(2)
maaa 128,72,16
ta 48,236,57344
v_add_f32_e32 v134,0xf35ce18d,v252
maaa 128,76,92
dl 225,36,52,1024
maaa 112,64,92
ta 50,236,57856
v_min_f32_e32 v132,0xc1a00000,v132
maaa 112,68,16
ta 60,237,57344
v_add_f32_e32 v135,0xf36980b9,v252
maaa 116,68,12
ta 62,237,57856
v_min_f32_e32 v133,0xc1a00000,v133
maaa 116,64,88
ta 56,237,40960
v_min_f32_e32 v134,0xc1a00000,v134
maaa 132,76,88
dl 226,36,52,2048
maaa 132,72,12
ta 58,237,41472
v_min_f32_e32 v135,0xc1a00000,v135
maaa 136,72,8
maaa 136,76,84
maaa 120,64,84
maaa 120,68,8
dl 227,36,52,3072
maaa 124,68,4
s_add_u32 s52,s52,s60
maaa 124,64,80
maaa 140,76,80
ta 68,238,45056
ta 70,238,45568
maaa 140,72,4
ta 64,238,61440
ta 66,238,61952
mvaa 40,0,4
ta 76,239,61440
mvaa 40,20,80
buffer_store_dwordx4 v[136:139],v244,s[24:27],s67 offen offset:2048 sc0 sc1
ta 78,239,61952
mvaa 44,44,80
ta 72,239,45056
ta 74,239,45568
mvaa 44,40,4
v_add_f32_e32 v136,0xf3c6caff,v252
v_add_f32_e32 v137,0xf3cd1a95,v252
mvaa 52,40,8
v_add_f32_e32 v138,0xf3d36a2b,v252
v_add_f32_e32 v139,0xf3d9b9c2,v252
mvaa 52,44,84
v_min_f32_e32 v136,0xc1a00000,v136
v_min_f32_e32 v137,0xc1a00000,v137
mvaa 48,20,84
v_min_f32_e32 v138,0xc1a00000,v138
v_min_f32_e32 v139,0xc1a00000,v139
mvaa 48,0,8
mvaa 56,0,12
mvaa 56,20,88
mvaa 60,44,88
mvaa 60,40,12
mvaa 68,40,16
mvaa 68,44,92
mvaa 64,20,92
mvaa 64,0,16
L_vs10:
s_cbranch_execz L_vs14
s_waitcnt lgkmcnt(0)
mvaa 32,52,16
s_waitcnt vmcnt(11)
s_barrier
mvaa 32,48,92
s_cmp_eq_u32 s12,0
s_cselect_b64 exec,1,0
buffer_atomic_add v247,off,s[8:11],s85 sc1
s_mov_b64 exec,-1
mvaa 36,60,92
s_add_u32 m0,s62,32768
ta 40,234,0
v_mov_b32_e32 v247,32
mvaa 36,56,16
ta 42,234,512
mvaa 28,56,12
ta 44,234,16384
mvaa 28,60,88
ta 46,234,16896
mvaa 24,48,88
dl 228,40,53,0
mvaa 24,52,12
ta 20,235,16384
mvaa 16,52,8
ta 22,235,16896
mvaa 16,48,84
ta 0,235,0
mvaa 20,60,84
ta 2,235,512
mvaa 20,56,8
dl 229,40,53,1024
mvaa 12,56,4
s_add_u32 s53,s53,s57
mvaa 12,60,80
mvaa 8,48,80
ta 56,236,0
mvaa 8,52,4
ta 58,236,512
mvaa 76,4,68
dl 230,44,54,2048
mvaa 76,80,64
ta 60,236,16384
mvaa 96,80,76
ta 62,236,16896
mvaa 96,4,72
ta 48,237,16384
mvaa 100,8,72
ta 50,237,16896
mvaa 100,84,76
dl 231,44,54,3072
mvaa 80,84,64
s_add_u32 m0,s62,49152
s_add_u32 s54,s54,s57
ta 52,237,0
mvaa 80,8,68
ta 54,237,512
mvaa 84,12,68
ra 80,233,32768
mvaa 84,88,64
ra 4,232,32768
mvaa 104,88,76
dl 228,40,53,0
mvaa 104,12,72
ra 84,233,34816
mvaa 108,16,72
ra 8,232,34816
mvaa 108,92,76
ra 12,232,36864
mvaa 88,92,64
ta 72,234,4096
mvaa 88,16,68
ta 74,234,4608
s_waitcnt lgkmcnt(9)
maaa 236,40,24
dl 229,40,53,1024
maaa 236,44,96
s_add_u32 s53,s53,s58
ta 76,234,20480
maaa 220,20,96
ta 78,234,20992
maaa 220,0,24
ta 64,235,20480
maaa 216,0,28
ta 66,235,20992
maaa 216,20,100
dl 230,44,54,2048
maaa 232,44,100
ta 68,235,4096
maaa 232,40,28
ta 70,235,4608
maaa 228,40,32
ra 88,233,36864
maaa 228,44,104
ra 92,233,38912
v_add_u32_e32 v233,0x10000,v233
maaa 212,20,104
dl 231,44,54,3072
maaa 212,0,32
s_add_u32 m0,s98,0
ra 16,232,38912
s_add_u32 s54,s54,s58
maaa 208,0,36
v_add_u32_e32 v232,0x10000,v232
maaa 208,20,108
maaa 224,44,108
ta 0,236,4096
maaa 224,40,36
dl 224,36,52,0
maaa 192,56,36
ta 2,236,4608
maaa 192,60,108
ta 20,236,20480
maaa 176,48,108
ta 22,236,20992
s_waitcnt lgkmcnt(6)
maaa 176,52,36
ta 44,237,20480
maaa 180,52,32
ta 46,237,20992
maaa 180,48,104
dl 225,36,52,1024
maaa 196,60,104
ta 40,237,4096
maaa 196,56,32
ta 42,237,4608
maaa 200,56,28
maaa 200,60,100
maaa 184,48,100
buffer_store_dwordx4 v[140:143],v244,s[24:27],s67 offen offset:3072 sc0 sc1
maaa 184,52,28
s_mov_b32 s67,12288
v_add_f32_e32 v140,0xf415e232,v252
v_add_f32_e32 v141,0xf41909fd,v252
maaa 188,52,24
v_add_f32_e32 v142,0xf41c31c8,v252
v_min_f32_e32 v140,0xc1a00000,v140
maaa 188,48,96
v_add_f32_e32 v143,0xf41f5993,v252
v_bfe_i32 v252,v244,4,1
maaa 204,60,96
ta 52,234,8192
v_min_f32_e32 v141,0xc1a00000,v141
maaa 204,56,24
dl 226,36,52,2048
maaa 172,72,24
ta 54,234,8704
v_min_f32_e32 v142,0xc1a00000,v142
maaa 172,76,96
ta 48,234,24576
v_min_f32_e32 v143,0xc1a00000,v143
maaa 156,64,96
ta 50,234,25088
v_xor_b32_e32 v252,65535,v252
maaa 156,68,24
ta 60,235,24576
maaa 152,68,28
dl 227,36,52,3072
maaa 152,64,100
s_add_u32 s52,s52,s56
s_add_u32 m0,s98,4096
ta 62,235,25088
maaa 168,76,100
ta 56,235,8192
maaa 168,72,28
ta 58,235,8704
maaa 164,72,32
maaa 164,76,104
maaa 148,64,104
dl 224,36,52,0
maaa 148,68,32
maaa 144,68,36
maaa 144,64,108
maaa 160,76,108
ta 68,236,8192
maaa 160,72,36
buffer_store_dwordx4 v[144:147],v244,s[24:27],s67 offen offset:0 sc0 sc1
s_waitcnt lgkmcnt(3)
maaa 128,0,36
ta 70,236,8704
maaa 128,20,108
ta 64,236,24576
maaa 112,44,108
ta 66,236,25088
maaa 112,40,36
ta 76,237,24576
maaa 116,40,32
dl 225,36,52,1024
maaa 116,44,104
ta 78,237,25088
maaa 132,20,104
ta 72,237,8192
maaa 132,0,32
ta 74,237,8704
maaa 136,0,28
maaa 136,20,100
dl 226,36,52,2048
maaa 120,44,100
maaa 120,40,28
maaa 124,40,24
maaa 124,44,96
maaa 140,20,96
buffer_store_dwordx4 v[148:151],v244,s[24:27],s67 offen offset:1024 sc0 sc1
ta 40,238,12288
ta 42,238,12800
maaa 140,0,24
ta 44,238,28672
ta 46,238,29184
mvaa 40,52,24
ta 20,239,28672
mvaa 40,48,96
ta 22,239,29184
mvaa 44,60,96
ta 0,239,12288
ta 2,239,12800
s_waitcnt lgkmcnt(8)
mvaa 44,56,24
mvaa 52,56,28
dl 227,36,52,3072
mvaa 52,60,100
s_add_u32 s52,s52,s60
mvaa 48,48,100
mvaa 48,52,28
mvaa 56,52,32
mvaa 56,48,104
mvaa 60,60,104
mvaa 60,56,32
mvaa 68,56,36
mvaa 68,60,108
mvaa 64,48,108
mvaa 64,52,36
mvaa 32,68,36
s_waitcnt vmcnt(11) lgkmcnt(0)
s_barrier
s_add_u32 m0,s62,0
mvaa 32,64,108
dl 228,40,53,0
mvaa 36,76,108
ta 56,234,32768
mvaa 36,72,36
ta 58,234,33280
mvaa 28,72,32
ta 60,234,49152
mvaa 28,76,104
ta 62,234,49664
mvaa 24,64,104
dl 229,40,53,1024
mvaa 24,68,32
s_add_u32 s53,s53,s57
ta 48,235,49152
mvaa 16,68,28
ta 50,235,49664
mvaa 16,64,100
ta 52,235,32768
mvaa 20,76,100
ta 54,235,33280
mvaa 20,72,28
dl 230,44,54,2048
mvaa 12,72,24
mvaa 12,76,96
mvaa 8,64,96
ta 72,236,32768
mvaa 8,68,24
ta 74,236,33280
mvaa 76,24,40
ta 76,236,49152
mvaa 76,96,44
dl 231,44,54,3072
mvaa 96,96,20
s_add_u32 m0,s62,16384
s_add_u32 s54,s54,s57
ta 78,236,49664
mvaa 96,24,0
ta 64,237,49152
mvaa 100,28,0
ta 66,237,49664
mvaa 100,100,20
ta 68,237,32768
mvaa 80,100,44
dl 228,40,53,0
mvaa 80,28,40
ta 70,237,33280
mvaa 84,32,40
ra 96,233,0
mvaa 84,104,44
ra 24,232,0
mvaa 104,104,20
ra 100,233,2048
mvaa 104,32,0
dl 229,40,53,1024
mvaa 108,36,0
s_add_u32 s53,s53,s58
ra 28,232,2048
mvaa 108,108,20
ra 104,233,4096
mvaa 88,108,44
ta 0,234,36864
mvaa 88,36,40
ta 2,234,37376
s_waitcnt lgkmcnt(8)
maaa 236,56,4
dl 230,44,54,2048
maaa 236,60,80
ta 20,234,53248
maaa 220,48,80
ta 22,234,53760
maaa 220,52,4
ta 44,235,53248
maaa 216,52,8
ta 46,235,53760
maaa 216,48,84
dl 231,44,54,3072
maaa 232,60,84
s_add_u32 m0,s98,32768
ta 40,235,36864
s_add_u32 s54,s54,s58
maaa 232,56,8
ta 42,235,37376
maaa 228,56,12
ra 32,232,4096
maaa 228,60,88
ra 108,233,6144
v_add_u32_e32 v233,0xffff0000,v233
maaa 212,48,88
ra 36,232,6144
v_add_u32_e32 v232,0xffff0000,v232
maaa 212,52,12
dl 224,36,52,0
maaa 208,52,16
maaa 208,48,92
maaa 224,60,92
ta 52,236,36864
maaa 224,56,16
ta 54,236,37376
maaa 192,72,16
dl 225,36,52,1024
maaa 192,76,92
ta 48,236,53248
maaa 176,64,92
ta 50,236,53760
)"
R"(s_waitcnt lgkmcnt(6)
maaa 176,68,16
ta 60,237,53248
maaa 180,68,12
ta 62,237,53760
maaa 180,64,88
dl 226,36,52,2048
maaa 196,76,88
ta 56,237,36864
maaa 196,72,12
ta 58,237,37376
maaa 200,72,8
maaa 200,76,84
maaa 184,64,84
buffer_store_dwordx4 v[152:155],v244,s[24:27],s67 offen offset:2048 sc0 sc1
maaa 184,68,8
maaa 188,68,4
maaa 188,64,80
maaa 204,76,80
ta 68,234,40960
maaa 204,72,4
ta 70,234,41472
maaa 172,0,4
dl 227,36,52,3072
maaa 172,20,80
s_add_u32 s52,s52,s56
s_add_u32 m0,s98,36864
ta 64,234,57344
maaa 156,44,80
ta 66,234,57856
maaa 156,40,4
ta 76,235,57344
maaa 152,40,8
ta 78,235,57856
maaa 152,44,84
dl 224,36,52,0
maaa 168,20,84
ta 72,235,40960
maaa 168,0,8
ta 74,235,41472
maaa 164,0,12
maaa 164,20,88
maaa 148,44,88
buffer_store_dwordx4 v[156:159],v244,s[24:27],s67 offen offset:3072 sc0 sc1
maaa 148,40,12
s_mov_b32 s67,16384
maaa 144,40,16
maaa 144,44,92
maaa 160,20,92
ta 40,236,40960
maaa 160,0,16
dl 225,36,52,1024
s_waitcnt lgkmcnt(3)
maaa 128,52,16
ta 42,236,41472
maaa 128,48,92
ta 44,236,57344
maaa 112,60,92
ta 46,236,57856
maaa 112,56,16
ta 20,237,57344
maaa 116,56,12
ta 22,237,57856
maaa 116,60,88
dl 226,36,52,2048
maaa 132,48,88
ta 0,237,40960
maaa 132,52,12
ta 2,237,41472
maaa 136,52,8
maaa 136,48,84
maaa 120,60,84
dl 227,36,52,3072
maaa 120,56,8
s_add_u32 s52,s52,s60
maaa 124,56,4
maaa 124,60,80
maaa 140,48,80
ta 56,238,45056
ta 58,238,45568
maaa 140,52,4
buffer_store_dwordx4 v[160:163],v244,s[24:27],s67 offen offset:0 sc0 sc1
ta 60,238,61440
ta 62,238,61952
mvaa 40,68,4
ta 48,239,61440
mvaa 40,64,80
ta 50,239,61952
mvaa 44,76,80
ta 52,239,45056
ta 54,239,45568
s_waitcnt lgkmcnt(8)
mvaa 44,72,4
mvaa 52,72,8
mvaa 52,76,84
mvaa 48,64,84
mvaa 48,68,8
mvaa 56,68,12
mvaa 56,64,88
mvaa 60,76,88
mvaa 60,72,12
mvaa 68,72,16
mvaa 68,76,92
mvaa 64,64,92
mvaa 64,68,16
mvaa 32,40,16
s_waitcnt vmcnt(11) lgkmcnt(0)
s_barrier
s_add_u32 m0,s62,32768
mvaa 32,44,92
dl 228,40,53,0
mvaa 36,20,92
ta 72,234,0
mvaa 36,0,16
ta 74,234,512
mvaa 28,0,12
ta 76,234,16384
mvaa 28,20,88
ta 78,234,16896
mvaa 24,44,88
ta 64,235,16384
mvaa 24,40,12
dl 229,40,53,1024
mvaa 16,40,8
s_add_u32 s53,s53,s57
ta 66,235,16896
mvaa 16,44,84
ta 68,235,0
mvaa 20,20,84
ta 70,235,512
mvaa 20,0,8
mvaa 12,0,4
dl 230,44,54,2048
mvaa 12,20,80
mvaa 8,44,80
ta 0,236,0
mvaa 8,40,4
ta 2,236,512
mvaa 76,4,56
ta 20,236,16384
mvaa 76,80,60
dl 231,44,54,3072
mvaa 96,80,48
s_add_u32 m0,s62,49152
s_add_u32 s54,s54,s57
ta 22,236,16896
mvaa 96,4,52
ta 44,237,16384
mvaa 100,8,52
ta 46,237,16896
mvaa 100,84,48
ta 40,237,0
mvaa 80,84,60
dl 228,40,53,0
mvaa 80,8,56
ta 42,237,512
mvaa 84,12,56
ra 80,233,0
mvaa 84,88,60
ra 4,232,0
mvaa 104,88,48
ra 84,233,2048
mvaa 104,12,52
ra 8,232,2048
mvaa 108,16,52
dl 229,40,53,1024
mvaa 108,92,48
s_add_u32 s53,s53,s58
ra 88,233,4096
mvaa 88,92,60
ta 52,234,4096
mvaa 88,16,56
ta 54,234,4608
s_waitcnt lgkmcnt(8)
maaa 236,72,24
ta 48,234,20480
maaa 236,76,96
dl 230,44,54,2048
maaa 220,64,96
ta 50,234,20992
maaa 220,68,24
ta 60,235,20480
maaa 216,68,28
ta 62,235,20992
maaa 216,64,100
ta 56,235,4096
v_accvgpr_read_b32 v0,a236
maaa 232,76,100
dl 231,44,54,3072
maaa 232,72,28
s_add_u32 m0,s98,65536
ta 58,235,4608
s_add_u32 s54,s54,s58
maaa 228,72,32
L_vs11:
s_cbranch_execz L_vs15
ra 12,232,4096
v_accvgpr_read_b32 v1,a237
maaa 228,76,104
ra 92,233,6144
v_accvgpr_read_b32 v2,a238
maaa 212,64,104
ra 16,232,6144
v_accvgpr_read_b32 v3,a239
maaa 212,68,32
dl 224,36,52,0
maaa 208,68,36
v_accvgpr_read_b32 v4,a220
v_accvgpr_read_b32 v5,a221
maaa 208,64,108
v_accvgpr_read_b32 v6,a222
v_accvgpr_read_b32 v7,a223
maaa 224,76,108
ta 68,236,4096
v_accvgpr_read_b32 v92,a216
maaa 224,72,36
ta 70,236,4608
v_accvgpr_read_b32 v93,a217
maaa 192,0,36
dl 225,36,52,1024
maaa 192,20,108
ta 64,236,20480
v_accvgpr_read_b32 v94,a218
maaa 176,44,108
ta 66,236,20992
v_accvgpr_read_b32 v95,a219
s_waitcnt lgkmcnt(6)
maaa 176,40,36
ta 76,237,20480
v_accvgpr_read_b32 v72,a232
maaa 180,40,32
ta 78,237,20992
v_accvgpr_read_b32 v73,a233
maaa 180,44,104
ta 72,237,4096
v_accvgpr_read_b32 v74,a234
maaa 196,20,104
s_mov_b64 exec,3
global_load_dword a250,v253,s[96:97]
s_mov_b64 exec,-1
maaa 196,0,32
ta 74,237,4608
v_accvgpr_read_b32 v75,a235
maaa 200,0,28
maaa 200,20,100
maaa 184,44,100
maaa 184,40,28
buffer_store_dwordx4 v[164:167],v244,s[24:27],s67 offen offset:1024 sc0 sc1
maaa 188,40,24
maaa 188,44,96
maaa 204,20,96
ta 40,234,8192
maaa 204,0,24
ta 42,234,8704
maaa 172,52,24
dl 226,36,52,2048
maaa 172,48,96
ta 44,234,24576
maaa 156,60,96
ta 46,234,25088
maaa 156,56,24
ta 20,235,24576
maaa 152,56,28
ta 22,235,25088
maaa 152,60,100
dl 227,36,52,3072
maaa 168,48,100
s_add_u32 s52,s52,s56
s_add_u32 m0,s98,69632
ta 0,235,8192
maaa 168,52,28
ta 2,235,8704
maaa 164,52,32
maaa 164,48,104
maaa 148,60,104
maaa 148,56,32
buffer_store_dwordx4 v[168:171],v244,s[24:27],s67 offen offset:2048 sc0 sc1
maaa 144,56,36
maaa 144,60,108
maaa 160,48,108
ta 56,236,8192
maaa 160,52,36
ta 58,236,8704
s_waitcnt lgkmcnt(4)
maaa 128,68,36
dl 224,36,52,0
maaa 128,64,108
ta 60,236,24576
maaa 112,76,108
ta 62,236,25088
maaa 112,72,36
ta 48,237,24576
maaa 116,72,32
ta 50,237,25088
maaa 116,76,104
dl 225,36,52,1024
maaa 132,64,104
ta 52,237,8192
maaa 132,68,32
ta 54,237,8704
maaa 136,68,28
maaa 136,64,100
maaa 120,76,100
dl 226,36,52,2048
maaa 120,72,28
maaa 124,72,24
maaa 124,76,96
maaa 140,64,96
ta 72,238,12288
ta 74,238,12800
maaa 140,68,24
ta 76,238,28672
ta 78,238,29184
mvaa 40,40,24
buffer_store_dwordx4 v[172:175],v244,s[24:27],s67 offen offset:3072 sc0 sc1
ta 64,239,28672
mvaa 40,44,96
s_mov_b32 s67,20480
ta 66,239,29184
mvaa 44,20,96
ta 68,239,12288
ta 70,239,12800
s_waitcnt lgkmcnt(8)
mvaa 44,0,24
mvaa 52,0,28
mvaa 52,20,100
dl 227,36,52,3072
mvaa 48,44,100
s_add_u32 s52,s52,s60
mvaa 48,40,28
mvaa 56,40,32
mvaa 56,44,104
mvaa 60,20,104
mvaa 60,0,32
mvaa 68,0,36
mvaa 68,20,108
mvaa 64,44,108
mvaa 64,40,36
mvaa 32,56,36
s_waitcnt vmcnt(12) lgkmcnt(0)
s_barrier
s_add_u32 m0,s62,0
mvaa 32,60,108
dl 228,40,53,0
mvaa 36,48,108
ta 0,234,32768
mvaa 36,52,36
ta 2,234,33280
mvaa 28,52,32
ta 20,234,49152
mvaa 28,48,104
ta 22,234,49664
mvaa 24,60,104
ta 44,235,49152
mvaa 24,56,32
dl 229,40,53,1024
mvaa 16,56,28
s_add_u32 s53,s53,s57
ta 46,235,49664
mvaa 16,60,100
ta 40,235,32768
mvaa 20,48,100
ta 42,235,33280
mvaa 20,52,28
ta 236,236,36864
mvaa 12,52,24
dl 230,44,54,2048
mvaa 12,48,96
ta 238,236,37376
mvaa 8,60,96
ta 52,236,32768
mvaa 8,56,24
ta 54,236,33280
mvaa 76,24,72
ta 48,236,49152
mvaa 76,96,76
ta 50,236,49664
mvaa 96,96,64
dl 231,44,54,3072
mvaa 96,24,68
s_add_u32 m0,s62,16384
s_add_u32 s54,s54,s57
ta 60,237,49152
mvaa 100,28,68
ta 62,237,49664
mvaa 100,100,64
ta 56,237,32768
mvaa 80,100,76
ta 58,237,33280
mvaa 80,28,72
dl 228,40,53,0
mvaa 84,32,72
ta 220,236,53248
mvaa 84,104,76
ta 222,236,53760
mvaa 104,104,64
ta 232,237,53248
mvaa 104,32,68
ta 234,237,53760
mvaa 108,36,68
dl 229,40,53,1024
mvaa 108,108,64
s_add_u32 s53,s53,s58
ta 216,237,36864
mvaa 88,108,76
ta 68,234,36864
mvaa 88,36,72
ta 70,234,37376
s_waitcnt lgkmcnt(8)
mvaa 0,0,4
ta 64,234,53248
mvaa 0,20,80
dl 230,44,54,2048
mvaa 4,44,80
ta 66,234,53760
mvaa 4,40,4
ta 76,235,53248
mvaa 92,40,8
ta 78,235,53760
mvaa 92,44,84
ta 72,235,36864
mvaa 72,20,84
ta 74,235,37376
mvaa 72,0,8
dl 231,44,54,3072
maaa 228,0,12
s_add_u32 m0,s98,0
ta 218,237,37376
s_add_u32 s54,s54,s58
maaa 228,20,88
ra 96,233,32768
maaa 212,44,88
ra 24,232,32768
maaa 212,40,12
ra 100,233,34816
maaa 208,40,16
dl 224,36,52,0
maaa 208,44,92
ra 28,232,34816
v_accvgpr_read_b32 v144,a228
maaa 224,20,92
ra 104,233,36864
v_accvgpr_read_b32 v145,a229
maaa 224,0,16
ra 32,232,36864
v_accvgpr_read_b32 v146,a230
maaa 192,52,16
ra 36,232,38912
v_accvgpr_read_b32 v147,a231
maaa 192,48,92
dl 225,36,52,1024
maaa 176,60,92
ra 108,233,38912
v_accvgpr_read_b32 v148,a212
s_waitcnt lgkmcnt(8)
maaa 176,56,16
ta 40,234,40960
v_accvgpr_read_b32 v149,a213
maaa 180,56,12
ta 42,234,41472
v_accvgpr_read_b32 v150,a214
maaa 180,60,88
ta 44,234,57344
v_accvgpr_read_b32 v151,a215
maaa 196,48,88
buffer_store_dwordx4 v[176:179],v244,s[24:27],s67 offen offset:0 sc0 sc1
maaa 196,52,12
ta 46,234,57856
v_accvgpr_read_b32 v156,a208
maaa 200,52,8
ta 20,235,57344
v_accvgpr_read_b32 v157,a209
maaa 200,48,84
ta 22,235,57856
v_accvgpr_read_b32 v158,a210
maaa 184,60,84
ta 0,235,40960
v_accvgpr_read_b32 v159,a211
maaa 184,56,8
ta 2,235,41472
v_accvgpr_read_b32 v152,a224
maaa 188,56,4
dl 226,36,52,2048
maaa 188,60,80
v_accvgpr_read_b32 v153,a225
v_accvgpr_read_b32 v154,a226
maaa 204,48,80
ta 56,236,40960
v_accvgpr_read_b32 v155,a227
maaa 204,52,4
ta 58,236,41472
v_add_u32_e32 v233,0x10000,v233
maaa 172,68,4
ta 60,236,57344
v_add_u32_e32 v232,0x10000,v232
maaa 172,64,80
dl 227,36,52,3072
maaa 156,76,80
s_add_u32 s52,s52,s56
s_add_u32 m0,s98,4096
ta 62,236,57856
maaa 156,72,4
ta 48,237,57344
maaa 152,72,8
ta 50,237,57856
maaa 152,76,84
ta 52,237,40960
maaa 168,64,84
dl 224,36,52,0
maaa 168,68,8
ta 54,237,41472
maaa 164,68,12
maaa 164,64,88
maaa 148,76,88
maaa 148,72,12
buffer_store_dwordx4 v[180:183],v244,s[24:27],s67 offen offset:1024 sc0 sc1
maaa 144,72,16
maaa 144,76,92
maaa 160,64,92
ta 72,238,45056
maaa 160,68,16
ta 74,238,45568
maaa 128,236,16
dl 225,36,52,1024
maaa 128,220,92
ta 76,238,61440
maaa 112,232,92
ta 78,238,61952
maaa 112,216,16
ta 64,239,61440
maaa 116,216,12
ta 66,239,61952
maaa 116,232,88
ta 68,239,45056
maaa 132,220,88
dl 226,36,52,2048
maaa 132,236,12
ta 70,239,45568
maaa 136,236,8
maaa 136,220,84
maaa 120,232,84
maaa 120,216,8
dl 227,36,52,3072
maaa 124,216,4
s_add_u32 s52,s52,s60
maaa 124,232,80
maaa 140,220,80
maaa 140,236,4
s_waitcnt lgkmcnt(1)
mvaa 40,40,4
buffer_store_dwordx4 v[184:187],v244,s[24:27],s67 offen offset:2048 sc0 sc1
mvaa 40,44,80
mvaa 44,20,80
mvaa 44,0,4
mvaa 52,0,8
mvaa 52,20,84
mvaa 48,44,84
mvaa 48,40,8
mvaa 56,40,12
mvaa 56,44,88
mvaa 60,20,88
mvaa 60,0,12
mvaa 68,0,16
mvaa 68,20,92
mvaa 64,44,92
mvaa 64,40,16
mvaa 32,56,16
s_waitcnt vmcnt(11) lgkmcnt(0)
s_barrier
s_add_u32 m0,s62,32768
mvaa 32,60,92
dl 228,40,53,0
mvaa 36,48,92
ta 216,234,0
mvaa 36,52,16
ta 218,234,512
mvaa 28,52,12
ta 232,234,16384
mvaa 28,48,88
ta 234,234,16896
mvaa 24,60,88
dl 229,40,53,1024
mvaa 24,56,12
s_add_u32 s53,s53,s57
ta 220,235,16384
mvaa 16,56,8
ta 222,235,16896
mvaa 16,60,84
ta 236,235,0
mvaa 20,48,84
ta 238,235,512
mvaa 20,52,8
dl 230,44,54,2048
mvaa 12,52,4
ta 0,236,0
mvaa 12,48,80
ta 2,236,512
mvaa 8,60,80
ta 20,236,16384
mvaa 8,56,4
ta 22,236,16896
mvaa 76,4,72
dl 231,44,54,3072
mvaa 76,80,76
s_add_u32 m0,s62,49152
s_add_u32 s54,s54,s57
ta 44,237,16384
mvaa 96,80,64
ta 46,237,16896
mvaa 96,4,68
ta 40,237,0
mvaa 100,8,68
ta 42,237,512
mvaa 100,84,64
dl 228,40,53,0
mvaa 80,84,76
ta 52,234,4096
mvaa 80,8,72
ta 54,234,4608
mvaa 84,12,72
ta 48,234,20480
mvaa 84,88,76
ta 50,234,20992
mvaa 104,88,64
dl 229,40,53,1024
mvaa 104,12,68
s_add_u32 s53,s53,s58
ta 60,235,20480
mvaa 108,16,68
ta 62,235,20992
mvaa 108,92,64
ta 56,235,4096
mvaa 88,92,76
ta 58,235,4608
mvaa 88,16,72
dl 230,44,54,2048
s_waitcnt lgkmcnt(8)
mvaa 0,216,24
ta 228,234,8192
mvaa 0,232,96
ta 68,236,4096
mvaa 4,220,96
ta 70,236,4608
mvaa 4,236,24
ta 64,236,20480
mvaa 92,236,28
ta 66,236,20992
mvaa 92,220,100
dl 231,44,54,3072
mvaa 72,232,100
s_add_u32 m0,s98,32768
ta 76,237,20480
s_add_u32 s54,s54,s58
mvaa 72,216,28
ta 78,237,20992
mvaa 144,216,32
ta 72,237,4096
mvaa 144,232,104
ta 74,237,4608
mvaa 148,220,104
dl 224,36,52,0
mvaa 148,236,32
ta 230,234,8704
mvaa 156,236,36
ta 212,234,24576
mvaa 156,220,108
ta 214,234,25088
mvaa 152,232,108
ta 224,235,24576
mvaa 152,216,36
dl 225,36,52,1024
maaa 192,0,36
ta 226,235,25088
maaa 192,20,108
ta 208,235,8192
maaa 176,44,108
ra 80,233,0
maaa 176,40,36
ta 210,235,8704
maaa 180,40,32
dl 226,36,52,2048
maaa 180,44,104
ra 4,232,0
v_accvgpr_read_b32 v160,a192
maaa 196,20,104
ra 84,233,2048
v_accvgpr_read_b32 v161,a193
maaa 196,0,32
ra 8,232,2048
v_accvgpr_read_b32 v162,a194
maaa 200,0,28
ra 12,232,4096
v_accvgpr_read_b32 v163,a195
maaa 200,20,100
buffer_store_dwordx4 v[188:191],v244,s[24:27],s67 offen offset:3072 sc0 sc1
maaa 184,44,100
s_mov_b32 s67,24576
ra 88,233,4096
v_accvgpr_read_b32 v164,a176
maaa 184,40,28
ra 92,233,6144
v_accvgpr_read_b32 v165,a177
maaa 188,40,24
ra 16,232,6144
v_accvgpr_read_b32 v166,a178
maaa 188,44,96
ta 236,236,8192
v_accvgpr_read_b32 v167,a179
maaa 204,20,96
ta 238,236,8704
v_accvgpr_read_b32 v172,a180
maaa 204,0,24
dl 227,36,52,3072
s_waitcnt lgkmcnt(8)
maaa 172,52,24
s_add_u32 s52,s52,s56
s_add_u32 m0,s98,36864
ta 220,236,24576
maaa 172,48,96
ta 222,236,25088
v_accvgpr_read_b32 v173,a181
maaa 156,60,96
L_vs12:
s_cbranch_execz L_vs16
ta 232,237,24576
v_accvgpr_read_b32 v174,a182
maaa 156,56,24
ta 234,237,25088
v_accvgpr_read_b32 v175,a183
maaa 152,56,28
dl 224,36,52,0
maaa 152,60,100
ta 216,237,8192
v_accvgpr_read_b32 v168,a196
maaa 168,48,100
ta 218,237,8704
v_accvgpr_read_b32 v169,a197
maaa 168,52,28
ta 40,238,12288
v_accvgpr_read_b32 v170,a198
maaa 164,52,32
ta 42,238,12800
v_accvgpr_read_b32 v171,a199
maaa 164,48,104
dl 225,36,52,1024
maaa 148,60,104
ta 44,238,28672
v_add_u32_e32 v233,0xffff0000,v233
maaa 148,56,32
ta 46,238,29184
v_add_u32_e32 v232,0xffff0000,v232
maaa 144,56,36
ta 20,239,28672
maaa 144,60,108
ta 22,239,29184
maaa 160,48,108
buffer_store_dwordx4 v[192:195],v244,s[24:27],s67 offen offset:0 sc0 sc1
maaa 160,52,36
ta 0,239,12288
maaa 128,68,36
ta 2,239,12800
maaa 128,64,108
maaa 112,76,108
maaa 112,72,36
maaa 116,72,32
dl 226,36,52,2048
maaa 116,76,104
maaa 132,64,104
maaa 132,68,32
maaa 136,68,28
maaa 136,64,100
dl 227,36,52,3072
maaa 120,76,100
s_add_u32 s52,s52,s60
maaa 120,72,28
maaa 124,72,24
maaa 124,76,96
maaa 140,64,96
buffer_store_dwordx4 v[196:199],v244,s[24:27],s67 offen offset:1024 sc0 sc1
maaa 140,68,24
mvaa 40,228,24
mvaa 40,212,96
mvaa 44,224,96
mvaa 44,208,24
mvaa 52,208,28
mvaa 52,224,100
mvaa 48,212,100
mvaa 48,228,28
mvaa 56,228,32
mvaa 56,212,104
mvaa 60,224,104
mvaa 60,208,32
mvaa 68,208,36
mvaa 68,224,108
mvaa 64,212,108
mvaa 64,228,36
s_waitcnt lgkmcnt(0)
mvaa 32,236,36
s_waitcnt vmcnt(11)
s_barrier
s_add_u32 m0,s62,0
mvaa 32,220,108
dl 228,40,53,0
mvaa 36,232,108
ta 56,234,32768
mvaa 36,216,36
ta 58,234,33280
mvaa 28,216,32
ta 60,234,49152
mvaa 28,232,104
ta 62,234,49664
mvaa 24,220,104
dl 229,40,53,1024
mvaa 24,236,32
s_add_u32 s53,s53,s57
ta 48,235,49152
mvaa 16,236,28
ta 50,235,49664
mvaa 16,220,100
ta 52,235,32768
mvaa 20,232,100
ta 54,235,33280
mvaa 20,216,28
dl 230,44,54,2048
mvaa 12,216,24
ta 72,236,32768
mvaa 12,232,96
ta 74,236,33280
mvaa 8,220,96
ta 76,236,49152
mvaa 8,236,24
ta 78,236,49664
mvaa 76,24,40
dl 231,44,54,3072
mvaa 76,96,44
s_add_u32 m0,s62,16384
s_add_u32 s54,s54,s57
ta 64,237,49152
mvaa 96,96,20
ta 66,237,49664
mvaa 96,24,0
ta 68,237,32768
mvaa 100,28,0
ta 70,237,33280
mvaa 100,100,20
dl 228,40,53,0
mvaa 80,100,44
ta 208,234,36864
mvaa 80,28,40
ta 210,234,37376
mvaa 84,32,40
ta 224,234,53248
mvaa 84,104,44
ta 226,234,53760
mvaa 104,104,20
ta 212,235,53248
mvaa 104,32,0
dl 229,40,53,1024
mvaa 108,36,0
s_add_u32 s53,s53,s58
ta 214,235,53760
mvaa 108,108,20
ta 228,235,36864
mvaa 88,108,44
ta 230,235,37376
mvaa 88,36,40
ta 192,236,40960
s_waitcnt lgkmcnt(9)
mvaa 0,56,4
dl 230,44,54,2048
mvaa 0,60,80
ta 216,236,36864
mvaa 4,48,80
ta 218,236,37376
mvaa 4,52,4
ta 232,236,53248
mvaa 92,52,8
ta 234,236,53760
mvaa 92,48,84
dl 231,44,54,3072
mvaa 72,60,84
s_add_u32 m0,s98,65536
ta 220,237,53248
s_add_u32 s54,s54,s58
mvaa 72,56,8
ta 222,237,53760
mvaa 144,56,12
ta 236,237,36864
mvaa 144,60,88
ta 238,237,37376
mvaa 148,48,88
dl 224,36,52,0
mvaa 148,52,12
ta 194,236,41472
mvaa 156,52,16
ta 176,236,57344
mvaa 156,48,92
ta 178,236,57856
mvaa 152,60,92
ta 196,237,57344
mvaa 152,56,16
ta 198,237,57856
mvaa 160,72,16
dl 225,36,52,1024
mvaa 160,76,92
ta 180,237,40960
mvaa 164,64,92
ta 182,237,41472
mvaa 164,68,16
ra 96,233,0
mvaa 172,68,12
ta 0,234,40960
mvaa 172,64,88
dl 226,36,52,2048
mvaa 168,76,88
ta 2,234,41472
mvaa 168,72,12
ta 20,234,57344
maaa 200,72,8
ta 22,234,57856
maaa 200,76,84
ta 44,235,57344
maaa 184,64,84
buffer_store_dwordx4 v[200:203],v244,s[24:27],s67 offen offset:2048 sc0 sc1
maaa 184,68,8
ta 46,235,57856
maaa 188,68,4
ta 40,235,40960
maaa 188,64,80
ta 42,235,41472
v_accvgpr_read_b32 v176,a200
maaa 204,76,80
ra 24,232,0
v_accvgpr_read_b32 v177,a201
maaa 204,72,4
dl 227,36,52,3072
s_waitcnt lgkmcnt(8)
maaa 172,208,4
s_add_u32 s52,s52,s56
s_add_u32 m0,s98,69632
ra 100,233,2048
maaa 172,224,80
ra 28,232,2048
v_accvgpr_read_b32 v178,a202
maaa 156,212,80
ra 32,232,4096
v_accvgpr_read_b32 v179,a203
maaa 156,228,4
ra 104,233,4096
v_accvgpr_read_b32 v180,a184
maaa 152,228,8
ra 108,233,6144
v_accvgpr_read_b32 v181,a185
maaa 152,212,84
dl 224,36,52,0
maaa 168,224,84
ra 36,232,6144
v_accvgpr_read_b32 v182,a186
maaa 168,208,8
ta 52,238,45056
v_accvgpr_read_b32 v183,a187
maaa 164,208,12
ta 54,238,45568
v_accvgpr_read_b32 v188,a188
maaa 164,224,88
ta 48,238,61440
v_accvgpr_read_b32 v189,a189
maaa 148,212,88
buffer_store_dwordx4 v[204:207],v244,s[24:27],s67 offen offset:3072 sc0 sc1
maaa 148,228,12
s_mov_b32 s67,28672
ta 50,238,61952
v_accvgpr_read_b32 v190,a190
maaa 144,228,16
ta 60,239,61440
v_accvgpr_read_b32 v184,a204
maaa 144,212,92
ta 62,239,61952
v_accvgpr_read_b32 v191,a191
maaa 160,224,92
ta 56,239,45056
v_accvgpr_read_b32 v185,a205
maaa 160,208,16
dl 225,36,52,1024
maaa 128,216,16
ta 58,239,45568
v_accvgpr_read_b32 v186,a206
maaa 128,232,92
v_accvgpr_read_b32 v187,a207
maaa 112,220,92
maaa 112,236,16
maaa 116,236,12
dl 226,36,52,2048
maaa 116,220,88
maaa 132,232,88
maaa 132,216,12
maaa 136,216,8
maaa 136,232,84
dl 227,36,52,3072
maaa 120,220,84
)"
R"(s_add_u32 s52,s52,s60
maaa 120,236,8
maaa 124,236,4
maaa 124,220,80
maaa 140,232,80
maaa 140,216,4
buffer_store_dwordx4 v[208:211],v244,s[24:27],s67 offen offset:0 sc0 sc1
s_waitcnt lgkmcnt(0)
mvaa 40,0,4
mvaa 40,20,80
mvaa 44,44,80
mvaa 44,40,4
mvaa 52,40,8
mvaa 52,44,84
mvaa 48,20,84
mvaa 48,0,8
mvaa 56,0,12
mvaa 56,20,88
mvaa 60,44,88
mvaa 60,40,12
mvaa 68,40,16
mvaa 68,44,92
mvaa 64,20,92
mvaa 64,0,16
mvaa 32,192,16
s_waitcnt vmcnt(11)
s_barrier
s_add_u32 m0,s62,32768
mvaa 32,176,92
dl 228,40,53,0
mvaa 36,196,92
ta 68,234,0
mvaa 36,180,16
ta 70,234,512
mvaa 28,180,12
ta 64,234,16384
mvaa 28,196,88
ta 66,234,16896
mvaa 24,176,88
dl 229,40,53,1024
mvaa 24,192,12
s_add_u32 s53,s53,s57
ta 76,235,16384
mvaa 16,192,8
ta 78,235,16896
mvaa 16,176,84
ta 72,235,0
mvaa 20,196,84
ta 74,235,512
mvaa 20,180,8
ta 228,236,0
mvaa 12,180,4
dl 230,44,54,2048
mvaa 12,196,80
ta 230,236,512
mvaa 8,176,80
ta 212,236,16384
mvaa 8,192,4
ta 214,236,16896
mvaa 76,4,52
ta 224,237,16384
mvaa 76,80,48
dl 231,44,54,3072
mvaa 96,80,60
s_add_u32 m0,s62,49152
s_add_u32 s54,s54,s57
ta 226,237,16896
mvaa 96,4,56
ta 208,237,0
mvaa 100,8,56
ta 210,237,512
mvaa 100,84,60
ta 236,234,4096
mvaa 80,84,48
dl 228,40,53,0
mvaa 80,8,52
ta 238,234,4608
mvaa 84,12,52
ta 220,234,20480
mvaa 84,88,48
ta 222,234,20992
mvaa 104,88,60
ta 232,235,20480
mvaa 104,12,56
dl 229,40,53,1024
mvaa 108,16,56
s_add_u32 s53,s53,s58
ta 234,235,20992
mvaa 108,92,60
ta 216,235,4096
mvaa 88,92,48
ta 218,235,4608
mvaa 88,16,52
ta 40,236,4096
s_waitcnt lgkmcnt(8)
mvaa 0,68,24
dl 230,44,54,2048
mvaa 0,64,96
ta 42,236,4608
mvaa 4,76,96
ta 44,236,20480
mvaa 4,72,24
ta 46,236,20992
mvaa 92,72,28
ta 20,237,20480
mvaa 92,76,100
ta 22,237,20992
mvaa 72,64,100
dl 231,44,54,3072
mvaa 72,68,28
s_add_u32 m0,s98,0
ta 200,238,12288
s_add_u32 s54,s54,s58
mvaa 144,68,32
ta 202,238,12800
mvaa 144,64,104
ta 0,237,4096
mvaa 148,76,104
ta 2,237,4608
mvaa 148,72,32
dl 224,36,52,0
mvaa 156,72,36
ta 184,238,28672
mvaa 156,76,108
ta 186,238,29184
mvaa 152,64,108
ta 204,239,28672
mvaa 152,68,36
ta 206,239,29184
mvaa 160,228,36
dl 225,36,52,1024
mvaa 160,212,108
ta 188,239,12288
mvaa 164,224,108
ta 190,239,12800
mvaa 164,208,36
ta 180,234,8192
mvaa 172,208,32
ta 182,234,8704
mvaa 172,224,104
dl 226,36,52,2048
mvaa 168,212,104
ta 196,234,24576
mvaa 168,228,32
ta 198,234,25088
mvaa 176,228,28
ta 176,235,24576
mvaa 176,212,100
ta 178,235,25088
mvaa 180,224,100
ta 192,235,8192
mvaa 180,208,28
buffer_store_dwordx4 v[212:215],v244,s[24:27],s67 offen offset:1024 sc0 sc1
mvaa 188,208,24
ta 194,235,8704
mvaa 188,224,96
ra 4,232,32768
mvaa 184,212,96
ra 80,233,32768
mvaa 184,228,24
ra 84,233,34816
s_waitcnt lgkmcnt(9)
maaa 172,236,24
dl 227,36,52,3072
maaa 172,220,96
s_add_u32 s52,s52,s56
s_add_u32 m0,s98,4096
ra 8,232,34816
maaa 156,232,96
ra 88,233,36864
maaa 156,216,24
ta 56,236,8192
maaa 152,216,28
ra 12,232,36864
v_accvgpr_read_b32 v192,a172
maaa 152,232,100
dl 224,36,52,0
maaa 168,220,100
ta 58,236,8704
v_accvgpr_read_b32 v193,a173
maaa 168,236,28
ta 60,236,24576
v_accvgpr_read_b32 v194,a174
maaa 164,236,32
ta 62,236,25088
v_accvgpr_read_b32 v195,a175
maaa 164,220,104
ta 48,237,24576
v_accvgpr_read_b32 v196,a156
maaa 148,232,104
buffer_store_dwordx4 v[216:219],v244,s[24:27],s67 offen offset:2048 sc0 sc1
maaa 148,216,32
ta 50,237,25088
v_accvgpr_read_b32 v197,a157
maaa 144,216,36
ra 92,233,38912
v_accvgpr_read_b32 v198,a158
maaa 144,232,108
ra 16,232,38912
v_accvgpr_read_b32 v199,a159
maaa 160,220,108
ta 52,237,8192
v_accvgpr_read_b32 v204,a152
maaa 160,236,36
ta 54,237,8704
v_accvgpr_read_b32 v205,a153
maaa 128,40,36
dl 225,36,52,1024
maaa 128,44,108
v_accvgpr_read_b32 v206,a154
v_accvgpr_read_b32 v207,a155
maaa 112,20,108
v_accvgpr_read_b32 v200,a168
v_accvgpr_read_b32 v201,a169
maaa 112,0,36
v_accvgpr_read_b32 v202,a170
v_accvgpr_read_b32 v203,a171
maaa 116,0,32
v_add_u32_e32 v233,0x10000,v233
v_add_u32_e32 v232,0x10000,v232
maaa 116,20,104
dl 226,36,52,2048
maaa 132,44,104
maaa 132,40,32
maaa 136,40,28
maaa 136,44,100
maaa 120,20,100
dl 227,36,52,3072
maaa 120,0,28
s_add_u32 s52,s52,s60
maaa 124,0,24
maaa 124,20,96
maaa 140,44,96
maaa 140,40,24
buffer_store_dwordx4 v[220:223],v244,s[24:27],s67 offen offset:3072 sc0 sc1
mvaa 40,180,24
s_mov_b32 s67,0x5550055
s_mov_b32 s26,2097152
v_cndmask_b32_e64 v116,v241,0,s[66:67]
s_waitcnt lgkmcnt(0)
mvaa 40,196,96
s_mov_b32 s66,0xa0000
s_mov_b32 s67,0xaaa00aa
v_cndmask_b32_e64 v117,v241,0,s[66:67]
mvaa 44,176,96
s_mov_b32 s66,0x150001
s_mov_b32 s67,0x15550155
v_cndmask_b32_e64 v118,v241,0,s[66:67]
mvaa 44,192,24
L_vs13:
s_cbranch_execz L_vr0
s_mov_b32 s66,0x2a0002
s_mov_b32 s67,0x2aaa02aa
v_cndmask_b32_e64 v119,v241,0,s[66:67]
mvaa 52,192,28
s_lshr_b32 s66,s18,2
s_lshl_b32 s67,s18,13
s_lshl_b32 s66,s66,13
mvaa 52,176,100
s_mov_b32 s27,131072
v_cndmask_b32_e64 v120,v116,v241,s[64:65]
v_cndmask_b32_e64 v121,v117,v241,s[64:65]
mvaa 48,196,100
s_mov_b64 s[24:25],s[82:83]
v_cndmask_b32_e64 v122,v118,v241,s[64:65]
v_cndmask_b32_e64 v123,v119,v241,s[64:65]
mvaa 48,180,28
s_mov_b32 s83,0
s_or_b32 s82,s15,16384
mvaa 56,180,32
mvaa 56,196,104
mvaa 60,176,104
mvaa 60,192,32
mvaa 68,192,36
mvaa 68,176,108
mvaa 64,196,108
mvaa 64,180,36
mvaa 32,56,36
s_waitcnt vmcnt(11)
s_barrier
s_add_u32 m0,s62,0
mvaa 32,60,108
dl 228,40,53,0
mvaa 36,48,108
ta 72,234,32768
mvaa 36,52,36
ta 74,234,33280
mvaa 28,52,32
ta 76,234,49152
mvaa 28,48,104
ta 78,234,49664
mvaa 24,60,104
ta 64,235,49152
mvaa 24,56,32
dl 229,40,53,1024
mvaa 16,56,28
s_add_u32 s53,s53,s57
ta 66,235,49664
mvaa 16,60,100
ta 68,235,32768
mvaa 20,48,100
ta 70,235,33280
mvaa 20,52,28
ta 208,236,32768
mvaa 12,52,24
dl 230,44,54,2048
mvaa 12,48,96
ta 210,236,33280
mvaa 8,60,96
ta 224,236,49152
mvaa 8,56,24
ta 226,236,49664
mvaa 76,24,200
ta 212,237,49152
mvaa 76,96,184
dl 231,44,54,3072
mvaa 96,96,204
s_add_u32 m0,s62,16384
s_add_u32 s54,s54,s57
ta 214,237,49664
mvaa 96,24,188
ta 228,237,32768
mvaa 100,28,188
ta 230,237,33280
mvaa 100,100,204
ta 216,234,36864
mvaa 80,100,184
ta 218,234,37376
mvaa 80,28,200
dl 228,40,53,0
mvaa 84,32,200
ta 232,234,53248
mvaa 84,104,184
ta 234,234,53760
mvaa 104,104,204
ta 220,235,53248
mvaa 104,32,188
ta 222,235,53760
mvaa 108,36,188
dl 229,40,53,1024
mvaa 108,108,204
s_add_u32 s53,s53,s58
ta 236,235,36864
mvaa 88,108,184
ta 238,235,37376
mvaa 88,36,200
ta 0,236,36864
s_waitcnt lgkmcnt(8)
mvaa 0,72,4
ta 2,236,37376
mvaa 0,76,80
dl 230,44,54,2048
mvaa 4,64,80
ta 20,236,53248
mvaa 4,68,4
ta 22,236,53760
mvaa 92,68,8
ta 44,237,53248
mvaa 92,64,84
ta 46,237,53760
mvaa 72,76,84
dl 231,44,54,3072
mvaa 72,72,8
s_add_u32 m0,s98,32768
ta 40,237,36864
s_add_u32 s54,s54,s58
mvaa 144,72,12
ta 42,237,37376
mvaa 144,76,88
ta 192,234,40960
mvaa 148,64,88
ta 194,234,41472
mvaa 148,68,12
ta 176,234,57344
mvaa 156,68,16
dl 224,36,52,0
mvaa 156,64,92
ta 178,234,57856
mvaa 152,76,92
ta 196,235,57344
mvaa 152,72,16
ta 198,235,57856
mvaa 160,208,16
ta 180,235,40960
mvaa 160,224,92
dl 225,36,52,1024
mvaa 164,212,92
ta 182,235,41472
mvaa 164,228,16
ta 52,236,40960
mvaa 172,228,12
ta 54,236,41472
mvaa 172,212,88
ta 48,236,57344
mvaa 168,224,88
s_mov_b64 exec,3
global_load_dword a250,v253,s[96:97]
s_mov_b64 exec,-1
mvaa 168,208,12
ta 50,236,57856
v_lshrrev_b32_e32 v253,1,v244
mvaa 176,208,8
ta 60,237,57344
mvaa 176,224,84
ta 62,237,57856
mvaa 180,212,84
ta 56,237,40960
mvaa 180,228,8
dl 226,36,52,2048
mvaa 188,228,4
ta 58,237,41472
mvaa 188,212,80
ra 96,233,0
mvaa 184,224,80
ra 24,232,0
mvaa 184,208,4
ra 100,233,2048
s_waitcnt lgkmcnt(8)
mvaa 192,216,4
dl 227,36,52,3072
mvaa 192,232,80
s_add_u32 s52,s52,s56
s_add_u32 m0,s98,36864
ra 28,232,2048
mvaa 196,220,80
ra 104,233,4096
mvaa 196,236,4
ra 32,232,4096
mvaa 204,236,8
ta 188,238,45056
mvaa 204,220,84
ta 190,238,45568
mvaa 200,232,84
dl 224,36,52,0
mvaa 200,216,8
ta 204,238,61440
maaa 164,216,12
ta 206,238,61952
maaa 164,232,88
ta 184,239,61440
maaa 148,220,88
ta 186,239,61952
maaa 148,236,12
dl 225,36,52,1024
maaa 144,236,16
ra 108,233,6144
v_add_u32_e32 v233,0xffff0000,v233
maaa 144,220,92
ra 36,232,6144
v_add_u32_e32 v232,0xffff0000,v232
maaa 160,232,92
ta 200,239,45056
v_accvgpr_read_b32 v208,a164
maaa 160,216,16
ta 202,239,45568
v_accvgpr_read_b32 v209,a165
maaa 128,0,16
dl 226,36,52,2048
maaa 128,20,92
v_accvgpr_read_b32 v210,a166
v_accvgpr_read_b32 v211,a167
maaa 112,44,92
v_accvgpr_read_b32 v212,a148
v_accvgpr_read_b32 v213,a149
maaa 112,40,16
v_accvgpr_read_b32 v214,a150
v_accvgpr_read_b32 v215,a151
maaa 116,40,12
v_accvgpr_read_b32 v220,a144
v_accvgpr_read_b32 v221,a145
maaa 116,44,88
dl 227,36,52,3072
maaa 132,20,88
s_add_u32 s52,s52,s60
v_accvgpr_read_b32 v222,a146
v_accvgpr_read_b32 v223,a147
maaa 132,0,12
s_lshl_b32 s60,%1,7
v_accvgpr_read_b32 v216,a160
v_accvgpr_read_b32 v217,a161
maaa 136,0,8
s_and_b32 s87,s60,7552
v_accvgpr_read_b32 v218,a162
v_accvgpr_read_b32 v219,a163
maaa 136,20,84
s_add_u32 s61,s60,83914752
maaa 120,44,84
maaa 120,40,8
maaa 124,40,4
maaa 124,44,80
maaa 140,20,80
maaa 140,0,4
mvaa 40,192,4
mvaa 40,176,80
mvaa 44,196,80
mvaa 44,180,4
mvaa 52,180,8
mvaa 52,196,84
mvaa 48,176,84
mvaa 48,192,8
mvaa 56,192,12
s_waitcnt vmcnt(9) lgkmcnt(0)
s_barrier
mvaa 56,176,88
s_cmp_eq_u32 s12,0
s_cselect_b64 exec,1,0
buffer_atomic_add v247,off,s[8:11],s85 sc1
s_mov_b64 exec,-1
mvaa 60,196,88
s_add_u32 m0,s62,32768
ta 172,234,0
s_lshr_b32 s85,s86,1
mvaa 60,180,12
s_lshl_b32 s85,s85,11
ta 174,234,512
v_bfe_u32 v247,v244,2,6
mvaa 68,180,16
ta 156,234,16384
mvaa 68,196,92
ta 158,234,16896
mvaa 64,176,92
dl 228,40,53,0
mvaa 64,192,16
ta 168,235,16384
mvaa 32,52,16
ta 170,235,16896
mvaa 32,48,92
ta 152,235,0
mvaa 36,60,92
ta 154,235,512
mvaa 36,56,16
dl 229,40,53,1024
mvaa 28,56,12
s_add_u32 s53,s53,s57
ta 68,236,0
mvaa 28,60,88
ta 70,236,512
mvaa 24,48,88
ta 64,236,16384
mvaa 24,52,12
ta 66,236,16896
mvaa 16,52,8
dl 230,44,54,2048
mvaa 16,48,84
ta 76,237,16384
mvaa 20,60,84
ta 78,237,16896
mvaa 20,56,8
ta 72,237,0
mvaa 12,56,4
ta 74,237,512
mvaa 12,60,80
dl 231,44,54,3072
mvaa 8,48,80
s_add_u32 m0,s62,49152
s_add_u32 s54,s54,s57
ta 228,234,4096
mvaa 8,52,4
ta 230,234,4608
mvaa 76,4,188
ta 212,234,20480
mvaa 76,80,204
ta 214,234,20992
mvaa 96,80,184
dl 228,40,53,0
mvaa 96,4,200
ta 224,235,20480
mvaa 100,8,200
ta 226,235,20992
mvaa 100,84,184
ta 208,235,4096
mvaa 80,84,204
ta 210,235,4608
mvaa 80,8,188
dl 229,40,53,1024
mvaa 84,12,188
s_add_u32 s53,s53,s58
ta 236,236,4096
mvaa 84,88,204
ta 238,236,4608
mvaa 104,88,184
ta 220,236,20480
mvaa 104,12,200
ta 222,236,20992
mvaa 108,16,200
dl 230,44,54,2048
mvaa 108,92,184
ta 232,237,20480
mvaa 88,92,204
ta 234,237,20992
mvaa 88,16,188
ta 216,237,4096
s_waitcnt lgkmcnt(8)
mvaa 0,172,24
ta 218,237,4608
mvaa 0,156,96
dl 231,44,54,3072
mvaa 4,168,96
s_add_u32 m0,s98,65536
ta 40,234,8192
s_add_u32 s54,s54,s58
mvaa 4,152,24
ta 42,234,8704
mvaa 92,152,28
ta 44,234,24576
mvaa 92,168,100
ta 46,234,25088
mvaa 72,156,100
dl 224,36,52,0
mvaa 72,172,28
ta 20,235,24576
mvaa 144,172,32
ta 22,235,25088
mvaa 144,156,104
ta 0,235,8192
mvaa 148,168,104
ta 2,235,8704
mvaa 148,152,32
dl 225,36,52,1024
mvaa 156,152,36
ta 180,236,8192
mvaa 156,168,108
ta 182,236,8704
mvaa 152,156,108
ta 196,236,24576
mvaa 152,172,36
ta 198,236,25088
mvaa 160,68,36
dl 226,36,52,2048
mvaa 160,64,108
ta 176,237,24576
mvaa 164,76,108
ta 178,237,25088
mvaa 164,72,36
ta 192,237,8192
mvaa 172,72,32
ta 194,237,8704
mvaa 172,76,104
dl 227,36,52,3072
mvaa 168,64,104
s_add_u32 s52,s52,s56
s_add_u32 m0,s98,69632
ta 56,238,12288
mvaa 168,68,32
s_mov_b32 s98,s94
ta 58,238,12800
s_mov_b32 s99,s98
mvaa 176,68,28
s_and_b32 s94,s12,1
ta 60,238,28672
s_lshl_b32 s94,s94,14
mvaa 176,64,100
s_mul_i32 s56,s59,12288
ta 62,238,29184
mvaa 180,76,100
dl 224,36,52,0
mvaa 180,72,28
ta 48,239,28672
v_mov_b32_e32 v224,v244
mvaa 188,72,24
ta 50,239,29184
mvaa 188,76,96
ta 52,239,12288
mvaa 184,64,96
ta 54,239,12800
mvaa 184,68,24
dl 225,36,52,1024
mvaa 192,228,24
ra 80,233,0
v_add_u32_e32 v225,65536,v224
mvaa 192,212,96
ra 4,232,0
mvaa 196,224,96
ra 84,233,2048
s_waitcnt lgkmcnt(8)
mvaa 196,208,24
ra 8,232,2048
mvaa 204,208,28
dl 226,36,52,2048
mvaa 204,224,100
ra 88,233,4096
v_mov_b32_e32 v226,1065369472
mvaa 200,212,100
ra 12,232,4096
mvaa 200,228,28
ra 16,232,6144
v_add_u32_e32 v232,0x8000,v232
mvaa 208,228,32
ra 92,233,6144
v_add_u32_e32 v233,0x8000,v233
mvaa 208,212,104
dl 227,36,52,3072
mvaa 212,224,104
s_mov_b64 s[36:37],s[28:29]
s_mov_b32 s38,8388608
s_mov_b32 s39,131072
mvaa 212,208,32
s_lshl_b32 s52,s12,12
mvaa 220,208,36
mvaa 220,224,108
mvaa 216,212,108
mvaa 216,228,36
maaa 128,236,36
maaa 128,220,108
maaa 112,232,108
maaa 112,216,36
maaa 116,216,32
maaa 116,232,104
maaa 132,220,104
maaa 132,236,32
maaa 136,236,28
maaa 136,220,100
maaa 120,232,100
maaa 120,216,28
maaa 124,216,24
maaa 124,232,96
maaa 140,220,96
s_waitcnt vmcnt(8) lgkmcnt(0)
s_barrier
s_add_u32 m0,s62,0
maaa 140,236,24
dl 228,40,53,0
mvaa 40,40,24
ta 200,234,32768
mvaa 40,44,96
ta 202,234,33280
mvaa 44,20,96
ta 184,234,49152
mvaa 44,0,24
ta 186,234,49664
mvaa 52,0,28
dl 229,40,53,1024
mvaa 52,20,100
s_add_u32 s53,s53,s57
ta 204,235,49152
mvaa 48,44,100
ta 206,235,49664
mvaa 48,40,28
ta 188,235,32768
mvaa 56,40,32
ta 190,235,33280
mvaa 56,44,104
dl 230,44,54,2048
mvaa 60,20,104
ta 164,236,32768
mvaa 60,0,32
ta 166,236,33280
mvaa 68,0,36
ta 148,236,49152
mvaa 68,20,108
ta 150,236,49664
mvaa 64,44,108
dl 231,44,54,3072
mvaa 64,40,36
s_add_u32 m0,s62,16384
s_add_u32 s54,s54,s57
ta 160,237,49152
mvaa 32,180,36
ta 162,237,49664
mvaa 32,196,108
ta 144,237,32768
mvaa 36,176,108
ta 146,237,33280
mvaa 36,192,36
dl 228,40,53,0
mvaa 28,192,32
ta 152,234,36864
mvaa 28,176,104
ta 154,234,37376
mvaa 24,196,104
ta 168,234,53248
mvaa 24,180,32
ta 170,234,53760
mvaa 16,180,28
dl 229,40,53,1024
mvaa 16,196,100
s_add_u32 s53,s53,s58
ta 156,235,53248
mvaa 20,176,100
ta 158,235,53760
mvaa 20,192,28
ta 172,235,36864
mvaa 12,192,24
L_vs14:
s_cbranch_execz L_vr0
ta 174,235,37376
mvaa 12,176,96
dl 230,44,54,2048
mvaa 8,196,96
ta 208,236,36864
mvaa 8,180,24
ta 210,236,37376
mvaa 76,24,56
ta 224,236,53248
mvaa 76,96,60
ta 226,236,53760
mvaa 96,96,48
dl 231,44,54,3072
mvaa 96,24,52
s_add_u32 s54,s54,s58
ta 212,237,53248
s_add_u32 s58,s56,8192
mvaa 100,28,52
ta 214,237,53760
mvaa 100,100,48
ta 228,237,36864
mvaa 80,100,60
ta 230,237,37376
mvaa 80,28,56
ta 216,234,40960
mvaa 84,32,56
ta 218,234,41472
mvaa 84,104,60
ta 232,234,57344
mvaa 104,104,48
ta 234,234,57856
mvaa 104,32,52
ta 220,235,57344
mvaa 108,36,52
ta 222,235,57856
mvaa 108,108,48
ta 236,235,40960
mvaa 88,108,60
ta 238,235,41472
mvaa 88,36,56
ta 0,236,40960
s_waitcnt lgkmcnt(10)
mvaa 0,200,4
ta 2,236,41472
mvaa 0,184,80
ta 20,236,57344
mvaa 4,204,80
ta 22,236,57856
mvaa 4,188,4
ta 192,237,57344
mvaa 92,188,8
ta 194,237,57856
mvaa 92,204,84
ta 176,237,40960
mvaa 72,184,84
ta 178,237,41472
mvaa 72,200,8
ta 196,238,45056
mvaa 144,200,12
ta 198,238,45568
mvaa 144,184,88
ta 180,238,61440
mvaa 148,204,88
ta 182,238,61952
mvaa 148,188,12
ra 24,232,0
mvaa 156,188,16
ra 28,232,2048
mvaa 156,204,92
ra 32,232,4096
mvaa 152,184,92
ra 36,232,6144
mvaa 152,200,16
ta 188,239,61440
mvaa 160,164,16
ta 190,239,61952
mvaa 160,148,92
ta 204,239,45056
mvaa 164,160,92
ta 206,239,45568
mvaa 164,144,16
mvaa 172,144,12
mvaa 172,160,88
mvaa 168,148,88
mvaa 168,164,12
mvaa 176,164,8
mvaa 176,148,84
mvaa 180,160,84
mvaa 180,144,8
mvaa 188,144,4
mvaa 188,160,80
mvaa 184,148,80
mvaa 184,164,4
mvaa 192,152,4
mvaa 192,168,80
mvaa 196,156,80
mvaa 196,172,4
mvaa 204,172,8
mvaa 204,156,84
mvaa 200,168,84
mvaa 200,152,8
mvaa 208,152,12
mvaa 208,168,88
mvaa 212,156,88
mvaa 212,172,12
mvaa 220,172,16
mvaa 220,156,92
mvaa 216,168,92
mvaa 216,152,16
maaa 128,208,16
maaa 128,224,92
maaa 112,212,92
s_waitcnt vmcnt(0) lgkmcnt(0)
s_barrier
s_add_u32 m0,s62,32768
maaa 112,228,16
dl 228,40,53,0
maaa 116,228,12
ta 40,234,0
maaa 116,212,88
ta 42,234,512
maaa 132,224,88
ta 44,235,0
maaa 132,208,12
ta 46,235,512
maaa 136,208,8
dl 229,40,53,1024
maaa 136,224,84
s_add_u32 s53,s53,s57
ta 48,236,0
maaa 120,212,84
ta 50,236,512
maaa 120,228,8
ta 52,237,0
maaa 124,228,4
ta 54,237,512
maaa 124,212,80
dl 230,44,54,2048
maaa 140,224,80
ta 56,234,4096
maaa 140,208,4
ta 58,234,4608
mvaa 40,216,4
ta 60,235,4096
mvaa 40,232,80
ta 62,235,4608
mvaa 44,220,80
dl 231,44,54,3072
mvaa 44,236,4
s_add_u32 m0,s62,49152
ta 64,236,4096
s_add_u32 s54,s54,s57
mvaa 52,236,8
s_add_u32 s57,s56,4096
ta 66,236,4608
mvaa 52,220,84
ta 68,237,4096
mvaa 48,232,84
ta 70,237,4608
mvaa 48,216,8
buffer_load_dword v248,v250,s[8:11],0 offen sc1
mvaa 56,216,12
ta 72,234,8192
mvaa 56,232,88
ta 74,234,8704
mvaa 60,220,88
ta 76,235,8192
mvaa 60,236,12
ta 78,235,8704
mvaa 68,236,16
dl 228,40,53,0
mvaa 68,220,92
ta 96,236,8192
mvaa 64,232,92
ta 98,236,8704
mvaa 64,216,16
ta 100,237,8192
mvaa 32,0,16
ta 102,237,8704
mvaa 32,20,92
dl 229,40,53,1024
mvaa 36,192,92
s_lshl_b32 s43,s19,15
ta 104,238,12288
s_add_u32 s42,s43,16384
mvaa 36,176,16
s_add_u32 s53,s87,83898368
ta 106,238,12800
s_lshl_b32 s87,s91,8
mvaa 28,176,12
s_lshl_b32 s91,s23,2
ta 108,239,12288
s_mov_b32 s40,858993459
mvaa 28,192,88
s_mov_b32 s41,858993459
ta 110,239,12800
v_cndmask_b32_e64 v228,0,v226,s[40:41]
mvaa 24,20,88
dl 230,44,54,2048
mvaa 24,0,12
ta 144,234,16384
v_cndmask_b32_e64 v229,0,v226,s[40:41]
mvaa 16,0,8
ta 146,234,16896
v_cndmask_b32_e64 v230,0,v226,s[40:41]
mvaa 16,20,84
ta 164,235,16384
mvaa 20,192,84
ta 166,235,16896
mvaa 20,176,8
dl 231,44,54,3072
mvaa 12,176,4
s_add_u32 s54,s87,s88
ta 168,236,16384
s_lshl_b32 s87,s12,16
)"
R"(mvaa 12,192,80
s_add_u32 s87,s87,s54
ta 170,236,16896
s_andn2_b32 s87,s87,64
mvaa 8,20,80
s_lshl_b32 s88,s66,5
ta 244,235,24576
s_add_u32 s63,s87,s88
mvaa 8,0,4
s_lshl_b32 s88,s67,5
ta 246,235,25088
s_add_u32 s65,s87,s88
mvaa 76,4,196
s_and_b32 s87,s20,4
ta 212,235,20480
s_lshl_b32 s87,s87,8
mvaa 76,80,180
s_and_b32 s88,s20,3
ta 214,235,20992
s_add_u32 s85,s85,s87
mvaa 96,80,188
s_add_u32 s85,s85,s89
ta 236,237,20480
s_lshl_b32 s89,s88,20
mvaa 96,4,204
s_add_u32 s85,s85,s89
ta 238,237,20992
s_and_b32 s89,s86,7
mvaa 100,8,204
s_add_u32 s49,s49,s87
ta 216,236,20480
s_lshl_b32 s89,s89,7
mvaa 100,84,188
s_add_u32 s49,s49,s89
ta 218,236,20992
s_lshl_b32 s89,s88,18
mvaa 80,84,180
s_add_u32 s49,s49,s89
ta 192,234,20480
s_lshl_b32 s89,s12,6
mvaa 80,8,196
s_add_u32 s89,s89,83886080
ta 194,234,20992
s_add_u32 s91,s91,s89
mvaa 84,12,196
s_mov_b32 s46,16384
ra 20,233,0
s_mov_b32 s86,268435456
mvaa 84,88,180
s_bfe_u32 s87,s98,327691
s_cselect_b32 s87,2048,0
ra 0,232,32768
v_cndmask_b32_e64 v231,0,v226,s[40:41]
mvaa 104,88,188
s_mov_b32 s40,0xf0000
ra 80,233,4096
s_mov_b32 s41,0xfff00ff
mvaa 104,12,204
s_sub_u32 s98,s98,s87
ra 4,233,32768
v_cndmask_b32_e64 v226,0,v252,s[40:41]
mvaa 108,16,204
s_mov_b32 s40,0x3f0003
ra 84,232,36864
s_mov_b32 s41,0x3fff03ff
mvaa 108,92,188
s_sub_u32 s47,32768,s43
ra 8,233,2048
v_cndmask_b32_e64 v227,0,v252,s[40:41]
mvaa 88,92,180
s_mov_b32 s40,0xfff0ffff
ra 88,233,36864
s_mov_b32 s41,0xf000ff00
mvaa 88,16,196
s_add_u32 s47,s47,32768
ra 12,232,34816
s_lshr_b32 s45,s15,1
s_waitcnt lgkmcnt(10)
mvaa 0,40,24
s_xor_b32 s62,s63,128
ta 188,237,16384
s_xor_b32 s64,s65,128
mvaa 4,44,24
s_ff1_i32_b32 s80,s45
ta 190,237,16896
s_sub_u32 s44,s56,2048
mvaa 92,44,28
ra 92,233,6144
mvaa 72,40,28
ra 16,233,34816
mvaa 144,40,32
mvaa 148,44,32
mvaa 156,44,36
mvaa 152,40,36
mvaa 160,48,36
ra 44,232,38912
v_mov_b32_e32 v232,0xc1a00000
mvaa 164,52,36
ra 40,233,38912
v_mov_b32_e32 v233,0xc1a00000
mvaa 172,52,32
mvaa 168,48,32
mvaa 176,48,28
mvaa 180,52,28
mvaa 188,52,24
mvaa 184,48,24
mvaa 192,56,24
ta 52,239,28672
mvaa 196,60,24
ta 54,239,29184
mvaa 204,60,28
mvaa 200,56,28
mvaa 208,56,32
mvaa 212,60,32
mvaa 220,60,36
mvaa 216,56,36
maaa 128,64,36
maaa 112,68,36
maaa 116,68,32
maaa 132,64,32
maaa 136,64,28
maaa 120,68,28
maaa 124,68,24
maaa 140,64,24
mvaa 40,72,24
mvaa 44,76,24
ta 64,238,28672
ta 66,238,29184
mvaa 52,76,28
s_waitcnt vmcnt(4)
v_and_b32_e32 v248,v242,v248
v_cmp_ne_u32_e64 s[100:101],v243,v248
mvaa 48,72,28
s_waitcnt vmcnt(0)
s_barrier
ta 148,234,32768
ta 150,234,33280
ta 152,234,49152
ta 154,234,49664
ta 156,235,49152
mvaa 56,72,32
s_cmp_lg_u64 s[100:101],0
s_cbranch_scc1 L_q_spin
L_q_ok:
ta 158,235,49664
ta 160,235,32768
ta 162,235,33280
ta 172,236,32768
ta 174,236,33280
ta 176,236,49152
ta 178,236,49664
mvaa 60,76,32
buffer_load_dword v242,v247,s[8:11],s91 offen offset:0 sc1
ta 180,237,49152
ta 182,237,49664
ta 184,237,32768
ta 186,237,33280
ta 196,234,36864
mvaa 68,76,36
s_mov_b32 m0,s95
ta 198,234,37376
ta 200,234,53248
ta 202,234,53760
ta 204,235,53248
ta 206,235,53760
ta 208,235,36864
ta 210,235,37376
mvaa 64,72,36
s_lshl_b32 s35,s90,2
ta 220,236,36864
ta 222,236,37376
ta 224,236,53248
mvaa 32,96,36
s_lshl_b32 s90,s22,2
ta 226,236,53760
ta 228,237,53248
mvaa 36,100,36
s_add_u32 s90,s90,s89
ta 230,237,53760
ta 232,237,36864
mvaa 28,100,32
ds 249,8,42,0
ta 234,237,37376
ta 240,235,40960
mvaa 24,96,32
s_add_u32 s35,s35,83886080
ta 242,235,41472
ta 48,239,45056
mvaa 16,96,28
ta 50,239,45568
ta 60,239,61440
mvaa 20,100,28
ta 62,239,61952
ta 56,238,61440
mvaa 12,100,24
ta 58,238,61952
ta 68,238,45056
mvaa 8,96,24
ds 249,8,42,1024
ta 70,238,45568
ta 76,237,24576
mvaa 76,24,104
ta 78,237,25088
ta 72,237,40960
mvaa 96,24,108
ta 74,237,41472
ta 100,237,57344
mvaa 100,28,108
ta 102,237,57856
ta 96,236,57344
mvaa 80,28,104
ta 98,236,57856
ta 24,236,40960
mvaa 84,32,104
ds 249,8,42,2048
ta 26,236,41472
mvaa 104,32,108
ta 28,236,24576
ta 30,236,25088
mvaa 108,36,108
v_lshl_add_u32 v238,v244,3,s4
v_min_u32_e32 v238,L_att_end-L_att_pc-4,v238
mvaa 88,36,104
ta 32,234,24576
ta 34,234,25088
s_waitcnt lgkmcnt(15)
mvaa 0,144,20
ta 108,234,40960
ta 110,234,41472
mvaa 0,148,0
ds 249,8,42,3072
ta 36,234,57344
ta 38,234,57856
ta 104,235,57344
ta 106,235,57856
mvaa 0,152,4
v_mov_b32_e32 v234,0xc1a00000
v_mov_b32_e32 v235,0xc1a00000
mvaa 4,156,4
v_cndmask_b32_e64 v250,0,v252,s[40:41]
s_mov_b32 s40,0xffc0fffc
mvaa 4,160,0
s_mov_b32 s41,0xc000fc00
v_bfe_i32 v248,v244,4,1
v_mul_f32_e32 v0,0x3e0293ee,v0
mvaa 4,164,20
v_mul_f32_e32 v1,0x3e0293ee,v1
v_mul_f32_e32 v2,0x3e0293ee,v2
mvaa 92,164,8
buffer_load_dword v240,v247,s[8:11],s90 offen offset:0 sc1
mvaa 92,160,12
v_mul_f32_e32 v3,0x3e0293ee,v3
cv 0,0,1
mvaa 92,156,16
v_mul_f32_e32 v4,0x3e0293ee,v4
cv 1,2,3
mvaa 72,152,16
v_mul_f32_e32 v5,0x3e0293ee,v5
s_waitcnt lgkmcnt(0)
s_barrier
mvaa 72,148,12
s_mov_b32 m0,s70
v_mul_f32_e32 v6,0x3e0293ee,v6
v_mul_f32_e32 v7,0x3e0293ee,v7
mvaa 72,144,8
buffer_load_dword v241,v247,s[8:11],s90 offen offset:256 sc1
mvaa 144,144,80
cv 2,4,5
cv 3,6,7
mvaa 144,148,84
v_mul_f32_e32 v92,0x3e0293ee,v92
v_mul_f32_e32 v93,0x3e0293ee,v93
mvaa 144,152,88
v_mul_f32_e32 v94,0x3e0293ee,v94
v_mul_f32_e32 v72,0x3e0293ee,v72
mvaa 148,156,88
v_mul_f32_e32 v95,0x3e0293ee,v95
v_mul_f32_e32 v73,0x3e0293ee,v73
mvaa 148,160,84
ds 249,8,43,0
mvaa 148,164,80
v_mul_f32_e32 v74,0x3e0293ee,v74
v_mul_f32_e32 v75,0x3e0293ee,v75
mvaa 156,164,92
cv 72,72,73
cv 73,74,75
mvaa 156,160,44
cv 74,92,93
cv 75,94,95
mvaa 156,156,40
v_mul_f32_e32 v144,0x3e0293ee,v144
v_mul_f32_e32 v145,0x3e0293ee,v145
mvaa 152,152,40
ds 249,8,43,1024
mvaa 152,148,44
v_mul_f32_e32 v146,0x3e0293ee,v146
v_mul_f32_e32 v147,0x3e0293ee,v147
mvaa 152,144,92
cv 144,144,145
v_mul_f32_e32 v148,0x3e0293ee,v148
mvaa 160,168,92
cv 145,146,147
v_mul_f32_e32 v149,0x3e0293ee,v149
mvaa 160,172,44
v_mul_f32_e32 v150,0x3e0293ee,v150
v_mul_f32_e32 v151,0x3e0293ee,v151
mvaa 160,176,40
ds 249,8,43,2048
mvaa 164,180,40
cv 146,148,149
cv 147,150,151
mvaa 164,184,44
v_mul_f32_e32 v156,0x3e0293ee,v156
v_mul_f32_e32 v157,0x3e0293ee,v157
mvaa 164,188,92
v_mul_f32_e32 v152,0x3e0293ee,v152
v_mul_f32_e32 v158,0x3e0293ee,v158
mvaa 172,188,80
v_mul_f32_e32 v159,0x3e0293ee,v159
v_mul_f32_e32 v153,0x3e0293ee,v153
mvaa 172,184,84
ds 249,8,43,3072
s_mov_b32 m0,s72
mvaa 172,180,88
L_vs15:
s_cbranch_execz L_vr0
v_mul_f32_e32 v154,0x3e0293ee,v154
v_mul_f32_e32 v155,0x3e0293ee,v155
mvaa 168,176,88
cv 152,152,153
cv 153,154,155
mvaa 168,172,84
ds_write_b128 v251,v[0:3] offset:0
mvaa 168,168,80
cv 154,156,157
cv 155,158,159
mvaa 176,168,8
ds 255,8,42,0
mvaa 176,172,12
v_mul_f32_e32 v160,0x3e0293ee,v160
v_mul_f32_e32 v161,0x3e0293ee,v161
mvaa 176,176,16
v_mul_f32_e32 v162,0x3e0293ee,v162
v_mul_f32_e32 v163,0x3e0293ee,v163
mvaa 180,180,16
cv 160,160,161
v_mul_f32_e32 v164,0x3e0293ee,v164
mvaa 180,184,12
v_mul_f32_e32 v165,0x3e0293ee,v165
cv 161,162,163
mvaa 180,188,8
ds 255,8,42,1024
ds_write_b128 v251,v[72:75] offset:4096
mvaa 188,188,20
v_mul_f32_e32 v166,0x3e0293ee,v166
v_mul_f32_e32 v167,0x3e0293ee,v167
mvaa 188,184,0
cv 162,164,165
cv 163,166,167
mvaa 188,180,4
v_mul_f32_e32 v172,0x3e0293ee,v172
v_mul_f32_e32 v173,0x3e0293ee,v173
mvaa 184,176,4
v_mul_f32_e32 v174,0x3e0293ee,v174
v_mul_f32_e32 v175,0x3e0293ee,v175
mvaa 184,172,0
ds 255,8,42,2048
mvaa 184,168,20
v_mul_f32_e32 v168,0x3e0293ee,v168
v_mul_f32_e32 v169,0x3e0293ee,v169
mvaa 192,192,20
ds_write_b128 v251,v[144:147] offset:8192
mvaa 192,196,0
v_mul_f32_e32 v170,0x3e0293ee,v170
v_mul_f32_e32 v171,0x3e0293ee,v171
mvaa 192,200,4
cv 168,168,169
cv 169,170,171
mvaa 196,204,4
ds 255,8,42,3072
mvaa 196,208,0
cv 170,172,173
cv 171,174,175
mvaa 196,212,20
v_mul_f32_e32 v176,0x3e0293ee,v176
v_mul_f32_e32 v177,0x3e0293ee,v177
mvaa 204,212,8
v_mul_f32_e32 v178,0x3e0293ee,v178
v_mul_f32_e32 v179,0x3e0293ee,v179
mvaa 204,208,12
ds_write_b128 v251,v[152:155] offset:12288
mvaa 204,204,16
buffer_load_dword v243,v247,s[8:11],s91 offen offset:256 sc1
mvaa 200,200,16
cv 176,176,177
cv 177,178,179
mvaa 200,196,12
v_mul_f32_e32 v180,0x3e0293ee,v180
v_mul_f32_e32 v181,0x3e0293ee,v181
mvaa 200,192,8
v_mul_f32_e32 v182,0x3e0293ee,v182
v_mul_f32_e32 v183,0x3e0293ee,v183
mvaa 208,192,80
cv 178,180,181
cv 179,182,183
mvaa 208,196,84
v_mul_f32_e32 v188,0x3e0293ee,v188
v_mul_f32_e32 v189,0x3e0293ee,v189
mvaa 208,200,88
ds_write_b128 v251,v[160:163] offset:13312
mvaa 212,204,88
v_mul_f32_e32 v190,0x3e0293ee,v190
v_mul_f32_e32 v191,0x3e0293ee,v191
mvaa 212,208,84
v_mul_f32_e32 v184,0x3e0293ee,v184
v_mul_f32_e32 v185,0x3e0293ee,v185
mvaa 212,212,80
v_mul_f32_e32 v186,0x3e0293ee,v186
v_mul_f32_e32 v187,0x3e0293ee,v187
mvaa 220,212,92
cv 184,184,185
cv 185,186,187
mvaa 220,208,44
cv 186,188,189
cv 187,190,191
mvaa 220,204,40
v_mul_f32_e32 v192,0x3e0293ee,v192
v_mul_f32_e32 v193,0x3e0293ee,v193
mvaa 216,200,40
ds_write_b128 v251,v[168:171] offset:9216
mvaa 216,196,44
v_mul_f32_e32 v194,0x3e0293ee,v194
v_mul_f32_e32 v195,0x3e0293ee,v195
mvaa 216,192,92
cv 192,192,193
cv 193,194,195
maaa 128,216,92
v_mul_f32_e32 v196,0x3e0293ee,v196
v_mul_f32_e32 v197,0x3e0293ee,v197
maaa 128,220,44
v_mul_f32_e32 v198,0x3e0293ee,v198
v_mul_f32_e32 v199,0x3e0293ee,v199
maaa 128,224,40
cv 194,196,197
cv 195,198,199
maaa 112,228,40
v_mul_f32_e32 v204,0x3e0293ee,v204
v_mul_f32_e32 v205,0x3e0293ee,v205
maaa 112,232,44
ds_write_b128 v251,v[176:179] offset:5120
maaa 112,236,92
v_mul_f32_e32 v206,0x3e0293ee,v206
v_mul_f32_e32 v207,0x3e0293ee,v207
maaa 116,236,80
v_mul_f32_e32 v200,0x3e0293ee,v200
v_mul_f32_e32 v201,0x3e0293ee,v201
maaa 116,232,84
v_mul_f32_e32 v202,0x3e0293ee,v202
v_mul_f32_e32 v203,0x3e0293ee,v203
maaa 116,228,88
cv 200,200,201
cv 201,202,203
maaa 132,224,88
cv 202,204,205
cv 203,206,207
maaa 132,220,84
v_mul_f32_e32 v208,0x3e0293ee,v208
v_mul_f32_e32 v209,0x3e0293ee,v209
maaa 132,216,80
ds_write_b128 v251,v[184:187] offset:1024
maaa 136,216,8
v_mul_f32_e32 v210,0x3e0293ee,v210
v_mul_f32_e32 v211,0x3e0293ee,v211
maaa 136,220,12
cv 208,208,209
cv 209,210,211
maaa 136,224,16
v_mul_f32_e32 v212,0x3e0293ee,v212
v_mul_f32_e32 v213,0x3e0293ee,v213
maaa 120,228,16
s_waitcnt vmcnt(11)
s_barrier
maaa 120,232,12
v_mul_f32_e32 v214,0x3e0293ee,v214
v_mul_f32_e32 v215,0x3e0293ee,v215
maaa 120,236,8
cv 210,212,213
cv 211,214,215
maaa 124,236,20
ds_write_b128 v251,v[192:195] offset:2048
maaa 124,232,0
v_mul_f32_e32 v220,0x3e0293ee,v220
v_mul_f32_e32 v221,0x3e0293ee,v221
maaa 124,228,4
v_mul_f32_e32 v216,0x3e0293ee,v216
v_mul_f32_e32 v217,0x3e0293ee,v217
maaa 140,224,4
v_mul_f32_e32 v222,0x3e0293ee,v222
v_mul_f32_e32 v223,0x3e0293ee,v223
maaa 140,220,0
v_mul_f32_e32 v218,0x3e0293ee,v218
v_mul_f32_e32 v219,0x3e0293ee,v219
maaa 140,216,20
cv 216,216,217
cv 217,218,219
mvaa 88,92,64
cv 218,220,221
cv 219,222,223
mvaa 88,44,68
ds_write_b128 v251,v[200:203] offset:6144
mvaa 88,40,56
v_accvgpr_read_b32 v0,a128
v_accvgpr_read_b32 v1,a129
mvaa 108,40,60
v_accvgpr_read_b32 v2,a130
v_accvgpr_read_b32 v3,a131
mvaa 108,44,48
v_mul_f32_e32 v0,0x3e0293ee,v0
v_mul_f32_e32 v1,0x3e0293ee,v1
cv 88,88,89
cv 89,90,91
mvaa 108,92,52
buffer_store_dwordx2 v[88:89],v253,s[8:11],s99 offen offset:1024 sc0 sc1
mvaa 36,76,92
v_mul_f32_e32 v2,0x3e0293ee,v2
v_mul_f32_e32 v3,0x3e0293ee,v3
mvaa 36,72,44
cv 0,0,1
cv 1,2,3
mvaa 36,100,40
ds_write_b128 v251,v[208:211] offset:10240
mvaa 32,96,40
cv 108,108,109
cv 109,110,111
mvaa 32,24,44
buffer_store_dwordx2 v[108:109],v253,s[8:11],s99 offen offset:1536 sc0 sc1
mvaa 32,28,92
v_accvgpr_read_b32 v4,a112
v_accvgpr_read_b32 v5,a113
mvaa 64,32,92
v_accvgpr_read_b32 v6,a114
v_mul_f32_e32 v4,0x3e0293ee,v4
mvaa 64,108,44
v_mul_f32_e32 v5,0x3e0293ee,v5
v_mul_f32_e32 v6,0x3e0293ee,v6
mvaa 64,36,40
cv 32,32,33
cv 33,34,35
cv 34,36,37
cv 35,38,39
mvaa 68,104,40
buffer_store_dwordx4 v[32:35],v244,s[8:11],s99 offen sc0 sc1
ds_write_b128 v251,v[216:219] offset:14336
mvaa 68,240,44
v_accvgpr_read_b32 v7,a115
v_mul_f32_e32 v7,0x3e0293ee,v7
mvaa 68,244,92
cv 2,4,5
cv 3,6,7
mvaa 60,244,80
ra 192,224,0
v_accvgpr_read_b32 v92,a116
mvaa 60,240,84
ra 196,224,1024
v_accvgpr_read_b32 v93,a117
mvaa 60,104,88
ra 200,224,2048
v_mul_f32_e32 v92,0x3e0293ee,v92
mvaa 56,36,88
ra 204,224,3072
v_mul_f32_e32 v93,0x3e0293ee,v93
mvaa 56,108,84
ds_write_b128 v251,v[0:3] offset:15360
mvaa 56,32,80
v_accvgpr_read_b32 v94,a118
v_accvgpr_read_b32 v95,a119
mvaa 24,28,80
v_accvgpr_read_b32 v72,a132
v_accvgpr_read_b32 v73,a133
mvaa 24,24,84
v_accvgpr_read_b32 v74,a134
v_mul_f32_e32 v94,0x3e0293ee,v94
mvaa 24,96,88
v_mul_f32_e32 v95,0x3e0293ee,v95
v_mul_f32_e32 v72,0x3e0293ee,v72
mvaa 28,100,88
v_mul_f32_e32 v73,0x3e0293ee,v73
v_mul_f32_e32 v74,0x3e0293ee,v74
v_accvgpr_read_b32 v75,a135
mvaa 28,72,84
v_mul_f32_e32 v75,0x3e0293ee,v75
cv 72,72,73
cv 73,74,75
cv 74,92,93
mvaa 28,76,80
ra 208,224,4096
cv 75,94,95
mvaa 20,76,8
ds_write_b128 v251,v[72:75] offset:11264
mvaa 20,72,12
v_accvgpr_read_b32 v144,a136
v_accvgpr_read_b32 v145,a137
mvaa 20,100,16
s_mov_b64 exec,3
global_load_dword v239,v238,s[96:97]
s_mov_b64 exec,-1
ra 212,224,5120
ra 248,224,14336
ra 252,224,15360
mvaa 16,96,16
v_accvgpr_read_b32 v146,a138
v_mul_f32_e32 v144,0x3e0293ee,v144
v_mul_f32_e32 v145,0x3e0293ee,v145
v_mul_f32_e32 v146,0x3e0293ee,v146
v_accvgpr_read_b32 v147,a139
v_mul_f32_e32 v147,0x3e0293ee,v147
cv 144,144,145
mvaa 16,24,12
ra 236,224,11264
v_accvgpr_read_b32 v148,a120
v_accvgpr_read_b32 v149,a121
v_accvgpr_read_b32 v150,a122
cv 145,146,147
mvaa 16,28,8
v_mul_f32_e32 v148,0x3e0293ee,v148
v_mul_f32_e32 v149,0x3e0293ee,v149
v_mul_f32_e32 v150,0x3e0293ee,v150
v_accvgpr_read_b32 v151,a123
v_mul_f32_e32 v151,0x3e0293ee,v151
cv 146,148,149
mvaa 48,32,8
ra 228,224,9216
cv 147,150,151
mvaa 48,108,12
ds_write_b128 v251,v[144:147] offset:7168
mvaa 48,36,16
v_accvgpr_read_b32 v156,a124
v_accvgpr_read_b32 v157,a125
mvaa 52,104,16
ra 232,224,10240
ra 216,224,6144
ra 220,224,7168
ra 224,224,8192
v_accvgpr_read_b32 v158,a126
v_accvgpr_read_b32 v159,a127
mvaa 52,240,12
v_accvgpr_read_b32 v152,a140
v_mul_f32_e32 v156,0x3e0293ee,v156
v_mul_f32_e32 v157,0x3e0293ee,v157
v_mul_f32_e32 v158,0x3e0293ee,v158
v_mul_f32_e32 v159,0x3e0293ee,v159
mvaa 52,244,8
v_mul_f32_e32 v152,0x3e0293ee,v152
v_accvgpr_read_b32 v153,a141
v_mul_f32_e32 v153,0x3e0293ee,v153
v_accvgpr_read_b32 v154,a142
v_accvgpr_read_b32 v155,a143
mvaa 44,244,20
v_mul_f32_e32 v154,0x3e0293ee,v154
v_mul_f32_e32 v155,0x3e0293ee,v155
cv 152,152,153
cv 153,154,155
cv 154,156,157
mvaa 44,240,0
ra 244,224,13312
cv 155,158,159
mvaa 44,104,4
ds_write_b128 v251,v[152:155] offset:3072
mvaa 40,36,4
cv 64,64,65
cv 56,56,57
mvaa 40,108,0
cv 57,58,59
cv 58,60,61
mvaa 40,32,20
ra 240,224,12288
cv 59,62,63
mvaa 8,28,20
cv 16,16,17
cv 17,18,19
mvaa 8,24,0
cv 48,48,49
cv 194,44,45
mvaa 8,96,4
cv 195,46,47
cv 192,40,41
mvaa 12,100,4
cv 193,42,43
cv 49,50,51
mvaa 12,72,0
s_cmp_eq_u32 s19,0
s_cselect_b32 s76,32768,0
s_cselect_b32 s77,49152,s86
s_cselect_b32 s78,65536,s46
s_cselect_b32 s79,81920,s86
mvaa 12,76,20
s_add_u32 s86,s95,98304
cv 8,8,9
v_add_u32_e32 v236,s86,v244
mvaa 76,20,64
s_add_u32 s46,s46,16384
v_xor_b32_e32 v237,4096,v236
s_waitcnt lgkmcnt(0)
s_barrier
s_mov_b32 m0,s68
mvaa 76,0,68
ds 249,8,76,0
ra 128,237,32768
ra 132,237,33792
mvaa 76,4,56
s_add_u32 s95,s47,32768
ra 136,237,34816
cv 9,10,11
mvaa 96,4,60
ra 140,237,35840
ra 144,237,49152
mvaa 96,0,48
cv 10,12,13
cv 11,14,15
mvaa 96,20,52
ra 148,237,50176
cv 50,52,53
mvaa 100,8,52
ds 249,8,76,1024
ra 152,237,51200
ra 156,237,52224
mvaa 100,12,48
ra 176,236,16384
cv 51,54,55
mvaa 100,16,60
ra 180,236,17408
cv 18,20,21
mvaa 80,16,56
ra 184,236,18432
cv 19,22,23
mvaa 80,12,68
ra 188,236,19456
cv 24,24,25
mvaa 80,8,64
ds 249,8,76,2048
ra 160,236,0
mvaa 84,80,64
ra 164,236,1024
cv 25,26,27
mvaa 84,84,68
ra 168,236,2048
ra 172,236,3072
mvaa 84,88,56
cv 26,28,29
cv 27,30,31
mvaa 104,88,60
cv 65,66,67
cv 66,68,69
mvaa 104,84,48
ds 249,8,76,3072
mvaa 104,80,52
s_mov_b32 m0,s69
cv 67,70,71
v_cndmask_b32_e64 v252,0,v252,s[40:41]
cvvv 164,8,192,112
cv 70,76,77
cv 71,78,79
cvvv 168,8,48,116
ds 249,8,77,0
cvvv 172,16,48,112
cv 94,96,97
v_mov_b32_e32 v76,v88
cvvv 176,16,56,116
cv 95,98,99
s_waitcnt vmcnt(9) lgkmcnt(0)
s_barrier
cvvv 180,24,56,112
ds 249,8,77,1024
cvvv 184,24,64,116
ra 0,224,32768
cv 96,100,101
cvvv 188,32,64,112
ra 4,224,33792
cv 97,102,103
cvaa 0,192,128,232
buffer_load_dword v17,v247,s[8:11],s35 offen offset:64 sc1
mvaa 0,200,132
ra 8,224,34816
cv 72,80,81
mvaa 0,208,136
ra 12,224,35840
cv 73,82,83
mvaa 0,216,140
ds 249,8,77,2048
cvaa 4,220,140,232
ra 16,224,36864
cv 74,84,85
mvaa 4,212,136
ra 20,224,37888
v_max3_f32 v172,v168,v169,v172
mvaa 4,204,132
ds 249,8,77,3072
mvaa 4,196,128
s_mov_b32 m0,s74
ra 24,224,38912
v_max_f32_e32 v172,v172,v173
cvaa 32,224,128,232
ra 28,224,39936
v_max3_f32 v174,v170,v171,v174
mvaa 32,232,132
buffer_load_dword v19,v247,s[8:11],s35 offen offset:192 sc1
mvaa 32,240,136
ra 32,224,40960
v_max_f32_e32 v174,v174,v175
mvaa 32,248,140
ra 36,224,41984
ra 40,224,43008
cvaa 36,252,140,232
buffer_load_dword v18,v247,s[8:11],s35 offen offset:128 sc1
ra 44,224,44032
mvaa 36,244,136
ra 48,224,45056
v_max_f32_e32 v148,v172,v174
mvaa 36,236,132
ra 56,224,47104
ra 60,224,48128
mvaa 36,228,128
buffer_load_dword v16,v247,s[8:11],s35 offen offset:0 sc1
ra 52,224,46080
cvaa 8,192,144,232
v_mov_b32_e32 v149,v148
cv 75,86,87
mvaa 8,200,148
v_permlane16_swap_b32_e32 v148,v149
mvaa 8,208,152
ds 255,8,43,0
mvaa 8,216,156
v_max_f32_e32 v148,v148,v149
v_mov_b32_e32 v149,v148
cvaa 12,220,156,232
cv 98,104,105
v_max3_f32 v180,v176,v177,v180
mvaa 12,212,152
ds 255,8,43,1024
mvaa 12,204,148
v_permlane32_swap_b32_e32 v148,v149
mvaa 12,196,144
v_max_f32_e32 v148,v148,v149
s_waitcnt lgkmcnt(0)
s_barrier
cvaa 40,224,144,232
ds 255,8,43,2048
mvaa 40,232,148
ra 64,225,0
v_mul_f32_e32 v148,0xbe8293ee,v148
mvaa 40,240,152
L_vs16:
s_cbranch_execz L_vr0
ra 68,225,1024
v_fmamk_f32 v172,v172,0x3e8293ee,v148
mvaa 40,248,156
ds 255,8,43,3072
cvaa 44,252,156,232
s_mov_b32 m0,s70
ra 72,225,2048
v_fmamk_f32 v174,v174,0x3e8293ee,v148
mvaa 44,244,152
ra 76,225,3072
v_max3_f32 v182,v178,v179,v182
mvaa 44,236,148
ds 249,8,78,0
mvaa 44,228,144
ra 80,225,4096
v_max_f32_e32 v180,v180,v181
cvaa 24,192,176,128
ra 84,225,5120
v_max_f32_e32 v182,v182,v183
mvaa 24,200,180
ds 249,8,78,1024
mvaa 24,208,184
ra 88,225,6144
v_max_f32_e32 v152,v180,v182
mvaa 24,216,188
ra 92,225,7168
v_mov_b32_e32 v153,v152
cvaa 28,220,188,132
ds 249,8,78,2048
mvaa 28,212,184
ra 96,225,8192
v_max3_f32 v188,v184,v185,v188
mvaa 28,204,180
ra 100,225,9216
ra 104,225,10240
mvaa 28,196,176
ds 249,8,78,3072
ra 108,225,11264
cvaa 56,224,176,136
s_mov_b32 m0,s71
ra 112,225,12288
v_max3_f32 v190,v186,v187,v190
mvaa 56,232,180
ra 116,225,13312
ra 120,225,14336
mvaa 56,240,184
ds 249,8,79,0
ra 124,225,15360
mvaa 56,248,188
ex 172
cvaa 60,252,188,140
ex 174
mvaa 60,244,184
ds 249,8,79,1024
mvaa 60,236,180
v_permlane16_swap_b32_e32 v152,v153
mvaa 60,228,176
cv 172,172,172
cv 174,174,174
cvaa 80,0,160,128
ds 249,8,79,2048
mvaa 80,8,164
v_and_b32_e32 v168,v226,v172
v_max_f32_e32 v152,v152,v153
mvaa 80,16,168
v_and_b32_e32 v169,v227,v174
v_and_b32_e32 v170,v250,v172
v_and_b32_e32 v171,v252,v174
mvaa 80,24,172
ds 249,8,79,3072
v_mov_b32_e32 v153,v152
cvaa 84,28,172,132
v_max_f32_e32 v188,v188,v189
v_permlane32_swap_b32_e32 v152,v153
v_max_f32_e32 v152,v152,v153
mvaa 84,20,168
v_mul_f32_e32 v152,0xbe8293ee,v152
v_fmamk_f32 v180,v180,0x3e8293ee,v152
mvaa 84,12,164
v_fmamk_f32 v182,v182,0x3e8293ee,v152
v_max_f32_e32 v190,v190,v191
mvaa 84,4,160
ex 180
cvaa 112,32,160,136
ex 182
v_max_f32_e32 v156,v188,v190
mvaa 112,40,164
v_mov_b32_e32 v157,v156
cv 180,180,180
mvaa 112,48,168
v_permlane16_swap_b32_e32 v156,v157
mvaa 112,56,172
cv 182,182,182
v_and_b32_e32 v176,v226,v180
v_max_f32_e32 v156,v156,v157
s_waitcnt vmcnt(15) lgkmcnt(0)
s_barrier
s_mov_b32 m0,s72
cvaa 116,60,172,140
ds 255,8,76,0
v_and_b32_e32 v177,v227,v182
mvaa 116,52,168
v_and_b32_e32 v178,v250,v180
v_and_b32_e32 v179,v252,v182
v_mov_b32_e32 v157,v156
mvaa 116,44,164
cv 99,106,107
v_permlane32_swap_b32_e32 v156,v157
v_max_f32_e32 v156,v156,v157
mvaa 116,36,160
ds 255,8,76,1024
v_mul_f32_e32 v156,0xbe8293ee,v156
zvvv 148,228,168
v_fmamk_f32 v188,v188,0x3e8293ee,v156
ex 188
zvvv 200,70,168
v_fmamk_f32 v190,v190,0x3e8293ee,v156
ex 190
zvvv 204,94,168
ds 255,8,76,2048
zvvv 152,228,176
cv 188,188,188
cv 190,190,190
v_and_b32_e32 v184,v226,v188
zvvv 208,72,176
v_and_b32_e32 v185,v227,v190
v_and_b32_e32 v186,v250,v188
v_and_b32_e32 v187,v252,v190
zvvv 212,96,176
ds 255,8,76,3072
s_mov_b32 m0,s73
v_mov_b32_e32 v77,v89
zvvv 156,228,184
v_mov_b32_e32 v100,v108
v_mov_b32_e32 v101,v109
zvvv 216,74,184
ex 0
zvvv 220,98,184
ds 255,8,77,0
cvaa 64,0,128,232
ex 1
mvaa 64,8,132
ex 2
mvaa 64,16,136
ds 255,8,77,1024
mvaa 64,24,140
ds 255,8,77,2048
)"
R"(v_mov_b32_e32 v170,v70
v_mov_b32_e32 v171,v71
cvaa 68,28,140,232
ex 3
mvaa 68,20,136
ex 4
mvaa 68,12,132
ds 255,8,77,3072
mvaa 68,4,128
ex 5
v_and_b32_e32 v248,2863311530,v248
cvaa 96,32,128,232
ex 6
mvaa 96,40,132
v_sub_f32_e32 v17,1.0,v17
v_rcp_f32_e32 v148,v148
mvaa 96,48,136
v_mul_f32_e32 v148,v148,v17
s_waitcnt vmcnt(21)
v_sub_f32_e32 v18,1.0,v18
v_sub_f32_e32 v19,1.0,v19
mvaa 96,56,140
v_mul_f32_e32 v200,v148,v200
v_mul_f32_e32 v201,v148,v201
v_mul_f32_e32 v202,v148,v202
cvaa 100,60,140,232
v_mul_f32_e32 v203,v148,v203
cv 200,200,201
mvaa 100,52,136
v_mul_f32_e32 v204,v148,v204
v_mul_f32_e32 v205,v148,v205
cv 201,202,203
mvaa 100,44,132
v_mul_f32_e32 v206,v148,v206
v_mul_f32_e32 v207,v148,v207
cv 202,204,205
mvaa 100,36,128
buffer_load_dwordx4 v[176:179],v244,s[8:11],s98 offen sc1
cv 203,206,207
cvaa 72,0,144,232
v_rcp_f32_e32 v152,v152
mvaa 72,8,148
v_mul_f32_e32 v152,v152,v18
v_mul_f32_e32 v208,v152,v208
v_mul_f32_e32 v209,v152,v209
mvaa 72,16,152
buffer_load_dwordx2 v[168:169],v253,s[8:11],s98 offen offset:1024 sc1
v_mul_f32_e32 v210,v152,v210
mvaa 72,24,156
v_mul_f32_e32 v211,v152,v211
cv 208,208,209
cv 209,210,211
cvaa 76,28,156,232
v_mul_f32_e32 v212,v152,v212
v_mul_f32_e32 v213,v152,v213
mvaa 76,20,152
buffer_load_dwordx2 v[172:173],v253,s[8:11],s98 offen offset:1536 sc1
v_mul_f32_e32 v214,v152,v214
mvaa 76,12,148
v_mul_f32_e32 v215,v152,v215
v_rcp_f32_e32 v156,v156
mvaa 76,4,144
cv 210,212,213
cv 211,214,215
v_mul_f32_e32 v156,v156,v19
cvaa 104,32,144,232
buffer_store_dwordx4 v[200:203],v244,s[8:11],s34 offen sc0 sc1
mvaa 104,40,148
v_mul_f32_e32 v216,v156,v216
v_mul_f32_e32 v217,v156,v217
v_mul_f32_e32 v218,v156,v218
mvaa 104,48,152
v_mul_f32_e32 v219,v156,v219
cv 216,216,217
cv 217,218,219
mvaa 104,56,156
buffer_store_dwordx4 v[208:211],v244,s[8:11],s50 offen sc0 sc1
v_mul_f32_e32 v220,v156,v220
cvaa 108,60,156,232
v_mul_f32_e32 v221,v156,v221
v_mul_f32_e32 v222,v156,v222
mvaa 108,52,152
v_mul_f32_e32 v223,v156,v223
cv 218,220,221
cv 219,222,223
mvaa 108,44,148
buffer_store_dwordx4 v[216:219],v244,s[8:11],s51 offen sc0 sc1
v_mov_b32_e32 v160,v120
mvaa 108,36,144
v_mov_b32_e32 v161,v121
ex 7
cvaa 88,0,176,232
ex 32
mvaa 88,8,180
v_mov_b32_e32 v162,v122
ex 33
mvaa 88,16,184
v_mov_b32_e32 v175,v95
ex 34
mvaa 88,24,188
s_cmp_ge_i32 s47,s46
s_cselect_b32 s76,s95,s46
s_cselect_b32 s77,s95,268435456
v_mov_b32_e32 v174,v94
ex 35
cvaa 92,28,188,232
s_add_u32 s77,s77,16384
ex 36
mvaa 92,20,184
v_mov_b32_e32 v163,v123
ex 37
mvaa 92,12,180
cv 0,0,1
ex 38
mvaa 92,4,176
cv 1,2,3
ex 39
cvaa 120,32,176,232
buffer_load_dword v247,v246,s[24:27],s49 offen
mvaa 120,40,180
s_add_u32 s49,s49,32768
cv 2,4,5
ex 8
mvaa 120,48,184
cv 3,6,7
ex 9
mvaa 120,56,188
buffer_load_dword v247,v245,s[36:39],s85 offen
cv 4,32,33
cvaa 124,60,188,232
s_add_u32 s85,s85,131072
ex 10
mvaa 124,52,184
cv 5,34,35
ex 11
mvaa 124,44,180
buffer_load_dword v247,v246,s[24:27],s49 offen
cv 6,36,37
mvaa 124,36,176
s_cmp_lg_u32 s19,0
s_cbranch_scc1 L_j0
s_nop 1
s_lshl_b32 s86,s12,12
s_add_u32 s86,s86,131072
v_add_u32_e32 v236,s86,v244
ra 160,236,0
ra 164,236,1024
ra 168,236,2048
ra 172,236,3072
ra 176,236,16384
ra 180,236,17408
ra 184,236,18432
ra 188,236,19456
v_add_u32_e32 v224,s94,v224
s_waitcnt lgkmcnt(0)
L_j0:
cv 7,38,39
ex 12
zaav 0,64,0
s_add_u32 s49,s49,32768
ex 13
maav 0,96,4
ex 14
v_or_b32_e32 v248,1145324612,v248
zaav 4,68,0
ex 15
v_mov_b64_e32 v[144:145],0
maav 4,100,4
cv 8,8,9
ex 40
zaav 8,72,0
ex 41
maav 8,104,4
cv 9,10,11
ex 42
zaav 12,76,0
cv 10,12,13
ex 43
maav 12,108,4
cv 11,14,15
ex 44
v_smfmac_f32_16x16x64_bf16 v[144:147],v[228:231],v[0:7],v248
ex 45
zaav 16,80,0
cv 12,40,41
ex 46
maav 16,112,4
s_waitcnt vmcnt(8)
v_max3_u32 v180,v176,v177,v178
cv 13,42,43
zaav 20,84,0
s_waitcnt vmcnt(7)
v_max3_u32 v180,v180,v179,v168
s_waitcnt vmcnt(6)
v_max3_u32 v180,v180,v169,v172
v_max_u32_e32 v180,v180,v173
v_cmp_eq_u32_e64 s[40:41],-1,v180
maav 20,116,4
s_cmp_lg_u64 s[40:41],0
s_cbranch_scc1 L_bx
L_bxr:
cv 14,44,45
ex 47
mvvv 160,176,192
zaav 24,88,0
cv 15,46,47
v_sub_f32_e32 v16,1.0,v16
maav 24,120,4
ex 24
zaav 28,92,0
ex 25
maav 28,124,4
v_max3_f32 v164,v160,v161,v164
v_max_f32_e32 v164,v164,v165
v_max3_f32 v166,v162,v163,v166
v_max_f32_e32 v166,v166,v167
v_max_f32_e32 v180,v164,v166
v_mov_b32_e32 v181,v180
zaav 32,64,8
ex 26
v_permlane16_swap_b32_e32 v180,v181
v_max_f32_e32 v180,v180,v181
v_mov_b32_e32 v181,v180
maav 32,96,12
ex 27
v_permlane32_swap_b32_e32 v180,v181
v_max_f32_e32 v180,v180,v181
v_mul_f32_e32 v180,0xbe8293ee,v180
v_fmamk_f32 v164,v164,0x3e8293ee,v180
zaav 36,68,8
ex 164
v_fmamk_f32 v166,v166,0x3e8293ee,v180
ex 166
cv 164,164,164
cv 166,166,166
maav 36,100,12
v_and_b32_e32 v160,v226,v164
v_and_b32_e32 v161,v227,v166
v_and_b32_e32 v162,v250,v164
v_and_b32_e32 v163,v252,v166
zaav 40,72,8
v_mov_b64_e32 v[148:149],0
zvvv 180,228,160
maav 40,104,12
ra 192,224,0
zvvv 184,168,160
zaav 44,76,8
ra 196,224,1024
zvvv 188,172,160
maav 44,108,12
ra 200,224,2048
v_rcp_f32_e32 v180,v180
cv 24,24,25
v_mul_f32_e32 v180,v180,v16
v_mul_f32_e32 v184,v180,v184
v_mul_f32_e32 v185,v180,v185
v_smfmac_f32_16x16x64_bf16 v[148:151],v[228:231],v[8:15],v248
v_mul_f32_e32 v186,v180,v186
v_mul_f32_e32 v187,v180,v187
v_mul_f32_e32 v188,v180,v188
v_mul_f32_e32 v189,v180,v189
v_mul_f32_e32 v190,v180,v190
v_mul_f32_e32 v191,v180,v191
zaav 48,80,8
cv 184,184,185
cv 185,186,187
cv 186,188,189
cv 187,190,191
cv 25,26,27
ex 28
maav 48,112,12
buffer_store_dwordx4 v[184:187],v244,s[8:11],s33 offen sc0 sc1
ex 29
ex 30
cv 26,28,29
zaav 52,84,8
ex 31
ex 56
cv 27,30,31
ex 57
maav 52,116,12
ex 58
ex 59
ex 60
zaav 56,88,8
ex 61
maav 56,120,12
cv 28,56,57
ex 62
zaav 60,92,8
ra 204,224,3072
ra 208,224,4096
ex 63
maav 60,124,12
ra 212,224,5120
cv 29,58,59
cv 30,60,61
cv 31,62,63
zvav 192,64,24
ra 216,224,6144
ra 220,224,7168
ex 64
mvav 192,96,28
ra 224,224,8192
v_mov_b32_e32 v251,-1
ex 65
zvav 196,68,24
ra 228,224,9216
ra 232,224,10240
ex 66
mvav 196,100,28
ra 236,224,11264
v_mov_b64_e32 v[152:153],0
ex 67
zvav 200,72,24
ra 240,224,12288
ra 244,224,13312
v_mov_b64_e32 v[156:157],0
cv 64,64,65
mvav 200,104,28
ra 248,224,14336
ra 252,224,15360
ex 68
zvav 204,76,24
ex 69
ex 70
mvav 204,108,28
cv 65,66,67
ex 71
cv 66,68,69
v_smfmac_f32_16x16x64_bf16 v[156:159],v[228:231],v[24:31],v248
ex 96
ex 97
zvav 208,80,24
ex 98
ex 99
mvav 208,112,28
cv 67,70,71
cv 68,96,97
ex 100
zvav 212,84,24
ex 101
ex 102
mvav 212,116,28
cv 69,98,99
ex 103
cv 70,100,101
zvav 216,88,24
ex 72
ex 73
mvav 216,120,28
ex 74
ex 75
zvav 220,92,24
ex 76
ex 77
mvav 220,124,28
v_mov_b32_e32 v250,-1
v_mov_b32_e32 v252,-1
v_mov_b32_e32 v253,-1
cv 71,102,103
s_mov_b64 exec,0
s_bitcmp1_b32 170,s20
s_cbranch_scc1 L_vbO
s_cmp_eq_u32 s12,0
s_cbranch_scc1 L_vs18
s_cmp_eq_u32 s12,1
s_cbranch_scc1 L_vs19
s_cmp_eq_u32 s12,2
s_cbranch_scc1 L_vs20
s_branch L_vs21
L_vbO:
s_cmp_eq_u32 s12,0
s_cbranch_scc1 L_vs25
s_cmp_eq_u32 s12,1
s_cbranch_scc1 L_vs26
s_cmp_eq_u32 s12,2
s_cbranch_scc1 L_vs27
s_branch L_vs28
L_vr1:
s_mov_b64 exec,-1
s_waitcnt lgkmcnt(0)
s_barrier
cvaa 0,192,128,232
mvaa 0,200,132
s_add_u32 s81,s81,1
ex 78
mvaa 0,208,136
s_add_u32 s44,s44,1024
ex 79
mvaa 0,216,140
cv 72,72,73
cv 73,74,75
cvaa 4,220,140,232
s_mov_b32 m0,s68
ex 104
mvaa 4,212,136
ds 249,8,76,0
mvaa 4,204,132
ex 105
mvaa 4,196,128
ex 106
cvaa 32,224,128,232
ra 64,225,32768
cv 74,76,77
mvaa 32,232,132
ex 107
mvaa 32,240,136
ra 68,225,33792
cv 75,78,79
mvaa 32,248,140
ex 108
cvaa 36,252,140,232
ds 249,8,76,1024
mvaa 36,244,136
ex 109
mvaa 36,236,132
ra 72,225,34816
cv 76,104,105
mvaa 36,228,128
ex 110
cvaa 8,192,144,232
ra 76,225,35840
cv 77,106,107
mvaa 8,200,148
ex 111
mvaa 8,208,152
ex 80
mvaa 8,216,156
ds 249,8,76,2048
cvaa 12,220,156,232
ra 80,225,36864
cv 78,108,109
mvaa 12,212,152
ex 81
mvaa 12,204,148
ra 84,225,37888
cv 79,110,111
mvaa 12,196,144
ex 82
cvaa 40,224,144,232
ex 83
mvaa 40,232,148
ra 88,225,38912
cv 80,80,81
mvaa 40,240,152
ds 249,8,76,3072
mvaa 40,248,156
ex 84
cvaa 44,252,156,232
ra 92,225,39936
cv 81,82,83
mvaa 44,244,152
ex 85
mvaa 44,236,148
ex 86
mvaa 44,228,144
ra 96,225,40960
cv 82,84,85
cvaa 16,192,160,232
s_mov_b32 m0,s69
ex 87
mvaa 16,200,164
ra 100,225,41984
cv 83,86,87
mvaa 16,208,168
ds 249,8,77,0
mvaa 16,216,172
ex 112
cvaa 20,220,172,232
ex 113
mvaa 20,212,168
ra 104,225,43008
cv 84,112,113
mvaa 20,204,164
ex 114
mvaa 20,196,160
ra 108,225,44032
cvaa 48,224,160,232
ex 115
mvaa 48,232,164
ds 249,8,77,1024
mvaa 48,240,168
ex 116
mvaa 48,248,172
ra 112,225,45056
cv 85,114,115
cvaa 52,252,172,232
ex 117
mvaa 52,244,168
ra 116,225,46080
cv 86,116,117
mvaa 52,236,164
L_vs17:
s_cbranch_execz L_vr1
ex 118
mvaa 52,228,160
ex 119
cvaa 24,192,176,232
ds 249,8,77,2048
mvaa 24,200,180
ra 120,225,47104
cv 87,118,119
mvaa 24,208,184
ex 88
mvaa 24,216,188
ra 124,225,48128
cvaa 28,220,188,232
mvaa 28,212,184
ex 89
mvaa 28,204,180
ex 90
mvaa 28,196,176
ds 249,8,77,3072
cvaa 56,224,176,232
ex 91
mvaa 56,232,180
ex 92
mvaa 56,240,184
ex 93
mvaa 56,248,188
ex 94
cvaa 60,252,188,232
mvaa 60,244,184
ex 95
mvaa 60,236,180
cv 88,88,89
cv 89,90,91
mvaa 60,228,176
s_waitcnt vmcnt(8) lgkmcnt(0)
s_barrier
maav 0,64,64
ex 120
maav 0,96,68
s_mov_b32 m0,s74
ex 121
maav 4,68,64
ds 255,8,78,0
maav 4,100,68
ex 122
maav 8,72,64
ex 123
maav 8,104,68
cv 90,92,93
cv 91,94,95
maav 12,76,64
ex 124
maav 12,108,68
ex 125
v_smfmac_f32_16x16x64_bf16 v[144:147],v[228:231],v[64:71],v248
cv 92,120,121
maav 16,80,64
ex 126
maav 16,112,68
buffer_load_dword v247,v245,s[36:39],s85 offen
maav 20,84,64
ex 127
maav 20,116,68
cv 93,122,123
cv 94,124,125
maav 24,88,64
cv 95,126,127
maav 24,120,68
ra 192,224,32768
maav 28,92,64
ds 255,8,78,1024
maav 28,124,68
s_lshl_b32 s86,s19,3
s_lshr_b32 s87,s18,2
s_add_u32 s86,s86,s87
maav 32,64,72
ra 196,224,33792
maav 32,96,76
s_add_u32 s85,s85,131072
maav 36,68,72
ra 200,224,34816
maav 36,100,76
s_lshl_b32 s86,s86,7
s_add_u32 s86,s86,83898368
v_mov_b32_e32 v238,1
maav 40,72,72
ds 255,8,78,2048
maav 40,104,76
s_cmp_eq_u32 s12,0
s_cselect_b64 exec,1,0
buffer_atomic_add v238,off,s[8:11],s86 sc1
s_mov_b64 exec,-1
maav 44,76,72
ra 204,224,35840
maav 44,108,76
v_smfmac_f32_16x16x64_bf16 v[148:151],v[228:231],v[72:79],v248
ra 208,224,36864
maav 48,80,72
maav 48,112,76
maav 52,84,72
ds 255,8,78,3072
maav 52,116,76
ra 212,224,37888
maav 56,88,72
ex 0
maav 56,120,76
ra 216,224,38912
maav 60,92,72
ex 1
maav 60,124,76
s_mov_b32 m0,s75
ex 2
zvav 160,64,80
ra 220,224,39936
cv 0,0,1
mvav 160,96,84
ds 255,8,79,0
zvav 164,68,80
ex 3
mvav 164,100,84
ra 224,224,40960
cv 1,2,3
zvav 168,72,80
ex 4
mvav 168,104,84
ra 228,224,41984
zvav 172,76,80
ds 255,8,79,1024
mvav 172,108,84
s_add_u32 s46,s46,16384
ex 5
v_smfmac_f32_16x16x64_bf16 v[152:155],v[228:231],v[80:87],v248
ra 232,224,43008
zvav 176,80,80
s_add_u32 s47,s47,32768
ex 6
mvav 176,112,84
s_add_u32 s95,s47,32768
ex 7
zvav 180,84,80
ra 236,224,44032
cv 2,4,5
mvav 180,116,84
ex 32
zvav 184,88,80
ds 255,8,79,2048
mvav 184,120,84
ex 33
zvav 188,92,80
ra 240,224,45056
cv 3,6,7
mvav 188,124,84
ex 34
mvav 192,64,88
ra 244,224,46080
cv 4,32,33
mvav 192,96,92
ex 35
mvav 196,68,88
ex 36
mvav 196,100,92
ds 255,8,79,3072
mvav 200,72,88
ra 248,224,47104
cv 5,34,35
mvav 200,104,92
s_cmp_ge_i32 s47,s46
ex 37
mvav 204,76,88
s_cselect_b32 s78,s95,s46
ex 38
mvav 204,108,92
s_cselect_b32 s79,s95,268435456
ex 39
v_smfmac_f32_16x16x64_bf16 v[156:159],v[228:231],v[88:95],v248
ra 252,224,48128
mvav 208,80,88
s_add_u32 s79,s79,16384
ex 8
mvav 208,112,92
ex 9
mvav 212,84,88
ex 10
mvav 212,116,92
ex 11
mvav 216,88,88
cv 6,36,37
cv 7,38,39
mvav 216,120,92
ex 12
mvav 220,92,88
ex 13
mvav 220,124,92
s_waitcnt vmcnt(16) lgkmcnt(0)
s_barrier
cvaa 64,192,128,232
s_bitcmp0_b32 s82,s81
s_cbranch_scc1 L_loop
s_bitcmp1_b32 s15,s81
s_cbranch_scc1 L_short
.p2align 6
L_loop:
mvaa 64,200,132
s_add_u32 s81,s81,1
ex 14
mvaa 64,208,136
s_add_u32 s44,s44,1024
ex 15
mvaa 64,216,140
cv 8,8,9
cv 9,10,11
cvaa 68,220,140,232
s_mov_b32 m0,s70
ex 40
mvaa 68,212,136
ds 249,8,78,0
mvaa 68,204,132
ex 41
mvaa 68,196,128
ex 42
cvaa 96,224,128,232
ra 64,225,0
cv 10,12,13
mvaa 96,232,132
ex 43
mvaa 96,240,136
ra 68,225,1024
cv 11,14,15
mvaa 96,248,140
ex 44
cvaa 100,252,140,232
ds 249,8,78,1024
mvaa 100,244,136
ex 45
mvaa 100,236,132
ra 72,225,2048
cv 12,40,41
mvaa 100,228,128
ex 46
cvaa 72,192,144,232
ra 76,225,3072
cv 13,42,43
mvaa 72,200,148
ex 47
mvaa 72,208,152
ex 16
mvaa 72,216,156
ds 249,8,78,2048
cvaa 76,220,156,232
ra 80,225,4096
cv 14,44,45
mvaa 76,212,152
ex 17
mvaa 76,204,148
ra 84,225,5120
cv 15,46,47
mvaa 76,196,144
ex 18
cvaa 104,224,144,232
ex 19
mvaa 104,232,148
ra 88,225,6144
cv 16,16,17
mvaa 104,240,152
ds 249,8,78,3072
mvaa 104,248,156
ex 20
cvaa 108,252,156,232
ra 92,225,7168
cv 17,18,19
mvaa 108,244,152
ex 21
mvaa 108,236,148
ex 22
mvaa 108,228,144
ra 96,225,8192
cv 18,20,21
cvaa 80,192,160,232
s_mov_b32 m0,s71
ex 23
mvaa 80,200,164
ra 100,225,9216
cv 19,22,23
mvaa 80,208,168
ds 249,8,79,0
mvaa 80,216,172
ex 48
cvaa 84,220,172,232
ex 49
mvaa 84,212,168
ra 104,225,10240
cv 20,48,49
mvaa 84,204,164
ex 50
mvaa 84,196,160
ra 108,225,11264
cvaa 112,224,160,232
ex 51
mvaa 112,232,164
ds 249,8,79,1024
mvaa 112,240,168
ex 52
mvaa 112,248,172
ra 112,225,12288
cv 21,50,51
cvaa 116,252,172,232
ex 53
mvaa 116,244,168
ra 116,225,13312
cv 22,52,53
mvaa 116,236,164
ex 54
mvaa 116,228,160
ex 55
cvaa 88,192,176,232
ds 249,8,79,2048
mvaa 88,200,180
ra 120,225,14336
cv 23,54,55
mvaa 88,208,184
ex 24
mvaa 88,216,188
ra 124,225,15360
cvaa 92,220,188,232
buffer_store_dwordx4 v[250:253],v244,s[8:11],s44 offen offset:1024 sc0 sc1
mvaa 92,212,184
ex 25
mvaa 92,204,180
ex 26
mvaa 92,196,176
ds 249,8,79,3072
cvaa 120,224,176,232
ex 27
mvaa 120,232,180
ex 28
mvaa 120,240,184
ex 29
mvaa 120,248,188
ex 30
cvaa 124,252,188,232
buffer_store_dwordx4 v[250:253],v244,s[8:11],s44 offen sc0 sc1
mvaa 124,244,184
ex 31
mvaa 124,236,180
cv 24,24,25
cv 25,26,27
mvaa 124,228,176
s_waitcnt vmcnt(19) lgkmcnt(0)
s_barrier
maav 0,64,0
ex 56
maav 0,96,4
s_mov_b32 m0,s72
ex 57
maav 4,68,0
ds 255,8,76,0
maav 4,100,4
ex 58
maav 8,72,0
ex 59
maav 8,104,4
cv 26,28,29
cv 27,30,31
maav 12,76,0
s_cmp_lg_u32 s80,s81
ex 60
maav 12,108,4
ex 61
v_smfmac_f32_16x16x64_bf16 v[144:147],v[228:231],v[0:7],v248
cv 28,56,57
maav 16,80,0
ex 62
maav 16,112,4
buffer_load_dword v247,v246,s[24:27],s49 offen
maav 20,84,0
ex 63
maav 20,116,4
cv 29,58,59
cv 30,60,61
maav 24,88,0
s_cbranch_scc1 L_evb_r
v_add_u32_e32 v224,s94,v224
s_lshl_b32 s86,s12,12
s_add_u32 s86,s86,131072
v_add_u32_e32 v236,s86,v244
ra 160,236,0
ra 164,236,1024
ra 168,236,2048
ra 172,236,3072
ra 176,236,16384
ra 180,236,17408
ra 184,236,18432
ra 188,236,19456
L_evb_r:
cv 31,62,63
maav 24,120,4
ra 192,224,0
maav 28,92,0
ds 255,8,76,1024
maav 28,124,4
maav 32,64,8
ra 196,224,1024
maav 32,96,12
s_add_u32 s49,s49,32768
maav 36,68,8
ra 200,224,2048
maav 36,100,12
maav 40,72,8
ds 255,8,76,2048
maav 40,104,12
maav 44,76,8
ra 204,224,3072
maav 44,108,12
v_smfmac_f32_16x16x64_bf16 v[148:151],v[228:231],v[8:15],v248
ra 208,224,4096
maav 48,80,8
maav 48,112,12
maav 52,84,8
ds 255,8,76,3072
maav 52,116,12
ra 212,224,5120
maav 56,88,8
ex 64
maav 56,120,12
ra 216,224,6144
maav 60,92,8
ex 65
maav 60,124,12
s_mov_b32 m0,s73
ex 66
mvav 160,64,16
ra 220,224,7168
cv 64,64,65
mvav 160,96,20
ds 255,8,77,0
mvav 164,68,16
ex 67
mvav 164,100,20
ra 224,224,8192
cv 65,66,67
mvav 168,72,16
ex 68
mvav 168,104,20
ra 228,224,9216
mvav 172,76,16
ds 255,8,77,1024
mvav 172,108,20
s_add_u32 s46,s46,16384
ex 69
v_smfmac_f32_16x16x64_bf16 v[152:155],v[228:231],v[16:23],v248
ra 232,224,10240
mvav 176,80,16
s_add_u32 s47,s47,32768
ex 70
mvav 176,112,20
s_add_u32 s95,s47,32768
ex 71
mvav 180,84,16
ra 236,224,11264
cv 66,68,69
mvav 180,116,20
ex 96
mvav 184,88,16
ds 255,8,77,2048
mvav 184,120,20
ex 97
mvav 188,92,16
ra 240,224,12288
cv 67,70,71
mvav 188,124,20
ex 98
mvav 192,64,24
ra 244,224,13312
cv 68,96,97
mvav 192,96,28
ex 99
mvav 196,68,24
ex 100
mvav 196,100,28
ds 255,8,77,3072
mvav 200,72,24
ra 248,224,14336
cv 69,98,99
mvav 200,104,28
s_cmp_ge_i32 s47,s46
ex 101
mvav 204,76,24
s_cselect_b32 s76,s95,s46
ex 102
mvav 204,108,28
s_cselect_b32 s77,s95,268435456
ex 103
v_smfmac_f32_16x16x64_bf16 v[156:159],v[228:231],v[24:31],v248
ra 252,224,15360
mvav 208,80,24
s_add_u32 s77,s77,16384
ex 72
mvav 208,112,28
ex 73
mvav 212,84,24
ex 74
mvav 212,116,28
ex 75
mvav 216,88,24
cv 70,100,101
)"
R"(cv 71,102,103
mvav 216,120,28
ex 76
mvav 220,92,24
ex 77
mvav 220,124,28
s_waitcnt vmcnt(19) lgkmcnt(0)
s_barrier
cvaa 0,192,128,232
mvaa 0,200,132
s_add_u32 s81,s81,1
ex 78
mvaa 0,208,136
s_add_u32 s44,s44,1024
ex 79
mvaa 0,216,140
cv 72,72,73
cv 73,74,75
cvaa 4,220,140,232
s_mov_b32 m0,s68
ex 104
mvaa 4,212,136
ds 249,8,76,0
mvaa 4,204,132
ex 105
mvaa 4,196,128
ex 106
cvaa 32,224,128,232
ra 64,225,32768
cv 74,76,77
mvaa 32,232,132
ex 107
mvaa 32,240,136
ra 68,225,33792
cv 75,78,79
mvaa 32,248,140
ex 108
cvaa 36,252,140,232
ds 249,8,76,1024
mvaa 36,244,136
ex 109
mvaa 36,236,132
ra 72,225,34816
cv 76,104,105
mvaa 36,228,128
ex 110
cvaa 8,192,144,232
ra 76,225,35840
cv 77,106,107
mvaa 8,200,148
ex 111
mvaa 8,208,152
ex 80
mvaa 8,216,156
ds 249,8,76,2048
cvaa 12,220,156,232
ra 80,225,36864
cv 78,108,109
mvaa 12,212,152
ex 81
mvaa 12,204,148
ra 84,225,37888
cv 79,110,111
mvaa 12,196,144
ex 82
cvaa 40,224,144,232
ex 83
mvaa 40,232,148
ra 88,225,38912
cv 80,80,81
mvaa 40,240,152
ds 249,8,76,3072
mvaa 40,248,156
ex 84
cvaa 44,252,156,232
ra 92,225,39936
cv 81,82,83
mvaa 44,244,152
ex 85
mvaa 44,236,148
ex 86
mvaa 44,228,144
ra 96,225,40960
cv 82,84,85
cvaa 16,192,160,232
s_mov_b32 m0,s69
ex 87
mvaa 16,200,164
ra 100,225,41984
cv 83,86,87
mvaa 16,208,168
ds 249,8,77,0
mvaa 16,216,172
ex 112
cvaa 20,220,172,232
ex 113
mvaa 20,212,168
ra 104,225,43008
cv 84,112,113
mvaa 20,204,164
ex 114
mvaa 20,196,160
ra 108,225,44032
cvaa 48,224,160,232
ex 115
mvaa 48,232,164
ds 249,8,77,1024
mvaa 48,240,168
ex 116
mvaa 48,248,172
ra 112,225,45056
cv 85,114,115
cvaa 52,252,172,232
ex 117
mvaa 52,244,168
ra 116,225,46080
cv 86,116,117
mvaa 52,236,164
ex 118
mvaa 52,228,160
ex 119
cvaa 24,192,176,232
ds 249,8,77,2048
mvaa 24,200,180
ra 120,225,47104
cv 87,118,119
mvaa 24,208,184
ex 88
mvaa 24,216,188
ra 124,225,48128
cvaa 28,220,188,232
mvaa 28,212,184
ex 89
mvaa 28,204,180
ex 90
mvaa 28,196,176
ds 249,8,77,3072
cvaa 56,224,176,232
ex 91
mvaa 56,232,180
ex 92
mvaa 56,240,184
ex 93
mvaa 56,248,188
ex 94
cvaa 60,252,188,232
mvaa 60,244,184
ex 95
mvaa 60,236,180
cv 88,88,89
cv 89,90,91
mvaa 60,228,176
s_waitcnt vmcnt(18) lgkmcnt(0)
s_barrier
maav 0,64,64
ex 120
maav 0,96,68
s_mov_b32 m0,s74
ex 121
maav 4,68,64
ds 255,8,78,0
maav 4,100,68
ex 122
maav 8,72,64
ex 123
maav 8,104,68
cv 90,92,93
cv 91,94,95
maav 12,76,64
ex 124
maav 12,108,68
ex 125
v_smfmac_f32_16x16x64_bf16 v[144:147],v[228:231],v[64:71],v248
cv 92,120,121
maav 16,80,64
ex 126
maav 16,112,68
buffer_load_dword v247,v245,s[36:39],s85 offen
maav 20,84,64
ex 127
maav 20,116,68
cv 93,122,123
cv 94,124,125
maav 24,88,64
cv 95,126,127
maav 24,120,68
ra 192,224,32768
maav 28,92,64
ds 255,8,78,1024
maav 28,124,68
maav 32,64,72
ra 196,224,33792
maav 32,96,76
s_add_u32 s85,s85,131072
maav 36,68,72
ra 200,224,34816
maav 36,100,76
maav 40,72,72
ds 255,8,78,2048
maav 40,104,76
maav 44,76,72
ra 204,224,35840
maav 44,108,76
v_smfmac_f32_16x16x64_bf16 v[148:151],v[228:231],v[72:79],v248
ra 208,224,36864
maav 48,80,72
maav 48,112,76
maav 52,84,72
ds 255,8,78,3072
maav 52,116,76
ra 212,224,37888
maav 56,88,72
ex 0
maav 56,120,76
ra 216,224,38912
maav 60,92,72
ex 1
maav 60,124,76
s_mov_b32 m0,s75
ex 2
mvav 160,64,80
ra 220,224,39936
cv 0,0,1
mvav 160,96,84
ds 255,8,79,0
mvav 164,68,80
ex 3
mvav 164,100,84
ra 224,224,40960
cv 1,2,3
mvav 168,72,80
ex 4
mvav 168,104,84
ra 228,224,41984
mvav 172,76,80
ds 255,8,79,1024
mvav 172,108,84
s_add_u32 s46,s46,16384
ex 5
v_smfmac_f32_16x16x64_bf16 v[152:155],v[228:231],v[80:87],v248
ra 232,224,43008
mvav 176,80,80
s_add_u32 s47,s47,32768
ex 6
mvav 176,112,84
s_add_u32 s95,s47,32768
ex 7
mvav 180,84,80
ra 236,224,44032
cv 2,4,5
mvav 180,116,84
ex 32
mvav 184,88,80
ds 255,8,79,2048
mvav 184,120,84
ex 33
mvav 188,92,80
ra 240,224,45056
cv 3,6,7
mvav 188,124,84
ex 34
mvav 192,64,88
ra 244,224,46080
cv 4,32,33
mvav 192,96,92
ex 35
mvav 196,68,88
ex 36
mvav 196,100,92
ds 255,8,79,3072
mvav 200,72,88
ra 248,224,47104
cv 5,34,35
mvav 200,104,92
s_cmp_ge_i32 s47,s46
ex 37
mvav 204,76,88
s_cselect_b32 s78,s95,s46
ex 38
mvav 204,108,92
s_cselect_b32 s79,s95,268435456
ex 39
v_smfmac_f32_16x16x64_bf16 v[156:159],v[228:231],v[88:95],v248
ra 252,224,48128
mvav 208,80,88
s_add_u32 s79,s79,16384
ex 8
mvav 208,112,92
ex 9
mvav 212,84,88
ex 10
mvav 212,116,92
ex 11
mvav 216,88,88
cv 6,36,37
cv 7,38,39
mvav 216,120,92
ex 12
mvav 220,92,88
ex 13
mvav 220,124,92
s_waitcnt vmcnt(17) lgkmcnt(0)
s_barrier
cvaa 64,192,128,232
s_bitcmp0_b32 s82,s81
s_cbranch_scc1 L_loop
s_bitcmp0_b32 s15,s81
s_cbranch_scc1 L_eva_r
L_short:
v_add_f32_e32 v226,v152,v153
v_add_f32_e32 v227,v156,v157
v_subrev_u32_e32 v236,0x23800000,v226
v_subrev_u32_e32 v237,0x23800000,v227
v_max_u32_e32 v236,v236,v237
v_cmp_lt_u32_e64 s[40:41],s48,v236
v_rcp_f32_e32 v226,v226
v_rcp_f32_e32 v227,v227
s_add_u32 s86,s13,4096
v_mov_b32_e32 v236,0
v_mov_b32_e32 v237,0
v_mov_b32_e32 v238,0
v_mov_b32_e32 v239,0
v_mov_b64_e32 v[152:153],0
v_mov_b64_e32 v[156:157],0
v_mul_f32_e32 v226,v242,v226
v_mul_f32_e32 v227,v243,v227
v_add_u32_e32 v225,s94,v225
v_pk_mul_f32 v[160:161],v[160:161],v[226:227] op_sel:[0,0] op_sel_hi:[1,0]
v_pk_mul_f32 v[162:163],v[162:163],v[226:227] op_sel:[0,0] op_sel_hi:[1,0]
v_pk_mul_f32 v[164:165],v[164:165],v[226:227] op_sel:[0,0] op_sel_hi:[1,0]
v_pk_mul_f32 v[166:167],v[166:167],v[226:227] op_sel:[0,0] op_sel_hi:[1,0]
cv 160,160,161
cv 161,162,163
cv 162,164,165
cv 163,166,167
buffer_store_dwordx4 v[160:163],v244,s[8:11],s13 offen offset:0 sc0 sc1
v_pk_mul_f32 v[168:169],v[168:169],v[226:227] op_sel:[0,0] op_sel_hi:[1,0]
v_pk_mul_f32 v[170:171],v[170:171],v[226:227] op_sel:[0,0] op_sel_hi:[1,0]
v_pk_mul_f32 v[172:173],v[172:173],v[226:227] op_sel:[0,0] op_sel_hi:[1,0]
v_pk_mul_f32 v[174:175],v[174:175],v[226:227] op_sel:[0,0] op_sel_hi:[1,0]
cv 168,168,169
cv 169,170,171
cv 170,172,173
cv 171,174,175
buffer_store_dwordx4 v[168:171],v244,s[8:11],s13 offen offset:2048 sc0 sc1
v_pk_mul_f32 v[176:177],v[176:177],v[226:227] op_sel:[0,0] op_sel_hi:[1,0]
v_pk_mul_f32 v[178:179],v[178:179],v[226:227] op_sel:[0,0] op_sel_hi:[1,0]
v_pk_mul_f32 v[180:181],v[180:181],v[226:227] op_sel:[0,0] op_sel_hi:[1,0]
v_pk_mul_f32 v[182:183],v[182:183],v[226:227] op_sel:[0,0] op_sel_hi:[1,0]
cv 176,176,177
cv 177,178,179
cv 178,180,181
v_mfma_f32_32x32x16_bf16 v[160:175],v[236:239],v[236:239],0
cv 179,182,183
buffer_store_dwordx4 v[176:179],v244,s[8:11],s86 offen offset:0 sc0 sc1
v_pk_mul_f32 v[184:185],v[184:185],v[226:227] op_sel:[0,0] op_sel_hi:[1,0]
v_pk_mul_f32 v[186:187],v[186:187],v[226:227] op_sel:[0,0] op_sel_hi:[1,0]
v_pk_mul_f32 v[188:189],v[188:189],v[226:227] op_sel:[0,0] op_sel_hi:[1,0]
v_pk_mul_f32 v[190:191],v[190:191],v[226:227] op_sel:[0,0] op_sel_hi:[1,0]
cv 184,184,185
cv 185,186,187
cv 186,188,189
cv 187,190,191
buffer_store_dwordx4 v[184:187],v244,s[8:11],s86 offen offset:2048 sc0 sc1
v_pk_mul_f32 v[192:193],v[192:193],v[226:227] op_sel:[0,1] op_sel_hi:[1,1]
v_pk_mul_f32 v[194:195],v[194:195],v[226:227] op_sel:[0,1] op_sel_hi:[1,1]
v_pk_mul_f32 v[196:197],v[196:197],v[226:227] op_sel:[0,1] op_sel_hi:[1,1]
v_pk_mul_f32 v[198:199],v[198:199],v[226:227] op_sel:[0,1] op_sel_hi:[1,1]
cv 192,192,193
cv 193,194,195
cv 194,196,197
v_mfma_f32_32x32x16_bf16 v[176:191],v[236:239],v[236:239],0
cv 195,198,199
buffer_store_dwordx4 v[192:195],v244,s[8:11],s13 offen offset:1024 sc0 sc1
v_pk_mul_f32 v[200:201],v[200:201],v[226:227] op_sel:[0,1] op_sel_hi:[1,1]
v_pk_mul_f32 v[202:203],v[202:203],v[226:227] op_sel:[0,1] op_sel_hi:[1,1]
v_pk_mul_f32 v[204:205],v[204:205],v[226:227] op_sel:[0,1] op_sel_hi:[1,1]
v_pk_mul_f32 v[206:207],v[206:207],v[226:227] op_sel:[0,1] op_sel_hi:[1,1]
cv 200,200,201
cv 201,202,203
cv 202,204,205
cv 203,206,207
buffer_store_dwordx4 v[200:203],v244,s[8:11],s13 offen offset:3072 sc0 sc1
v_pk_mul_f32 v[208:209],v[208:209],v[226:227] op_sel:[0,1] op_sel_hi:[1,1]
v_pk_mul_f32 v[210:211],v[210:211],v[226:227] op_sel:[0,1] op_sel_hi:[1,1]
v_pk_mul_f32 v[212:213],v[212:213],v[226:227] op_sel:[0,1] op_sel_hi:[1,1]
v_pk_mul_f32 v[214:215],v[214:215],v[226:227] op_sel:[0,1] op_sel_hi:[1,1]
cv 208,208,209
cv 209,210,211
cv 210,212,213
v_mfma_f32_32x32x16_bf16 v[192:207],v[236:239],v[236:239],0
cv 211,214,215
buffer_store_dwordx4 v[208:211],v244,s[8:11],s86 offen offset:1024 sc0 sc1
v_pk_mul_f32 v[216:217],v[216:217],v[226:227] op_sel:[0,1] op_sel_hi:[1,1]
v_pk_mul_f32 v[218:219],v[218:219],v[226:227] op_sel:[0,1] op_sel_hi:[1,1]
v_pk_mul_f32 v[220:221],v[220:221],v[226:227] op_sel:[0,1] op_sel_hi:[1,1]
v_pk_mul_f32 v[222:223],v[222:223],v[226:227] op_sel:[0,1] op_sel_hi:[1,1]
cv 216,216,217
cv 217,218,219
cv 218,220,221
cv 219,222,223
buffer_store_dwordx4 v[216:219],v244,s[8:11],s86 offen offset:3072 sc0 sc1
s_nop 1
v_mfma_f32_32x32x16_bf16 v[208:223],v[236:239],v[236:239],0
L_eva_r:
s_cmp_lg_u32 s81,14
s_cbranch_scc1 L_loop
s_add_u32 s81,s81,1
s_bitcmp1_b32 170,s20
s_cbranch_scc1 L_odd
L_last:
s_lshl_b32 s86,s21,2
s_sub_u32 s87,s13,s67
s_add_u32 s86,s86,s12
mvaa 64,200,132
s_lshl_b32 s86,s86,8
v_add_u32_e32 v252,s87,v244
s_add_u32 s86,s86,L_last-L_att_pc+1024
v_lshl_add_u32 v226,v244,3,s86
s_lshl_b32 s86,s12,1
s_bfe_u32 s87,s21,65539
mvaa 64,208,136
s_add_u32 s86,s86,s87
v_min_u32_e32 v226,L_att_end-L_att_pc-4,v226
s_lshl_b32 s86,s86,7
s_add_u32 s86,s86,L_pend2-L_att_pc
v_writelane_b32 v226,s86,2
mvaa 64,216,140
s_mov_b64 exec,7
global_load_dword v253,v226,s[96:97]
s_mov_b64 exec,-1
cvaa 68,220,140,232
s_add_u32 m0,s52,32768
v_and_b32_e32 v224,16,v244
v_lshlrev_b32_e32 v224,5,v224
mvaa 68,212,136
s_xor_b32 s51,s62,0
v_lshrrev_b32_e32 v239,4,v244
v_lshrrev_b32_e32 v236,3,v239
mvaa 68,204,132
s_lshl_b32 s87,s12,13
v_and_b32_e32 v237,112,v244
v_and_b32_e32 v238,48,v239
mvaa 68,196,128
s_xor_b32 s87,s87,8192
ra 64,225,0
v_lshlrev_b32_e32 v238,1,v238
cvaa 96,224,128,232
buffer_load_dword v253,v224,s[8:11],s53 offen sc1
mvaa 96,232,132
s_mov_b32 s84,6
ra 68,225,1024
v_xor_b32_e32 v237,v237,v238
mvaa 96,240,136
s_add_u32 s81,s81,1
ra 72,225,2048
v_lshl_add_u32 v245,v236,11,v237
mvaa 96,248,140
s_mov_b32 s84,4
v_add_u32_e32 v246,15360,v245
v_add_u32_e32 v249,30720,v245
cvaa 100,252,140,232
s_andn2_b32 s69,64,s54
ra 76,225,3072
s_waitcnt vmcnt(9)
v_add_u32_e32 v247,46080,v245
mvaa 100,244,136
dl 245,24,51,0
mvaa 100,236,132
s_add_u32 s88,s14,4096
ra 80,225,4096
cv 8,8,9
mvaa 100,228,128
s_lshl_b32 s35,s19,1
ra 84,225,5120
cv 9,10,11
cvaa 72,192,144,232
s_lshr_b32 s33,40569,s35
ra 88,225,6144
cv 10,12,13
mvaa 72,200,148
s_and_b32 s33,s33,3
ra 92,225,7168
v_add_u32_e32 v250,4096,v252
mvaa 72,208,152
dl 246,24,51,1024
mvaa 72,216,156
s_lshl_b32 s33,s33,13
ra 96,225,8192
v_mov_b32_e32 v251,4
cvaa 76,220,156,232
s_lshr_b32 s34,59294,s35
ra 100,225,9216
s_and_b32 s34,s34,3
mvaa 76,212,152
s_lshl_b32 s34,s34,13
ra 104,225,10240
s_xor_b32 s35,s33,s34
mvaa 76,204,148
s_and_b32 s98,s19,3
ra 108,225,11264
s_lshl_b32 s98,s98,13
mvaa 76,196,144
dl 249,24,51,2048
cvaa 104,224,144,232
s_mov_b32 s74,0x300003
ra 112,225,12288
s_mov_b32 s75,0x30000300
mvaa 104,232,148
s_lshl_b32 s71,s12,15
ra 116,225,13312
s_add_u32 s71,s71,s54
mvaa 104,240,152
s_mov_b64 s[44:45],s[30:31]
ra 120,225,14336
s_mov_b32 s46,4194304
mvaa 104,248,156
s_mov_b32 s47,131072
ex 14
cvaa 108,252,156,232
dl 247,24,51,3072
mvaa 108,244,152
s_add_u32 m0,s52,49152
s_xor_b32 s51,s63,0
ra 124,225,15360
mvaa 108,236,148
s_lshl_b32 s70,s23,11
ex 15
mvaa 108,228,144
s_add_u32 s70,s70,s71
ex 40
cvaa 80,192,160,232
ex 41
mvaa 80,200,164
dl 245,24,51,0
mvaa 80,208,168
ex 42
mvaa 80,216,172
ex 43
cvaa 84,220,172,232
ex 44
mvaa 84,212,168
ex 45
mvaa 84,204,164
dl 246,24,51,1024
mvaa 84,196,160
ex 46
cvaa 112,224,160,232
ex 47
mvaa 112,232,164
ex 16
mvaa 112,240,168
ex 17
mvaa 112,248,172
dl 249,24,51,2048
cvaa 116,252,172,232
ex 18
mvaa 116,244,168
ex 19
mvaa 116,236,164
ex 20
mvaa 116,228,160
ex 21
cvaa 88,192,176,232
dl 247,24,51,3072
s_waitcnt vmcnt(8)
v_cmp_ne_u32_e32 vcc,4,v253
mvaa 88,200,180
s_cbranch_vccnz L_spbe
L_sprbe:
ex 22
mvaa 88,208,184
s_xor_b32 s50,s66,262144
ex 23
mvaa 88,216,188
ex 48
cvaa 92,220,188,232
ex 49
mvaa 92,212,184
la 128,252,50,0
mvaa 92,204,180
s_waitcnt lgkmcnt(0)
s_barrier
mvaa 92,196,176
s_mov_b32 m0,s72
ex 50
cvaa 120,224,176,232
s_lshl_b32 s72,s22,11
ex 51
mvaa 120,232,180
s_add_u32 s72,s72,s71
ex 52
mvaa 120,240,184
ds 255,8,76,0
mvaa 120,248,188
s_add_u32 s71,s70,131072
ex 53
cvaa 124,252,188,232
ex 54
mvaa 124,244,184
ex 55
mvaa 124,236,180
ex 24
mvaa 124,228,176
ds 255,8,76,1024
maav 0,64,0
s_cmp_lg_u32 s19,7
s_cbranch_scc1 L_j7e
s_nop 1
s_lshl_b32 s86,s12,12
s_add_u32 s86,s86,131072
v_add_u32_e32 v236,s86,v244
ra 160,236,0
ra 164,236,1024
ra 168,236,2048
ra 172,236,3072
ra 176,236,16384
ra 180,236,17408
ra 184,236,18432
ra 188,236,19456
v_add_u32_e32 v224,s94,v224
s_waitcnt lgkmcnt(0)
L_j7e:
ex 25
maav 0,96,4
ex 26
maav 4,68,0
ex 27
maav 4,100,4
cv 11,14,15
cv 12,40,41
maav 8,72,0
ds 255,8,76,2048
maav 8,104,4
cv 13,42,43
cv 14,44,45
maav 12,76,0
cv 15,46,47
cv 16,16,17
maav 12,108,4
ex 28
v_smfmac_f32_16x16x64_bf16 v[144:147],v[228:231],v[0:7],v248
cv 17,18,19
maav 16,80,0
ds 255,8,76,3072
maav 16,112,4
s_mov_b32 m0,s73
ex 29
maav 20,84,0
s_add_u32 s73,s72,131072
ex 30
maav 20,116,4
ex 31
maav 24,88,0
ex 56
maav 24,120,4
ds 255,8,77,0
maav 28,92,0
ex 57
maav 28,124,4
ex 58
maav 32,64,8
ex 59
maav 32,96,12
ex 60
maav 36,68,8
ds 255,8,77,1024
maav 36,100,12
cv 18,20,21
cv 19,22,23
maav 40,72,8
cv 20,48,49
cv 21,50,51
maav 40,104,12
cv 22,52,53
cv 23,54,55
maav 44,76,8
ex 61
maav 44,108,12
ds 255,8,77,2048
v_smfmac_f32_16x16x64_bf16 v[148:151],v[228:231],v[8:15],v248
cv 24,24,25
maav 48,80,8
ex 62
maav 48,112,12
ex 63
maav 52,84,8
ra 192,244,0
cv 25,26,27
maav 52,116,12
L_vs18:
s_cbranch_execz L_vs22
ds 255,8,77,3072
maav 56,88,8
cv 26,28,29
cv 27,30,31
maav 56,120,12
cv 28,56,57
cv 29,58,59
maav 60,92,8
cv 30,60,61
cv 31,62,63
maav 60,124,12
ex 64
mvav 160,64,16
la 132,252,50,2048
mvav 160,96,20
ex 65
mvav 164,68,16
ex 66
mvav 164,100,20
ex 67
mvav 168,72,16
ex 68
mvav 168,104,20
la 136,250,50,0
mvav 172,76,16
ex 69
mvav 172,108,20
ex 70
v_smfmac_f32_16x16x64_bf16 v[152:155],v[228:231],v[16:23],v248
ra 200,244,2048
mvav 176,80,16
ra 208,244,4096
cv 64,64,65
mvav 176,112,20
la 140,250,50,2048
mvav 180,84,16
ra 216,244,6144
cv 65,66,67
mvav 180,116,20
ex 71
mvav 184,88,16
ex 96
mvav 184,120,20
s_barrier
ex 97
mvav 188,92,16
la 156,250,50,3072
mvav 188,124,20
s_add_u32 m0,s52,131072
ra 196,244,1024
s_xor_b32 s51,s62,1048576
mvav 192,64,24
ra 204,244,3072
cv 66,68,69
mvav 192,96,28
ra 212,244,5120
cv 67,70,71
mvav 196,68,24
ra 220,244,7168
cv 68,96,97
mvav 196,100,28
la 152,250,50,1024
mvav 200,72,24
ex 98
mvav 200,104,28
ex 99
mvav 204,76,24
ra 224,244,8192
cv 69,98,99
mvav 204,108,28
ra 232,244,10240
v_add_u32_e32 v6,s87,v244
v_smfmac_f32_16x16x64_bf16 v[156:159],v[228:231],v[24:31],v248
ra 240,244,12288
mvav 208,80,24
la 148,252,50,3072
mvav 208,112,28
s_xor_b32 s87,s52,4096
ra 248,244,14336
v_add_u32_e32 v7,98304,v6
mvav 212,84,24
s_add_u32 s87,s87,150528
ra 252,244,15360
v_add_u32_e32 v5,65536,v244
mvav 212,116,28
ra 244,244,13312
v_add_u32_e32 v32,s87,v244
mvav 216,88,24
ra 228,244,9216
v_and_b32_e32 v224,960,v244
mvav 216,120,28
la 144,252,50,1024
mvav 220,92,24
s_add_u32 s50,s50,s55
ra 236,244,11264
mvav 220,124,28
ra 64,225,32768
s_waitcnt lgkmcnt(8)
cvaa 24,192,176,232
dl 245,24,51,0
mvaa 24,200,180
ra 96,225,40960
mvaa 24,208,184
ra 68,225,33792
mvaa 24,216,188
dl 246,24,51,1024
cvaa 16,192,160,128
ra 100,225,41984
mvaa 16,200,164
ra 72,225,34816
mvaa 16,208,168
dl 249,24,51,2048
mvaa 16,216,172
ra 104,225,43008
cvaa 28,196,176,232
ra 76,225,35840
mvaa 28,204,180
dl 247,24,51,3072
mvaa 28,212,184
s_add_u32 m0,s52,147456
s_xor_b32 s51,s63,1048576
ra 108,225,44032
mvaa 28,220,188
ra 80,225,36864
cvaa 20,196,160,132
dl 245,24,51,0
mvaa 20,204,164
ra 112,225,45056
mvaa 20,212,168
ra 192,244,16384
mvaa 20,220,172
dl 246,24,51,1024
cvaa 56,224,176,232
ra 200,244,18432
s_waitcnt lgkmcnt(7)
mvaa 56,232,180
ra 208,244,20480
mvaa 56,240,184
dl 249,24,51,2048
mvaa 56,248,188
ra 216,244,22528
cvaa 48,224,160,136
ra 196,244,17408
mvaa 48,232,164
ra 204,244,19456
mvaa 48,240,168
ra 212,244,21504
mvaa 48,248,172
ra 220,244,23552
cvaa 60,228,176,232
ra 224,244,24576
mvaa 60,236,180
ra 232,244,26624
mvaa 60,244,184
ra 240,244,28672
mvaa 60,252,188
ra 248,244,30720
cvaa 52,228,160,140
ra 84,225,37888
mvaa 52,236,164
ra 116,225,46080
mvaa 52,244,168
la 160,252,50,0
ra 228,244,25600
mvaa 52,252,172
ra 236,244,27648
s_waitcnt lgkmcnt(10)
cvaa 8,192,176,128
ra 244,244,29696
mvaa 8,200,180
la 164,252,50,2048
ra 252,244,31744
mvaa 8,208,184
ra 88,225,38912
mvaa 8,216,188
ra 120,225,47104
cvaa 12,196,176,132
la 168,250,50,0
mvaa 12,204,180
ra 92,225,39936
ra 124,225,48128
s_waitcnt lgkmcnt(9)
mvaa 12,212,184
ex 100
mvaa 12,220,188
la 172,250,50,2048
cvaa 40,224,176,136
ex 101
mvaa 40,232,180
ex 102
mvaa 40,240,184
ex 103
mvaa 40,248,188
cv 70,100,101
cv 71,102,103
s_waitcnt lgkmcnt(4)
cvaa 44,228,176,140
ex 72
mvaa 44,236,180
s_waitcnt vmcnt(18) lgkmcnt(0)
s_barrier
ex 73
mvaa 44,244,184
la 176,252,50,1024
mvaa 44,252,188
ex 74
maav 0,64,64
ex 75
maav 0,96,68
la 188,250,50,3072
maav 4,68,64
ra 192,5,0
cv 72,72,73
maav 4,100,68
ra 196,5,1024
cv 73,74,75
maav 8,72,64
la 184,250,50,1024
ds_write_b128 v6,a[0:3] offset:0
maav 8,104,68
ex 76
maav 12,76,64
ex 77
maav 12,108,68
la 180,252,50,3072
ds_write_b128 v6,a[4:7] offset:1024
v_smfmac_f32_16x16x64_bf16 v[144:147],v[228:231],v[64:71],v248
cv 74,76,77
maav 16,80,64
ex 78
maav 16,112,68
ds_write_b128 v6,a[8:11] offset:2048
ex 79
maav 20,84,64
ex 104
maav 20,116,68
ds_write_b128 v6,a[12:15] offset:3072
ex 105
maav 24,88,64
ex 106
ex 107
ex 108
maav 24,120,68
ds_write_b128 v6,a[16:19] offset:4096
cv 75,78,79
maav 28,92,64
ex 109
ex 110
ex 111
maav 28,124,68
cv 76,104,105
cv 77,106,107
cv 78,108,109
cv 79,110,111
maav 32,64,72
ds_write_b128 v6,a[20:23] offset:5120
maav 32,96,76
ex 80
maav 36,68,72
ex 81
maav 36,100,76
ds_write_b128 v6,a[24:27] offset:6144
maav 40,72,72
ex 82
maav 40,104,76
ex 83
maav 44,76,72
ds_write_b128 v6,a[28:31] offset:7168
maav 44,108,76
cv 80,80,81
cv 81,82,83
v_smfmac_f32_16x16x64_bf16 v[148:151],v[228:231],v[72:79],v248
ex 84
maav 48,80,72
ds_write_b128 v7,a[32:35] offset:0
maav 48,112,76
ex 85
ex 86
ex 87
maav 52,84,72
ex 112
ex 113
maav 52,116,76
ds_write_b128 v7,a[36:39] offset:1024
cv 82,84,85
maav 56,88,72
ex 114
ex 115
ex 116
maav 56,120,76
ex 117
ex 118
ex 119
maav 60,92,72
ds_write_b128 v7,a[40:43] offset:2048
cv 83,86,87
maav 60,124,76
cv 84,112,113
cv 85,114,115
cv 86,116,117
cv 87,118,119
mvav 160,64,80
ex 88
mvav 160,96,84
ds_write_b128 v7,a[44:47] offset:3072
mvav 164,68,80
v_add_f32_e32 v130,v144,v145
v_add_f32_e32 v131,v148,v149
)"
R"(mvav 164,100,84
ex 89
mvav 168,72,80
ds_write_b128 v7,a[48:51] offset:4096
mvav 168,104,84
ex 90
mvav 172,76,80
ex 91
mvav 172,108,84
ds_write_b128 v32,v[128:131]
v_smfmac_f32_16x16x64_bf16 v[152:155],v[228:231],v[80:87],v248
ex 92
ex 93
mvav 176,80,80
ds_write_b128 v7,a[52:55] offset:5120
ex 94
mvav 176,112,84
ra 200,5,2048
ra 204,5,3072
ex 95
cv 88,88,89
mvav 180,84,80
ra 208,5,4096
ra 212,5,5120
cv 89,90,91
cv 90,92,93
ex 120
mvav 180,116,84
ds_write_b128 v7,a[56:59] offset:6144
ex 121
mvav 184,88,80
ex 122
ex 123
cv 91,94,95
mvav 184,120,84
ex 124
ex 125
mvav 188,92,80
ds_write_b128 v7,a[60:63] offset:7168
ex 126
mvav 188,124,84
ra 216,5,6144
ra 220,5,7168
ra 224,5,8192
ra 232,5,10240
ex 127
cv 92,120,121
mvav 192,64,88
ra 240,5,12288
ra 248,5,14336
ra 228,5,9216
ra 236,5,11264
ra 244,5,13312
ra 252,5,15360
cv 93,122,123
cv 94,124,125
cv 95,126,127
v_xor_b32_e32 v6,8192,v6
s_nop 0
mvav 192,96,92
ex 16
mvav 196,68,88
s_waitcnt lgkmcnt(10)
s_xor_b32 s87,s87,0
mvav 196,100,92
s_xor_b32 s50,s66,294912
s_barrier
v_xor_b32_e32 v7,8192,v7
mvav 200,72,88
la 4,252,50,0
ra 64,5,16384
ra 96,5,24576
rv 64,6,0
rv 68,6,1024
rv 72,6,2048
mvav 200,104,92
rv 76,6,3072
rv 80,6,4096
rv 84,6,5120
ex 17
ex 18
mvav 204,76,88
rv 96,7,0
rv 100,7,1024
ex 19
mvav 204,108,92
la 12,252,50,2048
rv 104,7,2048
rv 108,7,3072
cv 16,16,17
v_smfmac_f32_16x16x64_bf16 v[156:159],v[228:231],v[88:95],v248
rv 112,7,4096
rv 116,7,5120
ex 20
ex 21
mvav 208,80,88
rv 120,7,6144
rv 124,7,7168
ex 22
ex 23
mvav 208,112,92
la 20,250,50,0
ra 68,5,17408
ra 100,5,25600
cv 17,18,19
cv 18,20,21
mvav 212,84,88
ra 72,5,18432
ra 104,5,26624
ex 48
ex 49
mvav 212,116,92
ra 76,5,19456
ra 108,5,27648
ex 50
ex 51
mvav 216,88,88
la 28,250,50,2048
ra 80,5,20480
ra 112,5,28672
ra 84,5,21504
mvav 216,120,92
ra 116,5,29696
cv 19,22,23
ex 52
ex 53
mvav 220,92,88
ra 88,5,22528
cv 20,48,49
ex 54
ex 55
mvav 220,124,92
la 60,250,50,3072
ra 120,5,30720
cv 21,50,51
cv 22,52,53
cv 23,54,55
s_bitcmp1_b32 s82,s81
s_cbranch_scc1 L_pend2
L_ret_eb4:
mvav 160,192,16
ra 92,5,23552
rv 88,6,6144
ra 124,5,31744
rv 92,6,7168
ex 24
s_waitcnt lgkmcnt(10)
mvav 160,224,20
ex 25
ex 26
v_smfmac_f32_16x16x64_bf16 v[152:155],v[228:231],v[16:23],v248
ex 27
v_xor_b32_e32 v32,4096,v32
mvav 164,196,16
la 52,250,50,1024
ds_read_b128 v[236:239],v32
cv 24,24,25
ex 28
mvav 164,228,20
ex 29
cv 25,26,27
ex 30
ex 31
mvav 168,200,16
ex 56
ex 57
mvav 168,232,20
la 44,252,50,3072
cv 26,28,29
ex 58
ex 59
mvav 172,204,16
ex 60
ex 61
ex 62
mvav 172,236,20
s_waitcnt lgkmcnt(0)
s_barrier
cv 27,30,31
ex 63
mvav 176,208,16
la 36,252,50,1024
cv 28,56,57
cv 29,58,59
cv 30,60,61
cv 31,62,63
mvav 176,240,20
s_add_u32 m0,s52,147456
ex 8
s_xor_b32 s51,s63,1048576
ex 9
ex 10
mvav 180,212,16
s_add_u32 s50,s50,s55
ex 11
cv 8,8,9
cv 9,10,11
ex 12
mvav 180,244,20
dl 247,24,51,3072
ex 13
ex 14
mvav 184,216,16
s_add_u32 m0,s52,0
ex 15
ex 40
ex 41
mvav 184,248,20
s_xor_b32 s51,s64,0
ex 42
ex 43
ex 44
mvav 188,220,16
la 0,252,50,0
ex 45
ex 46
mvav 188,252,20
ex 47
cv 10,12,13
cv 11,14,15
cv 12,40,41
cv 13,42,43
cv 14,44,45
v_smfmac_f32_16x16x64_bf16 v[156:159],v[228:231],v[24:31],v248
cv 15,46,47
v_bfe_u32 v225,v244,1,5
s_nop 0
v_smfmac_f32_16x16x64_bf16 v[156:159],v[228:231],v[8:15],v248
v_lshl_add_u32 v224,v224,1,v225
v_bfe_u32 v225,v244,7,2
v_lshl_add_u32 v224,v225,5,v224
mvav 192,192,24
la 8,252,50,2048
v_xor_b32_e32 v224,s69,v224
v_add_f32_e32 v152,v152,v153
v_add_f32_e32 v137,v152,v238
v_subrev_u32_e32 v236,0x23800000,v137
mvav 192,224,28
v_rcp_f32_e32 v137,v137
mvav 192,64,8
tv 128,224,32768
v_mul_f32_e32 v232,v240,v137
v_mul_f32_e32 v64,v232,v64
v_mul_f32_e32 v65,v232,v65
v_mul_f32_e32 v66,v232,v66
mvav 192,96,12
la 16,250,50,0
v_mul_f32_e32 v67,v232,v67
v_mul_f32_e32 v68,v232,v68
v_mul_f32_e32 v69,v232,v69
v_mul_f32_e32 v70,v232,v70
mvav 196,196,24
v_fmac_f32_e32 v64,v232,v160
v_fmac_f32_e32 v65,v232,v161
v_mul_f32_e32 v71,v232,v71
mvav 196,228,28
v_fmac_f32_e32 v66,v232,v162
v_fmac_f32_e32 v67,v232,v163
v_fmac_f32_e32 v68,v232,v164
v_fmac_f32_e32 v69,v232,v165
v_fmac_f32_e32 v70,v232,v166
mvav 196,68,8
la 24,250,50,2048
v_fmac_f32_e32 v71,v232,v167
cv 160,64,65
cv 161,66,67
cv 162,68,69
cv 163,70,71
mvav 196,100,12
tv 130,224,34816
tv 132,224,36864
mvav 200,200,24
tv 134,224,38912
tv 144,224,40960
v_mul_f32_e32 v72,v232,v72
v_mul_f32_e32 v73,v232,v73
v_mul_f32_e32 v74,v232,v74
mvav 200,232,28
buffer_store_dwordx4 v[160:163],v244,s[8:11],s14 offen offset:0 sc0 sc1
v_mul_f32_e32 v75,v232,v75
v_mul_f32_e32 v76,v232,v76
v_mul_f32_e32 v77,v232,v77
v_mul_f32_e32 v78,v232,v78
mvav 200,72,8
v_fmac_f32_e32 v72,v232,v168
v_fmac_f32_e32 v73,v232,v169
v_mul_f32_e32 v79,v232,v79
mvav 200,104,12
L_vs19:
s_cbranch_execz L_vs23
v_fmac_f32_e32 v74,v232,v170
v_fmac_f32_e32 v75,v232,v171
v_fmac_f32_e32 v76,v232,v172
v_fmac_f32_e32 v77,v232,v173
v_fmac_f32_e32 v78,v232,v174
mvav 204,204,24
la 56,250,50,3072
v_fmac_f32_e32 v79,v232,v175
cv 168,72,73
cv 169,74,75
cv 170,76,77
cv 171,78,79
mvav 204,236,28
tv 146,224,43008
tv 148,224,45056
mvav 204,76,8
tv 150,224,47104
v_mul_f32_e32 v80,v232,v80
v_mul_f32_e32 v81,v232,v81
v_mul_f32_e32 v82,v232,v82
mvav 204,108,12
buffer_store_dwordx4 v[168:171],v244,s[8:11],s14 offen offset:2048 sc0 sc1
v_mul_f32_e32 v83,v232,v83
v_mul_f32_e32 v84,v232,v84
v_mul_f32_e32 v85,v232,v85
v_mul_f32_e32 v86,v232,v86
v_mul_f32_e32 v87,v232,v87
mvav 208,208,24
v_fmac_f32_e32 v80,v232,v176
v_fmac_f32_e32 v81,v232,v177
v_fmac_f32_e32 v82,v232,v178
mvav 208,240,28
v_fmac_f32_e32 v83,v232,v179
v_fmac_f32_e32 v84,v232,v180
v_fmac_f32_e32 v85,v232,v181
cv 176,80,81
cv 177,82,83
mvav 208,80,8
la 48,250,50,1024
v_fmac_f32_e32 v86,v232,v182
v_fmac_f32_e32 v87,v232,v183
cv 178,84,85
cv 179,86,87
mvav 208,112,12
v_mul_f32_e32 v88,v232,v88
v_mul_f32_e32 v89,v232,v89
v_mul_f32_e32 v90,v232,v90
mvav 212,212,24
v_mul_f32_e32 v91,v232,v91
v_mul_f32_e32 v92,v232,v92
v_mul_f32_e32 v93,v232,v93
v_mul_f32_e32 v94,v232,v94
v_mul_f32_e32 v95,v232,v95
mvav 212,244,28
buffer_store_dwordx4 v[176:179],v244,s[8:11],s88 offen offset:0 sc0 sc1
v_fmac_f32_e32 v88,v232,v184
v_fmac_f32_e32 v89,v232,v185
v_fmac_f32_e32 v90,v232,v186
v_fmac_f32_e32 v91,v232,v187
cv 184,88,89
mvav 212,84,8
v_fmac_f32_e32 v92,v232,v188
cv 185,90,91
mvav 212,116,12
v_fmac_f32_e32 v93,v232,v189
v_fmac_f32_e32 v94,v232,v190
v_fmac_f32_e32 v95,v232,v191
cv 186,92,93
cv 187,94,95
mvav 216,216,24
la 40,252,50,3072
v_add_f32_e32 v156,v156,v157
v_add_f32_e32 v141,v156,v239
v_subrev_u32_e32 v237,0x23800000,v141
v_rcp_f32_e32 v141,v141
mvav 216,248,28
v_mul_f32_e32 v233,v241,v141
v_mul_f32_e32 v96,v233,v96
mvav 216,88,8
v_mul_f32_e32 v97,v233,v97
v_mul_f32_e32 v98,v233,v98
v_mul_f32_e32 v99,v233,v99
v_mul_f32_e32 v100,v233,v100
mvav 216,120,12
buffer_store_dwordx4 v[184:187],v244,s[8:11],s88 offen offset:2048 sc0 sc1
v_fmac_f32_e32 v96,v233,v192
v_fmac_f32_e32 v97,v233,v193
v_mul_f32_e32 v101,v233,v101
v_mul_f32_e32 v102,v233,v102
v_mul_f32_e32 v103,v233,v103
mvav 220,220,24
v_fmac_f32_e32 v98,v233,v194
v_fmac_f32_e32 v99,v233,v195
mvav 220,252,28
v_fmac_f32_e32 v100,v233,v196
v_fmac_f32_e32 v101,v233,v197
cv 192,96,97
cv 193,98,99
mvav 220,92,8
la 32,252,50,1024
v_fmac_f32_e32 v102,v233,v198
v_fmac_f32_e32 v103,v233,v199
cv 194,100,101
cv 195,102,103
mvav 220,124,12
v_xor_b32_e32 v225,32,v224
v_xor_b32_e32 v226,64,v224
s_waitcnt lgkmcnt(0)
zvva 0,128,128
tv 138,225,34816
tv 142,225,38912
tv 152,225,40960
s_waitcnt vmcnt(36)
mvva 0,132,132
tv 136,225,32768
tv 154,225,43008
mvva 0,144,136
tv 140,225,36864
tv 158,225,47104
mvva 0,148,140
tv 156,225,45056
v_mul_f32_e32 v104,v233,v104
v_mul_f32_e32 v105,v233,v105
v_mul_f32_e32 v106,v233,v106
zvva 4,148,156
buffer_store_dwordx4 v[192:195],v244,s[8:11],s14 offen offset:1024 sc0 sc1
v_mul_f32_e32 v107,v233,v107
v_mul_f32_e32 v108,v233,v108
v_mul_f32_e32 v109,v233,v109
v_mul_f32_e32 v110,v233,v110
mvva 4,144,152
v_fmac_f32_e32 v104,v233,v200
v_fmac_f32_e32 v105,v233,v201
v_fmac_f32_e32 v106,v233,v202
v_mul_f32_e32 v111,v233,v111
mvva 4,132,148
v_fmac_f32_e32 v107,v233,v203
v_fmac_f32_e32 v108,v233,v204
v_fmac_f32_e32 v109,v233,v205
cv 200,104,105
cv 201,106,107
mvva 4,128,144
v_fmac_f32_e32 v110,v233,v206
v_fmac_f32_e32 v111,v233,v207
cv 202,108,109
cv 203,110,111
s_waitcnt vmcnt(16)
zvva 8,128,160
v_cmp_lt_u32_e64 s[42:43],s48,v236
v_mul_f32_e32 v112,v233,v112
s_or_b64 s[40:41],s[40:41],s[42:43]
v_cmp_lt_u32_e64 s[42:43],s48,v237
mvva 8,132,164
s_or_b64 s[40:41],s[40:41],s[42:43]
v_mul_f32_e32 v113,v233,v113
v_mul_f32_e32 v114,v233,v114
v_mul_f32_e32 v115,v233,v115
mvva 8,144,168
dl 245,36,51,0
v_mul_f32_e32 v116,v233,v116
v_mul_f32_e32 v117,v233,v117
v_mul_f32_e32 v118,v233,v118
v_mul_f32_e32 v119,v233,v119
v_mul_f32_e32 v120,v233,v120
mvva 8,148,172
v_fmac_f32_e32 v112,v233,v208
v_fmac_f32_e32 v113,v233,v209
v_fmac_f32_e32 v114,v233,v210
v_fmac_f32_e32 v115,v233,v211
zvva 12,148,188
v_fmac_f32_e32 v116,v233,v212
v_fmac_f32_e32 v117,v233,v213
cv 208,112,113
cv 209,114,115
mvva 12,144,184
v_fmac_f32_e32 v118,v233,v214
v_fmac_f32_e32 v119,v233,v215
cv 210,116,117
cv 211,118,119
mvva 12,132,180
v_mul_f32_e32 v121,v233,v121
v_mul_f32_e32 v122,v233,v122
v_mul_f32_e32 v123,v233,v123
mvva 12,128,176
v_mul_f32_e32 v124,v233,v124
v_mul_f32_e32 v125,v233,v125
v_mul_f32_e32 v126,v233,v126
v_mul_f32_e32 v127,v233,v127
s_waitcnt lgkmcnt(0)
zvva 28,136,176
buffer_store_dwordx4 v[200:203],v244,s[8:11],s14 offen offset:3072 sc0 sc1
tv 144,226,40960
v_fmac_f32_e32 v120,v233,v216
v_fmac_f32_e32 v121,v233,v217
v_fmac_f32_e32 v122,v233,v218
v_fmac_f32_e32 v123,v233,v219
mvva 28,140,180
tv 128,226,32768
tv 130,226,34816
cv 216,120,121
cv 217,122,123
mvva 28,152,184
tv 132,226,36864
tv 134,226,38912
v_fmac_f32_e32 v124,v233,v220
v_fmac_f32_e32 v125,v233,v221
v_fmac_f32_e32 v126,v233,v222
mvva 28,156,188
tv 146,226,43008
v_fmac_f32_e32 v127,v233,v223
cv 218,124,125
cv 219,126,127
zvva 24,156,172
tv 148,226,45056
tv 150,226,47104
mvva 24,152,168
v_xor_b32_e32 v227,96,v224
v_add_u32_e32 v232,131072,v224
mvva 24,140,164
buffer_store_dwordx4 v[208:211],v244,s[8:11],s88 offen offset:1024 sc0 sc1
mvva 24,136,160
v_add_u32_e32 v233,131072,v225
v_add_u32_e32 v234,131072,v226
zvva 20,136,144
v_add_u32_e32 v235,131072,v227
v_mov_b32_e32 v242,83914752
mvva 20,140,148
v_add_u32_e32 v228,65536,v224
v_add_u32_e32 v229,65536,v225
mvva 20,152,152
v_add_u32_e32 v230,65536,v226
v_add_u32_e32 v231,65536,v227
mvva 20,156,156
v_and_b32_e32 v243,240,v244
v_lshlrev_b32_e32 v243,7,v243
zvva 16,156,140
buffer_store_dwordx4 v[216:219],v244,s[8:11],s88 offen offset:3072 sc0 sc1
s_cmp_lg_u64 s[40:41],0
s_cbranch_scc1 L_redo
L_redo_r4:
mvva 16,152,136
tv 156,227,45056
v_mov_b32_e32 v236,16256
mvva 16,140,132
tv 158,227,47104
v_and_b32_e32 v248,256,v244
mvva 16,136,128
tv 152,227,40960
v_lshrrev_b32_e32 v248,3,v248
s_waitcnt lgkmcnt(3)
zvva 32,128,128
tv 140,227,36864
v_add_u32_e32 v243,v243,v248
mvva 32,132,132
tv 136,227,32768
tv 138,227,34816
mvva 32,144,136
dl 246,36,51,1024
tv 142,227,38912
mvva 32,148,140
tv 154,227,43008
v_and_b32_e32 v248,512,v244
zvva 36,148,156
v_lshrrev_b32_e32 v248,5,v248
v_add_u32_e32 v243,v243,v248
mvva 36,144,152
v_and_b32_e32 v248,16,v244
v_lshlrev_b32_e32 v248,v248,v236
mvva 36,132,148
v_cndmask_b32_e64 v238,0,v248,s[74:75]
s_mov_b32 s74,0xc0000c
mvva 36,128,144
s_mov_b32 s75,0xc0000c00
v_mov_b32_e32 v240,0
v_cndmask_b32_e64 v239,0,v248,s[74:75]
zvva 40,128,160
dl 249,36,51,2048
mvva 40,132,164
v_mov_b32_e32 v241,0
v_mov_b32_e32 v236,0
mvva 40,144,168
v_mov_b32_e32 v237,0
mvva 40,148,172
zvva 44,148,188
mvva 44,144,184
mvva 44,132,180
dl 247,36,51,3072
s_add_u32 m0,s52,16384
s_xor_b32 s51,s65,0
mvva 44,128,176
tv 144,224,57344
s_waitcnt lgkmcnt(1)
zvva 60,136,176
tv 132,224,53248
tv 134,224,55296
mvva 60,140,180
tv 128,224,49152
tv 130,224,51200
mvva 60,152,184
mvva 60,156,188
tv 146,224,59392
zvva 56,156,172
dl 245,36,51,0
tv 148,224,61440
tv 150,224,63488
mvva 56,152,168
mvva 56,140,164
mvva 56,136,160
zvva 52,136,144
mvva 52,140,148
mvva 52,152,152
dl 246,36,51,1024
mvva 52,156,156
zvva 48,156,140
mvva 48,152,136
mvva 48,140,132
tv 156,225,61440
mvva 48,136,128
tv 152,225,57344
s_waitcnt lgkmcnt(2)
zvva 64,128,128
dl 249,36,51,2048
tv 140,225,53248
mvva 64,132,132
tv 136,225,49152
tv 138,225,51200
mvva 64,144,136
tv 142,225,55296
mvva 64,148,140
tv 154,225,59392
zvva 68,148,156
tv 158,225,63488
mvva 68,144,152
mvva 68,132,148
dl 247,36,51,3072
s_lshl_b32 s51,s33,5
s_add_u32 m0,s52,98304
s_xor_b32 s51,s64,s51
mvva 68,128,144
zvva 72,128,160
mvva 72,132,164
s_waitcnt vmcnt(18)
s_mov_b32 s90,0
mvva 72,144,168
s_xor_b32 s50,s67,0
mvva 72,148,172
zvva 76,148,188
dl 245,36,51,0
mvva 76,144,184
mvva 76,132,180
tv 148,226,61440
mvva 76,128,176
tv 144,226,57344
s_waitcnt lgkmcnt(2)
zvva 92,136,176
tv 132,226,53248
mvva 92,140,180
tv 128,226,49152
tv 130,226,51200
mvva 92,152,184
dl 246,36,51,1024
tv 134,226,55296
mvva 92,156,188
tv 146,226,59392
zvva 88,156,172
tv 150,226,63488
mvva 88,152,168
mvva 88,140,164
mvva 88,136,160
zvva 84,136,144
dl 249,36,51,2048
mvva 84,140,148
mvva 84,152,152
mvva 84,156,156
zvva 80,156,140
mvva 80,152,136
mvva 80,140,132
dl 247,36,51,3072
mvva 80,136,128
s_lshl_b32 s51,s33,5
tv 152,227,57344
s_add_u32 m0,s52,114688
s_waitcnt lgkmcnt(1)
zvva 96,128,128
s_xor_b32 s51,s65,s51
tv 140,227,53248
tv 142,227,55296
mvva 96,132,132
tv 136,227,49152
tv 138,227,51200
mvva 96,144,136
mvva 96,148,140
tv 154,227,59392
zvva 100,148,156
lv 164,252,50,0
tv 156,227,61440
tv 158,227,63488
mvva 100,144,152
mvva 100,132,148
mvva 100,128,144
zvva 104,128,160
mvva 104,132,164
mvva 104,144,168
lv 172,252,50,2048
mvva 104,148,172
zvva 108,148,188
mvva 108,144,184
mvva 108,132,180
mvva 108,128,176
s_barrier
s_waitcnt lgkmcnt(0)
zvva 124,136,176
lv 180,250,50,0
tv 132,232,4096
tv 144,232,8192
tv 146,232,10240
mvva 124,140,180
tv 128,232,0
tv 130,232,2048
mvva 124,152,184
tv 134,232,6144
mvva 124,156,188
tv 148,232,12288
tv 150,232,14336
zvva 120,156,172
mvva 120,152,168
mvva 120,140,164
lv 188,250,50,2048
mvva 120,136,160
zvva 116,136,144
mvva 116,140,148
mvva 116,152,152
mvva 116,156,156
zvva 112,156,140
lv 220,250,50,3072
mvva 112,152,136
mvva 112,140,132
tv 156,233,12288
mvva 112,136,128
tv 152,233,8192
s_waitcnt lgkmcnt(2)
mvva 0,128,4
tv 140,233,4096
mvva 0,132,12
tv 136,233,0
tv 138,233,2048
mvva 0,144,20
lv 212,250,50,1024
tv 142,233,6144
mvva 0,148,28
tv 154,233,10240
mvva 4,148,60
tv 158,233,14336
mvva 4,144,52
mvva 4,132,44
mvva 4,128,36
mvva 8,128,0
lv 204,252,50,3072
mvva 8,132,8
mvva 8,144,16
mvva 8,148,24
mvva 12,148,56
s_waitcnt vmcnt(27)
mvva 12,144,48
s_waitcnt vmcnt(25)
mvva 12,132,40
lv 196,252,50,1024
s_waitcnt vmcnt(24)
mvva 12,128,32
tv 144,234,8192
s_waitcnt lgkmcnt(1)
mvva 28,136,32
tv 132,234,4096
tv 134,234,6144
mvva 28,140,40
tv 128,234,0
tv 130,234,2048
mvva 28,152,48
mvva 28,156,56
tv 146,234,10240
mvva 24,156,24
dl 245,36,51,0
s_waitcnt vmcnt(20)
s_barrier
tv 148,234,12288
tv 150,234,14336
mvva 24,152,16
mvva 24,140,8
mvva 24,136,0
mvva 20,136,36
mvva 20,140,44
mvva 20,152,52
s_cmp_eq_u32 s12,0
s_cselect_b64 exec,1,0
buffer_store_dword v251,off,s[8:11],s61 sc0 sc1
s_mov_b64 exec,-1
mvva 20,156,60
mvva 16,156,28
mvva 16,152,20
mvva 16,140,12
tv 156,235,12288
mvva 16,136,4
tv 152,235,8192
s_waitcnt lgkmcnt(2)
mvva 32,128,4
dl 246,36,51,1024
tv 140,235,4096
mvva 32,132,12
tv 136,235,0
tv 138,235,2048
mvva 32,144,20
tv 142,235,6144
mvva 32,148,28
tv 154,235,10240
mvva 36,148,60
tv 158,235,14336
mvva 36,144,52
mvva 36,132,44
dl 249,36,51,2048
mvva 36,128,36
mvva 40,128,0
mvva 40,132,8
mvva 40,144,16
mvva 40,148,24
mvva 44,148,56
dl 247,36,51,3072
s_lshl_b32 s51,s34,5
s_add_u32 m0,s52,65536
s_xor_b32 s51,s64,s51
mvva 44,144,48
tv 148,232,28672
mvva 44,132,40
s_xor_b32 s53,s60,s33
tv 150,232,30720
mvva 44,128,32
tv 144,232,24576
s_waitcnt lgkmcnt(3)
mvva 60,136,32
tv 132,232,20480
mvva 60,140,40
tv 128,232,16384
tv 130,232,18432
mvva 60,152,48
dl 245,36,51,0
tv 134,232,22528
mvva 60,156,56
tv 146,232,26624
mvva 56,156,24
mvva 56,152,16
mvva 56,140,8
mvva 56,136,0
mvva 52,136,36
buffer_load_dword v253,v242,s[8:11],s53 offen sc1
mvva 52,140,44
mvva 52,152,52
mvva 52,156,60
mvva 48,156,28
mvva 48,152,20
mvva 48,140,12
dl 246,36,51,1024
mvva 48,136,4
tv 152,233,24576
s_waitcnt lgkmcnt(1)
mvva 64,128,4
tv 140,233,20480
tv 142,233,22528
mvva 64,132,12
tv 136,233,16384
tv 138,233,18432
mvva 64,144,20
mvva 64,148,28
tv 154,233,26624
mvva 68,148,60
dl 249,36,51,2048
tv 156,233,28672
tv 158,233,30720
mvva 68,144,52
mvva 68,132,44
mvva 68,128,36
mvva 72,128,0
mvva 72,132,8
mvva 72,144,16
dl 247,36,51,3072
s_lshl_b32 s51,s34,5
s_add_u32 m0,s52,81920
s_xor_b32 s51,s65,s51
mvva 72,148,24
mvva 76,148,56
mvva 76,144,48
mvva 76,132,40
tv 148,234,28672
mvva 76,128,32
L_vs20:
s_cbranch_execz L_vs24
tv 144,234,24576
s_waitcnt lgkmcnt(2)
mvva 92,136,32
dl 245,36,51,0
tv 132,234,20480
mvva 92,140,40
tv 128,234,16384
tv 130,234,18432
mvva 92,152,48
tv 134,234,22528
mvva 92,156,56
tv 146,234,26624
mvva 88,156,24
tv 150,234,30720
mvva 88,152,16
mvva 88,140,8
dl 246,36,51,1024
mvva 88,136,0
mvva 84,136,36
mvva 84,140,44
mvva 84,152,52
mvva 84,156,60
mvva 80,156,28
dl 249,36,51,2048
mvva 80,152,20
mvva 80,140,12
tv 156,235,28672
mvva 80,136,4
tv 152,235,24576
s_waitcnt lgkmcnt(2)
mvva 96,128,4
tv 140,235,20480
mvva 96,132,12
tv 136,235,16384
tv 138,235,18432
mvva 96,144,20
dl 247,36,51,3072
s_lshl_b32 s51,s35,5
s_add_u32 m0,s52,32768
s_xor_b32 s51,s64,s51
tv 142,235,22528
mvva 96,148,28
tv 154,235,26624
mvva 100,148,60
tv 158,235,30720
mvva 100,144,52
mvva 100,132,44
mvva 100,128,36
mvva 104,128,0
dl 245,36,51,0
mvva 104,132,8
mvva 104,144,16
mvva 104,148,24
mvva 108,148,56
mvva 108,144,48
mvva 108,132,40
dl 246,36,51,1024
mvva 108,128,32
s_waitcnt vmcnt(27)
s_barrier
s_waitcnt lgkmcnt(0)
mvva 124,136,32
tv 132,224,4096
tv 134,224,6144
tv 144,224,8192
mvva 124,140,40
tv 128,224,0
tv 130,224,2048
mvva 124,152,48
mvva 124,156,56
tv 146,224,10240
mvva 120,156,24
dl 249,36,51,2048
tv 148,224,12288
tv 150,224,14336
mvva 120,152,16
mvva 120,140,8
mvva 120,136,0
mvva 116,136,36
mvva 116,140,44
mvva 116,152,52
dl 247,36,51,3072
s_lshl_b32 s51,s35,5
s_add_u32 m0,s52,49152
s_xor_b32 s51,s65,s51
mvva 116,156,60
mvva 112,156,28
mvva 112,152,20
s_waitcnt vmcnt(11)
v_cmp_ne_u32_e32 vcc,4,v253
mvva 112,140,12
dl 245,36,51,0
s_cbranch_vccnz L_spa3e
L_spra3e:
mvva 112,136,4
s_xor_b32 s50,s67,s33
tv 152,225,8192
s_waitcnt lgkmcnt(1)
mvvv 0,128,164
tv 140,225,4096
tv 142,225,6144
mvvv 0,132,172
tv 136,225,0
tv 138,225,2048
mvvv 0,144,180
mvvv 0,148,188
dl 246,36,51,1024
tv 154,225,10240
mvvv 4,148,220
tv 156,225,12288
tv 158,225,14336
mvvv 4,144,212
mvvv 4,132,204
mvvv 4,128,196
mvvv 8,128,160
la 196,252,50,0
)"
R"(s_xor_b32 s53,s60,s34
mvvv 8,132,168
mvvv 8,144,176
mvvv 8,148,184
mvvv 12,148,216
mvvv 12,144,208
la 204,252,50,2048
mvvv 12,132,200
tv 148,226,12288
mvvv 12,128,192
tv 144,226,8192
s_waitcnt lgkmcnt(2)
mvvv 28,136,192
tv 132,226,4096
mvvv 28,140,200
tv 128,226,0
tv 130,226,2048
mvvv 28,152,208
buffer_load_dword v253,v242,s[8:11],s53 offen sc1
tv 134,226,6144
mvvv 28,156,216
tv 146,226,10240
mvvv 24,156,184
tv 150,226,14336
mvvv 24,152,176
mvvv 24,140,168
mvvv 24,136,160
la 212,250,50,0
mvvv 20,136,196
mvvv 20,140,204
mvvv 20,152,212
mvvv 20,156,220
la 220,250,50,2048
mvvv 16,156,188
mvvv 16,152,180
mvvv 16,140,172
tv 156,227,12288
mvvv 16,136,164
la 252,250,50,3072
tv 152,227,8192
s_waitcnt lgkmcnt(2)
mvvv 32,128,164
tv 140,227,4096
mvvv 32,132,172
tv 136,227,0
tv 138,227,2048
mvvv 32,144,180
tv 142,227,6144
mvvv 32,148,188
la 244,250,50,1024
tv 154,227,10240
mvvv 36,148,220
tv 158,227,14336
mvvv 36,144,212
mvvv 36,132,204
mvvv 36,128,196
la 236,252,50,3072
mvvv 40,128,160
mvvv 40,132,168
mvvv 40,144,176
mvvv 40,148,184
mvvv 44,148,216
la 228,252,50,1024
s_add_u32 s50,s50,s55
mvvv 44,144,208
mvvv 44,132,200
tv 148,224,28672
tv 150,224,30720
mvvv 44,128,192
s_waitcnt lgkmcnt(2)
mvvv 60,136,192
la 192,252,50,0
tv 132,224,20480
mvvv 60,140,200
tv 128,224,16384
tv 130,224,18432
mvvv 60,152,208
tv 134,224,22528
mvvv 60,156,216
tv 144,224,24576
tv 146,224,26624
mvvv 56,156,184
la 200,252,50,2048
mvvv 56,152,176
mvvv 56,140,168
mvvv 56,136,160
mvvv 52,136,196
la 208,250,50,0
mvvv 52,140,204
mvvv 52,152,212
mvvv 52,156,220
mvvv 48,156,188
la 216,250,50,2048
mvvv 48,152,180
mvvv 48,140,172
tv 156,225,28672
mvvv 48,136,164
tv 152,225,24576
s_waitcnt lgkmcnt(2)
mvvv 64,128,164
tv 140,225,20480
mvvv 64,132,172
la 248,250,50,3072
tv 136,225,16384
tv 138,225,18432
mvvv 64,144,180
tv 142,225,22528
mvvv 64,148,188
tv 154,225,26624
mvvv 68,148,220
tv 158,225,30720
mvvv 68,144,212
la 240,250,50,1024
mvvv 68,132,204
mvvv 68,128,196
mvvv 72,128,160
mvvv 72,132,168
la 232,252,50,3072
mvvv 72,144,176
mvvv 72,148,184
mvvv 76,148,216
mvvv 76,144,208
mvvv 76,132,200
la 224,252,50,1024
mvvv 76,128,192
tv 144,226,24576
s_waitcnt lgkmcnt(1)
mvvv 92,136,192
tv 132,226,20480
tv 134,226,22528
mvvv 92,140,200
tv 128,226,16384
tv 130,226,18432
mvvv 92,152,208
dl 249,36,51,2048
mvvv 92,156,216
tv 146,226,26624
mvvv 88,156,184
tv 148,226,28672
tv 150,226,30720
mvvv 88,152,176
mvvv 88,140,168
dl 247,36,51,3072
s_lshl_b32 s51,s98,5
s_xor_b32 s51,s64,s51
s_add_u32 m0,s52,131072
s_xor_b32 s51,s51,4194304
mvvv 88,136,160
s_waitcnt vmcnt(16)
v_cmp_ne_u32_e32 vcc,4,v253
mvvv 84,136,196
s_cbranch_vccnz L_spa4e
L_spra4e:
mvvv 84,140,204
s_xor_b32 s50,s67,s34
mvvv 84,152,212
dl 245,36,51,0
mvvv 84,156,220
mvvv 80,156,188
mvvv 80,152,180
mvvv 80,140,172
la 68,252,50,0
mvvv 80,136,164
tv 152,227,24576
s_waitcnt lgkmcnt(1)
mvvv 96,128,164
tv 140,227,20480
tv 142,227,22528
mvvv 96,132,172
tv 136,227,16384
tv 138,227,18432
mvvv 96,144,180
s_xor_b32 s53,s60,s35
mvvv 96,148,188
la 76,252,50,2048
tv 154,227,26624
mvvv 100,148,220
tv 156,227,28672
tv 158,227,30720
mvvv 100,144,212
mvvv 100,132,204
mvvv 100,128,196
la 84,250,50,0
mvvv 104,128,160
mvvv 104,132,168
mvvv 104,144,176
mvvv 104,148,184
buffer_load_dword v253,v242,s[8:11],s53 offen sc1
mvvv 108,148,216
mvvv 108,144,208
mvvv 108,132,200
mvvv 108,128,192
s_barrier
s_waitcnt lgkmcnt(0)
mvvv 124,136,192
la 92,250,50,2048
tv 132,228,36864
tv 144,228,40960
tv 146,228,43008
mvvv 124,140,200
tv 128,228,32768
tv 130,228,34816
mvvv 124,152,208
tv 134,228,38912
mvvv 124,156,216
tv 148,228,45056
tv 150,228,47104
mvvv 120,156,184
la 124,250,50,3072
mvvv 120,152,176
mvvv 120,140,168
mvvv 120,136,160
mvvv 116,136,196
la 116,250,50,1024
mvvv 116,140,204
mvvv 116,152,212
mvvv 116,156,220
mvvv 112,156,188
la 108,252,50,3072
mvvv 112,152,180
mvvv 112,140,172
tv 156,229,45056
tv 158,229,47104
mvvv 112,136,164
s_waitcnt lgkmcnt(2)
mvva 0,128,196
la 100,252,50,1024
s_add_u32 s50,s50,s55
tv 140,229,36864
mvva 0,132,204
tv 136,229,32768
tv 138,229,34816
s_waitcnt vmcnt(21)
mvva 0,144,212
tv 142,229,38912
mvva 0,148,220
tv 152,229,40960
tv 154,229,43008
mvva 4,148,252
mvva 4,144,244
la 64,252,50,0
mvva 4,132,236
s_waitcnt vmcnt(21)
mvva 4,128,228
s_waitcnt vmcnt(20)
mvva 8,128,192
s_waitcnt vmcnt(19)
mvva 8,132,200
la 72,252,50,2048
s_waitcnt vmcnt(19)
mvva 8,144,208
s_waitcnt vmcnt(18)
mvva 8,148,216
s_waitcnt vmcnt(17)
mvva 12,148,248
s_waitcnt vmcnt(16)
mvva 12,144,240
la 80,250,50,0
s_waitcnt vmcnt(16)
mvva 12,132,232
tv 148,230,45056
s_waitcnt vmcnt(15)
mvva 12,128,224
tv 144,230,40960
s_waitcnt lgkmcnt(2)
mvva 28,136,224
tv 132,230,36864
mvva 28,140,232
la 88,250,50,2048
tv 128,230,32768
tv 130,230,34816
mvva 28,152,240
tv 134,230,38912
mvva 28,156,248
tv 146,230,43008
mvva 24,156,216
tv 150,230,47104
mvva 24,152,208
la 120,250,50,3072
mvva 24,140,200
mvva 24,136,192
mvva 20,136,228
mvva 20,140,236
mvva 20,152,244
la 112,250,50,1024
mvva 20,156,252
mvva 16,156,220
mvva 16,152,212
mvva 16,140,204
la 104,252,50,3072
mvva 16,136,196
tv 152,231,40960
s_waitcnt lgkmcnt(1)
mvva 32,128,196
tv 140,231,36864
tv 142,231,38912
mvva 32,132,204
tv 136,231,32768
tv 138,231,34816
mvva 32,144,212
la 96,252,50,1024
mvva 32,148,220
tv 154,231,43008
mvva 36,148,252
tv 156,231,45056
tv 158,231,47104
mvva 36,144,244
mvva 36,132,236
mvva 36,128,228
dl 246,36,51,1024
mvva 40,128,192
mvva 40,132,200
mvva 40,144,208
mvva 40,148,216
dl 249,36,51,2048
mvva 44,148,248
mvva 44,144,240
mvva 44,132,232
tv 148,228,61440
mvva 44,128,224
dl 247,36,51,3072
s_lshl_b32 s51,s98,5
s_xor_b32 s51,s65,s51
s_add_u32 m0,s52,147456
s_xor_b32 s51,s51,4194304
tv 144,228,57344
s_waitcnt lgkmcnt(2)
mvva 60,136,224
tv 132,228,53248
mvva 60,140,232
tv 128,228,49152
tv 130,228,51200
s_waitcnt vmcnt(16)
v_cmp_ne_u32_e32 vcc,4,v253
mvva 60,152,240
s_cbranch_vccnz L_spa5e
L_spra5e:
tv 134,228,55296
s_xor_b32 s50,s67,s35
mvva 60,156,248
dl 245,36,51,0
tv 146,228,59392
mvva 56,156,216
tv 150,228,63488
mvva 56,152,208
mvva 56,140,200
mvva 56,136,192
la 132,252,50,0
mvva 52,136,228
mvva 52,140,236
mvva 52,152,244
mvva 52,156,252
mvva 48,156,220
la 140,252,50,2048
s_xor_b32 s53,s60,s98
s_xor_b32 s53,s53,512
mvva 48,152,212
tv 156,229,61440
mvva 48,140,204
tv 158,229,63488
mvva 48,136,196
tv 152,229,57344
s_waitcnt lgkmcnt(3)
mvva 64,128,196
la 148,250,50,0
tv 140,229,53248
mvva 64,132,204
tv 136,229,49152
tv 138,229,51200
mvva 64,144,212
tv 142,229,55296
mvva 64,148,220
tv 154,229,59392
mvva 68,148,252
buffer_load_dword v253,v242,s[8:11],s53 offen sc1
mvva 68,144,244
mvva 68,132,236
mvva 68,128,228
mvva 72,128,192
mvva 72,132,200
la 156,250,50,2048
mvva 72,144,208
mvva 72,148,216
mvva 76,148,248
mvva 76,144,240
la 188,250,50,3072
mvva 76,132,232
tv 148,230,61440
mvva 76,128,224
tv 144,230,57344
s_waitcnt lgkmcnt(2)
mvva 92,136,224
tv 132,230,53248
mvva 92,140,232
la 180,250,50,1024
tv 128,230,49152
tv 130,230,51200
mvva 92,152,240
tv 134,230,55296
mvva 92,156,248
tv 146,230,59392
mvva 88,156,216
tv 150,230,63488
mvva 88,152,208
la 172,252,50,3072
mvva 88,140,200
mvva 88,136,192
mvva 84,136,228
mvva 84,140,236
la 164,252,50,1024
s_add_u32 s50,s50,s55
mvva 84,152,244
mvva 84,156,252
mvva 80,156,220
mvva 80,152,212
mvva 80,140,204
la 128,252,50,0
mvva 80,136,196
tv 152,231,57344
s_waitcnt lgkmcnt(1)
mvva 96,128,196
tv 140,231,53248
tv 142,231,55296
mvva 96,132,204
tv 136,231,49152
tv 138,231,51200
mvva 96,144,212
L_vs21:
s_cbranch_execz L_vs17
la 136,252,50,2048
mvva 96,148,220
tv 154,231,59392
mvva 100,148,252
tv 156,231,61440
tv 158,231,63488
mvva 100,144,244
mvva 100,132,236
la 144,250,50,0
mvva 100,128,228
mvva 104,128,192
mvva 104,132,200
mvva 104,144,208
la 152,250,50,2048
mvva 104,148,216
mvva 108,148,248
mvva 108,144,240
mvva 108,132,232
la 184,250,50,3072
mvva 108,128,224
s_barrier
s_waitcnt lgkmcnt(0)
mvva 124,136,224
tv 132,228,4096
tv 134,228,6144
tv 144,228,8192
mvva 124,140,232
tv 128,228,0
tv 130,228,2048
mvva 124,152,240
mvva 124,156,248
la 176,250,50,1024
tv 146,228,10240
mvva 120,156,216
tv 148,228,12288
tv 150,228,14336
mvva 120,152,208
mvva 120,140,200
mvva 120,136,192
la 168,252,50,3072
mvva 116,136,228
mvva 116,140,236
mvva 116,152,244
mvva 116,156,252
la 160,252,50,1024
mvva 112,156,220
mvva 112,152,212
mvva 112,140,204
tv 156,229,12288
tv 158,229,14336
mvva 112,136,196
s_waitcnt lgkmcnt(2)
mvva 0,128,68
dl 246,36,51,1024
tv 140,229,4096
mvva 0,132,76
tv 136,229,0
tv 138,229,2048
mvva 0,144,84
tv 142,229,6144
s_waitcnt vmcnt(22)
mvva 0,148,92
tv 152,229,8192
tv 154,229,10240
mvva 4,148,124
dl 249,36,51,2048
mvva 4,144,116
mvva 4,132,108
mvva 4,128,100
mvva 8,128,64
dl 247,36,51,3072
s_lshl_b32 s51,s98,5
s_xor_b32 s51,s64,s51
s_add_u32 m0,s52,0
s_xor_b32 s51,s51,4456448
mvva 8,132,72
mvva 8,144,80
mvva 8,148,88
s_waitcnt vmcnt(16)
v_cmp_ne_u32_e32 vcc,4,v253
mvva 12,148,120
dl 245,36,51,0
s_cbranch_vccnz L_spa6e
L_spra6e:
mvva 12,144,112
s_xor_b32 s50,s67,s98
mvva 12,132,104
s_xor_b32 s50,s50,131072
tv 148,230,12288
tv 150,230,14336
mvva 12,128,96
s_waitcnt lgkmcnt(2)
mvva 28,136,96
dl 246,36,51,1024
tv 132,230,4096
mvva 28,140,104
tv 128,230,0
tv 130,230,2048
mvva 28,152,112
tv 134,230,6144
mvva 28,156,120
tv 144,230,8192
tv 146,230,10240
mvva 24,156,88
mvva 24,152,80
dl 249,36,51,2048
mvva 24,140,72
s_xor_b32 s53,s60,s98
mvva 24,136,64
s_xor_b32 s53,s53,8704
mvva 20,136,100
mvva 20,140,108
dl 247,36,51,3072
mvva 20,152,116
s_lshl_b32 s51,s98,5
s_add_u32 m0,s52,16384
s_xor_b32 s51,s65,s51
mvva 20,156,124
s_xor_b32 s51,s51,4456448
mvva 16,156,92
mvva 16,152,84
buffer_load_dword v253,v242,s[8:11],s53 offen sc1
mvva 16,140,76
tv 156,231,12288
mvva 16,136,68
tv 152,231,8192
s_waitcnt lgkmcnt(2)
mvva 32,128,68
tv 140,231,4096
mvva 32,132,76
la 4,252,50,0
tv 136,231,0
tv 138,231,2048
mvva 32,144,84
tv 142,231,6144
mvva 32,148,92
tv 154,231,10240
mvva 36,148,124
tv 158,231,14336
mvva 36,144,116
la 12,252,50,2048
mvva 36,132,108
mvva 36,128,100
mvva 40,128,64
mvva 40,132,72
mvva 40,144,80
la 20,250,50,0
mvva 40,148,88
mvva 44,148,120
mvva 44,144,112
mvva 44,132,104
la 28,250,50,2048
mvva 44,128,96
tv 144,228,24576
s_waitcnt lgkmcnt(1)
mvva 60,136,96
tv 132,228,20480
tv 134,228,22528
mvva 60,140,104
tv 128,228,16384
tv 130,228,18432
mvva 60,152,112
la 60,250,50,3072
mvva 60,156,120
tv 146,228,26624
mvva 56,156,88
tv 148,228,28672
tv 150,228,30720
mvva 56,152,80
mvva 56,140,72
mvva 56,136,64
la 52,250,50,1024
mvva 52,136,100
mvva 52,140,108
mvva 52,152,116
mvva 52,156,124
la 44,252,50,3072
mvva 48,156,92
mvva 48,152,84
mvva 48,140,76
tv 156,229,28672
mvva 48,136,68
la 36,252,50,1024
s_add_u32 s50,s50,s55
tv 152,229,24576
s_waitcnt lgkmcnt(2)
mvva 64,128,68
tv 140,229,20480
mvva 64,132,76
tv 136,229,16384
tv 138,229,18432
mvva 64,144,84
tv 142,229,22528
mvva 64,148,92
la 0,252,50,0
tv 154,229,26624
mvva 68,148,124
tv 158,229,30720
mvva 68,144,116
mvva 68,132,108
mvva 68,128,100
la 8,252,50,2048
mvva 72,128,64
mvva 72,132,72
mvva 72,144,80
mvva 72,148,88
mvva 76,148,120
la 16,250,50,0
mvva 76,144,112
mvva 76,132,104
tv 148,230,28672
tv 150,230,30720
mvva 76,128,96
s_waitcnt lgkmcnt(2)
mvva 92,136,96
la 24,250,50,2048
tv 132,230,20480
mvva 92,140,104
tv 128,230,16384
tv 130,230,18432
mvva 92,152,112
tv 134,230,22528
mvva 92,156,120
tv 144,230,24576
tv 146,230,26624
mvva 88,156,88
la 56,250,50,3072
mvva 88,152,80
mvva 88,140,72
mvva 88,136,64
mvva 84,136,100
mvva 84,140,108
la 48,250,50,1024
mvva 84,152,116
mvva 84,156,124
mvva 80,156,92
mvva 80,152,84
la 40,252,50,3072
mvva 80,140,76
tv 156,231,28672
mvva 80,136,68
tv 152,231,24576
s_waitcnt lgkmcnt(2)
mvva 96,128,68
tv 140,231,20480
mvva 96,132,76
la 32,252,50,1024
tv 136,231,16384
tv 138,231,18432
mvva 96,144,84
tv 142,231,22528
mvva 96,148,92
tv 154,231,26624
mvva 100,148,124
tv 158,231,30720
mvva 100,144,116
dl 245,36,51,0
s_waitcnt vmcnt(17)
v_cmp_ne_u32_e32 vcc,4,v253
mvva 100,132,108
s_cbranch_vccnz L_spa7e
L_spra7e:
mvva 100,128,100
s_xor_b32 s50,s67,s98
mvva 104,128,64
s_xor_b32 s50,s50,139264
mvva 104,132,72
dl 246,36,51,1024
mvva 104,144,80
mvva 104,148,88
mvva 108,148,120
mvva 108,144,112
mvva 108,132,104
lv 164,252,50,0
mvva 108,128,96
s_barrier
s_waitcnt lgkmcnt(0)
mvva 124,136,96
s_xor_b32 s53,s60,s98
tv 132,224,36864
tv 134,224,38912
tv 144,224,40960
s_xor_b32 s53,s53,16896
mvva 124,140,104
tv 128,224,32768
tv 130,224,34816
mvva 124,152,112
lv 172,252,50,2048
mvva 124,156,120
tv 146,224,43008
mvva 120,156,88
tv 148,224,45056
tv 150,224,47104
mvva 120,152,80
mvva 120,140,72
buffer_load_dword v253,v242,s[8:11],s53 offen sc1
mvva 120,136,64
mvva 116,136,100
mvva 116,140,108
mvva 116,152,116
lv 180,250,50,0
mvva 116,156,124
mvva 112,156,92
mvva 112,152,84
mvva 112,140,76
lv 188,250,50,2048
mvva 112,136,68
tv 152,225,40960
s_waitcnt lgkmcnt(1)
mvva 0,128,132
tv 140,225,36864
tv 142,225,38912
mvva 0,132,140
tv 136,225,32768
tv 138,225,34816
mvva 0,144,148
mvva 0,148,156
lv 220,250,50,3072
tv 154,225,43008
mvva 4,148,188
tv 156,225,45056
tv 158,225,47104
mvva 4,144,180
mvva 4,132,172
mvva 4,128,164
lv 212,250,50,1024
mvva 8,128,128
mvva 8,132,136
mvva 8,144,144
mvva 8,148,152
lv 204,252,50,3072
mvva 12,148,184
mvva 12,144,176
mvva 12,132,168
tv 148,226,45056
tv 150,226,47104
mvva 12,128,160
s_waitcnt lgkmcnt(2)
mvva 28,136,160
lv 196,252,50,1024
s_add_u32 s50,s50,s55
tv 132,226,36864
mvva 28,140,168
tv 128,226,32768
tv 130,226,34816
mvva 28,152,176
tv 134,226,38912
mvva 28,156,184
tv 144,226,40960
tv 146,226,43008
mvva 24,156,152
lv 160,252,50,0
mvva 24,152,144
mvva 24,140,136
mvva 24,136,128
mvva 20,136,164
lv 168,252,50,2048
mvva 20,140,172
mvva 20,152,180
mvva 20,156,188
mvva 16,156,156
lv 176,250,50,0
mvva 16,152,148
mvva 16,140,140
tv 156,227,45056
tv 158,227,47104
mvva 16,136,132
s_waitcnt lgkmcnt(2)
mvva 32,128,132
lv 184,250,50,2048
tv 140,227,36864
mvva 32,132,140
tv 136,227,32768
tv 138,227,34816
mvva 32,144,148
tv 142,227,38912
mvva 32,148,156
tv 152,227,40960
tv 154,227,43008
mvva 36,148,188
mvva 36,144,180
lv 216,250,50,3072
mvva 36,132,172
mvva 36,128,164
mvva 40,128,128
mvva 40,132,136
lv 208,250,50,1024
mvva 40,144,144
mvva 40,148,152
mvva 44,148,184
mvva 44,144,176
lv 200,252,50,3072
mvva 44,132,168
tv 148,224,61440
mvva 44,128,160
tv 144,224,57344
s_waitcnt lgkmcnt(2)
mvva 60,136,160
tv 132,224,53248
mvva 60,140,168
tv 128,224,49152
tv 130,224,51200
mvva 60,152,176
lv 192,252,50,1024
tv 134,224,55296
mvva 60,156,184
tv 146,224,59392
mvva 56,156,152
tv 150,224,63488
mvva 56,152,144
mvva 56,140,136
dl 249,36,51,2048
mvva 56,136,128
mvva 52,136,164
mvva 52,140,172
mvva 52,152,180
dl 247,36,51,3072
s_lshl_b32 s51,s98,5
s_xor_b32 s51,s64,s51
s_add_u32 m0,s52,98304
s_xor_b32 s51,s51,4718592
mvva 52,156,188
mvva 48,156,156
mvva 48,152,148
mvva 48,140,140
dl 245,36,51,0
mvva 48,136,132
tv 152,225,57344
s_waitcnt vmcnt(17)
v_cmp_ne_u32_e32 vcc,4,v253
s_waitcnt lgkmcnt(1)
mvva 64,128,132
s_cbranch_vccnz L_spa8e
L_spra8e:
tv 140,225,53248
tv 142,225,55296
mvva 64,132,140
s_xor_b32 s50,s67,s98
tv 136,225,49152
tv 138,225,51200
mvva 64,144,148
dl 246,36,51,1024
mvva 64,148,156
s_xor_b32 s50,s50,147456
tv 154,225,59392
mvva 68,148,188
tv 156,225,61440
tv 158,225,63488
mvva 68,144,180
mvva 68,132,172
mvva 68,128,164
dl 249,36,51,2048
mvva 72,128,128
mvva 72,132,136
mvva 72,144,144
s_xor_b32 s53,s60,s98
mvva 72,148,152
dl 247,36,51,3072
s_xor_b32 s53,s53,25088
mvva 76,148,184
s_lshl_b32 s51,s98,5
s_add_u32 m0,s52,114688
s_xor_b32 s51,s65,s51
mvva 76,144,176
s_xor_b32 s51,s51,4718592
mvva 76,132,168
tv 148,226,61440
mvva 76,128,160
buffer_load_dword v253,v242,s[8:11],s53 offen sc1
tv 144,226,57344
s_waitcnt lgkmcnt(2)
mvva 92,136,160
tv 132,226,53248
mvva 92,140,168
tv 128,226,49152
tv 130,226,51200
mvva 92,152,176
tv 134,226,55296
mvva 92,156,184
la 196,252,50,0
tv 146,226,59392
mvva 88,156,152
tv 150,226,63488
mvva 88,152,144
mvva 88,140,136
mvva 88,136,128
la 204,252,50,2048
mvva 84,136,164
mvva 84,140,172
mvva 84,152,180
mvva 84,156,188
mvva 80,156,156
la 212,250,50,0
mvva 80,152,148
mvva 80,140,140
tv 156,227,61440
tv 158,227,63488
mvva 80,136,132
s_waitcnt lgkmcnt(2)
mvva 96,128,132
la 220,250,50,2048
tv 140,227,53248
mvva 96,132,140
tv 136,227,49152
tv 138,227,51200
mvva 96,144,148
tv 142,227,55296
mvva 96,148,156
tv 152,227,57344
tv 154,227,59392
mvva 100,148,188
la 252,250,50,3072
mvva 100,144,180
mvva 100,132,172
mvva 100,128,164
mvva 104,128,128
mvva 104,132,136
la 244,250,50,1024
mvva 104,144,144
mvva 104,148,152
mvva 108,148,184
mvva 108,144,176
la 236,252,50,3072
mvva 108,132,168
mvva 108,128,160
s_barrier
s_waitcnt lgkmcnt(0)
mvva 124,136,160
tv 132,232,4096
tv 144,232,8192
mvva 124,140,168
la 228,252,50,1024
s_add_u32 s50,s50,s55
tv 128,232,0
tv 130,232,2048
mvva 124,152,176
tv 134,232,6144
mvva 124,156,184
tv 146,232,10240
mvva 120,156,152
L_vs22:
s_cbranch_execz L_vr1
tv 148,232,12288
tv 150,232,14336
mvva 120,152,144
la 192,252,50,0
mvva 120,140,136
mvva 120,136,128
mvva 116,136,164
mvva 116,140,172
la 200,252,50,2048
mvva 116,152,180
mvva 116,156,188
mvva 112,156,156
mvva 112,152,148
mvva 112,140,140
la 208,250,50,0
mvva 112,136,132
tv 152,233,8192
s_waitcnt lgkmcnt(1)
mvva 0,128,4
tv 140,233,4096
tv 142,233,6144
mvva 0,132,12
tv 136,233,0
tv 138,233,2048
mvva 0,144,20
la 216,250,50,2048
mvva 0,148,28
tv 154,233,10240
mvva 4,148,60
)"
R"(tv 156,233,12288
tv 158,233,14336
mvva 4,144,52
mvva 4,132,44
la 248,250,50,3072
mvva 4,128,36
mvva 8,128,0
mvva 8,132,8
mvva 8,144,16
la 240,250,50,1024
mvva 8,148,24
mvva 12,148,56
mvva 12,144,48
mvva 12,132,40
la 232,252,50,3072
mvva 12,128,32
tv 144,234,8192
s_waitcnt lgkmcnt(1)
mvva 28,136,32
tv 132,234,4096
tv 134,234,6144
mvva 28,140,40
tv 128,234,0
tv 130,234,2048
mvva 28,152,48
mvva 28,156,56
la 224,252,50,1024
tv 146,234,10240
mvva 24,156,24
tv 148,234,12288
tv 150,234,14336
mvva 24,152,16
mvva 24,140,8
mvva 24,136,0
dl 245,36,51,0
mvva 20,136,36
mvva 20,140,44
s_waitcnt vmcnt(17)
v_cmp_ne_u32_e32 vcc,4,v253
mvva 20,152,52
s_cbranch_vccnz L_spa9e
L_spra9e:
mvva 20,156,60
dl 246,36,51,1024
mvva 16,156,28
s_xor_b32 s50,s67,s98
mvva 16,152,20
s_xor_b32 s50,s50,155648
mvva 16,140,12
tv 156,235,12288
tv 158,235,14336
mvva 16,136,4
s_waitcnt lgkmcnt(2)
mvva 32,128,4
dl 249,36,51,2048
tv 140,235,4096
mvva 32,132,12
tv 136,235,0
tv 138,235,2048
mvva 32,144,20
tv 142,235,6144
mvva 32,148,28
tv 152,235,8192
tv 154,235,10240
mvva 36,148,60
dl 247,36,51,3072
s_lshl_b32 s51,s98,5
s_xor_b32 s51,s64,s51
s_add_u32 m0,s52,65536
s_xor_b32 s51,s51,4980736
s_xor_b32 s53,s60,s98
mvva 36,144,52
s_xor_b32 s53,s53,25216
mvva 36,132,44
mvva 36,128,36
mvva 40,128,0
dl 245,36,51,0
mvva 40,132,8
mvva 40,144,16
mvva 40,148,24
mvva 44,148,56
buffer_load_dword v253,v242,s[8:11],s53 offen sc1
mvva 44,144,48
mvva 44,132,40
tv 148,232,28672
tv 150,232,30720
mvva 44,128,32
s_waitcnt lgkmcnt(2)
mvva 60,136,32
dl 246,36,51,1024
tv 132,232,20480
mvva 60,140,40
tv 128,232,16384
tv 130,232,18432
mvva 60,152,48
tv 134,232,22528
mvva 60,156,56
tv 144,232,24576
tv 146,232,26624
mvva 56,156,24
mvva 56,152,16
dl 249,36,51,2048
mvva 56,140,8
mvva 56,136,0
mvva 52,136,36
mvva 52,140,44
dl 247,36,51,3072
s_lshl_b32 s51,s98,5
s_xor_b32 s51,s64,s51
s_add_u32 m0,s52,81920
s_xor_b32 s51,s51,6029312
mvva 52,152,52
mvva 52,156,60
mvva 48,156,28
mvva 48,152,20
dl 245,36,51,0
mvva 48,140,12
tv 156,233,28672
mvva 48,136,4
tv 152,233,24576
s_waitcnt lgkmcnt(2)
mvva 64,128,4
tv 140,233,20480
mvva 64,132,12
tv 136,233,16384
tv 138,233,18432
mvva 64,144,20
dl 246,36,51,1024
tv 142,233,22528
mvva 64,148,28
tv 154,233,26624
mvva 68,148,60
tv 158,233,30720
mvva 68,144,52
mvva 68,132,44
dl 249,36,51,2048
mvva 68,128,36
mvva 72,128,0
mvva 72,132,8
mvva 72,144,16
dl 247,36,51,3072
mvva 72,148,24
s_lshl_b32 s51,s98,5
s_add_u32 m0,s52,32768
s_xor_b32 s51,s65,s51
mvva 76,148,56
s_xor_b32 s51,s51,4980736
mvva 76,144,48
mvva 76,132,40
la 68,252,50,0
mvva 76,128,32
tv 144,224,8192
s_waitcnt lgkmcnt(1)
mvva 92,136,32
tv 132,224,4096
tv 134,224,6144
mvva 92,140,40
tv 128,224,0
tv 130,224,2048
mvva 92,152,48
la 76,252,50,2048
mvva 92,156,56
tv 146,224,10240
mvva 88,156,24
tv 148,224,12288
tv 150,224,14336
mvva 88,152,16
mvva 88,140,8
mvva 88,136,0
la 84,250,50,0
mvva 84,136,36
mvva 84,140,44
mvva 84,152,52
mvva 84,156,60
la 92,250,50,2048
mvva 80,156,28
mvva 80,152,20
mvva 80,140,12
tv 156,225,12288
mvva 80,136,4
la 124,250,50,3072
tv 152,225,8192
s_waitcnt lgkmcnt(2)
mvvv 0,128,164
tv 140,225,4096
mvvv 0,132,172
tv 136,225,0
tv 138,225,2048
mvvv 0,144,180
tv 142,225,6144
mvvv 0,148,188
la 116,250,50,1024
tv 154,225,10240
mvvv 4,148,220
tv 158,225,14336
mvvv 4,144,212
mvvv 4,132,204
mvvv 4,128,196
la 108,252,50,3072
mvvv 8,128,160
mvvv 8,132,168
mvvv 8,144,176
mvvv 8,148,184
mvvv 12,148,216
la 100,252,50,1024
s_add_u32 s50,s50,s55
mvvv 12,144,208
mvvv 12,132,200
tv 148,226,12288
tv 150,226,14336
mvvv 12,128,192
s_barrier
tv 144,226,8192
s_waitcnt lgkmcnt(3)
mvvv 28,136,192
la 64,252,50,0
mvvv 28,140,200
tv 128,226,0
tv 130,226,2048
mvvv 28,152,208
tv 132,226,4096
tv 134,226,6144
mvvv 28,156,216
tv 146,226,10240
s_waitcnt vmcnt(16)
v_cmp_ne_u32_e32 vcc,4,v253
mvvv 24,156,184
la 72,252,50,2048
s_cbranch_vccnz L_spa10e
L_spra10e:
mvvv 24,152,176
v_and_b32_e32 v242,48,v244
v_lshlrev_b32_e32 v242,3,v242
mvvv 24,140,168
mvvv 24,136,160
mvvv 20,136,196
mvvv 20,140,204
la 80,250,50,0
mvvv 20,152,212
mvvv 20,156,220
mvvv 16,156,188
s_andn2_b32 s53,s61,384
mvvv 16,152,180
la 88,250,50,2048
mvvv 16,140,172
tv 156,227,12288
mvvv 16,136,164
tv 152,227,8192
s_waitcnt lgkmcnt(2)
mvvv 32,128,164
tv 140,227,4096
mvvv 32,132,172
buffer_load_dword v253,v242,s[8:11],s53 offen sc1
tv 136,227,0
tv 138,227,2048
mvvv 32,144,180
tv 142,227,6144
mvvv 32,148,188
tv 154,227,10240
mvvv 36,148,220
tv 158,227,14336
mvvv 36,144,212
la 120,250,50,3072
mvvv 36,132,204
mvvv 36,128,196
mvvv 40,128,160
mvvv 40,132,168
la 112,250,50,1024
mvvv 40,144,176
mvvv 40,148,184
mvvv 44,148,216
mvvv 44,144,208
mvvv 44,132,200
la 104,252,50,3072
mvvv 44,128,192
tv 144,224,24576
s_waitcnt lgkmcnt(1)
mvvv 60,136,192
tv 132,224,20480
tv 134,224,22528
mvvv 60,140,200
tv 128,224,16384
tv 130,224,18432
mvvv 60,152,208
la 96,252,50,1024
s_xor_b32 s50,s67,s98
s_xor_b32 s50,s50,188416
mvvv 60,156,216
tv 146,224,26624
mvvv 56,156,184
tv 148,224,28672
tv 150,224,30720
mvvv 56,152,176
mvvv 56,140,168
la 132,252,50,0
mvvv 56,136,160
mvvv 52,136,196
mvvv 52,140,204
mvvv 52,152,212
la 140,252,50,2048
mvvv 52,156,220
mvvv 48,156,188
mvvv 48,152,180
mvvv 48,140,172
la 148,250,50,0
mvvv 48,136,164
tv 152,225,24576
s_waitcnt lgkmcnt(1)
mvvv 64,128,164
tv 140,225,20480
tv 142,225,22528
mvvv 64,132,172
tv 136,225,16384
tv 138,225,18432
mvvv 64,144,180
mvvv 64,148,188
la 156,250,50,2048
tv 154,225,26624
mvvv 68,148,220
tv 156,225,28672
tv 158,225,30720
mvvv 68,144,212
mvvv 68,132,204
mvvv 68,128,196
la 188,250,50,3072
mvvv 72,128,160
mvvv 72,132,168
mvvv 72,144,176
mvvv 72,148,184
la 180,250,50,1024
mvvv 76,148,216
mvvv 76,144,208
mvvv 76,132,200
tv 148,228,45056
tv 150,228,47104
mvvv 76,128,192
s_waitcnt lgkmcnt(2)
mvvv 92,136,192
la 172,252,50,3072
tv 132,228,36864
mvvv 92,140,200
tv 128,228,32768
tv 130,228,34816
mvvv 92,152,208
tv 134,228,38912
mvvv 92,156,216
tv 144,228,40960
tv 146,228,43008
mvvv 88,156,184
la 164,252,50,1024
s_add_u32 s50,s50,s55
mvvv 88,152,176
mvvv 88,140,168
mvvv 88,136,160
mvvv 84,136,196
la 128,252,50,0
mvvv 84,140,204
mvvv 84,152,212
mvvv 84,156,220
mvvv 80,156,188
la 136,252,50,2048
mvvv 80,152,180
mvvv 80,140,172
tv 156,229,45056
tv 158,229,47104
mvvv 80,136,164
s_waitcnt lgkmcnt(2)
mvva 0,128,196
la 144,250,50,0
tv 140,229,36864
mvva 0,132,204
tv 136,229,32768
tv 138,229,34816
mvva 0,144,212
tv 142,229,38912
mvva 0,148,220
tv 152,229,40960
tv 154,229,43008
mvva 4,148,252
mvva 4,144,244
la 152,250,50,2048
mvva 4,132,236
mvva 4,128,228
mvva 8,128,192
mvva 8,132,200
la 184,250,50,3072
mvva 8,144,208
mvva 8,148,216
s_waitcnt vmcnt(17)
v_cmp_ne_u32_e32 vcc,4,v253
mvva 12,148,248
s_cbranch_vccnz L_sppe
L_sprpe:
mvva 12,144,240
la 176,250,50,1024
mvva 12,132,232
tv 148,230,45056
mvva 12,128,224
tv 144,230,40960
s_waitcnt lgkmcnt(2)
mvva 28,136,224
tv 132,230,36864
mvva 28,140,232
tv 128,230,32768
tv 130,230,34816
mvva 28,152,240
la 168,252,50,3072
tv 134,230,38912
mvva 28,156,248
tv 146,230,43008
mvva 24,156,216
tv 150,230,47104
mvva 24,152,208
mvva 24,140,200
la 160,252,50,1024
mvva 24,136,192
mvva 20,136,228
mvva 20,140,236
mvva 20,152,244
dl 245,36,51,0
mvva 20,156,252
mvva 16,156,220
mvva 16,152,212
mvva 16,140,204
dl 246,36,51,1024
mvva 16,136,196
tv 152,231,40960
s_waitcnt lgkmcnt(1)
mvva 32,128,196
tv 140,231,36864
tv 142,231,38912
mvva 32,132,204
tv 136,231,32768
tv 138,231,34816
mvva 32,144,212
dl 249,36,51,2048
mvva 32,148,220
tv 154,231,43008
mvva 36,148,252
tv 156,231,45056
tv 158,231,47104
mvva 36,144,244
mvva 36,132,236
mvva 36,128,228
dl 247,36,51,3072
s_lshl_b32 s51,s98,5
s_xor_b32 s51,s65,s51
s_add_u32 m0,s52,49152
s_xor_b32 s51,s51,6029312
mvva 40,128,192
mvva 40,132,200
mvva 40,144,208
mvva 40,148,216
dl 245,36,51,0
mvva 44,148,248
mvva 44,144,240
mvva 44,132,232
mvva 44,128,224
dl 246,36,51,1024
s_waitcnt lgkmcnt(0)
mvva 60,136,224
s_barrier
mvva 60,140,232
tv 144,228,57344
tv 146,228,59392
tv 128,228,49152
tv 130,228,51200
mvva 60,152,240
tv 132,228,53248
tv 134,228,55296
mvva 60,156,248
dl 249,36,51,2048
mvva 56,156,216
tv 148,228,61440
tv 150,228,63488
mvva 56,152,208
mvva 56,140,200
mvva 56,136,192
dl 247,36,51,3072
mvva 52,136,228
mvva 52,140,236
mvva 52,152,244
mvva 52,156,252
mvva 48,156,220
mvva 48,152,212
mvva 48,140,204
tv 156,229,61440
mvva 48,136,196
tv 152,229,57344
s_waitcnt lgkmcnt(2)
mvva 64,128,196
tv 140,229,53248
mvva 64,132,204
tv 136,229,49152
tv 138,229,51200
mvva 64,144,212
tv 142,229,55296
mvva 64,148,220
tv 154,229,59392
mvva 68,148,252
tv 158,229,63488
mvva 68,144,244
mvva 68,132,236
mvva 68,128,228
mvva 72,128,192
mvva 72,132,200
L_vs23:
s_cbranch_execz L_vr1
mvva 72,144,208
mvva 72,148,216
mvva 76,148,248
mvva 76,144,240
mvva 76,132,232
tv 148,228,12288
mvva 76,128,224
tv 144,228,8192
s_waitcnt lgkmcnt(2)
mvva 92,136,224
tv 132,228,4096
mvva 92,140,232
tv 128,228,0
tv 130,228,2048
mvva 92,152,240
tv 134,228,6144
mvva 92,156,248
tv 146,228,10240
mvva 88,156,216
tv 150,228,14336
mvva 88,152,208
mvva 88,140,200
mvva 88,136,192
mvva 84,136,228
mvva 84,140,236
mvva 84,152,244
mvva 84,156,252
mvva 80,156,220
mvva 80,152,212
mvva 80,140,204
tv 156,229,12288
mvva 80,136,196
tv 152,229,8192
s_waitcnt lgkmcnt(2)
mvva 0,128,68
tv 140,229,4096
mvva 0,132,76
tv 136,229,0
tv 138,229,2048
mvva 0,144,84
tv 142,229,6144
mvva 0,148,92
tv 154,229,10240
mvva 4,148,124
tv 158,229,14336
mvva 4,144,116
mvva 4,132,108
mvva 4,128,100
mvva 8,128,64
mvva 8,132,72
mvva 8,144,80
mvva 8,148,88
s_waitcnt vmcnt(10)
mvva 12,148,120
mvva 12,144,112
mvva 12,132,104
tv 148,228,28672
mvva 12,128,96
tv 144,228,24576
s_waitcnt lgkmcnt(2)
mvva 28,136,96
tv 132,228,20480
mvva 28,140,104
tv 128,228,16384
tv 130,228,18432
mvva 28,152,112
tv 134,228,22528
mvva 28,156,120
tv 146,228,26624
mvva 24,156,88
tv 150,228,30720
mvva 24,152,80
mvva 24,140,72
mvva 24,136,64
mvva 20,136,100
mvva 20,140,108
mvva 20,152,116
mvva 20,156,124
mvva 16,156,92
mvva 16,152,84
mvva 16,140,76
tv 156,229,28672
mvva 16,136,68
tv 152,229,24576
s_waitcnt lgkmcnt(2)
mvva 0,128,132
tv 140,229,20480
mvva 0,132,140
tv 136,229,16384
tv 138,229,18432
mvva 0,144,148
tv 142,229,22528
mvva 0,148,156
tv 154,229,26624
mvva 4,148,188
tv 158,229,30720
mvva 4,144,180
mvva 4,132,172
mvva 4,128,164
mvva 8,128,128
mvva 8,132,136
cv 0,0,1
cv 1,2,3
mvva 8,144,144
mvva 8,148,152
mvva 12,148,184
mvva 12,144,176
cv 4,4,5
cv 5,6,7
s_waitcnt vmcnt(8)
mvva 12,132,168
tv 148,230,12288
mvva 12,128,160
tv 144,230,8192
cv 8,8,9
s_waitcnt lgkmcnt(2)
mvva 28,136,160
tv 132,230,4096
cv 9,10,11
mvva 28,140,168
tv 128,230,0
tv 130,230,2048
mvva 28,152,176
tv 134,230,6144
cv 12,12,13
mvva 28,156,184
tv 146,230,10240
cv 13,14,15
mvva 24,156,152
tv 150,230,14336
mvva 24,152,144
mvva 24,140,136
mvva 24,136,128
mvva 20,136,164
cv 14,28,29
cv 15,30,31
mvva 20,140,172
buffer_store_dwordx4 v[12:15],v244,s[8:11],s56 offen offset:3072 sc0 sc1
mvva 20,152,180
mvva 20,156,188
mvva 16,156,156
cv 10,24,25
cv 11,26,27
mvva 16,152,148
buffer_store_dwordx4 v[8:11],v244,s[8:11],s56 offen offset:2048 sc0 sc1
mvva 16,140,140
tv 156,231,12288
mvva 16,136,132
cv 6,20,21
cv 7,22,23
s_waitcnt lgkmcnt(1)
mvva 32,128,68
tv 140,231,4096
mvva 32,132,76
tv 136,231,0
tv 138,231,2048
mvva 32,144,84
buffer_store_dwordx4 v[4:7],v244,s[8:11],s56 offen offset:1024 sc0 sc1
tv 142,231,6144
mvva 32,148,92
tv 152,231,8192
tv 154,231,10240
mvva 36,148,124
tv 158,231,14336
cv 2,16,17
cv 3,18,19
mvva 36,144,116
mvva 36,132,108
buffer_store_dwordx4 v[0:3],v244,s[8:11],s56 offen offset:0 sc0 sc1
mvva 36,128,100
mvva 40,128,64
mvva 40,132,72
mvva 40,144,80
mvva 40,148,88
mvva 44,148,120
mvva 44,144,112
mvva 44,132,104
tv 148,230,28672
mvva 44,128,96
s_waitcnt vmcnt(4)
s_barrier
tv 144,230,24576
s_waitcnt lgkmcnt(2)
mvva 60,136,96
tv 132,230,20480
mvva 60,140,104
tv 128,230,16384
tv 130,230,18432
mvva 60,152,112
tv 134,230,22528
mvva 60,156,120
tv 146,230,26624
mvva 56,156,88
tv 150,230,30720
mvva 56,152,80
mvva 56,140,72
mvva 56,136,64
mvva 52,136,100
mvva 52,140,108
mvva 52,152,116
mvva 52,156,124
mvva 48,156,92
mvva 48,152,84
mvva 48,140,76
tv 156,231,28672
mvva 48,136,68
tv 152,231,24576
s_waitcnt lgkmcnt(2)
mvva 32,128,132
tv 140,231,20480
mvva 32,132,140
tv 136,231,16384
tv 138,231,18432
mvva 32,144,148
tv 142,231,22528
mvva 32,148,156
tv 154,231,26624
mvva 36,148,188
tv 158,231,30720
mvva 36,144,180
mvva 36,132,172
mvva 36,128,164
mvva 40,128,128
mvva 40,132,136
cv 32,32,33
cv 33,34,35
mvva 40,144,144
mvva 40,148,152
mvva 44,148,184
mvva 44,144,176
cv 36,36,37
cv 37,38,39
mvva 44,132,168
tv 148,224,45056
mvva 44,128,160
tv 144,224,40960
cv 40,40,41
s_waitcnt lgkmcnt(2)
mvva 60,136,160
tv 132,224,36864
cv 41,42,43
mvva 60,140,168
tv 128,224,32768
tv 130,224,34816
mvva 60,152,176
tv 134,224,38912
cv 44,44,45
mvva 60,156,184
tv 146,224,43008
cv 45,46,47
mvva 56,156,152
tv 150,224,47104
mvva 56,152,144
mvva 56,140,136
mvva 56,136,128
mvva 52,136,164
cv 46,60,61
cv 47,62,63
mvva 52,140,172
buffer_store_dwordx4 v[44:47],v244,s[8:11],s57 offen offset:3072 sc0 sc1
mvva 52,152,180
mvva 52,156,188
mvva 48,156,156
cv 42,56,57
cv 43,58,59
mvva 48,152,148
buffer_store_dwordx4 v[40:43],v244,s[8:11],s57 offen offset:2048 sc0 sc1
mvva 48,140,140
tv 156,225,45056
mvva 48,136,132
cv 38,52,53
cv 39,54,55
s_waitcnt lgkmcnt(1)
mvva 64,128,68
tv 140,225,36864
mvva 64,132,76
buffer_store_dwordx4 v[36:39],v244,s[8:11],s57 offen offset:1024 sc0 sc1
tv 136,225,32768
tv 138,225,34816
mvva 64,144,84
tv 142,225,38912
cv 34,48,49
mvva 64,148,92
tv 152,225,40960
tv 154,225,43008
mvva 68,148,124
tv 158,225,47104
cv 35,50,51
mvva 68,144,116
buffer_store_dwordx4 v[32:35],v244,s[8:11],s57 offen offset:0 sc0 sc1
mvva 68,132,108
mvva 68,128,100
mvva 72,128,64
mvva 72,132,72
mvva 72,144,80
mvva 72,148,88
mvva 76,148,120
mvva 76,144,112
mvva 76,132,104
tv 148,224,61440
mvva 76,128,96
tv 144,224,57344
s_waitcnt lgkmcnt(2)
mvva 92,136,96
tv 132,224,53248
mvva 92,140,104
tv 128,224,49152
tv 130,224,51200
mvva 92,152,112
tv 134,224,55296
mvva 92,156,120
tv 146,224,59392
mvva 88,156,88
tv 150,224,63488
mvva 88,152,80
mvva 88,140,72
mvva 88,136,64
mvva 84,136,100
mvva 84,140,108
mvva 84,152,116
mvva 84,156,124
mvva 80,156,92
mvva 80,152,84
mvva 80,140,76
tv 156,225,61440
mvva 80,136,68
tv 152,225,57344
s_waitcnt lgkmcnt(2)
mvva 64,128,132
tv 140,225,53248
mvva 64,132,140
tv 136,225,49152
tv 138,225,51200
mvva 64,144,148
tv 142,225,55296
mvva 64,148,156
tv 154,225,59392
mvva 68,148,188
tv 158,225,63488
mvva 68,144,180
mvva 68,132,172
mvva 68,128,164
s_xor_b32 s82,s59,12
mvva 72,128,128
s_mul_i32 s82,s82,12288
cv 64,64,65
s_add_u32 s82,s82,0
mvva 72,132,136
lv 16,244,82,0
mvva 72,144,144
cv 65,66,67
cv 68,68,69
mvva 72,148,152
cv 69,70,71
mvva 76,148,184
mvva 76,144,176
mvva 76,132,168
lv 20,244,82,1024
mvva 76,128,160
tv 144,234,24576
s_waitcnt lgkmcnt(1)
mvva 92,136,160
tv 132,234,20480
tv 134,234,22528
mvva 92,140,168
tv 128,234,16384
tv 130,234,18432
mvva 92,152,176
cv 72,72,73
cv 73,74,75
mvva 92,156,184
lv 24,244,82,2048
tv 146,234,26624
mvva 88,156,152
tv 148,234,28672
tv 150,234,30720
mvva 88,152,144
cv 76,76,77
cv 77,78,79
mvva 88,140,136
cv 78,92,93
cv 79,94,95
mvva 88,136,128
mvva 84,136,164
lv 28,244,82,3072
mvva 84,140,172
mvva 84,152,180
mvva 84,156,188
mvva 80,156,156
buffer_store_dwordx4 v[76:79],v244,s[8:11],s58 offen offset:3072 sc0 sc1
mvva 80,152,148
cv 74,88,89
cv 75,90,91
mvva 80,140,140
tv 156,235,28672
mvva 80,136,132
cv 70,84,85
cv 71,86,87
s_waitcnt lgkmcnt(1)
mvva 96,128,4
buffer_store_dwordx4 v[72:75],v244,s[8:11],s58 offen offset:2048 sc0 sc1
tv 140,235,20480
mvva 96,132,12
tv 136,235,16384
tv 138,235,18432
mvva 96,144,20
tv 142,235,22528
cv 66,80,81
mvva 96,148,28
tv 152,235,24576
tv 154,235,26624
mvva 100,148,60
tv 158,235,30720
cv 67,82,83
mvva 100,144,52
buffer_store_dwordx4 v[68:71],v244,s[8:11],s58 offen offset:1024 sc0 sc1
mvva 100,132,44
mvva 100,128,36
mvva 104,128,0
mvva 104,132,8
buffer_store_dwordx4 v[64:67],v244,s[8:11],s58 offen offset:0 sc0 sc1
mvva 104,144,16
mvva 104,148,24
mvva 108,148,56
mvva 108,144,48
mvva 108,132,40
tv 148,226,28672
mvva 108,128,32
tv 144,226,24576
s_waitcnt lgkmcnt(2)
mvva 124,136,32
tv 132,226,20480
mvva 124,140,40
tv 128,226,16384
tv 130,226,18432
mvva 124,152,48
tv 134,226,22528
mvva 124,156,56
tv 146,226,26624
mvva 120,156,24
tv 150,226,30720
mvva 120,152,16
mvva 120,140,8
mvva 120,136,0
mvva 116,136,36
mvva 116,140,44
s_waitcnt vmcnt(7)
v_max_u32_e32 v248,v16,v17
mvva 116,152,52
v_max3_u32 v248,v248,v18,v19
s_waitcnt vmcnt(6)
v_max3_u32 v248,v248,v20,v21
v_max3_u32 v248,v248,v22,v23
mvva 116,156,60
s_waitcnt vmcnt(5)
v_max3_u32 v248,v248,v24,v25
v_max3_u32 v248,v248,v26,v27
s_waitcnt vmcnt(4)
v_max3_u32 v248,v248,v28,v29
mvva 112,156,28
v_max3_u32 v248,v248,v30,v31
v_cmp_eq_u32_e32 vcc,-1,v248
mvva 112,152,20
s_cbranch_vccnz L_spx0e
L_sprx0e:
mvva 112,140,12
tv 156,227,28672
tv 158,227,30720
v_lshlrev_b32_e32 v251,16,v18
v_and_b32_e32 v255,4294901760,v18
mvva 112,136,4
tv 152,227,24576
tv 154,227,26624
s_waitcnt lgkmcnt(4)
mvvv 96,128,164
v_lshlrev_b32_e32 v253,16,v19
v_and_b32_e32 v242,4294901760,v19
mvvv 96,132,172
tv 136,227,16384
tv 138,227,18432
mvvv 96,144,180
tv 140,227,20480
tv 142,227,22528
v_add_f32_e32 v112,v112,v251
mvvv 96,148,188
v_add_f32_e32 v113,v113,v255
v_add_f32_e32 v114,v114,v253
mvvv 100,148,220
v_add_f32_e32 v115,v115,v242
v_lshlrev_b32_e32 v251,16,v22
mvvv 100,144,212
v_and_b32_e32 v255,4294901760,v22
)"
R"(v_lshlrev_b32_e32 v253,16,v23
mvvv 100,132,204
v_and_b32_e32 v242,4294901760,v23
v_add_f32_e32 v116,v116,v251
v_add_f32_e32 v117,v117,v255
mvvv 100,128,196
v_add_f32_e32 v118,v118,v253
v_add_f32_e32 v119,v119,v242
mvvv 104,128,160
v_lshlrev_b32_e32 v251,16,v26
v_and_b32_e32 v255,4294901760,v26
mvvv 104,132,168
v_lshlrev_b32_e32 v253,16,v27
v_and_b32_e32 v242,4294901760,v27
mvvv 104,144,176
v_add_f32_e32 v120,v120,v251
v_add_f32_e32 v121,v121,v255
v_add_f32_e32 v122,v122,v253
mvvv 104,148,184
v_add_f32_e32 v123,v123,v242
v_lshlrev_b32_e32 v251,16,v30
mvvv 108,148,216
v_and_b32_e32 v255,4294901760,v30
v_lshlrev_b32_e32 v253,16,v31
mvvv 108,144,208
v_and_b32_e32 v242,4294901760,v31
v_add_f32_e32 v124,v124,v251
mvvv 108,132,200
v_add_f32_e32 v125,v125,v255
v_add_f32_e32 v126,v126,v253
v_add_f32_e32 v127,v127,v242
mvvv 108,128,192
tv 144,230,57344
v_lshlrev_b32_e32 v251,16,v16
s_waitcnt lgkmcnt(1)
mvvv 124,136,192
tv 132,230,53248
v_and_b32_e32 v255,4294901760,v16
mvvv 124,140,200
tv 128,230,49152
tv 130,230,51200
mvvv 124,152,208
tv 134,230,55296
v_lshlrev_b32_e32 v253,16,v17
mvvv 124,156,216
tv 146,230,59392
v_and_b32_e32 v242,4294901760,v17
mvvv 120,156,184
L_vs24:
s_cbranch_execz L_vr1
tv 148,230,61440
tv 150,230,63488
mvvv 120,152,176
v_add_f32_e32 v96,v96,v251
v_add_f32_e32 v97,v97,v255
mvvv 120,140,168
v_add_f32_e32 v98,v98,v253
v_add_f32_e32 v99,v99,v242
mvvv 120,136,160
v_lshlrev_b32_e32 v251,16,v20
v_and_b32_e32 v255,4294901760,v20
mvvv 116,136,196
v_lshlrev_b32_e32 v253,16,v21
v_and_b32_e32 v242,4294901760,v21
mvvv 116,140,204
v_add_f32_e32 v100,v100,v251
v_add_f32_e32 v101,v101,v255
mvvv 116,152,212
v_add_f32_e32 v102,v102,v253
v_add_f32_e32 v103,v103,v242
mvvv 116,156,220
v_lshlrev_b32_e32 v251,16,v24
v_and_b32_e32 v255,4294901760,v24
mvvv 112,156,188
v_lshlrev_b32_e32 v253,16,v25
v_and_b32_e32 v242,4294901760,v25
mvvv 112,152,180
v_add_f32_e32 v104,v104,v251
v_add_f32_e32 v105,v105,v255
mvvv 112,140,172
tv 156,231,61440
v_add_f32_e32 v106,v106,v253
mvvv 112,136,164
tv 152,231,57344
v_add_f32_e32 v107,v107,v242
s_waitcnt lgkmcnt(2)
mvva 96,128,196
tv 140,231,53248
v_lshlrev_b32_e32 v251,16,v28
mvva 96,132,204
tv 136,231,49152
tv 138,231,51200
mvva 96,144,212
tv 142,231,55296
v_and_b32_e32 v255,4294901760,v28
mvva 96,148,220
tv 154,231,59392
v_lshlrev_b32_e32 v253,16,v29
mvva 100,148,252
tv 158,231,63488
v_and_b32_e32 v242,4294901760,v29
mvva 100,144,244
v_add_f32_e32 v108,v108,v251
v_add_f32_e32 v109,v109,v255
mvva 100,132,236
v_add_f32_e32 v110,v110,v253
v_add_f32_e32 v111,v111,v242
mvva 100,128,228
mvva 104,128,192
mvva 104,132,200
mvva 104,144,208
mvva 104,148,216
mvva 108,148,248
mvva 108,144,240
mvva 108,132,232
tv 148,226,45056
mvva 108,128,224
tv 144,226,40960
s_waitcnt lgkmcnt(2)
mvva 124,136,224
s_xor_b32 s83,s59,8
tv 132,226,36864
s_mul_i32 s83,s83,12288
mvva 124,140,232
s_add_u32 s83,s83,4096
tv 128,226,32768
tv 130,226,34816
mvva 124,152,240
lv 48,244,83,0
tv 134,226,38912
mvva 124,156,248
tv 146,226,43008
mvva 120,156,216
lv 52,244,83,1024
tv 150,226,47104
mvva 120,152,208
mvva 120,140,200
lv 56,244,83,2048
mvva 120,136,192
mvva 116,136,228
lv 60,244,83,3072
mvva 116,140,236
mvva 116,152,244
s_xor_b32 s84,s59,4
mvva 116,156,252
s_mul_i32 s84,s84,12288
mvva 112,156,220
s_add_u32 s84,s84,8192
mvva 112,152,212
lv 80,244,84,0
mvva 112,140,204
tv 156,227,45056
mvva 112,136,196
lv 84,244,84,1024
tv 152,227,40960
s_waitcnt lgkmcnt(2)
mvva 96,128,68
tv 140,227,36864
mvva 96,132,76
lv 88,244,84,2048
tv 136,227,32768
tv 138,227,34816
mvva 96,144,84
tv 142,227,38912
mvva 96,148,92
lv 92,244,84,3072
tv 154,227,43008
mvva 100,148,124
tv 158,227,47104
mvva 100,144,116
mvva 100,132,108
mvva 100,128,100
mvva 104,128,64
mvva 104,132,72
mvva 104,144,80
mvva 104,148,88
mvva 108,148,120
mvva 108,144,112
mvva 108,132,104
tv 148,226,61440
mvva 108,128,96
tv 144,226,57344
s_waitcnt lgkmcnt(2)
mvva 124,136,96
tv 132,226,53248
mvva 124,140,104
tv 128,226,49152
tv 130,226,51200
mvva 124,152,112
tv 134,226,55296
mvva 124,156,120
tv 146,226,59392
mvva 120,156,88
tv 150,226,63488
mvva 120,152,80
mvva 120,140,72
mvva 120,136,64
mvva 116,136,100
mvva 116,140,108
mvva 116,152,116
mvva 116,156,124
mvva 112,156,92
mvva 112,152,84
mvva 112,140,76
tv 156,227,61440
mvva 112,136,68
tv 152,227,57344
s_waitcnt lgkmcnt(2)
mvva 96,128,132
tv 140,227,53248
mvva 96,132,140
tv 136,227,49152
tv 138,227,51200
mvva 96,144,148
tv 142,227,55296
mvva 96,148,156
tv 154,227,59392
mvva 100,148,188
tv 158,227,63488
mvva 100,144,180
mvva 100,132,172
mvva 100,128,164
mvva 104,128,128
mvva 104,132,136
mvva 104,144,144
mvva 104,148,152
s_waitcnt vmcnt(7)
v_max_u32_e32 v248,v48,v49
mvva 108,148,184
v_max3_u32 v248,v248,v50,v51
s_waitcnt vmcnt(6)
v_max3_u32 v248,v248,v52,v53
mvva 108,144,176
v_max3_u32 v248,v248,v54,v55
s_waitcnt vmcnt(5)
v_max3_u32 v248,v248,v56,v57
mvva 108,132,168
v_max3_u32 v248,v248,v58,v59
s_waitcnt vmcnt(4)
v_max3_u32 v248,v248,v60,v61
mvva 108,128,160
v_max3_u32 v248,v248,v62,v63
v_cmp_eq_u32_e32 vcc,-1,v248
s_waitcnt lgkmcnt(0)
mvva 124,136,160
s_cbranch_vccnz L_spx1e
L_sprx1e:
mvva 124,140,168
mvva 124,152,176
mvva 124,156,184
s_waitcnt vmcnt(3)
v_max_u32_e32 v248,v80,v81
mvva 120,156,152
v_max3_u32 v248,v248,v82,v83
s_waitcnt vmcnt(2)
v_max3_u32 v248,v248,v84,v85
mvva 120,152,144
v_max3_u32 v248,v248,v86,v87
s_waitcnt vmcnt(1)
v_max3_u32 v248,v248,v88,v89
mvva 120,140,136
v_max3_u32 v248,v248,v90,v91
s_waitcnt vmcnt(0)
v_max3_u32 v248,v248,v92,v93
mvva 120,136,128
v_max3_u32 v248,v248,v94,v95
v_cmp_eq_u32_e32 vcc,-1,v248
mvva 116,136,164
s_cbranch_vccnz L_spx2e
L_sprx2e:
mvva 116,140,172
mvva 116,152,180
mvva 116,156,188
mvva 112,156,156
mvva 112,152,148
mvva 112,140,140
mvva 112,136,132
mvvv 108,238,60
mvvv 108,238,92
mvvv 124,236,60
mvvv 124,236,92
mvvv 104,238,56
mvvv 104,238,88
mvvv 120,236,56
mvvv 120,236,88
mvvv 100,238,52
mvvv 100,238,84
cv 108,108,109
cv 109,110,111
cv 110,124,125
cv 111,126,127
cv 104,104,105
v_permlane16_swap_b32_e32 v108,v110
mvvv 116,236,52
v_permlane16_swap_b32_e32 v109,v111
cv 105,106,107
cv 106,120,121
cv 107,122,123
mvvv 116,236,84
buffer_store_dwordx4 v[108:111],v243,s[44:47],s73 offen sc0 sc1
v_permlane16_swap_b32_e32 v104,v106
v_permlane16_swap_b32_e32 v105,v107
mvvv 96,238,48
cv 100,100,101
cv 101,102,103
mvvv 96,238,80
buffer_store_dwordx4 v[104:107],v243,s[44:47],s72 offen sc0 sc1
cv 102,116,117
cv 103,118,119
mvvv 112,236,48
v_permlane16_swap_b32_e32 v100,v102
v_permlane16_swap_b32_e32 v101,v103
mvvv 112,236,80
buffer_store_dwordx4 v[100:103],v243,s[44:47],s71 offen sc0 sc1
cv 96,96,97
cv 97,98,99
s_nop 4
cv 98,112,113
cv 99,114,115
s_nop 0
v_permlane16_swap_b32_e32 v96,v98
v_permlane16_swap_b32_e32 v97,v99
buffer_store_dwordx4 v[96:99],v243,s[44:47],s70 offen sc0 sc1
s_endpgm
L_odd:
L_vs25:
s_cbranch_execz L_vs29
s_lshl_b32 s86,s21,2
s_sub_u32 s87,s13,s67
s_add_u32 s86,s86,s12
mvaa 64,200,132
s_lshl_b32 s86,s86,8
v_add_u32_e32 v252,s87,v244
s_add_u32 s86,s86,L_odd-L_att_pc+1024
v_lshl_add_u32 v226,v244,3,s86
s_lshl_b32 s86,s12,1
s_bfe_u32 s87,s21,65539
mvaa 64,208,136
s_add_u32 s86,s86,s87
v_min_u32_e32 v226,L_att_end-L_att_pc-4,v226
s_lshl_b32 s86,s86,7
s_add_u32 s86,s86,L_pend2-L_att_pc
v_writelane_b32 v226,s86,2
mvaa 64,216,140
s_mov_b64 exec,7
global_load_dword v253,v226,s[96:97]
s_mov_b64 exec,-1
cvaa 68,220,140,232
s_add_u32 m0,s52,32768
v_and_b32_e32 v224,16,v244
v_lshlrev_b32_e32 v224,5,v224
mvaa 68,212,136
s_xor_b32 s51,s62,0
v_lshrrev_b32_e32 v239,4,v244
v_lshrrev_b32_e32 v236,3,v239
mvaa 68,204,132
s_lshl_b32 s87,s12,13
v_and_b32_e32 v237,112,v244
v_and_b32_e32 v238,48,v239
mvaa 68,196,128
s_xor_b32 s87,s87,8192
ra 64,225,0
v_lshlrev_b32_e32 v238,1,v238
cvaa 96,224,128,232
buffer_load_dword v253,v224,s[8:11],s53 offen sc1
mvaa 96,232,132
s_mov_b32 s84,7
ra 68,225,1024
v_xor_b32_e32 v237,v237,v238
mvaa 96,240,136
s_add_u32 s81,s81,1
ra 72,225,2048
v_lshl_add_u32 v245,v236,11,v237
mvaa 96,248,140
s_mov_b32 s84,5
v_add_u32_e32 v246,15360,v245
v_add_u32_e32 v249,30720,v245
cvaa 100,252,140,232
s_andn2_b32 s69,64,s54
ra 76,225,3072
s_waitcnt vmcnt(9)
v_add_u32_e32 v247,46080,v245
mvaa 100,244,136
dl 245,24,51,0
mvaa 100,236,132
s_add_u32 s88,s14,4096
ra 80,225,4096
cv 8,8,9
mvaa 100,228,128
s_lshl_b32 s35,s19,1
ra 84,225,5120
cv 9,10,11
cvaa 72,192,144,232
s_lshr_b32 s33,40569,s35
ra 88,225,6144
cv 10,12,13
mvaa 72,200,148
s_and_b32 s33,s33,3
ra 92,225,7168
v_add_u32_e32 v250,4096,v252
mvaa 72,208,152
dl 246,24,51,1024
mvaa 72,216,156
s_lshl_b32 s33,s33,13
ra 96,225,8192
v_mov_b32_e32 v251,4
cvaa 76,220,156,232
s_lshr_b32 s34,59294,s35
ra 100,225,9216
s_and_b32 s34,s34,3
mvaa 76,212,152
s_lshl_b32 s34,s34,13
ra 104,225,10240
s_xor_b32 s35,s33,s34
mvaa 76,204,148
s_and_b32 s98,s19,3
ra 108,225,11264
s_lshl_b32 s98,s98,13
mvaa 76,196,144
dl 249,24,51,2048
cvaa 104,224,144,232
s_mov_b32 s74,0x300003
ra 112,225,12288
s_mov_b32 s75,0x30000300
mvaa 104,232,148
s_lshl_b32 s71,s12,15
ra 116,225,13312
s_add_u32 s71,s71,s54
mvaa 104,240,152
s_mov_b64 s[44:45],s[30:31]
ra 120,225,14336
s_mov_b32 s46,4194304
mvaa 104,248,156
s_mov_b32 s47,131072
ex 14
cvaa 108,252,156,232
dl 247,24,51,3072
mvaa 108,244,152
s_add_u32 m0,s52,49152
s_xor_b32 s51,s63,0
ra 124,225,15360
mvaa 108,236,148
s_lshl_b32 s70,s23,11
ex 15
mvaa 108,228,144
s_add_u32 s70,s70,s71
ex 40
cvaa 80,192,160,232
ex 41
mvaa 80,200,164
dl 245,24,51,0
mvaa 80,208,168
ex 42
mvaa 80,216,172
ex 43
cvaa 84,220,172,232
ex 44
mvaa 84,212,168
ex 45
mvaa 84,204,164
dl 246,24,51,1024
mvaa 84,196,160
ex 46
cvaa 112,224,160,232
ex 47
mvaa 112,232,164
ex 16
mvaa 112,240,168
ex 17
mvaa 112,248,172
dl 249,24,51,2048
cvaa 116,252,172,232
ex 18
mvaa 116,244,168
ex 19
mvaa 116,236,164
ex 20
mvaa 116,228,160
ex 21
cvaa 88,192,176,232
dl 247,24,51,3072
s_waitcnt vmcnt(8)
v_cmp_ne_u32_e32 vcc,4,v253
mvaa 88,200,180
s_cbranch_vccnz L_spbo
L_sprbo:
ex 22
mvaa 88,208,184
s_xor_b32 s50,s66,262144
ex 23
mvaa 88,216,188
ex 48
cvaa 92,220,188,232
ex 49
mvaa 92,212,184
la 128,252,50,0
mvaa 92,204,180
s_waitcnt lgkmcnt(0)
s_barrier
mvaa 92,196,176
s_mov_b32 m0,s72
ex 50
cvaa 120,224,176,232
s_lshl_b32 s72,s22,11
ex 51
mvaa 120,232,180
s_add_u32 s72,s72,s71
ex 52
mvaa 120,240,184
ds 255,8,76,0
mvaa 120,248,188
s_add_u32 s71,s70,131072
ex 53
cvaa 124,252,188,232
ex 54
mvaa 124,244,184
ex 55
mvaa 124,236,180
ex 24
mvaa 124,228,176
ds 255,8,76,1024
maav 0,64,0
s_cmp_lg_u32 s19,7
s_cbranch_scc1 L_j7o
s_nop 1
s_lshl_b32 s86,s12,12
s_add_u32 s86,s86,131072
v_add_u32_e32 v236,s86,v244
ra 160,236,0
ra 164,236,1024
ra 168,236,2048
ra 172,236,3072
ra 176,236,16384
ra 180,236,17408
ra 184,236,18432
ra 188,236,19456
v_add_u32_e32 v224,s94,v224
s_waitcnt lgkmcnt(0)
L_j7o:
ex 25
maav 0,96,4
ex 26
maav 4,68,0
ex 27
maav 4,100,4
cv 11,14,15
cv 12,40,41
maav 8,72,0
ds 255,8,76,2048
maav 8,104,4
cv 13,42,43
cv 14,44,45
maav 12,76,0
cv 15,46,47
cv 16,16,17
maav 12,108,4
ex 28
v_smfmac_f32_16x16x64_bf16 v[144:147],v[228:231],v[0:7],v248
cv 17,18,19
maav 16,80,0
ds 255,8,76,3072
maav 16,112,4
s_mov_b32 m0,s73
ex 29
maav 20,84,0
s_add_u32 s73,s72,131072
ex 30
maav 20,116,4
ex 31
maav 24,88,0
ex 56
maav 24,120,4
ds 255,8,77,0
maav 28,92,0
ex 57
maav 28,124,4
ex 58
maav 32,64,8
ex 59
maav 32,96,12
ex 60
maav 36,68,8
ds 255,8,77,1024
maav 36,100,12
cv 18,20,21
cv 19,22,23
maav 40,72,8
cv 20,48,49
cv 21,50,51
maav 40,104,12
cv 22,52,53
cv 23,54,55
maav 44,76,8
ex 61
maav 44,108,12
ds 255,8,77,2048
v_smfmac_f32_16x16x64_bf16 v[148:151],v[228:231],v[8:15],v248
cv 24,24,25
maav 48,80,8
ex 62
maav 48,112,12
ex 63
maav 52,84,8
ra 192,244,0
cv 25,26,27
maav 52,116,12
ds 255,8,77,3072
maav 56,88,8
cv 26,28,29
cv 27,30,31
maav 56,120,12
cv 28,56,57
cv 29,58,59
maav 60,92,8
cv 30,60,61
cv 31,62,63
maav 60,124,12
ex 64
mvav 160,64,16
la 132,252,50,2048
mvav 160,96,20
ex 65
mvav 164,68,16
ex 66
mvav 164,100,20
ex 67
mvav 168,72,16
ex 68
mvav 168,104,20
la 136,250,50,0
mvav 172,76,16
ex 69
mvav 172,108,20
ex 70
v_smfmac_f32_16x16x64_bf16 v[152:155],v[228:231],v[16:23],v248
ra 200,244,2048
mvav 176,80,16
ra 208,244,4096
cv 64,64,65
mvav 176,112,20
la 140,250,50,2048
mvav 180,84,16
ra 216,244,6144
cv 65,66,67
mvav 180,116,20
ex 71
mvav 184,88,16
ex 96
mvav 184,120,20
L_vs26:
s_cbranch_execz L_vs30
s_barrier
ex 97
mvav 188,92,16
la 156,250,50,3072
mvav 188,124,20
s_add_u32 m0,s52,131072
ra 196,244,1024
s_xor_b32 s51,s62,1048576
mvav 192,64,24
ra 204,244,3072
cv 66,68,69
mvav 192,96,28
ra 212,244,5120
cv 67,70,71
mvav 196,68,24
ra 220,244,7168
cv 68,96,97
mvav 196,100,28
la 152,250,50,1024
mvav 200,72,24
ex 98
mvav 200,104,28
ex 99
mvav 204,76,24
ra 224,244,8192
cv 69,98,99
mvav 204,108,28
ra 232,244,10240
v_add_u32_e32 v6,s87,v244
v_smfmac_f32_16x16x64_bf16 v[156:159],v[228:231],v[24:31],v248
ra 240,244,12288
mvav 208,80,24
la 148,252,50,3072
mvav 208,112,28
s_xor_b32 s87,s52,4096
ra 248,244,14336
v_add_u32_e32 v7,98304,v6
mvav 212,84,24
s_add_u32 s87,s87,150528
ra 252,244,15360
v_add_u32_e32 v5,65536,v244
mvav 212,116,28
ra 244,244,13312
v_add_u32_e32 v32,s87,v244
mvav 216,88,24
ra 228,244,9216
v_and_b32_e32 v224,960,v244
mvav 216,120,28
la 144,252,50,1024
mvav 220,92,24
s_add_u32 s50,s50,s55
ra 236,244,11264
mvav 220,124,28
ra 64,225,32768
s_waitcnt lgkmcnt(8)
cvaa 24,192,176,232
dl 245,24,51,0
mvaa 24,200,180
ra 96,225,40960
mvaa 24,208,184
ra 68,225,33792
mvaa 24,216,188
dl 246,24,51,1024
cvaa 16,192,160,128
ra 100,225,41984
mvaa 16,200,164
ra 72,225,34816
mvaa 16,208,168
dl 249,24,51,2048
mvaa 16,216,172
ra 104,225,43008
cvaa 28,196,176,232
ra 76,225,35840
mvaa 28,204,180
dl 247,24,51,3072
mvaa 28,212,184
s_add_u32 m0,s52,147456
s_xor_b32 s51,s63,1048576
ra 108,225,44032
mvaa 28,220,188
ra 80,225,36864
cvaa 20,196,160,132
dl 245,24,51,0
mvaa 20,204,164
ra 112,225,45056
mvaa 20,212,168
ra 192,244,16384
mvaa 20,220,172
dl 246,24,51,1024
cvaa 56,224,176,232
ra 200,244,18432
s_waitcnt lgkmcnt(7)
mvaa 56,232,180
ra 208,244,20480
mvaa 56,240,184
dl 249,24,51,2048
mvaa 56,248,188
ra 216,244,22528
cvaa 48,224,160,136
ra 196,244,17408
mvaa 48,232,164
ra 204,244,19456
mvaa 48,240,168
ra 212,244,21504
mvaa 48,248,172
ra 220,244,23552
cvaa 60,228,176,232
ra 224,244,24576
mvaa 60,236,180
ra 232,244,26624
mvaa 60,244,184
ra 240,244,28672
mvaa 60,252,188
ra 248,244,30720
cvaa 52,228,160,140
ra 84,225,37888
mvaa 52,236,164
ra 116,225,46080
mvaa 52,244,168
la 160,252,50,0
ra 228,244,25600
mvaa 52,252,172
ra 236,244,27648
s_waitcnt lgkmcnt(10)
cvaa 8,192,176,128
ra 244,244,29696
mvaa 8,200,180
la 164,252,50,2048
ra 252,244,31744
mvaa 8,208,184
ra 88,225,38912
mvaa 8,216,188
ra 120,225,47104
cvaa 12,196,176,132
la 168,250,50,0
mvaa 12,204,180
ra 92,225,39936
ra 124,225,48128
s_waitcnt lgkmcnt(9)
mvaa 12,212,184
ex 100
mvaa 12,220,188
la 172,250,50,2048
cvaa 40,224,176,136
ex 101
mvaa 40,232,180
ex 102
mvaa 40,240,184
ex 103
mvaa 40,248,188
cv 70,100,101
cv 71,102,103
s_waitcnt lgkmcnt(4)
cvaa 44,228,176,140
ex 72
mvaa 44,236,180
s_waitcnt vmcnt(18) lgkmcnt(0)
s_barrier
ex 73
mvaa 44,244,184
la 176,252,50,1024
mvaa 44,252,188
ex 74
maav 0,64,64
ex 75
maav 0,96,68
la 188,250,50,3072
maav 4,68,64
ra 192,5,0
cv 72,72,73
maav 4,100,68
ra 196,5,1024
cv 73,74,75
maav 8,72,64
la 184,250,50,1024
ds_write_b128 v6,a[0:3] offset:0
maav 8,104,68
ex 76
maav 12,76,64
ex 77
maav 12,108,68
la 180,252,50,3072
ds_write_b128 v6,a[4:7] offset:1024
v_smfmac_f32_16x16x64_bf16 v[144:147],v[228:231],v[64:71],v248
cv 74,76,77
maav 16,80,64
ex 78
maav 16,112,68
ds_write_b128 v6,a[8:11] offset:2048
ex 79
maav 20,84,64
ex 104
maav 20,116,68
ds_write_b128 v6,a[12:15] offset:3072
ex 105
maav 24,88,64
ex 106
ex 107
ex 108
maav 24,120,68
ds_write_b128 v6,a[16:19] offset:4096
cv 75,78,79
maav 28,92,64
ex 109
ex 110
ex 111
maav 28,124,68
cv 76,104,105
cv 77,106,107
cv 78,108,109
cv 79,110,111
maav 32,64,72
ds_write_b128 v6,a[20:23] offset:5120
maav 32,96,76
ex 80
maav 36,68,72
ex 81
maav 36,100,76
ds_write_b128 v6,a[24:27] offset:6144
maav 40,72,72
ex 82
maav 40,104,76
ex 83
maav 44,76,72
ds_write_b128 v6,a[28:31] offset:7168
maav 44,108,76
cv 80,80,81
cv 81,82,83
v_smfmac_f32_16x16x64_bf16 v[148:151],v[228:231],v[72:79],v248
ex 84
maav 48,80,72
ds_write_b128 v7,a[32:35] offset:0
maav 48,112,76
ex 85
ex 86
ex 87
maav 52,84,72
ex 112
ex 113
maav 52,116,76
ds_write_b128 v7,a[36:39] offset:1024
cv 82,84,85
maav 56,88,72
ex 114
ex 115
ex 116
maav 56,120,76
ex 117
ex 118
ex 119
maav 60,92,72
ds_write_b128 v7,a[40:43] offset:2048
cv 83,86,87
maav 60,124,76
cv 84,112,113
cv 85,114,115
cv 86,116,117
cv 87,118,119
mvav 160,64,80
ex 88
mvav 160,96,84
ds_write_b128 v7,a[44:47] offset:3072
mvav 164,68,80
v_add_f32_e32 v130,v144,v145
v_add_f32_e32 v131,v148,v149
mvav 164,100,84
ex 89
mvav 168,72,80
ds_write_b128 v7,a[48:51] offset:4096
mvav 168,104,84
ex 90
mvav 172,76,80
ex 91
mvav 172,108,84
ds_write_b128 v32,v[128:131]
v_smfmac_f32_16x16x64_bf16 v[152:155],v[228:231],v[80:87],v248
ex 92
ex 93
mvav 176,80,80
ds_write_b128 v7,a[52:55] offset:5120
ex 94
mvav 176,112,84
ra 200,5,2048
ra 204,5,3072
ex 95
cv 88,88,89
mvav 180,84,80
ra 208,5,4096
ra 212,5,5120
cv 89,90,91
cv 90,92,93
ex 120
mvav 180,116,84
ds_write_b128 v7,a[56:59] offset:6144
ex 121
mvav 184,88,80
ex 122
ex 123
cv 91,94,95
mvav 184,120,84
ex 124
ex 125
mvav 188,92,80
ds_write_b128 v7,a[60:63] offset:7168
ex 126
mvav 188,124,84
ra 216,5,6144
ra 220,5,7168
ra 224,5,8192
ra 232,5,10240
ex 127
cv 92,120,121
mvav 192,64,88
ra 240,5,12288
ra 248,5,14336
ra 228,5,9216
ra 236,5,11264
ra 244,5,13312
ra 252,5,15360
cv 93,122,123
cv 94,124,125
cv 95,126,127
v_xor_b32_e32 v6,8192,v6
s_nop 0
mvav 192,96,92
ex 16
mvav 196,68,88
s_waitcnt lgkmcnt(10)
s_xor_b32 s87,s87,0
mvav 196,100,92
s_xor_b32 s50,s66,294912
s_barrier
v_xor_b32_e32 v7,8192,v7
mvav 200,72,88
la 4,252,50,0
ra 64,5,16384
ra 96,5,24576
rv 64,6,0
rv 68,6,1024
rv 72,6,2048
mvav 200,104,92
rv 76,6,3072
rv 80,6,4096
rv 84,6,5120
ex 17
ex 18
mvav 204,76,88
rv 96,7,0
rv 100,7,1024
)"
R"(ex 19
mvav 204,108,92
la 12,252,50,2048
rv 104,7,2048
rv 108,7,3072
cv 16,16,17
v_smfmac_f32_16x16x64_bf16 v[156:159],v[228:231],v[88:95],v248
rv 112,7,4096
rv 116,7,5120
ex 20
ex 21
mvav 208,80,88
rv 120,7,6144
rv 124,7,7168
ex 22
ex 23
mvav 208,112,92
la 20,250,50,0
ra 68,5,17408
ra 100,5,25600
cv 17,18,19
cv 18,20,21
mvav 212,84,88
ra 72,5,18432
ra 104,5,26624
ex 48
ex 49
mvav 212,116,92
ra 76,5,19456
ra 108,5,27648
ex 50
ex 51
mvav 216,88,88
la 28,250,50,2048
ra 80,5,20480
ra 112,5,28672
ra 84,5,21504
mvav 216,120,92
ra 116,5,29696
cv 19,22,23
ex 52
ex 53
mvav 220,92,88
ra 88,5,22528
cv 20,48,49
ex 54
ex 55
mvav 220,124,92
la 60,250,50,3072
ra 120,5,30720
cv 21,50,51
cv 22,52,53
cv 23,54,55
s_bitcmp1_b32 s82,s81
s_cbranch_scc1 L_pend2
L_ret_eb5:
mvav 160,192,16
ra 92,5,23552
rv 88,6,6144
ra 124,5,31744
rv 92,6,7168
ex 24
s_waitcnt lgkmcnt(10)
mvav 160,224,20
ex 25
ex 26
v_smfmac_f32_16x16x64_bf16 v[152:155],v[228:231],v[16:23],v248
ex 27
v_xor_b32_e32 v32,4096,v32
mvav 164,196,16
la 52,250,50,1024
ds_read_b128 v[236:239],v32
cv 24,24,25
ex 28
mvav 164,228,20
ex 29
cv 25,26,27
ex 30
ex 31
mvav 168,200,16
ex 56
ex 57
mvav 168,232,20
la 44,252,50,3072
cv 26,28,29
ex 58
ex 59
mvav 172,204,16
ex 60
ex 61
ex 62
mvav 172,236,20
s_waitcnt lgkmcnt(0)
s_barrier
cv 27,30,31
ex 63
mvav 176,208,16
la 36,252,50,1024
cv 28,56,57
cv 29,58,59
cv 30,60,61
cv 31,62,63
mvav 176,240,20
s_add_u32 m0,s52,147456
ex 8
s_xor_b32 s51,s63,1048576
ex 9
ex 10
mvav 180,212,16
s_add_u32 s50,s50,s55
ex 11
cv 8,8,9
cv 9,10,11
ex 12
mvav 180,244,20
dl 247,24,51,3072
ex 13
ex 14
mvav 184,216,16
s_add_u32 m0,s52,0
ex 15
ex 40
ex 41
mvav 184,248,20
s_xor_b32 s51,s64,0
ex 42
ex 43
ex 44
mvav 188,220,16
la 0,252,50,0
ex 45
ex 46
mvav 188,252,20
ex 47
cv 10,12,13
cv 11,14,15
cv 12,40,41
cv 13,42,43
cv 14,44,45
v_smfmac_f32_16x16x64_bf16 v[156:159],v[228:231],v[24:31],v248
cv 15,46,47
v_bfe_u32 v225,v244,1,5
s_nop 0
v_smfmac_f32_16x16x64_bf16 v[156:159],v[228:231],v[8:15],v248
v_lshl_add_u32 v224,v224,1,v225
v_bfe_u32 v225,v244,7,2
v_lshl_add_u32 v224,v225,5,v224
mvav 192,192,24
la 8,252,50,2048
v_xor_b32_e32 v224,s69,v224
v_add_f32_e32 v152,v152,v153
v_add_f32_e32 v137,v152,v238
v_subrev_u32_e32 v236,0x23800000,v137
mvav 192,224,28
v_rcp_f32_e32 v137,v137
mvav 192,64,8
tv 128,224,32768
v_mul_f32_e32 v232,v240,v137
v_mul_f32_e32 v64,v232,v64
v_mul_f32_e32 v65,v232,v65
v_mul_f32_e32 v66,v232,v66
mvav 192,96,12
la 16,250,50,0
v_mul_f32_e32 v67,v232,v67
v_mul_f32_e32 v68,v232,v68
v_mul_f32_e32 v69,v232,v69
v_mul_f32_e32 v70,v232,v70
mvav 196,196,24
v_fmac_f32_e32 v64,v232,v160
v_fmac_f32_e32 v65,v232,v161
v_mul_f32_e32 v71,v232,v71
mvav 196,228,28
v_fmac_f32_e32 v66,v232,v162
v_fmac_f32_e32 v67,v232,v163
v_fmac_f32_e32 v68,v232,v164
v_fmac_f32_e32 v69,v232,v165
v_fmac_f32_e32 v70,v232,v166
mvav 196,68,8
la 24,250,50,2048
v_fmac_f32_e32 v71,v232,v167
cv 160,64,65
cv 161,66,67
cv 162,68,69
cv 163,70,71
mvav 196,100,12
tv 130,224,34816
tv 132,224,36864
mvav 200,200,24
tv 134,224,38912
tv 144,224,40960
v_mul_f32_e32 v72,v232,v72
v_mul_f32_e32 v73,v232,v73
v_mul_f32_e32 v74,v232,v74
mvav 200,232,28
buffer_store_dwordx4 v[160:163],v244,s[8:11],s14 offen offset:0 sc0 sc1
v_mul_f32_e32 v75,v232,v75
v_mul_f32_e32 v76,v232,v76
v_mul_f32_e32 v77,v232,v77
v_mul_f32_e32 v78,v232,v78
mvav 200,72,8
v_fmac_f32_e32 v72,v232,v168
v_fmac_f32_e32 v73,v232,v169
v_mul_f32_e32 v79,v232,v79
mvav 200,104,12
v_fmac_f32_e32 v74,v232,v170
v_fmac_f32_e32 v75,v232,v171
v_fmac_f32_e32 v76,v232,v172
v_fmac_f32_e32 v77,v232,v173
v_fmac_f32_e32 v78,v232,v174
mvav 204,204,24
la 56,250,50,3072
v_fmac_f32_e32 v79,v232,v175
cv 168,72,73
cv 169,74,75
cv 170,76,77
cv 171,78,79
mvav 204,236,28
tv 146,224,43008
tv 148,224,45056
mvav 204,76,8
tv 150,224,47104
v_mul_f32_e32 v80,v232,v80
v_mul_f32_e32 v81,v232,v81
v_mul_f32_e32 v82,v232,v82
mvav 204,108,12
buffer_store_dwordx4 v[168:171],v244,s[8:11],s14 offen offset:2048 sc0 sc1
v_mul_f32_e32 v83,v232,v83
v_mul_f32_e32 v84,v232,v84
v_mul_f32_e32 v85,v232,v85
v_mul_f32_e32 v86,v232,v86
v_mul_f32_e32 v87,v232,v87
mvav 208,208,24
v_fmac_f32_e32 v80,v232,v176
v_fmac_f32_e32 v81,v232,v177
v_fmac_f32_e32 v82,v232,v178
mvav 208,240,28
v_fmac_f32_e32 v83,v232,v179
v_fmac_f32_e32 v84,v232,v180
v_fmac_f32_e32 v85,v232,v181
cv 176,80,81
cv 177,82,83
mvav 208,80,8
la 48,250,50,1024
v_fmac_f32_e32 v86,v232,v182
v_fmac_f32_e32 v87,v232,v183
cv 178,84,85
cv 179,86,87
mvav 208,112,12
v_mul_f32_e32 v88,v232,v88
v_mul_f32_e32 v89,v232,v89
v_mul_f32_e32 v90,v232,v90
mvav 212,212,24
v_mul_f32_e32 v91,v232,v91
v_mul_f32_e32 v92,v232,v92
v_mul_f32_e32 v93,v232,v93
v_mul_f32_e32 v94,v232,v94
v_mul_f32_e32 v95,v232,v95
mvav 212,244,28
L_vs27:
s_cbranch_execz L_vs31
buffer_store_dwordx4 v[176:179],v244,s[8:11],s88 offen offset:0 sc0 sc1
v_fmac_f32_e32 v88,v232,v184
v_fmac_f32_e32 v89,v232,v185
v_fmac_f32_e32 v90,v232,v186
v_fmac_f32_e32 v91,v232,v187
cv 184,88,89
mvav 212,84,8
v_fmac_f32_e32 v92,v232,v188
cv 185,90,91
mvav 212,116,12
v_fmac_f32_e32 v93,v232,v189
v_fmac_f32_e32 v94,v232,v190
v_fmac_f32_e32 v95,v232,v191
cv 186,92,93
cv 187,94,95
mvav 216,216,24
la 40,252,50,3072
v_add_f32_e32 v156,v156,v157
v_add_f32_e32 v141,v156,v239
v_subrev_u32_e32 v237,0x23800000,v141
v_rcp_f32_e32 v141,v141
mvav 216,248,28
v_mul_f32_e32 v233,v241,v141
v_mul_f32_e32 v96,v233,v96
mvav 216,88,8
v_mul_f32_e32 v97,v233,v97
v_mul_f32_e32 v98,v233,v98
v_mul_f32_e32 v99,v233,v99
v_mul_f32_e32 v100,v233,v100
mvav 216,120,12
buffer_store_dwordx4 v[184:187],v244,s[8:11],s88 offen offset:2048 sc0 sc1
v_fmac_f32_e32 v96,v233,v192
v_fmac_f32_e32 v97,v233,v193
v_mul_f32_e32 v101,v233,v101
v_mul_f32_e32 v102,v233,v102
v_mul_f32_e32 v103,v233,v103
mvav 220,220,24
v_fmac_f32_e32 v98,v233,v194
v_fmac_f32_e32 v99,v233,v195
mvav 220,252,28
v_fmac_f32_e32 v100,v233,v196
v_fmac_f32_e32 v101,v233,v197
cv 192,96,97
cv 193,98,99
mvav 220,92,8
la 32,252,50,1024
v_fmac_f32_e32 v102,v233,v198
v_fmac_f32_e32 v103,v233,v199
cv 194,100,101
cv 195,102,103
mvav 220,124,12
v_xor_b32_e32 v225,32,v224
v_xor_b32_e32 v226,64,v224
s_waitcnt lgkmcnt(0)
zvva 0,128,128
tv 138,225,34816
tv 142,225,38912
tv 152,225,40960
s_waitcnt vmcnt(36)
mvva 0,132,132
tv 136,225,32768
tv 154,225,43008
mvva 0,144,136
tv 140,225,36864
tv 158,225,47104
mvva 0,148,140
tv 156,225,45056
v_mul_f32_e32 v104,v233,v104
v_mul_f32_e32 v105,v233,v105
v_mul_f32_e32 v106,v233,v106
zvva 4,148,156
buffer_store_dwordx4 v[192:195],v244,s[8:11],s14 offen offset:1024 sc0 sc1
v_mul_f32_e32 v107,v233,v107
v_mul_f32_e32 v108,v233,v108
v_mul_f32_e32 v109,v233,v109
v_mul_f32_e32 v110,v233,v110
mvva 4,144,152
v_fmac_f32_e32 v104,v233,v200
v_fmac_f32_e32 v105,v233,v201
v_fmac_f32_e32 v106,v233,v202
v_mul_f32_e32 v111,v233,v111
mvva 4,132,148
v_fmac_f32_e32 v107,v233,v203
v_fmac_f32_e32 v108,v233,v204
v_fmac_f32_e32 v109,v233,v205
cv 200,104,105
cv 201,106,107
mvva 4,128,144
v_fmac_f32_e32 v110,v233,v206
v_fmac_f32_e32 v111,v233,v207
cv 202,108,109
cv 203,110,111
s_waitcnt vmcnt(16)
zvva 8,128,160
v_cmp_lt_u32_e64 s[42:43],s48,v236
v_mul_f32_e32 v112,v233,v112
s_or_b64 s[40:41],s[40:41],s[42:43]
v_cmp_lt_u32_e64 s[42:43],s48,v237
mvva 8,132,164
s_or_b64 s[40:41],s[40:41],s[42:43]
v_mul_f32_e32 v113,v233,v113
v_mul_f32_e32 v114,v233,v114
v_mul_f32_e32 v115,v233,v115
mvva 8,144,168
dl 245,36,51,0
v_mul_f32_e32 v116,v233,v116
v_mul_f32_e32 v117,v233,v117
v_mul_f32_e32 v118,v233,v118
v_mul_f32_e32 v119,v233,v119
v_mul_f32_e32 v120,v233,v120
mvva 8,148,172
v_fmac_f32_e32 v112,v233,v208
v_fmac_f32_e32 v113,v233,v209
v_fmac_f32_e32 v114,v233,v210
v_fmac_f32_e32 v115,v233,v211
zvva 12,148,188
v_fmac_f32_e32 v116,v233,v212
v_fmac_f32_e32 v117,v233,v213
cv 208,112,113
cv 209,114,115
mvva 12,144,184
v_fmac_f32_e32 v118,v233,v214
v_fmac_f32_e32 v119,v233,v215
cv 210,116,117
cv 211,118,119
mvva 12,132,180
v_mul_f32_e32 v121,v233,v121
v_mul_f32_e32 v122,v233,v122
v_mul_f32_e32 v123,v233,v123
mvva 12,128,176
v_mul_f32_e32 v124,v233,v124
v_mul_f32_e32 v125,v233,v125
v_mul_f32_e32 v126,v233,v126
v_mul_f32_e32 v127,v233,v127
s_waitcnt lgkmcnt(0)
zvva 28,136,176
buffer_store_dwordx4 v[200:203],v244,s[8:11],s14 offen offset:3072 sc0 sc1
tv 144,226,40960
v_fmac_f32_e32 v120,v233,v216
v_fmac_f32_e32 v121,v233,v217
v_fmac_f32_e32 v122,v233,v218
v_fmac_f32_e32 v123,v233,v219
mvva 28,140,180
tv 128,226,32768
tv 130,226,34816
cv 216,120,121
cv 217,122,123
mvva 28,152,184
tv 132,226,36864
tv 134,226,38912
v_fmac_f32_e32 v124,v233,v220
v_fmac_f32_e32 v125,v233,v221
v_fmac_f32_e32 v126,v233,v222
mvva 28,156,188
tv 146,226,43008
v_fmac_f32_e32 v127,v233,v223
cv 218,124,125
cv 219,126,127
zvva 24,156,172
tv 148,226,45056
tv 150,226,47104
mvva 24,152,168
v_xor_b32_e32 v227,96,v224
v_add_u32_e32 v232,131072,v224
mvva 24,140,164
buffer_store_dwordx4 v[208:211],v244,s[8:11],s88 offen offset:1024 sc0 sc1
mvva 24,136,160
v_add_u32_e32 v233,131072,v225
v_add_u32_e32 v234,131072,v226
zvva 20,136,144
v_add_u32_e32 v235,131072,v227
v_mov_b32_e32 v242,83914752
mvva 20,140,148
v_add_u32_e32 v228,65536,v224
v_add_u32_e32 v229,65536,v225
mvva 20,152,152
v_add_u32_e32 v230,65536,v226
v_add_u32_e32 v231,65536,v227
mvva 20,156,156
v_and_b32_e32 v243,240,v244
v_lshlrev_b32_e32 v243,7,v243
zvva 16,156,140
buffer_store_dwordx4 v[216:219],v244,s[8:11],s88 offen offset:3072 sc0 sc1
s_cmp_lg_u64 s[40:41],0
s_cbranch_scc1 L_redo
L_redo_r5:
mvva 16,152,136
tv 156,227,45056
v_mov_b32_e32 v236,16256
mvva 16,140,132
tv 158,227,47104
v_and_b32_e32 v248,256,v244
mvva 16,136,128
tv 152,227,40960
v_lshrrev_b32_e32 v248,3,v248
s_waitcnt lgkmcnt(3)
zvva 32,128,128
tv 140,227,36864
v_add_u32_e32 v243,v243,v248
mvva 32,132,132
tv 136,227,32768
tv 138,227,34816
mvva 32,144,136
dl 246,36,51,1024
tv 142,227,38912
mvva 32,148,140
tv 154,227,43008
v_and_b32_e32 v248,512,v244
zvva 36,148,156
v_lshrrev_b32_e32 v248,5,v248
v_add_u32_e32 v243,v243,v248
mvva 36,144,152
v_and_b32_e32 v248,16,v244
v_lshlrev_b32_e32 v248,v248,v236
mvva 36,132,148
v_cndmask_b32_e64 v238,0,v248,s[74:75]
s_mov_b32 s74,0xc0000c
mvva 36,128,144
s_mov_b32 s75,0xc0000c00
v_mov_b32_e32 v240,0
v_cndmask_b32_e64 v239,0,v248,s[74:75]
zvva 40,128,160
dl 249,36,51,2048
mvva 40,132,164
v_mov_b32_e32 v241,0
v_mov_b32_e32 v236,0
mvva 40,144,168
v_mov_b32_e32 v237,0
mvva 40,148,172
zvva 44,148,188
mvva 44,144,184
mvva 44,132,180
dl 247,36,51,3072
s_add_u32 m0,s52,16384
s_xor_b32 s51,s65,0
mvva 44,128,176
tv 144,224,57344
s_waitcnt lgkmcnt(1)
zvva 60,136,176
tv 132,224,53248
tv 134,224,55296
mvva 60,140,180
tv 128,224,49152
tv 130,224,51200
mvva 60,152,184
mvva 60,156,188
tv 146,224,59392
zvva 56,156,172
dl 245,36,51,0
tv 148,224,61440
tv 150,224,63488
mvva 56,152,168
mvva 56,140,164
mvva 56,136,160
zvva 52,136,144
mvva 52,140,148
mvva 52,152,152
dl 246,36,51,1024
mvva 52,156,156
zvva 48,156,140
mvva 48,152,136
mvva 48,140,132
tv 156,225,61440
mvva 48,136,128
tv 152,225,57344
s_waitcnt lgkmcnt(2)
zvva 64,128,128
dl 249,36,51,2048
tv 140,225,53248
mvva 64,132,132
tv 136,225,49152
tv 138,225,51200
mvva 64,144,136
tv 142,225,55296
mvva 64,148,140
tv 154,225,59392
zvva 68,148,156
tv 158,225,63488
mvva 68,144,152
mvva 68,132,148
dl 247,36,51,3072
s_lshl_b32 s51,s33,5
s_add_u32 m0,s52,98304
s_xor_b32 s51,s64,s51
mvva 68,128,144
zvva 72,128,160
mvva 72,132,164
s_waitcnt vmcnt(18)
s_mov_b32 s90,0
mvva 72,144,168
s_xor_b32 s50,s67,0
mvva 72,148,172
zvva 76,148,188
dl 245,36,51,0
mvva 76,144,184
mvva 76,132,180
tv 148,226,61440
mvva 76,128,176
tv 144,226,57344
s_waitcnt lgkmcnt(2)
zvva 92,136,176
tv 132,226,53248
mvva 92,140,180
tv 128,226,49152
tv 130,226,51200
mvva 92,152,184
dl 246,36,51,1024
tv 134,226,55296
mvva 92,156,188
tv 146,226,59392
zvva 88,156,172
tv 150,226,63488
mvva 88,152,168
mvva 88,140,164
mvva 88,136,160
zvva 84,136,144
dl 249,36,51,2048
mvva 84,140,148
mvva 84,152,152
mvva 84,156,156
zvva 80,156,140
mvva 80,152,136
mvva 80,140,132
dl 247,36,51,3072
mvva 80,136,128
s_lshl_b32 s51,s33,5
tv 152,227,57344
s_add_u32 m0,s52,114688
s_waitcnt lgkmcnt(1)
zvva 96,128,128
s_xor_b32 s51,s65,s51
tv 140,227,53248
tv 142,227,55296
mvva 96,132,132
tv 136,227,49152
tv 138,227,51200
mvva 96,144,136
mvva 96,148,140
tv 154,227,59392
zvva 100,148,156
lv 164,252,50,0
tv 156,227,61440
tv 158,227,63488
mvva 100,144,152
mvva 100,132,148
mvva 100,128,144
zvva 104,128,160
mvva 104,132,164
mvva 104,144,168
lv 172,252,50,2048
mvva 104,148,172
zvva 108,148,188
mvva 108,144,184
mvva 108,132,180
mvva 108,128,176
s_barrier
s_waitcnt lgkmcnt(0)
zvva 124,136,176
lv 180,250,50,0
tv 132,232,4096
tv 144,232,8192
tv 146,232,10240
mvva 124,140,180
tv 128,232,0
tv 130,232,2048
mvva 124,152,184
tv 134,232,6144
mvva 124,156,188
tv 148,232,12288
tv 150,232,14336
zvva 120,156,172
mvva 120,152,168
mvva 120,140,164
lv 188,250,50,2048
mvva 120,136,160
zvva 116,136,144
mvva 116,140,148
mvva 116,152,152
mvva 116,156,156
zvva 112,156,140
lv 220,250,50,3072
mvva 112,152,136
mvva 112,140,132
tv 156,233,12288
mvva 112,136,128
tv 152,233,8192
s_waitcnt lgkmcnt(2)
mvva 0,128,4
tv 140,233,4096
mvva 0,132,12
tv 136,233,0
tv 138,233,2048
mvva 0,144,20
lv 212,250,50,1024
tv 142,233,6144
mvva 0,148,28
tv 154,233,10240
mvva 4,148,60
tv 158,233,14336
mvva 4,144,52
mvva 4,132,44
mvva 4,128,36
mvva 8,128,0
lv 204,252,50,3072
mvva 8,132,8
mvva 8,144,16
mvva 8,148,24
mvva 12,148,56
s_waitcnt vmcnt(27)
mvva 12,144,48
s_waitcnt vmcnt(25)
mvva 12,132,40
lv 196,252,50,1024
s_waitcnt vmcnt(24)
mvva 12,128,32
tv 144,234,8192
s_waitcnt lgkmcnt(1)
mvva 28,136,32
tv 132,234,4096
tv 134,234,6144
mvva 28,140,40
tv 128,234,0
tv 130,234,2048
mvva 28,152,48
mvva 28,156,56
tv 146,234,10240
mvva 24,156,24
dl 245,36,51,0
s_waitcnt vmcnt(20)
s_barrier
tv 148,234,12288
tv 150,234,14336
mvva 24,152,16
mvva 24,140,8
mvva 24,136,0
mvva 20,136,36
mvva 20,140,44
mvva 20,152,52
s_cmp_eq_u32 s12,0
s_cselect_b64 exec,1,0
buffer_store_dword v251,off,s[8:11],s61 sc0 sc1
s_mov_b64 exec,-1
mvva 20,156,60
mvva 16,156,28
mvva 16,152,20
mvva 16,140,12
tv 156,235,12288
mvva 16,136,4
tv 152,235,8192
s_waitcnt lgkmcnt(2)
mvva 32,128,4
dl 246,36,51,1024
tv 140,235,4096
mvva 32,132,12
tv 136,235,0
tv 138,235,2048
mvva 32,144,20
tv 142,235,6144
mvva 32,148,28
tv 154,235,10240
mvva 36,148,60
tv 158,235,14336
mvva 36,144,52
mvva 36,132,44
dl 249,36,51,2048
mvva 36,128,36
mvva 40,128,0
mvva 40,132,8
mvva 40,144,16
mvva 40,148,24
mvva 44,148,56
dl 247,36,51,3072
s_lshl_b32 s51,s34,5
s_add_u32 m0,s52,65536
s_xor_b32 s51,s64,s51
mvva 44,144,48
tv 148,232,28672
mvva 44,132,40
s_xor_b32 s53,s60,s33
tv 150,232,30720
mvva 44,128,32
tv 144,232,24576
s_waitcnt lgkmcnt(3)
mvva 60,136,32
tv 132,232,20480
mvva 60,140,40
tv 128,232,16384
tv 130,232,18432
mvva 60,152,48
dl 245,36,51,0
tv 134,232,22528
mvva 60,156,56
tv 146,232,26624
mvva 56,156,24
mvva 56,152,16
mvva 56,140,8
mvva 56,136,0
mvva 52,136,36
buffer_load_dword v253,v242,s[8:11],s53 offen sc1
mvva 52,140,44
mvva 52,152,52
mvva 52,156,60
mvva 48,156,28
mvva 48,152,20
mvva 48,140,12
dl 246,36,51,1024
mvva 48,136,4
tv 152,233,24576
s_waitcnt lgkmcnt(1)
mvva 64,128,4
tv 140,233,20480
tv 142,233,22528
mvva 64,132,12
tv 136,233,16384
tv 138,233,18432
mvva 64,144,20
mvva 64,148,28
tv 154,233,26624
mvva 68,148,60
dl 249,36,51,2048
tv 156,233,28672
tv 158,233,30720
mvva 68,144,52
mvva 68,132,44
mvva 68,128,36
mvva 72,128,0
mvva 72,132,8
mvva 72,144,16
dl 247,36,51,3072
s_lshl_b32 s51,s34,5
s_add_u32 m0,s52,81920
s_xor_b32 s51,s65,s51
mvva 72,148,24
mvva 76,148,56
mvva 76,144,48
mvva 76,132,40
tv 148,234,28672
mvva 76,128,32
tv 144,234,24576
s_waitcnt lgkmcnt(2)
mvva 92,136,32
dl 245,36,51,0
tv 132,234,20480
mvva 92,140,40
tv 128,234,16384
tv 130,234,18432
mvva 92,152,48
tv 134,234,22528
mvva 92,156,56
tv 146,234,26624
mvva 88,156,24
tv 150,234,30720
mvva 88,152,16
mvva 88,140,8
dl 246,36,51,1024
mvva 88,136,0
mvva 84,136,36
mvva 84,140,44
mvva 84,152,52
mvva 84,156,60
mvva 80,156,28
dl 249,36,51,2048
mvva 80,152,20
mvva 80,140,12
tv 156,235,28672
mvva 80,136,4
tv 152,235,24576
s_waitcnt lgkmcnt(2)
mvva 96,128,4
tv 140,235,20480
mvva 96,132,12
tv 136,235,16384
tv 138,235,18432
mvva 96,144,20
dl 247,36,51,3072
s_lshl_b32 s51,s35,5
s_add_u32 m0,s52,32768
s_xor_b32 s51,s64,s51
tv 142,235,22528
mvva 96,148,28
L_vs28:
s_cbranch_execz L_vs17
tv 154,235,26624
mvva 100,148,60
tv 158,235,30720
mvva 100,144,52
mvva 100,132,44
mvva 100,128,36
mvva 104,128,0
dl 245,36,51,0
mvva 104,132,8
mvva 104,144,16
mvva 104,148,24
mvva 108,148,56
mvva 108,144,48
mvva 108,132,40
dl 246,36,51,1024
mvva 108,128,32
s_waitcnt vmcnt(27)
s_barrier
s_waitcnt lgkmcnt(0)
mvva 124,136,32
tv 132,224,4096
tv 134,224,6144
tv 144,224,8192
mvva 124,140,40
tv 128,224,0
tv 130,224,2048
mvva 124,152,48
mvva 124,156,56
tv 146,224,10240
mvva 120,156,24
dl 249,36,51,2048
tv 148,224,12288
tv 150,224,14336
mvva 120,152,16
mvva 120,140,8
mvva 120,136,0
mvva 116,136,36
mvva 116,140,44
mvva 116,152,52
dl 247,36,51,3072
s_lshl_b32 s51,s35,5
s_add_u32 m0,s52,49152
s_xor_b32 s51,s65,s51
mvva 116,156,60
mvva 112,156,28
mvva 112,152,20
s_waitcnt vmcnt(11)
v_cmp_ne_u32_e32 vcc,4,v253
mvva 112,140,12
dl 245,36,51,0
s_cbranch_vccnz L_spa3o
L_spra3o:
mvva 112,136,4
s_xor_b32 s50,s67,s33
tv 152,225,8192
s_waitcnt lgkmcnt(1)
mvvv 0,128,164
tv 140,225,4096
tv 142,225,6144
mvvv 0,132,172
tv 136,225,0
tv 138,225,2048
mvvv 0,144,180
dl 246,36,51,1024
mvvv 0,148,188
tv 154,225,10240
mvvv 4,148,220
tv 156,225,12288
tv 158,225,14336
mvvv 4,144,212
mvvv 4,132,204
la 196,252,50,0
mvvv 4,128,196
mvvv 8,128,160
s_xor_b32 s53,s60,s34
mvvv 8,132,168
mvvv 8,144,176
la 204,252,50,2048
mvvv 8,148,184
mvvv 12,148,216
mvvv 12,144,208
mvvv 12,132,200
buffer_load_dword v253,v242,s[8:11],s53 offen sc1
mvvv 12,128,192
tv 144,226,8192
s_waitcnt lgkmcnt(1)
mvvv 28,136,192
tv 132,226,4096
tv 134,226,6144
mvvv 28,140,200
tv 128,226,0
tv 130,226,2048
mvvv 28,152,208
la 212,250,50,0
mvvv 28,156,216
tv 146,226,10240
mvvv 24,156,184
tv 148,226,12288
tv 150,226,14336
mvvv 24,152,176
mvvv 24,140,168
mvvv 24,136,160
la 220,250,50,2048
mvvv 20,136,196
mvvv 20,140,204
mvvv 20,152,212
mvvv 20,156,220
la 252,250,50,3072
mvvv 16,156,188
mvvv 16,152,180
mvvv 16,140,172
tv 156,227,12288
mvvv 16,136,164
la 244,250,50,1024
tv 152,227,8192
s_waitcnt lgkmcnt(2)
mvvv 32,128,164
tv 140,227,4096
mvvv 32,132,172
tv 136,227,0
tv 138,227,2048
mvvv 32,144,180
tv 142,227,6144
mvvv 32,148,188
la 236,252,50,3072
tv 154,227,10240
mvvv 36,148,220
tv 158,227,14336
mvvv 36,144,212
mvvv 36,132,204
mvvv 36,128,196
la 228,252,50,1024
s_add_u32 s50,s50,s55
mvvv 40,128,160
mvvv 40,132,168
mvvv 40,144,176
mvvv 40,148,184
mvvv 44,148,216
la 192,252,50,0
mvvv 44,144,208
mvvv 44,132,200
tv 148,224,28672
tv 150,224,30720
mvvv 44,128,192
s_waitcnt lgkmcnt(2)
mvvv 60,136,192
la 200,252,50,2048
tv 132,224,20480
mvvv 60,140,200
tv 128,224,16384
tv 130,224,18432
mvvv 60,152,208
tv 134,224,22528
mvvv 60,156,216
tv 144,224,24576
tv 146,224,26624
mvvv 56,156,184
la 208,250,50,0
)"
R"(mvvv 56,152,176
mvvv 56,140,168
mvvv 56,136,160
mvvv 52,136,196
la 216,250,50,2048
mvvv 52,140,204
mvvv 52,152,212
mvvv 52,156,220
mvvv 48,156,188
la 248,250,50,3072
mvvv 48,152,180
mvvv 48,140,172
tv 156,225,28672
mvvv 48,136,164
tv 152,225,24576
s_waitcnt lgkmcnt(2)
mvvv 64,128,164
tv 140,225,20480
mvvv 64,132,172
la 240,250,50,1024
tv 136,225,16384
tv 138,225,18432
mvvv 64,144,180
tv 142,225,22528
mvvv 64,148,188
tv 154,225,26624
mvvv 68,148,220
tv 158,225,30720
mvvv 68,144,212
la 232,252,50,3072
mvvv 68,132,204
mvvv 68,128,196
mvvv 72,128,160
mvvv 72,132,168
la 224,252,50,1024
mvvv 72,144,176
mvvv 72,148,184
mvvv 76,148,216
mvvv 76,144,208
mvvv 76,132,200
dl 249,36,51,2048
mvvv 76,128,192
tv 144,226,24576
s_waitcnt lgkmcnt(1)
mvvv 92,136,192
tv 132,226,20480
tv 134,226,22528
mvvv 92,140,200
tv 128,226,16384
tv 130,226,18432
mvvv 92,152,208
dl 247,36,51,3072
s_lshl_b32 s51,s98,5
s_xor_b32 s51,s64,s51
s_add_u32 m0,s52,131072
s_xor_b32 s51,s51,4194304
mvvv 92,156,216
tv 146,226,26624
mvvv 88,156,184
tv 148,226,28672
tv 150,226,30720
mvvv 88,152,176
mvvv 88,140,168
dl 245,36,51,0
mvvv 88,136,160
s_waitcnt vmcnt(17)
v_cmp_ne_u32_e32 vcc,4,v253
mvvv 84,136,196
s_cbranch_vccnz L_spa4o
L_spra4o:
mvvv 84,140,204
s_xor_b32 s50,s67,s34
mvvv 84,152,212
dl 246,36,51,1024
mvvv 84,156,220
mvvv 80,156,188
mvvv 80,152,180
mvvv 80,140,172
la 68,252,50,0
mvvv 80,136,164
tv 152,227,24576
s_waitcnt lgkmcnt(1)
mvvv 96,128,164
tv 140,227,20480
tv 142,227,22528
mvvv 96,132,172
tv 136,227,16384
tv 138,227,18432
mvvv 96,144,180
s_xor_b32 s53,s60,s35
mvvv 96,148,188
la 76,252,50,2048
tv 154,227,26624
mvvv 100,148,220
tv 156,227,28672
tv 158,227,30720
mvvv 100,144,212
mvvv 100,132,204
mvvv 100,128,196
la 84,250,50,0
mvvv 104,128,160
mvvv 104,132,168
mvvv 104,144,176
mvvv 104,148,184
buffer_load_dword v253,v242,s[8:11],s53 offen sc1
mvvv 108,148,216
mvvv 108,144,208
mvvv 108,132,200
mvvv 108,128,192
s_barrier
s_waitcnt lgkmcnt(0)
mvvv 124,136,192
la 92,250,50,2048
tv 132,228,36864
tv 144,228,40960
tv 146,228,43008
mvvv 124,140,200
tv 128,228,32768
tv 130,228,34816
mvvv 124,152,208
tv 134,228,38912
mvvv 124,156,216
tv 148,228,45056
tv 150,228,47104
mvvv 120,156,184
la 124,250,50,3072
mvvv 120,152,176
mvvv 120,140,168
mvvv 120,136,160
mvvv 116,136,196
la 116,250,50,1024
mvvv 116,140,204
mvvv 116,152,212
mvvv 116,156,220
mvvv 112,156,188
la 108,252,50,3072
mvvv 112,152,180
mvvv 112,140,172
tv 156,229,45056
tv 158,229,47104
mvvv 112,136,164
s_waitcnt lgkmcnt(2)
mvva 0,128,196
la 100,252,50,1024
s_add_u32 s50,s50,s55
tv 140,229,36864
mvva 0,132,204
tv 136,229,32768
tv 138,229,34816
s_waitcnt vmcnt(21)
mvva 0,144,212
tv 142,229,38912
mvva 0,148,220
tv 152,229,40960
tv 154,229,43008
mvva 4,148,252
mvva 4,144,244
la 64,252,50,0
mvva 4,132,236
mvva 4,128,228
s_waitcnt vmcnt(21)
mvva 8,128,192
s_waitcnt vmcnt(20)
mvva 8,132,200
la 72,252,50,2048
s_waitcnt vmcnt(20)
mvva 8,144,208
s_waitcnt vmcnt(19)
mvva 8,148,216
s_waitcnt vmcnt(18)
mvva 12,148,248
s_waitcnt vmcnt(17)
mvva 12,144,240
la 80,250,50,0
s_waitcnt vmcnt(17)
mvva 12,132,232
tv 148,230,45056
s_waitcnt vmcnt(16)
mvva 12,128,224
tv 144,230,40960
s_waitcnt lgkmcnt(2)
mvva 28,136,224
tv 132,230,36864
mvva 28,140,232
la 88,250,50,2048
tv 128,230,32768
tv 130,230,34816
mvva 28,152,240
tv 134,230,38912
mvva 28,156,248
tv 146,230,43008
mvva 24,156,216
tv 150,230,47104
mvva 24,152,208
la 120,250,50,3072
mvva 24,140,200
mvva 24,136,192
mvva 20,136,228
mvva 20,140,236
mvva 20,152,244
la 112,250,50,1024
mvva 20,156,252
mvva 16,156,220
mvva 16,152,212
mvva 16,140,204
la 104,252,50,3072
mvva 16,136,196
tv 152,231,40960
s_waitcnt lgkmcnt(1)
mvva 32,128,196
tv 140,231,36864
tv 142,231,38912
mvva 32,132,204
tv 136,231,32768
tv 138,231,34816
mvva 32,144,212
la 96,252,50,1024
mvva 32,148,220
tv 154,231,43008
mvva 36,148,252
tv 156,231,45056
tv 158,231,47104
mvva 36,144,244
mvva 36,132,236
mvva 36,128,228
dl 249,36,51,2048
mvva 40,128,192
mvva 40,132,200
mvva 40,144,208
mvva 40,148,216
dl 247,36,51,3072
s_lshl_b32 s51,s98,5
s_xor_b32 s51,s65,s51
s_add_u32 m0,s52,147456
s_xor_b32 s51,s51,4194304
mvva 44,148,248
mvva 44,144,240
mvva 44,132,232
tv 148,228,61440
mvva 44,128,224
dl 245,36,51,0
tv 144,228,57344
s_waitcnt lgkmcnt(2)
mvva 60,136,224
tv 132,228,53248
mvva 60,140,232
tv 128,228,49152
tv 130,228,51200
s_waitcnt vmcnt(16)
v_cmp_ne_u32_e32 vcc,4,v253
mvva 60,152,240
s_cbranch_vccnz L_spa5o
L_spra5o:
tv 134,228,55296
s_xor_b32 s50,s67,s35
mvva 60,156,248
dl 246,36,51,1024
tv 146,228,59392
mvva 56,156,216
tv 150,228,63488
mvva 56,152,208
mvva 56,140,200
mvva 56,136,192
la 132,252,50,0
mvva 52,136,228
mvva 52,140,236
mvva 52,152,244
mvva 52,156,252
mvva 48,156,220
la 140,252,50,2048
s_xor_b32 s53,s60,s98
s_xor_b32 s53,s53,512
mvva 48,152,212
tv 156,229,61440
mvva 48,140,204
tv 158,229,63488
mvva 48,136,196
tv 152,229,57344
s_waitcnt lgkmcnt(3)
mvva 64,128,196
la 148,250,50,0
tv 140,229,53248
mvva 64,132,204
tv 136,229,49152
tv 138,229,51200
mvva 64,144,212
tv 142,229,55296
mvva 64,148,220
tv 154,229,59392
mvva 68,148,252
buffer_load_dword v253,v242,s[8:11],s53 offen sc1
mvva 68,144,244
mvva 68,132,236
mvva 68,128,228
mvva 72,128,192
mvva 72,132,200
la 156,250,50,2048
mvva 72,144,208
mvva 72,148,216
mvva 76,148,248
mvva 76,144,240
la 188,250,50,3072
mvva 76,132,232
tv 148,230,61440
mvva 76,128,224
tv 144,230,57344
s_waitcnt lgkmcnt(2)
mvva 92,136,224
tv 132,230,53248
mvva 92,140,232
la 180,250,50,1024
tv 128,230,49152
tv 130,230,51200
mvva 92,152,240
tv 134,230,55296
mvva 92,156,248
tv 146,230,59392
mvva 88,156,216
tv 150,230,63488
mvva 88,152,208
la 172,252,50,3072
mvva 88,140,200
mvva 88,136,192
mvva 84,136,228
mvva 84,140,236
la 164,252,50,1024
s_add_u32 s50,s50,s55
mvva 84,152,244
mvva 84,156,252
mvva 80,156,220
mvva 80,152,212
mvva 80,140,204
la 128,252,50,0
mvva 80,136,196
tv 152,231,57344
s_waitcnt lgkmcnt(1)
mvva 96,128,196
tv 140,231,53248
tv 142,231,55296
mvva 96,132,204
tv 136,231,49152
tv 138,231,51200
mvva 96,144,212
la 136,252,50,2048
mvva 96,148,220
tv 154,231,59392
mvva 100,148,252
tv 156,231,61440
tv 158,231,63488
mvva 100,144,244
mvva 100,132,236
la 144,250,50,0
mvva 100,128,228
mvva 104,128,192
mvva 104,132,200
mvva 104,144,208
la 152,250,50,2048
mvva 104,148,216
mvva 108,148,248
mvva 108,144,240
mvva 108,132,232
la 184,250,50,3072
mvva 108,128,224
s_barrier
s_waitcnt lgkmcnt(0)
mvva 124,136,224
tv 132,228,4096
tv 134,228,6144
tv 144,228,8192
mvva 124,140,232
tv 128,228,0
tv 130,228,2048
mvva 124,152,240
mvva 124,156,248
la 176,250,50,1024
tv 146,228,10240
mvva 120,156,216
tv 148,228,12288
tv 150,228,14336
mvva 120,152,208
mvva 120,140,200
mvva 120,136,192
la 168,252,50,3072
mvva 116,136,228
L_vs29:
s_cbranch_execz L_vr1
mvva 116,140,236
mvva 116,152,244
mvva 116,156,252
la 160,252,50,1024
mvva 112,156,220
mvva 112,152,212
mvva 112,140,204
tv 156,229,12288
tv 158,229,14336
mvva 112,136,196
s_waitcnt lgkmcnt(2)
mvva 0,128,68
dl 249,36,51,2048
tv 140,229,4096
mvva 0,132,76
tv 136,229,0
tv 138,229,2048
mvva 0,144,84
tv 142,229,6144
s_waitcnt vmcnt(22)
mvva 0,148,92
tv 152,229,8192
tv 154,229,10240
mvva 4,148,124
dl 247,36,51,3072
s_lshl_b32 s51,s98,5
s_xor_b32 s51,s64,s51
s_add_u32 m0,s52,0
s_xor_b32 s51,s51,4456448
mvva 4,144,116
mvva 4,132,108
mvva 4,128,100
mvva 8,128,64
dl 245,36,51,0
mvva 8,132,72
mvva 8,144,80
mvva 8,148,88
s_waitcnt vmcnt(16)
v_cmp_ne_u32_e32 vcc,4,v253
mvva 12,148,120
dl 246,36,51,1024
s_cbranch_vccnz L_spa6o
L_spra6o:
mvva 12,144,112
s_xor_b32 s50,s67,s98
mvva 12,132,104
s_xor_b32 s50,s50,131072
tv 148,230,12288
tv 150,230,14336
mvva 12,128,96
s_waitcnt lgkmcnt(2)
mvva 28,136,96
dl 249,36,51,2048
tv 132,230,4096
mvva 28,140,104
tv 128,230,0
tv 130,230,2048
mvva 28,152,112
tv 134,230,6144
mvva 28,156,120
tv 144,230,8192
tv 146,230,10240
mvva 24,156,88
mvva 24,152,80
la 4,252,50,0
mvva 24,140,72
s_xor_b32 s53,s60,s98
mvva 24,136,64
s_xor_b32 s53,s53,8704
mvva 20,136,100
mvva 20,140,108
la 12,252,50,2048
mvva 20,152,116
mvva 20,156,124
mvva 16,156,92
mvva 16,152,84
buffer_load_dword v253,v242,s[8:11],s53 offen sc1
mvva 16,140,76
tv 156,231,12288
mvva 16,136,68
tv 152,231,8192
s_waitcnt lgkmcnt(2)
mvva 32,128,68
tv 140,231,4096
mvva 32,132,76
la 20,250,50,0
tv 136,231,0
tv 138,231,2048
mvva 32,144,84
tv 142,231,6144
mvva 32,148,92
tv 154,231,10240
mvva 36,148,124
tv 158,231,14336
mvva 36,144,116
la 28,250,50,2048
mvva 36,132,108
mvva 36,128,100
mvva 40,128,64
mvva 40,132,72
mvva 40,144,80
la 60,250,50,3072
mvva 40,148,88
mvva 44,148,120
mvva 44,144,112
mvva 44,132,104
la 52,250,50,1024
mvva 44,128,96
tv 144,228,24576
s_waitcnt lgkmcnt(1)
mvva 60,136,96
tv 132,228,20480
tv 134,228,22528
mvva 60,140,104
tv 128,228,16384
tv 130,228,18432
mvva 60,152,112
la 44,252,50,3072
mvva 60,156,120
tv 146,228,26624
mvva 56,156,88
tv 148,228,28672
tv 150,228,30720
mvva 56,152,80
mvva 56,140,72
mvva 56,136,64
la 36,252,50,1024
s_add_u32 s50,s50,s55
mvva 52,136,100
mvva 52,140,108
mvva 52,152,116
mvva 52,156,124
la 0,252,50,0
mvva 48,156,92
mvva 48,152,84
mvva 48,140,76
tv 156,229,28672
mvva 48,136,68
la 8,252,50,2048
tv 152,229,24576
s_waitcnt lgkmcnt(2)
mvva 64,128,68
tv 140,229,20480
mvva 64,132,76
tv 136,229,16384
tv 138,229,18432
mvva 64,144,84
tv 142,229,22528
mvva 64,148,92
la 16,250,50,0
tv 154,229,26624
mvva 68,148,124
tv 158,229,30720
mvva 68,144,116
mvva 68,132,108
mvva 68,128,100
la 24,250,50,2048
mvva 72,128,64
mvva 72,132,72
mvva 72,144,80
mvva 72,148,88
mvva 76,148,120
la 56,250,50,3072
mvva 76,144,112
mvva 76,132,104
tv 148,224,45056
tv 150,224,47104
mvva 76,128,96
s_waitcnt lgkmcnt(2)
mvva 92,136,96
la 48,250,50,1024
tv 132,224,36864
mvva 92,140,104
tv 128,224,32768
tv 130,224,34816
mvva 92,152,112
tv 134,224,38912
mvva 92,156,120
tv 144,224,40960
tv 146,224,43008
mvva 88,156,88
la 40,252,50,3072
mvva 88,152,80
mvva 88,140,72
mvva 88,136,64
mvva 84,136,100
mvva 84,140,108
la 32,252,50,1024
mvva 84,152,116
mvva 84,156,124
mvva 80,156,92
mvva 80,152,84
dl 247,36,51,3072
s_lshl_b32 s51,s98,5
s_xor_b32 s51,s64,s51
s_add_u32 m0,s52,16384
s_xor_b32 s51,s51,4718592
mvva 80,140,76
tv 152,225,40960
mvva 80,136,68
tv 154,225,43008
s_waitcnt lgkmcnt(2)
mvva 0,128,132
tv 140,225,36864
mvva 0,132,140
dl 245,36,51,0
tv 136,225,32768
tv 138,225,34816
mvva 0,144,148
tv 142,225,38912
s_waitcnt vmcnt(24)
mvva 0,148,156
mvva 4,148,188
tv 156,225,45056
tv 158,225,47104
mvva 4,144,180
dl 246,36,51,1024
s_waitcnt vmcnt(17)
v_cmp_ne_u32_e32 vcc,4,v253
mvva 4,132,172
s_cbranch_vccnz L_spa7o
L_spra7o:
mvva 4,128,164
s_xor_b32 s50,s67,s98
mvva 8,128,128
s_xor_b32 s50,s50,139264
mvva 8,132,136
dl 249,36,51,2048
mvva 8,144,144
mvva 8,148,152
mvva 12,148,184
mvva 12,144,176
mvva 12,132,168
dl 247,36,51,3072
mvva 12,128,160
s_barrier
tv 144,226,40960
s_lshl_b32 s51,s98,5
s_waitcnt lgkmcnt(1)
mvva 28,136,160
s_xor_b32 s53,s60,s98
tv 132,226,36864
tv 134,226,38912
s_xor_b32 s53,s53,16896
mvva 28,140,168
s_xor_b32 s51,s65,s51
tv 128,226,32768
tv 130,226,34816
mvva 28,152,176
lv 164,252,50,0
mvva 28,156,184
s_add_u32 m0,s52,98304
tv 146,226,43008
s_xor_b32 s51,s51,4456448
mvva 24,156,152
tv 148,226,45056
tv 150,226,47104
mvva 24,152,144
mvva 24,140,136
buffer_load_dword v253,v242,s[8:11],s53 offen sc1
mvva 24,136,128
mvva 20,136,164
mvva 20,140,172
mvva 20,152,180
lv 172,252,50,2048
mvva 20,156,188
mvva 16,156,156
mvva 16,152,148
mvva 16,140,140
lv 180,250,50,0
mvva 16,136,132
tv 152,227,40960
s_waitcnt lgkmcnt(1)
mvva 32,128,132
tv 140,227,36864
tv 142,227,38912
mvva 32,132,140
tv 136,227,32768
tv 138,227,34816
mvva 32,144,148
mvva 32,148,156
lv 188,250,50,2048
tv 154,227,43008
mvva 36,148,188
tv 156,227,45056
tv 158,227,47104
mvva 36,144,180
mvva 36,132,172
mvva 36,128,164
lv 220,250,50,3072
mvva 40,128,128
mvva 40,132,136
mvva 40,144,144
mvva 40,148,152
lv 212,250,50,1024
mvva 44,148,184
mvva 44,144,176
mvva 44,132,168
tv 148,224,61440
tv 150,224,63488
mvva 44,128,160
s_waitcnt lgkmcnt(2)
mvva 60,136,160
lv 204,252,50,3072
tv 132,224,53248
mvva 60,140,168
tv 128,224,49152
tv 130,224,51200
mvva 60,152,176
tv 134,224,55296
mvva 60,156,184
tv 144,224,57344
tv 146,224,59392
mvva 56,156,152
lv 196,252,50,1024
s_add_u32 s50,s50,s55
mvva 56,152,144
mvva 56,140,136
mvva 56,136,128
mvva 52,136,164
lv 160,252,50,0
mvva 52,140,172
mvva 52,152,180
mvva 52,156,188
mvva 48,156,156
lv 168,252,50,2048
mvva 48,152,148
mvva 48,140,140
tv 156,225,61440
tv 158,225,63488
mvva 48,136,132
s_waitcnt lgkmcnt(2)
mvva 64,128,132
lv 176,250,50,0
tv 140,225,53248
mvva 64,132,140
tv 136,225,49152
tv 138,225,51200
mvva 64,144,148
tv 142,225,55296
mvva 64,148,156
tv 152,225,57344
tv 154,225,59392
mvva 68,148,188
mvva 68,144,180
lv 184,250,50,2048
mvva 68,132,172
mvva 68,128,164
mvva 72,128,128
mvva 72,132,136
lv 216,250,50,3072
mvva 72,144,144
mvva 72,148,152
mvva 76,148,184
mvva 76,144,176
lv 208,250,50,1024
mvva 76,132,168
tv 148,232,12288
mvva 76,128,160
tv 144,232,8192
s_waitcnt lgkmcnt(2)
mvva 92,136,160
tv 132,232,4096
mvva 92,140,168
tv 128,232,0
tv 130,232,2048
mvva 92,152,176
lv 200,252,50,3072
tv 134,232,6144
mvva 92,156,184
tv 146,232,10240
mvva 88,156,152
tv 150,232,14336
mvva 88,152,144
mvva 88,140,136
lv 192,252,50,1024
mvva 88,136,128
mvva 84,136,164
mvva 84,140,172
mvva 84,152,180
dl 245,36,51,0
mvva 84,156,188
mvva 80,156,156
mvva 80,152,148
mvva 80,140,140
dl 246,36,51,1024
mvva 80,136,132
tv 152,233,8192
s_waitcnt vmcnt(17)
v_cmp_ne_u32_e32 vcc,4,v253
s_waitcnt lgkmcnt(1)
mvva 0,128,4
s_cbranch_vccnz L_spa8o
L_spra8o:
tv 140,233,4096
tv 142,233,6144
mvva 0,132,12
s_xor_b32 s50,s67,s98
tv 136,233,0
s_xor_b32 s50,s50,147456
tv 138,233,2048
mvva 0,144,20
dl 249,36,51,2048
mvva 0,148,28
tv 154,233,10240
v_and_b32_e32 v242,48,v244
mvva 4,148,60
tv 156,233,12288
tv 158,233,14336
mvva 4,144,52
v_lshlrev_b32_e32 v242,3,v242
mvva 4,132,44
mvva 4,128,36
la 196,252,50,0
mvva 8,128,0
s_andn2_b32 s53,s61,384
mvva 8,132,8
mvva 8,144,16
mvva 8,148,24
la 204,252,50,2048
mvva 12,148,56
mvva 12,144,48
mvva 12,132,40
tv 148,234,12288
mvva 12,128,32
buffer_load_dword v253,v242,s[8:11],s53 offen sc1
tv 144,234,8192
s_waitcnt lgkmcnt(2)
mvva 28,136,32
tv 132,234,4096
mvva 28,140,40
tv 128,234,0
tv 130,234,2048
mvva 28,152,48
tv 134,234,6144
mvva 28,156,56
la 212,250,50,0
tv 146,234,10240
mvva 24,156,24
tv 150,234,14336
mvva 24,152,16
mvva 24,140,8
mvva 24,136,0
la 220,250,50,2048
mvva 20,136,36
mvva 20,140,44
mvva 20,152,52
mvva 20,156,60
mvva 16,156,28
la 252,250,50,3072
mvva 16,152,20
mvva 16,140,12
tv 156,235,12288
tv 158,235,14336
mvva 16,136,4
s_waitcnt lgkmcnt(2)
mvva 32,128,4
la 244,250,50,1024
tv 140,235,4096
mvva 32,132,12
tv 136,235,0
tv 138,235,2048
mvva 32,144,20
tv 142,235,6144
mvva 32,148,28
tv 152,235,8192
tv 154,235,10240
mvva 36,148,60
la 236,252,50,3072
mvva 36,144,52
mvva 36,132,44
mvva 36,128,36
mvva 40,128,0
mvva 40,132,8
la 228,252,50,1024
s_add_u32 s50,s50,s55
mvva 40,144,16
mvva 40,148,24
mvva 44,148,56
mvva 44,144,48
la 192,252,50,0
mvva 44,132,40
mvva 44,128,32
s_barrier
s_waitcnt lgkmcnt(0)
mvva 60,136,32
tv 132,232,20480
tv 144,232,24576
mvva 60,140,40
la 200,252,50,2048
tv 128,232,16384
tv 130,232,18432
mvva 60,152,48
tv 134,232,22528
mvva 60,156,56
tv 146,232,26624
mvva 56,156,24
tv 148,232,28672
tv 150,232,30720
mvva 56,152,16
la 208,250,50,0
mvva 56,140,8
mvva 56,136,0
mvva 52,136,36
mvva 52,140,44
la 216,250,50,2048
mvva 52,152,52
mvva 52,156,60
mvva 48,156,28
mvva 48,152,20
mvva 48,140,12
la 248,250,50,3072
mvva 48,136,4
tv 152,233,24576
s_waitcnt lgkmcnt(1)
mvva 64,128,4
tv 140,233,20480
tv 142,233,22528
mvva 64,132,12
tv 136,233,16384
tv 138,233,18432
mvva 64,144,20
la 240,250,50,1024
mvva 64,148,28
tv 154,233,26624
mvva 68,148,60
tv 156,233,28672
tv 158,233,30720
mvva 68,144,52
mvva 68,132,44
la 232,252,50,3072
mvva 68,128,36
mvva 72,128,0
mvva 72,132,8
mvva 72,144,16
la 224,252,50,1024
mvva 72,148,24
L_vs30:
s_cbranch_execz L_vr1
mvva 76,148,56
mvva 76,144,48
mvva 76,132,40
dl 247,36,51,3072
s_lshl_b32 s51,s98,5
s_xor_b32 s51,s65,s51
s_add_u32 m0,s52,114688
s_xor_b32 s51,s51,4718592
mvva 76,128,32
tv 144,224,8192
s_waitcnt lgkmcnt(1)
mvva 92,136,32
tv 132,224,4096
tv 134,224,6144
mvva 92,140,40
tv 128,224,0
tv 130,224,2048
mvva 92,152,48
mvva 92,156,56
dl 245,36,51,0
tv 146,224,10240
mvva 88,156,24
tv 148,224,12288
tv 150,224,14336
mvva 88,152,16
mvva 88,140,8
mvva 88,136,0
dl 246,36,51,1024
s_waitcnt vmcnt(17)
v_cmp_ne_u32_e32 vcc,4,v253
mvva 84,136,36
s_cbranch_vccnz L_sppo
L_sprpo:
mvva 84,140,44
mvva 84,152,52
mvva 84,156,60
dl 249,36,51,2048
mvva 80,156,28
mvva 80,152,20
mvva 80,140,12
tv 156,225,12288
mvva 80,136,4
tv 152,225,8192
s_waitcnt lgkmcnt(2)
mvvv 0,128,164
dl 247,36,51,3072
tv 140,225,4096
mvvv 0,132,172
tv 136,225,0
tv 138,225,2048
mvvv 0,144,180
tv 142,225,6144
mvvv 0,148,188
tv 154,225,10240
mvvv 4,148,220
tv 158,225,14336
mvvv 4,144,212
mvvv 4,132,204
mvvv 4,128,196
mvvv 8,128,160
mvvv 8,132,168
mvvv 8,144,176
mvvv 8,148,184
mvvv 12,148,216
mvvv 12,144,208
mvvv 12,132,200
tv 148,224,28672
mvvv 12,128,192
tv 144,224,24576
s_waitcnt lgkmcnt(2)
mvvv 28,136,192
tv 132,224,20480
mvvv 28,140,200
tv 128,224,16384
tv 130,224,18432
)"
R"(mvvv 28,152,208
tv 134,224,22528
mvvv 28,156,216
tv 146,224,26624
mvvv 24,156,184
tv 150,224,30720
mvvv 24,152,176
mvvv 24,140,168
mvvv 24,136,160
mvvv 20,136,196
mvvv 20,140,204
mvvv 20,152,212
mvvv 20,156,220
mvvv 16,156,188
mvvv 16,152,180
mvvv 16,140,172
tv 156,225,28672
mvvv 16,136,164
tv 152,225,24576
s_waitcnt lgkmcnt(2)
mvva 0,128,196
tv 140,225,20480
mvva 0,132,204
tv 136,225,16384
tv 138,225,18432
s_waitcnt vmcnt(14)
mvva 0,144,212
tv 142,225,22528
mvva 0,148,220
tv 154,225,26624
mvva 4,148,252
tv 158,225,30720
mvva 4,144,244
mvva 4,132,236
s_waitcnt vmcnt(13)
mvva 4,128,228
s_waitcnt vmcnt(12)
mvva 8,128,192
cv 0,0,1
cv 1,2,3
s_waitcnt vmcnt(11)
mvva 8,132,200
s_waitcnt vmcnt(10)
mvva 8,144,208
cv 4,4,5
cv 5,6,7
s_waitcnt vmcnt(9)
mvva 8,148,216
s_waitcnt vmcnt(8)
mvva 12,148,248
s_waitcnt vmcnt(7)
mvva 12,144,240
s_waitcnt vmcnt(6)
mvva 12,132,232
tv 148,226,12288
s_waitcnt vmcnt(5)
mvva 12,128,224
tv 144,226,8192
cv 8,8,9
s_waitcnt lgkmcnt(2)
mvva 28,136,224
tv 132,226,4096
cv 9,10,11
mvva 28,140,232
tv 128,226,0
tv 130,226,2048
mvva 28,152,240
tv 134,226,6144
cv 12,12,13
mvva 28,156,248
tv 146,226,10240
cv 13,14,15
mvva 24,156,216
tv 150,226,14336
mvva 24,152,208
mvva 24,140,200
mvva 24,136,192
mvva 20,136,228
cv 14,28,29
cv 15,30,31
mvva 20,140,236
buffer_store_dwordx4 v[12:15],v244,s[8:11],s56 offen offset:3072 sc0 sc1
mvva 20,152,244
mvva 20,156,252
mvva 16,156,220
cv 10,24,25
cv 11,26,27
mvva 16,152,212
buffer_store_dwordx4 v[8:11],v244,s[8:11],s56 offen offset:2048 sc0 sc1
mvva 16,140,204
tv 156,227,12288
mvva 16,136,196
cv 6,20,21
cv 7,22,23
s_waitcnt lgkmcnt(1)
mvvv 32,128,164
tv 140,227,4096
mvvv 32,132,172
buffer_store_dwordx4 v[4:7],v244,s[8:11],s56 offen offset:1024 sc0 sc1
tv 136,227,0
tv 138,227,2048
mvvv 32,144,180
tv 142,227,6144
cv 2,16,17
mvvv 32,148,188
tv 152,227,8192
tv 154,227,10240
mvvv 36,148,220
tv 158,227,14336
cv 3,18,19
mvvv 36,144,212
buffer_store_dwordx4 v[0:3],v244,s[8:11],s56 offen offset:0 sc0 sc1
mvvv 36,132,204
mvvv 36,128,196
mvvv 40,128,160
mvvv 40,132,168
mvvv 40,144,176
mvvv 40,148,184
mvvv 44,148,216
mvvv 44,144,208
mvvv 44,132,200
tv 148,226,28672
mvvv 44,128,192
s_waitcnt vmcnt(4)
s_barrier
tv 144,226,24576
s_waitcnt lgkmcnt(2)
mvvv 60,136,192
tv 132,226,20480
mvvv 60,140,200
tv 128,226,16384
tv 130,226,18432
mvvv 60,152,208
tv 134,226,22528
mvvv 60,156,216
tv 146,226,26624
mvvv 56,156,184
tv 150,226,30720
mvvv 56,152,176
mvvv 56,140,168
mvvv 56,136,160
mvvv 52,136,196
mvvv 52,140,204
mvvv 52,152,212
mvvv 52,156,220
mvvv 48,156,188
mvvv 48,152,180
mvvv 48,140,172
tv 156,227,28672
mvvv 48,136,164
tv 152,227,24576
s_waitcnt lgkmcnt(2)
mvva 32,128,196
tv 140,227,20480
mvva 32,132,204
tv 136,227,16384
tv 138,227,18432
mvva 32,144,212
tv 142,227,22528
mvva 32,148,220
tv 154,227,26624
mvva 36,148,252
tv 158,227,30720
mvva 36,144,244
mvva 36,132,236
mvva 36,128,228
mvva 40,128,192
mvva 40,132,200
cv 32,32,33
cv 33,34,35
mvva 40,144,208
mvva 40,148,216
mvva 44,148,248
mvva 44,144,240
cv 36,36,37
cv 37,38,39
mvva 44,132,232
tv 148,228,45056
mvva 44,128,224
tv 144,228,40960
cv 40,40,41
s_waitcnt lgkmcnt(2)
mvva 60,136,224
tv 132,228,36864
cv 41,42,43
mvva 60,140,232
tv 128,228,32768
tv 130,228,34816
mvva 60,152,240
tv 134,228,38912
cv 44,44,45
mvva 60,156,248
tv 146,228,43008
cv 45,46,47
mvva 56,156,216
tv 150,228,47104
mvva 56,152,208
mvva 56,140,200
mvva 56,136,192
mvva 52,136,228
cv 46,60,61
cv 47,62,63
mvva 52,140,236
buffer_store_dwordx4 v[44:47],v244,s[8:11],s57 offen offset:3072 sc0 sc1
mvva 52,152,244
mvva 52,156,252
mvva 48,156,220
cv 42,56,57
cv 43,58,59
mvva 48,152,212
buffer_store_dwordx4 v[40:43],v244,s[8:11],s57 offen offset:2048 sc0 sc1
mvva 48,140,204
tv 156,229,45056
mvva 48,136,196
cv 38,52,53
cv 39,54,55
s_waitcnt lgkmcnt(1)
mvvv 64,128,164
tv 140,229,36864
mvvv 64,132,172
buffer_store_dwordx4 v[36:39],v244,s[8:11],s57 offen offset:1024 sc0 sc1
tv 136,229,32768
tv 138,229,34816
mvvv 64,144,180
tv 142,229,38912
cv 34,48,49
mvvv 64,148,188
tv 152,229,40960
tv 154,229,43008
mvvv 68,148,220
tv 158,229,47104
cv 35,50,51
mvvv 68,144,212
buffer_store_dwordx4 v[32:35],v244,s[8:11],s57 offen offset:0 sc0 sc1
mvvv 68,132,204
mvvv 68,128,196
mvvv 72,128,160
mvvv 72,132,168
mvvv 72,144,176
mvvv 72,148,184
mvvv 76,148,216
mvvv 76,144,208
mvvv 76,132,200
tv 148,228,61440
mvvv 76,128,192
tv 144,228,57344
s_waitcnt lgkmcnt(2)
mvvv 92,136,192
tv 132,228,53248
mvvv 92,140,200
tv 128,228,49152
tv 130,228,51200
mvvv 92,152,208
tv 134,228,55296
mvvv 92,156,216
tv 146,228,59392
mvvv 88,156,184
tv 150,228,63488
mvvv 88,152,176
mvvv 88,140,168
mvvv 88,136,160
mvvv 84,136,196
mvvv 84,140,204
mvvv 84,152,212
mvvv 84,156,220
mvvv 80,156,188
mvvv 80,152,180
mvvv 80,140,172
tv 156,229,61440
mvvv 80,136,164
tv 152,229,57344
s_waitcnt lgkmcnt(2)
mvva 64,128,196
tv 140,229,53248
mvva 64,132,204
tv 136,229,49152
tv 138,229,51200
mvva 64,144,212
tv 142,229,55296
mvva 64,148,220
tv 154,229,59392
mvva 68,148,252
tv 158,229,63488
mvva 68,144,244
mvva 68,132,236
mvva 68,128,228
s_xor_b32 s82,s59,12
mvva 72,128,192
s_mul_i32 s82,s82,12288
cv 64,64,65
s_add_u32 s82,s82,0
mvva 72,132,200
lv 16,244,82,0
mvva 72,144,208
cv 65,66,67
cv 68,68,69
mvva 72,148,216
cv 69,70,71
mvva 76,148,248
mvva 76,144,240
lv 20,244,82,1024
mvva 76,132,232
tv 148,230,28672
mvva 76,128,224
tv 144,230,24576
cv 72,72,73
s_waitcnt lgkmcnt(2)
mvva 92,136,224
tv 132,230,20480
cv 73,74,75
mvva 92,140,232
tv 128,230,16384
tv 130,230,18432
mvva 92,152,240
lv 24,244,82,2048
tv 134,230,22528
mvva 92,156,248
tv 146,230,26624
cv 76,76,77
mvva 88,156,216
tv 150,230,30720
cv 77,78,79
mvva 88,152,208
mvva 88,140,200
lv 28,244,82,3072
mvva 88,136,192
cv 78,92,93
cv 79,94,95
mvva 84,136,228
mvva 84,140,236
mvva 84,152,244
buffer_store_dwordx4 v[76:79],v244,s[8:11],s58 offen offset:3072 sc0 sc1
mvva 84,156,252
mvva 80,156,220
cv 74,88,89
cv 75,90,91
mvva 80,152,212
mvva 80,140,204
buffer_store_dwordx4 v[72:75],v244,s[8:11],s58 offen offset:2048 sc0 sc1
tv 156,231,28672
mvva 80,136,196
cv 70,84,85
cv 71,86,87
s_waitcnt lgkmcnt(1)
mvva 96,128,68
tv 140,231,20480
mvva 96,132,76
tv 136,231,16384
tv 138,231,18432
mvva 96,144,84
buffer_store_dwordx4 v[68:71],v244,s[8:11],s58 offen offset:1024 sc0 sc1
tv 142,231,22528
mvva 96,148,92
tv 152,231,24576
tv 154,231,26624
mvva 100,148,124
tv 158,231,30720
cv 66,80,81
cv 67,82,83
mvva 100,144,116
mvva 100,132,108
mvva 100,128,100
buffer_store_dwordx4 v[64:67],v244,s[8:11],s58 offen offset:0 sc0 sc1
mvva 104,128,64
mvva 104,132,72
mvva 104,144,80
mvva 104,148,88
mvva 108,148,120
mvva 108,144,112
mvva 108,132,104
tv 148,226,61440
mvva 108,128,96
tv 144,226,57344
s_waitcnt lgkmcnt(2)
mvva 124,136,96
tv 132,226,53248
mvva 124,140,104
tv 128,226,49152
tv 130,226,51200
mvva 124,152,112
tv 134,226,55296
mvva 124,156,120
tv 146,226,59392
mvva 120,156,88
tv 150,226,63488
mvva 120,152,80
mvva 120,140,72
mvva 120,136,64
mvva 116,136,100
mvva 116,140,108
s_waitcnt vmcnt(7)
v_max_u32_e32 v248,v16,v17
mvva 116,152,116
v_max3_u32 v248,v248,v18,v19
s_waitcnt vmcnt(6)
v_max3_u32 v248,v248,v20,v21
v_max3_u32 v248,v248,v22,v23
mvva 116,156,124
s_waitcnt vmcnt(5)
v_max3_u32 v248,v248,v24,v25
v_max3_u32 v248,v248,v26,v27
s_waitcnt vmcnt(4)
v_max3_u32 v248,v248,v28,v29
mvva 112,156,92
v_max3_u32 v248,v248,v30,v31
v_cmp_eq_u32_e32 vcc,-1,v248
mvva 112,152,84
s_cbranch_vccnz L_spx0o
L_sprx0o:
mvva 112,140,76
tv 156,227,61440
tv 158,227,63488
v_lshlrev_b32_e32 v251,16,v18
v_and_b32_e32 v255,4294901760,v18
mvva 112,136,68
tv 152,227,57344
tv 154,227,59392
s_waitcnt lgkmcnt(4)
mvva 96,128,132
v_lshlrev_b32_e32 v253,16,v19
v_and_b32_e32 v242,4294901760,v19
mvva 96,132,140
tv 136,227,49152
tv 138,227,51200
mvva 96,144,148
tv 140,227,53248
tv 142,227,55296
v_add_f32_e32 v112,v112,v251
mvva 96,148,156
v_add_f32_e32 v113,v113,v255
v_add_f32_e32 v114,v114,v253
mvva 100,148,188
v_add_f32_e32 v115,v115,v242
v_lshlrev_b32_e32 v251,16,v22
mvva 100,144,180
v_and_b32_e32 v255,4294901760,v22
v_lshlrev_b32_e32 v253,16,v23
mvva 100,132,172
v_and_b32_e32 v242,4294901760,v23
v_add_f32_e32 v116,v116,v251
v_add_f32_e32 v117,v117,v255
mvva 100,128,164
v_add_f32_e32 v118,v118,v253
v_add_f32_e32 v119,v119,v242
mvva 104,128,128
v_lshlrev_b32_e32 v251,16,v26
v_and_b32_e32 v255,4294901760,v26
mvva 104,132,136
v_lshlrev_b32_e32 v253,16,v27
v_and_b32_e32 v242,4294901760,v27
mvva 104,144,144
v_add_f32_e32 v120,v120,v251
v_add_f32_e32 v121,v121,v255
v_add_f32_e32 v122,v122,v253
mvva 104,148,152
v_add_f32_e32 v123,v123,v242
v_lshlrev_b32_e32 v251,16,v30
mvva 108,148,184
v_and_b32_e32 v255,4294901760,v30
v_lshlrev_b32_e32 v253,16,v31
mvva 108,144,176
v_and_b32_e32 v242,4294901760,v31
v_add_f32_e32 v124,v124,v251
mvva 108,132,168
v_add_f32_e32 v125,v125,v255
v_add_f32_e32 v126,v126,v253
v_add_f32_e32 v127,v127,v242
mvva 108,128,160
tv 144,234,24576
v_lshlrev_b32_e32 v251,16,v16
s_waitcnt lgkmcnt(1)
mvva 124,136,160
tv 132,234,20480
v_and_b32_e32 v255,4294901760,v16
mvva 124,140,168
L_vs31:
s_cbranch_execz L_vr1
tv 128,234,16384
tv 130,234,18432
mvva 124,152,176
tv 134,234,22528
v_lshlrev_b32_e32 v253,16,v17
mvva 124,156,184
tv 146,234,26624
v_and_b32_e32 v242,4294901760,v17
mvva 120,156,152
tv 148,234,28672
tv 150,234,30720
mvva 120,152,144
v_add_f32_e32 v96,v96,v251
v_add_f32_e32 v97,v97,v255
mvva 120,140,136
v_add_f32_e32 v98,v98,v253
v_add_f32_e32 v99,v99,v242
mvva 120,136,128
v_lshlrev_b32_e32 v251,16,v20
v_and_b32_e32 v255,4294901760,v20
mvva 116,136,164
v_lshlrev_b32_e32 v253,16,v21
v_and_b32_e32 v242,4294901760,v21
mvva 116,140,172
v_add_f32_e32 v100,v100,v251
v_add_f32_e32 v101,v101,v255
mvva 116,152,180
v_add_f32_e32 v102,v102,v253
v_add_f32_e32 v103,v103,v242
mvva 116,156,188
v_lshlrev_b32_e32 v251,16,v24
v_and_b32_e32 v255,4294901760,v24
mvva 112,156,156
v_lshlrev_b32_e32 v253,16,v25
v_and_b32_e32 v242,4294901760,v25
mvva 112,152,148
v_add_f32_e32 v104,v104,v251
v_add_f32_e32 v105,v105,v255
mvva 112,140,140
tv 156,235,28672
v_add_f32_e32 v106,v106,v253
mvva 112,136,132
tv 152,235,24576
v_add_f32_e32 v107,v107,v242
s_waitcnt lgkmcnt(2)
mvva 96,128,4
tv 140,235,20480
v_lshlrev_b32_e32 v251,16,v28
mvva 96,132,12
tv 136,235,16384
tv 138,235,18432
mvva 96,144,20
tv 142,235,22528
v_and_b32_e32 v255,4294901760,v28
mvva 96,148,28
tv 154,235,26624
v_lshlrev_b32_e32 v253,16,v29
mvva 100,148,60
tv 158,235,30720
v_and_b32_e32 v242,4294901760,v29
mvva 100,144,52
v_add_f32_e32 v108,v108,v251
v_add_f32_e32 v109,v109,v255
mvva 100,132,44
v_add_f32_e32 v110,v110,v253
v_add_f32_e32 v111,v111,v242
mvva 100,128,36
mvva 104,128,0
mvva 104,132,8
mvva 104,144,16
mvva 104,148,24
mvva 108,148,56
mvva 108,144,48
mvva 108,132,40
tv 148,230,45056
mvva 108,128,32
tv 144,230,40960
s_waitcnt lgkmcnt(2)
mvva 124,136,32
s_xor_b32 s83,s59,8
tv 132,230,36864
s_mul_i32 s83,s83,12288
mvva 124,140,40
s_add_u32 s83,s83,4096
tv 128,230,32768
tv 130,230,34816
mvva 124,152,48
lv 48,244,83,0
tv 134,230,38912
mvva 124,156,56
tv 146,230,43008
mvva 120,156,24
lv 52,244,83,1024
tv 150,230,47104
mvva 120,152,16
mvva 120,140,8
lv 56,244,83,2048
mvva 120,136,0
mvva 116,136,36
lv 60,244,83,3072
mvva 116,140,44
mvva 116,152,52
s_xor_b32 s84,s59,4
mvva 116,156,60
s_mul_i32 s84,s84,12288
mvva 112,156,28
s_add_u32 s84,s84,8192
mvva 112,152,20
lv 80,244,84,0
mvva 112,140,12
tv 156,231,45056
mvva 112,136,4
lv 84,244,84,1024
tv 152,231,40960
s_waitcnt lgkmcnt(2)
mvvv 96,128,164
tv 140,231,36864
mvvv 96,132,172
lv 88,244,84,2048
tv 136,231,32768
tv 138,231,34816
mvvv 96,144,180
tv 142,231,38912
mvvv 96,148,188
lv 92,244,84,3072
tv 154,231,43008
mvvv 100,148,220
tv 158,231,47104
mvvv 100,144,212
mvvv 100,132,204
mvvv 100,128,196
mvvv 104,128,160
mvvv 104,132,168
mvvv 104,144,176
mvvv 104,148,184
mvvv 108,148,216
mvvv 108,144,208
mvvv 108,132,200
tv 148,230,61440
mvvv 108,128,192
tv 144,230,57344
s_waitcnt lgkmcnt(2)
mvvv 124,136,192
tv 132,230,53248
mvvv 124,140,200
tv 128,230,49152
tv 130,230,51200
mvvv 124,152,208
tv 134,230,55296
mvvv 124,156,216
tv 146,230,59392
mvvv 120,156,184
tv 150,230,63488
mvvv 120,152,176
mvvv 120,140,168
mvvv 120,136,160
mvvv 116,136,196
mvvv 116,140,204
mvvv 116,152,212
mvvv 116,156,220
mvvv 112,156,188
mvvv 112,152,180
mvvv 112,140,172
tv 156,231,61440
mvvv 112,136,164
tv 152,231,57344
s_waitcnt lgkmcnt(2)
mvva 96,128,196
tv 140,231,53248
mvva 96,132,204
tv 136,231,49152
tv 138,231,51200
mvva 96,144,212
tv 142,231,55296
mvva 96,148,220
tv 154,231,59392
mvva 100,148,252
tv 158,231,63488
mvva 100,144,244
mvva 100,132,236
mvva 100,128,228
mvva 104,128,192
mvva 104,132,200
mvva 104,144,208
mvva 104,148,216
s_waitcnt vmcnt(7)
v_max_u32_e32 v248,v48,v49
mvva 108,148,248
v_max3_u32 v248,v248,v50,v51
s_waitcnt vmcnt(6)
v_max3_u32 v248,v248,v52,v53
mvva 108,144,240
v_max3_u32 v248,v248,v54,v55
s_waitcnt vmcnt(5)
v_max3_u32 v248,v248,v56,v57
mvva 108,132,232
v_max3_u32 v248,v248,v58,v59
s_waitcnt vmcnt(4)
v_max3_u32 v248,v248,v60,v61
mvva 108,128,224
v_max3_u32 v248,v248,v62,v63
v_cmp_eq_u32_e32 vcc,-1,v248
s_waitcnt lgkmcnt(0)
mvva 124,136,224
s_cbranch_vccnz L_spx1o
L_sprx1o:
mvva 124,140,232
mvva 124,152,240
mvva 124,156,248
s_waitcnt vmcnt(3)
v_max_u32_e32 v248,v80,v81
mvva 120,156,216
v_max3_u32 v248,v248,v82,v83
s_waitcnt vmcnt(2)
v_max3_u32 v248,v248,v84,v85
mvva 120,152,208
v_max3_u32 v248,v248,v86,v87
s_waitcnt vmcnt(1)
v_max3_u32 v248,v248,v88,v89
mvva 120,140,200
v_max3_u32 v248,v248,v90,v91
s_waitcnt vmcnt(0)
v_max3_u32 v248,v248,v92,v93
mvva 120,136,192
v_max3_u32 v248,v248,v94,v95
v_cmp_eq_u32_e32 vcc,-1,v248
mvva 116,136,228
s_cbranch_vccnz L_spx2o
L_sprx2o:
mvva 116,140,236
mvva 116,152,244
mvva 116,156,252
mvva 112,156,220
mvva 112,152,212
mvva 112,140,204
mvva 112,136,196
mvvv 108,238,60
mvvv 108,238,92
mvvv 124,236,60
mvvv 124,236,92
mvvv 104,238,56
mvvv 104,238,88
mvvv 120,236,56
mvvv 120,236,88
mvvv 100,238,52
mvvv 100,238,84
cv 108,108,109
cv 109,110,111
cv 110,124,125
cv 111,126,127
cv 104,104,105
v_permlane16_swap_b32_e32 v108,v110
mvvv 116,236,52
v_permlane16_swap_b32_e32 v109,v111
cv 105,106,107
cv 106,120,121
cv 107,122,123
mvvv 116,236,84
buffer_store_dwordx4 v[108:111],v243,s[44:47],s73 offen sc0 sc1
v_permlane16_swap_b32_e32 v104,v106
v_permlane16_swap_b32_e32 v105,v107
mvvv 96,238,48
cv 100,100,101
cv 101,102,103
mvvv 96,238,80
buffer_store_dwordx4 v[104:107],v243,s[44:47],s72 offen sc0 sc1
cv 102,116,117
cv 103,118,119
mvvv 112,236,48
v_permlane16_swap_b32_e32 v100,v102
v_permlane16_swap_b32_e32 v101,v103
mvvv 112,236,80
buffer_store_dwordx4 v[100:103],v243,s[44:47],s71 offen sc0 sc1
cv 96,96,97
cv 97,98,99
s_nop 4
cv 98,112,113
cv 99,114,115
s_nop 0
v_permlane16_swap_b32_e32 v96,v98
v_permlane16_swap_b32_e32 v97,v99
buffer_store_dwordx4 v[96:99],v243,s[44:47],s70 offen sc0 sc1
L_oddx:
s_endpgm
L_k_ec1:
buffer_store_dwordx4 a[252:255],v244,s[8:11],s35 offen offset:1024 sc0 sc1
s_branch L_k_ecr1
L_q_ack:
s_mov_b32 s86,65536
L_q_ack2:
s_sleep 8
buffer_load_dword v246,off,s[8:11],s35 offset:1024 sc1
s_waitcnt vmcnt(0)
v_readfirstlane_b32 s87,v246
s_nop 0
s_cmp_eq_u32 s87,s32
s_cbranch_scc1 L_q_acked
s_sub_u32 s86,s86,1
s_cbranch_scc0 L_q_ack2
s_branch L_q_acked
L_q_spin:
s_mov_b32 s6,65536
L_q_spin1:
s_sleep 8
buffer_load_dword v248,v250,s[8:11],0 offen sc1
s_or_b32 s7,%1,s12
s_cbranch_scc1 L_q_spin2
buffer_load_dwordx4 a[252:255],v244,s[8:11],s35 offen sc1
s_waitcnt vmcnt(0)
buffer_store_dwordx4 a[252:255],v244,s[8:11],s35 offen offset:1024 sc0 sc1
L_q_spin2:
s_waitcnt vmcnt(0)
v_and_b32_e32 v248,v242,v248
v_cmp_ne_u32_e64 s[100:101],v243,v248
s_nop 1
s_cmp_eq_u64 s[100:101],0
s_cbranch_scc1 L_q_ok
s_sub_u32 s6,s6,1
s_cbranch_scc0 L_q_spin1
s_branch L_q_ok
L_bx:
s_mov_b32 s87,65536
L_bx1:
s_sleep 2
buffer_load_dwordx4 v[176:179],v244,s[8:11],s98 offen sc1
buffer_load_dwordx2 v[168:169],v253,s[8:11],s98 offen offset:1024 sc1
buffer_load_dwordx2 v[172:173],v253,s[8:11],s98 offen offset:1536 sc1
s_waitcnt vmcnt(0)
v_max3_u32 v180,v176,v177,v178
v_max3_u32 v180,v180,v179,v168
v_max3_u32 v180,v180,v169,v172
v_max_u32_e32 v180,v180,v173
v_cmp_eq_u32_e64 s[40:41],-1,v180
s_nop 1
s_cmp_eq_u64 s[40:41],0
s_cbranch_scc1 L_bxr
s_sub_u32 s87,s87,1
s_cbranch_scc0 L_bx1
s_branch L_bxr
L_pend2:
s_nop 7
v_add_f32_e32 v226,v152,v153
v_add_f32_e32 v227,v156,v157
v_subrev_u32_e32 v236,0x23800000,v226
v_subrev_u32_e32 v237,0x23800000,v227
v_max_u32_e32 v236,v236,v237
v_cmp_lt_u32_e64 s[40:41],s48,v236
v_rcp_f32_e32 v226,v226
v_rcp_f32_e32 v227,v227
s_add_u32 s86,s13,4096
v_mov_b32_e32 v236,0
v_mov_b32_e32 v237,0
v_mov_b32_e32 v238,0
v_mov_b32_e32 v239,0
v_mov_b64_e32 v[152:153],0
v_mov_b64_e32 v[156:157],0
v_mul_f32_e32 v226,v242,v226
v_mul_f32_e32 v227,v243,v227
v_add_u32_e32 v225,s94,v225
v_pk_mul_f32 v[160:161],v[160:161],v[226:227] op_sel:[0,0] op_sel_hi:[1,0]
v_pk_mul_f32 v[162:163],v[162:163],v[226:227] op_sel:[0,0] op_sel_hi:[1,0]
v_pk_mul_f32 v[164:165],v[164:165],v[226:227] op_sel:[0,0] op_sel_hi:[1,0]
v_pk_mul_f32 v[166:167],v[166:167],v[226:227] op_sel:[0,0] op_sel_hi:[1,0]
cv 160,160,161
cv 161,162,163
cv 162,164,165
cv 163,166,167
buffer_store_dwordx4 v[160:163],v244,s[8:11],s13 offen offset:0 sc0 sc1
v_pk_mul_f32 v[168:169],v[168:169],v[226:227] op_sel:[0,0] op_sel_hi:[1,0]
v_pk_mul_f32 v[170:171],v[170:171],v[226:227] op_sel:[0,0] op_sel_hi:[1,0]
v_pk_mul_f32 v[172:173],v[172:173],v[226:227] op_sel:[0,0] op_sel_hi:[1,0]
v_pk_mul_f32 v[174:175],v[174:175],v[226:227] op_sel:[0,0] op_sel_hi:[1,0]
cv 168,168,169
cv 169,170,171
cv 170,172,173
cv 171,174,175
buffer_store_dwordx4 v[168:171],v244,s[8:11],s13 offen offset:2048 sc0 sc1
v_pk_mul_f32 v[176:177],v[176:177],v[226:227] op_sel:[0,0] op_sel_hi:[1,0]
v_pk_mul_f32 v[178:179],v[178:179],v[226:227] op_sel:[0,0] op_sel_hi:[1,0]
v_pk_mul_f32 v[180:181],v[180:181],v[226:227] op_sel:[0,0] op_sel_hi:[1,0]
v_pk_mul_f32 v[182:183],v[182:183],v[226:227] op_sel:[0,0] op_sel_hi:[1,0]
cv 176,176,177
cv 177,178,179
cv 178,180,181
v_mfma_f32_32x32x16_bf16 v[160:175],v[236:239],v[236:239],0
cv 179,182,183
buffer_store_dwordx4 v[176:179],v244,s[8:11],s86 offen offset:0 sc0 sc1
v_pk_mul_f32 v[184:185],v[184:185],v[226:227] op_sel:[0,0] op_sel_hi:[1,0]
v_pk_mul_f32 v[186:187],v[186:187],v[226:227] op_sel:[0,0] op_sel_hi:[1,0]
v_pk_mul_f32 v[188:189],v[188:189],v[226:227] op_sel:[0,0] op_sel_hi:[1,0]
v_pk_mul_f32 v[190:191],v[190:191],v[226:227] op_sel:[0,0] op_sel_hi:[1,0]
cv 184,184,185
cv 185,186,187
cv 186,188,189
cv 187,190,191
buffer_store_dwordx4 v[184:187],v244,s[8:11],s86 offen offset:2048 sc0 sc1
v_pk_mul_f32 v[192:193],v[192:193],v[226:227] op_sel:[0,1] op_sel_hi:[1,1]
v_pk_mul_f32 v[194:195],v[194:195],v[226:227] op_sel:[0,1] op_sel_hi:[1,1]
v_pk_mul_f32 v[196:197],v[196:197],v[226:227] op_sel:[0,1] op_sel_hi:[1,1]
v_pk_mul_f32 v[198:199],v[198:199],v[226:227] op_sel:[0,1] op_sel_hi:[1,1]
cv 192,192,193
cv 193,194,195
cv 194,196,197
v_mfma_f32_32x32x16_bf16 v[176:191],v[236:239],v[236:239],0
cv 195,198,199
buffer_store_dwordx4 v[192:195],v244,s[8:11],s13 offen offset:1024 sc0 sc1
v_pk_mul_f32 v[200:201],v[200:201],v[226:227] op_sel:[0,1] op_sel_hi:[1,1]
v_pk_mul_f32 v[202:203],v[202:203],v[226:227] op_sel:[0,1] op_sel_hi:[1,1]
v_pk_mul_f32 v[204:205],v[204:205],v[226:227] op_sel:[0,1] op_sel_hi:[1,1]
v_pk_mul_f32 v[206:207],v[206:207],v[226:227] op_sel:[0,1] op_sel_hi:[1,1]
cv 200,200,201
cv 201,202,203
cv 202,204,205
cv 203,206,207
buffer_store_dwordx4 v[200:203],v244,s[8:11],s13 offen offset:3072 sc0 sc1
v_pk_mul_f32 v[208:209],v[208:209],v[226:227] op_sel:[0,1] op_sel_hi:[1,1]
v_pk_mul_f32 v[210:211],v[210:211],v[226:227] op_sel:[0,1] op_sel_hi:[1,1]
v_pk_mul_f32 v[212:213],v[212:213],v[226:227] op_sel:[0,1] op_sel_hi:[1,1]
v_pk_mul_f32 v[214:215],v[214:215],v[226:227] op_sel:[0,1] op_sel_hi:[1,1]
cv 208,208,209
cv 209,210,211
cv 210,212,213
v_mfma_f32_32x32x16_bf16 v[192:207],v[236:239],v[236:239],0
cv 211,214,215
buffer_store_dwordx4 v[208:211],v244,s[8:11],s86 offen offset:1024 sc0 sc1
v_pk_mul_f32 v[216:217],v[216:217],v[226:227] op_sel:[0,1] op_sel_hi:[1,1]
v_pk_mul_f32 v[218:219],v[218:219],v[226:227] op_sel:[0,1] op_sel_hi:[1,1]
v_pk_mul_f32 v[220:221],v[220:221],v[226:227] op_sel:[0,1] op_sel_hi:[1,1]
v_pk_mul_f32 v[222:223],v[222:223],v[226:227] op_sel:[0,1] op_sel_hi:[1,1]
cv 216,216,217
cv 217,218,219
cv 218,220,221
cv 219,222,223
buffer_store_dwordx4 v[216:219],v244,s[8:11],s86 offen offset:3072 sc0 sc1
s_nop 1
v_mfma_f32_32x32x16_bf16 v[208:223],v[236:239],v[236:239],0
s_nop 7
s_nop 1
s_cmp_eq_u32 s84,4
s_cbranch_scc1 L_ret_eb4
s_cmp_lg_u32 s80,s81
s_branch L_ret_eb5
L_redo:
s_waitcnt vmcnt(0) lgkmcnt(0)
s_nop 15
s_nop 3
s_load_dwordx2 s[76:77],%0,0
s_load_dwordx2 s[80:81],%0,16
s_mov_b32 s78,4194304
s_mov_b32 s79,131072
s_mov_b32 s82,8388608
s_mov_b32 s83,131072
s_mov_b32 s86,0
s_waitcnt lgkmcnt(0)
L_rd_t:
s_nop 15
s_nop 3
s_cmp_lt_u32 s86,2
s_cselect_b32 s88,s23,s22
s_cselect_b32 s95,s13,s14
s_and_b32 s89,s86,1
s_lshl_b32 s90,s89,10
s_add_u32 s95,s95,s90
s_lshl_b32 s89,s89,6
s_add_u32 s88,s88,s89
s_lshl_b32 s89,s12,4
s_add_u32 s88,s88,s89
v_bfe_u32 v213,v244,4,4
v_lshrrev_b32_e32 v214,8,v244
v_add_u32_e32 v210,s88,v213
v_lshlrev_b32_e32 v208,11,v210
v_lshl_add_u32 v208,v214,4,v208
v_lshlrev_b32_e32 v209,16,v214
v_lshl_add_u32 v209,v213,1,v209
s_lshl_b32 s89,s18,8
v_add_u32_e32 v209,s89,v209
v_lshlrev_b32_e32 v214,2,v214
v_sub_u32_e32 v210,v210,v214
v_accvgpr_write_b32 a192,0
v_accvgpr_write_b32 a193,0
v_accvgpr_write_b32 a194,0
v_accvgpr_write_b32 a195,0
v_accvgpr_write_b32 a196,0
v_accvgpr_write_b32 a197,0
v_accvgpr_write_b32 a198,0
v_accvgpr_write_b32 a199,0
v_accvgpr_write_b32 a200,0
v_accvgpr_write_b32 a201,0
v_accvgpr_write_b32 a202,0
v_accvgpr_write_b32 a203,0
v_accvgpr_write_b32 a204,0
v_accvgpr_write_b32 a205,0
v_accvgpr_write_b32 a206,0
v_accvgpr_write_b32 a207,0
v_accvgpr_write_b32 a208,0
v_accvgpr_write_b32 a209,0
v_accvgpr_write_b32 a210,0
v_accvgpr_write_b32 a211,0
v_accvgpr_write_b32 a212,0
v_accvgpr_write_b32 a213,0
v_accvgpr_write_b32 a214,0
v_accvgpr_write_b32 a215,0
v_accvgpr_write_b32 a216,0
v_accvgpr_write_b32 a217,0
v_accvgpr_write_b32 a218,0
v_accvgpr_write_b32 a219,0
v_accvgpr_write_b32 a220,0
v_accvgpr_write_b32 a221,0
v_accvgpr_write_b32 a222,0
v_accvgpr_write_b32 a223,0
s_mov_b32 s87,0
L_rd_q:
s_nop 15
s_nop 3
s_lshl_b32 s89,s87,6
s_lshl_b32 s90,s87,18
buffer_load_dwordx4 v[200:203],v208,s[76:79],s89 offen
buffer_load_ushort v160,v209,s[80:83],s90 offen offset:0
buffer_load_ushort v164,v209,s[80:83],s90 offen offset:32
buffer_load_ushort v168,v209,s[80:83],s90 offen offset:64
buffer_load_ushort v172,v209,s[80:83],s90 offen offset:96
buffer_load_ushort v176,v209,s[80:83],s90 offen offset:128
buffer_load_ushort v180,v209,s[80:83],s90 offen offset:160
)"
R"(buffer_load_ushort v184,v209,s[80:83],s90 offen offset:192
buffer_load_ushort v188,v209,s[80:83],s90 offen offset:224
s_add_u32 s90,s90,8192
buffer_load_ushort v192,v209,s[80:83],s90 offen offset:0
buffer_load_ushort v193,v209,s[80:83],s90 offen offset:32
buffer_load_ushort v194,v209,s[80:83],s90 offen offset:64
buffer_load_ushort v195,v209,s[80:83],s90 offen offset:96
buffer_load_ushort v196,v209,s[80:83],s90 offen offset:128
buffer_load_ushort v197,v209,s[80:83],s90 offen offset:160
buffer_load_ushort v198,v209,s[80:83],s90 offen offset:192
buffer_load_ushort v199,v209,s[80:83],s90 offen offset:224
s_add_u32 s90,s90,8192
s_waitcnt vmcnt(0)
v_lshl_or_b32 v160,v192,16,v160
v_lshl_or_b32 v164,v193,16,v164
v_lshl_or_b32 v168,v194,16,v168
v_lshl_or_b32 v172,v195,16,v172
v_lshl_or_b32 v176,v196,16,v176
v_lshl_or_b32 v180,v197,16,v180
v_lshl_or_b32 v184,v198,16,v184
v_lshl_or_b32 v188,v199,16,v188
buffer_load_ushort v161,v209,s[80:83],s90 offen offset:0
buffer_load_ushort v165,v209,s[80:83],s90 offen offset:32
buffer_load_ushort v169,v209,s[80:83],s90 offen offset:64
buffer_load_ushort v173,v209,s[80:83],s90 offen offset:96
buffer_load_ushort v177,v209,s[80:83],s90 offen offset:128
buffer_load_ushort v181,v209,s[80:83],s90 offen offset:160
buffer_load_ushort v185,v209,s[80:83],s90 offen offset:192
buffer_load_ushort v189,v209,s[80:83],s90 offen offset:224
s_add_u32 s90,s90,8192
buffer_load_ushort v192,v209,s[80:83],s90 offen offset:0
buffer_load_ushort v193,v209,s[80:83],s90 offen offset:32
buffer_load_ushort v194,v209,s[80:83],s90 offen offset:64
buffer_load_ushort v195,v209,s[80:83],s90 offen offset:96
buffer_load_ushort v196,v209,s[80:83],s90 offen offset:128
buffer_load_ushort v197,v209,s[80:83],s90 offen offset:160
buffer_load_ushort v198,v209,s[80:83],s90 offen offset:192
buffer_load_ushort v199,v209,s[80:83],s90 offen offset:224
s_add_u32 s90,s90,8192
s_waitcnt vmcnt(0)
v_lshl_or_b32 v161,v192,16,v161
v_lshl_or_b32 v165,v193,16,v165
v_lshl_or_b32 v169,v194,16,v169
v_lshl_or_b32 v173,v195,16,v173
v_lshl_or_b32 v177,v196,16,v177
v_lshl_or_b32 v181,v197,16,v181
v_lshl_or_b32 v185,v198,16,v185
v_lshl_or_b32 v189,v199,16,v189
buffer_load_ushort v162,v209,s[80:83],s90 offen offset:0
buffer_load_ushort v166,v209,s[80:83],s90 offen offset:32
buffer_load_ushort v170,v209,s[80:83],s90 offen offset:64
buffer_load_ushort v174,v209,s[80:83],s90 offen offset:96
buffer_load_ushort v178,v209,s[80:83],s90 offen offset:128
buffer_load_ushort v182,v209,s[80:83],s90 offen offset:160
buffer_load_ushort v186,v209,s[80:83],s90 offen offset:192
buffer_load_ushort v190,v209,s[80:83],s90 offen offset:224
s_add_u32 s90,s90,8192
buffer_load_ushort v192,v209,s[80:83],s90 offen offset:0
buffer_load_ushort v193,v209,s[80:83],s90 offen offset:32
buffer_load_ushort v194,v209,s[80:83],s90 offen offset:64
buffer_load_ushort v195,v209,s[80:83],s90 offen offset:96
buffer_load_ushort v196,v209,s[80:83],s90 offen offset:128
buffer_load_ushort v197,v209,s[80:83],s90 offen offset:160
buffer_load_ushort v198,v209,s[80:83],s90 offen offset:192
buffer_load_ushort v199,v209,s[80:83],s90 offen offset:224
s_add_u32 s90,s90,8192
s_waitcnt vmcnt(0)
v_lshl_or_b32 v162,v192,16,v162
v_lshl_or_b32 v166,v193,16,v166
v_lshl_or_b32 v170,v194,16,v170
v_lshl_or_b32 v174,v195,16,v174
v_lshl_or_b32 v178,v196,16,v178
v_lshl_or_b32 v182,v197,16,v182
v_lshl_or_b32 v186,v198,16,v186
v_lshl_or_b32 v190,v199,16,v190
buffer_load_ushort v163,v209,s[80:83],s90 offen offset:0
buffer_load_ushort v167,v209,s[80:83],s90 offen offset:32
buffer_load_ushort v171,v209,s[80:83],s90 offen offset:64
buffer_load_ushort v175,v209,s[80:83],s90 offen offset:96
buffer_load_ushort v179,v209,s[80:83],s90 offen offset:128
buffer_load_ushort v183,v209,s[80:83],s90 offen offset:160
buffer_load_ushort v187,v209,s[80:83],s90 offen offset:192
buffer_load_ushort v191,v209,s[80:83],s90 offen offset:224
s_add_u32 s90,s90,8192
buffer_load_ushort v192,v209,s[80:83],s90 offen offset:0
buffer_load_ushort v193,v209,s[80:83],s90 offen offset:32
buffer_load_ushort v194,v209,s[80:83],s90 offen offset:64
buffer_load_ushort v195,v209,s[80:83],s90 offen offset:96
buffer_load_ushort v196,v209,s[80:83],s90 offen offset:128
buffer_load_ushort v197,v209,s[80:83],s90 offen offset:160
buffer_load_ushort v198,v209,s[80:83],s90 offen offset:192
buffer_load_ushort v199,v209,s[80:83],s90 offen offset:224
s_add_u32 s90,s90,8192
s_waitcnt vmcnt(0)
v_lshl_or_b32 v163,v192,16,v163
v_lshl_or_b32 v167,v193,16,v167
v_lshl_or_b32 v171,v194,16,v171
v_lshl_or_b32 v175,v195,16,v175
v_lshl_or_b32 v179,v196,16,v179
v_lshl_or_b32 v183,v197,16,v183
v_lshl_or_b32 v187,v198,16,v187
v_lshl_or_b32 v191,v199,16,v191
mavv 192,160,200
mavv 196,164,200
mavv 200,168,200
mavv 204,172,200
mavv 208,176,200
mavv 212,180,200
mavv 216,184,200
mavv 220,188,200
s_add_u32 s87,s87,1
s_cmp_lt_u32 s87,32
s_cbranch_scc1 L_rd_q
s_nop 15
s_nop 3
v_accvgpr_read_b32 v192,a192
v_accvgpr_read_b32 v193,a193
v_accvgpr_read_b32 v194,a194
v_accvgpr_read_b32 v195,a195
v_accvgpr_read_b32 v196,a196
v_accvgpr_read_b32 v197,a197
v_accvgpr_read_b32 v198,a198
v_accvgpr_read_b32 v199,a199
v_mul_f32_e32 v192,0x3e0293ee,v192
v_mul_f32_e32 v193,0x3e0293ee,v193
v_mul_f32_e32 v194,0x3e0293ee,v194
v_mul_f32_e32 v195,0x3e0293ee,v195
v_mul_f32_e32 v196,0x3e0293ee,v196
v_mul_f32_e32 v197,0x3e0293ee,v197
v_mul_f32_e32 v198,0x3e0293ee,v198
v_mul_f32_e32 v199,0x3e0293ee,v199
cv 192,192,193
cv 193,194,195
cv 194,196,197
cv 195,198,199
v_accvgpr_write_b32 a224,v192
v_accvgpr_write_b32 a225,v193
v_accvgpr_write_b32 a226,v194
v_accvgpr_write_b32 a227,v195
v_accvgpr_read_b32 v192,a200
v_accvgpr_read_b32 v193,a201
v_accvgpr_read_b32 v194,a202
v_accvgpr_read_b32 v195,a203
v_accvgpr_read_b32 v196,a204
v_accvgpr_read_b32 v197,a205
v_accvgpr_read_b32 v198,a206
v_accvgpr_read_b32 v199,a207
v_mul_f32_e32 v192,0x3e0293ee,v192
v_mul_f32_e32 v193,0x3e0293ee,v193
v_mul_f32_e32 v194,0x3e0293ee,v194
v_mul_f32_e32 v195,0x3e0293ee,v195
v_mul_f32_e32 v196,0x3e0293ee,v196
v_mul_f32_e32 v197,0x3e0293ee,v197
v_mul_f32_e32 v198,0x3e0293ee,v198
v_mul_f32_e32 v199,0x3e0293ee,v199
cv 192,192,193
cv 193,194,195
cv 194,196,197
cv 195,198,199
v_accvgpr_write_b32 a228,v192
v_accvgpr_write_b32 a229,v193
v_accvgpr_write_b32 a230,v194
v_accvgpr_write_b32 a231,v195
v_accvgpr_read_b32 v192,a208
v_accvgpr_read_b32 v193,a209
v_accvgpr_read_b32 v194,a210
v_accvgpr_read_b32 v195,a211
v_accvgpr_read_b32 v196,a212
v_accvgpr_read_b32 v197,a213
v_accvgpr_read_b32 v198,a214
v_accvgpr_read_b32 v199,a215
v_mul_f32_e32 v192,0x3e0293ee,v192
v_mul_f32_e32 v193,0x3e0293ee,v193
v_mul_f32_e32 v194,0x3e0293ee,v194
v_mul_f32_e32 v195,0x3e0293ee,v195
v_mul_f32_e32 v196,0x3e0293ee,v196
v_mul_f32_e32 v197,0x3e0293ee,v197
v_mul_f32_e32 v198,0x3e0293ee,v198
v_mul_f32_e32 v199,0x3e0293ee,v199
cv 192,192,193
cv 193,194,195
cv 194,196,197
cv 195,198,199
v_accvgpr_write_b32 a232,v192
v_accvgpr_write_b32 a233,v193
v_accvgpr_write_b32 a234,v194
v_accvgpr_write_b32 a235,v195
v_accvgpr_read_b32 v192,a216
v_accvgpr_read_b32 v193,a217
v_accvgpr_read_b32 v194,a218
v_accvgpr_read_b32 v195,a219
v_accvgpr_read_b32 v196,a220
v_accvgpr_read_b32 v197,a221
v_accvgpr_read_b32 v198,a222
v_accvgpr_read_b32 v199,a223
v_mul_f32_e32 v192,0x3e0293ee,v192
v_mul_f32_e32 v193,0x3e0293ee,v193
v_mul_f32_e32 v194,0x3e0293ee,v194
v_mul_f32_e32 v195,0x3e0293ee,v195
v_mul_f32_e32 v196,0x3e0293ee,v196
v_mul_f32_e32 v197,0x3e0293ee,v197
v_mul_f32_e32 v198,0x3e0293ee,v198
v_mul_f32_e32 v199,0x3e0293ee,v199
cv 192,192,193
cv 193,194,195
cv 194,196,197
cv 195,198,199
v_accvgpr_write_b32 a236,v192
v_accvgpr_write_b32 a237,v193
v_accvgpr_write_b32 a238,v194
v_accvgpr_write_b32 a239,v195
v_mov_b32_e32 v160,0
v_mov_b32_e32 v161,0
v_mov_b32_e32 v162,0
v_mov_b32_e32 v163,0
v_mov_b32_e32 v164,0
v_mov_b32_e32 v165,0
v_mov_b32_e32 v166,0
v_mov_b32_e32 v167,0
v_mov_b32_e32 v168,0
v_mov_b32_e32 v169,0
v_mov_b32_e32 v170,0
v_mov_b32_e32 v171,0
v_mov_b32_e32 v172,0
v_mov_b32_e32 v173,0
v_mov_b32_e32 v174,0
v_mov_b32_e32 v175,0
v_mov_b32_e32 v176,0
v_mov_b32_e32 v177,0
v_mov_b32_e32 v178,0
v_mov_b32_e32 v179,0
v_mov_b32_e32 v180,0
v_mov_b32_e32 v181,0
v_mov_b32_e32 v182,0
v_mov_b32_e32 v183,0
v_mov_b32_e32 v184,0
v_mov_b32_e32 v185,0
v_mov_b32_e32 v186,0
v_mov_b32_e32 v187,0
v_mov_b32_e32 v188,0
v_mov_b32_e32 v189,0
v_mov_b32_e32 v190,0
v_mov_b32_e32 v191,0
v_mov_b32_e32 v216,0xf149f2ca
v_mov_b32_e32 v211,v216
v_mov_b32_e32 v212,0
s_lshr_b32 s91,s88,5
s_mov_b32 s87,0
L_rd_a:
s_nop 15
s_nop 3
s_mul_i32 s89,s18,524288
s_lshl_b32 s90,s87,13
s_add_u32 s89,s89,s90
s_add_u32 s90,s89,16777216
la 64,244,90,0
la 68,244,90,1024
la 72,244,90,2048
la 76,244,90,3072
s_add_u32 s90,s89,16781312
la 80,244,90,0
la 84,244,90,1024
la 88,244,90,2048
la 92,244,90,3072
s_add_u32 s90,s89,33554432
la 96,244,90,0
la 100,244,90,1024
la 104,244,90,2048
la 108,244,90,3072
s_add_u32 s90,s89,33558528
la 112,244,90,0
la 116,244,90,1024
la 120,244,90,2048
la 124,244,90,3072
s_waitcnt vmcnt(0)
zvaa 192,64,224
mvaa 192,72,228
mvaa 192,80,232
mvaa 192,88,236
zvaa 196,68,224
mvaa 196,76,228
mvaa 196,84,232
mvaa 196,92,236
v_cmp_gt_i32_e32 vcc,0,v210
s_nop 2
v_cndmask_b32_e32 v192,v192,v216,vcc
v_cmp_gt_i32_e32 vcc,1,v210
s_nop 1
v_cndmask_b32_e32 v193,v193,v216,vcc
v_cmp_gt_i32_e32 vcc,2,v210
s_nop 1
v_cndmask_b32_e32 v194,v194,v216,vcc
v_cmp_gt_i32_e32 vcc,3,v210
s_nop 1
v_cndmask_b32_e32 v195,v195,v216,vcc
v_cmp_gt_i32_e32 vcc,16,v210
s_nop 1
v_cndmask_b32_e32 v196,v196,v216,vcc
v_cmp_gt_i32_e32 vcc,17,v210
s_nop 1
v_cndmask_b32_e32 v197,v197,v216,vcc
v_cmp_gt_i32_e32 vcc,18,v210
s_nop 1
v_cndmask_b32_e32 v198,v198,v216,vcc
v_cmp_gt_i32_e32 vcc,19,v210
s_nop 1
v_cndmask_b32_e32 v199,v199,v216,vcc
v_max3_f32 v213,v192,v193,v194
v_max3_f32 v214,v195,v196,v197
v_max3_f32 v213,v213,v198,v199
v_max_f32_e32 v213,v213,v214
v_mov_b32_e32 v214,v213
s_nop 1
v_permlane16_swap_b32_e32 v213,v214
v_max_f32_e32 v213,v213,v214
v_mov_b32_e32 v214,v213
s_nop 1
v_permlane32_swap_b32_e32 v213,v214
v_max_f32_e32 v213,v213,v214
v_max_f32_e32 v213,v213,v211
v_sub_f32_e32 v215,v211,v213
ex 215
v_mov_b32_e32 v211,v213
v_sub_f32_e32 v192,v192,v211
v_sub_f32_e32 v193,v193,v211
v_sub_f32_e32 v194,v194,v211
v_sub_f32_e32 v195,v195,v211
v_sub_f32_e32 v196,v196,v211
v_sub_f32_e32 v197,v197,v211
v_sub_f32_e32 v198,v198,v211
v_sub_f32_e32 v199,v199,v211
ex 192
ex 193
ex 194
ex 195
ex 196
ex 197
ex 198
ex 199
v_mul_f32_e32 v212,v215,v212
v_add_f32_e32 v212,v212,v192
v_add_f32_e32 v212,v212,v193
v_add_f32_e32 v212,v212,v194
v_add_f32_e32 v212,v212,v195
v_add_f32_e32 v212,v212,v196
v_add_f32_e32 v212,v212,v197
v_add_f32_e32 v212,v212,v198
v_add_f32_e32 v212,v212,v199
cv 192,192,193
cv 193,194,195
cv 194,196,197
cv 195,198,199
v_mul_f32_e32 v160,v215,v160
v_mul_f32_e32 v161,v215,v161
v_mul_f32_e32 v162,v215,v162
v_mul_f32_e32 v163,v215,v163
v_mul_f32_e32 v164,v215,v164
v_mul_f32_e32 v165,v215,v165
v_mul_f32_e32 v166,v215,v166
v_mul_f32_e32 v167,v215,v167
v_mul_f32_e32 v168,v215,v168
v_mul_f32_e32 v169,v215,v169
v_mul_f32_e32 v170,v215,v170
v_mul_f32_e32 v171,v215,v171
v_mul_f32_e32 v172,v215,v172
v_mul_f32_e32 v173,v215,v173
v_mul_f32_e32 v174,v215,v174
v_mul_f32_e32 v175,v215,v175
v_mul_f32_e32 v176,v215,v176
v_mul_f32_e32 v177,v215,v177
v_mul_f32_e32 v178,v215,v178
v_mul_f32_e32 v179,v215,v179
v_mul_f32_e32 v180,v215,v180
v_mul_f32_e32 v181,v215,v181
v_mul_f32_e32 v182,v215,v182
v_mul_f32_e32 v183,v215,v183
v_mul_f32_e32 v184,v215,v184
v_mul_f32_e32 v185,v215,v185
v_mul_f32_e32 v186,v215,v186
v_mul_f32_e32 v187,v215,v187
v_mul_f32_e32 v188,v215,v188
v_mul_f32_e32 v189,v215,v189
v_mul_f32_e32 v190,v215,v190
v_mul_f32_e32 v191,v215,v191
mvav 160,96,192
mvav 164,100,192
mvav 168,104,192
mvav 172,108,192
mvav 176,112,192
mvav 180,116,192
mvav 184,120,192
mvav 188,124,192
v_subrev_u32_e32 v210,32,v210
s_add_u32 s87,s87,1
s_cmp_le_u32 s87,s91
s_cbranch_scc1 L_rd_a
s_nop 15
s_nop 3
s_lshl_b32 s89,s88,2
s_add_u32 s89,s89,83886080
v_bfe_u32 v213,v244,2,6
buffer_load_dword v215,v213,s[8:11],s89 offen sc1
v_mov_b32_e32 v214,v212
s_nop 1
v_permlane16_swap_b32_e32 v212,v214
v_add_f32_e32 v212,v212,v214
v_mov_b32_e32 v214,v212
s_nop 1
v_permlane32_swap_b32_e32 v212,v214
v_add_f32_e32 v212,v212,v214
v_rcp_f32_e32 v212,v212
s_waitcnt vmcnt(0)
v_mul_f32_e32 v215,v215,v212
v_mul_f32_e32 v160,v215,v160
v_mul_f32_e32 v161,v215,v161
v_mul_f32_e32 v162,v215,v162
v_mul_f32_e32 v163,v215,v163
v_mul_f32_e32 v164,v215,v164
v_mul_f32_e32 v165,v215,v165
v_mul_f32_e32 v166,v215,v166
v_mul_f32_e32 v167,v215,v167
v_mul_f32_e32 v168,v215,v168
v_mul_f32_e32 v169,v215,v169
v_mul_f32_e32 v170,v215,v170
v_mul_f32_e32 v171,v215,v171
v_mul_f32_e32 v172,v215,v172
v_mul_f32_e32 v173,v215,v173
v_mul_f32_e32 v174,v215,v174
v_mul_f32_e32 v175,v215,v175
v_mul_f32_e32 v176,v215,v176
v_mul_f32_e32 v177,v215,v177
v_mul_f32_e32 v178,v215,v178
v_mul_f32_e32 v179,v215,v179
v_mul_f32_e32 v180,v215,v180
v_mul_f32_e32 v181,v215,v181
v_mul_f32_e32 v182,v215,v182
v_mul_f32_e32 v183,v215,v183
v_mul_f32_e32 v184,v215,v184
v_mul_f32_e32 v185,v215,v185
v_mul_f32_e32 v186,v215,v186
v_mul_f32_e32 v187,v215,v187
v_mul_f32_e32 v188,v215,v188
v_mul_f32_e32 v189,v215,v189
v_mul_f32_e32 v190,v215,v190
v_mul_f32_e32 v191,v215,v191
s_add_u32 s89,s95,4096
cv 160,160,161
cv 161,162,163
cv 162,164,165
cv 163,166,167
buffer_store_dwordx4 v[160:163],v244,s[8:11],s95 offen offset:0 sc0 sc1
cv 168,168,169
cv 169,170,171
cv 170,172,173
cv 171,174,175
buffer_store_dwordx4 v[168:171],v244,s[8:11],s95 offen offset:2048 sc0 sc1
cv 176,176,177
cv 177,178,179
cv 178,180,181
cv 179,182,183
buffer_store_dwordx4 v[176:179],v244,s[8:11],s89 offen offset:0 sc0 sc1
cv 184,184,185
cv 185,186,187
cv 186,188,189
cv 187,190,191
buffer_store_dwordx4 v[184:187],v244,s[8:11],s89 offen offset:2048 sc0 sc1
s_waitcnt vmcnt(0)
v_mov_b32_e32 v192,v160
v_accvgpr_read_b32 v160,a240
v_accvgpr_write_b32 a240,v192
v_mov_b32_e32 v193,v161
v_accvgpr_read_b32 v161,a241
v_accvgpr_write_b32 a241,v193
v_mov_b32_e32 v194,v162
v_accvgpr_read_b32 v162,a242
v_accvgpr_write_b32 a242,v194
v_mov_b32_e32 v195,v163
v_accvgpr_read_b32 v163,a243
v_accvgpr_write_b32 a243,v195
v_mov_b32_e32 v200,v168
v_accvgpr_read_b32 v168,a244
v_accvgpr_write_b32 a244,v200
v_mov_b32_e32 v201,v169
v_accvgpr_read_b32 v169,a245
v_accvgpr_write_b32 a245,v201
v_mov_b32_e32 v202,v170
v_accvgpr_read_b32 v170,a246
v_accvgpr_write_b32 a246,v202
v_mov_b32_e32 v203,v171
v_accvgpr_read_b32 v171,a247
v_accvgpr_write_b32 a247,v203
v_mov_b32_e32 v208,v176
v_accvgpr_read_b32 v176,a248
v_accvgpr_write_b32 a248,v208
v_mov_b32_e32 v209,v177
v_accvgpr_read_b32 v177,a249
v_accvgpr_write_b32 a249,v209
v_mov_b32_e32 v210,v178
v_accvgpr_read_b32 v178,a250
v_accvgpr_write_b32 a250,v210
v_mov_b32_e32 v211,v179
v_accvgpr_read_b32 v179,a251
v_accvgpr_write_b32 a251,v211
v_mov_b32_e32 v216,v184
v_accvgpr_read_b32 v184,a252
v_accvgpr_write_b32 a252,v216
v_mov_b32_e32 v217,v185
v_accvgpr_read_b32 v185,a253
v_accvgpr_write_b32 a253,v217
v_mov_b32_e32 v218,v186
v_accvgpr_read_b32 v186,a254
v_accvgpr_write_b32 a254,v218
v_mov_b32_e32 v219,v187
v_accvgpr_read_b32 v187,a255
v_accvgpr_write_b32 a255,v219
s_add_u32 s86,s86,1
s_cmp_lt_u32 s86,4
s_cbranch_scc1 L_rd_t
s_nop 15
s_nop 3
s_nop 1
s_cmp_eq_u32 s84,4
s_cbranch_scc1 L_redo_r4
s_cmp_lg_u32 s80,s81
s_branch L_redo_r5
L_k_ev:
L_v_ev:
buffer_load_dwordx4 a[252:255],v244,s[8:11],s35 offen sc1
s_branch L_k_evr
L_spbe:
s_mov_b32 s91,65536
L_splbe:
s_sleep 2
buffer_load_dword v253,v224,s[8:11],s53 offen sc1
s_waitcnt vmcnt(0)
v_cmp_ne_u32_e32 vcc,4,v253
s_nop 1
s_cbranch_vccz L_sprbe
s_sub_u32 s91,s91,1
s_cbranch_scc0 L_splbe
s_branch L_sprbe
L_spa3e:
s_mov_b32 s91,65536
L_spla3e:
s_sleep 2
buffer_load_dword v253,v242,s[8:11],s53 offen sc1
s_waitcnt vmcnt(0)
v_cmp_ne_u32_e32 vcc,4,v253
s_nop 1
s_cbranch_vccz L_spra3e
s_sub_u32 s91,s91,1
s_cbranch_scc0 L_spla3e
s_branch L_spra3e
L_spa4e:
s_mov_b32 s91,65536
L_spla4e:
s_sleep 2
buffer_load_dword v253,v242,s[8:11],s53 offen sc1
s_waitcnt vmcnt(0)
v_cmp_ne_u32_e32 vcc,4,v253
s_nop 1
s_cbranch_vccz L_spra4e
s_sub_u32 s91,s91,1
s_cbranch_scc0 L_spla4e
s_branch L_spra4e
L_spa5e:
s_mov_b32 s91,65536
L_spla5e:
s_sleep 2
buffer_load_dword v253,v242,s[8:11],s53 offen sc1
s_waitcnt vmcnt(0)
v_cmp_ne_u32_e32 vcc,4,v253
s_nop 1
s_cbranch_vccz L_spra5e
s_sub_u32 s91,s91,1
s_cbranch_scc0 L_spla5e
s_branch L_spra5e
L_spa6e:
s_mov_b32 s91,65536
L_spla6e:
s_sleep 2
buffer_load_dword v253,v242,s[8:11],s53 offen sc1
s_waitcnt vmcnt(0)
v_cmp_ne_u32_e32 vcc,4,v253
s_nop 1
s_cbranch_vccz L_spra6e
s_sub_u32 s91,s91,1
s_cbranch_scc0 L_spla6e
s_branch L_spra6e
L_spa7e:
s_mov_b32 s91,65536
L_spla7e:
s_sleep 2
buffer_load_dword v253,v242,s[8:11],s53 offen sc1
s_waitcnt vmcnt(0)
v_cmp_ne_u32_e32 vcc,4,v253
s_nop 1
s_cbranch_vccz L_spra7e
s_sub_u32 s91,s91,1
s_cbranch_scc0 L_spla7e
s_branch L_spra7e
L_spa8e:
s_mov_b32 s91,65536
L_spla8e:
s_sleep 2
buffer_load_dword v253,v242,s[8:11],s53 offen sc1
s_waitcnt vmcnt(0)
v_cmp_ne_u32_e32 vcc,4,v253
s_nop 1
s_cbranch_vccz L_spra8e
s_sub_u32 s91,s91,1
s_cbranch_scc0 L_spla8e
s_branch L_spra8e
L_spa9e:
s_mov_b32 s91,65536
L_spla9e:
s_sleep 2
buffer_load_dword v253,v242,s[8:11],s53 offen sc1
s_waitcnt vmcnt(0)
v_cmp_ne_u32_e32 vcc,4,v253
s_nop 1
s_cbranch_vccz L_spra9e
s_sub_u32 s91,s91,1
s_cbranch_scc0 L_spla9e
s_branch L_spra9e
L_spa10e:
s_mov_b32 s91,65536
L_spla10e:
s_sleep 2
buffer_load_dword v253,v242,s[8:11],s53 offen sc1
s_waitcnt vmcnt(0)
v_cmp_ne_u32_e32 vcc,4,v253
s_nop 1
s_cbranch_vccz L_spra10e
s_sub_u32 s91,s91,1
s_cbranch_scc0 L_spla10e
s_branch L_spra10e
L_sppe:
s_mov_b32 s91,65536
L_splpe:
s_sleep 2
buffer_load_dword v253,v242,s[8:11],s53 offen sc1
s_waitcnt vmcnt(0)
v_cmp_ne_u32_e32 vcc,4,v253
s_nop 1
s_cbranch_vccz L_sprpe
s_sub_u32 s91,s91,1
s_cbranch_scc0 L_splpe
s_branch L_sprpe
L_spx0e:
s_mov_b32 s91,65536
L_splx0e:
lv 16,244,82,0
lv 20,244,82,1024
lv 24,244,82,2048
lv 28,244,82,3072
s_waitcnt vmcnt(0)
v_max_u32_e32 v248,v16,v17
v_max3_u32 v248,v248,v18,v19
v_max3_u32 v248,v248,v20,v21
v_max3_u32 v248,v248,v22,v23
v_max3_u32 v248,v248,v24,v25
v_max3_u32 v248,v248,v26,v27
v_max3_u32 v248,v248,v28,v29
v_max3_u32 v248,v248,v30,v31
v_cmp_eq_u32_e32 vcc,-1,v248
s_nop 1
s_cbranch_vccz L_sprx0e
s_sub_u32 s91,s91,1
s_cbranch_scc0 L_splx0e
s_branch L_sprx0e
L_spx1e:
s_mov_b32 s91,65536
L_splx1e:
lv 48,244,83,0
lv 52,244,83,1024
lv 56,244,83,2048
lv 60,244,83,3072
s_waitcnt vmcnt(0)
v_max_u32_e32 v248,v48,v49
v_max3_u32 v248,v248,v50,v51
v_max3_u32 v248,v248,v52,v53
v_max3_u32 v248,v248,v54,v55
v_max3_u32 v248,v248,v56,v57
v_max3_u32 v248,v248,v58,v59
v_max3_u32 v248,v248,v60,v61
v_max3_u32 v248,v248,v62,v63
v_cmp_eq_u32_e32 vcc,-1,v248
s_nop 1
s_cbranch_vccz L_sprx1e
s_sub_u32 s91,s91,1
s_cbranch_scc0 L_splx1e
s_branch L_sprx1e
L_spx2e:
s_mov_b32 s91,65536
L_splx2e:
lv 80,244,84,0
lv 84,244,84,1024
lv 88,244,84,2048
lv 92,244,84,3072
s_waitcnt vmcnt(0)
v_max_u32_e32 v248,v80,v81
v_max3_u32 v248,v248,v82,v83
v_max3_u32 v248,v248,v84,v85
v_max3_u32 v248,v248,v86,v87
v_max3_u32 v248,v248,v88,v89
v_max3_u32 v248,v248,v90,v91
v_max3_u32 v248,v248,v92,v93
v_max3_u32 v248,v248,v94,v95
v_cmp_eq_u32_e32 vcc,-1,v248
s_nop 1
s_cbranch_vccz L_sprx2e
s_sub_u32 s91,s91,1
s_cbranch_scc0 L_splx2e
s_branch L_sprx2e
L_spbo:
s_mov_b32 s91,65536
L_splbo:
s_sleep 2
buffer_load_dword v253,v224,s[8:11],s53 offen sc1
s_waitcnt vmcnt(0)
v_cmp_ne_u32_e32 vcc,4,v253
s_nop 1
s_cbranch_vccz L_sprbo
s_sub_u32 s91,s91,1
s_cbranch_scc0 L_splbo
s_branch L_sprbo
L_spa3o:
s_mov_b32 s91,65536
L_spla3o:
s_sleep 2
buffer_load_dword v253,v242,s[8:11],s53 offen sc1
s_waitcnt vmcnt(0)
v_cmp_ne_u32_e32 vcc,4,v253
s_nop 1
s_cbranch_vccz L_spra3o
s_sub_u32 s91,s91,1
s_cbranch_scc0 L_spla3o
s_branch L_spra3o
L_spa4o:
s_mov_b32 s91,65536
L_spla4o:
s_sleep 2
buffer_load_dword v253,v242,s[8:11],s53 offen sc1
s_waitcnt vmcnt(0)
v_cmp_ne_u32_e32 vcc,4,v253
s_nop 1
s_cbranch_vccz L_spra4o
s_sub_u32 s91,s91,1
s_cbranch_scc0 L_spla4o
s_branch L_spra4o
L_spa5o:
s_mov_b32 s91,65536
L_spla5o:
s_sleep 2
buffer_load_dword v253,v242,s[8:11],s53 offen sc1
s_waitcnt vmcnt(0)
v_cmp_ne_u32_e32 vcc,4,v253
s_nop 1
s_cbranch_vccz L_spra5o
s_sub_u32 s91,s91,1
s_cbranch_scc0 L_spla5o
s_branch L_spra5o
L_spa6o:
s_mov_b32 s91,65536
L_spla6o:
s_sleep 2
buffer_load_dword v253,v242,s[8:11],s53 offen sc1
s_waitcnt vmcnt(0)
v_cmp_ne_u32_e32 vcc,4,v253
s_nop 1
s_cbranch_vccz L_spra6o
s_sub_u32 s91,s91,1
s_cbranch_scc0 L_spla6o
s_branch L_spra6o
L_spa7o:
s_mov_b32 s91,65536
L_spla7o:
s_sleep 2
buffer_load_dword v253,v242,s[8:11],s53 offen sc1
s_waitcnt vmcnt(0)
v_cmp_ne_u32_e32 vcc,4,v253
s_nop 1
s_cbranch_vccz L_spra7o
s_sub_u32 s91,s91,1
s_cbranch_scc0 L_spla7o
s_branch L_spra7o
L_spa8o:
s_mov_b32 s91,65536
L_spla8o:
s_sleep 2
buffer_load_dword v253,v242,s[8:11],s53 offen sc1
s_waitcnt vmcnt(0)
v_cmp_ne_u32_e32 vcc,4,v253
s_nop 1
s_cbranch_vccz L_spra8o
s_sub_u32 s91,s91,1
s_cbranch_scc0 L_spla8o
s_branch L_spra8o
L_sppo:
s_mov_b32 s91,65536
L_splpo:
s_sleep 2
buffer_load_dword v253,v242,s[8:11],s53 offen sc1
s_waitcnt vmcnt(0)
v_cmp_ne_u32_e32 vcc,4,v253
s_nop 1
s_cbranch_vccz L_sprpo
s_sub_u32 s91,s91,1
s_cbranch_scc0 L_splpo
s_branch L_sprpo
L_spx0o:
s_mov_b32 s91,65536
L_splx0o:
lv 16,244,82,0
lv 20,244,82,1024
lv 24,244,82,2048
lv 28,244,82,3072
s_waitcnt vmcnt(0)
v_max_u32_e32 v248,v16,v17
v_max3_u32 v248,v248,v18,v19
v_max3_u32 v248,v248,v20,v21
v_max3_u32 v248,v248,v22,v23
v_max3_u32 v248,v248,v24,v25
v_max3_u32 v248,v248,v26,v27
v_max3_u32 v248,v248,v28,v29
v_max3_u32 v248,v248,v30,v31
v_cmp_eq_u32_e32 vcc,-1,v248
s_nop 1
s_cbranch_vccz L_sprx0o
s_sub_u32 s91,s91,1
s_cbranch_scc0 L_splx0o
s_branch L_sprx0o
L_spx1o:
s_mov_b32 s91,65536
L_splx1o:
lv 48,244,83,0
lv 52,244,83,1024
lv 56,244,83,2048
lv 60,244,83,3072
s_waitcnt vmcnt(0)
v_max_u32_e32 v248,v48,v49
v_max3_u32 v248,v248,v50,v51
v_max3_u32 v248,v248,v52,v53
v_max3_u32 v248,v248,v54,v55
v_max3_u32 v248,v248,v56,v57
v_max3_u32 v248,v248,v58,v59
v_max3_u32 v248,v248,v60,v61
v_max3_u32 v248,v248,v62,v63
v_cmp_eq_u32_e32 vcc,-1,v248
s_nop 1
s_cbranch_vccz L_sprx1o
s_sub_u32 s91,s91,1
s_cbranch_scc0 L_splx1o
s_branch L_sprx1o
L_spx2o:
s_mov_b32 s91,65536
L_splx2o:
lv 80,244,84,0
lv 84,244,84,1024
lv 88,244,84,2048
lv 92,244,84,3072
s_waitcnt vmcnt(0)
v_max_u32_e32 v248,v80,v81
v_max3_u32 v248,v248,v82,v83
v_max3_u32 v248,v248,v84,v85
v_max3_u32 v248,v248,v86,v87
v_max3_u32 v248,v248,v88,v89
v_max3_u32 v248,v248,v90,v91
v_max3_u32 v248,v248,v92,v93
v_max3_u32 v248,v248,v94,v95
v_cmp_eq_u32_e32 vcc,-1,v248
s_nop 1
s_cbranch_vccz L_sprx2o
s_sub_u32 s91,s91,1
s_cbranch_scc0 L_splx2o
s_branch L_sprx2o
L_att_end:
s_endpgm
.p2align 12
.purgem cv
.purgem cvaa
.purgem cvvv
.purgem dl
.purgem ds
.purgem ex
.purgem la
.purgem lv
.purgem maaa
.purgem maav
.purgem mavv
.purgem mvaa
.purgem mvav
.purgem mvva
.purgem mvvv
.purgem ra
.purgem rv
.purgem ta
.purgem tv
.purgem zaaa
.purgem zaav
.purgem zvaa
.purgem zvav
.purgem zvva
.purgem zvvv
)"
        :: "s"(__builtin_amdgcn_kernarg_segment_ptr()), "s"(blockIdx.x), "v"(tid)
        : "v0", "v1", "v2", "v3", "v4", "v5", "v6", "v7", "v8", "v9", "v10", "v11", "v12", "v13", "v14", "v15", "v16", "v17", "v18", "v19", "v20", "v21", "v22", "v23", "v24", "v25", "v26", "v27", "v28", "v29", "v30", "v31", "v32", "v33", "v34", "v35", "v36", "v37", "v38", "v39", "v40", "v41", "v42", "v43", "v44", "v45", "v46", "v47", "v48", "v49", "v50", "v51", "v52", "v53", "v54", "v55", "v56", "v57", "v58", "v59", "v60", "v61", "v62", "v63", "v64", "v65", "v66", "v67", "v68", "v69", "v70", "v71", "v72", "v73", "v74", "v75", "v76", "v77", "v78", "v79", "v80", "v81", "v82", "v83", "v84", "v85", "v86", "v87", "v88", "v89", "v90", "v91", "v92", "v93", "v94", "v95", "v96", "v97", "v98", "v99", "v100", "v101", "v102", "v103", "v104", "v105", "v106", "v107", "v108", "v109", "v110", "v111", "v112", "v113", "v114", "v115", "v116", "v117", "v118", "v119", "v120", "v121", "v122", "v123", "v124", "v125", "v126", "v127", "v128", "v129", "v130", "v131", "v132", "v133", "v134", "v135", "v136", "v137", "v138", "v139", "v140", "v141", "v142", "v143", "v144", "v145", "v146", "v147", "v148", "v149", "v150", "v151", "v152", "v153", "v154", "v155", "v156", "v157", "v158", "v159", "v160", "v161", "v162", "v163", "v164", "v165", "v166", "v167", "v168", "v169", "v170", "v171", "v172", "v173", "v174", "v175", "v176", "v177", "v178", "v179", "v180", "v181", "v182", "v183", "v184", "v185", "v186", "v187", "v188", "v189", "v190", "v191", "v192", "v193", "v194", "v195", "v196", "v197", "v198", "v199", "v200", "v201", "v202", "v203", "v204", "v205", "v206", "v207", "v208", "v209", "v210", "v211", "v212", "v213", "v214", "v215", "v216", "v217", "v218", "v219", "v220", "v221", "v222", "v223", "v224", "v225", "v226", "v227", "v228", "v229", "v230", "v231", "v232", "v233", "v234", "v235", "v236", "v237", "v238", "v239", "v240", "v241", "v242", "v243", "v244", "v245", "v246", "v247", "v248", "v249", "v250", "v251", "v252", "v253", "v254", "a0", "a1", "a2", "a3", "a4", "a5", "a6", "a7", "a8", "a9", "a10", "a11", "a12", "a13", "a14", "a15", "a16", "a17", "a18", "a19", "a20", "a21", "a22", "a23", "a24", "a25", "a26", "a27", "a28", "a29", "a30", "a31", "a32", "a33", "a34", "a35", "a36", "a37", "a38", "a39", "a40", "a41", "a42", "a43", "a44", "a45", "a46", "a47", "a48", "a49", "a50", "a51", "a52", "a53", "a54", "a55", "a56", "a57", "a58", "a59", "a60", "a61", "a62", "a63", "a64", "a65", "a66", "a67", "a68", "a69", "a70", "a71", "a72", "a73", "a74", "a75", "a76", "a77", "a78", "a79", "a80", "a81", "a82", "a83", "a84", "a85", "a86", "a87", "a88", "a89", "a90", "a91", "a92", "a93", "a94", "a95", "a96", "a97", "a98", "a99", "a100", "a101", "a102", "a103", "a104", "a105", "a106", "a107", "a108", "a109", "a110", "a111", "a112", "a113", "a114", "a115", "a116", "a117", "a118", "a119", "a120", "a121", "a122", "a123", "a124", "a125", "a126", "a127", "a128", "a129", "a130", "a131", "a132", "a133", "a134", "a135", "a136", "a137", "a138", "a139", "a140", "a141", "a142", "a143", "a144", "a145", "a146", "a147", "a148", "a149", "a150", "a151", "a152", "a153", "a154", "a155", "a156", "a157", "a158", "a159", "a160", "a161", "a162", "a163", "a164", "a165", "a166", "a167", "a168", "a169", "a170", "a171", "a172", "a173", "a174", "a175", "a176", "a177", "a178", "a179", "a180", "a181", "a182", "a183", "a184", "a185", "a186", "a187", "a188", "a189", "a190", "a191", "a192", "a193", "a194", "a195", "a196", "a197", "a198", "a199", "a200", "a201", "a202", "a203", "a204", "a205", "a206", "a207", "a208", "a209", "a210", "a211", "a212", "a213", "a214", "a215", "a216", "a217", "a218", "a219", "a220", "a221", "a222", "a223", "a224", "a225", "a226", "a227", "a228", "a229", "a230", "a231", "a232", "a233", "a234", "a235", "a236", "a237", "a238", "a239", "a240", "a241", "a242", "a243", "a244", "a245", "a246", "a247", "a248", "a249", "a250", "a251", "a252", "a253", "a254", "a255", "s4", "s5", "s6", "s7", "s8", "s9", "s10", "s11", "s12", "s13", "s14", "s15", "s16", "s17", "s18", "s19", "s20", "s21", "s22", "s23", "s24", "s25", "s26", "s27", "s28", "s29", "s30", "s31", "s32", "s33", "s34", "s35", "s36", "s37", "s38", "s39", "s40", "s41", "s42", "s43", "s44", "s45", "s46", "s47", "s48", "s49", "s50", "s51", "s52", "s53", "s54", "s55", "s56", "s57", "s58", "s59", "s60", "s61", "s62", "s63", "s64", "s65", "s66", "s67", "s68", "s69", "s70", "s71", "s72", "s73", "s74", "s75", "s76", "s77", "s78", "s79", "s80", "s81", "s82", "s83", "s84", "s85", "s86", "s87", "s88", "s89", "s90", "s91", "s92", "s93", "s94", "s95", "s96", "s97", "s98", "s99", "s100", "s101", "vcc", "scc", "m0", "memory");
}

extern "C" void kernel_launch(void* const* d_in, const int* in_sizes, int n_in, void* d_out, int out_size, void* d_ws, size_t ws_size, hipStream_t stream) {
    (void)in_sizes; (void)n_in; (void)out_size; (void)ws_size;
    char* ws = (char*)d_ws + 0ull;
    Args a;
    a.p[0] = d_in[0]; a.p[1] = d_in[1]; a.p[2] = d_in[2]; a.p[3] = d_in[3]; a.p[4] = d_in[4]; a.p[5] = d_in[5]; a.p[6] = d_in[7]; a.p[7] = d_in[8]; a.p[8] = d_in[9]; a.p[9] = d_out; a.p[10] = ws;
    mega<<<256, 256, 0, stream>>>(a);
}
```

```cpp
#include <hip/hip_runtime.h>
#include <hip/hip_bf16.h>
#include <stdint.h>
#include <type_traits>

#define LAS __attribute__((address_space(3)))
struct Args { const void* p[11]; };
__global__ __launch_bounds__(256, 1)
void mega(Args args) {
    __shared__ __attribute__((aligned(1024))) char smem[163840];
    const unsigned tid = threadIdx.x + (unsigned)(uintptr_t)(LAS char*)smem;
    asm volatile(
R"(.macro cv a,b,c
v_cvt_pk_bf16_f32 v\a,v\b,v\c
.endm
.macro cvaa a,b,c,d
v_mfma_f32_16x16x32_bf16 v[\a:\a+3],a[\b:\b+3],a[\c:\c+3],v[\d:\d+3]
.endm
.macro cvvv a,b,c,d
v_mfma_f32_16x16x32_bf16 v[\a:\a+3],v[\b:\b+3],v[\c:\c+3],v[\d:\d+3]
.endm
.macro dl a,b,c,d
buffer_load_dwordx4 v\a,s[\b:\b+3],s\c offen offset:\d lds
.endm
.macro ds a,b,c,d
buffer_load_dwordx4 v\a,s[\b:\b+3],s\c offen offset:\d sc1 lds
.endm
.macro ex a
v_exp_f32_e32 v\a,v\a
.endm
.macro la a,b,c,d
buffer_load_dwordx4 a[\a:\a+3],v\b,s[8:11],s\c offen offset:\d sc1
.endm
.macro lv a,b,c,d
buffer_load_dwordx4 v[\a:\a+3],v\b,s[8:11],s\c offen offset:\d sc1
.endm
.macro maaa a,b,c
v_mfma_f32_16x16x32_bf16 a[\a:\a+3],a[\b:\b+3],a[\c:\c+3],a[\a:\a+3]
.endm
.macro maav a,b,c
v_mfma_f32_16x16x32_bf16 a[\a:\a+3],a[\b:\b+3],v[\c:\c+3],a[\a:\a+3]
.endm
.macro mavv a,b,c
v_mfma_f32_16x16x32_bf16 a[\a:\a+3],v[\b:\b+3],v[\c:\c+3],a[\a:\a+3]
.endm
.macro mvaa a,b,c
v_mfma_f32_16x16x32_bf16 v[\a:\a+3],a[\b:\b+3],a[\c:\c+3],v[\a:\a+3]
.endm
.macro mvav a,b,c
v_mfma_f32_16x16x32_bf16 v[\a:\a+3],a[\b:\b+3],v[\c:\c+3],v[\a:\a+3]
.endm
.macro mvva a,b,c
v_mfma_f32_16x16x32_bf16 v[\a:\a+3],v[\b:\b+3],a[\c:\c+3],v[\a:\a+3]
.endm
.macro mvvv a,b,c
v_mfma_f32_16x16x32_bf16 v[\a:\a+3],v[\b:\b+3],v[\c:\c+3],v[\a:\a+3]
.endm
.macro ra a,b,c
ds_read_b128 a[\a:\a+3],v\b offset:\c
.endm
.macro rv a,b,c
ds_read_b128 v[\a:\a+3],v\b offset:\c
.endm
.macro ta a,b,c
ds_read_b64_tr_b16 a[\a:\a+1],v\b offset:\c
.endm
.macro tv a,b,c
ds_read_b64_tr_b16 v[\a:\a+1],v\b offset:\c
.endm
.macro zaaa a,b,c
v_mfma_f32_16x16x32_bf16 a[\a:\a+3],a[\b:\b+3],a[\c:\c+3],0
.endm
.macro zaav a,b,c
v_mfma_f32_16x16x32_bf16 a[\a:\a+3],a[\b:\b+3],v[\c:\c+3],0
.endm
.macro zvaa a,b,c
v_mfma_f32_16x16x32_bf16 v[\a:\a+3],a[\b:\b+3],a[\c:\c+3],0
.endm
.macro zvav a,b,c
v_mfma_f32_16x16x32_bf16 v[\a:\a+3],a[\b:\b+3],v[\c:\c+3],0
.endm
.macro zvva a,b,c
v_mfma_f32_16x16x32_bf16 v[\a:\a+3],v[\b:\b+3],a[\c:\c+3],0
.endm
.macro zvvv a,b,c
v_mfma_f32_16x16x32_bf16 v[\a:\a+3],v[\b:\b+3],v[\c:\c+3],0
.endm
L_begin:
s_load_dwordx2 s[28:29],%0,72
s_load_dwordx16 s[68:83],%0,0
s_load_dwordx2 s[84:85],%0,64
s_load_dwordx2 s[8:9],%0,80
v_lshrrev_b32_e32 v246,6,%2
v_and_b32_e32 v247,63,%2
v_lshlrev_b32_e32 v244,4,v247
v_readfirstlane_b32 s12,v246
s_and_b32 s20,%1,7
s_lshr_b32 s21,%1,3
s_getpc_b64 s[96:97]
L_att_pc:
s_lshl_b32 s86,s21,2
s_add_u32 s86,s86,s12
s_mul_i32 s86,s86,512
v_lshl_add_u32 v246,v247,7,s86
v_min_u32_e32 v246,L_att_end-L_att_pc-4,v246
s_mov_b64 exec,15
global_load_dword a188,v246,s[96:97]
s_mov_b64 exec,-1
s_and_b32 s19,s21,7
s_lshr_b32 s86,s21,3
s_lshl_b32 s18,s20,2
s_add_u32 s18,s18,s86
v_lshrrev_b32_e32 v248,3,v247
v_and_b32_e32 v250,112,v244
v_and_b32_e32 v249,48,v247
v_xor_b32_e32 v249,v249,v250
v_and_b32_e32 v251,16,v247
v_lshlrev_b32_e32 v251,1,v251
v_xor_b32_e32 v250,v250,v251
v_lshl_add_u32 v224,v248,10,v249
v_xor_b32_e32 v225,64,v224
v_add_u32_e32 v227,21504,v225
v_add_u32_e32 v226,14336,v224
v_add_u32_e32 v225,7168,v225
v_lshl_add_u32 v228,v248,13,v250
v_xor_b32_e32 v229,64,v228
v_add_u32_e32 v231,193536,v229
v_add_u32_e32 v230,129024,v228
v_add_u32_e32 v229,64512,v229
s_lshl_b32 s86,s19,2
s_add_u32 s86,s86,s12
s_lshl_b32 s52,s86,16
s_bfe_u32 s87,s21,65539
s_lshl_b32 s88,s20,1
s_add_u32 s87,s87,s88
s_lshl_b32 s87,s87,2
s_add_u32 s87,s87,s12
s_lshl_b32 s53,s87,7
s_mov_b32 s54,s53
s_mov_b32 s39,131072
s_mov_b32 s43,131072
s_mov_b32 s47,131072
s_mov_b32 s51,131072
s_mov_b32 s38,2097152
s_mov_b32 s42,4194304
s_mov_b32 s46,4194304
s_mov_b32 s56,32768
s_mov_b32 s60,-32640
s_mov_b32 s57,262144
s_mov_b32 s58,262144
s_lshl_b32 s98,s12,13
s_lshl_b32 s62,s12,12
s_add_u32 s62,s62,98304
s_lshl_b32 s35,%1,2
s_add_u32 s35,s35,83896320
v_lshrrev_b32_e32 v48,2,v247
v_and_b32_e32 v49,3,v247
v_lshlrev_b32_e32 v49,7,v49
v_lshl_add_u32 v48,v48,13,v49
s_lshl_b32 s89,s86,14
s_and_b32 s90,s53,7680
s_add_u32 s90,s90,s89
s_lshr_b32 s89,s21,3
s_lshl_b32 s89,s89,2
s_add_u32 s89,s89,s12
s_lshl_b32 s89,s89,14
s_lshl_b32 s91,s19,18
s_add_u32 s89,s89,s91
s_cmp_ge_u32 s21,16
s_waitcnt lgkmcnt(0)
s_mov_b64 s[36:37],s[70:71]
s_cselect_b64 s[40:41],s[76:77],s[74:75]
s_mov_b64 s[44:45],s[40:41]
s_bfm_b64 exec,8,0
buffer_load_dword a189,v48,s[40:43],s90 offen
s_mov_b64 exec,-1
s_and_b32 s9,s9,65535
s_mov_b32 s10,83951616
s_mov_b32 s11,131072
buffer_load_dword v44,off,s[8:11],s35 sc1
buffer_load_dword v45,off,s[8:11],s35 offset:1024 sc1
s_add_u32 m0,s62,0
s_nop 0
dl 228,40,53,0
dl 229,40,53,1024
s_add_u32 s53,s53,s57
dl 230,44,54,2048
dl 231,44,54,3072
s_add_u32 s54,s54,s57
s_add_u32 m0,s98,0
s_nop 0
dl 224,36,52,0
dl 225,36,52,1024
dl 226,36,52,2048
dl 227,36,52,3072
s_add_u32 s52,s52,s56
s_add_u32 m0,s98,4096
s_nop 0
dl 224,36,52,0
dl 225,36,52,1024
dl 226,36,52,2048
dl 227,36,52,3072
s_add_u32 s52,s52,s60
v_and_b32_e32 v246,15,v247
v_lshlrev_b32_e32 v248,3,v247
v_xor_b32_e32 v248,v248,v247
v_and_b32_e32 v248,112,v248
v_lshl_add_u32 v232,v246,7,v248
s_and_b32 s86,s98,16384
v_add_u32_e32 v232,s86,v232
v_xor_b32_e32 v233,64,v232
v_and_b32_e32 v246,12,v247
v_lshlrev_b32_e32 v246,5,v246
v_and_b32_e32 v248,3,v247
v_lshl_add_u32 v246,v248,3,v246
v_and_b32_e32 v248,24,v247
v_lshl_add_u32 v246,v248,2,v246
s_and_b32 s87,s98,8192
s_add_u32 s87,s87,98304
v_add_u32_e32 v246,s87,v246
v_and_b32_e32 v248,48,v247
v_lshl_add_u32 v234,v248,6,v246
v_xor_b32_e32 v235,32,v234
v_xor_b32_e32 v236,64,v234
v_xor_b32_e32 v237,96,v234
s_add_u32 m0,s62,16384
s_nop 0
dl 228,40,53,0
dl 229,40,53,1024
s_add_u32 s53,s53,s58
dl 230,44,54,2048
dl 231,44,54,3072
s_add_u32 s54,s54,s58
s_add_u32 m0,s98,32768
s_nop 0
dl 224,36,52,0
dl 225,36,52,1024
dl 226,36,52,2048
dl 227,36,52,3072
s_add_u32 s52,s52,s56
s_waitcnt vmcnt(20)
s_cmp_eq_u32 s12,0
s_cbranch_scc1 L_rq0
s_branch L_rqx
L_vr0:
s_setpc_b64 s[4:5]
L_rq0:
v_readfirstlane_b32 s32,v44
v_readfirstlane_b32 s86,v45
s_add_u32 s32,s32,1
s_cmp_eq_u32 s32,s86
s_addc_u32 s32,s32,0
v_mov_b32_e32 v44,s32
v_mov_b32_e32 v45,0
v_lshlrev_b32_e32 v46,3,v244
v_mov_b32_e32 v47,0
s_lshl_b32 s86,%1,7
s_add_u32 s87,s86,83914752
s_and_b32 s86,s86,16383
s_add_u32 s86,s86,83898368
s_mov_b64 exec,1
buffer_store_dword v44,off,s[8:11],s35 sc0 sc1
v_mov_b32_e32 v48,0
v_mov_b32_e32 v49,0
buffer_store_dwordx4 v[46:49],off,s[8:11],s86 sc0 sc1
buffer_store_dwordx4 v[46:49],off,s[8:11],s87 sc0 sc1
s_cmp_lg_u32 %1,0
s_cbranch_scc1 L_rqy
s_mov_b64 exec,65535
s_mov_b32 s86,83894272
buffer_store_dword v45,v46,s[8:11],s86 offen sc0 sc1
L_rqy:
s_mov_b64 exec,-1
L_rqx:
s_cmp_ge_u32 s21,16
s_cbranch_scc1 L_vtile
s_waitcnt vmcnt(8)
s_barrier
ta 64,234,0
ta 66,234,512
ra 0,232,0
ta 68,235,0
ta 70,235,512
ra 4,232,2048
ra 8,232,4096
ra 12,232,6144
ra 16,232,8192
ra 20,232,10240
ra 24,232,12288
ra 28,232,14336
s_add_u32 m0,s62,32768
v_mov_b32_e32 v216,0
dl 228,40,53,0
v_mov_b32_e32 v217,0
v_mov_b32_e32 v218,0
v_mov_b32_e32 v219,0
v_mov_b32_e32 v220,0
v_mov_b32_e32 v221,0
v_mov_b32_e32 v222,0
v_mov_b32_e32 v223,0
v_mov_b32_e32 v212,0
v_mov_b32_e32 v213,0
v_mov_b32_e32 v214,0
v_mov_b32_e32 v215,0
v_mov_b32_e32 v208,0
v_mov_b32_e32 v209,0
v_mov_b32_e32 v210,0
v_mov_b32_e32 v211,0
v_mov_b32_e32 v152,0
s_waitcnt lgkmcnt(9)
zaaa 128,64,0
s_lshl_b32 s4,%1,1
ta 72,236,0
v_mov_b32_e32 v153,0
s_waitcnt lgkmcnt(8)
zaaa 132,68,0
s_add_u32 s4,s4,s12
ta 74,236,512
v_mov_b32_e32 v154,0
s_waitcnt lgkmcnt(8)
zaaa 140,68,4
s_mul_i32 s4,s4,1280
ta 76,237,0
ta 78,237,512
zaaa 136,64,4
dl 229,40,53,1024
s_waitcnt lgkmcnt(4)
zvaa 32,64,8
s_add_u32 s53,s53,s57
v_mov_b32_e32 v155,0
v_mov_b32_e32 v156,0
zvaa 36,68,8
s_mov_b32 s55,s4
v_mov_b32_e32 v157,0
v_mov_b32_e32 v158,0
zvaa 44,68,12
s_mul_i32 s4,%1,1792
ta 80,234,4096
v_mov_b32_e32 v159,0
zvaa 40,64,12
dl 230,44,54,2048
zvaa 96,64,16
s_lshl_b32 s88,%1,2
ta 82,234,4608
v_mov_b32_e32 v148,0
zvaa 100,68,16
s_cmp_eq_u32 s12,2
s_cselect_b32 s55,s4,s55
ta 84,235,4096
v_mov_b32_e32 v149,0
zvaa 108,68,20
s_lshl_b32 s4,%1,14
ta 86,235,4608
v_mov_b32_e32 v150,0
zvaa 104,64,20
dl 231,44,54,3072
zvaa 160,64,24
s_add_u32 m0,s98,36864
v_mov_b32_e32 v151,0
v_mov_b32_e32 v144,0
zvaa 164,68,24
s_add_u32 s54,s54,s57
v_mov_b32_e32 v145,0
v_mov_b32_e32 v146,0
zvaa 172,68,28
s_add_u32 s88,s88,s12
v_mov_b32_e32 v147,0
v_mov_b32_e32 v88,0
zvaa 168,64,28
dl 224,36,52,0
s_waitcnt lgkmcnt(4)
zvaa 184,72,28
s_add_u32 s5,s4,16384
v_mov_b32_e32 v89,0
v_mov_b32_e32 v90,0
zvaa 188,76,28
s_cmp_eq_u32 s12,3
s_cselect_b32 s55,s4,s55
ra 32,233,0
v_mov_b32_e32 v91,0
zvaa 180,76,24
s_mov_b32 s4,10
v_mov_b32_e32 v92,0
v_mov_b32_e32 v93,0
zvaa 176,72,24
dl 225,36,52,1024
zvaa 120,72,20
s_lshl_b32 s88,s88,12
ra 36,233,2048
v_mov_b32_e32 v94,0
zvaa 124,76,20
s_xor_b32 s87,s19,15
ra 40,233,4096
v_mov_b32_e32 v95,0
zvaa 116,76,16
s_cmp_eq_u32 s12,2
s_cselect_b32 s4,14,s4
v_mov_b32_e32 v84,0
v_mov_b32_e32 v85,0
zvaa 112,72,16
dl 226,36,52,2048
zvaa 56,72,12
s_waitcnt vmcnt(11)
s_barrier
zvaa 60,76,12
s_add_u32 s30,s68,s88
s_addc_u32 s31,s69,0
ta 96,234,16384
v_mov_b32_e32 v86,0
zvaa 52,76,8
s_lshl_b32 s22,s87,7
ta 98,234,16896
v_mov_b32_e32 v87,0
zvaa 48,72,8
dl 227,36,52,3072
zaaa 152,72,4
s_add_u32 m0,s62,49152
ta 100,235,16384
v_mov_b32_e32 v80,0
zaaa 156,76,4
s_add_u32 s52,s52,s60
ta 102,235,16896
v_mov_b32_e32 v81,0
zaaa 148,76,0
s_lshl_b32 s87,s87,2
ra 44,233,6144
v_mov_b32_e32 v82,0
zaaa 144,72,0
dl 228,40,53,0
s_waitcnt lgkmcnt(5)
zvaa 0,80,0
s_lshl_b32 s88,s19,2
ra 48,233,8192
v_mov_b32_e32 v83,0
zvaa 4,84,0
s_cmp_eq_u32 s12,3
s_cselect_b32 s4,10,s4
ra 52,233,10240
v_mov_b32_e32 v24,0
zvaa 12,84,4
s_add_u32 s87,s87,s12
ra 56,233,12288
v_mov_b32_e32 v25,0
zvaa 8,80,4
dl 229,40,53,1024
zvaa 64,80,8
s_add_u32 s88,s88,s12
v_mov_b32_e32 v26,0
v_mov_b32_e32 v27,0
zvaa 68,84,8
s_lshl_b32 s90,s18,13
ra 60,233,14336
v_mov_b32_e32 v28,0
zvaa 76,84,12
s_add_u32 s90,s90,62914560
ta 104,236,16384
v_mov_b32_e32 v29,0
zvaa 72,80,12
dl 230,44,54,2048
zvaa 128,80,16
s_mul_i32 s14,s87,327680
ta 106,236,16896
v_mov_b32_e32 v30,0
zvaa 132,84,16
s_mul_i32 s13,s88,327680
ta 108,237,16384
v_mov_b32_e32 v31,0
zvaa 140,84,20
s_add_u32 s14,s14,s90
ta 110,237,16896
v_mov_b32_e32 v20,0
zvaa 136,80,20
dl 231,44,54,3072
zvaa 192,80,24
s_add_u32 m0,s98,65536
v_mov_b32_e32 v21,0
v_mov_b32_e32 v22,0
zvaa 196,84,24
s_add_u32 s13,s13,s90
v_mov_b32_e32 v23,0
v_mov_b32_e32 v16,0
zvaa 204,84,28
s_mul_i32 s90,s18,524288
v_mov_b32_e32 v17,0
v_mov_b32_e32 v18,0
zvaa 200,80,28
dl 224,36,52,0
s_waitcnt lgkmcnt(4)
maaa 128,96,32
s_lshl_b32 s91,s12,12
ta 208,236,4096
v_mov_b32_e32 v19,0
maaa 132,100,32
s_add_u32 s90,s90,s91
ta 210,236,4608
v_lshlrev_b32_e32 v241,7,v247
maaa 140,100,36
s_add_u32 s90,s90,16777216
ta 216,237,4096
v_mov_b32_e32 v242,134217728
maaa 136,96,36
dl 225,36,52,1024
mvaa 32,96,40
s_cmp_eq_u32 s12,3
s_cselect_b64 s[26:27],-1,0
ta 218,237,4608
v_lshrrev_b32_e32 v248,3,v247
mvaa 36,100,40
s_lshl_b32 s86,s19,1
v_and_b32_e32 v250,112,v244
v_and_b32_e32 v251,16,v247
mvaa 44,100,44
s_mov_b64 s[48:49],s[72:73]
v_lshlrev_b32_e32 v251,1,v251
v_cmp_gt_u32_e64 s[24:25],s4,v247
mvaa 40,96,44
dl 226,36,52,2048
mvaa 96,96,48
s_mov_b32 s50,8388608
v_mul_u32_u24_e32 v246,6144,v248
v_add_u32_e32 v249,s90,v244
mvaa 100,100,48
s_mov_b32 s59,655360
ta 112,234,20480
v_cndmask_b32_e64 v241,v242,v241,s[24:25]
mvaa 108,100,52
s_cmp_eq_u32 s12,2
s_cselect_b64 s[24:25],-1,0
ta 114,234,20992
v_add_u32_e32 v255,16777216,v249
mvaa 104,96,52
dl 227,36,52,3072
mvaa 160,96,56
s_add_u32 s52,s52,s56
s_add_u32 m0,s98,69632
ta 116,235,20480
mvaa 164,100,56
s_add_u32 s86,s86,2
ta 118,235,20992
v_accvgpr_write_b32 a240,v249
mvaa 172,100,60
s_cmp_eq_u32 s12,2
s_cselect_b64 s[48:49],s[80:81],s[48:49]
v_and_b32_e32 v249,48,v247
v_accvgpr_write_b32 a241,v255
mvaa 168,96,60
dl 224,36,52,0
s_waitcnt lgkmcnt(4)
mvaa 184,104,60
s_cmp_eq_u32 s12,2
s_cselect_b32 s50,6291456,s50
ta 212,236,20480
v_xor_b32_e32 v249,v249,v250
mvaa 188,108,60
s_cmp_eq_u32 s12,2
s_cselect_b32 s59,458752,s59
v_xor_b32_e32 v250,v250,v251
v_lshl_add_u32 v252,v248,11,v249
mvaa 180,108,56
s_lshl_b32 s15,1,s86
v_and_b32_e32 v251,48,v250
v_xor_b32_e32 v253,64,v252
mvaa 176,104,56
dl 225,36,52,1024
mvaa 120,104,52
s_lshl_b32 s23,s19,7
v_accvgpr_write_b32 a242,v252
v_add_u32_e32 v246,v246,v251
mvaa 124,108,52
s_or_b32 s86,%1,s12
s_cselect_b32 s65,0,4
v_add_u32_e32 v242,15360,v253
v_and_b32_e32 v251,64,v250
mvaa 116,108,48
s_cmp_eq_u32 s12,3
s_cselect_b64 s[48:49],s[68:69],s[48:49]
v_lshl_add_u32 v248,v251,5,v246
v_xor_b32_e32 v249,64,v251
mvaa 112,104,48
dl 226,36,52,2048
mvaa 56,104,44
s_cmp_eq_u32 s12,3
s_cselect_b32 s50,s5,s50
v_accvgpr_write_b32 a243,v242
v_mul_u32_u24_e32 v251,768,v251
mvaa 60,108,44
s_cmp_eq_u32 s12,3
s_cselect_b32 s59,1280,s59
v_add_u32_e32 v242,30720,v252
v_lshl_add_u32 v249,v249,5,v246
mvaa 52,108,40
s_add_u32 s53,s53,s58
v_add_u32_e32 v246,v246,v251
v_accvgpr_write_b32 a244,v242
mvaa 48,104,40
dl 227,36,52,3072
maaa 152,104,36
s_add_u32 s54,s54,s58
v_add_u32_e32 v242,46080,v253
v_accvgpr_write_b32 a245,v242
maaa 156,108,36
s_add_u32 s52,s52,s60
v_add_u32_e32 v243,4096,v246
v_add_u32_e32 v242,0,v248
maaa 148,108,32
s_mov_b32 s63,32768
ta 214,236,20992
v_cndmask_b32_e64 v242,v228,v242,s[24:25]
maaa 144,104,32
s_mov_b32 s61,s98
ta 220,237,20480
v_cndmask_b32_e64 v242,v242,v243,s[26:27]
s_waitcnt lgkmcnt(2)
mvaa 0,112,32
s_mov_b32 s64,1
v_add_u32_e32 v243,101376,v246
v_and_b32_e32 v246,15,v247
mvaa 4,116,32
v_and_b32_e32 v246,12,v247
v_accvgpr_write_b32 a246,v242
mvaa 12,116,36
ta 222,237,20992
v_add_u32_e32 v242,48128,v249
mvaa 8,112,36
v_cndmask_b32_e64 v242,v229,v242,s[24:25]
v_lshlrev_b32_e32 v246,5,v246
mvaa 64,112,40
v_cndmask_b32_e64 v242,v242,v243,s[26:27]
v_accvgpr_write_b32 a247,v242
mvaa 68,116,40
v_add_u32_e32 v242,96256,v248
v_and_b32_e32 v248,3,v247
mvaa 76,116,44
v_cndmask_b32_e64 v242,v230,v242,s[24:25]
v_lshl_add_u32 v246,v248,3,v246
mvaa 72,112,44
v_and_b32_e32 v248,24,v247
v_accvgpr_write_b32 a248,v242
mvaa 128,112,48
v_add_u32_e32 v242,144384,v249
v_lshl_add_u32 v246,v248,2,v246
mvaa 132,116,48
v_and_b32_e32 v248,32,v247
v_cndmask_b32_e64 v242,v231,v242,s[24:25]
mvaa 140,116,52
v_add_u32_e32 v246,98304,v246
v_accvgpr_write_b32 a249,v242
mvaa 136,112,52
v_lshl_add_u32 v238,v248,5,v246
v_xor_b32_e32 v239,32,v238
mvaa 192,112,56
v_add_u32_e32 v232,32768,v232
v_add_u32_e32 v233,32768,v233
mvaa 196,116,56
mvaa 204,116,60
mvaa 200,112,60
s_add_u32 s4,s96,L_vrk-L_att_pc
s_addc_u32 s5,s97,0
s_mov_b64 exec,0
s_cmp_eq_u32 s12,0
s_cbranch_scc1 L_vs1
s_cmp_eq_u32 s12,1
s_cbranch_scc1 L_vs2
s_cmp_eq_u32 s12,2
s_cbranch_scc1 L_vs3
s_branch L_vs7
L_vrk:
s_mov_b64 exec,-1
s_waitcnt vmcnt(8)
s_waitcnt lgkmcnt(0)
s_barrier
L_k_head:
mvaa 216,208,28
ra 64,232,0
mvaa 216,212,60
s_add_u32 m0,s62,0
ta 192,234,32768
mvaa 220,220,60
dl 228,40,53,0
mvaa 220,216,28
ta 194,234,33280
s_nop 3
mvaa 212,216,24
ra 96,233,0
mvaa 212,220,56
ta 196,234,49152
mvaa 208,212,56
ta 198,234,49664
mvaa 208,208,24
ta 204,235,49152
mvaa 152,208,20
dl 229,40,53,1024
mvaa 152,212,52
s_add_u32 s53,s53,s57
ta 206,235,49664
mvaa 156,220,52
ta 200,235,32768
mvaa 156,216,20
ta 202,235,33280
mvaa 148,216,16
ra 68,232,2048
mvaa 148,220,48
ra 100,233,2048
mvaa 144,212,48
ra 72,232,4096
mvaa 144,208,16
dl 230,44,54,2048
mvaa 88,208,12
ra 104,233,4096
mvaa 88,212,44
ra 76,232,6144
mvaa 92,220,44
ra 108,233,6144
mvaa 92,216,12
ra 80,232,8192
mvaa 84,216,8
ra 112,233,8192
mvaa 84,220,40
dl 231,44,54,3072
mvaa 80,212,40
s_add_u32 s54,s54,s57
ra 84,232,10240
mvaa 80,208,8
ra 116,233,10240
mvaa 24,208,4
ra 88,232,12288
mvaa 24,212,36
ra 120,233,12288
mvaa 28,220,36
ra 92,232,14336
mvaa 28,216,4
s_add_u32 m0,s62,16384
ra 124,233,14336
mvaa 20,216,0
dl 228,40,53,0
mvaa 20,220,32
mvaa 16,212,32
mvaa 16,208,0
s_waitcnt lgkmcnt(8)
maaa 128,192,64
maaa 128,196,96
maaa 132,204,96
dl 229,40,53,1024
maaa 132,200,64
s_add_u32 s53,s53,s58
maaa 140,200,68
L_vs1:
s_cbranch_execz L_vs8
maaa 140,204,100
maaa 136,196,100
maaa 136,192,68
mvaa 32,192,72
mvaa 32,196,104
dl 230,44,54,2048
mvaa 36,204,104
mvaa 36,200,72
s_waitcnt lgkmcnt(0)
mvaa 44,200,76
ta 208,236,32768
mvaa 44,204,108
ta 210,236,33280
mvaa 40,196,108
ta 212,236,49152
mvaa 40,192,76
dl 231,44,54,3072
mvaa 96,192,80
s_add_u32 s54,s54,s58
ta 214,236,49664
mvaa 96,196,112
ta 220,237,49152
mvaa 100,204,112
ta 222,237,49664
mvaa 100,200,80
ta 216,237,32768
mvaa 108,200,84
ta 218,237,33280
mvaa 108,204,116
mvaa 104,196,116
s_add_u32 m0,s61,0
mvaa 104,192,84
dl 224,36,52,0
mvaa 160,192,88
mvaa 160,196,120
mvaa 164,204,120
mvaa 164,200,88
mvaa 172,200,92
mvaa 172,204,124
mvaa 168,196,124
dl 225,36,52,1024
mvaa 168,192,92
s_waitcnt lgkmcnt(0)
mvaa 184,208,92
mvaa 184,212,124
mvaa 188,220,124
ta 192,234,36864
mvaa 188,216,92
ta 194,234,37376
mvaa 180,216,88
ta 196,234,53248
mvaa 180,220,120
dl 226,36,52,2048
mvaa 176,212,120
ta 198,234,53760
mvaa 176,208,88
ta 204,235,53248
mvaa 120,208,84
ta 206,235,53760
mvaa 120,212,116
ta 200,235,36864
mvaa 124,220,116
ta 202,235,37376
mvaa 124,216,84
mvaa 116,216,80
dl 227,36,52,3072
mvaa 116,220,112
s_add_u32 s52,s52,s56
mvaa 112,212,112
mvaa 112,208,80
mvaa 56,208,76
mvaa 56,212,108
mvaa 60,220,108
mvaa 60,216,76
mvaa 52,216,72
s_add_u32 m0,s61,4096
mvaa 52,220,104
dl 224,36,52,0
mvaa 48,212,104
s_add_u32 s61,s63,s98
mvaa 48,208,72
s_cmp_ge_u32 s63,65536
s_cselect_b32 s99,-2,1
s_lshl_b32 s99,s99,15
maaa 152,208,68
s_add_u32 s63,s63,s99
v_add_u32_e32 v232,s99,v232
v_add_u32_e32 v233,s99,v233
maaa 152,212,100
maaa 156,220,100
maaa 156,216,68
maaa 148,216,64
dl 225,36,52,1024
maaa 148,220,96
maaa 144,212,96
s_waitcnt lgkmcnt(0)
maaa 144,208,64
mvaa 0,192,64
mvaa 0,196,96
mvaa 4,204,96
ta 208,236,36864
mvaa 4,200,64
ta 210,236,37376
mvaa 12,200,68
dl 226,36,52,2048
mvaa 12,204,100
ta 212,236,53248
mvaa 8,196,100
ta 214,236,53760
mvaa 8,192,68
ta 220,237,53248
mvaa 64,192,72
ta 222,237,53760
mvaa 64,196,104
ta 216,237,36864
mvaa 68,204,104
ta 218,237,37376
mvaa 68,200,72
dl 227,36,52,3072
mvaa 76,200,76
s_add_u32 s52,s52,s60
mvaa 76,204,108
mvaa 72,196,108
mvaa 72,192,76
mvaa 128,192,80
mvaa 128,196,112
buffer_load_dword a184,v241,s[48:51],s55 offen
mvaa 132,204,112
s_add_u32 s55,s55,s59
mvaa 132,200,80
mvaa 140,200,84
mvaa 140,204,116
mvaa 136,196,116
mvaa 136,192,84
buffer_load_dword a185,v241,s[48:51],s55 offen
mvaa 192,192,88
s_add_u32 s55,s55,s59
mvaa 192,196,120
mvaa 196,204,120
mvaa 196,200,88
mvaa 204,200,92
mvaa 204,204,124
s_cmp_eq_u32 s64,3
mvaa 200,196,124
s_waitcnt vmcnt(10) lgkmcnt(0)
s_barrier
mvaa 200,192,92
s_cbranch_scc1 L_k_exit
mvaa 216,208,92
ra 0,232,0
mvaa 216,212,124
s_add_u32 m0,s62,32768
ta 192,234,0
mvaa 220,220,124
dl 228,40,53,0
mvaa 220,216,92
ta 194,234,512
s_nop 3
mvaa 212,216,88
ra 32,233,0
mvaa 212,220,120
ta 196,234,16384
mvaa 208,212,120
ta 198,234,16896
mvaa 208,208,88
ta 204,235,16384
mvaa 152,208,84
dl 229,40,53,1024
mvaa 152,212,116
s_add_u32 s53,s53,s57
ta 206,235,16896
mvaa 156,220,116
ta 200,235,0
mvaa 156,216,84
ta 202,235,512
mvaa 148,216,80
ra 4,232,2048
mvaa 148,220,112
ra 36,233,2048
mvaa 144,212,112
ra 8,232,4096
mvaa 144,208,80
dl 230,44,54,2048
mvaa 88,208,76
ra 40,233,4096
mvaa 88,212,108
ra 12,232,6144
mvaa 92,220,108
ra 44,233,6144
mvaa 92,216,76
ra 16,232,8192
mvaa 84,216,72
ra 48,233,8192
mvaa 84,220,104
dl 231,44,54,3072
mvaa 80,212,104
s_add_u32 s54,s54,s57
ra 20,232,10240
mvaa 80,208,72
ra 52,233,10240
mvaa 24,208,68
ra 24,232,12288
mvaa 24,212,100
ra 56,233,12288
mvaa 28,220,100
ra 28,232,14336
mvaa 28,216,68
s_add_u32 m0,s62,49152
ra 60,233,14336
mvaa 20,216,64
dl 228,40,53,0
mvaa 20,220,96
mvaa 16,212,96
mvaa 16,208,64
s_waitcnt lgkmcnt(8)
maaa 128,192,0
maaa 128,196,32
maaa 132,204,32
dl 229,40,53,1024
maaa 132,200,0
s_add_u32 s53,s53,s58
maaa 140,200,4
maaa 140,204,36
maaa 136,196,36
maaa 136,192,4
mvaa 32,192,8
mvaa 32,196,40
dl 230,44,54,2048
mvaa 36,204,40
mvaa 36,200,8
s_waitcnt lgkmcnt(0)
mvaa 44,200,12
ta 208,236,0
mvaa 44,204,44
ta 210,236,512
mvaa 40,196,44
ta 212,236,16384
mvaa 40,192,12
dl 231,44,54,3072
mvaa 96,192,16
s_add_u32 s54,s54,s58
ta 214,236,16896
mvaa 96,196,48
ta 220,237,16384
mvaa 100,204,48
ta 222,237,16896
mvaa 100,200,16
ta 216,237,0
mvaa 108,200,20
ta 218,237,512
mvaa 108,204,52
mvaa 104,196,52
s_add_u32 m0,s61,0
mvaa 104,192,20
dl 224,36,52,0
mvaa 160,192,24
s_lshl_b32 s86,s21,2
s_add_u32 s86,s86,s12
s_mul_i32 s86,s86,384
mvaa 160,196,56
s_add_u32 s86,s86,4096
v_lshl_add_u32 v253,v244,3,s86
mvaa 164,204,56
s_mov_b64 exec,7
)"
R"(global_load_dword a188,v253,s[96:97]
s_mov_b64 exec,-1
mvaa 164,200,24
mvaa 172,200,28
mvaa 172,204,60
mvaa 168,196,60
dl 225,36,52,1024
mvaa 168,192,28
s_waitcnt lgkmcnt(0)
mvaa 184,208,28
mvaa 184,212,60
mvaa 188,220,60
ta 192,234,4096
mvaa 188,216,28
ta 194,234,4608
mvaa 180,216,24
ta 196,234,20480
mvaa 180,220,56
dl 226,36,52,2048
mvaa 176,212,56
ta 198,234,20992
mvaa 176,208,24
ta 204,235,20480
mvaa 120,208,20
ta 206,235,20992
mvaa 120,212,52
ta 200,235,4096
mvaa 124,220,52
ta 202,235,4608
mvaa 124,216,20
mvaa 116,216,16
dl 227,36,52,3072
mvaa 116,220,48
s_add_u32 s52,s52,s56
mvaa 112,212,48
mvaa 112,208,16
mvaa 56,208,12
mvaa 56,212,44
s_bitcmp1_b32 s65,s64
s_cbranch_scc1 L_k_ev
L_k_evr:
mvaa 60,220,44
mvaa 60,216,12
mvaa 52,216,8
s_add_u32 m0,s61,4096
mvaa 52,220,40
dl 224,36,52,0
mvaa 48,212,40
s_add_u32 s61,s63,s98
mvaa 48,208,8
s_cmp_ge_u32 s63,65536
s_cselect_b32 s99,-2,1
s_lshl_b32 s99,s99,15
maaa 152,208,4
s_add_u32 s63,s63,s99
v_add_u32_e32 v232,s99,v232
v_add_u32_e32 v233,s99,v233
maaa 152,212,36
maaa 156,220,36
maaa 156,216,4
maaa 148,216,0
dl 225,36,52,1024
maaa 148,220,32
maaa 144,212,32
s_waitcnt lgkmcnt(0)
maaa 144,208,0
mvaa 0,192,0
mvaa 0,196,32
mvaa 4,204,32
ta 208,236,4096
mvaa 4,200,0
ta 210,236,4608
mvaa 12,200,4
dl 226,36,52,2048
mvaa 12,204,36
ta 212,236,20480
mvaa 8,196,36
ta 214,236,20992
mvaa 8,192,4
ta 220,237,20480
mvaa 64,192,8
ta 222,237,20992
mvaa 64,196,40
ta 216,237,4096
mvaa 68,204,40
ta 218,237,4608
mvaa 68,200,8
dl 227,36,52,3072
mvaa 76,200,12
s_add_u32 s52,s52,s60
mvaa 76,204,44
mvaa 72,196,44
mvaa 72,192,12
mvaa 128,192,16
mvaa 128,196,48
buffer_load_dword a187,v241,s[48:51],s55 offen
mvaa 132,204,48
s_add_u32 s55,s55,s59
mvaa 132,200,16
mvaa 140,200,20
mvaa 140,204,52
mvaa 136,196,52
mvaa 136,192,20
buffer_load_dword a190,v241,s[48:51],s55 offen
mvaa 192,192,24
s_add_u32 s55,s55,s59
mvaa 192,196,56
mvaa 196,204,56
s_add_u32 s64,s64,1
mvaa 196,200,24
mvaa 204,200,28
mvaa 204,204,60
mvaa 200,196,60
s_waitcnt vmcnt(10) lgkmcnt(0)
s_barrier
mvaa 200,192,28
s_branch L_k_head
L_k_exit:
global_load_dwordx4 a[160:163],v244,s[30:31] offset:0
s_add_u32 m0,s62,32768
mvaa 216,208,92
s_cmp_lt_u32 s12,2
s_cselect_b32 s86,s22,s23
ta 32,234,0
s_and_b32 s87,s12,1
mvaa 216,212,124
s_lshl_b32 s86,s86,11
ta 34,234,512
s_lshl_b32 s87,s87,17
mvaa 220,220,124
s_add_u32 s86,s86,s87
ra 0,232,0
s_mov_b64 s[36:37],s[68:69]
mvaa 220,216,92
s_mov_b32 s38,4194304
ta 36,235,0
s_mov_b32 s52,s86
mvaa 212,216,88
s_mov_b32 s56,65536
ta 38,235,512
v_accvgpr_read_b32 v224,a242
mvaa 212,220,120
dl 228,40,53,0
mvaa 208,212,120
s_lshl_b32 s88,s18,1
ra 4,232,2048
v_accvgpr_read_b32 v225,a243
mvaa 208,208,88
s_add_u32 s88,s88,s12
ra 8,232,4096
v_accvgpr_read_b32 v226,a244
mvaa 152,208,84
s_mov_b32 s91,196608
ra 12,232,6144
v_accvgpr_read_b32 v227,a245
mvaa 152,212,116
s_lshl_b32 s88,s88,7
ra 16,232,8192
s_lshl_b32 s89,s18,6
mvaa 156,220,116
s_mov_b32 s90,6291456
ra 20,232,10240
s_mov_b32 s60,-65408
mvaa 156,216,84
dl 229,40,53,1024
mvaa 148,216,80
s_add_u32 s53,s53,s57
ra 24,232,12288
s_bfe_u32 s4,s21,65536
mvaa 148,220,112
s_lshl_b32 s4,s4,6
ra 28,232,14336
s_lshl_b32 s87,s20,2
mvaa 144,212,112
s_mov_b32 s26,32768
ta 40,236,0
s_mov_b32 s27,131072
mvaa 144,208,80
s_add_u32 s61,s98,32768
ta 42,236,512
s_mov_b32 s63,65536
mvaa 88,208,76
s_cmp_ge_u32 s63,65536
s_cselect_b32 s99,-2,1
ta 44,237,0
s_lshl_b32 s99,s99,15
mvaa 88,212,108
dl 230,44,54,2048
mvaa 92,220,108
s_mov_b32 s67,2048
ta 46,237,512
s_mov_b32 s66,8
mvaa 92,216,76
s_mov_b32 s64,1
ta 48,234,4096
mvaa 84,216,72
s_bitcmp1_b32 s12,0
s_cselect_b64 vcc,-1,0
ta 50,234,4608
mvaa 84,220,104
ta 52,235,4096
mvaa 80,212,104
ta 54,235,4608
mvaa 80,208,72
dl 231,44,54,3072
s_add_u32 m0,s62,49152
mvaa 24,208,68
s_add_u32 s54,s54,s57
ta 56,236,4096
mvaa 24,212,100
s_cmp_lt_u32 s12,2
s_cselect_b32 s57,262144,s91
ta 58,236,4608
mvaa 28,220,100
ta 60,237,4096
mvaa 28,216,68
ta 62,237,4608
mvaa 20,216,64
mvaa 20,220,96
dl 228,40,53,0
mvaa 16,212,96
v_accvgpr_read_b32 v228,a246
mvaa 16,208,64
s_waitcnt lgkmcnt(5)
maaa 128,32,0
maaa 132,36,0
maaa 140,36,4
dl 229,40,53,1024
maaa 136,32,4
s_waitcnt lgkmcnt(0)
s_barrier
mvaa 32,32,8
s_add_u32 s53,s53,s58
ta 96,234,16384
v_accvgpr_read_b32 v229,a247
mvaa 36,36,8
s_cmp_lt_u32 s12,2
s_cselect_b32 s42,8388608,s90
ta 98,234,16896
mvaa 44,36,12
s_cmp_lt_u32 s12,2
s_cselect_b32 s53,s88,s89
ra 64,233,0
s_and_b32 s89,s98,8192
mvaa 40,32,12
dl 230,44,54,2048
mvaa 96,32,16
s_bitcmp1_b32 s21,0
s_cselect_b32 s90,s57,0
ta 100,235,16384
s_add_u32 s53,s53,s90
mvaa 100,36,16
s_cmp_lt_u32 s12,2
s_cselect_b64 s[40:41],s[72:73],s[80:81]
ta 102,235,16896
v_accvgpr_read_b32 v230,a248
mvaa 108,36,20
s_lshl_b32 s90,s90,1
ra 68,233,2048
s_add_u32 s91,s89,65536
mvaa 104,32,20
s_bfe_u32 s88,s21,65539
ra 72,233,4096
s_lshl_b32 s88,s88,1
mvaa 160,32,24
dl 231,44,54,3072
mvaa 164,36,24
s_add_u32 m0,s98,65536
ra 76,233,6144
s_add_u32 s54,s54,s58
mvaa 172,36,28
s_cmp_eq_u32 s12,3
s_cselect_b64 s[44:45],s[68:69],s[40:41]
ra 80,233,8192
v_accvgpr_read_b32 v231,a249
mvaa 168,32,28
s_cmp_eq_u32 s12,3
s_cselect_b32 s46,0,s42
ra 84,233,10240
s_add_u32 s58,s57,s90
mvaa 184,40,28
s_cmp_eq_u32 s12,3
s_cselect_b32 s54,0,s53
ra 88,233,12288
s_sub_u32 s57,s57,s90
mvaa 188,44,28
global_load_dwordx4 a[168:171],v244,s[30:31] offset:2048
mvaa 180,44,24
s_add_u32 s90,s89,65536
ta 104,236,16384
s_add_u32 s88,s88,s87
mvaa 176,40,24
s_and_b32 s87,s12,1
ra 92,233,14336
s_add_u32 s88,s88,s87
mvaa 120,40,20
s_mul_i32 s88,s88,524288
ta 106,236,16896
s_and_b32 s87,s21,7
mvaa 124,44,20
L_vs2:
s_cbranch_execz L_vs9
s_add_u32 s86,s88,16777216
ta 108,237,16384
s_lshl_b32 s87,s87,16
mvaa 116,44,16
global_load_dwordx4 a[176:179],v244,s[84:85] offset:0
mvaa 112,40,16
s_add_u32 s86,s86,s87
ta 110,237,16896
s_lshr_b32 s87,s12,1
mvaa 56,40,12
s_lshl_b32 s87,s87,15
ta 112,234,20480
s_add_u32 s86,s86,s87
mvaa 60,44,12
s_add_u32 s24,s8,s86
s_addc_u32 s25,s9,0
ta 114,234,20992
s_lshl_b32 s86,%1,5
mvaa 52,44,8
s_lshl_b32 s87,s12,3
ta 116,235,20480
s_add_u32 s86,s86,s87
mvaa 48,40,8
global_load_dwordx4 a[164:167],v244,s[30:31] offset:1024
maaa 152,40,4
s_add_u32 s86,s86,83886080
ta 118,235,20992
s_mov_b32 s88,4096
maaa 156,44,4
s_cmp_lg_u32 s65,0
s_cbranch_scc1 L_k_ec1
L_k_ecr1:
ta 120,236,20480
maaa 148,44,0
s_cmp_eq_u32 s12,0
s_cselect_b32 s65,96,0
ta 122,236,20992
s_or_b32 s65,s65,128
maaa 144,40,0
ta 124,237,20480
mvaa 0,48,0
global_load_dwordx4 a[172:175],v244,s[30:31] offset:3072
mvaa 4,52,0
s_mov_b32 s30,1048577
ta 126,237,20992
s_mov_b32 s31,268435712
mvaa 12,52,4
mvaa 8,48,4
mvaa 64,48,8
mvaa 68,52,8
global_load_dwordx4 a[180:183],v244,s[84:85] offset:1024
mvaa 76,52,12
mvaa 72,48,12
mvaa 128,48,16
mvaa 132,52,16
mvaa 140,52,20
dl 224,36,52,0
mvaa 136,48,20
mvaa 192,48,24
mvaa 196,52,24
mvaa 204,52,28
mvaa 200,48,28
dl 225,36,52,1024
mvaa 216,56,28
mvaa 220,60,28
mvaa 212,60,24
mvaa 208,56,24
mvaa 152,56,20
dl 226,36,52,2048
mvaa 156,60,20
mvaa 148,60,16
mvaa 144,56,16
mvaa 88,56,12
s_waitcnt vmcnt(12) lgkmcnt(0)
s_barrier
mvaa 92,60,12
dl 227,36,52,3072
mvaa 84,60,8
s_add_u32 s52,s52,s56
s_add_u32 m0,s98,69632
ta 32,234,32768
mvaa 80,56,8
ta 34,234,33280
mvaa 24,56,4
ta 36,235,32768
mvaa 28,60,4
ta 38,235,33280
mvaa 20,60,0
dl 224,36,52,0
mvaa 16,56,0
ta 40,236,32768
maaa 128,96,64
ta 42,236,33280
maaa 132,100,64
ta 44,237,32768
maaa 140,100,68
ta 46,237,33280
maaa 136,96,68
dl 225,36,52,1024
mvaa 32,96,72
ta 48,234,36864
mvaa 36,100,72
ta 50,234,37376
mvaa 44,100,76
ta 52,235,36864
mvaa 40,96,76
ta 54,235,37376
mvaa 96,96,80
dl 226,36,52,2048
mvaa 100,100,80
ra 20,232,43008
mvaa 108,100,84
ra 24,232,45056
mvaa 104,96,84
ra 0,232,32768
mvaa 160,96,88
ra 28,232,47104
mvaa 164,100,88
dl 227,36,52,3072
mvaa 172,100,92
s_add_u32 m0,s62,0
ra 4,232,34816
s_add_u32 s52,s52,s60
mvaa 168,96,92
ra 16,232,40960
mvaa 184,104,92
ra 8,232,36864
mvaa 188,108,92
ra 12,232,38912
v_add_u32_e32 v232,s90,v232
mvaa 180,108,88
dl 228,40,53,0
mvaa 176,104,88
ta 60,237,36864
v_xor_b32_e32 v232,s4,v232
mvaa 120,104,84
ta 62,237,37376
mvaa 124,108,84
ta 56,236,36864
mvaa 116,108,80
ta 58,236,37376
mvaa 112,104,80
dl 229,40,53,1024
mvaa 56,104,76
s_add_u32 s53,s53,s57
mvaa 60,108,76
mvaa 52,108,72
mvaa 48,104,72
maaa 152,104,68
dl 230,44,54,2048
maaa 156,108,68
maaa 148,108,64
maaa 144,104,64
mvaa 0,112,64
mvaa 4,116,64
dl 231,44,54,3072
mvaa 12,116,68
s_add_u32 m0,s62,16384
s_add_u32 s54,s54,s57
mvaa 8,112,68
mvaa 64,112,72
mvaa 68,116,72
mvaa 76,116,76
dl 228,40,53,0
mvaa 72,112,76
mvaa 128,112,80
mvaa 132,116,80
mvaa 140,116,84
mvaa 136,112,84
dl 229,40,53,1024
mvaa 192,112,88
s_add_u32 s53,s53,s58
mvaa 196,116,88
mvaa 204,116,92
mvaa 200,112,92
mvaa 216,120,92
dl 230,44,54,2048
mvaa 220,124,92
mvaa 212,124,88
s_waitcnt vmcnt(20) lgkmcnt(0)
s_barrier
mvaa 208,120,88
ra 92,233,47104
mvaa 152,120,84
ta 96,234,49152
mvaa 156,124,84
dl 231,44,54,3072
mvaa 148,124,80
s_add_u32 m0,s98,0
ra 88,233,45056
s_add_u32 s54,s54,s58
mvaa 144,120,80
ta 98,234,49664
mvaa 88,120,76
ta 112,234,53248
mvaa 92,124,76
ta 116,235,53248
mvaa 84,124,72
dl 224,36,52,0
mvaa 80,120,72
ta 118,235,53760
mvaa 24,120,68
ta 114,234,53760
v_subrev_u32_e32 v234,s89,v234
mvaa 28,124,68
ta 100,235,49152
mvaa 20,124,64
ta 102,235,49664
v_subrev_u32_e32 v235,s89,v235
mvaa 16,120,64
dl 225,36,52,1024
maaa 128,32,0
ta 124,237,53248
maaa 132,36,0
ta 126,237,53760
maaa 140,36,4
ta 120,236,53248
maaa 136,32,4
ta 122,236,53760
mvaa 32,32,8
dl 226,36,52,2048
mvaa 36,36,8
ta 104,236,49152
mvaa 44,36,12
ta 106,236,49664
v_subrev_u32_e32 v236,s89,v236
mvaa 40,32,12
ta 108,237,49152
mvaa 56,40,12
ta 110,237,49664
v_subrev_u32_e32 v237,s89,v237
mvaa 60,44,12
dl 227,36,52,3072
mvaa 52,44,8
s_add_u32 s52,s52,s56
s_add_u32 m0,s98,4096
ra 84,233,43008
mvaa 48,40,8
ra 80,233,40960
maaa 152,40,4
ra 72,233,36864
maaa 156,44,4
ra 76,233,38912
maaa 148,44,0
dl 224,36,52,0
maaa 144,40,0
ra 68,233,34816
mvaa 0,48,0
ra 64,233,32768
v_add_u32_e32 v233,s91,v233
mvaa 4,52,0
v_xor_b32_e32 v233,s4,v233
mvaa 12,52,4
mvaa 8,48,4
dl 225,36,52,1024
mvaa 24,56,4
mvaa 28,60,4
mvaa 20,60,0
mvaa 16,56,0
mvaa 220,60,28
dl 226,36,52,2048
s_waitcnt lgkmcnt(2)
mvaa 220,124,92
mvaa 216,120,92
mvaa 216,56,28
mvaa 212,60,24
mvaa 212,124,88
dl 227,36,52,3072
mvaa 208,120,88
s_add_u32 m0,s62,32768
s_add_u32 s52,s52,s60
s_waitcnt vmcnt(25)
zvaa 246,160,176
mvaa 208,56,24
cv 223,222,223
cv 222,220,221
mvaa 204,52,28
cv 221,218,219
zvaa 250,168,176
mvaa 204,116,92
cv 220,216,217
cv 219,214,215
mvaa 200,112,92
dl 228,40,53,0
s_waitcnt vmcnt(25)
mvaa 246,164,180
mvaa 200,48,28
cv 218,212,213
mvaa 250,172,180
mvaa 196,52,24
cv 217,210,211
cv 216,208,209
mvaa 196,116,88
cv 215,206,207
v_cndmask_b32_e64 v246,0,v246,s[30:31]
mvaa 192,112,88
cv 214,204,205
v_cndmask_b32_e64 v250,0,v250,s[30:31]
mvaa 192,48,24
dl 229,40,53,1024
s_lshl_b64 s[30:31],s[30:31],1
v_cndmask_b32_e64 v246,v246,v247,s[30:31]
v_cndmask_b32_e64 v250,v250,v251,s[30:31]
mvaa 188,44,28
s_lshl_b64 s[30:31],s[30:31],1
s_add_u32 s53,s53,s57
v_cndmask_b32_e64 v246,v246,v248,s[30:31]
v_cndmask_b32_e64 v250,v250,v252,s[30:31]
mvaa 188,108,92
s_lshl_b64 s[30:31],s[30:31],1
cv 213,202,203
v_cndmask_b32_e64 v246,v246,v249,s[30:31]
mvaa 184,104,92
v_cndmask_b32_e64 v250,v250,v253,s[30:31]
v_add_f32_dpp v246,v246,v246 quad_perm:[1,0,3,2] row_mask:0xf bank_mask:0xf
mvaa 184,40,28
v_add_f32_dpp v250,v250,v250 quad_perm:[1,0,3,2] row_mask:0xf bank_mask:0xf
v_add_f32_dpp v246,v246,v246 quad_perm:[2,3,0,1] row_mask:0xf bank_mask:0xf
mvaa 180,44,24
dl 230,44,54,2048
v_add_f32_dpp v250,v250,v250 quad_perm:[2,3,0,1] row_mask:0xf bank_mask:0xf
mvaa 180,108,88
v_add_f32_dpp v246,v246,v246 row_half_mirror row_mask:0xf bank_mask:0xf
v_add_f32_dpp v250,v250,v250 row_half_mirror row_mask:0xf bank_mask:0xf
mvaa 176,104,88
v_add_f32_dpp v246,v246,v246 row_mirror row_mask:0xf bank_mask:0xf
v_add_f32_dpp v250,v250,v250 row_mirror row_mask:0xf bank_mask:0xf
mvaa 176,40,24
cv 212,200,201
cv 211,198,199
mvaa 172,36,28
v_permlane16_swap_b32_e32 v246,v250
mvaa 172,100,92
dl 231,44,54,3072
v_add_f32_e32 v246,v246,v250
v_mov_b32_e32 v250,v246
mvaa 168,96,92
s_add_u32 s54,s54,s57
v_permlane32_swap_b32_e32 v246,v250
mvaa 168,32,28
v_add_f32_e32 v246,v246,v250
v_lshrrev_b32_e32 v250,6,v244
mvaa 164,36,24
v_mul_f32_e32 v246,0xbfb8aa3b,v246
cv 210,196,197
mvaa 164,100,88
ex 246
mvaa 160,96,88
buffer_load_dword a242,v241,s[48:51],s55 offen
mvaa 160,32,24
s_add_u32 s55,s55,s59
v_add_f32_e32 v246,1.0,v246
cv 209,194,195
mvaa 156,60,20
v_rcp_f32_e32 v246,v246
mvaa 156,124,84
s_waitcnt vmcnt(17) lgkmcnt(0)
s_barrier
s_add_u32 m0,s62,49152
mvaa 152,120,84
cv 208,192,193
cv 207,190,191
mvaa 152,56,20
dl 228,40,53,0
mvaa 148,60,16
cv 206,188,189
cv 205,186,187
mvaa 148,124,80
cv 204,184,185
cv 203,182,183
mvaa 144,120,80
cv 202,180,181
cv 201,178,179
mvaa 144,56,16
cv 200,176,177
cv 199,174,175
mvaa 140,52,20
dl 229,40,53,1024
mvaa 140,116,84
s_add_u32 s53,s53,s58
cv 198,172,173
cv 197,170,171
mvaa 136,112,84
ra 4,232,0
cv 196,168,169
mvaa 136,48,20
ta 92,239,12288
cv 195,166,167
mvaa 132,52,16
ta 94,239,12800
cv 194,164,165
mvaa 132,116,80
dl 230,44,54,2048
mvaa 128,112,80
ta 88,238,12288
cv 193,162,163
mvaa 128,48,16
ta 90,238,12800
cv 192,160,161
mvaa 124,44,20
cv 191,158,159
cv 190,156,157
mvaa 124,108,84
cv 189,154,155
cv 188,152,153
mvaa 120,104,84
dl 231,44,54,3072
mvaa 120,40,20
s_add_u32 m0,s98,32768
cv 187,150,151
cv 186,148,149
mvaa 116,44,16
s_add_u32 s54,s54,s58
cv 185,146,147
cv 184,144,145
mvaa 116,108,80
cv 183,142,143
cv 182,140,141
mvaa 112,104,80
cv 181,138,139
cv 180,136,137
mvaa 112,40,16
dl 224,36,52,0
mvaa 108,36,20
cv 179,134,135
cv 178,132,133
mvaa 108,100,84
cv 177,130,131
cv 176,128,129
mvaa 104,96,84
cv 175,126,127
cv 174,124,125
mvaa 104,32,20
cv 173,122,123
cv 172,120,121
mvaa 100,36,16
dl 225,36,52,1024
mvaa 100,100,80
ta 44,235,0
cv 171,118,119
mvaa 96,96,80
ta 46,235,512
cv 170,116,117
mvaa 96,32,16
ta 40,234,0
cv 169,114,115
mvaa 92,60,12
ta 42,234,512
cv 168,112,113
mvaa 92,124,76
dl 226,36,52,2048
mvaa 88,120,76
cv 167,110,111
cv 166,108,109
mvaa 88,56,12
ta 84,237,8192
cv 165,106,107
mvaa 84,60,8
ta 86,237,8704
cv 164,104,105
mvaa 84,124,72
ta 80,236,8192
cv 163,102,103
mvaa 80,120,72
dl 227,36,52,3072
mvaa 80,56,8
s_add_u32 m0,s62,0
ta 82,236,8704
cv 162,100,101
mvaa 76,52,12
s_add_u32 s52,s52,s56
ra 16,232,6144
cv 161,98,99
mvaa 76,116,76
ta 60,235,4096
cv 160,96,97
mvaa 72,112,76
ta 56,234,4096
cv 159,94,95
mvaa 72,48,12
buffer_load_dword a243,v241,s[48:51],s55 offen
mvaa 68,52,8
s_add_u32 s55,s55,s59
ta 62,235,4608
cv 151,78,79
mvaa 68,116,72
ta 58,234,4608
cv 150,76,77
mvaa 64,112,72
ra 12,232,4096
cv 158,92,93
mvaa 64,48,8
ta 52,237,0
cv 157,90,91
mvaa 60,108,76
buffer_load_dword a244,v241,s[48:51],s55 offen
mvaa 56,104,76
s_add_u32 s55,s55,s59
ta 54,237,512
cv 147,70,71
mvaa 52,108,72
ra 8,232,2048
cv 146,68,69
mvaa 48,104,72
ta 48,236,0
cv 145,66,67
mvaa 44,100,76
ta 50,236,512
cv 144,64,65
mvaa 40,96,76
buffer_load_dword a245,v241,s[48:51],s55 offen
mvaa 36,100,72
s_add_u32 s55,s55,s59
cv 143,62,63
cv 142,60,61
mvaa 32,96,72
cv 141,58,59
cv 140,56,57
mvaa 28,124,68
cv 139,54,55
cv 138,52,53
mvaa 24,120,68
cv 137,50,51
cv 136,48,49
mvaa 20,124,64
s_mov_b64 exec,65537
buffer_store_dword v246,v250,s[8:11],s86 offen sc0 sc1
s_mov_b64 exec,-1
mvaa 16,120,64
cv 135,46,47
cv 134,44,45
mvaa 12,116,68
ta 76,235,8192
cv 133,42,43
mvaa 8,112,68
ta 78,235,8704
cv 132,40,41
mvaa 4,116,64
cv 131,38,39
cv 130,36,37
mvaa 0,112,64
ta 72,234,8192
cv 129,34,35
maaa 156,108,68
L_vs3:
s_cbranch_execz L_vs10
ta 74,234,8704
cv 128,32,33
maaa 152,104,68
cv 127,30,31
cv 126,28,29
maaa 148,108,64
cv 125,26,27
cv 124,24,25
maaa 144,104,64
v_accvgpr_read_b32 v63,a159
v_accvgpr_read_b32 v62,a158
maaa 140,100,68
v_accvgpr_read_b32 v61,a157
v_accvgpr_read_b32 v60,a156
maaa 136,96,68
v_accvgpr_read_b32 v59,a155
v_accvgpr_read_b32 v58,a154
maaa 132,100,64
v_accvgpr_read_b32 v57,a153
v_accvgpr_read_b32 v56,a152
maaa 128,96,64
ta 68,237,4096
ta 70,237,4608
s_waitcnt lgkmcnt(4)
zaaa 236,40,4
cv 56,56,57
cv 57,58,59
zaaa 220,44,4
ta 64,236,4096
ta 66,236,4608
zaaa 216,44,8
cv 58,60,61
cv 59,62,63
zaaa 232,40,8
buffer_store_dwordx4 v[56:59],v244,s[24:27],0 offen offset:3072 sc0 sc1
zaaa 228,40,12
v_accvgpr_read_b32 v55,a151
v_accvgpr_read_b32 v54,a150
zaaa 212,44,12
v_accvgpr_read_b32 v53,a149
v_accvgpr_read_b32 v52,a148
zaaa 208,44,16
v_accvgpr_read_b32 v51,a147
v_accvgpr_read_b32 v50,a146
zaaa 224,40,16
v_accvgpr_read_b32 v49,a145
v_accvgpr_read_b32 v48,a144
zaaa 192,48,16
cv 48,48,49
cv 49,50,51
zaaa 176,52,16
cv 50,52,53
cv 51,54,55
zaaa 180,52,12
buffer_store_dwordx4 v[48:51],v244,s[24:27],0 offen offset:2048 sc0 sc1
zaaa 196,48,12
s_waitcnt vmcnt(27) lgkmcnt(0)
s_barrier
ra 24,233,0
v_accvgpr_read_b32 v47,a143
zaaa 200,48,8
ra 28,233,2048
v_accvgpr_read_b32 v46,a142
v_accvgpr_read_b32 v45,a141
zaaa 184,52,8
ra 32,233,4096
ra 36,233,6144
zaaa 188,52,4
v_accvgpr_read_b32 v43,a139
v_accvgpr_read_b32 v42,a138
zaaa 204,48,4
dl 228,40,53,0
ta 96,236,24576
zaaa 172,56,4
ta 98,236,25088
v_accvgpr_read_b32 v41,a137
zaaa 156,60,4
ta 100,237,24576
v_accvgpr_read_b32 v39,a135
zaaa 152,60,8
ta 102,237,25088
ta 104,238,28672
zaaa 168,56,8
v_accvgpr_read_b32 v38,a134
v_accvgpr_read_b32 v35,a131
zaaa 164,56,12
dl 229,40,53,1024
ta 106,238,29184
zaaa 148,60,12
s_add_u32 s53,s53,s57
ta 108,239,28672
v_accvgpr_read_b32 v34,a130
zaaa 144,60,16
ta 110,239,29184
v_accvgpr_read_b32 v33,a129
zaaa 160,56,16
ta 44,235,16384
ta 46,235,16896
ta 40,234,16384
ta 42,234,16896
ta 52,237,16384
ta 54,237,16896
ta 48,236,16384
ta 50,236,16896
ta 60,235,20480
ta 62,235,20992
ta 56,234,20480
ta 58,234,20992
v_accvgpr_read_b32 v32,a128
zaaa 128,64,16
v_accvgpr_read_b32 v37,a133
v_accvgpr_read_b32 v36,a132
zaaa 112,68,16
dl 230,44,54,2048
zaaa 116,68,12
v_accvgpr_read_b32 v40,a136
v_accvgpr_read_b32 v44,a140
zaaa 132,64,12
cv 251,42,43
cv 250,40,41
zaaa 136,64,8
cv 253,46,47
cv 252,44,45
zaaa 120,68,8
cv 249,38,39
cv 247,34,35
zaaa 124,68,4
dl 231,44,54,3072
zaaa 140,64,4
s_add_u32 m0,s61,4096
cv 246,32,33
cv 248,36,37
zvaa 40,72,4
s_add_u32 s54,s54,s57
ta 68,237,20480
cv 123,22,23
zvaa 44,76,4
s_add_u32 s61,s63,s98
ta 70,237,20992
cv 121,18,19
zvaa 52,76,8
s_add_u32 s63,s63,s99
ta 64,236,20480
cv 120,16,17
zvaa 48,72,8
dl 224,36,52,0
zvaa 56,72,12
ta 66,236,20992
cv 122,20,21
zvaa 60,76,12
cv 119,14,15
cv 118,12,13
)"
R"(zvaa 68,76,16
cv 117,10,11
cv 116,8,9
zvaa 64,72,16
cv 156,88,89
cv 153,82,83
zvaa 32,80,16
dl 225,36,52,1024
ta 76,235,24576
ta 78,235,25088
zvaa 36,84,16
ta 72,234,24576
ta 74,234,25088
zvaa 28,84,12
cv 152,80,81
cv 155,86,87
zvaa 24,80,12
cv 154,84,85
cv 149,74,75
zvaa 16,80,8
cv 148,72,73
v_add_u32_e32 v232,s99,v232
zvaa 20,84,8
dl 226,36,52,2048
zvaa 12,84,4
cv 115,6,7
cv 114,4,5
zvaa 8,80,4
cv 113,2,3
cv 112,0,1
zvaa 76,4,88
zvaa 96,4,92
zvaa 100,8,92
dl 227,36,52,3072
zvaa 80,8,88
s_add_u32 s52,s52,s60
zvaa 84,12,88
zvaa 104,12,92
zvaa 108,16,92
s_waitcnt vmcnt(23) lgkmcnt(0)
s_barrier
zvaa 88,16,88
buffer_store_dwordx4 v[250:253],v244,s[24:27],0 offen offset:1024 sc0 sc1
buffer_store_dwordx4 v[246:249],v244,s[24:27],0 offen offset:0 sc0 sc1
s_branch L_kvx
L_vtile:
s_waitcnt vmcnt(8)
s_barrier
ta 64,234,0
ta 66,234,512
ra 0,232,0
ta 68,235,0
ta 70,235,512
ra 4,232,2048
ra 8,232,4096
ra 12,232,6144
ra 16,232,8192
ra 20,232,10240
ra 24,232,12288
ra 28,232,14336
s_add_u32 m0,s62,32768
v_mov_b32_e32 v212,0
dl 228,40,53,0
v_mov_b32_e32 v213,0
v_mov_b32_e32 v214,0
v_mov_b32_e32 v215,0
v_mov_b32_e32 v220,0
v_mov_b32_e32 v221,0
v_mov_b32_e32 v222,0
v_mov_b32_e32 v223,0
v_mov_b32_e32 v216,0
v_mov_b32_e32 v217,0
v_mov_b32_e32 v218,0
v_mov_b32_e32 v219,0
v_mov_b32_e32 v208,0
v_mov_b32_e32 v209,0
v_mov_b32_e32 v210,0
v_mov_b32_e32 v211,0
v_mov_b32_e32 v148,0
s_waitcnt lgkmcnt(9)
zaaa 128,0,64
s_lshl_b32 s4,%1,1
ta 72,236,0
v_mov_b32_e32 v149,0
s_waitcnt lgkmcnt(8)
zaaa 136,0,68
s_add_u32 s4,s4,s12
ta 74,236,512
v_mov_b32_e32 v150,0
s_waitcnt lgkmcnt(8)
zaaa 140,4,68
s_mul_i32 s4,s4,1280
ta 76,237,0
ta 78,237,512
zaaa 132,4,64
dl 229,40,53,1024
s_waitcnt lgkmcnt(4)
zvaa 32,8,64
s_add_u32 s53,s53,s57
v_mov_b32_e32 v151,0
v_mov_b32_e32 v156,0
zvaa 40,8,68
s_mov_b32 s55,s4
v_mov_b32_e32 v157,0
v_mov_b32_e32 v158,0
zvaa 44,12,68
s_mul_i32 s4,%1,1792
ta 80,234,4096
v_mov_b32_e32 v159,0
zvaa 36,12,64
dl 230,44,54,2048
zvaa 96,16,64
s_lshl_b32 s88,%1,2
ta 82,234,4608
v_mov_b32_e32 v152,0
zvaa 104,16,68
s_cmp_eq_u32 s12,2
s_cselect_b32 s55,s4,s55
ta 84,235,4096
v_mov_b32_e32 v153,0
zvaa 108,20,68
s_lshl_b32 s4,%1,14
ta 86,235,4608
v_mov_b32_e32 v154,0
zvaa 100,20,64
dl 231,44,54,3072
zvaa 160,24,64
s_add_u32 m0,s98,36864
v_mov_b32_e32 v155,0
v_mov_b32_e32 v144,0
zvaa 168,24,68
s_add_u32 s54,s54,s57
v_mov_b32_e32 v145,0
v_mov_b32_e32 v146,0
zvaa 172,28,68
s_add_u32 s88,s88,s12
v_mov_b32_e32 v147,0
v_mov_b32_e32 v84,0
zvaa 164,28,64
dl 224,36,52,0
s_waitcnt lgkmcnt(4)
zvaa 180,28,72
s_add_u32 s5,s4,16384
v_mov_b32_e32 v85,0
v_mov_b32_e32 v86,0
zvaa 188,28,76
s_cmp_eq_u32 s12,3
s_cselect_b32 s55,s4,s55
ra 32,233,0
v_mov_b32_e32 v87,0
zvaa 184,24,76
s_mov_b32 s4,10
v_mov_b32_e32 v92,0
v_mov_b32_e32 v93,0
zvaa 176,24,72
dl 225,36,52,1024
zvaa 116,20,72
s_lshl_b32 s88,s88,12
ra 36,233,2048
v_mov_b32_e32 v94,0
zvaa 124,20,76
s_xor_b32 s87,s19,15
ra 40,233,4096
v_mov_b32_e32 v95,0
zvaa 120,16,76
s_cmp_eq_u32 s12,2
s_cselect_b32 s4,14,s4
v_mov_b32_e32 v88,0
v_mov_b32_e32 v89,0
zvaa 112,16,72
dl 226,36,52,2048
zvaa 52,12,72
s_waitcnt vmcnt(11)
s_barrier
zvaa 60,12,76
s_add_u32 s30,s68,s88
s_addc_u32 s31,s69,0
ta 96,234,16384
v_mov_b32_e32 v90,0
zvaa 56,8,76
s_lshl_b32 s22,s87,7
ta 98,234,16896
v_mov_b32_e32 v91,0
zvaa 48,8,72
dl 227,36,52,3072
zaaa 148,4,72
s_add_u32 m0,s62,49152
ta 100,235,16384
v_mov_b32_e32 v80,0
zaaa 156,4,76
s_add_u32 s52,s52,s60
ta 102,235,16896
v_mov_b32_e32 v81,0
zaaa 152,0,76
s_lshl_b32 s87,s87,2
ra 44,233,6144
v_mov_b32_e32 v82,0
zaaa 144,0,72
dl 228,40,53,0
s_waitcnt lgkmcnt(5)
zvaa 0,0,80
s_lshl_b32 s88,s19,2
ra 48,233,8192
v_mov_b32_e32 v83,0
zvaa 8,0,84
s_cmp_eq_u32 s12,3
s_cselect_b32 s4,10,s4
ra 52,233,10240
v_mov_b32_e32 v20,0
zvaa 12,4,84
s_add_u32 s87,s87,s12
ra 56,233,12288
v_mov_b32_e32 v21,0
zvaa 4,4,80
dl 229,40,53,1024
zvaa 64,8,80
s_add_u32 s88,s88,s12
v_mov_b32_e32 v22,0
v_mov_b32_e32 v23,0
zvaa 72,8,84
s_lshl_b32 s90,s18,13
ra 60,233,14336
v_mov_b32_e32 v28,0
zvaa 76,12,84
s_add_u32 s90,s90,62914560
ta 104,236,16384
v_mov_b32_e32 v29,0
zvaa 68,12,80
dl 230,44,54,2048
zvaa 128,16,80
s_mul_i32 s14,s87,327680
ta 106,236,16896
v_mov_b32_e32 v30,0
zvaa 136,16,84
s_mul_i32 s13,s88,327680
ta 108,237,16384
v_mov_b32_e32 v31,0
zvaa 140,20,84
s_add_u32 s14,s14,s90
ta 110,237,16896
v_mov_b32_e32 v24,0
zvaa 132,20,80
dl 231,44,54,3072
zvaa 192,24,80
s_add_u32 m0,s98,65536
v_mov_b32_e32 v25,0
v_mov_b32_e32 v26,0
zvaa 200,24,84
s_add_u32 s13,s13,s90
v_mov_b32_e32 v27,0
v_mov_b32_e32 v16,0
zvaa 204,28,84
s_mul_i32 s90,s18,524288
v_mov_b32_e32 v17,0
v_mov_b32_e32 v18,0
zvaa 196,28,80
dl 224,36,52,0
s_waitcnt lgkmcnt(4)
maaa 128,32,96
s_lshl_b32 s91,s12,12
ta 208,236,4096
v_mov_b32_e32 v19,0
maaa 136,32,100
s_add_u32 s90,s90,s91
ta 210,236,4608
v_lshlrev_b32_e32 v241,7,v247
maaa 140,36,100
s_add_u32 s90,s90,16777216
ta 216,237,4096
v_mov_b32_e32 v242,134217728
maaa 132,36,96
dl 225,36,52,1024
mvaa 32,40,96
s_cmp_eq_u32 s12,3
s_cselect_b64 s[26:27],-1,0
ta 218,237,4608
v_lshrrev_b32_e32 v248,3,v247
mvaa 40,40,100
s_lshl_b32 s86,s19,1
v_and_b32_e32 v250,112,v244
v_and_b32_e32 v251,16,v247
mvaa 44,44,100
s_mov_b64 s[48:49],s[72:73]
v_lshlrev_b32_e32 v251,1,v251
v_cmp_gt_u32_e64 s[24:25],s4,v247
mvaa 36,44,96
dl 226,36,52,2048
mvaa 96,48,96
s_mov_b32 s50,8388608
v_mul_u32_u24_e32 v246,6144,v248
v_add_u32_e32 v249,s90,v244
mvaa 104,48,100
s_mov_b32 s59,655360
ta 112,234,20480
v_cndmask_b32_e64 v241,v242,v241,s[24:25]
mvaa 108,52,100
s_cmp_eq_u32 s12,2
s_cselect_b64 s[24:25],-1,0
ta 114,234,20992
v_add_u32_e32 v255,16777216,v249
mvaa 100,52,96
dl 227,36,52,3072
mvaa 160,56,96
s_add_u32 s52,s52,s56
s_add_u32 m0,s98,69632
ta 116,235,20480
mvaa 168,56,100
s_add_u32 s86,s86,2
ta 118,235,20992
v_accvgpr_write_b32 a240,v249
mvaa 172,60,100
s_cmp_eq_u32 s12,2
s_cselect_b64 s[48:49],s[80:81],s[48:49]
v_and_b32_e32 v249,48,v247
v_accvgpr_write_b32 a241,v255
mvaa 164,60,96
dl 224,36,52,0
s_waitcnt lgkmcnt(4)
mvaa 180,60,104
s_cmp_eq_u32 s12,2
s_cselect_b32 s50,6291456,s50
ta 212,236,20480
v_xor_b32_e32 v249,v249,v250
mvaa 188,60,108
s_cmp_eq_u32 s12,2
s_cselect_b32 s59,458752,s59
v_xor_b32_e32 v250,v250,v251
v_lshl_add_u32 v252,v248,11,v249
mvaa 184,56,108
s_lshl_b32 s15,1,s86
v_and_b32_e32 v251,48,v250
v_xor_b32_e32 v253,64,v252
mvaa 176,56,104
dl 225,36,52,1024
mvaa 116,52,104
s_lshl_b32 s23,s19,7
v_accvgpr_write_b32 a242,v252
v_add_u32_e32 v246,v246,v251
mvaa 124,52,108
s_or_b32 s86,%1,s12
s_cselect_b32 s65,0,4
v_add_u32_e32 v242,15360,v253
v_and_b32_e32 v251,64,v250
mvaa 120,48,108
s_cmp_eq_u32 s12,3
s_cselect_b64 s[48:49],s[68:69],s[48:49]
v_lshl_add_u32 v248,v251,5,v246
v_xor_b32_e32 v249,64,v251
mvaa 112,48,104
dl 226,36,52,2048
mvaa 52,44,104
s_cmp_eq_u32 s12,3
s_cselect_b32 s50,s5,s50
v_accvgpr_write_b32 a243,v242
v_mul_u32_u24_e32 v251,768,v251
mvaa 60,44,108
s_cmp_eq_u32 s12,3
s_cselect_b32 s59,1280,s59
v_add_u32_e32 v242,30720,v252
v_lshl_add_u32 v249,v249,5,v246
mvaa 56,40,108
s_add_u32 s53,s53,s58
v_add_u32_e32 v246,v246,v251
v_accvgpr_write_b32 a244,v242
mvaa 48,40,104
dl 227,36,52,3072
maaa 148,36,104
s_add_u32 s54,s54,s58
v_add_u32_e32 v242,46080,v253
v_accvgpr_write_b32 a245,v242
maaa 156,36,108
s_add_u32 s52,s52,s60
v_add_u32_e32 v243,4096,v246
v_add_u32_e32 v242,0,v248
maaa 152,32,108
s_mov_b32 s63,32768
ta 214,236,20992
v_cndmask_b32_e64 v242,v228,v242,s[24:25]
maaa 144,32,104
s_mov_b32 s61,s98
ta 220,237,20480
v_cndmask_b32_e64 v242,v242,v243,s[26:27]
s_waitcnt lgkmcnt(2)
mvaa 0,32,112
s_mov_b32 s64,1
v_add_u32_e32 v243,101376,v246
v_and_b32_e32 v246,15,v247
mvaa 8,32,116
v_and_b32_e32 v246,12,v247
v_accvgpr_write_b32 a246,v242
mvaa 12,36,116
ta 222,237,20992
v_add_u32_e32 v242,48128,v249
mvaa 4,36,112
v_cndmask_b32_e64 v242,v229,v242,s[24:25]
v_lshlrev_b32_e32 v246,5,v246
mvaa 64,40,112
v_cndmask_b32_e64 v242,v242,v243,s[26:27]
v_accvgpr_write_b32 a247,v242
mvaa 72,40,116
v_add_u32_e32 v242,96256,v248
v_and_b32_e32 v248,3,v247
mvaa 76,44,116
v_cndmask_b32_e64 v242,v230,v242,s[24:25]
v_lshl_add_u32 v246,v248,3,v246
mvaa 68,44,112
v_and_b32_e32 v248,24,v247
v_accvgpr_write_b32 a248,v242
mvaa 128,48,112
v_add_u32_e32 v242,144384,v249
v_lshl_add_u32 v246,v248,2,v246
mvaa 136,48,116
v_and_b32_e32 v248,32,v247
v_cndmask_b32_e64 v242,v231,v242,s[24:25]
mvaa 140,52,116
v_add_u32_e32 v246,98304,v246
v_accvgpr_write_b32 a249,v242
mvaa 132,52,112
v_lshl_add_u32 v238,v248,5,v246
v_xor_b32_e32 v239,32,v238
mvaa 192,56,112
v_add_u32_e32 v232,32768,v232
v_add_u32_e32 v233,32768,v233
mvaa 200,56,116
mvaa 204,60,116
mvaa 196,60,112
s_add_u32 s4,s96,L_vrv-L_att_pc
s_addc_u32 s5,s97,0
s_mov_b64 exec,0
s_cmp_eq_u32 s12,0
s_cbranch_scc1 L_vs4
s_cmp_eq_u32 s12,1
s_cbranch_scc1 L_vs5
s_cmp_eq_u32 s12,2
s_cbranch_scc1 L_vs6
s_branch L_vs7
L_vrv:
s_mov_b64 exec,-1
s_waitcnt vmcnt(8)
s_waitcnt lgkmcnt(0)
s_barrier
L_v_head:
mvaa 212,28,208
ra 64,232,0
mvaa 212,60,212
s_add_u32 m0,s62,0
ta 192,234,32768
mvaa 220,60,220
dl 228,40,53,0
mvaa 220,28,216
ta 194,234,33280
s_nop 3
mvaa 216,24,216
ra 96,233,0
mvaa 216,56,220
L_vs4:
s_cbranch_execz L_vs8
ta 196,234,49152
mvaa 208,56,212
ta 198,234,49664
mvaa 208,24,208
ta 204,235,49152
mvaa 148,20,208
dl 229,40,53,1024
mvaa 148,52,212
s_add_u32 s53,s53,s57
ta 206,235,49664
mvaa 156,52,220
ta 200,235,32768
mvaa 156,20,216
ta 202,235,33280
mvaa 152,16,216
ra 68,232,2048
mvaa 152,48,220
ra 100,233,2048
mvaa 144,48,212
ra 72,232,4096
mvaa 144,16,208
dl 230,44,54,2048
mvaa 84,12,208
ra 104,233,4096
mvaa 84,44,212
ra 76,232,6144
mvaa 92,44,220
ra 108,233,6144
mvaa 92,12,216
ra 80,232,8192
mvaa 88,8,216
ra 112,233,8192
mvaa 88,40,220
dl 231,44,54,3072
mvaa 80,40,212
s_add_u32 s54,s54,s57
ra 84,232,10240
mvaa 80,8,208
ra 116,233,10240
mvaa 20,4,208
ra 88,232,12288
mvaa 20,36,212
ra 120,233,12288
mvaa 28,36,220
ra 92,232,14336
mvaa 28,4,216
s_add_u32 m0,s62,16384
ra 124,233,14336
mvaa 24,0,216
dl 228,40,53,0
mvaa 24,32,220
mvaa 16,32,212
mvaa 16,0,208
s_waitcnt lgkmcnt(8)
maaa 128,64,192
maaa 128,96,196
maaa 136,96,204
dl 229,40,53,1024
maaa 136,64,200
s_add_u32 s53,s53,s58
maaa 140,68,200
maaa 140,100,204
maaa 132,100,196
maaa 132,68,192
mvaa 32,72,192
mvaa 32,104,196
dl 230,44,54,2048
mvaa 40,104,204
mvaa 40,72,200
s_waitcnt lgkmcnt(0)
mvaa 44,76,200
ta 208,236,32768
mvaa 44,108,204
ta 210,236,33280
mvaa 36,108,196
ta 212,236,49152
mvaa 36,76,192
dl 231,44,54,3072
mvaa 96,80,192
s_add_u32 s54,s54,s58
ta 214,236,49664
mvaa 96,112,196
ta 220,237,49152
mvaa 104,112,204
ta 222,237,49664
mvaa 104,80,200
ta 216,237,32768
mvaa 108,84,200
ta 218,237,33280
mvaa 108,116,204
mvaa 100,116,196
s_add_u32 m0,s61,0
mvaa 100,84,192
dl 224,36,52,0
mvaa 160,88,192
mvaa 160,120,196
mvaa 168,120,204
mvaa 168,88,200
mvaa 172,92,200
mvaa 172,124,204
mvaa 164,124,196
dl 225,36,52,1024
mvaa 164,92,192
s_waitcnt lgkmcnt(0)
mvaa 180,92,208
mvaa 180,124,212
mvaa 188,124,220
ta 192,234,36864
mvaa 188,92,216
ta 194,234,37376
mvaa 184,88,216
ta 196,234,53248
mvaa 184,120,220
dl 226,36,52,2048
mvaa 176,120,212
ta 198,234,53760
mvaa 176,88,208
ta 204,235,53248
mvaa 116,84,208
ta 206,235,53760
mvaa 116,116,212
ta 200,235,36864
mvaa 124,116,220
ta 202,235,37376
mvaa 124,84,216
mvaa 120,80,216
dl 227,36,52,3072
mvaa 120,112,220
s_add_u32 s52,s52,s56
mvaa 112,112,212
mvaa 112,80,208
mvaa 52,76,208
mvaa 52,108,212
mvaa 60,108,220
mvaa 60,76,216
mvaa 56,72,216
s_add_u32 m0,s61,4096
mvaa 56,104,220
dl 224,36,52,0
mvaa 48,104,212
s_add_u32 s61,s63,s98
mvaa 48,72,208
s_cmp_ge_u32 s63,65536
s_cselect_b32 s99,-2,1
s_lshl_b32 s99,s99,15
maaa 148,68,208
s_add_u32 s63,s63,s99
v_add_u32_e32 v232,s99,v232
v_add_u32_e32 v233,s99,v233
maaa 148,100,212
maaa 156,100,220
maaa 156,68,216
maaa 152,64,216
dl 225,36,52,1024
maaa 152,96,220
maaa 144,96,212
s_waitcnt lgkmcnt(0)
maaa 144,64,208
mvaa 0,64,192
mvaa 0,96,196
mvaa 8,96,204
ta 208,236,36864
mvaa 8,64,200
ta 210,236,37376
mvaa 12,68,200
dl 226,36,52,2048
mvaa 12,100,204
ta 212,236,53248
mvaa 4,100,196
ta 214,236,53760
mvaa 4,68,192
ta 220,237,53248
mvaa 64,72,192
ta 222,237,53760
mvaa 64,104,196
ta 216,237,36864
mvaa 72,104,204
ta 218,237,37376
mvaa 72,72,200
dl 227,36,52,3072
mvaa 76,76,200
s_add_u32 s52,s52,s60
mvaa 76,108,204
mvaa 68,108,196
mvaa 68,76,192
mvaa 128,80,192
mvaa 128,112,196
buffer_load_dword a184,v241,s[48:51],s55 offen
mvaa 136,112,204
s_add_u32 s55,s55,s59
mvaa 136,80,200
mvaa 140,84,200
mvaa 140,116,204
mvaa 132,116,196
mvaa 132,84,192
buffer_load_dword a185,v241,s[48:51],s55 offen
mvaa 192,88,192
s_add_u32 s55,s55,s59
mvaa 192,120,196
mvaa 200,120,204
mvaa 200,88,200
mvaa 204,92,200
mvaa 204,124,204
s_cmp_eq_u32 s64,3
mvaa 196,124,196
s_waitcnt vmcnt(10) lgkmcnt(0)
s_barrier
mvaa 196,92,192
s_cbranch_scc1 L_v_exit
mvaa 212,92,208
ra 0,232,0
mvaa 212,124,212
s_add_u32 m0,s62,32768
ta 192,234,0
mvaa 220,124,220
dl 228,40,53,0
mvaa 220,92,216
ta 194,234,512
s_nop 3
mvaa 216,88,216
ra 32,233,0
mvaa 216,120,220
ta 196,234,16384
mvaa 208,120,212
ta 198,234,16896
mvaa 208,88,208
ta 204,235,16384
mvaa 148,84,208
dl 229,40,53,1024
mvaa 148,116,212
s_add_u32 s53,s53,s57
ta 206,235,16896
mvaa 156,116,220
ta 200,235,0
mvaa 156,84,216
ta 202,235,512
mvaa 152,80,216
ra 4,232,2048
mvaa 152,112,220
ra 36,233,2048
mvaa 144,112,212
ra 8,232,4096
mvaa 144,80,208
dl 230,44,54,2048
mvaa 84,76,208
ra 40,233,4096
mvaa 84,108,212
ra 12,232,6144
mvaa 92,108,220
ra 44,233,6144
mvaa 92,76,216
ra 16,232,8192
mvaa 88,72,216
ra 48,233,8192
mvaa 88,104,220
dl 231,44,54,3072
mvaa 80,104,212
s_add_u32 s54,s54,s57
ra 20,232,10240
mvaa 80,72,208
ra 52,233,10240
mvaa 20,68,208
ra 24,232,12288
mvaa 20,100,212
ra 56,233,12288
mvaa 28,100,220
ra 28,232,14336
mvaa 28,68,216
s_add_u32 m0,s62,49152
ra 60,233,14336
mvaa 24,64,216
dl 228,40,53,0
mvaa 24,96,220
mvaa 16,96,212
mvaa 16,64,208
s_waitcnt lgkmcnt(8)
maaa 128,0,192
maaa 128,32,196
maaa 136,32,204
dl 229,40,53,1024
maaa 136,0,200
s_add_u32 s53,s53,s58
maaa 140,4,200
maaa 140,36,204
maaa 132,36,196
maaa 132,4,192
mvaa 32,8,192
mvaa 32,40,196
dl 230,44,54,2048
mvaa 40,40,204
mvaa 40,8,200
s_waitcnt lgkmcnt(0)
mvaa 44,12,200
ta 208,236,0
mvaa 44,44,204
ta 210,236,512
mvaa 36,44,196
ta 212,236,16384
mvaa 36,12,192
dl 231,44,54,3072
mvaa 96,16,192
s_add_u32 s54,s54,s58
ta 214,236,16896
mvaa 96,48,196
ta 220,237,16384
mvaa 104,48,204
ta 222,237,16896
mvaa 104,16,200
ta 216,237,0
mvaa 108,20,200
ta 218,237,512
mvaa 108,52,204
mvaa 100,52,196
s_add_u32 m0,s61,0
mvaa 100,20,192
dl 224,36,52,0
mvaa 160,24,192
s_lshl_b32 s86,s21,2
s_add_u32 s86,s86,s12
s_mul_i32 s86,s86,384
mvaa 160,56,196
s_add_u32 s86,s86,4096
v_lshl_add_u32 v253,v244,3,s86
mvaa 168,56,204
s_mov_b64 exec,7
global_load_dword a188,v253,s[96:97]
s_mov_b64 exec,-1
mvaa 168,24,200
mvaa 172,28,200
mvaa 172,60,204
mvaa 164,60,196
dl 225,36,52,1024
mvaa 164,28,192
s_waitcnt lgkmcnt(0)
mvaa 180,28,208
mvaa 180,60,212
mvaa 188,60,220
ta 192,234,4096
mvaa 188,28,216
ta 194,234,4608
mvaa 184,24,216
ta 196,234,20480
mvaa 184,56,220
dl 226,36,52,2048
mvaa 176,56,212
ta 198,234,20992
mvaa 176,24,208
ta 204,235,20480
mvaa 116,20,208
ta 206,235,20992
mvaa 116,52,212
ta 200,235,4096
mvaa 124,52,220
ta 202,235,4608
mvaa 124,20,216
mvaa 120,16,216
dl 227,36,52,3072
mvaa 120,48,220
s_add_u32 s52,s52,s56
mvaa 112,48,212
mvaa 112,16,208
mvaa 52,12,208
mvaa 52,44,212
s_bitcmp1_b32 s65,s64
s_cbranch_scc1 L_v_ev
L_v_evr:
mvaa 60,44,220
mvaa 60,12,216
mvaa 56,8,216
s_add_u32 m0,s61,4096
mvaa 56,40,220
dl 224,36,52,0
mvaa 48,40,212
s_add_u32 s61,s63,s98
mvaa 48,8,208
s_cmp_ge_u32 s63,65536
s_cselect_b32 s99,-2,1
s_lshl_b32 s99,s99,15
maaa 148,4,208
s_add_u32 s63,s63,s99
v_add_u32_e32 v232,s99,v232
v_add_u32_e32 v233,s99,v233
maaa 148,36,212
maaa 156,36,220
maaa 156,4,216
maaa 152,0,216
dl 225,36,52,1024
maaa 152,32,220
maaa 144,32,212
s_waitcnt lgkmcnt(0)
maaa 144,0,208
mvaa 0,0,192
mvaa 0,32,196
mvaa 8,32,204
ta 208,236,4096
mvaa 8,0,200
ta 210,236,4608
mvaa 12,4,200
dl 226,36,52,2048
mvaa 12,36,204
ta 212,236,20480
mvaa 4,36,196
ta 214,236,20992
mvaa 4,4,192
ta 220,237,20480
mvaa 64,8,192
ta 222,237,20992
mvaa 64,40,196
ta 216,237,4096
mvaa 72,40,204
ta 218,237,4608
mvaa 72,8,200
dl 227,36,52,3072
mvaa 76,12,200
s_add_u32 s52,s52,s60
mvaa 76,44,204
mvaa 68,44,196
mvaa 68,12,192
mvaa 128,16,192
mvaa 128,48,196
buffer_load_dword a187,v241,s[48:51],s55 offen
mvaa 136,48,204
s_add_u32 s55,s55,s59
mvaa 136,16,200
mvaa 140,20,200
mvaa 140,52,204
mvaa 132,52,196
mvaa 132,20,192
buffer_load_dword a190,v241,s[48:51],s55 offen
mvaa 192,24,192
s_add_u32 s55,s55,s59
mvaa 192,56,196
mvaa 200,56,204
s_add_u32 s64,s64,1
mvaa 200,24,200
mvaa 204,28,200
mvaa 204,60,204
mvaa 196,60,196
s_waitcnt vmcnt(10) lgkmcnt(0)
s_barrier
mvaa 196,28,192
s_branch L_v_head
L_v_exit:
global_load_dwordx4 a[160:163],v244,s[30:31] offset:0
s_add_u32 m0,s62,32768
mvaa 212,92,208
s_cmp_lt_u32 s12,2
s_cselect_b32 s86,s22,s23
ta 32,234,0
s_and_b32 s87,s12,1
mvaa 212,124,212
s_lshl_b32 s86,s86,11
ta 34,234,512
s_lshl_b32 s87,s87,17
mvaa 220,124,220
s_add_u32 s86,s86,s87
ra 0,232,0
s_mov_b64 s[36:37],s[68:69]
mvaa 220,92,216
s_mov_b32 s38,4194304
ta 36,235,0
s_mov_b32 s52,s86
mvaa 216,88,216
s_mov_b32 s56,65536
ta 38,235,512
v_accvgpr_read_b32 v224,a242
mvaa 216,120,220
dl 228,40,53,0
mvaa 208,120,212
s_lshl_b32 s88,s18,1
ra 4,232,2048
v_accvgpr_read_b32 v225,a243
mvaa 208,88,208
s_add_u32 s88,s88,s12
ra 8,232,4096
v_accvgpr_read_b32 v226,a244
mvaa 148,84,208
s_mov_b32 s91,196608
ra 12,232,6144
v_accvgpr_read_b32 v227,a245
mvaa 148,116,212
s_lshl_b32 s88,s88,7
ra 16,232,8192
s_lshl_b32 s89,s18,6
mvaa 156,116,220
s_mov_b32 s90,6291456
ra 20,232,10240
s_mov_b32 s60,-65408
mvaa 156,84,216
dl 229,40,53,1024
mvaa 152,80,216
s_add_u32 s53,s53,s57
ra 24,232,12288
s_bfe_u32 s4,s21,65536
mvaa 152,112,220
s_lshl_b32 s4,s4,6
ra 28,232,14336
s_lshl_b32 s87,s20,2
mvaa 144,112,212
s_mov_b32 s26,32768
ta 40,236,0
s_mov_b32 s27,131072
mvaa 144,80,208
s_add_u32 s61,s98,32768
ta 42,236,512
s_mov_b32 s63,65536
mvaa 84,76,208
s_cmp_ge_u32 s63,65536
s_cselect_b32 s99,-2,1
ta 44,237,0
s_lshl_b32 s99,s99,15
mvaa 84,108,212
dl 230,44,54,2048
mvaa 92,108,220
s_cmp_eq_u32 s12,0
s_cselect_b32 s65,96,0
ta 46,237,512
s_mov_b32 s67,2048
mvaa 92,76,216
s_mov_b32 s66,8
ta 48,234,4096
s_mov_b32 s64,1
mvaa 88,72,216
s_or_b32 s65,s65,128
ta 50,234,4608
mvaa 88,104,220
s_bitcmp1_b32 s12,0
s_cselect_b64 vcc,-1,0
ta 52,235,4096
mvaa 80,104,212
ta 54,235,4608
mvaa 80,72,208
dl 231,44,54,3072
s_add_u32 m0,s62,49152
mvaa 20,68,208
s_add_u32 s54,s54,s57
ta 56,236,4096
mvaa 20,100,212
s_cmp_lt_u32 s12,2
s_cselect_b32 s57,262144,s91
ta 58,236,4608
mvaa 28,100,220
ta 60,237,4096
)"
R"(mvaa 28,68,216
ta 62,237,4608
mvaa 24,64,216
mvaa 24,96,220
dl 228,40,53,0
mvaa 16,96,212
v_accvgpr_read_b32 v228,a246
mvaa 16,64,208
L_vs5:
s_cbranch_execz L_vs9
s_waitcnt lgkmcnt(5)
maaa 128,0,32
maaa 136,0,36
maaa 140,4,36
dl 229,40,53,1024
maaa 132,4,32
s_waitcnt lgkmcnt(0)
s_barrier
mvaa 32,8,32
s_add_u32 s53,s53,s58
ta 96,234,16384
v_accvgpr_read_b32 v229,a247
mvaa 40,8,36
s_cmp_lt_u32 s12,2
s_cselect_b32 s42,8388608,s90
ta 98,234,16896
mvaa 44,12,36
s_cmp_lt_u32 s12,2
s_cselect_b32 s53,s88,s89
ra 64,233,0
s_and_b32 s89,s98,8192
mvaa 36,12,32
dl 230,44,54,2048
mvaa 96,16,32
s_bitcmp1_b32 s21,0
s_cselect_b32 s90,s57,0
ta 100,235,16384
s_add_u32 s53,s53,s90
mvaa 104,16,36
s_cmp_lt_u32 s12,2
s_cselect_b64 s[40:41],s[72:73],s[80:81]
ta 102,235,16896
v_accvgpr_read_b32 v230,a248
mvaa 108,20,36
s_lshl_b32 s90,s90,1
ra 68,233,2048
s_add_u32 s91,s89,65536
mvaa 100,20,32
s_bfe_u32 s88,s21,65539
ra 72,233,4096
s_lshl_b32 s88,s88,1
mvaa 160,24,32
dl 231,44,54,3072
mvaa 168,24,36
s_add_u32 m0,s98,65536
ra 76,233,6144
s_add_u32 s54,s54,s58
mvaa 172,28,36
s_cmp_eq_u32 s12,3
s_cselect_b64 s[44:45],s[68:69],s[40:41]
ra 80,233,8192
v_accvgpr_read_b32 v231,a249
mvaa 164,28,32
s_cmp_eq_u32 s12,3
s_cselect_b32 s46,0,s42
ra 84,233,10240
s_add_u32 s58,s57,s90
mvaa 180,28,40
s_cmp_eq_u32 s12,3
s_cselect_b32 s54,0,s53
ra 88,233,12288
s_sub_u32 s57,s57,s90
mvaa 188,28,44
global_load_dwordx4 a[168:171],v244,s[30:31] offset:2048
mvaa 184,24,44
s_add_u32 s90,s89,65536
ta 104,236,16384
s_add_u32 s88,s88,s87
mvaa 176,24,40
s_and_b32 s87,s12,1
ra 92,233,14336
s_add_u32 s88,s88,s87
mvaa 116,20,40
s_mul_i32 s88,s88,524288
ta 106,236,16896
s_and_b32 s87,s21,7
mvaa 124,20,44
s_add_u32 s86,s88,33554432
ta 108,237,16384
s_lshl_b32 s87,s87,16
mvaa 120,16,44
global_load_dwordx4 a[176:179],v244,s[84:85] offset:0
mvaa 112,16,40
s_add_u32 s86,s86,s87
ta 110,237,16896
s_lshr_b32 s87,s12,1
mvaa 52,12,40
s_lshl_b32 s87,s87,15
ta 112,234,20480
s_add_u32 s86,s86,s87
mvaa 60,12,44
s_add_u32 s24,s8,s86
s_addc_u32 s25,s9,0
ta 114,234,20992
s_lshl_b32 s86,%1,5
mvaa 56,8,44
s_lshl_b32 s87,s12,3
ta 116,235,20480
s_add_u32 s86,s86,s87
mvaa 48,8,40
global_load_dwordx4 a[164:167],v244,s[30:31] offset:1024
maaa 148,4,40
s_add_u32 s86,s86,83886080
ta 118,235,20992
s_mov_b32 s88,4096
maaa 156,4,44
ta 120,236,20480
maaa 152,0,44
ta 122,236,20992
maaa 144,0,40
ta 124,237,20480
mvaa 0,0,48
global_load_dwordx4 a[172:175],v244,s[30:31] offset:3072
mvaa 8,0,52
s_mov_b32 s30,1048577
ta 126,237,20992
s_mov_b32 s31,268435712
mvaa 12,4,52
mvaa 4,4,48
mvaa 64,8,48
mvaa 72,8,52
global_load_dwordx4 a[180:183],v244,s[84:85] offset:1024
mvaa 76,12,52
mvaa 68,12,48
mvaa 128,16,48
mvaa 136,16,52
mvaa 140,20,52
dl 224,36,52,0
mvaa 132,20,48
mvaa 192,24,48
mvaa 200,24,52
mvaa 204,28,52
mvaa 196,28,48
dl 225,36,52,1024
mvaa 212,28,56
mvaa 220,28,60
mvaa 216,24,60
mvaa 208,24,56
mvaa 148,20,56
dl 226,36,52,2048
mvaa 156,20,60
mvaa 152,16,60
mvaa 144,16,56
mvaa 84,12,56
s_waitcnt vmcnt(12) lgkmcnt(0)
s_barrier
mvaa 92,12,60
dl 227,36,52,3072
mvaa 88,8,60
s_add_u32 s52,s52,s56
s_add_u32 m0,s98,69632
ta 32,234,32768
mvaa 80,8,56
ta 34,234,33280
mvaa 20,4,56
ta 36,235,32768
mvaa 28,4,60
ta 38,235,33280
mvaa 24,0,60
dl 224,36,52,0
mvaa 16,0,56
ta 40,236,32768
maaa 128,64,96
ta 42,236,33280
maaa 136,64,100
ta 44,237,32768
maaa 140,68,100
ta 46,237,33280
maaa 132,68,96
dl 225,36,52,1024
mvaa 32,72,96
ta 48,234,36864
mvaa 40,72,100
ta 50,234,37376
mvaa 44,76,100
ta 52,235,36864
mvaa 36,76,96
ta 54,235,37376
mvaa 96,80,96
dl 226,36,52,2048
mvaa 104,80,100
ra 20,232,43008
mvaa 108,84,100
ra 0,232,32768
mvaa 100,84,96
ra 24,232,45056
mvaa 160,88,96
ra 4,232,34816
mvaa 168,88,100
dl 227,36,52,3072
mvaa 172,92,100
s_add_u32 m0,s62,0
ra 28,232,47104
s_add_u32 s52,s52,s60
mvaa 164,92,96
ra 8,232,36864
mvaa 180,92,104
ra 12,232,38912
mvaa 188,92,108
ra 16,232,40960
v_add_u32_e32 v232,s90,v232
mvaa 184,88,108
dl 228,40,53,0
mvaa 176,88,104
ta 60,237,36864
v_xor_b32_e32 v232,s4,v232
mvaa 116,84,104
ta 62,237,37376
mvaa 124,84,108
ta 56,236,36864
mvaa 120,80,108
ta 58,236,37376
mvaa 112,80,104
dl 229,40,53,1024
mvaa 52,76,104
s_add_u32 s53,s53,s57
mvaa 60,76,108
mvaa 56,72,108
mvaa 48,72,104
maaa 148,68,104
dl 230,44,54,2048
maaa 156,68,108
maaa 152,64,108
maaa 144,64,104
mvaa 0,64,112
mvaa 8,64,116
dl 231,44,54,3072
mvaa 12,68,116
s_add_u32 m0,s62,16384
s_add_u32 s54,s54,s57
mvaa 4,68,112
mvaa 64,72,112
mvaa 72,72,116
mvaa 76,76,116
dl 228,40,53,0
mvaa 68,76,112
mvaa 128,80,112
mvaa 136,80,116
mvaa 140,84,116
mvaa 132,84,112
dl 229,40,53,1024
mvaa 192,88,112
s_add_u32 s53,s53,s58
mvaa 200,88,116
mvaa 204,92,116
mvaa 196,92,112
mvaa 212,92,120
dl 230,44,54,2048
mvaa 220,92,124
mvaa 216,88,124
s_waitcnt vmcnt(20) lgkmcnt(0)
s_barrier
mvaa 208,88,120
ra 92,233,47104
mvaa 148,84,120
ta 96,234,49152
mvaa 156,84,124
dl 231,44,54,3072
mvaa 152,80,124
s_add_u32 m0,s98,0
ra 88,233,45056
s_add_u32 s54,s54,s58
mvaa 144,80,120
ta 98,234,49664
mvaa 84,76,120
ta 100,235,49152
mvaa 92,76,124
ta 116,235,53248
mvaa 88,72,124
dl 224,36,52,0
mvaa 80,72,120
ta 118,235,53760
mvaa 20,68,120
ta 102,235,49664
v_subrev_u32_e32 v235,s89,v235
mvaa 28,68,124
ta 104,236,49152
mvaa 24,64,124
ta 112,234,53248
mvaa 16,64,120
dl 225,36,52,1024
maaa 128,0,32
ta 124,237,53248
maaa 136,0,36
ta 126,237,53760
maaa 140,4,36
ta 120,236,53248
maaa 132,4,32
ta 122,236,53760
mvaa 32,8,32
dl 226,36,52,2048
mvaa 40,8,36
ta 114,234,53760
v_subrev_u32_e32 v234,s89,v234
mvaa 44,12,36
ta 106,236,49664
v_subrev_u32_e32 v236,s89,v236
mvaa 36,12,32
ta 108,237,49152
mvaa 52,12,40
ta 110,237,49664
v_subrev_u32_e32 v237,s89,v237
mvaa 60,12,44
dl 227,36,52,3072
mvaa 56,8,44
s_add_u32 s52,s52,s56
s_add_u32 m0,s98,4096
ra 84,233,43008
mvaa 48,8,40
ra 80,233,40960
maaa 148,4,40
ra 72,233,36864
maaa 156,4,44
ra 76,233,38912
maaa 152,0,44
dl 224,36,52,0
maaa 144,0,40
ra 68,233,34816
mvaa 0,0,48
ra 64,233,32768
v_add_u32_e32 v233,s91,v233
mvaa 8,0,52
v_xor_b32_e32 v233,s4,v233
mvaa 12,4,52
mvaa 4,4,48
dl 225,36,52,1024
mvaa 20,4,56
mvaa 28,4,60
mvaa 24,0,60
mvaa 16,0,56
mvaa 220,28,60
dl 226,36,52,2048
s_waitcnt lgkmcnt(2)
mvaa 220,92,124
mvaa 216,88,124
mvaa 216,24,60
mvaa 212,28,56
mvaa 212,92,120
dl 227,36,52,3072
mvaa 208,88,120
s_add_u32 m0,s62,32768
s_add_u32 s52,s52,s60
s_waitcnt vmcnt(25)
zvaa 246,160,176
mvaa 208,24,56
cv 223,222,223
cv 222,220,221
mvaa 204,28,52
cv 221,218,219
zvaa 250,168,176
mvaa 204,92,116
cv 220,216,217
cv 219,214,215
mvaa 200,88,116
dl 228,40,53,0
s_waitcnt vmcnt(25)
mvaa 246,164,180
mvaa 200,24,52
cv 218,212,213
mvaa 250,172,180
mvaa 196,28,48
cv 217,210,211
cv 216,208,209
mvaa 196,92,112
cv 215,206,207
v_cndmask_b32_e64 v246,0,v246,s[30:31]
mvaa 192,88,112
cv 214,204,205
v_cndmask_b32_e64 v250,0,v250,s[30:31]
mvaa 192,24,48
dl 229,40,53,1024
s_lshl_b64 s[30:31],s[30:31],1
v_cndmask_b32_e64 v246,v246,v247,s[30:31]
v_cndmask_b32_e64 v250,v250,v251,s[30:31]
mvaa 188,28,44
s_lshl_b64 s[30:31],s[30:31],1
s_add_u32 s53,s53,s57
v_cndmask_b32_e64 v246,v246,v248,s[30:31]
v_cndmask_b32_e64 v250,v250,v252,s[30:31]
mvaa 188,92,108
s_lshl_b64 s[30:31],s[30:31],1
cv 213,202,203
v_cndmask_b32_e64 v246,v246,v249,s[30:31]
mvaa 184,88,108
v_cndmask_b32_e64 v250,v250,v253,s[30:31]
v_add_f32_dpp v246,v246,v246 quad_perm:[1,0,3,2] row_mask:0xf bank_mask:0xf
mvaa 184,24,44
v_add_f32_dpp v250,v250,v250 quad_perm:[1,0,3,2] row_mask:0xf bank_mask:0xf
v_add_f32_dpp v246,v246,v246 quad_perm:[2,3,0,1] row_mask:0xf bank_mask:0xf
mvaa 180,28,40
dl 230,44,54,2048
v_add_f32_dpp v250,v250,v250 quad_perm:[2,3,0,1] row_mask:0xf bank_mask:0xf
mvaa 180,92,104
v_add_f32_dpp v246,v246,v246 row_half_mirror row_mask:0xf bank_mask:0xf
v_add_f32_dpp v250,v250,v250 row_half_mirror row_mask:0xf bank_mask:0xf
mvaa 176,88,104
v_add_f32_dpp v246,v246,v246 row_mirror row_mask:0xf bank_mask:0xf
v_add_f32_dpp v250,v250,v250 row_mirror row_mask:0xf bank_mask:0xf
mvaa 176,24,40
cv 212,200,201
cv 211,198,199
mvaa 172,28,36
v_permlane16_swap_b32_e32 v246,v250
mvaa 172,92,100
dl 231,44,54,3072
v_add_f32_e32 v246,v246,v250
v_mov_b32_e32 v250,v246
mvaa 168,88,100
s_add_u32 s54,s54,s57
v_permlane32_swap_b32_e32 v246,v250
mvaa 168,24,36
v_add_f32_e32 v246,v246,v250
v_lshrrev_b32_e32 v250,6,v244
mvaa 164,28,32
v_mul_f32_e32 v246,0xbfb8aa3b,v246
cv 210,196,197
mvaa 164,92,96
ex 246
mvaa 160,88,96
buffer_load_dword a242,v241,s[48:51],s55 offen
mvaa 160,24,32
s_add_u32 s55,s55,s59
v_add_f32_e32 v246,1.0,v246
cv 209,194,195
mvaa 156,20,60
v_rcp_f32_e32 v246,v246
mvaa 156,84,124
s_waitcnt vmcnt(17) lgkmcnt(0)
s_barrier
s_add_u32 m0,s62,49152
mvaa 152,80,124
cv 208,192,193
cv 207,190,191
mvaa 152,16,60
dl 228,40,53,0
mvaa 148,20,56
cv 206,188,189
cv 205,186,187
mvaa 148,84,120
cv 204,184,185
cv 203,182,183
mvaa 144,80,120
cv 202,180,181
cv 201,178,179
mvaa 144,16,56
cv 200,176,177
cv 199,174,175
mvaa 140,20,52
dl 229,40,53,1024
mvaa 140,84,116
s_add_u32 s53,s53,s58
cv 198,172,173
cv 197,170,171
mvaa 136,80,116
ra 4,232,0
cv 196,168,169
mvaa 136,16,52
ta 92,239,12288
cv 195,166,167
mvaa 132,20,48
ta 94,239,12800
cv 194,164,165
mvaa 132,84,112
dl 230,44,54,2048
mvaa 128,80,112
ta 88,238,12288
cv 193,162,163
mvaa 128,16,48
ta 90,238,12800
cv 192,160,161
mvaa 124,20,44
cv 191,158,159
cv 190,156,157
mvaa 124,84,108
cv 189,154,155
cv 188,152,153
mvaa 120,80,108
dl 231,44,54,3072
mvaa 120,16,44
s_add_u32 m0,s98,32768
cv 187,150,151
cv 186,148,149
mvaa 116,20,40
s_add_u32 s54,s54,s58
cv 185,146,147
cv 184,144,145
mvaa 116,84,104
cv 183,142,143
cv 182,140,141
mvaa 112,80,104
cv 181,138,139
cv 180,136,137
mvaa 112,16,40
dl 224,36,52,0
mvaa 108,20,36
cv 179,134,135
cv 178,132,133
mvaa 108,84,100
cv 177,130,131
cv 176,128,129
mvaa 104,80,100
cv 175,126,127
cv 174,124,125
mvaa 104,16,36
ta 44,235,0
cv 173,122,123
mvaa 100,20,32
dl 225,36,52,1024
mvaa 100,84,96
ta 46,235,512
cv 172,120,121
mvaa 96,80,96
cv 171,118,119
cv 170,116,117
mvaa 96,16,32
ta 40,234,0
cv 169,114,115
mvaa 92,12,60
ta 42,234,512
cv 168,112,113
mvaa 92,76,124
dl 226,36,52,2048
mvaa 88,72,124
cv 167,110,111
cv 166,108,109
mvaa 88,8,60
cv 165,106,107
cv 164,104,105
mvaa 84,12,56
ta 84,237,8192
cv 163,102,103
mvaa 84,76,120
ta 80,236,8192
cv 162,100,101
mvaa 80,72,120
dl 227,36,52,3072
mvaa 80,8,56
s_add_u32 m0,s62,0
ta 86,237,8704
cv 161,98,99
mvaa 76,12,52
s_add_u32 s52,s52,s56
ta 82,236,8704
cv 160,96,97
mvaa 76,76,116
ra 16,232,6144
cv 159,94,95
mvaa 72,72,116
ta 60,235,4096
cv 158,92,93
mvaa 72,8,52
buffer_load_dword a243,v241,s[48:51],s55 offen
mvaa 68,12,48
L_vs6:
s_cbranch_execz L_vs10
s_add_u32 s55,s55,s59
ta 56,234,4096
cv 151,78,79
mvaa 68,76,112
ta 62,235,4608
cv 150,76,77
mvaa 64,72,112
ta 58,234,4608
cv 157,90,91
mvaa 64,8,48
ta 52,237,0
cv 156,88,89
mvaa 60,76,108
buffer_load_dword a244,v241,s[48:51],s55 offen
mvaa 56,72,108
s_add_u32 s55,s55,s59
ra 12,232,4096
cv 147,70,71
mvaa 52,76,104
ta 54,237,512
cv 146,68,69
mvaa 48,72,104
ra 8,232,2048
cv 145,66,67
mvaa 44,76,100
ta 48,236,0
cv 144,64,65
mvaa 40,72,100
buffer_load_dword a245,v241,s[48:51],s55 offen
mvaa 36,76,96
s_add_u32 s55,s55,s59
ta 50,236,512
cv 143,62,63
mvaa 32,72,96
cv 142,60,61
cv 141,58,59
mvaa 28,68,124
cv 140,56,57
cv 139,54,55
mvaa 24,64,124
cv 138,52,53
cv 137,50,51
mvaa 20,68,120
s_mov_b64 exec,65537
buffer_store_dword v246,v250,s[8:11],s86 offen sc0 sc1
s_mov_b64 exec,-1
mvaa 16,64,120
cv 136,48,49
cv 135,46,47
mvaa 12,68,116
cv 134,44,45
cv 133,42,43
mvaa 8,64,116
ta 76,235,8192
cv 132,40,41
mvaa 4,68,112
ta 78,235,8704
cv 131,38,39
mvaa 0,64,112
ta 72,234,8192
cv 130,36,37
maaa 156,68,108
cv 129,34,35
cv 128,32,33
maaa 152,64,108
ta 74,234,8704
cv 127,30,31
maaa 148,68,104
cv 126,28,29
cv 125,26,27
maaa 144,64,104
v_accvgpr_read_b32 v63,a159
v_accvgpr_read_b32 v62,a158
maaa 140,68,100
v_accvgpr_read_b32 v61,a157
v_accvgpr_read_b32 v60,a156
maaa 136,64,100
v_accvgpr_read_b32 v59,a155
v_accvgpr_read_b32 v58,a154
maaa 132,68,96
v_accvgpr_read_b32 v57,a153
v_accvgpr_read_b32 v56,a152
maaa 128,64,96
cv 56,56,57
cv 57,58,59
s_waitcnt lgkmcnt(3)
zaaa 236,40,4
ta 68,237,4096
ta 70,237,4608
zaaa 220,44,4
ta 64,236,4096
ta 66,236,4608
zaaa 216,44,8
cv 58,60,61
cv 59,62,63
zaaa 232,40,8
buffer_store_dwordx4 v[56:59],v244,s[24:27],0 offen offset:3072 sc0 sc1
zaaa 228,40,12
v_accvgpr_read_b32 v55,a151
v_accvgpr_read_b32 v54,a150
zaaa 212,44,12
v_accvgpr_read_b32 v53,a149
v_accvgpr_read_b32 v52,a148
zaaa 208,44,16
v_accvgpr_read_b32 v51,a147
v_accvgpr_read_b32 v50,a146
zaaa 224,40,16
v_accvgpr_read_b32 v49,a145
v_accvgpr_read_b32 v48,a144
zaaa 192,48,16
cv 48,48,49
cv 49,50,51
zaaa 176,52,16
cv 50,52,53
cv 51,54,55
zaaa 180,52,12
buffer_store_dwordx4 v[48:51],v244,s[24:27],0 offen offset:2048 sc0 sc1
zaaa 196,48,12
s_waitcnt vmcnt(27) lgkmcnt(0)
s_barrier
ra 24,233,0
v_accvgpr_read_b32 v47,a143
zaaa 200,48,8
ra 28,233,2048
v_accvgpr_read_b32 v46,a142
v_accvgpr_read_b32 v45,a141
zaaa 184,52,8
ra 32,233,4096
ra 36,233,6144
zaaa 188,52,4
v_accvgpr_read_b32 v43,a139
v_accvgpr_read_b32 v42,a138
zaaa 204,48,4
dl 228,40,53,0
ta 96,236,24576
zaaa 172,56,4
ta 98,236,25088
v_accvgpr_read_b32 v41,a137
zaaa 156,60,4
ta 100,237,24576
v_accvgpr_read_b32 v39,a135
zaaa 152,60,8
ta 102,237,25088
ta 104,238,28672
zaaa 168,56,8
v_accvgpr_read_b32 v38,a134
v_accvgpr_read_b32 v35,a131
zaaa 164,56,12
dl 229,40,53,1024
ta 106,238,29184
zaaa 148,60,12
s_add_u32 s53,s53,s57
ta 108,239,28672
v_accvgpr_read_b32 v34,a130
zaaa 144,60,16
ta 110,239,29184
v_accvgpr_read_b32 v33,a129
zaaa 160,56,16
ta 44,235,16384
ta 46,235,16896
ta 40,234,16384
ta 42,234,16896
ta 52,237,16384
ta 54,237,16896
ta 48,236,16384
ta 50,236,16896
ta 60,235,20480
ta 62,235,20992
ta 56,234,20480
ta 58,234,20992
v_accvgpr_read_b32 v32,a128
zaaa 128,64,16
v_accvgpr_read_b32 v37,a133
v_accvgpr_read_b32 v36,a132
zaaa 112,68,16
dl 230,44,54,2048
zaaa 116,68,12
v_accvgpr_read_b32 v40,a136
v_accvgpr_read_b32 v44,a140
zaaa 132,64,12
cv 251,42,43
cv 250,40,41
zaaa 136,64,8
cv 253,46,47
cv 252,44,45
zaaa 120,68,8
cv 249,38,39
cv 247,34,35
zaaa 124,68,4
dl 231,44,54,3072
zaaa 140,64,4
s_add_u32 m0,s61,4096
cv 246,32,33
cv 248,36,37
zvaa 40,72,4
s_add_u32 s54,s54,s57
ta 68,237,20480
cv 124,24,25
zvaa 44,76,4
s_add_u32 s61,s63,s98
ta 70,237,20992
cv 123,22,23
zvaa 52,76,8
s_add_u32 s63,s63,s99
ta 64,236,20480
cv 121,18,19
zvaa 48,72,8
dl 224,36,52,0
zvaa 56,72,12
ta 66,236,20992
cv 120,16,17
zvaa 60,76,12
cv 122,20,21
cv 119,14,15
zvaa 68,76,16
cv 118,12,13
cv 117,10,11
zvaa 64,72,16
cv 116,8,9
cv 153,82,83
zvaa 32,80,16
dl 225,36,52,1024
ta 76,235,24576
ta 78,235,25088
zvaa 36,84,16
ta 72,234,24576
ta 74,234,25088
zvaa 28,84,12
cv 152,80,81
cv 155,86,87
zvaa 24,80,12
cv 154,84,85
cv 149,74,75
zvaa 16,80,8
cv 148,72,73
v_add_u32_e32 v232,s99,v232
zvaa 20,84,8
dl 226,36,52,2048
zvaa 12,84,4
cv 115,6,7
cv 114,4,5
zvaa 8,80,4
cv 113,2,3
cv 112,0,1
zvaa 76,4,88
zvaa 96,4,92
zvaa 100,8,92
dl 227,36,52,3072
zvaa 80,8,88
s_add_u32 s52,s52,s60
zvaa 84,12,88
zvaa 104,12,92
zvaa 108,16,92
s_waitcnt vmcnt(23) lgkmcnt(0)
s_barrier
zvaa 88,16,88
buffer_store_dwordx4 v[250:253],v244,s[24:27],0 offen offset:1024 sc0 sc1
buffer_store_dwordx4 v[246:249],v244,s[24:27],0 offen offset:0 sc0 sc1
L_kvx:
L_vs7:
s_cbranch_execz L_vs11
L_p_exit:
s_add_u32 m0,s62,16384
ra 4,232,0
s_lshl_b32 s86,s21,2
maaa 236,40,24
dl 228,40,53,0
maaa 220,44,24
s_add_u32 s86,s86,s12
ra 8,232,2048
s_lshl_b32 s86,s86,8
maaa 216,44,28
s_add_u32 s86,s86,L_p_exit-L_att_pc+2048
ra 12,232,4096
v_lshl_add_u32 v253,v244,3,s86
maaa 232,40,28
s_mov_b32 s67,4096
ra 16,232,6144
v_add_u32_e32 v233,0xffff0000,v233
maaa 228,40,32
s_cmp_lt_u32 s12,2
s_cselect_b32 s94,s22,s23
ra 80,233,0
v_mov_b32_e32 v248,-1
maaa 212,44,32
dl 229,40,53,1024
maaa 208,44,36
s_lshr_b32 s94,s94,6
ra 84,233,2048
v_mov_b32_e32 v249,-1
maaa 224,40,36
s_and_b32 s87,s12,1
ra 88,233,4096
v_mov_b32_e32 v250,-1
maaa 192,48,36
s_add_u32 s94,s94,s87
ra 92,233,6144
v_mov_b32_e32 v251,-1
maaa 176,52,36
s_lshl_b32 s87,s18,5
ta 0,234,32768
s_add_u32 s94,s94,s87
maaa 180,52,32
dl 230,44,54,2048
maaa 196,48,32
s_lshl_b32 s94,s94,11
ta 2,234,33280
s_add_u32 s94,s94,12582912
maaa 200,48,28
s_add_u32 s53,s53,s58
ta 40,235,32768
s_bfe_u32 s88,s21,65539
maaa 184,52,28
s_lshl_b32 s87,s20,1
ta 42,235,33280
s_add_u32 s88,s88,s87
maaa 188,52,24
s_lshl_b32 s88,s88,7
v_mov_b32_e32 v247,1
s_add_u32 s85,s88,83894272
maaa 204,48,24
dl 231,44,54,3072
maaa 172,56,24
s_add_u32 m0,s98,65536
ta 52,236,32768
s_add_u32 s54,s54,s58
maaa 156,60,24
s_lshl_b32 s91,s12,4
ta 54,236,33280
v_bfe_u32 v252,v244,4,4
maaa 152,60,28
s_cmp_lt_u32 s12,2
s_cselect_b32 s90,s22,s23
v_bfe_u32 v241,v244,6,4
v_and_b32_e32 v241,12,v241
maaa 168,56,28
s_mul_i32 s33,s90,10240
v_sub_u32_e32 v252,v252,v241
v_add_u32_e32 v252,s91,v252
maaa 164,56,32
s_mov_b64 exec,3
global_load_dword a250,v253,s[96:97]
s_mov_b64 exec,-1
maaa 148,60,32
s_cmp_lt_u32 s12,2
s_cselect_b32 s91,32768,0
v_add_u32_e32 v253,10240,v253
s_add_u32 s91,s91,98304
maaa 144,60,36
s_mov_b32 s64,0x55505555
v_accvgpr_read_b32 v255,a241
v_mov_b32_e32 v241,0xf149f2ca
maaa 160,56,36
s_mov_b32 s65,0x50005500
s_lshl_b32 s95,s12,12
s_mov_b32 s66,0x50000
maaa 128,64,36
s_add_u32 s70,s95,32768
s_add_u32 s72,s95,65536
s_lshl_b32 s4,s21,2
maaa 112,68,36
dl 224,36,52,0
maaa 116,68,32
s_add_u32 s4,s4,s12
ta 56,237,32768
s_lshl_b32 s4,s4,8
maaa 132,64,32
s_bitcmp1_b32 170,s20
s_cselect_b32 s5,L_odd-L_last,0
ta 58,237,33280
s_add_u32 s4,s4,s5
maaa 136,64,28
s_add_u32 s4,s4,L_last-L_att_pc
s_mov_b64 s[30:31],s[28:29]
s_mov_b64 s[28:29],s[78:79]
maaa 120,68,28
s_add_u32 s68,s95,0
v_cvt_f32_i32_e32 v252,v252
v_mul_f32_e32 v252,0x7149f2ca,v252
maaa 124,68,24
dl 225,36,52,1024
maaa 140,64,24
s_add_u32 s69,s95,16384
s_add_u32 s74,s95,98304
s_add_u32 s71,s95,49152
mvaa 40,72,24
s_waitcnt vmcnt(17) lgkmcnt(2)
s_barrier
mvaa 44,76,24
s_add_u32 s73,s95,81920
ta 20,234,49152
v_and_b32_e32 v245,48,v244
mvaa 52,76,28
s_lshl_b32 s59,%1,2
ta 22,234,49664
v_lshlrev_b32_e32 v245,3,v245
mvaa 48,72,28
s_lshl_b32 s84,%1,2
ta 44,235,49152
s_add_u32 s59,s59,s12
mvaa 56,72,32
dl 226,36,52,2048
mvaa 60,76,32
s_add_u32 s75,s95,114688
ta 46,235,49664
s_add_u32 s84,s84,s12
mvaa 68,76,36
s_mul_i32 s84,s84,12288
ta 48,236,49152
s_sub_u32 s55,s14,s13
mvaa 64,72,36
s_mov_b32 s48,0x38000000
ta 50,236,49664
s_mov_b32 s81,1
mvaa 32,96,36
s_mov_b32 s84,0
ta 60,237,49152
mvaa 36,100,36
dl 227,36,52,3072
mvaa 28,100,32
s_add_u32 s52,s52,s56
s_add_u32 m0,s98,69632
ta 62,237,49664
mvaa 24,96,32
ta 68,234,36864
mvaa 16,96,28
ta 70,234,37376
mvaa 20,100,28
ta 64,234,53248
mvaa 12,100,24
dl 224,36,52,0
mvaa 8,96,24
ta 66,234,53760
mvaa 76,24,104
ta 76,235,53248
)"
R"(mvaa 96,24,108
ta 78,235,53760
mvaa 100,28,108
ta 72,235,36864
mvaa 80,28,104
dl 225,36,52,1024
mvaa 84,32,104
ta 74,235,37376
mvaa 104,32,108
mvaa 108,36,108
mvaa 88,36,104
maaa 236,0,4
s_waitcnt lgkmcnt(5)
maaa 236,20,80
dl 226,36,52,2048
maaa 220,44,80
maaa 220,40,4
maaa 216,40,8
maaa 216,44,84
maaa 232,20,84
dl 227,36,52,3072
maaa 232,0,8
s_add_u32 s52,s52,s60
s_add_u32 m0,s98,0
maaa 228,0,12
maaa 228,20,88
maaa 212,44,88
maaa 212,40,12
dl 224,36,52,0
maaa 208,40,16
maaa 208,44,92
maaa 224,20,92
ta 40,236,36864
maaa 224,0,16
ta 42,236,37376
maaa 192,52,16
dl 225,36,52,1024
maaa 192,48,92
ta 44,236,53248
maaa 176,60,92
ta 46,236,53760
maaa 176,56,16
ta 20,237,53248
maaa 180,56,12
ta 22,237,53760
maaa 180,60,88
dl 226,36,52,2048
maaa 196,48,88
ta 0,237,36864
maaa 196,52,12
ta 2,237,37376
maaa 200,52,8
s_waitcnt vmcnt(18)
ra 24,232,32768
maaa 200,48,84
ra 28,232,34816
maaa 184,60,84
ra 32,232,36864
maaa 184,56,8
buffer_store_dwordx4 v[112:115],v244,s[24:27],s67 offen offset:0 sc0 sc1
maaa 188,56,4
ra 36,232,38912
v_add_u32_e32 v232,0x10000,v232
maaa 188,60,80
ra 96,233,32768
v_cndmask_b32_e64 v112,v241,0,s[64:65]
maaa 204,48,80
s_mov_b32 s64,0xaaa0aaaa
ra 100,233,34816
s_mov_b32 s65,0xa000aa00
maaa 204,52,4
ra 104,233,36864
v_cndmask_b32_e64 v113,v241,0,s[64:65]
maaa 172,68,4
dl 227,36,52,3072
s_waitcnt lgkmcnt(8)
maaa 172,64,80
s_add_u32 s52,s52,s56
s_add_u32 m0,s98,4096
ra 108,233,38912
maaa 156,76,80
s_mov_b32 s64,0x55405554
ta 56,234,40960
v_add_u32_e32 v233,0x10000,v233
maaa 156,72,4
s_mov_b32 s65,0x40005400
ta 58,234,41472
v_cndmask_b32_e64 v114,v241,0,s[64:65]
maaa 152,72,8
s_mov_b32 s64,0xaa80aaa8
ta 60,234,57344
s_mov_b32 s65,0x8000a800
maaa 152,76,84
dl 224,36,52,0
maaa 168,64,84
ta 62,234,57856
v_cndmask_b32_e64 v115,v241,0,s[64:65]
maaa 168,68,8
ta 48,235,57344
maaa 164,68,12
ta 50,235,57856
maaa 164,64,88
ta 52,235,40960
maaa 148,76,88
buffer_store_dwordx4 v[248:251],v244,s[8:11],s94 offen offset:0 sc0 sc1
maaa 148,72,12
ta 54,235,41472
maaa 144,72,16
maaa 144,76,92
maaa 160,64,92
ta 72,236,40960
maaa 160,68,16
ta 74,236,41472
maaa 128,40,16
dl 225,36,52,1024
maaa 128,44,92
L_vs8:
s_cbranch_execz L_vs12
ta 76,236,57344
maaa 112,20,92
ta 78,236,57856
s_waitcnt lgkmcnt(6)
maaa 112,0,16
ta 64,237,57344
maaa 116,0,12
ta 66,237,57856
maaa 116,20,88
dl 226,36,52,2048
maaa 132,44,88
ta 68,237,40960
maaa 132,40,12
ta 70,237,41472
maaa 136,40,8
maaa 136,44,84
maaa 120,20,84
dl 227,36,52,3072
maaa 120,0,8
s_add_u32 s52,s52,s60
maaa 124,0,4
maaa 124,20,80
maaa 140,44,80
ta 0,238,45056
ta 2,238,45568
maaa 140,40,4
buffer_store_dwordx4 v[248:251],v244,s[8:11],s94 offen offset:1024 sc0 sc1
ta 20,238,61440
ta 22,238,61952
mvaa 40,56,4
ta 44,239,61440
ta 46,239,61952
mvaa 40,60,80
ta 40,239,45056
ta 42,239,45568
mvaa 44,48,80
v_and_b32_e32 v250,240,v244
v_accvgpr_read_b32 v249,a240
s_waitcnt lgkmcnt(8)
mvaa 44,52,4
mvaa 52,52,8
mvaa 52,48,84
mvaa 48,60,84
mvaa 48,56,8
mvaa 56,56,12
mvaa 56,60,88
mvaa 60,48,88
mvaa 60,52,12
mvaa 68,52,16
mvaa 68,48,92
mvaa 64,60,92
mvaa 64,56,16
mvaa 32,72,16
s_waitcnt vmcnt(11) lgkmcnt(0)
s_barrier
s_add_u32 m0,s62,32768
mvaa 32,76,92
dl 228,40,53,0
mvaa 36,64,92
ta 52,234,0
mvaa 36,68,16
ta 54,234,512
mvaa 28,68,12
ta 48,234,16384
mvaa 28,64,88
ta 50,234,16896
mvaa 24,76,88
ta 60,235,16384
mvaa 24,72,12
dl 229,40,53,1024
mvaa 16,72,8
s_add_u32 s53,s53,s57
ta 62,235,16896
mvaa 16,76,84
ta 56,235,0
mvaa 20,64,84
ta 58,235,512
mvaa 20,68,8
mvaa 12,68,4
dl 230,44,54,2048
mvaa 12,64,80
mvaa 8,76,80
ta 68,236,0
mvaa 8,72,4
ta 70,236,512
mvaa 76,4,0
ta 64,236,16384
mvaa 76,80,20
dl 231,44,54,3072
mvaa 96,80,44
s_add_u32 m0,s62,49152
s_add_u32 s54,s54,s57
ta 66,236,16896
mvaa 96,4,40
ta 76,237,16384
mvaa 100,8,40
ta 78,237,16896
mvaa 100,84,44
ta 72,237,0
mvaa 80,84,20
ta 74,237,512
mvaa 80,8,0
dl 228,40,53,0
mvaa 84,12,0
ra 80,233,0
mvaa 84,88,20
ra 4,232,0
mvaa 104,88,44
ra 84,233,2048
mvaa 104,12,40
ra 8,232,2048
mvaa 108,16,40
dl 229,40,53,1024
mvaa 108,92,44
s_add_u32 s53,s53,s58
ra 88,233,4096
mvaa 88,92,20
ta 40,234,4096
mvaa 88,16,0
ta 42,234,4608
s_waitcnt lgkmcnt(8)
maaa 236,52,24
ta 44,234,20480
maaa 236,48,96
dl 230,44,54,2048
maaa 220,60,96
ta 46,234,20992
maaa 220,56,24
ta 20,235,20480
maaa 216,56,28
ta 22,235,20992
maaa 216,60,100
ta 0,235,4096
maaa 232,48,100
dl 231,44,54,3072
maaa 232,52,28
s_add_u32 m0,s98,32768
ta 2,235,4608
s_add_u32 s54,s54,s58
maaa 228,52,32
ra 12,232,4096
maaa 228,48,104
ra 16,232,6144
v_add_u32_e32 v232,0xffff0000,v232
maaa 212,60,104
ra 92,233,6144
v_add_u32_e32 v233,0xffff0000,v233
maaa 212,56,32
maaa 208,56,36
dl 224,36,52,0
maaa 208,60,108
maaa 224,48,108
ta 56,236,4096
maaa 224,52,36
ta 58,236,4608
maaa 192,68,36
ta 60,236,20480
maaa 192,64,108
dl 225,36,52,1024
maaa 176,76,108
ta 62,236,20992
s_waitcnt lgkmcnt(6)
maaa 176,72,36
ta 48,237,20480
maaa 180,72,32
ta 50,237,20992
maaa 180,76,104
ta 52,237,4096
maaa 196,64,104
buffer_store_dwordx4 v[116:119],v244,s[24:27],s67 offen offset:1024 sc0 sc1
maaa 196,68,32
ta 54,237,4608
maaa 200,68,28
maaa 200,64,100
maaa 184,76,100
maaa 184,72,28
dl 226,36,52,2048
maaa 188,72,24
maaa 188,76,96
maaa 204,64,96
ta 72,234,8192
maaa 204,68,24
ta 74,234,8704
maaa 172,40,24
ta 76,234,24576
maaa 172,44,96
dl 227,36,52,3072
maaa 156,20,96
s_add_u32 s52,s52,s56
s_add_u32 m0,s98,36864
ta 78,234,25088
maaa 156,0,24
ta 64,235,24576
maaa 152,0,28
ta 66,235,25088
maaa 152,20,100
ta 68,235,8192
maaa 168,44,100
dl 224,36,52,0
maaa 168,40,28
ta 70,235,8704
maaa 164,40,32
maaa 164,44,104
maaa 148,20,104
maaa 148,0,32
buffer_store_dwordx4 v[120:123],v244,s[24:27],s67 offen offset:2048 sc0 sc1
maaa 144,0,36
maaa 144,20,108
maaa 160,44,108
ta 0,236,8192
maaa 160,40,36
ta 2,236,8704
s_waitcnt lgkmcnt(3)
maaa 128,56,36
dl 225,36,52,1024
maaa 128,60,108
ta 20,236,24576
maaa 112,48,108
ta 22,236,25088
maaa 112,52,36
ta 44,237,24576
maaa 116,52,32
ta 46,237,25088
maaa 116,48,104
dl 226,36,52,2048
maaa 132,60,104
ta 40,237,8192
maaa 132,56,32
ta 42,237,8704
maaa 136,56,28
maaa 136,60,100
maaa 120,48,100
maaa 120,52,28
buffer_load_dword v246,off,s[8:11],s35 offset:1024 sc1
maaa 124,52,24
maaa 124,48,96
maaa 140,60,96
ta 52,238,12288
ta 54,238,12800
maaa 140,56,24
ta 48,238,28672
ta 50,238,29184
mvaa 40,72,24
buffer_store_dwordx4 v[124:127],v244,s[24:27],s67 offen offset:3072 sc0 sc1
ta 60,239,28672
mvaa 40,76,96
s_mov_b32 s67,8192
ta 62,239,29184
mvaa 44,64,96
ta 56,239,12288
ta 58,239,12800
s_waitcnt lgkmcnt(8)
mvaa 44,68,24
mvaa 52,68,28
mvaa 52,64,100
dl 227,36,52,3072
mvaa 48,76,100
s_add_u32 s52,s52,s60
mvaa 48,72,28
mvaa 56,72,32
mvaa 56,76,104
mvaa 60,64,104
mvaa 60,68,32
mvaa 68,68,36
mvaa 68,64,108
mvaa 64,76,108
mvaa 64,72,36
mvaa 32,0,36
s_waitcnt vmcnt(12) lgkmcnt(0)
s_barrier
s_add_u32 m0,s62,0
mvaa 32,20,108
dl 228,40,53,0
mvaa 36,44,108
ta 68,234,32768
mvaa 36,40,36
ta 70,234,33280
mvaa 28,40,32
ta 64,234,49152
mvaa 28,44,104
ta 66,234,49664
mvaa 24,20,104
ta 76,235,49152
mvaa 24,0,32
dl 229,40,53,1024
mvaa 16,0,28
s_add_u32 s53,s53,s57
ta 78,235,49664
mvaa 16,20,100
ta 72,235,32768
mvaa 20,44,100
ta 74,235,33280
mvaa 20,40,28
mvaa 12,40,24
mvaa 12,44,96
dl 230,44,54,2048
mvaa 8,20,96
ta 40,236,32768
mvaa 8,0,24
ta 42,236,33280
mvaa 76,24,52
ta 44,236,49152
mvaa 76,96,48
ta 46,236,49664
mvaa 96,96,60
dl 231,44,54,3072
mvaa 96,24,56
s_add_u32 m0,s62,16384
s_add_u32 s54,s54,s57
ta 20,237,49152
mvaa 100,28,56
ta 22,237,49664
mvaa 100,100,60
ta 0,237,32768
mvaa 80,100,48
ta 2,237,33280
mvaa 80,28,52
dl 228,40,53,0
mvaa 84,32,52
ra 96,233,0
mvaa 84,104,48
ra 24,232,0
mvaa 104,104,60
ra 100,233,2048
mvaa 104,32,56
ra 28,232,2048
mvaa 108,36,56
dl 229,40,53,1024
mvaa 108,108,60
s_add_u32 s53,s53,s58
ra 104,233,4096
mvaa 88,108,48
ta 56,234,36864
mvaa 88,36,52
ta 58,234,37376
s_waitcnt lgkmcnt(8)
maaa 236,68,4
ta 60,234,53248
maaa 236,64,80
ta 62,234,53760
maaa 220,76,80
dl 230,44,54,2048
maaa 220,72,4
ta 48,235,53248
maaa 216,72,8
ta 50,235,53760
maaa 216,76,84
ta 52,235,36864
maaa 232,64,84
ta 54,235,37376
maaa 232,68,8
dl 231,44,54,3072
maaa 228,68,12
s_add_u32 m0,s98,65536
ra 32,232,4096
s_add_u32 s54,s54,s58
maaa 228,64,88
ra 36,232,6144
maaa 212,76,88
ra 108,233,6144
maaa 212,72,12
s_waitcnt vmcnt(10)
v_readfirstlane_b32 s87,v246
maaa 208,72,16
dl 224,36,52,0
maaa 208,76,92
s_cmp_eq_u32 s12,0
s_cselect_b32 s89,s32,s87
s_cmp_lg_u32 s87,s89
s_cbranch_scc1 L_q_ack
L_q_acked:
maaa 224,64,92
s_and_b32 s87,s12,1
ta 72,236,36864
s_lshl_b32 s88,s87,6
maaa 224,68,16
s_add_u32 s90,s90,s88
ta 74,236,37376
s_lshl_b32 s88,s87,10
maaa 192,40,16
s_lshl_b32 s87,s87,14
ta 76,236,53248
s_add_u32 s91,s91,s87
maaa 192,44,92
dl 225,36,52,1024
maaa 176,20,92
s_lshr_b32 s87,s18,1
ta 78,236,53760
s_lshl_b32 s87,s87,4
s_waitcnt lgkmcnt(5)
maaa 176,0,16
s_add_u32 s33,s33,s88
ta 64,237,53248
v_cmp_eq_u32_e64 s[100:101],s87,v250
maaa 180,0,12
s_lshl_b32 s88,s18,11
ta 66,237,53760
v_lshlrev_b32_e32 v250,3,v250
maaa 180,20,88
s_add_u32 s88,s33,s88
ta 68,237,36864
v_add_u32_e32 v250,83894272,v250
maaa 196,44,88
s_add_u32 s33,s88,63176704
ta 70,237,37376
v_add_u32_e32 v251,s91,v244
maaa 196,40,12
buffer_store_dwordx4 v[128:131],v244,s[24:27],s67 offen offset:0 sc0 sc1
maaa 200,40,8
s_add_u32 s34,s88,63504384
v_cndmask_b32_e64 v242,31,-1,s[100:101]
v_and_b32_e32 v243,528,v242
maaa 200,44,84
s_add_u32 s50,s88,63832064
s_add_u32 s51,s88,64159744
s_lshr_b32 s91,s21,3
maaa 184,20,84
s_and_b32 s88,s20,4
s_and_b32 s89,s20,3
s_add_u32 s91,s91,s88
maaa 184,0,8
s_lshl_b32 s88,s89,6
s_lshl_b32 s86,s21,2
s_add_u32 s86,s86,s12
maaa 188,0,4
dl 226,36,52,2048
maaa 188,20,80
s_cmp_eq_u32 s90,0
s_cselect_b64 s[64:65],-1,0
s_and_b32 s89,s86,1
ta 0,234,40960
maaa 204,44,80
s_lshl_b32 s89,s89,9
ta 2,234,41472
s_lshr_b32 s49,s86,3
maaa 204,40,4
s_lshl_b32 s49,s49,11
ta 20,234,57344
v_add_f32_e32 v128,0x70c9f2ca,v252
maaa 172,56,4
ta 22,234,57856
v_add_f32_e32 v129,0xf0c9f2ca,v252
maaa 172,60,80
dl 227,36,52,3072
maaa 156,48,80
s_add_u32 s52,s52,s56
s_add_u32 m0,s98,69632
ta 44,235,57344
maaa 156,52,4
ta 46,235,57856
v_add_f32_e32 v130,0xf1977617,v252
maaa 152,52,8
ta 40,235,40960
v_add_f32_e32 v131,0xf1fc6f7c,v252
maaa 152,48,84
ta 42,235,41472
v_min_f32_e32 v128,0xc1a00000,v128
maaa 168,60,84
dl 224,36,52,0
maaa 168,56,8
v_min_f32_e32 v129,0xc1a00000,v129
v_min_f32_e32 v130,0xc1a00000,v130
maaa 164,56,12
v_min_f32_e32 v131,0xc1a00000,v131
v_lshrrev_b32_e32 v246,6,v244
maaa 164,60,88
v_lshl_add_u32 v245,v246,22,v245
v_lshlrev_b32_e32 v246,16,v244
maaa 148,48,88
maaa 148,52,12
maaa 144,52,16
buffer_store_dwordx4 v[132:135],v244,s[24:27],s67 offen offset:1024 sc0 sc1
maaa 144,48,92
maaa 160,60,92
ta 52,236,40960
v_add_f32_e32 v132,0xf343a333,v252
maaa 160,56,16
ta 54,236,41472
v_add_f32_e32 v133,0xf3504260,v252
s_waitcnt lgkmcnt(2)
maaa 128,72,16
ta 48,236,57344
v_add_f32_e32 v134,0xf35ce18d,v252
maaa 128,76,92
dl 225,36,52,1024
maaa 112,64,92
ta 50,236,57856
v_min_f32_e32 v132,0xc1a00000,v132
maaa 112,68,16
ta 60,237,57344
v_add_f32_e32 v135,0xf36980b9,v252
maaa 116,68,12
ta 62,237,57856
v_min_f32_e32 v133,0xc1a00000,v133
maaa 116,64,88
ta 56,237,40960
v_min_f32_e32 v134,0xc1a00000,v134
maaa 132,76,88
dl 226,36,52,2048
maaa 132,72,12
ta 58,237,41472
v_min_f32_e32 v135,0xc1a00000,v135
maaa 136,72,8
maaa 136,76,84
maaa 120,64,84
maaa 120,68,8
dl 227,36,52,3072
maaa 124,68,4
s_add_u32 s52,s52,s60
maaa 124,64,80
maaa 140,76,80
ta 68,238,45056
ta 70,238,45568
maaa 140,72,4
ta 64,238,61440
ta 66,238,61952
mvaa 40,0,4
ta 76,239,61440
mvaa 40,20,80
buffer_store_dwordx4 v[136:139],v244,s[24:27],s67 offen offset:2048 sc0 sc1
ta 78,239,61952
mvaa 44,44,80
ta 72,239,45056
ta 74,239,45568
mvaa 44,40,4
v_add_f32_e32 v136,0xf3c6caff,v252
v_add_f32_e32 v137,0xf3cd1a95,v252
mvaa 52,40,8
v_add_f32_e32 v138,0xf3d36a2b,v252
v_add_f32_e32 v139,0xf3d9b9c2,v252
mvaa 52,44,84
v_min_f32_e32 v136,0xc1a00000,v136
v_min_f32_e32 v137,0xc1a00000,v137
mvaa 48,20,84
v_min_f32_e32 v138,0xc1a00000,v138
v_min_f32_e32 v139,0xc1a00000,v139
mvaa 48,0,8
mvaa 56,0,12
mvaa 56,20,88
mvaa 60,44,88
mvaa 60,40,12
mvaa 68,40,16
mvaa 68,44,92
L_vs9:
s_cbranch_execz L_vs13
mvaa 64,20,92
mvaa 64,0,16
s_waitcnt lgkmcnt(0)
mvaa 32,52,16
s_waitcnt vmcnt(11)
s_barrier
mvaa 32,48,92
s_cmp_eq_u32 s12,0
s_cselect_b64 exec,1,0
buffer_atomic_add v247,off,s[8:11],s85 sc1
s_mov_b64 exec,-1
mvaa 36,60,92
s_add_u32 m0,s62,32768
ta 40,234,0
v_mov_b32_e32 v247,32
mvaa 36,56,16
ta 42,234,512
mvaa 28,56,12
ta 44,234,16384
mvaa 28,60,88
ta 46,234,16896
mvaa 24,48,88
dl 228,40,53,0
mvaa 24,52,12
ta 20,235,16384
mvaa 16,52,8
ta 22,235,16896
mvaa 16,48,84
ta 0,235,0
mvaa 20,60,84
ta 2,235,512
mvaa 20,56,8
dl 229,40,53,1024
mvaa 12,56,4
s_add_u32 s53,s53,s57
mvaa 12,60,80
mvaa 8,48,80
ta 56,236,0
mvaa 8,52,4
ta 58,236,512
mvaa 76,4,68
dl 230,44,54,2048
mvaa 76,80,64
ta 60,236,16384
mvaa 96,80,76
ta 62,236,16896
mvaa 96,4,72
ta 48,237,16384
mvaa 100,8,72
ta 50,237,16896
mvaa 100,84,76
dl 231,44,54,3072
mvaa 80,84,64
s_add_u32 m0,s62,49152
s_add_u32 s54,s54,s57
ta 52,237,0
mvaa 80,8,68
ta 54,237,512
mvaa 84,12,68
ra 80,233,32768
mvaa 84,88,64
ra 4,232,32768
mvaa 104,88,76
dl 228,40,53,0
mvaa 104,12,72
ra 84,233,34816
mvaa 108,16,72
ra 8,232,34816
mvaa 108,92,76
ra 12,232,36864
mvaa 88,92,64
ta 72,234,4096
mvaa 88,16,68
ta 74,234,4608
s_waitcnt lgkmcnt(9)
maaa 236,40,24
dl 229,40,53,1024
maaa 236,44,96
s_add_u32 s53,s53,s58
ta 76,234,20480
maaa 220,20,96
ta 78,234,20992
maaa 220,0,24
ta 64,235,20480
maaa 216,0,28
ta 66,235,20992
maaa 216,20,100
dl 230,44,54,2048
maaa 232,44,100
ta 68,235,4096
maaa 232,40,28
ta 70,235,4608
maaa 228,40,32
ra 88,233,36864
maaa 228,44,104
ra 92,233,38912
v_add_u32_e32 v233,0x10000,v233
maaa 212,20,104
dl 231,44,54,3072
maaa 212,0,32
s_add_u32 m0,s98,0
ra 16,232,38912
s_add_u32 s54,s54,s58
maaa 208,0,36
v_add_u32_e32 v232,0x10000,v232
maaa 208,20,108
maaa 224,44,108
ta 0,236,4096
maaa 224,40,36
dl 224,36,52,0
maaa 192,56,36
ta 2,236,4608
maaa 192,60,108
ta 20,236,20480
maaa 176,48,108
ta 22,236,20992
s_waitcnt lgkmcnt(6)
maaa 176,52,36
ta 44,237,20480
maaa 180,52,32
ta 46,237,20992
maaa 180,48,104
dl 225,36,52,1024
maaa 196,60,104
ta 40,237,4096
maaa 196,56,32
ta 42,237,4608
maaa 200,56,28
maaa 200,60,100
maaa 184,48,100
buffer_store_dwordx4 v[140:143],v244,s[24:27],s67 offen offset:3072 sc0 sc1
maaa 184,52,28
s_mov_b32 s67,12288
v_add_f32_e32 v140,0xf415e232,v252
v_add_f32_e32 v141,0xf41909fd,v252
maaa 188,52,24
v_add_f32_e32 v142,0xf41c31c8,v252
v_min_f32_e32 v140,0xc1a00000,v140
maaa 188,48,96
v_add_f32_e32 v143,0xf41f5993,v252
v_bfe_i32 v252,v244,4,1
maaa 204,60,96
ta 52,234,8192
v_min_f32_e32 v141,0xc1a00000,v141
maaa 204,56,24
dl 226,36,52,2048
maaa 172,72,24
ta 54,234,8704
v_min_f32_e32 v142,0xc1a00000,v142
maaa 172,76,96
ta 48,234,24576
v_min_f32_e32 v143,0xc1a00000,v143
maaa 156,64,96
ta 50,234,25088
v_xor_b32_e32 v252,65535,v252
maaa 156,68,24
ta 60,235,24576
maaa 152,68,28
dl 227,36,52,3072
maaa 152,64,100
s_add_u32 s52,s52,s56
s_add_u32 m0,s98,4096
ta 62,235,25088
maaa 168,76,100
ta 56,235,8192
maaa 168,72,28
ta 58,235,8704
maaa 164,72,32
maaa 164,76,104
maaa 148,64,104
dl 224,36,52,0
maaa 148,68,32
maaa 144,68,36
maaa 144,64,108
maaa 160,76,108
ta 68,236,8192
maaa 160,72,36
buffer_store_dwordx4 v[144:147],v244,s[24:27],s67 offen offset:0 sc0 sc1
s_waitcnt lgkmcnt(3)
maaa 128,0,36
ta 70,236,8704
maaa 128,20,108
ta 64,236,24576
maaa 112,44,108
ta 66,236,25088
maaa 112,40,36
ta 76,237,24576
maaa 116,40,32
dl 225,36,52,1024
maaa 116,44,104
ta 78,237,25088
maaa 132,20,104
ta 72,237,8192
maaa 132,0,32
ta 74,237,8704
maaa 136,0,28
maaa 136,20,100
dl 226,36,52,2048
maaa 120,44,100
maaa 120,40,28
maaa 124,40,24
maaa 124,44,96
maaa 140,20,96
buffer_store_dwordx4 v[148:151],v244,s[24:27],s67 offen offset:1024 sc0 sc1
ta 40,238,12288
ta 42,238,12800
maaa 140,0,24
ta 44,238,28672
ta 46,238,29184
mvaa 40,52,24
ta 20,239,28672
mvaa 40,48,96
ta 22,239,29184
mvaa 44,60,96
ta 0,239,12288
ta 2,239,12800
s_waitcnt lgkmcnt(8)
mvaa 44,56,24
mvaa 52,56,28
dl 227,36,52,3072
mvaa 52,60,100
s_add_u32 s52,s52,s60
mvaa 48,48,100
mvaa 48,52,28
mvaa 56,52,32
mvaa 56,48,104
mvaa 60,60,104
mvaa 60,56,32
mvaa 68,56,36
mvaa 68,60,108
mvaa 64,48,108
mvaa 64,52,36
mvaa 32,68,36
s_waitcnt vmcnt(11) lgkmcnt(0)
s_barrier
s_add_u32 m0,s62,0
mvaa 32,64,108
dl 228,40,53,0
mvaa 36,76,108
ta 56,234,32768
mvaa 36,72,36
ta 58,234,33280
mvaa 28,72,32
ta 60,234,49152
mvaa 28,76,104
ta 62,234,49664
mvaa 24,64,104
dl 229,40,53,1024
mvaa 24,68,32
s_add_u32 s53,s53,s57
ta 48,235,49152
mvaa 16,68,28
ta 50,235,49664
mvaa 16,64,100
ta 52,235,32768
mvaa 20,76,100
ta 54,235,33280
mvaa 20,72,28
dl 230,44,54,2048
mvaa 12,72,24
mvaa 12,76,96
mvaa 8,64,96
ta 72,236,32768
mvaa 8,68,24
ta 74,236,33280
mvaa 76,24,40
ta 76,236,49152
mvaa 76,96,44
dl 231,44,54,3072
mvaa 96,96,20
s_add_u32 m0,s62,16384
s_add_u32 s54,s54,s57
ta 78,236,49664
mvaa 96,24,0
ta 64,237,49152
mvaa 100,28,0
ta 66,237,49664
mvaa 100,100,20
ta 68,237,32768
mvaa 80,100,44
dl 228,40,53,0
mvaa 80,28,40
ta 70,237,33280
mvaa 84,32,40
ra 96,233,0
mvaa 84,104,44
ra 24,232,0
mvaa 104,104,20
ra 100,233,2048
mvaa 104,32,0
dl 229,40,53,1024
mvaa 108,36,0
s_add_u32 s53,s53,s58
ra 28,232,2048
mvaa 108,108,20
ra 104,233,4096
mvaa 88,108,44
ta 0,234,36864
mvaa 88,36,40
ta 2,234,37376
s_waitcnt lgkmcnt(8)
maaa 236,56,4
dl 230,44,54,2048
maaa 236,60,80
ta 20,234,53248
maaa 220,48,80
ta 22,234,53760
maaa 220,52,4
ta 44,235,53248
maaa 216,52,8
ta 46,235,53760
maaa 216,48,84
dl 231,44,54,3072
maaa 232,60,84
s_add_u32 m0,s98,32768
ta 40,235,36864
s_add_u32 s54,s54,s58
maaa 232,56,8
ta 42,235,37376
maaa 228,56,12
ra 32,232,4096
maaa 228,60,88
ra 108,233,6144
v_add_u32_e32 v233,0xffff0000,v233
maaa 212,48,88
ra 36,232,6144
v_add_u32_e32 v232,0xffff0000,v232
maaa 212,52,12
dl 224,36,52,0
maaa 208,52,16
maaa 208,48,92
maaa 224,60,92
ta 52,236,36864
maaa 224,56,16
ta 54,236,37376
maaa 192,72,16
dl 225,36,52,1024
)"
R"(maaa 192,76,92
ta 48,236,53248
maaa 176,64,92
ta 50,236,53760
s_waitcnt lgkmcnt(6)
maaa 176,68,16
ta 60,237,53248
maaa 180,68,12
ta 62,237,53760
maaa 180,64,88
dl 226,36,52,2048
maaa 196,76,88
ta 56,237,36864
maaa 196,72,12
ta 58,237,37376
maaa 200,72,8
maaa 200,76,84
maaa 184,64,84
buffer_store_dwordx4 v[152:155],v244,s[24:27],s67 offen offset:2048 sc0 sc1
maaa 184,68,8
maaa 188,68,4
maaa 188,64,80
maaa 204,76,80
ta 68,234,40960
maaa 204,72,4
ta 70,234,41472
maaa 172,0,4
dl 227,36,52,3072
maaa 172,20,80
s_add_u32 s52,s52,s56
s_add_u32 m0,s98,36864
ta 64,234,57344
maaa 156,44,80
ta 66,234,57856
maaa 156,40,4
ta 76,235,57344
maaa 152,40,8
ta 78,235,57856
maaa 152,44,84
dl 224,36,52,0
maaa 168,20,84
ta 72,235,40960
maaa 168,0,8
ta 74,235,41472
maaa 164,0,12
maaa 164,20,88
maaa 148,44,88
buffer_store_dwordx4 v[156:159],v244,s[24:27],s67 offen offset:3072 sc0 sc1
maaa 148,40,12
s_mov_b32 s67,16384
maaa 144,40,16
maaa 144,44,92
maaa 160,20,92
ta 40,236,40960
maaa 160,0,16
dl 225,36,52,1024
s_waitcnt lgkmcnt(3)
maaa 128,52,16
ta 42,236,41472
maaa 128,48,92
ta 44,236,57344
maaa 112,60,92
ta 46,236,57856
maaa 112,56,16
ta 20,237,57344
maaa 116,56,12
ta 22,237,57856
maaa 116,60,88
dl 226,36,52,2048
maaa 132,48,88
ta 0,237,40960
maaa 132,52,12
ta 2,237,41472
maaa 136,52,8
maaa 136,48,84
maaa 120,60,84
dl 227,36,52,3072
maaa 120,56,8
s_add_u32 s52,s52,s60
maaa 124,56,4
maaa 124,60,80
maaa 140,48,80
ta 56,238,45056
ta 58,238,45568
maaa 140,52,4
buffer_store_dwordx4 v[160:163],v244,s[24:27],s67 offen offset:0 sc0 sc1
ta 60,238,61440
ta 62,238,61952
mvaa 40,68,4
ta 48,239,61440
mvaa 40,64,80
ta 50,239,61952
mvaa 44,76,80
ta 52,239,45056
ta 54,239,45568
s_waitcnt lgkmcnt(8)
mvaa 44,72,4
mvaa 52,72,8
mvaa 52,76,84
mvaa 48,64,84
mvaa 48,68,8
mvaa 56,68,12
mvaa 56,64,88
mvaa 60,76,88
mvaa 60,72,12
mvaa 68,72,16
mvaa 68,76,92
mvaa 64,64,92
mvaa 64,68,16
mvaa 32,40,16
s_waitcnt vmcnt(11) lgkmcnt(0)
s_barrier
s_add_u32 m0,s62,32768
mvaa 32,44,92
dl 228,40,53,0
mvaa 36,20,92
ta 72,234,0
mvaa 36,0,16
ta 74,234,512
mvaa 28,0,12
ta 76,234,16384
mvaa 28,20,88
ta 78,234,16896
mvaa 24,44,88
ta 64,235,16384
mvaa 24,40,12
dl 229,40,53,1024
mvaa 16,40,8
s_add_u32 s53,s53,s57
ta 66,235,16896
mvaa 16,44,84
ta 68,235,0
mvaa 20,20,84
ta 70,235,512
mvaa 20,0,8
mvaa 12,0,4
dl 230,44,54,2048
mvaa 12,20,80
mvaa 8,44,80
ta 0,236,0
mvaa 8,40,4
ta 2,236,512
mvaa 76,4,56
ta 20,236,16384
mvaa 76,80,60
dl 231,44,54,3072
mvaa 96,80,48
s_add_u32 m0,s62,49152
s_add_u32 s54,s54,s57
ta 22,236,16896
mvaa 96,4,52
ta 44,237,16384
mvaa 100,8,52
ta 46,237,16896
mvaa 100,84,48
ta 40,237,0
mvaa 80,84,60
dl 228,40,53,0
mvaa 80,8,56
ta 42,237,512
mvaa 84,12,56
ra 80,233,0
mvaa 84,88,60
ra 4,232,0
mvaa 104,88,48
ra 84,233,2048
mvaa 104,12,52
ra 8,232,2048
mvaa 108,16,52
dl 229,40,53,1024
mvaa 108,92,48
s_add_u32 s53,s53,s58
ra 88,233,4096
mvaa 88,92,60
ta 52,234,4096
mvaa 88,16,56
ta 54,234,4608
s_waitcnt lgkmcnt(8)
maaa 236,72,24
ta 48,234,20480
maaa 236,76,96
dl 230,44,54,2048
maaa 220,64,96
ta 50,234,20992
maaa 220,68,24
ta 60,235,20480
maaa 216,68,28
ta 62,235,20992
maaa 216,64,100
ta 56,235,4096
v_accvgpr_read_b32 v0,a236
maaa 232,76,100
dl 231,44,54,3072
maaa 232,72,28
L_vs10:
s_cbranch_execz L_vs14
s_add_u32 m0,s98,65536
ta 58,235,4608
s_add_u32 s54,s54,s58
maaa 228,72,32
ra 12,232,4096
v_accvgpr_read_b32 v1,a237
maaa 228,76,104
ra 92,233,6144
v_accvgpr_read_b32 v2,a238
maaa 212,64,104
ra 16,232,6144
v_accvgpr_read_b32 v3,a239
maaa 212,68,32
dl 224,36,52,0
maaa 208,68,36
v_accvgpr_read_b32 v4,a220
v_accvgpr_read_b32 v5,a221
maaa 208,64,108
v_accvgpr_read_b32 v6,a222
v_accvgpr_read_b32 v7,a223
maaa 224,76,108
ta 68,236,4096
v_accvgpr_read_b32 v92,a216
maaa 224,72,36
ta 70,236,4608
v_accvgpr_read_b32 v93,a217
maaa 192,0,36
dl 225,36,52,1024
maaa 192,20,108
ta 64,236,20480
v_accvgpr_read_b32 v94,a218
maaa 176,44,108
ta 66,236,20992
v_accvgpr_read_b32 v95,a219
s_waitcnt lgkmcnt(6)
maaa 176,40,36
ta 76,237,20480
v_accvgpr_read_b32 v72,a232
maaa 180,40,32
ta 78,237,20992
v_accvgpr_read_b32 v73,a233
maaa 180,44,104
ta 72,237,4096
v_accvgpr_read_b32 v74,a234
maaa 196,20,104
s_mov_b64 exec,3
global_load_dword a250,v253,s[96:97]
s_mov_b64 exec,-1
maaa 196,0,32
ta 74,237,4608
v_accvgpr_read_b32 v75,a235
maaa 200,0,28
maaa 200,20,100
maaa 184,44,100
maaa 184,40,28
buffer_store_dwordx4 v[164:167],v244,s[24:27],s67 offen offset:1024 sc0 sc1
maaa 188,40,24
maaa 188,44,96
maaa 204,20,96
ta 40,234,8192
maaa 204,0,24
ta 42,234,8704
maaa 172,52,24
dl 226,36,52,2048
maaa 172,48,96
ta 44,234,24576
maaa 156,60,96
ta 46,234,25088
maaa 156,56,24
ta 20,235,24576
maaa 152,56,28
ta 22,235,25088
maaa 152,60,100
dl 227,36,52,3072
maaa 168,48,100
s_add_u32 s52,s52,s56
s_add_u32 m0,s98,69632
ta 0,235,8192
maaa 168,52,28
ta 2,235,8704
maaa 164,52,32
maaa 164,48,104
maaa 148,60,104
maaa 148,56,32
buffer_store_dwordx4 v[168:171],v244,s[24:27],s67 offen offset:2048 sc0 sc1
maaa 144,56,36
maaa 144,60,108
maaa 160,48,108
ta 56,236,8192
maaa 160,52,36
ta 58,236,8704
s_waitcnt lgkmcnt(4)
maaa 128,68,36
dl 224,36,52,0
maaa 128,64,108
ta 60,236,24576
maaa 112,76,108
ta 62,236,25088
maaa 112,72,36
ta 48,237,24576
maaa 116,72,32
ta 50,237,25088
maaa 116,76,104
dl 225,36,52,1024
maaa 132,64,104
ta 52,237,8192
maaa 132,68,32
ta 54,237,8704
maaa 136,68,28
maaa 136,64,100
maaa 120,76,100
dl 226,36,52,2048
maaa 120,72,28
maaa 124,72,24
maaa 124,76,96
maaa 140,64,96
ta 72,238,12288
ta 74,238,12800
maaa 140,68,24
ta 76,238,28672
ta 78,238,29184
mvaa 40,40,24
buffer_store_dwordx4 v[172:175],v244,s[24:27],s67 offen offset:3072 sc0 sc1
ta 64,239,28672
mvaa 40,44,96
s_mov_b32 s67,20480
ta 66,239,29184
mvaa 44,20,96
ta 68,239,12288
ta 70,239,12800
s_waitcnt lgkmcnt(8)
mvaa 44,0,24
mvaa 52,0,28
mvaa 52,20,100
dl 227,36,52,3072
mvaa 48,44,100
s_add_u32 s52,s52,s60
mvaa 48,40,28
mvaa 56,40,32
mvaa 56,44,104
mvaa 60,20,104
mvaa 60,0,32
mvaa 68,0,36
mvaa 68,20,108
mvaa 64,44,108
mvaa 64,40,36
mvaa 32,56,36
s_waitcnt vmcnt(12) lgkmcnt(0)
s_barrier
s_add_u32 m0,s62,0
mvaa 32,60,108
dl 228,40,53,0
mvaa 36,48,108
ta 0,234,32768
mvaa 36,52,36
ta 2,234,33280
mvaa 28,52,32
ta 20,234,49152
mvaa 28,48,104
ta 22,234,49664
mvaa 24,60,104
ta 44,235,49152
mvaa 24,56,32
dl 229,40,53,1024
mvaa 16,56,28
s_add_u32 s53,s53,s57
ta 46,235,49664
mvaa 16,60,100
ta 40,235,32768
mvaa 20,48,100
ta 42,235,33280
mvaa 20,52,28
ta 236,236,36864
mvaa 12,52,24
dl 230,44,54,2048
mvaa 12,48,96
ta 238,236,37376
mvaa 8,60,96
ta 52,236,32768
mvaa 8,56,24
ta 54,236,33280
mvaa 76,24,72
ta 48,236,49152
mvaa 76,96,76
ta 50,236,49664
mvaa 96,96,64
dl 231,44,54,3072
mvaa 96,24,68
s_add_u32 m0,s62,16384
s_add_u32 s54,s54,s57
ta 60,237,49152
mvaa 100,28,68
ta 62,237,49664
mvaa 100,100,64
ta 56,237,32768
mvaa 80,100,76
ta 58,237,33280
mvaa 80,28,72
dl 228,40,53,0
mvaa 84,32,72
ta 220,236,53248
mvaa 84,104,76
ta 222,236,53760
mvaa 104,104,64
ta 232,237,53248
mvaa 104,32,68
ta 234,237,53760
mvaa 108,36,68
dl 229,40,53,1024
mvaa 108,108,64
s_add_u32 s53,s53,s58
ta 216,237,36864
mvaa 88,108,76
ta 68,234,36864
mvaa 88,36,72
ta 70,234,37376
s_waitcnt lgkmcnt(8)
mvaa 0,0,4
ta 64,234,53248
mvaa 0,20,80
dl 230,44,54,2048
mvaa 4,44,80
ta 66,234,53760
mvaa 4,40,4
ta 76,235,53248
mvaa 92,40,8
ta 78,235,53760
mvaa 92,44,84
ta 72,235,36864
mvaa 72,20,84
ta 74,235,37376
mvaa 72,0,8
dl 231,44,54,3072
maaa 228,0,12
s_add_u32 m0,s98,0
ta 218,237,37376
s_add_u32 s54,s54,s58
maaa 228,20,88
ra 96,233,32768
maaa 212,44,88
ra 24,232,32768
maaa 212,40,12
ra 100,233,34816
maaa 208,40,16
dl 224,36,52,0
maaa 208,44,92
ra 28,232,34816
v_accvgpr_read_b32 v144,a228
maaa 224,20,92
ra 104,233,36864
v_accvgpr_read_b32 v145,a229
maaa 224,0,16
ra 32,232,36864
v_accvgpr_read_b32 v146,a230
maaa 192,52,16
ra 36,232,38912
v_accvgpr_read_b32 v147,a231
maaa 192,48,92
dl 225,36,52,1024
maaa 176,60,92
ra 108,233,38912
v_accvgpr_read_b32 v148,a212
s_waitcnt lgkmcnt(8)
maaa 176,56,16
ta 40,234,40960
v_accvgpr_read_b32 v149,a213
maaa 180,56,12
ta 42,234,41472
v_accvgpr_read_b32 v150,a214
maaa 180,60,88
ta 44,234,57344
v_accvgpr_read_b32 v151,a215
maaa 196,48,88
buffer_store_dwordx4 v[176:179],v244,s[24:27],s67 offen offset:0 sc0 sc1
maaa 196,52,12
ta 46,234,57856
v_accvgpr_read_b32 v156,a208
maaa 200,52,8
ta 20,235,57344
v_accvgpr_read_b32 v157,a209
maaa 200,48,84
ta 22,235,57856
v_accvgpr_read_b32 v158,a210
maaa 184,60,84
ta 0,235,40960
v_accvgpr_read_b32 v159,a211
maaa 184,56,8
ta 2,235,41472
v_accvgpr_read_b32 v152,a224
maaa 188,56,4
dl 226,36,52,2048
maaa 188,60,80
v_accvgpr_read_b32 v153,a225
v_accvgpr_read_b32 v154,a226
maaa 204,48,80
ta 56,236,40960
v_accvgpr_read_b32 v155,a227
maaa 204,52,4
ta 58,236,41472
v_add_u32_e32 v233,0x10000,v233
maaa 172,68,4
ta 60,236,57344
v_add_u32_e32 v232,0x10000,v232
maaa 172,64,80
dl 227,36,52,3072
maaa 156,76,80
s_add_u32 s52,s52,s56
s_add_u32 m0,s98,4096
ta 62,236,57856
maaa 156,72,4
ta 48,237,57344
maaa 152,72,8
ta 50,237,57856
maaa 152,76,84
ta 52,237,40960
maaa 168,64,84
dl 224,36,52,0
maaa 168,68,8
ta 54,237,41472
maaa 164,68,12
maaa 164,64,88
maaa 148,76,88
maaa 148,72,12
buffer_store_dwordx4 v[180:183],v244,s[24:27],s67 offen offset:1024 sc0 sc1
maaa 144,72,16
maaa 144,76,92
maaa 160,64,92
ta 72,238,45056
maaa 160,68,16
ta 74,238,45568
maaa 128,236,16
dl 225,36,52,1024
maaa 128,220,92
ta 76,238,61440
maaa 112,232,92
ta 78,238,61952
maaa 112,216,16
ta 64,239,61440
maaa 116,216,12
ta 66,239,61952
maaa 116,232,88
ta 68,239,45056
maaa 132,220,88
dl 226,36,52,2048
maaa 132,236,12
ta 70,239,45568
maaa 136,236,8
maaa 136,220,84
maaa 120,232,84
maaa 120,216,8
dl 227,36,52,3072
maaa 124,216,4
s_add_u32 s52,s52,s60
maaa 124,232,80
maaa 140,220,80
maaa 140,236,4
s_waitcnt lgkmcnt(1)
mvaa 40,40,4
buffer_store_dwordx4 v[184:187],v244,s[24:27],s67 offen offset:2048 sc0 sc1
mvaa 40,44,80
mvaa 44,20,80
mvaa 44,0,4
mvaa 52,0,8
mvaa 52,20,84
mvaa 48,44,84
mvaa 48,40,8
mvaa 56,40,12
mvaa 56,44,88
mvaa 60,20,88
mvaa 60,0,12
mvaa 68,0,16
mvaa 68,20,92
mvaa 64,44,92
mvaa 64,40,16
mvaa 32,56,16
s_waitcnt vmcnt(11) lgkmcnt(0)
s_barrier
s_add_u32 m0,s62,32768
mvaa 32,60,92
dl 228,40,53,0
mvaa 36,48,92
ta 216,234,0
mvaa 36,52,16
ta 218,234,512
mvaa 28,52,12
ta 232,234,16384
mvaa 28,48,88
ta 234,234,16896
mvaa 24,60,88
dl 229,40,53,1024
mvaa 24,56,12
s_add_u32 s53,s53,s57
ta 220,235,16384
mvaa 16,56,8
ta 222,235,16896
mvaa 16,60,84
ta 236,235,0
mvaa 20,48,84
ta 238,235,512
mvaa 20,52,8
dl 230,44,54,2048
mvaa 12,52,4
ta 0,236,0
mvaa 12,48,80
ta 2,236,512
mvaa 8,60,80
ta 20,236,16384
mvaa 8,56,4
ta 22,236,16896
mvaa 76,4,72
dl 231,44,54,3072
mvaa 76,80,76
s_add_u32 m0,s62,49152
s_add_u32 s54,s54,s57
ta 44,237,16384
mvaa 96,80,64
ta 46,237,16896
mvaa 96,4,68
ta 40,237,0
mvaa 100,8,68
ta 42,237,512
mvaa 100,84,64
dl 228,40,53,0
mvaa 80,84,76
ta 52,234,4096
mvaa 80,8,72
ta 54,234,4608
mvaa 84,12,72
ta 48,234,20480
mvaa 84,88,76
ta 50,234,20992
mvaa 104,88,64
dl 229,40,53,1024
mvaa 104,12,68
s_add_u32 s53,s53,s58
ta 60,235,20480
mvaa 108,16,68
ta 62,235,20992
mvaa 108,92,64
ta 56,235,4096
mvaa 88,92,76
ta 58,235,4608
mvaa 88,16,72
dl 230,44,54,2048
s_waitcnt lgkmcnt(8)
mvaa 0,216,24
ta 228,234,8192
mvaa 0,232,96
ta 68,236,4096
mvaa 4,220,96
ta 70,236,4608
mvaa 4,236,24
ta 64,236,20480
mvaa 92,236,28
ta 66,236,20992
mvaa 92,220,100
dl 231,44,54,3072
mvaa 72,232,100
s_add_u32 m0,s98,32768
ta 76,237,20480
s_add_u32 s54,s54,s58
mvaa 72,216,28
ta 78,237,20992
mvaa 144,216,32
ta 72,237,4096
mvaa 144,232,104
ta 74,237,4608
mvaa 148,220,104
dl 224,36,52,0
mvaa 148,236,32
ta 230,234,8704
mvaa 156,236,36
ta 212,234,24576
mvaa 156,220,108
ta 214,234,25088
mvaa 152,232,108
ta 224,235,24576
mvaa 152,216,36
dl 225,36,52,1024
maaa 192,0,36
ta 226,235,25088
maaa 192,20,108
ta 208,235,8192
maaa 176,44,108
ra 80,233,0
maaa 176,40,36
ta 210,235,8704
maaa 180,40,32
dl 226,36,52,2048
maaa 180,44,104
ra 4,232,0
v_accvgpr_read_b32 v160,a192
maaa 196,20,104
ra 84,233,2048
v_accvgpr_read_b32 v161,a193
maaa 196,0,32
ra 8,232,2048
v_accvgpr_read_b32 v162,a194
maaa 200,0,28
ra 12,232,4096
v_accvgpr_read_b32 v163,a195
maaa 200,20,100
buffer_store_dwordx4 v[188:191],v244,s[24:27],s67 offen offset:3072 sc0 sc1
maaa 184,44,100
s_mov_b32 s67,24576
ra 88,233,4096
v_accvgpr_read_b32 v164,a176
maaa 184,40,28
ra 92,233,6144
v_accvgpr_read_b32 v165,a177
maaa 188,40,24
ra 16,232,6144
v_accvgpr_read_b32 v166,a178
maaa 188,44,96
ta 236,236,8192
v_accvgpr_read_b32 v167,a179
maaa 204,20,96
ta 238,236,8704
v_accvgpr_read_b32 v172,a180
maaa 204,0,24
dl 227,36,52,3072
s_waitcnt lgkmcnt(8)
maaa 172,52,24
s_add_u32 s52,s52,s56
s_add_u32 m0,s98,36864
ta 220,236,24576
maaa 172,48,96
L_vs11:
s_cbranch_execz L_vs15
ta 222,236,25088
v_accvgpr_read_b32 v173,a181
maaa 156,60,96
ta 232,237,24576
v_accvgpr_read_b32 v174,a182
maaa 156,56,24
ta 234,237,25088
v_accvgpr_read_b32 v175,a183
maaa 152,56,28
dl 224,36,52,0
maaa 152,60,100
ta 216,237,8192
v_accvgpr_read_b32 v168,a196
maaa 168,48,100
ta 218,237,8704
v_accvgpr_read_b32 v169,a197
maaa 168,52,28
ta 40,238,12288
v_accvgpr_read_b32 v170,a198
maaa 164,52,32
ta 42,238,12800
v_accvgpr_read_b32 v171,a199
maaa 164,48,104
dl 225,36,52,1024
maaa 148,60,104
ta 44,238,28672
v_add_u32_e32 v233,0xffff0000,v233
maaa 148,56,32
ta 46,238,29184
v_add_u32_e32 v232,0xffff0000,v232
maaa 144,56,36
ta 20,239,28672
maaa 144,60,108
ta 22,239,29184
maaa 160,48,108
buffer_store_dwordx4 v[192:195],v244,s[24:27],s67 offen offset:0 sc0 sc1
maaa 160,52,36
ta 0,239,12288
maaa 128,68,36
ta 2,239,12800
maaa 128,64,108
maaa 112,76,108
maaa 112,72,36
maaa 116,72,32
dl 226,36,52,2048
maaa 116,76,104
maaa 132,64,104
maaa 132,68,32
maaa 136,68,28
maaa 136,64,100
dl 227,36,52,3072
maaa 120,76,100
s_add_u32 s52,s52,s60
maaa 120,72,28
maaa 124,72,24
maaa 124,76,96
maaa 140,64,96
buffer_store_dwordx4 v[196:199],v244,s[24:27],s67 offen offset:1024 sc0 sc1
maaa 140,68,24
mvaa 40,228,24
mvaa 40,212,96
mvaa 44,224,96
mvaa 44,208,24
mvaa 52,208,28
mvaa 52,224,100
mvaa 48,212,100
mvaa 48,228,28
mvaa 56,228,32
mvaa 56,212,104
mvaa 60,224,104
mvaa 60,208,32
mvaa 68,208,36
mvaa 68,224,108
mvaa 64,212,108
mvaa 64,228,36
s_waitcnt lgkmcnt(0)
mvaa 32,236,36
s_waitcnt vmcnt(11)
s_barrier
s_add_u32 m0,s62,0
mvaa 32,220,108
dl 228,40,53,0
mvaa 36,232,108
ta 56,234,32768
mvaa 36,216,36
ta 58,234,33280
mvaa 28,216,32
ta 60,234,49152
mvaa 28,232,104
ta 62,234,49664
mvaa 24,220,104
dl 229,40,53,1024
mvaa 24,236,32
s_add_u32 s53,s53,s57
ta 48,235,49152
mvaa 16,236,28
ta 50,235,49664
mvaa 16,220,100
ta 52,235,32768
mvaa 20,232,100
ta 54,235,33280
mvaa 20,216,28
dl 230,44,54,2048
mvaa 12,216,24
ta 72,236,32768
mvaa 12,232,96
ta 74,236,33280
mvaa 8,220,96
ta 76,236,49152
mvaa 8,236,24
ta 78,236,49664
mvaa 76,24,40
dl 231,44,54,3072
mvaa 76,96,44
s_add_u32 m0,s62,16384
s_add_u32 s54,s54,s57
ta 64,237,49152
mvaa 96,96,20
ta 66,237,49664
mvaa 96,24,0
ta 68,237,32768
mvaa 100,28,0
ta 70,237,33280
mvaa 100,100,20
dl 228,40,53,0
mvaa 80,100,44
ta 208,234,36864
mvaa 80,28,40
ta 210,234,37376
mvaa 84,32,40
ta 224,234,53248
mvaa 84,104,44
ta 226,234,53760
mvaa 104,104,20
ta 212,235,53248
mvaa 104,32,0
dl 229,40,53,1024
mvaa 108,36,0
s_add_u32 s53,s53,s58
ta 214,235,53760
mvaa 108,108,20
ta 228,235,36864
mvaa 88,108,44
ta 230,235,37376
mvaa 88,36,40
ta 192,236,40960
s_waitcnt lgkmcnt(9)
mvaa 0,56,4
dl 230,44,54,2048
mvaa 0,60,80
ta 216,236,36864
mvaa 4,48,80
ta 218,236,37376
mvaa 4,52,4
ta 232,236,53248
mvaa 92,52,8
ta 234,236,53760
mvaa 92,48,84
dl 231,44,54,3072
mvaa 72,60,84
s_add_u32 m0,s98,65536
ta 220,237,53248
s_add_u32 s54,s54,s58
mvaa 72,56,8
ta 222,237,53760
mvaa 144,56,12
ta 236,237,36864
mvaa 144,60,88
ta 238,237,37376
mvaa 148,48,88
dl 224,36,52,0
mvaa 148,52,12
ta 194,236,41472
mvaa 156,52,16
ta 176,236,57344
mvaa 156,48,92
ta 178,236,57856
mvaa 152,60,92
ta 196,237,57344
mvaa 152,56,16
ta 198,237,57856
mvaa 160,72,16
dl 225,36,52,1024
mvaa 160,76,92
ta 180,237,40960
mvaa 164,64,92
ta 182,237,41472
mvaa 164,68,16
ra 96,233,0
mvaa 172,68,12
ta 0,234,40960
mvaa 172,64,88
dl 226,36,52,2048
mvaa 168,76,88
ta 2,234,41472
mvaa 168,72,12
ta 20,234,57344
maaa 200,72,8
ta 22,234,57856
maaa 200,76,84
ta 44,235,57344
maaa 184,64,84
buffer_store_dwordx4 v[200:203],v244,s[24:27],s67 offen offset:2048 sc0 sc1
maaa 184,68,8
ta 46,235,57856
maaa 188,68,4
ta 40,235,40960
maaa 188,64,80
ta 42,235,41472
v_accvgpr_read_b32 v176,a200
maaa 204,76,80
ra 24,232,0
v_accvgpr_read_b32 v177,a201
maaa 204,72,4
dl 227,36,52,3072
s_waitcnt lgkmcnt(8)
maaa 172,208,4
s_add_u32 s52,s52,s56
s_add_u32 m0,s98,69632
ra 100,233,2048
maaa 172,224,80
ra 28,232,2048
v_accvgpr_read_b32 v178,a202
maaa 156,212,80
ra 32,232,4096
v_accvgpr_read_b32 v179,a203
maaa 156,228,4
ra 104,233,4096
v_accvgpr_read_b32 v180,a184
maaa 152,228,8
ra 108,233,6144
v_accvgpr_read_b32 v181,a185
maaa 152,212,84
dl 224,36,52,0
maaa 168,224,84
ra 36,232,6144
v_accvgpr_read_b32 v182,a186
maaa 168,208,8
ta 52,238,45056
v_accvgpr_read_b32 v183,a187
maaa 164,208,12
ta 54,238,45568
v_accvgpr_read_b32 v188,a188
maaa 164,224,88
ta 48,238,61440
v_accvgpr_read_b32 v189,a189
maaa 148,212,88
buffer_store_dwordx4 v[204:207],v244,s[24:27],s67 offen offset:3072 sc0 sc1
maaa 148,228,12
s_mov_b32 s67,28672
ta 50,238,61952
v_accvgpr_read_b32 v190,a190
maaa 144,228,16
ta 60,239,61440
v_accvgpr_read_b32 v184,a204
maaa 144,212,92
ta 62,239,61952
v_accvgpr_read_b32 v191,a191
maaa 160,224,92
ta 56,239,45056
v_accvgpr_read_b32 v185,a205
maaa 160,208,16
dl 225,36,52,1024
maaa 128,216,16
ta 58,239,45568
v_accvgpr_read_b32 v186,a206
maaa 128,232,92
v_accvgpr_read_b32 v187,a207
maaa 112,220,92
maaa 112,236,16
maaa 116,236,12
dl 226,36,52,2048
maaa 116,220,88
maaa 132,232,88
maaa 132,216,12
)"
R"(maaa 136,216,8
maaa 136,232,84
dl 227,36,52,3072
maaa 120,220,84
s_add_u32 s52,s52,s60
maaa 120,236,8
maaa 124,236,4
maaa 124,220,80
maaa 140,232,80
maaa 140,216,4
buffer_store_dwordx4 v[208:211],v244,s[24:27],s67 offen offset:0 sc0 sc1
s_waitcnt lgkmcnt(0)
mvaa 40,0,4
mvaa 40,20,80
mvaa 44,44,80
mvaa 44,40,4
mvaa 52,40,8
mvaa 52,44,84
mvaa 48,20,84
mvaa 48,0,8
mvaa 56,0,12
mvaa 56,20,88
mvaa 60,44,88
mvaa 60,40,12
mvaa 68,40,16
mvaa 68,44,92
mvaa 64,20,92
mvaa 64,0,16
mvaa 32,192,16
s_waitcnt vmcnt(11)
s_barrier
s_add_u32 m0,s62,32768
mvaa 32,176,92
dl 228,40,53,0
mvaa 36,196,92
ta 68,234,0
mvaa 36,180,16
ta 70,234,512
mvaa 28,180,12
ta 64,234,16384
mvaa 28,196,88
ta 66,234,16896
mvaa 24,176,88
dl 229,40,53,1024
mvaa 24,192,12
s_add_u32 s53,s53,s57
ta 76,235,16384
mvaa 16,192,8
ta 78,235,16896
mvaa 16,176,84
ta 72,235,0
mvaa 20,196,84
ta 74,235,512
mvaa 20,180,8
ta 228,236,0
mvaa 12,180,4
dl 230,44,54,2048
mvaa 12,196,80
ta 230,236,512
mvaa 8,176,80
ta 212,236,16384
mvaa 8,192,4
ta 214,236,16896
mvaa 76,4,52
ta 224,237,16384
mvaa 76,80,48
dl 231,44,54,3072
mvaa 96,80,60
s_add_u32 m0,s62,49152
s_add_u32 s54,s54,s57
ta 226,237,16896
mvaa 96,4,56
ta 208,237,0
mvaa 100,8,56
ta 210,237,512
mvaa 100,84,60
ta 236,234,4096
mvaa 80,84,48
dl 228,40,53,0
mvaa 80,8,52
ta 238,234,4608
mvaa 84,12,52
ta 220,234,20480
mvaa 84,88,48
ta 222,234,20992
mvaa 104,88,60
ta 232,235,20480
mvaa 104,12,56
dl 229,40,53,1024
mvaa 108,16,56
s_add_u32 s53,s53,s58
ta 234,235,20992
mvaa 108,92,60
ta 216,235,4096
mvaa 88,92,48
ta 218,235,4608
mvaa 88,16,52
ta 40,236,4096
s_waitcnt lgkmcnt(8)
mvaa 0,68,24
dl 230,44,54,2048
mvaa 0,64,96
ta 42,236,4608
mvaa 4,76,96
ta 44,236,20480
mvaa 4,72,24
ta 46,236,20992
mvaa 92,72,28
ta 20,237,20480
mvaa 92,76,100
ta 22,237,20992
mvaa 72,64,100
dl 231,44,54,3072
mvaa 72,68,28
s_add_u32 m0,s98,0
ta 200,238,12288
s_add_u32 s54,s54,s58
mvaa 144,68,32
ta 202,238,12800
mvaa 144,64,104
ta 0,237,4096
mvaa 148,76,104
ta 2,237,4608
mvaa 148,72,32
dl 224,36,52,0
mvaa 156,72,36
ta 184,238,28672
mvaa 156,76,108
ta 186,238,29184
mvaa 152,64,108
ta 204,239,28672
mvaa 152,68,36
ta 206,239,29184
mvaa 160,228,36
dl 225,36,52,1024
mvaa 160,212,108
ta 188,239,12288
mvaa 164,224,108
ta 190,239,12800
mvaa 164,208,36
ta 180,234,8192
mvaa 172,208,32
ta 182,234,8704
mvaa 172,224,104
dl 226,36,52,2048
mvaa 168,212,104
ta 196,234,24576
mvaa 168,228,32
ta 198,234,25088
mvaa 176,228,28
ta 176,235,24576
mvaa 176,212,100
ta 178,235,25088
mvaa 180,224,100
ta 192,235,8192
mvaa 180,208,28
buffer_store_dwordx4 v[212:215],v244,s[24:27],s67 offen offset:1024 sc0 sc1
mvaa 188,208,24
ta 194,235,8704
mvaa 188,224,96
ra 4,232,32768
mvaa 184,212,96
ra 80,233,32768
mvaa 184,228,24
ra 84,233,34816
s_waitcnt lgkmcnt(9)
maaa 172,236,24
dl 227,36,52,3072
maaa 172,220,96
s_add_u32 s52,s52,s56
s_add_u32 m0,s98,4096
ra 8,232,34816
maaa 156,232,96
ra 88,233,36864
maaa 156,216,24
ta 56,236,8192
maaa 152,216,28
ra 12,232,36864
v_accvgpr_read_b32 v192,a172
maaa 152,232,100
dl 224,36,52,0
maaa 168,220,100
ta 58,236,8704
v_accvgpr_read_b32 v193,a173
maaa 168,236,28
ta 60,236,24576
v_accvgpr_read_b32 v194,a174
maaa 164,236,32
ta 62,236,25088
v_accvgpr_read_b32 v195,a175
maaa 164,220,104
ta 48,237,24576
v_accvgpr_read_b32 v196,a156
maaa 148,232,104
buffer_store_dwordx4 v[216:219],v244,s[24:27],s67 offen offset:2048 sc0 sc1
maaa 148,216,32
ta 50,237,25088
v_accvgpr_read_b32 v197,a157
maaa 144,216,36
ra 92,233,38912
v_accvgpr_read_b32 v198,a158
maaa 144,232,108
ra 16,232,38912
v_accvgpr_read_b32 v199,a159
maaa 160,220,108
ta 52,237,8192
v_accvgpr_read_b32 v204,a152
maaa 160,236,36
ta 54,237,8704
v_accvgpr_read_b32 v205,a153
maaa 128,40,36
dl 225,36,52,1024
maaa 128,44,108
v_accvgpr_read_b32 v206,a154
v_accvgpr_read_b32 v207,a155
maaa 112,20,108
v_accvgpr_read_b32 v200,a168
v_accvgpr_read_b32 v201,a169
maaa 112,0,36
v_accvgpr_read_b32 v202,a170
v_accvgpr_read_b32 v203,a171
maaa 116,0,32
v_add_u32_e32 v233,0x10000,v233
v_add_u32_e32 v232,0x10000,v232
maaa 116,20,104
dl 226,36,52,2048
maaa 132,44,104
maaa 132,40,32
maaa 136,40,28
maaa 136,44,100
maaa 120,20,100
dl 227,36,52,3072
maaa 120,0,28
s_add_u32 s52,s52,s60
maaa 124,0,24
maaa 124,20,96
maaa 140,44,96
maaa 140,40,24
buffer_store_dwordx4 v[220:223],v244,s[24:27],s67 offen offset:3072 sc0 sc1
mvaa 40,180,24
s_mov_b32 s67,0x5550055
s_mov_b32 s26,2097152
v_cndmask_b32_e64 v116,v241,0,s[66:67]
s_waitcnt lgkmcnt(0)
mvaa 40,196,96
s_mov_b32 s66,0xa0000
s_mov_b32 s67,0xaaa00aa
v_cndmask_b32_e64 v117,v241,0,s[66:67]
mvaa 44,176,96
L_vs12:
s_cbranch_execz L_vr0
s_mov_b32 s66,0x150001
s_mov_b32 s67,0x15550155
v_cndmask_b32_e64 v118,v241,0,s[66:67]
mvaa 44,192,24
s_mov_b32 s66,0x2a0002
s_mov_b32 s67,0x2aaa02aa
v_cndmask_b32_e64 v119,v241,0,s[66:67]
mvaa 52,192,28
s_lshr_b32 s66,s18,2
s_lshl_b32 s67,s18,13
s_lshl_b32 s66,s66,13
mvaa 52,176,100
s_mov_b32 s27,131072
v_cndmask_b32_e64 v120,v116,v241,s[64:65]
v_cndmask_b32_e64 v121,v117,v241,s[64:65]
mvaa 48,196,100
s_mov_b64 s[24:25],s[82:83]
v_cndmask_b32_e64 v122,v118,v241,s[64:65]
v_cndmask_b32_e64 v123,v119,v241,s[64:65]
mvaa 48,180,28
s_mov_b32 s83,0
s_or_b32 s82,s15,16384
mvaa 56,180,32
mvaa 56,196,104
mvaa 60,176,104
mvaa 60,192,32
mvaa 68,192,36
mvaa 68,176,108
mvaa 64,196,108
mvaa 64,180,36
mvaa 32,56,36
s_waitcnt vmcnt(11)
s_barrier
s_add_u32 m0,s62,0
mvaa 32,60,108
dl 228,40,53,0
mvaa 36,48,108
ta 72,234,32768
mvaa 36,52,36
ta 74,234,33280
mvaa 28,52,32
ta 76,234,49152
mvaa 28,48,104
ta 78,234,49664
mvaa 24,60,104
ta 64,235,49152
mvaa 24,56,32
dl 229,40,53,1024
mvaa 16,56,28
s_add_u32 s53,s53,s57
ta 66,235,49664
mvaa 16,60,100
ta 68,235,32768
mvaa 20,48,100
ta 70,235,33280
mvaa 20,52,28
ta 208,236,32768
mvaa 12,52,24
dl 230,44,54,2048
mvaa 12,48,96
ta 210,236,33280
mvaa 8,60,96
ta 224,236,49152
mvaa 8,56,24
ta 226,236,49664
mvaa 76,24,200
ta 212,237,49152
mvaa 76,96,184
dl 231,44,54,3072
mvaa 96,96,204
s_add_u32 m0,s62,16384
s_add_u32 s54,s54,s57
ta 214,237,49664
mvaa 96,24,188
ta 228,237,32768
mvaa 100,28,188
ta 230,237,33280
mvaa 100,100,204
ta 216,234,36864
mvaa 80,100,184
ta 218,234,37376
mvaa 80,28,200
dl 228,40,53,0
mvaa 84,32,200
ta 232,234,53248
mvaa 84,104,184
ta 234,234,53760
mvaa 104,104,204
ta 220,235,53248
mvaa 104,32,188
ta 222,235,53760
mvaa 108,36,188
dl 229,40,53,1024
mvaa 108,108,204
s_add_u32 s53,s53,s58
ta 236,235,36864
mvaa 88,108,184
ta 238,235,37376
mvaa 88,36,200
ta 0,236,36864
s_waitcnt lgkmcnt(8)
mvaa 0,72,4
ta 2,236,37376
mvaa 0,76,80
dl 230,44,54,2048
mvaa 4,64,80
ta 20,236,53248
mvaa 4,68,4
ta 22,236,53760
mvaa 92,68,8
ta 44,237,53248
mvaa 92,64,84
ta 46,237,53760
mvaa 72,76,84
dl 231,44,54,3072
mvaa 72,72,8
s_add_u32 m0,s98,32768
ta 40,237,36864
s_add_u32 s54,s54,s58
mvaa 144,72,12
ta 42,237,37376
mvaa 144,76,88
ta 192,234,40960
mvaa 148,64,88
ta 194,234,41472
mvaa 148,68,12
ta 176,234,57344
mvaa 156,68,16
dl 224,36,52,0
mvaa 156,64,92
ta 178,234,57856
mvaa 152,76,92
ta 196,235,57344
mvaa 152,72,16
ta 198,235,57856
mvaa 160,208,16
ta 180,235,40960
mvaa 160,224,92
dl 225,36,52,1024
mvaa 164,212,92
ta 182,235,41472
mvaa 164,228,16
ta 52,236,40960
mvaa 172,228,12
ta 54,236,41472
mvaa 172,212,88
ta 48,236,57344
mvaa 168,224,88
s_mov_b64 exec,3
global_load_dword a250,v253,s[96:97]
s_mov_b64 exec,-1
mvaa 168,208,12
ta 50,236,57856
v_lshrrev_b32_e32 v253,1,v244
mvaa 176,208,8
ta 60,237,57344
mvaa 176,224,84
ta 62,237,57856
mvaa 180,212,84
ta 56,237,40960
mvaa 180,228,8
dl 226,36,52,2048
mvaa 188,228,4
ta 58,237,41472
mvaa 188,212,80
ra 96,233,0
mvaa 184,224,80
ra 24,232,0
mvaa 184,208,4
ra 100,233,2048
s_waitcnt lgkmcnt(8)
mvaa 192,216,4
dl 227,36,52,3072
mvaa 192,232,80
s_add_u32 s52,s52,s56
s_add_u32 m0,s98,36864
ra 28,232,2048
mvaa 196,220,80
ra 104,233,4096
mvaa 196,236,4
ra 32,232,4096
mvaa 204,236,8
ta 188,238,45056
mvaa 204,220,84
ta 190,238,45568
mvaa 200,232,84
dl 224,36,52,0
mvaa 200,216,8
ta 204,238,61440
maaa 164,216,12
ta 206,238,61952
maaa 164,232,88
ta 184,239,61440
maaa 148,220,88
ta 186,239,61952
maaa 148,236,12
dl 225,36,52,1024
maaa 144,236,16
ra 108,233,6144
v_add_u32_e32 v233,0xffff0000,v233
maaa 144,220,92
ra 36,232,6144
v_add_u32_e32 v232,0xffff0000,v232
maaa 160,232,92
ta 200,239,45056
v_accvgpr_read_b32 v208,a164
maaa 160,216,16
ta 202,239,45568
v_accvgpr_read_b32 v209,a165
maaa 128,0,16
dl 226,36,52,2048
maaa 128,20,92
v_accvgpr_read_b32 v210,a166
v_accvgpr_read_b32 v211,a167
maaa 112,44,92
v_accvgpr_read_b32 v212,a148
v_accvgpr_read_b32 v213,a149
maaa 112,40,16
v_accvgpr_read_b32 v214,a150
v_accvgpr_read_b32 v215,a151
maaa 116,40,12
v_accvgpr_read_b32 v220,a144
v_accvgpr_read_b32 v221,a145
maaa 116,44,88
dl 227,36,52,3072
maaa 132,20,88
s_add_u32 s52,s52,s60
v_accvgpr_read_b32 v222,a146
v_accvgpr_read_b32 v223,a147
maaa 132,0,12
s_lshl_b32 s60,%1,7
v_accvgpr_read_b32 v216,a160
v_accvgpr_read_b32 v217,a161
maaa 136,0,8
s_and_b32 s87,s60,7552
v_accvgpr_read_b32 v218,a162
v_accvgpr_read_b32 v219,a163
maaa 136,20,84
s_add_u32 s61,s60,83914752
maaa 120,44,84
maaa 120,40,8
maaa 124,40,4
maaa 124,44,80
maaa 140,20,80
maaa 140,0,4
mvaa 40,192,4
mvaa 40,176,80
mvaa 44,196,80
mvaa 44,180,4
mvaa 52,180,8
mvaa 52,196,84
mvaa 48,176,84
mvaa 48,192,8
mvaa 56,192,12
s_waitcnt vmcnt(9) lgkmcnt(0)
s_barrier
mvaa 56,176,88
s_cmp_eq_u32 s12,0
s_cselect_b64 exec,1,0
buffer_atomic_add v247,off,s[8:11],s85 sc1
s_mov_b64 exec,-1
mvaa 60,196,88
s_add_u32 m0,s62,32768
ta 172,234,0
s_lshr_b32 s85,s86,1
mvaa 60,180,12
s_lshl_b32 s85,s85,11
ta 174,234,512
v_bfe_u32 v247,v244,2,6
mvaa 68,180,16
ta 156,234,16384
mvaa 68,196,92
ta 158,234,16896
mvaa 64,176,92
dl 228,40,53,0
mvaa 64,192,16
ta 168,235,16384
mvaa 32,52,16
ta 170,235,16896
mvaa 32,48,92
ta 152,235,0
mvaa 36,60,92
ta 154,235,512
mvaa 36,56,16
dl 229,40,53,1024
mvaa 28,56,12
s_add_u32 s53,s53,s57
ta 68,236,0
mvaa 28,60,88
ta 70,236,512
mvaa 24,48,88
ta 64,236,16384
mvaa 24,52,12
ta 66,236,16896
mvaa 16,52,8
dl 230,44,54,2048
mvaa 16,48,84
ta 76,237,16384
mvaa 20,60,84
ta 78,237,16896
mvaa 20,56,8
ta 72,237,0
mvaa 12,56,4
ta 74,237,512
mvaa 12,60,80
dl 231,44,54,3072
mvaa 8,48,80
s_add_u32 m0,s62,49152
s_add_u32 s54,s54,s57
ta 228,234,4096
mvaa 8,52,4
ta 230,234,4608
mvaa 76,4,188
ta 212,234,20480
mvaa 76,80,204
ta 214,234,20992
mvaa 96,80,184
dl 228,40,53,0
mvaa 96,4,200
ta 224,235,20480
mvaa 100,8,200
ta 226,235,20992
mvaa 100,84,184
ta 208,235,4096
mvaa 80,84,204
ta 210,235,4608
mvaa 80,8,188
dl 229,40,53,1024
mvaa 84,12,188
s_add_u32 s53,s53,s58
ta 236,236,4096
mvaa 84,88,204
ta 238,236,4608
mvaa 104,88,184
ta 220,236,20480
mvaa 104,12,200
ta 222,236,20992
mvaa 108,16,200
dl 230,44,54,2048
mvaa 108,92,184
ta 232,237,20480
mvaa 88,92,204
ta 234,237,20992
mvaa 88,16,188
ta 216,237,4096
s_waitcnt lgkmcnt(8)
mvaa 0,172,24
ta 218,237,4608
mvaa 0,156,96
dl 231,44,54,3072
mvaa 4,168,96
s_add_u32 m0,s98,65536
ta 40,234,8192
s_add_u32 s54,s54,s58
mvaa 4,152,24
ta 42,234,8704
mvaa 92,152,28
ta 44,234,24576
mvaa 92,168,100
ta 46,234,25088
mvaa 72,156,100
dl 224,36,52,0
mvaa 72,172,28
ta 20,235,24576
mvaa 144,172,32
ta 22,235,25088
mvaa 144,156,104
ta 0,235,8192
mvaa 148,168,104
ta 2,235,8704
mvaa 148,152,32
dl 225,36,52,1024
mvaa 156,152,36
ta 180,236,8192
mvaa 156,168,108
ta 182,236,8704
mvaa 152,156,108
ta 196,236,24576
mvaa 152,172,36
ta 198,236,25088
mvaa 160,68,36
dl 226,36,52,2048
mvaa 160,64,108
ta 176,237,24576
mvaa 164,76,108
ta 178,237,25088
mvaa 164,72,36
ta 192,237,8192
mvaa 172,72,32
ta 194,237,8704
mvaa 172,76,104
dl 227,36,52,3072
mvaa 168,64,104
s_add_u32 s52,s52,s56
s_add_u32 m0,s98,69632
ta 56,238,12288
mvaa 168,68,32
s_mov_b32 s98,s94
ta 58,238,12800
s_mov_b32 s99,s98
mvaa 176,68,28
s_and_b32 s94,s12,1
ta 60,238,28672
s_lshl_b32 s94,s94,14
mvaa 176,64,100
s_mul_i32 s56,s59,12288
ta 62,238,29184
mvaa 180,76,100
dl 224,36,52,0
mvaa 180,72,28
ta 48,239,28672
v_mov_b32_e32 v224,v244
mvaa 188,72,24
ta 50,239,29184
mvaa 188,76,96
ta 52,239,12288
mvaa 184,64,96
ta 54,239,12800
mvaa 184,68,24
dl 225,36,52,1024
mvaa 192,228,24
ra 80,233,0
v_add_u32_e32 v225,65536,v224
mvaa 192,212,96
ra 4,232,0
mvaa 196,224,96
ra 84,233,2048
s_waitcnt lgkmcnt(8)
mvaa 196,208,24
ra 8,232,2048
mvaa 204,208,28
dl 226,36,52,2048
mvaa 204,224,100
ra 88,233,4096
v_mov_b32_e32 v226,1065369472
mvaa 200,212,100
ra 12,232,4096
mvaa 200,228,28
ra 16,232,6144
v_add_u32_e32 v232,0x8000,v232
mvaa 208,228,32
ra 92,233,6144
v_add_u32_e32 v233,0x8000,v233
mvaa 208,212,104
dl 227,36,52,3072
mvaa 212,224,104
s_mov_b64 s[36:37],s[28:29]
s_mov_b32 s38,8388608
s_mov_b32 s39,131072
mvaa 212,208,32
s_lshl_b32 s52,s12,12
mvaa 220,208,36
mvaa 220,224,108
mvaa 216,212,108
mvaa 216,228,36
maaa 128,236,36
maaa 128,220,108
maaa 112,232,108
maaa 112,216,36
maaa 116,216,32
maaa 116,232,104
maaa 132,220,104
maaa 132,236,32
maaa 136,236,28
maaa 136,220,100
maaa 120,232,100
maaa 120,216,28
maaa 124,216,24
maaa 124,232,96
maaa 140,220,96
s_waitcnt vmcnt(8) lgkmcnt(0)
s_barrier
s_add_u32 m0,s62,0
maaa 140,236,24
dl 228,40,53,0
mvaa 40,40,24
ta 200,234,32768
mvaa 40,44,96
ta 202,234,33280
mvaa 44,20,96
ta 184,234,49152
mvaa 44,0,24
ta 186,234,49664
mvaa 52,0,28
dl 229,40,53,1024
mvaa 52,20,100
s_add_u32 s53,s53,s57
ta 204,235,49152
mvaa 48,44,100
ta 206,235,49664
mvaa 48,40,28
ta 188,235,32768
mvaa 56,40,32
ta 190,235,33280
mvaa 56,44,104
dl 230,44,54,2048
mvaa 60,20,104
ta 164,236,32768
mvaa 60,0,32
ta 166,236,33280
mvaa 68,0,36
ta 148,236,49152
mvaa 68,20,108
ta 150,236,49664
mvaa 64,44,108
dl 231,44,54,3072
mvaa 64,40,36
s_add_u32 m0,s62,16384
s_add_u32 s54,s54,s57
ta 160,237,49152
mvaa 32,180,36
ta 162,237,49664
mvaa 32,196,108
ta 144,237,32768
mvaa 36,176,108
ta 146,237,33280
mvaa 36,192,36
dl 228,40,53,0
mvaa 28,192,32
ta 152,234,36864
mvaa 28,176,104
ta 154,234,37376
mvaa 24,196,104
ta 168,234,53248
mvaa 24,180,32
ta 170,234,53760
mvaa 16,180,28
dl 229,40,53,1024
mvaa 16,196,100
s_add_u32 s53,s53,s58
ta 156,235,53248
mvaa 20,176,100
ta 158,235,53760
mvaa 20,192,28
L_vs13:
s_cbranch_execz L_vr0
ta 172,235,36864
mvaa 12,192,24
ta 174,235,37376
mvaa 12,176,96
dl 230,44,54,2048
mvaa 8,196,96
ta 208,236,36864
mvaa 8,180,24
ta 210,236,37376
mvaa 76,24,56
ta 224,236,53248
mvaa 76,96,60
ta 226,236,53760
mvaa 96,96,48
dl 231,44,54,3072
mvaa 96,24,52
s_add_u32 s54,s54,s58
ta 212,237,53248
s_add_u32 s58,s56,8192
mvaa 100,28,52
ta 214,237,53760
mvaa 100,100,48
ta 228,237,36864
mvaa 80,100,60
ta 230,237,37376
mvaa 80,28,56
ta 216,234,40960
mvaa 84,32,56
ta 218,234,41472
mvaa 84,104,60
ta 232,234,57344
mvaa 104,104,48
ta 234,234,57856
mvaa 104,32,52
ta 220,235,57344
mvaa 108,36,52
ta 222,235,57856
mvaa 108,108,48
ta 236,235,40960
mvaa 88,108,60
ta 238,235,41472
mvaa 88,36,56
ta 0,236,40960
s_waitcnt lgkmcnt(10)
mvaa 0,200,4
ta 2,236,41472
mvaa 0,184,80
ta 20,236,57344
mvaa 4,204,80
ta 22,236,57856
mvaa 4,188,4
ta 192,237,57344
mvaa 92,188,8
ta 194,237,57856
mvaa 92,204,84
ta 176,237,40960
mvaa 72,184,84
ta 178,237,41472
mvaa 72,200,8
ta 196,238,45056
mvaa 144,200,12
ta 198,238,45568
mvaa 144,184,88
ta 180,238,61440
mvaa 148,204,88
ta 182,238,61952
mvaa 148,188,12
ra 24,232,0
mvaa 156,188,16
ra 28,232,2048
mvaa 156,204,92
ra 32,232,4096
mvaa 152,184,92
ra 36,232,6144
mvaa 152,200,16
ta 188,239,61440
mvaa 160,164,16
ta 190,239,61952
mvaa 160,148,92
ta 204,239,45056
mvaa 164,160,92
ta 206,239,45568
mvaa 164,144,16
mvaa 172,144,12
mvaa 172,160,88
mvaa 168,148,88
mvaa 168,164,12
mvaa 176,164,8
mvaa 176,148,84
mvaa 180,160,84
mvaa 180,144,8
mvaa 188,144,4
mvaa 188,160,80
mvaa 184,148,80
mvaa 184,164,4
mvaa 192,152,4
mvaa 192,168,80
mvaa 196,156,80
mvaa 196,172,4
mvaa 204,172,8
mvaa 204,156,84
mvaa 200,168,84
mvaa 200,152,8
mvaa 208,152,12
mvaa 208,168,88
mvaa 212,156,88
mvaa 212,172,12
mvaa 220,172,16
mvaa 220,156,92
mvaa 216,168,92
mvaa 216,152,16
maaa 128,208,16
maaa 128,224,92
maaa 112,212,92
s_waitcnt vmcnt(0) lgkmcnt(0)
s_barrier
s_add_u32 m0,s62,32768
maaa 112,228,16
dl 228,40,53,0
maaa 116,228,12
ta 40,234,0
maaa 116,212,88
ta 42,234,512
maaa 132,224,88
ta 44,235,0
maaa 132,208,12
ta 46,235,512
maaa 136,208,8
dl 229,40,53,1024
maaa 136,224,84
s_add_u32 s53,s53,s57
ta 48,236,0
maaa 120,212,84
ta 50,236,512
maaa 120,228,8
ta 52,237,0
maaa 124,228,4
ta 54,237,512
maaa 124,212,80
dl 230,44,54,2048
maaa 140,224,80
ta 56,234,4096
maaa 140,208,4
ta 58,234,4608
mvaa 40,216,4
ta 60,235,4096
mvaa 40,232,80
ta 62,235,4608
mvaa 44,220,80
dl 231,44,54,3072
mvaa 44,236,4
s_add_u32 m0,s62,49152
ta 64,236,4096
s_add_u32 s54,s54,s57
mvaa 52,236,8
s_add_u32 s57,s56,4096
ta 66,236,4608
mvaa 52,220,84
ta 68,237,4096
mvaa 48,232,84
ta 70,237,4608
mvaa 48,216,8
buffer_load_dword v248,v250,s[8:11],0 offen sc1
mvaa 56,216,12
ta 72,234,8192
mvaa 56,232,88
ta 74,234,8704
mvaa 60,220,88
ta 76,235,8192
mvaa 60,236,12
ta 78,235,8704
mvaa 68,236,16
dl 228,40,53,0
mvaa 68,220,92
ta 96,236,8192
mvaa 64,232,92
ta 98,236,8704
mvaa 64,216,16
ta 100,237,8192
mvaa 32,0,16
ta 102,237,8704
mvaa 32,20,92
dl 229,40,53,1024
mvaa 36,192,92
s_lshl_b32 s43,s19,15
ta 104,238,12288
s_add_u32 s42,s43,16384
mvaa 36,176,16
s_add_u32 s53,s87,83898368
ta 106,238,12800
s_lshl_b32 s87,s91,8
mvaa 28,176,12
s_lshl_b32 s91,s23,2
ta 108,239,12288
s_mov_b32 s40,858993459
mvaa 28,192,88
s_mov_b32 s41,858993459
ta 110,239,12800
v_cndmask_b32_e64 v228,0,v226,s[40:41]
mvaa 24,20,88
dl 230,44,54,2048
mvaa 24,0,12
ta 144,234,16384
v_cndmask_b32_e64 v229,0,v226,s[40:41]
mvaa 16,0,8
ta 146,234,16896
v_cndmask_b32_e64 v230,0,v226,s[40:41]
mvaa 16,20,84
ta 164,235,16384
mvaa 20,192,84
ta 166,235,16896
mvaa 20,176,8
dl 231,44,54,3072
)"
R"(mvaa 12,176,4
s_add_u32 s54,s87,s88
ta 168,236,16384
s_lshl_b32 s87,s12,16
mvaa 12,192,80
s_add_u32 s87,s87,s54
ta 170,236,16896
s_andn2_b32 s87,s87,64
mvaa 8,20,80
s_lshl_b32 s88,s66,5
ta 244,235,24576
s_add_u32 s63,s87,s88
mvaa 8,0,4
s_lshl_b32 s88,s67,5
ta 246,235,25088
s_add_u32 s65,s87,s88
mvaa 76,4,196
s_and_b32 s87,s20,4
ta 212,235,20480
s_lshl_b32 s87,s87,8
mvaa 76,80,180
s_and_b32 s88,s20,3
ta 214,235,20992
s_add_u32 s85,s85,s87
mvaa 96,80,188
s_add_u32 s85,s85,s89
ta 236,237,20480
s_lshl_b32 s89,s88,20
mvaa 96,4,204
s_add_u32 s85,s85,s89
ta 238,237,20992
s_and_b32 s89,s86,7
mvaa 100,8,204
s_add_u32 s49,s49,s87
ta 216,236,20480
s_lshl_b32 s89,s89,7
mvaa 100,84,188
s_add_u32 s49,s49,s89
ta 218,236,20992
s_lshl_b32 s89,s88,18
mvaa 80,84,180
s_add_u32 s49,s49,s89
ta 192,234,20480
s_lshl_b32 s89,s12,6
mvaa 80,8,196
s_add_u32 s89,s89,83886080
ta 194,234,20992
s_add_u32 s91,s91,s89
mvaa 84,12,196
s_mov_b32 s46,16384
ra 20,233,0
s_mov_b32 s86,268435456
mvaa 84,88,180
s_bfe_u32 s87,s98,327691
s_cselect_b32 s87,2048,0
ra 0,232,32768
v_cndmask_b32_e64 v231,0,v226,s[40:41]
mvaa 104,88,188
s_mov_b32 s40,0xf0000
ra 80,233,4096
s_mov_b32 s41,0xfff00ff
mvaa 104,12,204
s_sub_u32 s98,s98,s87
ra 4,233,32768
v_cndmask_b32_e64 v226,0,v252,s[40:41]
mvaa 108,16,204
s_mov_b32 s40,0x3f0003
ra 84,232,36864
s_mov_b32 s41,0x3fff03ff
mvaa 108,92,188
s_sub_u32 s47,32768,s43
ra 8,233,2048
v_cndmask_b32_e64 v227,0,v252,s[40:41]
mvaa 88,92,180
s_mov_b32 s40,0xfff0ffff
ra 88,233,36864
s_mov_b32 s41,0xf000ff00
mvaa 88,16,196
s_add_u32 s47,s47,32768
ra 12,232,34816
s_lshr_b32 s45,s15,1
s_waitcnt lgkmcnt(10)
mvaa 0,40,24
s_xor_b32 s62,s63,128
ta 188,237,16384
s_xor_b32 s64,s65,128
mvaa 4,44,24
s_ff1_i32_b32 s80,s45
ta 190,237,16896
s_sub_u32 s44,s56,2048
mvaa 92,44,28
ra 92,233,6144
mvaa 72,40,28
ra 16,233,34816
mvaa 144,40,32
mvaa 148,44,32
mvaa 156,44,36
mvaa 152,40,36
mvaa 160,48,36
ra 44,232,38912
v_mov_b32_e32 v232,0xc1a00000
mvaa 164,52,36
ra 40,233,38912
v_mov_b32_e32 v233,0xc1a00000
mvaa 172,52,32
mvaa 168,48,32
mvaa 176,48,28
mvaa 180,52,28
mvaa 188,52,24
mvaa 184,48,24
mvaa 192,56,24
ta 52,239,28672
mvaa 196,60,24
ta 54,239,29184
mvaa 204,60,28
mvaa 200,56,28
mvaa 208,56,32
mvaa 212,60,32
mvaa 220,60,36
mvaa 216,56,36
maaa 128,64,36
maaa 112,68,36
maaa 116,68,32
maaa 132,64,32
maaa 136,64,28
maaa 120,68,28
maaa 124,68,24
maaa 140,64,24
mvaa 40,72,24
mvaa 44,76,24
ta 64,238,28672
ta 66,238,29184
mvaa 52,76,28
s_waitcnt vmcnt(4)
v_and_b32_e32 v248,v242,v248
v_cmp_ne_u32_e64 s[100:101],v243,v248
mvaa 48,72,28
s_waitcnt vmcnt(0)
s_barrier
ta 148,234,32768
ta 150,234,33280
ta 152,234,49152
ta 154,234,49664
ta 156,235,49152
mvaa 56,72,32
s_cmp_lg_u64 s[100:101],0
s_cbranch_scc1 L_q_spin
L_q_ok:
ta 158,235,49664
ta 160,235,32768
ta 162,235,33280
ta 172,236,32768
ta 174,236,33280
ta 176,236,49152
ta 178,236,49664
mvaa 60,76,32
buffer_load_dword v242,v247,s[8:11],s91 offen offset:0 sc1
ta 180,237,49152
ta 182,237,49664
ta 184,237,32768
ta 186,237,33280
ta 196,234,36864
mvaa 68,76,36
s_mov_b32 m0,s95
ta 198,234,37376
ta 200,234,53248
ta 202,234,53760
ta 204,235,53248
ta 206,235,53760
ta 208,235,36864
ta 210,235,37376
mvaa 64,72,36
s_lshl_b32 s35,s90,2
ta 220,236,36864
ta 222,236,37376
ta 224,236,53248
mvaa 32,96,36
s_lshl_b32 s90,s22,2
ta 226,236,53760
ta 228,237,53248
mvaa 36,100,36
s_add_u32 s90,s90,s89
ta 230,237,53760
ta 232,237,36864
mvaa 28,100,32
ds 249,8,42,0
ta 234,237,37376
ta 240,235,40960
mvaa 24,96,32
s_add_u32 s35,s35,83886080
ta 242,235,41472
ta 48,239,45056
mvaa 16,96,28
ta 50,239,45568
ta 60,239,61440
mvaa 20,100,28
ta 62,239,61952
ta 56,238,61440
mvaa 12,100,24
ta 58,238,61952
ta 68,238,45056
mvaa 8,96,24
ds 249,8,42,1024
ta 70,238,45568
ta 76,237,24576
mvaa 76,24,104
ta 78,237,25088
ta 72,237,40960
mvaa 96,24,108
ta 74,237,41472
ta 100,237,57344
mvaa 100,28,108
ta 102,237,57856
ta 96,236,57344
mvaa 80,28,104
ta 98,236,57856
ta 24,236,40960
mvaa 84,32,104
ds 249,8,42,2048
ta 26,236,41472
mvaa 104,32,108
ta 28,236,24576
ta 30,236,25088
mvaa 108,36,108
v_lshl_add_u32 v238,v244,3,s4
v_min_u32_e32 v238,L_att_end-L_att_pc-4,v238
mvaa 88,36,104
ta 32,234,24576
ta 34,234,25088
s_waitcnt lgkmcnt(15)
mvaa 0,144,20
ta 108,234,40960
ta 110,234,41472
mvaa 0,148,0
ds 249,8,42,3072
ta 36,234,57344
ta 38,234,57856
ta 104,235,57344
ta 106,235,57856
mvaa 0,152,4
v_mov_b32_e32 v234,0xc1a00000
v_mov_b32_e32 v235,0xc1a00000
mvaa 4,156,4
v_cndmask_b32_e64 v250,0,v252,s[40:41]
s_mov_b32 s40,0xffc0fffc
mvaa 4,160,0
s_mov_b32 s41,0xc000fc00
v_bfe_i32 v248,v244,4,1
v_mul_f32_e32 v0,0x3e0293ee,v0
mvaa 4,164,20
v_mul_f32_e32 v1,0x3e0293ee,v1
v_mul_f32_e32 v2,0x3e0293ee,v2
mvaa 92,164,8
buffer_load_dword v240,v247,s[8:11],s90 offen offset:0 sc1
mvaa 92,160,12
v_mul_f32_e32 v3,0x3e0293ee,v3
cv 0,0,1
mvaa 92,156,16
v_mul_f32_e32 v4,0x3e0293ee,v4
cv 1,2,3
mvaa 72,152,16
v_mul_f32_e32 v5,0x3e0293ee,v5
s_waitcnt lgkmcnt(0)
s_barrier
mvaa 72,148,12
s_mov_b32 m0,s70
v_mul_f32_e32 v6,0x3e0293ee,v6
v_mul_f32_e32 v7,0x3e0293ee,v7
mvaa 72,144,8
buffer_load_dword v241,v247,s[8:11],s90 offen offset:256 sc1
mvaa 144,144,80
cv 2,4,5
cv 3,6,7
mvaa 144,148,84
v_mul_f32_e32 v92,0x3e0293ee,v92
v_mul_f32_e32 v93,0x3e0293ee,v93
mvaa 144,152,88
v_mul_f32_e32 v94,0x3e0293ee,v94
v_mul_f32_e32 v72,0x3e0293ee,v72
mvaa 148,156,88
v_mul_f32_e32 v95,0x3e0293ee,v95
v_mul_f32_e32 v73,0x3e0293ee,v73
mvaa 148,160,84
ds 249,8,43,0
mvaa 148,164,80
v_mul_f32_e32 v74,0x3e0293ee,v74
v_mul_f32_e32 v75,0x3e0293ee,v75
mvaa 156,164,92
cv 72,72,73
cv 73,74,75
mvaa 156,160,44
cv 74,92,93
cv 75,94,95
mvaa 156,156,40
v_mul_f32_e32 v144,0x3e0293ee,v144
v_mul_f32_e32 v145,0x3e0293ee,v145
mvaa 152,152,40
ds 249,8,43,1024
mvaa 152,148,44
v_mul_f32_e32 v146,0x3e0293ee,v146
v_mul_f32_e32 v147,0x3e0293ee,v147
mvaa 152,144,92
cv 144,144,145
v_mul_f32_e32 v148,0x3e0293ee,v148
mvaa 160,168,92
cv 145,146,147
v_mul_f32_e32 v149,0x3e0293ee,v149
mvaa 160,172,44
v_mul_f32_e32 v150,0x3e0293ee,v150
v_mul_f32_e32 v151,0x3e0293ee,v151
mvaa 160,176,40
ds 249,8,43,2048
mvaa 164,180,40
cv 146,148,149
cv 147,150,151
mvaa 164,184,44
v_mul_f32_e32 v156,0x3e0293ee,v156
v_mul_f32_e32 v157,0x3e0293ee,v157
mvaa 164,188,92
v_mul_f32_e32 v152,0x3e0293ee,v152
v_mul_f32_e32 v158,0x3e0293ee,v158
mvaa 172,188,80
v_mul_f32_e32 v159,0x3e0293ee,v159
v_mul_f32_e32 v153,0x3e0293ee,v153
mvaa 172,184,84
L_vs14:
s_cbranch_execz L_vr0
ds 249,8,43,3072
s_mov_b32 m0,s72
mvaa 172,180,88
v_mul_f32_e32 v154,0x3e0293ee,v154
v_mul_f32_e32 v155,0x3e0293ee,v155
mvaa 168,176,88
cv 152,152,153
cv 153,154,155
mvaa 168,172,84
ds_write_b128 v251,v[0:3] offset:0
mvaa 168,168,80
cv 154,156,157
cv 155,158,159
mvaa 176,168,8
ds 255,8,42,0
mvaa 176,172,12
v_mul_f32_e32 v160,0x3e0293ee,v160
v_mul_f32_e32 v161,0x3e0293ee,v161
mvaa 176,176,16
v_mul_f32_e32 v162,0x3e0293ee,v162
v_mul_f32_e32 v163,0x3e0293ee,v163
mvaa 180,180,16
cv 160,160,161
v_mul_f32_e32 v164,0x3e0293ee,v164
mvaa 180,184,12
v_mul_f32_e32 v165,0x3e0293ee,v165
cv 161,162,163
mvaa 180,188,8
ds 255,8,42,1024
ds_write_b128 v251,v[72:75] offset:4096
mvaa 188,188,20
v_mul_f32_e32 v166,0x3e0293ee,v166
v_mul_f32_e32 v167,0x3e0293ee,v167
mvaa 188,184,0
cv 162,164,165
cv 163,166,167
mvaa 188,180,4
v_mul_f32_e32 v172,0x3e0293ee,v172
v_mul_f32_e32 v173,0x3e0293ee,v173
mvaa 184,176,4
v_mul_f32_e32 v174,0x3e0293ee,v174
v_mul_f32_e32 v175,0x3e0293ee,v175
mvaa 184,172,0
ds 255,8,42,2048
mvaa 184,168,20
v_mul_f32_e32 v168,0x3e0293ee,v168
v_mul_f32_e32 v169,0x3e0293ee,v169
mvaa 192,192,20
ds_write_b128 v251,v[144:147] offset:8192
mvaa 192,196,0
v_mul_f32_e32 v170,0x3e0293ee,v170
v_mul_f32_e32 v171,0x3e0293ee,v171
mvaa 192,200,4
cv 168,168,169
cv 169,170,171
mvaa 196,204,4
ds 255,8,42,3072
mvaa 196,208,0
cv 170,172,173
cv 171,174,175
mvaa 196,212,20
v_mul_f32_e32 v176,0x3e0293ee,v176
v_mul_f32_e32 v177,0x3e0293ee,v177
mvaa 204,212,8
v_mul_f32_e32 v178,0x3e0293ee,v178
v_mul_f32_e32 v179,0x3e0293ee,v179
mvaa 204,208,12
ds_write_b128 v251,v[152:155] offset:12288
mvaa 204,204,16
buffer_load_dword v243,v247,s[8:11],s91 offen offset:256 sc1
mvaa 200,200,16
cv 176,176,177
cv 177,178,179
mvaa 200,196,12
v_mul_f32_e32 v180,0x3e0293ee,v180
v_mul_f32_e32 v181,0x3e0293ee,v181
mvaa 200,192,8
v_mul_f32_e32 v182,0x3e0293ee,v182
v_mul_f32_e32 v183,0x3e0293ee,v183
mvaa 208,192,80
cv 178,180,181
cv 179,182,183
mvaa 208,196,84
v_mul_f32_e32 v188,0x3e0293ee,v188
v_mul_f32_e32 v189,0x3e0293ee,v189
mvaa 208,200,88
ds_write_b128 v251,v[160:163] offset:13312
mvaa 212,204,88
v_mul_f32_e32 v190,0x3e0293ee,v190
v_mul_f32_e32 v191,0x3e0293ee,v191
mvaa 212,208,84
v_mul_f32_e32 v184,0x3e0293ee,v184
v_mul_f32_e32 v185,0x3e0293ee,v185
mvaa 212,212,80
v_mul_f32_e32 v186,0x3e0293ee,v186
v_mul_f32_e32 v187,0x3e0293ee,v187
mvaa 220,212,92
cv 184,184,185
cv 185,186,187
mvaa 220,208,44
cv 186,188,189
cv 187,190,191
mvaa 220,204,40
v_mul_f32_e32 v192,0x3e0293ee,v192
v_mul_f32_e32 v193,0x3e0293ee,v193
mvaa 216,200,40
ds_write_b128 v251,v[168:171] offset:9216
mvaa 216,196,44
v_mul_f32_e32 v194,0x3e0293ee,v194
v_mul_f32_e32 v195,0x3e0293ee,v195
mvaa 216,192,92
cv 192,192,193
cv 193,194,195
maaa 128,216,92
v_mul_f32_e32 v196,0x3e0293ee,v196
v_mul_f32_e32 v197,0x3e0293ee,v197
maaa 128,220,44
v_mul_f32_e32 v198,0x3e0293ee,v198
v_mul_f32_e32 v199,0x3e0293ee,v199
maaa 128,224,40
cv 194,196,197
cv 195,198,199
maaa 112,228,40
v_mul_f32_e32 v204,0x3e0293ee,v204
v_mul_f32_e32 v205,0x3e0293ee,v205
maaa 112,232,44
ds_write_b128 v251,v[176:179] offset:5120
maaa 112,236,92
v_mul_f32_e32 v206,0x3e0293ee,v206
v_mul_f32_e32 v207,0x3e0293ee,v207
maaa 116,236,80
v_mul_f32_e32 v200,0x3e0293ee,v200
v_mul_f32_e32 v201,0x3e0293ee,v201
maaa 116,232,84
v_mul_f32_e32 v202,0x3e0293ee,v202
v_mul_f32_e32 v203,0x3e0293ee,v203
maaa 116,228,88
cv 200,200,201
cv 201,202,203
maaa 132,224,88
cv 202,204,205
cv 203,206,207
maaa 132,220,84
v_mul_f32_e32 v208,0x3e0293ee,v208
v_mul_f32_e32 v209,0x3e0293ee,v209
maaa 132,216,80
ds_write_b128 v251,v[184:187] offset:1024
maaa 136,216,8
v_mul_f32_e32 v210,0x3e0293ee,v210
v_mul_f32_e32 v211,0x3e0293ee,v211
maaa 136,220,12
cv 208,208,209
cv 209,210,211
maaa 136,224,16
v_mul_f32_e32 v212,0x3e0293ee,v212
v_mul_f32_e32 v213,0x3e0293ee,v213
maaa 120,228,16
s_waitcnt vmcnt(11)
s_barrier
maaa 120,232,12
v_mul_f32_e32 v214,0x3e0293ee,v214
v_mul_f32_e32 v215,0x3e0293ee,v215
maaa 120,236,8
cv 210,212,213
cv 211,214,215
maaa 124,236,20
ds_write_b128 v251,v[192:195] offset:2048
maaa 124,232,0
v_mul_f32_e32 v220,0x3e0293ee,v220
v_mul_f32_e32 v221,0x3e0293ee,v221
maaa 124,228,4
v_mul_f32_e32 v216,0x3e0293ee,v216
v_mul_f32_e32 v217,0x3e0293ee,v217
maaa 140,224,4
v_mul_f32_e32 v222,0x3e0293ee,v222
v_mul_f32_e32 v223,0x3e0293ee,v223
maaa 140,220,0
v_mul_f32_e32 v218,0x3e0293ee,v218
v_mul_f32_e32 v219,0x3e0293ee,v219
maaa 140,216,20
cv 216,216,217
cv 217,218,219
mvaa 88,92,64
cv 218,220,221
cv 219,222,223
mvaa 88,44,68
ds_write_b128 v251,v[200:203] offset:6144
mvaa 88,40,56
v_accvgpr_read_b32 v0,a128
v_accvgpr_read_b32 v1,a129
mvaa 108,40,60
v_accvgpr_read_b32 v2,a130
v_accvgpr_read_b32 v3,a131
mvaa 108,44,48
v_mul_f32_e32 v0,0x3e0293ee,v0
v_mul_f32_e32 v1,0x3e0293ee,v1
cv 88,88,89
cv 89,90,91
mvaa 108,92,52
buffer_store_dwordx2 v[88:89],v253,s[8:11],s99 offen offset:1024 sc0 sc1
mvaa 36,76,92
v_mul_f32_e32 v2,0x3e0293ee,v2
v_mul_f32_e32 v3,0x3e0293ee,v3
mvaa 36,72,44
cv 0,0,1
cv 1,2,3
mvaa 36,100,40
ds_write_b128 v251,v[208:211] offset:10240
mvaa 32,96,40
cv 108,108,109
cv 109,110,111
mvaa 32,24,44
buffer_store_dwordx2 v[108:109],v253,s[8:11],s99 offen offset:1536 sc0 sc1
mvaa 32,28,92
v_accvgpr_read_b32 v4,a112
v_accvgpr_read_b32 v5,a113
mvaa 64,32,92
v_accvgpr_read_b32 v6,a114
v_mul_f32_e32 v4,0x3e0293ee,v4
mvaa 64,108,44
v_mul_f32_e32 v5,0x3e0293ee,v5
v_mul_f32_e32 v6,0x3e0293ee,v6
mvaa 64,36,40
cv 32,32,33
cv 33,34,35
cv 34,36,37
cv 35,38,39
mvaa 68,104,40
buffer_store_dwordx4 v[32:35],v244,s[8:11],s99 offen sc0 sc1
ds_write_b128 v251,v[216:219] offset:14336
mvaa 68,240,44
v_accvgpr_read_b32 v7,a115
v_mul_f32_e32 v7,0x3e0293ee,v7
mvaa 68,244,92
cv 2,4,5
cv 3,6,7
mvaa 60,244,80
ra 192,224,0
v_accvgpr_read_b32 v92,a116
mvaa 60,240,84
ra 196,224,1024
v_accvgpr_read_b32 v93,a117
mvaa 60,104,88
ra 200,224,2048
v_mul_f32_e32 v92,0x3e0293ee,v92
mvaa 56,36,88
ra 204,224,3072
v_mul_f32_e32 v93,0x3e0293ee,v93
mvaa 56,108,84
ds_write_b128 v251,v[0:3] offset:15360
mvaa 56,32,80
v_accvgpr_read_b32 v94,a118
v_accvgpr_read_b32 v95,a119
mvaa 24,28,80
v_accvgpr_read_b32 v72,a132
v_accvgpr_read_b32 v73,a133
mvaa 24,24,84
v_accvgpr_read_b32 v74,a134
v_mul_f32_e32 v94,0x3e0293ee,v94
mvaa 24,96,88
v_mul_f32_e32 v95,0x3e0293ee,v95
v_mul_f32_e32 v72,0x3e0293ee,v72
mvaa 28,100,88
v_mul_f32_e32 v73,0x3e0293ee,v73
v_mul_f32_e32 v74,0x3e0293ee,v74
v_accvgpr_read_b32 v75,a135
mvaa 28,72,84
v_mul_f32_e32 v75,0x3e0293ee,v75
cv 72,72,73
cv 73,74,75
cv 74,92,93
mvaa 28,76,80
ra 208,224,4096
cv 75,94,95
mvaa 20,76,8
ds_write_b128 v251,v[72:75] offset:11264
mvaa 20,72,12
v_accvgpr_read_b32 v144,a136
v_accvgpr_read_b32 v145,a137
mvaa 20,100,16
s_mov_b64 exec,3
global_load_dword v239,v238,s[96:97]
s_mov_b64 exec,-1
ra 212,224,5120
ra 248,224,14336
ra 252,224,15360
mvaa 16,96,16
v_accvgpr_read_b32 v146,a138
v_mul_f32_e32 v144,0x3e0293ee,v144
v_mul_f32_e32 v145,0x3e0293ee,v145
v_mul_f32_e32 v146,0x3e0293ee,v146
v_accvgpr_read_b32 v147,a139
v_mul_f32_e32 v147,0x3e0293ee,v147
cv 144,144,145
mvaa 16,24,12
ra 236,224,11264
v_accvgpr_read_b32 v148,a120
v_accvgpr_read_b32 v149,a121
v_accvgpr_read_b32 v150,a122
cv 145,146,147
mvaa 16,28,8
v_mul_f32_e32 v148,0x3e0293ee,v148
v_mul_f32_e32 v149,0x3e0293ee,v149
v_mul_f32_e32 v150,0x3e0293ee,v150
v_accvgpr_read_b32 v151,a123
v_mul_f32_e32 v151,0x3e0293ee,v151
cv 146,148,149
mvaa 48,32,8
ra 228,224,9216
cv 147,150,151
mvaa 48,108,12
ds_write_b128 v251,v[144:147] offset:7168
mvaa 48,36,16
v_accvgpr_read_b32 v156,a124
v_accvgpr_read_b32 v157,a125
mvaa 52,104,16
ra 232,224,10240
ra 216,224,6144
ra 220,224,7168
ra 224,224,8192
v_accvgpr_read_b32 v158,a126
v_accvgpr_read_b32 v159,a127
mvaa 52,240,12
v_accvgpr_read_b32 v152,a140
v_mul_f32_e32 v156,0x3e0293ee,v156
v_mul_f32_e32 v157,0x3e0293ee,v157
v_mul_f32_e32 v158,0x3e0293ee,v158
v_mul_f32_e32 v159,0x3e0293ee,v159
mvaa 52,244,8
v_mul_f32_e32 v152,0x3e0293ee,v152
v_accvgpr_read_b32 v153,a141
v_mul_f32_e32 v153,0x3e0293ee,v153
v_accvgpr_read_b32 v154,a142
v_accvgpr_read_b32 v155,a143
mvaa 44,244,20
v_mul_f32_e32 v154,0x3e0293ee,v154
v_mul_f32_e32 v155,0x3e0293ee,v155
cv 152,152,153
cv 153,154,155
cv 154,156,157
mvaa 44,240,0
ra 244,224,13312
cv 155,158,159
mvaa 44,104,4
ds_write_b128 v251,v[152:155] offset:3072
mvaa 40,36,4
cv 64,64,65
cv 56,56,57
mvaa 40,108,0
cv 57,58,59
cv 58,60,61
mvaa 40,32,20
ra 240,224,12288
cv 59,62,63
mvaa 8,28,20
cv 16,16,17
cv 17,18,19
mvaa 8,24,0
cv 48,48,49
cv 194,44,45
mvaa 8,96,4
cv 195,46,47
cv 192,40,41
mvaa 12,100,4
cv 193,42,43
cv 49,50,51
mvaa 12,72,0
s_cmp_eq_u32 s19,0
s_cselect_b32 s76,32768,0
s_cselect_b32 s77,49152,s86
s_cselect_b32 s78,65536,s46
s_cselect_b32 s79,81920,s86
mvaa 12,76,20
s_add_u32 s86,s95,98304
cv 8,8,9
v_add_u32_e32 v236,s86,v244
mvaa 76,20,64
s_add_u32 s46,s46,16384
v_xor_b32_e32 v237,4096,v236
s_waitcnt lgkmcnt(0)
s_barrier
s_mov_b32 m0,s68
mvaa 76,0,68
ds 249,8,76,0
ra 128,237,32768
ra 132,237,33792
mvaa 76,4,56
s_add_u32 s95,s47,32768
ra 136,237,34816
cv 9,10,11
mvaa 96,4,60
ra 140,237,35840
ra 144,237,49152
mvaa 96,0,48
cv 10,12,13
cv 11,14,15
mvaa 96,20,52
ra 148,237,50176
cv 50,52,53
mvaa 100,8,52
ds 249,8,76,1024
ra 152,237,51200
ra 156,237,52224
mvaa 100,12,48
ra 176,236,16384
cv 51,54,55
mvaa 100,16,60
ra 180,236,17408
cv 18,20,21
mvaa 80,16,56
ra 184,236,18432
cv 19,22,23
mvaa 80,12,68
ra 188,236,19456
cv 24,24,25
mvaa 80,8,64
ds 249,8,76,2048
ra 160,236,0
mvaa 84,80,64
ra 164,236,1024
cv 25,26,27
mvaa 84,84,68
ra 168,236,2048
ra 172,236,3072
mvaa 84,88,56
cv 26,28,29
cv 27,30,31
mvaa 104,88,60
cv 65,66,67
cv 66,68,69
mvaa 104,84,48
ds 249,8,76,3072
mvaa 104,80,52
s_mov_b32 m0,s69
cv 67,70,71
v_cndmask_b32_e64 v252,0,v252,s[40:41]
cvvv 164,8,192,112
cv 70,76,77
cv 71,78,79
cvvv 168,8,48,116
ds 249,8,77,0
cvvv 172,16,48,112
cv 94,96,97
v_mov_b32_e32 v76,v88
cvvv 176,16,56,116
cv 95,98,99
s_waitcnt vmcnt(9) lgkmcnt(0)
s_barrier
cvvv 180,24,56,112
ds 249,8,77,1024
cvvv 184,24,64,116
ra 0,224,32768
cv 96,100,101
cvvv 188,32,64,112
ra 4,224,33792
cv 97,102,103
cvaa 0,192,128,232
buffer_load_dword v17,v247,s[8:11],s35 offen offset:64 sc1
mvaa 0,200,132
ra 8,224,34816
cv 72,80,81
mvaa 0,208,136
ra 12,224,35840
cv 73,82,83
mvaa 0,216,140
ds 249,8,77,2048
cvaa 4,220,140,232
ra 16,224,36864
cv 74,84,85
mvaa 4,212,136
ra 20,224,37888
v_max3_f32 v172,v168,v169,v172
mvaa 4,204,132
ds 249,8,77,3072
mvaa 4,196,128
s_mov_b32 m0,s74
ra 24,224,38912
v_max_f32_e32 v172,v172,v173
cvaa 32,224,128,232
ra 28,224,39936
v_max3_f32 v174,v170,v171,v174
mvaa 32,232,132
buffer_load_dword v19,v247,s[8:11],s35 offen offset:192 sc1
mvaa 32,240,136
ra 32,224,40960
v_max_f32_e32 v174,v174,v175
mvaa 32,248,140
ra 36,224,41984
ra 40,224,43008
cvaa 36,252,140,232
buffer_load_dword v18,v247,s[8:11],s35 offen offset:128 sc1
ra 44,224,44032
mvaa 36,244,136
ra 48,224,45056
v_max_f32_e32 v148,v172,v174
mvaa 36,236,132
ra 56,224,47104
ra 60,224,48128
mvaa 36,228,128
buffer_load_dword v16,v247,s[8:11],s35 offen offset:0 sc1
ra 52,224,46080
cvaa 8,192,144,232
v_mov_b32_e32 v149,v148
cv 75,86,87
mvaa 8,200,148
v_permlane16_swap_b32_e32 v148,v149
mvaa 8,208,152
ds 255,8,43,0
mvaa 8,216,156
v_max_f32_e32 v148,v148,v149
v_mov_b32_e32 v149,v148
cvaa 12,220,156,232
cv 98,104,105
v_max3_f32 v180,v176,v177,v180
mvaa 12,212,152
ds 255,8,43,1024
mvaa 12,204,148
v_permlane32_swap_b32_e32 v148,v149
mvaa 12,196,144
v_max_f32_e32 v148,v148,v149
s_waitcnt lgkmcnt(0)
s_barrier
cvaa 40,224,144,232
ds 255,8,43,2048
mvaa 40,232,148
L_vs15:
s_cbranch_execz L_vr0
ra 64,225,0
v_mul_f32_e32 v148,0xbe8293ee,v148
mvaa 40,240,152
ra 68,225,1024
v_fmamk_f32 v172,v172,0x3e8293ee,v148
mvaa 40,248,156
ds 255,8,43,3072
cvaa 44,252,156,232
s_mov_b32 m0,s70
ra 72,225,2048
v_fmamk_f32 v174,v174,0x3e8293ee,v148
mvaa 44,244,152
ra 76,225,3072
v_max3_f32 v182,v178,v179,v182
mvaa 44,236,148
ds 249,8,78,0
mvaa 44,228,144
ra 80,225,4096
v_max_f32_e32 v180,v180,v181
cvaa 24,192,176,128
ra 84,225,5120
v_max_f32_e32 v182,v182,v183
mvaa 24,200,180
ds 249,8,78,1024
mvaa 24,208,184
ra 88,225,6144
v_max_f32_e32 v152,v180,v182
mvaa 24,216,188
ra 92,225,7168
v_mov_b32_e32 v153,v152
cvaa 28,220,188,132
ds 249,8,78,2048
mvaa 28,212,184
ra 96,225,8192
v_max3_f32 v188,v184,v185,v188
mvaa 28,204,180
ra 100,225,9216
ra 104,225,10240
mvaa 28,196,176
ds 249,8,78,3072
ra 108,225,11264
cvaa 56,224,176,136
s_mov_b32 m0,s71
ra 112,225,12288
v_max3_f32 v190,v186,v187,v190
mvaa 56,232,180
ra 116,225,13312
ra 120,225,14336
mvaa 56,240,184
ds 249,8,79,0
ra 124,225,15360
mvaa 56,248,188
ex 172
cvaa 60,252,188,140
ex 174
mvaa 60,244,184
ds 249,8,79,1024
mvaa 60,236,180
v_permlane16_swap_b32_e32 v152,v153
mvaa 60,228,176
cv 172,172,172
cv 174,174,174
cvaa 80,0,160,128
ds 249,8,79,2048
mvaa 80,8,164
v_and_b32_e32 v168,v226,v172
v_max_f32_e32 v152,v152,v153
mvaa 80,16,168
v_and_b32_e32 v169,v227,v174
v_and_b32_e32 v170,v250,v172
v_and_b32_e32 v171,v252,v174
mvaa 80,24,172
ds 249,8,79,3072
v_mov_b32_e32 v153,v152
cvaa 84,28,172,132
v_max_f32_e32 v188,v188,v189
v_permlane32_swap_b32_e32 v152,v153
v_max_f32_e32 v152,v152,v153
mvaa 84,20,168
v_mul_f32_e32 v152,0xbe8293ee,v152
v_fmamk_f32 v180,v180,0x3e8293ee,v152
mvaa 84,12,164
v_fmamk_f32 v182,v182,0x3e8293ee,v152
v_max_f32_e32 v190,v190,v191
mvaa 84,4,160
ex 180
cvaa 112,32,160,136
ex 182
v_max_f32_e32 v156,v188,v190
mvaa 112,40,164
v_mov_b32_e32 v157,v156
cv 180,180,180
mvaa 112,48,168
v_permlane16_swap_b32_e32 v156,v157
mvaa 112,56,172
cv 182,182,182
v_and_b32_e32 v176,v226,v180
v_max_f32_e32 v156,v156,v157
s_waitcnt vmcnt(15) lgkmcnt(0)
s_barrier
s_mov_b32 m0,s72
cvaa 116,60,172,140
ds 255,8,76,0
v_and_b32_e32 v177,v227,v182
mvaa 116,52,168
v_and_b32_e32 v178,v250,v180
v_and_b32_e32 v179,v252,v182
v_mov_b32_e32 v157,v156
mvaa 116,44,164
cv 99,106,107
v_permlane32_swap_b32_e32 v156,v157
v_max_f32_e32 v156,v156,v157
mvaa 116,36,160
ds 255,8,76,1024
v_mul_f32_e32 v156,0xbe8293ee,v156
zvvv 148,228,168
v_fmamk_f32 v188,v188,0x3e8293ee,v156
ex 188
zvvv 200,70,168
v_fmamk_f32 v190,v190,0x3e8293ee,v156
ex 190
zvvv 204,94,168
ds 255,8,76,2048
zvvv 152,228,176
cv 188,188,188
cv 190,190,190
v_and_b32_e32 v184,v226,v188
zvvv 208,72,176
v_and_b32_e32 v185,v227,v190
v_and_b32_e32 v186,v250,v188
v_and_b32_e32 v187,v252,v190
zvvv 212,96,176
ds 255,8,76,3072
s_mov_b32 m0,s73
v_mov_b32_e32 v77,v89
zvvv 156,228,184
v_mov_b32_e32 v100,v108
v_mov_b32_e32 v101,v109
zvvv 216,74,184
ex 0
zvvv 220,98,184
ds 255,8,77,0
cvaa 64,0,128,232
ex 1
mvaa 64,8,132
ex 2
)"
R"(mvaa 64,16,136
ds 255,8,77,1024
mvaa 64,24,140
ds 255,8,77,2048
v_mov_b32_e32 v170,v70
v_mov_b32_e32 v171,v71
cvaa 68,28,140,232
ex 3
mvaa 68,20,136
ex 4
mvaa 68,12,132
ds 255,8,77,3072
mvaa 68,4,128
ex 5
v_and_b32_e32 v248,2863311530,v248
cvaa 96,32,128,232
ex 6
mvaa 96,40,132
v_sub_f32_e32 v17,1.0,v17
v_rcp_f32_e32 v148,v148
mvaa 96,48,136
v_mul_f32_e32 v148,v148,v17
s_waitcnt vmcnt(21)
v_sub_f32_e32 v18,1.0,v18
v_sub_f32_e32 v19,1.0,v19
mvaa 96,56,140
v_mul_f32_e32 v200,v148,v200
v_mul_f32_e32 v201,v148,v201
v_mul_f32_e32 v202,v148,v202
cvaa 100,60,140,232
v_mul_f32_e32 v203,v148,v203
cv 200,200,201
mvaa 100,52,136
v_mul_f32_e32 v204,v148,v204
v_mul_f32_e32 v205,v148,v205
cv 201,202,203
mvaa 100,44,132
v_mul_f32_e32 v206,v148,v206
v_mul_f32_e32 v207,v148,v207
cv 202,204,205
mvaa 100,36,128
buffer_load_dwordx4 v[176:179],v244,s[8:11],s98 offen sc1
cv 203,206,207
cvaa 72,0,144,232
v_rcp_f32_e32 v152,v152
mvaa 72,8,148
v_mul_f32_e32 v152,v152,v18
v_mul_f32_e32 v208,v152,v208
v_mul_f32_e32 v209,v152,v209
mvaa 72,16,152
buffer_load_dwordx2 v[168:169],v253,s[8:11],s98 offen offset:1024 sc1
v_mul_f32_e32 v210,v152,v210
mvaa 72,24,156
v_mul_f32_e32 v211,v152,v211
cv 208,208,209
cv 209,210,211
cvaa 76,28,156,232
v_mul_f32_e32 v212,v152,v212
v_mul_f32_e32 v213,v152,v213
mvaa 76,20,152
buffer_load_dwordx2 v[172:173],v253,s[8:11],s98 offen offset:1536 sc1
v_mul_f32_e32 v214,v152,v214
mvaa 76,12,148
v_mul_f32_e32 v215,v152,v215
v_rcp_f32_e32 v156,v156
mvaa 76,4,144
cv 210,212,213
cv 211,214,215
v_mul_f32_e32 v156,v156,v19
cvaa 104,32,144,232
buffer_store_dwordx4 v[200:203],v244,s[8:11],s34 offen sc0 sc1
mvaa 104,40,148
v_mul_f32_e32 v216,v156,v216
v_mul_f32_e32 v217,v156,v217
v_mul_f32_e32 v218,v156,v218
mvaa 104,48,152
v_mul_f32_e32 v219,v156,v219
cv 216,216,217
cv 217,218,219
mvaa 104,56,156
buffer_store_dwordx4 v[208:211],v244,s[8:11],s50 offen sc0 sc1
v_mul_f32_e32 v220,v156,v220
cvaa 108,60,156,232
v_mul_f32_e32 v221,v156,v221
v_mul_f32_e32 v222,v156,v222
mvaa 108,52,152
v_mul_f32_e32 v223,v156,v223
cv 218,220,221
cv 219,222,223
mvaa 108,44,148
buffer_store_dwordx4 v[216:219],v244,s[8:11],s51 offen sc0 sc1
v_mov_b32_e32 v160,v120
mvaa 108,36,144
v_mov_b32_e32 v161,v121
ex 7
cvaa 88,0,176,232
ex 32
mvaa 88,8,180
v_mov_b32_e32 v162,v122
ex 33
mvaa 88,16,184
v_mov_b32_e32 v175,v95
ex 34
mvaa 88,24,188
s_cmp_ge_i32 s47,s46
s_cselect_b32 s76,s95,s46
s_cselect_b32 s77,s95,268435456
v_mov_b32_e32 v174,v94
ex 35
cvaa 92,28,188,232
s_add_u32 s77,s77,16384
ex 36
mvaa 92,20,184
v_mov_b32_e32 v163,v123
ex 37
mvaa 92,12,180
cv 0,0,1
ex 38
mvaa 92,4,176
cv 1,2,3
ex 39
cvaa 120,32,176,232
buffer_load_dword v247,v246,s[24:27],s49 offen
mvaa 120,40,180
s_add_u32 s49,s49,32768
cv 2,4,5
ex 8
mvaa 120,48,184
cv 3,6,7
ex 9
mvaa 120,56,188
buffer_load_dword v247,v245,s[36:39],s85 offen
cv 4,32,33
cvaa 124,60,188,232
s_add_u32 s85,s85,131072
ex 10
mvaa 124,52,184
cv 5,34,35
ex 11
mvaa 124,44,180
buffer_load_dword v247,v246,s[24:27],s49 offen
cv 6,36,37
mvaa 124,36,176
s_cmp_lg_u32 s19,0
s_cbranch_scc1 L_j0
s_nop 1
s_lshl_b32 s86,s12,12
s_add_u32 s86,s86,131072
v_add_u32_e32 v236,s86,v244
ra 160,236,0
ra 164,236,1024
ra 168,236,2048
ra 172,236,3072
ra 176,236,16384
ra 180,236,17408
ra 184,236,18432
ra 188,236,19456
v_add_u32_e32 v224,s94,v224
s_waitcnt lgkmcnt(0)
L_j0:
cv 7,38,39
ex 12
zaav 0,64,0
s_add_u32 s49,s49,32768
ex 13
maav 0,96,4
ex 14
v_or_b32_e32 v248,1145324612,v248
zaav 4,68,0
ex 15
v_mov_b64_e32 v[144:145],0
maav 4,100,4
cv 8,8,9
ex 40
zaav 8,72,0
ex 41
maav 8,104,4
cv 9,10,11
ex 42
zaav 12,76,0
cv 10,12,13
ex 43
maav 12,108,4
cv 11,14,15
ex 44
v_smfmac_f32_16x16x64_bf16 v[144:147],v[228:231],v[0:7],v248
ex 45
zaav 16,80,0
cv 12,40,41
ex 46
maav 16,112,4
s_waitcnt vmcnt(8)
v_max3_u32 v180,v176,v177,v178
cv 13,42,43
zaav 20,84,0
s_waitcnt vmcnt(7)
v_max3_u32 v180,v180,v179,v168
s_waitcnt vmcnt(6)
v_max3_u32 v180,v180,v169,v172
v_max_u32_e32 v180,v180,v173
v_cmp_eq_u32_e64 s[40:41],-1,v180
maav 20,116,4
s_cmp_lg_u64 s[40:41],0
s_cbranch_scc1 L_bx
L_bxr:
cv 14,44,45
ex 47
mvvv 160,176,192
zaav 24,88,0
cv 15,46,47
v_sub_f32_e32 v16,1.0,v16
maav 24,120,4
ex 24
zaav 28,92,0
ex 25
maav 28,124,4
v_max3_f32 v164,v160,v161,v164
v_max_f32_e32 v164,v164,v165
v_max3_f32 v166,v162,v163,v166
v_max_f32_e32 v166,v166,v167
v_max_f32_e32 v180,v164,v166
v_mov_b32_e32 v181,v180
zaav 32,64,8
ex 26
v_permlane16_swap_b32_e32 v180,v181
v_max_f32_e32 v180,v180,v181
v_mov_b32_e32 v181,v180
maav 32,96,12
ex 27
v_permlane32_swap_b32_e32 v180,v181
v_max_f32_e32 v180,v180,v181
v_mul_f32_e32 v180,0xbe8293ee,v180
v_fmamk_f32 v164,v164,0x3e8293ee,v180
zaav 36,68,8
ex 164
v_fmamk_f32 v166,v166,0x3e8293ee,v180
ex 166
cv 164,164,164
cv 166,166,166
maav 36,100,12
v_and_b32_e32 v160,v226,v164
v_and_b32_e32 v161,v227,v166
v_and_b32_e32 v162,v250,v164
v_and_b32_e32 v163,v252,v166
zaav 40,72,8
v_mov_b64_e32 v[148:149],0
zvvv 180,228,160
maav 40,104,12
ra 192,224,0
zvvv 184,168,160
zaav 44,76,8
ra 196,224,1024
zvvv 188,172,160
maav 44,108,12
ra 200,224,2048
v_rcp_f32_e32 v180,v180
cv 24,24,25
v_mul_f32_e32 v180,v180,v16
v_mul_f32_e32 v184,v180,v184
v_mul_f32_e32 v185,v180,v185
v_smfmac_f32_16x16x64_bf16 v[148:151],v[228:231],v[8:15],v248
v_mul_f32_e32 v186,v180,v186
v_mul_f32_e32 v187,v180,v187
v_mul_f32_e32 v188,v180,v188
v_mul_f32_e32 v189,v180,v189
v_mul_f32_e32 v190,v180,v190
v_mul_f32_e32 v191,v180,v191
zaav 48,80,8
cv 184,184,185
cv 185,186,187
cv 186,188,189
cv 187,190,191
cv 25,26,27
ex 28
maav 48,112,12
buffer_store_dwordx4 v[184:187],v244,s[8:11],s33 offen sc0 sc1
ex 29
ex 30
cv 26,28,29
zaav 52,84,8
ex 31
ex 56
cv 27,30,31
ex 57
maav 52,116,12
ex 58
ex 59
ex 60
zaav 56,88,8
ex 61
maav 56,120,12
cv 28,56,57
ex 62
zaav 60,92,8
ra 204,224,3072
ra 208,224,4096
ex 63
maav 60,124,12
ra 212,224,5120
cv 29,58,59
cv 30,60,61
cv 31,62,63
zvav 192,64,24
ra 216,224,6144
ra 220,224,7168
ex 64
mvav 192,96,28
ra 224,224,8192
v_mov_b32_e32 v251,-1
ex 65
zvav 196,68,24
ra 228,224,9216
ra 232,224,10240
ex 66
mvav 196,100,28
ra 236,224,11264
v_mov_b64_e32 v[152:153],0
ex 67
zvav 200,72,24
ra 240,224,12288
ra 244,224,13312
v_mov_b64_e32 v[156:157],0
cv 64,64,65
mvav 200,104,28
ra 248,224,14336
ra 252,224,15360
ex 68
zvav 204,76,24
ex 69
ex 70
mvav 204,108,28
cv 65,66,67
ex 71
cv 66,68,69
v_smfmac_f32_16x16x64_bf16 v[156:159],v[228:231],v[24:31],v248
ex 96
ex 97
zvav 208,80,24
ex 98
ex 99
mvav 208,112,28
cv 67,70,71
cv 68,96,97
ex 100
zvav 212,84,24
ex 101
ex 102
mvav 212,116,28
cv 69,98,99
ex 103
cv 70,100,101
zvav 216,88,24
ex 72
ex 73
mvav 216,120,28
ex 74
ex 75
zvav 220,92,24
ex 76
ex 77
mvav 220,124,28
v_mov_b32_e32 v250,-1
v_mov_b32_e32 v252,-1
v_mov_b32_e32 v253,-1
cv 71,102,103
s_mov_b64 exec,0
s_bitcmp1_b32 170,s20
s_cbranch_scc1 L_vbO
s_cmp_eq_u32 s12,0
s_cbranch_scc1 L_vs17
s_cmp_eq_u32 s12,1
s_cbranch_scc1 L_vs18
s_cmp_eq_u32 s12,2
s_cbranch_scc1 L_vs19
s_branch L_vs20
L_vbO:
s_cmp_eq_u32 s12,0
s_cbranch_scc1 L_vs24
s_cmp_eq_u32 s12,1
s_cbranch_scc1 L_vs25
s_cmp_eq_u32 s12,2
s_cbranch_scc1 L_vs26
s_branch L_vs27
L_vr1:
s_mov_b64 exec,-1
s_waitcnt lgkmcnt(0)
s_barrier
cvaa 0,192,128,232
mvaa 0,200,132
s_add_u32 s81,s81,1
ex 78
mvaa 0,208,136
s_add_u32 s44,s44,1024
ex 79
mvaa 0,216,140
cv 72,72,73
cv 73,74,75
cvaa 4,220,140,232
s_mov_b32 m0,s68
ex 104
mvaa 4,212,136
ds 249,8,76,0
mvaa 4,204,132
ex 105
mvaa 4,196,128
ex 106
cvaa 32,224,128,232
ra 64,225,32768
cv 74,76,77
mvaa 32,232,132
ex 107
mvaa 32,240,136
ra 68,225,33792
cv 75,78,79
mvaa 32,248,140
ex 108
cvaa 36,252,140,232
ds 249,8,76,1024
mvaa 36,244,136
ex 109
mvaa 36,236,132
ra 72,225,34816
cv 76,104,105
mvaa 36,228,128
ex 110
cvaa 8,192,144,232
ra 76,225,35840
cv 77,106,107
mvaa 8,200,148
ex 111
mvaa 8,208,152
ex 80
mvaa 8,216,156
ds 249,8,76,2048
cvaa 12,220,156,232
ra 80,225,36864
cv 78,108,109
mvaa 12,212,152
ex 81
mvaa 12,204,148
ra 84,225,37888
cv 79,110,111
mvaa 12,196,144
ex 82
cvaa 40,224,144,232
ex 83
mvaa 40,232,148
ra 88,225,38912
cv 80,80,81
mvaa 40,240,152
ds 249,8,76,3072
mvaa 40,248,156
ex 84
cvaa 44,252,156,232
ra 92,225,39936
cv 81,82,83
mvaa 44,244,152
ex 85
mvaa 44,236,148
ex 86
mvaa 44,228,144
ra 96,225,40960
cv 82,84,85
cvaa 16,192,160,232
s_mov_b32 m0,s69
ex 87
mvaa 16,200,164
ra 100,225,41984
cv 83,86,87
mvaa 16,208,168
ds 249,8,77,0
mvaa 16,216,172
ex 112
cvaa 20,220,172,232
ex 113
mvaa 20,212,168
ra 104,225,43008
cv 84,112,113
mvaa 20,204,164
ex 114
mvaa 20,196,160
ra 108,225,44032
cvaa 48,224,160,232
ex 115
mvaa 48,232,164
ds 249,8,77,1024
mvaa 48,240,168
ex 116
mvaa 48,248,172
ra 112,225,45056
cv 85,114,115
cvaa 52,252,172,232
ex 117
mvaa 52,244,168
L_vs16:
s_cbranch_execz L_vr1
ra 116,225,46080
cv 86,116,117
mvaa 52,236,164
ex 118
mvaa 52,228,160
ex 119
cvaa 24,192,176,232
ds 249,8,77,2048
mvaa 24,200,180
ra 120,225,47104
cv 87,118,119
mvaa 24,208,184
ex 88
mvaa 24,216,188
ra 124,225,48128
cvaa 28,220,188,232
mvaa 28,212,184
ex 89
mvaa 28,204,180
ex 90
mvaa 28,196,176
ds 249,8,77,3072
cvaa 56,224,176,232
ex 91
mvaa 56,232,180
ex 92
mvaa 56,240,184
ex 93
mvaa 56,248,188
ex 94
cvaa 60,252,188,232
mvaa 60,244,184
ex 95
mvaa 60,236,180
cv 88,88,89
cv 89,90,91
mvaa 60,228,176
s_waitcnt vmcnt(8) lgkmcnt(0)
s_barrier
maav 0,64,64
ex 120
maav 0,96,68
s_mov_b32 m0,s74
ex 121
maav 4,68,64
ds 255,8,78,0
maav 4,100,68
ex 122
maav 8,72,64
ex 123
maav 8,104,68
cv 90,92,93
cv 91,94,95
maav 12,76,64
ex 124
maav 12,108,68
ex 125
v_smfmac_f32_16x16x64_bf16 v[144:147],v[228:231],v[64:71],v248
cv 92,120,121
maav 16,80,64
ex 126
maav 16,112,68
buffer_load_dword v247,v245,s[36:39],s85 offen
maav 20,84,64
ex 127
maav 20,116,68
cv 93,122,123
cv 94,124,125
maav 24,88,64
cv 95,126,127
maav 24,120,68
ra 192,224,32768
maav 28,92,64
ds 255,8,78,1024
maav 28,124,68
s_lshl_b32 s86,s19,3
s_lshr_b32 s87,s18,2
s_add_u32 s86,s86,s87
maav 32,64,72
ra 196,224,33792
maav 32,96,76
s_add_u32 s85,s85,131072
maav 36,68,72
ra 200,224,34816
maav 36,100,76
s_lshl_b32 s86,s86,7
s_add_u32 s86,s86,83898368
v_mov_b32_e32 v238,1
maav 40,72,72
ds 255,8,78,2048
maav 40,104,76
s_cmp_eq_u32 s12,0
s_cselect_b64 exec,1,0
buffer_atomic_add v238,off,s[8:11],s86 sc1
s_mov_b64 exec,-1
maav 44,76,72
ra 204,224,35840
maav 44,108,76
v_smfmac_f32_16x16x64_bf16 v[148:151],v[228:231],v[72:79],v248
ra 208,224,36864
maav 48,80,72
maav 48,112,76
maav 52,84,72
ds 255,8,78,3072
maav 52,116,76
ra 212,224,37888
maav 56,88,72
ex 0
maav 56,120,76
ra 216,224,38912
maav 60,92,72
ex 1
maav 60,124,76
s_mov_b32 m0,s75
ex 2
zvav 160,64,80
ra 220,224,39936
cv 0,0,1
mvav 160,96,84
ds 255,8,79,0
zvav 164,68,80
ex 3
mvav 164,100,84
ra 224,224,40960
cv 1,2,3
zvav 168,72,80
ex 4
mvav 168,104,84
ra 228,224,41984
zvav 172,76,80
ds 255,8,79,1024
mvav 172,108,84
s_add_u32 s46,s46,16384
ex 5
v_smfmac_f32_16x16x64_bf16 v[152:155],v[228:231],v[80:87],v248
ra 232,224,43008
zvav 176,80,80
s_add_u32 s47,s47,32768
ex 6
mvav 176,112,84
s_add_u32 s95,s47,32768
ex 7
zvav 180,84,80
ra 236,224,44032
cv 2,4,5
mvav 180,116,84
ex 32
zvav 184,88,80
ds 255,8,79,2048
mvav 184,120,84
ex 33
zvav 188,92,80
ra 240,224,45056
cv 3,6,7
mvav 188,124,84
ex 34
mvav 192,64,88
ra 244,224,46080
cv 4,32,33
mvav 192,96,92
ex 35
mvav 196,68,88
ex 36
mvav 196,100,92
ds 255,8,79,3072
mvav 200,72,88
ra 248,224,47104
cv 5,34,35
mvav 200,104,92
s_cmp_ge_i32 s47,s46
ex 37
mvav 204,76,88
s_cselect_b32 s78,s95,s46
ex 38
mvav 204,108,92
s_cselect_b32 s79,s95,268435456
ex 39
v_smfmac_f32_16x16x64_bf16 v[156:159],v[228:231],v[88:95],v248
ra 252,224,48128
mvav 208,80,88
s_add_u32 s79,s79,16384
ex 8
mvav 208,112,92
ex 9
mvav 212,84,88
ex 10
mvav 212,116,92
ex 11
mvav 216,88,88
cv 6,36,37
cv 7,38,39
mvav 216,120,92
ex 12
mvav 220,92,88
ex 13
mvav 220,124,92
s_waitcnt vmcnt(16) lgkmcnt(0)
s_barrier
cvaa 64,192,128,232
s_bitcmp0_b32 s82,s81
s_cbranch_scc1 L_loop
s_bitcmp1_b32 s15,s81
s_cbranch_scc1 L_short
.p2align 6
L_loop:
mvaa 64,200,132
s_add_u32 s81,s81,1
ex 14
mvaa 64,208,136
s_add_u32 s44,s44,1024
ex 15
mvaa 64,216,140
cv 8,8,9
cv 9,10,11
cvaa 68,220,140,232
s_mov_b32 m0,s70
ex 40
mvaa 68,212,136
ds 249,8,78,0
mvaa 68,204,132
ex 41
mvaa 68,196,128
ex 42
cvaa 96,224,128,232
ra 64,225,0
cv 10,12,13
mvaa 96,232,132
ex 43
mvaa 96,240,136
ra 68,225,1024
cv 11,14,15
mvaa 96,248,140
ex 44
cvaa 100,252,140,232
ds 249,8,78,1024
mvaa 100,244,136
ex 45
mvaa 100,236,132
ra 72,225,2048
cv 12,40,41
mvaa 100,228,128
ex 46
cvaa 72,192,144,232
ra 76,225,3072
cv 13,42,43
mvaa 72,200,148
ex 47
mvaa 72,208,152
ex 16
mvaa 72,216,156
ds 249,8,78,2048
cvaa 76,220,156,232
ra 80,225,4096
cv 14,44,45
mvaa 76,212,152
ex 17
mvaa 76,204,148
ra 84,225,5120
cv 15,46,47
mvaa 76,196,144
ex 18
cvaa 104,224,144,232
ex 19
mvaa 104,232,148
ra 88,225,6144
cv 16,16,17
mvaa 104,240,152
ds 249,8,78,3072
mvaa 104,248,156
ex 20
cvaa 108,252,156,232
ra 92,225,7168
cv 17,18,19
mvaa 108,244,152
ex 21
mvaa 108,236,148
ex 22
mvaa 108,228,144
ra 96,225,8192
cv 18,20,21
cvaa 80,192,160,232
s_mov_b32 m0,s71
ex 23
mvaa 80,200,164
ra 100,225,9216
cv 19,22,23
mvaa 80,208,168
ds 249,8,79,0
mvaa 80,216,172
ex 48
cvaa 84,220,172,232
ex 49
mvaa 84,212,168
ra 104,225,10240
cv 20,48,49
mvaa 84,204,164
ex 50
mvaa 84,196,160
ra 108,225,11264
cvaa 112,224,160,232
ex 51
mvaa 112,232,164
ds 249,8,79,1024
mvaa 112,240,168
ex 52
mvaa 112,248,172
ra 112,225,12288
cv 21,50,51
cvaa 116,252,172,232
ex 53
mvaa 116,244,168
ra 116,225,13312
cv 22,52,53
mvaa 116,236,164
ex 54
mvaa 116,228,160
ex 55
cvaa 88,192,176,232
ds 249,8,79,2048
mvaa 88,200,180
ra 120,225,14336
cv 23,54,55
mvaa 88,208,184
ex 24
mvaa 88,216,188
ra 124,225,15360
cvaa 92,220,188,232
buffer_store_dwordx4 v[250:253],v244,s[8:11],s44 offen offset:1024 sc0 sc1
mvaa 92,212,184
ex 25
mvaa 92,204,180
ex 26
mvaa 92,196,176
ds 249,8,79,3072
cvaa 120,224,176,232
ex 27
mvaa 120,232,180
ex 28
mvaa 120,240,184
ex 29
mvaa 120,248,188
ex 30
cvaa 124,252,188,232
buffer_store_dwordx4 v[250:253],v244,s[8:11],s44 offen sc0 sc1
mvaa 124,244,184
ex 31
mvaa 124,236,180
cv 24,24,25
cv 25,26,27
mvaa 124,228,176
s_waitcnt vmcnt(19) lgkmcnt(0)
s_barrier
maav 0,64,0
ex 56
maav 0,96,4
s_mov_b32 m0,s72
ex 57
maav 4,68,0
ds 255,8,76,0
maav 4,100,4
ex 58
maav 8,72,0
ex 59
maav 8,104,4
cv 26,28,29
cv 27,30,31
maav 12,76,0
s_cmp_lg_u32 s80,s81
ex 60
maav 12,108,4
ex 61
v_smfmac_f32_16x16x64_bf16 v[144:147],v[228:231],v[0:7],v248
cv 28,56,57
maav 16,80,0
ex 62
maav 16,112,4
buffer_load_dword v247,v246,s[24:27],s49 offen
maav 20,84,0
ex 63
maav 20,116,4
cv 29,58,59
cv 30,60,61
maav 24,88,0
s_cbranch_scc1 L_evb_r
v_add_u32_e32 v224,s94,v224
s_lshl_b32 s86,s12,12
s_add_u32 s86,s86,131072
v_add_u32_e32 v236,s86,v244
ra 160,236,0
ra 164,236,1024
ra 168,236,2048
ra 172,236,3072
ra 176,236,16384
ra 180,236,17408
ra 184,236,18432
ra 188,236,19456
L_evb_r:
cv 31,62,63
maav 24,120,4
ra 192,224,0
maav 28,92,0
ds 255,8,76,1024
maav 28,124,4
maav 32,64,8
ra 196,224,1024
maav 32,96,12
s_add_u32 s49,s49,32768
maav 36,68,8
ra 200,224,2048
maav 36,100,12
maav 40,72,8
ds 255,8,76,2048
maav 40,104,12
maav 44,76,8
ra 204,224,3072
maav 44,108,12
v_smfmac_f32_16x16x64_bf16 v[148:151],v[228:231],v[8:15],v248
ra 208,224,4096
maav 48,80,8
maav 48,112,12
maav 52,84,8
ds 255,8,76,3072
maav 52,116,12
ra 212,224,5120
maav 56,88,8
ex 64
maav 56,120,12
ra 216,224,6144
maav 60,92,8
ex 65
maav 60,124,12
s_mov_b32 m0,s73
ex 66
mvav 160,64,16
ra 220,224,7168
cv 64,64,65
mvav 160,96,20
ds 255,8,77,0
mvav 164,68,16
ex 67
mvav 164,100,20
ra 224,224,8192
cv 65,66,67
mvav 168,72,16
ex 68
mvav 168,104,20
ra 228,224,9216
mvav 172,76,16
ds 255,8,77,1024
mvav 172,108,20
s_add_u32 s46,s46,16384
ex 69
v_smfmac_f32_16x16x64_bf16 v[152:155],v[228:231],v[16:23],v248
ra 232,224,10240
mvav 176,80,16
s_add_u32 s47,s47,32768
ex 70
mvav 176,112,20
s_add_u32 s95,s47,32768
ex 71
mvav 180,84,16
ra 236,224,11264
cv 66,68,69
mvav 180,116,20
ex 96
mvav 184,88,16
ds 255,8,77,2048
mvav 184,120,20
ex 97
mvav 188,92,16
ra 240,224,12288
cv 67,70,71
mvav 188,124,20
ex 98
mvav 192,64,24
ra 244,224,13312
cv 68,96,97
mvav 192,96,28
ex 99
mvav 196,68,24
ex 100
mvav 196,100,28
ds 255,8,77,3072
mvav 200,72,24
ra 248,224,14336
cv 69,98,99
mvav 200,104,28
s_cmp_ge_i32 s47,s46
ex 101
mvav 204,76,24
s_cselect_b32 s76,s95,s46
ex 102
mvav 204,108,28
s_cselect_b32 s77,s95,268435456
ex 103
v_smfmac_f32_16x16x64_bf16 v[156:159],v[228:231],v[24:31],v248
ra 252,224,15360
mvav 208,80,24
s_add_u32 s77,s77,16384
ex 72
mvav 208,112,28
ex 73
mvav 212,84,24
ex 74
)"
R"(mvav 212,116,28
ex 75
mvav 216,88,24
cv 70,100,101
cv 71,102,103
mvav 216,120,28
ex 76
mvav 220,92,24
ex 77
mvav 220,124,28
s_waitcnt vmcnt(19) lgkmcnt(0)
s_barrier
cvaa 0,192,128,232
mvaa 0,200,132
s_add_u32 s81,s81,1
ex 78
mvaa 0,208,136
s_add_u32 s44,s44,1024
ex 79
mvaa 0,216,140
cv 72,72,73
cv 73,74,75
cvaa 4,220,140,232
s_mov_b32 m0,s68
ex 104
mvaa 4,212,136
ds 249,8,76,0
mvaa 4,204,132
ex 105
mvaa 4,196,128
ex 106
cvaa 32,224,128,232
ra 64,225,32768
cv 74,76,77
mvaa 32,232,132
ex 107
mvaa 32,240,136
ra 68,225,33792
cv 75,78,79
mvaa 32,248,140
ex 108
cvaa 36,252,140,232
ds 249,8,76,1024
mvaa 36,244,136
ex 109
mvaa 36,236,132
ra 72,225,34816
cv 76,104,105
mvaa 36,228,128
ex 110
cvaa 8,192,144,232
ra 76,225,35840
cv 77,106,107
mvaa 8,200,148
ex 111
mvaa 8,208,152
ex 80
mvaa 8,216,156
ds 249,8,76,2048
cvaa 12,220,156,232
ra 80,225,36864
cv 78,108,109
mvaa 12,212,152
ex 81
mvaa 12,204,148
ra 84,225,37888
cv 79,110,111
mvaa 12,196,144
ex 82
cvaa 40,224,144,232
ex 83
mvaa 40,232,148
ra 88,225,38912
cv 80,80,81
mvaa 40,240,152
ds 249,8,76,3072
mvaa 40,248,156
ex 84
cvaa 44,252,156,232
ra 92,225,39936
cv 81,82,83
mvaa 44,244,152
ex 85
mvaa 44,236,148
ex 86
mvaa 44,228,144
ra 96,225,40960
cv 82,84,85
cvaa 16,192,160,232
s_mov_b32 m0,s69
ex 87
mvaa 16,200,164
ra 100,225,41984
cv 83,86,87
mvaa 16,208,168
ds 249,8,77,0
mvaa 16,216,172
ex 112
cvaa 20,220,172,232
ex 113
mvaa 20,212,168
ra 104,225,43008
cv 84,112,113
mvaa 20,204,164
ex 114
mvaa 20,196,160
ra 108,225,44032
cvaa 48,224,160,232
ex 115
mvaa 48,232,164
ds 249,8,77,1024
mvaa 48,240,168
ex 116
mvaa 48,248,172
ra 112,225,45056
cv 85,114,115
cvaa 52,252,172,232
ex 117
mvaa 52,244,168
ra 116,225,46080
cv 86,116,117
mvaa 52,236,164
ex 118
mvaa 52,228,160
ex 119
cvaa 24,192,176,232
ds 249,8,77,2048
mvaa 24,200,180
ra 120,225,47104
cv 87,118,119
mvaa 24,208,184
ex 88
mvaa 24,216,188
ra 124,225,48128
cvaa 28,220,188,232
mvaa 28,212,184
ex 89
mvaa 28,204,180
ex 90
mvaa 28,196,176
ds 249,8,77,3072
cvaa 56,224,176,232
ex 91
mvaa 56,232,180
ex 92
mvaa 56,240,184
ex 93
mvaa 56,248,188
ex 94
cvaa 60,252,188,232
mvaa 60,244,184
ex 95
mvaa 60,236,180
cv 88,88,89
cv 89,90,91
mvaa 60,228,176
s_waitcnt vmcnt(18) lgkmcnt(0)
s_barrier
maav 0,64,64
ex 120
maav 0,96,68
s_mov_b32 m0,s74
ex 121
maav 4,68,64
ds 255,8,78,0
maav 4,100,68
ex 122
maav 8,72,64
ex 123
maav 8,104,68
cv 90,92,93
cv 91,94,95
maav 12,76,64
ex 124
maav 12,108,68
ex 125
v_smfmac_f32_16x16x64_bf16 v[144:147],v[228:231],v[64:71],v248
cv 92,120,121
maav 16,80,64
ex 126
maav 16,112,68
buffer_load_dword v247,v245,s[36:39],s85 offen
maav 20,84,64
ex 127
maav 20,116,68
cv 93,122,123
cv 94,124,125
maav 24,88,64
cv 95,126,127
maav 24,120,68
ra 192,224,32768
maav 28,92,64
ds 255,8,78,1024
maav 28,124,68
maav 32,64,72
ra 196,224,33792
maav 32,96,76
s_add_u32 s85,s85,131072
maav 36,68,72
ra 200,224,34816
maav 36,100,76
maav 40,72,72
ds 255,8,78,2048
maav 40,104,76
maav 44,76,72
ra 204,224,35840
maav 44,108,76
v_smfmac_f32_16x16x64_bf16 v[148:151],v[228:231],v[72:79],v248
ra 208,224,36864
maav 48,80,72
maav 48,112,76
maav 52,84,72
ds 255,8,78,3072
maav 52,116,76
ra 212,224,37888
maav 56,88,72
ex 0
maav 56,120,76
ra 216,224,38912
maav 60,92,72
ex 1
maav 60,124,76
s_mov_b32 m0,s75
ex 2
mvav 160,64,80
ra 220,224,39936
cv 0,0,1
mvav 160,96,84
ds 255,8,79,0
mvav 164,68,80
ex 3
mvav 164,100,84
ra 224,224,40960
cv 1,2,3
mvav 168,72,80
ex 4
mvav 168,104,84
ra 228,224,41984
mvav 172,76,80
ds 255,8,79,1024
mvav 172,108,84
s_add_u32 s46,s46,16384
ex 5
v_smfmac_f32_16x16x64_bf16 v[152:155],v[228:231],v[80:87],v248
ra 232,224,43008
mvav 176,80,80
s_add_u32 s47,s47,32768
ex 6
mvav 176,112,84
s_add_u32 s95,s47,32768
ex 7
mvav 180,84,80
ra 236,224,44032
cv 2,4,5
mvav 180,116,84
ex 32
mvav 184,88,80
ds 255,8,79,2048
mvav 184,120,84
ex 33
mvav 188,92,80
ra 240,224,45056
cv 3,6,7
mvav 188,124,84
ex 34
mvav 192,64,88
ra 244,224,46080
cv 4,32,33
mvav 192,96,92
ex 35
mvav 196,68,88
ex 36
mvav 196,100,92
ds 255,8,79,3072
mvav 200,72,88
ra 248,224,47104
cv 5,34,35
mvav 200,104,92
s_cmp_ge_i32 s47,s46
ex 37
mvav 204,76,88
s_cselect_b32 s78,s95,s46
ex 38
mvav 204,108,92
s_cselect_b32 s79,s95,268435456
ex 39
v_smfmac_f32_16x16x64_bf16 v[156:159],v[228:231],v[88:95],v248
ra 252,224,48128
mvav 208,80,88
s_add_u32 s79,s79,16384
ex 8
mvav 208,112,92
ex 9
mvav 212,84,88
ex 10
mvav 212,116,92
ex 11
mvav 216,88,88
cv 6,36,37
cv 7,38,39
mvav 216,120,92
ex 12
mvav 220,92,88
ex 13
mvav 220,124,92
s_waitcnt vmcnt(17) lgkmcnt(0)
s_barrier
cvaa 64,192,128,232
s_bitcmp0_b32 s82,s81
s_cbranch_scc1 L_loop
s_bitcmp0_b32 s15,s81
s_cbranch_scc1 L_eva_r
L_short:
v_add_f32_e32 v226,v152,v153
v_add_f32_e32 v227,v156,v157
v_subrev_u32_e32 v236,0x23800000,v226
v_subrev_u32_e32 v237,0x23800000,v227
v_max_u32_e32 v236,v236,v237
v_cmp_lt_u32_e64 s[40:41],s48,v236
v_rcp_f32_e32 v226,v226
v_rcp_f32_e32 v227,v227
s_add_u32 s86,s13,4096
v_mov_b32_e32 v236,0
v_mov_b32_e32 v237,0
v_mov_b32_e32 v238,0
v_mov_b32_e32 v239,0
v_mov_b64_e32 v[152:153],0
v_mov_b64_e32 v[156:157],0
v_mul_f32_e32 v226,v242,v226
v_mul_f32_e32 v227,v243,v227
v_add_u32_e32 v225,s94,v225
v_pk_mul_f32 v[160:161],v[160:161],v[226:227] op_sel:[0,0] op_sel_hi:[1,0]
v_pk_mul_f32 v[162:163],v[162:163],v[226:227] op_sel:[0,0] op_sel_hi:[1,0]
v_pk_mul_f32 v[164:165],v[164:165],v[226:227] op_sel:[0,0] op_sel_hi:[1,0]
v_pk_mul_f32 v[166:167],v[166:167],v[226:227] op_sel:[0,0] op_sel_hi:[1,0]
cv 160,160,161
cv 161,162,163
cv 162,164,165
cv 163,166,167
buffer_store_dwordx4 v[160:163],v244,s[8:11],s13 offen offset:0 sc0 sc1
v_pk_mul_f32 v[168:169],v[168:169],v[226:227] op_sel:[0,0] op_sel_hi:[1,0]
v_pk_mul_f32 v[170:171],v[170:171],v[226:227] op_sel:[0,0] op_sel_hi:[1,0]
v_pk_mul_f32 v[172:173],v[172:173],v[226:227] op_sel:[0,0] op_sel_hi:[1,0]
v_pk_mul_f32 v[174:175],v[174:175],v[226:227] op_sel:[0,0] op_sel_hi:[1,0]
cv 168,168,169
cv 169,170,171
cv 170,172,173
cv 171,174,175
buffer_store_dwordx4 v[168:171],v244,s[8:11],s13 offen offset:2048 sc0 sc1
v_pk_mul_f32 v[176:177],v[176:177],v[226:227] op_sel:[0,0] op_sel_hi:[1,0]
v_pk_mul_f32 v[178:179],v[178:179],v[226:227] op_sel:[0,0] op_sel_hi:[1,0]
v_pk_mul_f32 v[180:181],v[180:181],v[226:227] op_sel:[0,0] op_sel_hi:[1,0]
v_pk_mul_f32 v[182:183],v[182:183],v[226:227] op_sel:[0,0] op_sel_hi:[1,0]
cv 176,176,177
cv 177,178,179
cv 178,180,181
v_mfma_f32_32x32x16_bf16 v[160:175],v[236:239],v[236:239],0
cv 179,182,183
buffer_store_dwordx4 v[176:179],v244,s[8:11],s86 offen offset:0 sc0 sc1
v_pk_mul_f32 v[184:185],v[184:185],v[226:227] op_sel:[0,0] op_sel_hi:[1,0]
v_pk_mul_f32 v[186:187],v[186:187],v[226:227] op_sel:[0,0] op_sel_hi:[1,0]
v_pk_mul_f32 v[188:189],v[188:189],v[226:227] op_sel:[0,0] op_sel_hi:[1,0]
v_pk_mul_f32 v[190:191],v[190:191],v[226:227] op_sel:[0,0] op_sel_hi:[1,0]
cv 184,184,185
cv 185,186,187
cv 186,188,189
cv 187,190,191
buffer_store_dwordx4 v[184:187],v244,s[8:11],s86 offen offset:2048 sc0 sc1
v_pk_mul_f32 v[192:193],v[192:193],v[226:227] op_sel:[0,1] op_sel_hi:[1,1]
v_pk_mul_f32 v[194:195],v[194:195],v[226:227] op_sel:[0,1] op_sel_hi:[1,1]
v_pk_mul_f32 v[196:197],v[196:197],v[226:227] op_sel:[0,1] op_sel_hi:[1,1]
v_pk_mul_f32 v[198:199],v[198:199],v[226:227] op_sel:[0,1] op_sel_hi:[1,1]
cv 192,192,193
cv 193,194,195
cv 194,196,197
v_mfma_f32_32x32x16_bf16 v[176:191],v[236:239],v[236:239],0
cv 195,198,199
buffer_store_dwordx4 v[192:195],v244,s[8:11],s13 offen offset:1024 sc0 sc1
v_pk_mul_f32 v[200:201],v[200:201],v[226:227] op_sel:[0,1] op_sel_hi:[1,1]
v_pk_mul_f32 v[202:203],v[202:203],v[226:227] op_sel:[0,1] op_sel_hi:[1,1]
v_pk_mul_f32 v[204:205],v[204:205],v[226:227] op_sel:[0,1] op_sel_hi:[1,1]
v_pk_mul_f32 v[206:207],v[206:207],v[226:227] op_sel:[0,1] op_sel_hi:[1,1]
cv 200,200,201
cv 201,202,203
cv 202,204,205
cv 203,206,207
buffer_store_dwordx4 v[200:203],v244,s[8:11],s13 offen offset:3072 sc0 sc1
v_pk_mul_f32 v[208:209],v[208:209],v[226:227] op_sel:[0,1] op_sel_hi:[1,1]
v_pk_mul_f32 v[210:211],v[210:211],v[226:227] op_sel:[0,1] op_sel_hi:[1,1]
v_pk_mul_f32 v[212:213],v[212:213],v[226:227] op_sel:[0,1] op_sel_hi:[1,1]
v_pk_mul_f32 v[214:215],v[214:215],v[226:227] op_sel:[0,1] op_sel_hi:[1,1]
cv 208,208,209
cv 209,210,211
cv 210,212,213
v_mfma_f32_32x32x16_bf16 v[192:207],v[236:239],v[236:239],0
cv 211,214,215
buffer_store_dwordx4 v[208:211],v244,s[8:11],s86 offen offset:1024 sc0 sc1
v_pk_mul_f32 v[216:217],v[216:217],v[226:227] op_sel:[0,1] op_sel_hi:[1,1]
v_pk_mul_f32 v[218:219],v[218:219],v[226:227] op_sel:[0,1] op_sel_hi:[1,1]
v_pk_mul_f32 v[220:221],v[220:221],v[226:227] op_sel:[0,1] op_sel_hi:[1,1]
v_pk_mul_f32 v[222:223],v[222:223],v[226:227] op_sel:[0,1] op_sel_hi:[1,1]
cv 216,216,217
cv 217,218,219
cv 218,220,221
cv 219,222,223
buffer_store_dwordx4 v[216:219],v244,s[8:11],s86 offen offset:3072 sc0 sc1
s_nop 1
v_mfma_f32_32x32x16_bf16 v[208:223],v[236:239],v[236:239],0
L_eva_r:
s_cmp_lg_u32 s81,14
s_cbranch_scc1 L_loop
s_add_u32 s81,s81,1
s_bitcmp1_b32 170,s20
s_cbranch_scc1 L_odd
L_last:
s_lshl_b32 s86,s21,2
s_sub_u32 s87,s13,s67
s_add_u32 s86,s86,s12
mvaa 64,200,132
s_lshl_b32 s86,s86,8
v_add_u32_e32 v252,s87,v244
s_add_u32 s86,s86,L_last-L_att_pc+1024
v_lshl_add_u32 v226,v244,3,s86
s_lshl_b32 s86,s12,1
s_bfe_u32 s87,s21,65539
mvaa 64,208,136
s_add_u32 s86,s86,s87
v_min_u32_e32 v226,L_att_end-L_att_pc-4,v226
s_lshl_b32 s86,s86,7
s_add_u32 s86,s86,L_pend2-L_att_pc
v_writelane_b32 v226,s86,2
mvaa 64,216,140
s_mov_b64 exec,7
global_load_dword v253,v226,s[96:97]
s_mov_b64 exec,-1
cvaa 68,220,140,232
s_add_u32 m0,s52,32768
v_and_b32_e32 v224,16,v244
v_lshlrev_b32_e32 v224,5,v224
mvaa 68,212,136
s_xor_b32 s51,s62,0
v_lshrrev_b32_e32 v239,4,v244
v_lshrrev_b32_e32 v236,3,v239
mvaa 68,204,132
s_lshl_b32 s87,s12,13
v_and_b32_e32 v237,112,v244
v_and_b32_e32 v238,48,v239
mvaa 68,196,128
s_xor_b32 s87,s87,8192
ra 64,225,0
v_lshlrev_b32_e32 v238,1,v238
cvaa 96,224,128,232
buffer_load_dword v253,v224,s[8:11],s53 offen sc1
mvaa 96,232,132
s_mov_b32 s84,6
ra 68,225,1024
v_xor_b32_e32 v237,v237,v238
mvaa 96,240,136
s_add_u32 s81,s81,1
ra 72,225,2048
v_lshl_add_u32 v245,v236,11,v237
mvaa 96,248,140
s_mov_b32 s84,4
v_add_u32_e32 v246,15360,v245
v_add_u32_e32 v249,30720,v245
cvaa 100,252,140,232
s_andn2_b32 s69,64,s54
ra 76,225,3072
s_waitcnt vmcnt(9)
v_add_u32_e32 v247,46080,v245
mvaa 100,244,136
dl 245,24,51,0
mvaa 100,236,132
s_add_u32 s88,s14,4096
ra 80,225,4096
cv 8,8,9
mvaa 100,228,128
s_lshl_b32 s35,s19,1
ra 84,225,5120
cv 9,10,11
cvaa 72,192,144,232
s_lshr_b32 s33,40569,s35
ra 88,225,6144
cv 10,12,13
mvaa 72,200,148
s_and_b32 s33,s33,3
ra 92,225,7168
v_add_u32_e32 v250,4096,v252
mvaa 72,208,152
dl 246,24,51,1024
mvaa 72,216,156
s_lshl_b32 s33,s33,13
ra 96,225,8192
v_mov_b32_e32 v251,4
cvaa 76,220,156,232
s_lshr_b32 s34,59294,s35
ra 100,225,9216
s_and_b32 s34,s34,3
mvaa 76,212,152
s_lshl_b32 s34,s34,13
ra 104,225,10240
s_xor_b32 s35,s33,s34
mvaa 76,204,148
s_and_b32 s98,s19,3
ra 108,225,11264
s_lshl_b32 s98,s98,13
mvaa 76,196,144
dl 249,24,51,2048
cvaa 104,224,144,232
s_mov_b32 s74,0x300003
ra 112,225,12288
s_mov_b32 s75,0x30000300
mvaa 104,232,148
s_lshl_b32 s71,s12,15
ra 116,225,13312
s_add_u32 s71,s71,s54
mvaa 104,240,152
s_mov_b64 s[44:45],s[30:31]
ra 120,225,14336
s_mov_b32 s46,4194304
mvaa 104,248,156
s_mov_b32 s47,131072
ex 14
cvaa 108,252,156,232
dl 247,24,51,3072
mvaa 108,244,152
s_add_u32 m0,s52,49152
s_xor_b32 s51,s63,0
ra 124,225,15360
mvaa 108,236,148
s_lshl_b32 s70,s23,11
ex 15
mvaa 108,228,144
s_add_u32 s70,s70,s71
ex 40
cvaa 80,192,160,232
ex 41
mvaa 80,200,164
dl 245,24,51,0
mvaa 80,208,168
ex 42
mvaa 80,216,172
ex 43
cvaa 84,220,172,232
ex 44
mvaa 84,212,168
ex 45
mvaa 84,204,164
dl 246,24,51,1024
mvaa 84,196,160
ex 46
cvaa 112,224,160,232
ex 47
mvaa 112,232,164
ex 16
mvaa 112,240,168
ex 17
mvaa 112,248,172
dl 249,24,51,2048
cvaa 116,252,172,232
ex 18
mvaa 116,244,168
ex 19
mvaa 116,236,164
ex 20
mvaa 116,228,160
ex 21
cvaa 88,192,176,232
dl 247,24,51,3072
s_waitcnt vmcnt(8)
v_cmp_ne_u32_e32 vcc,4,v253
mvaa 88,200,180
s_cbranch_vccnz L_spbe
L_sprbe:
ex 22
mvaa 88,208,184
s_xor_b32 s50,s66,262144
ex 23
mvaa 88,216,188
ex 48
cvaa 92,220,188,232
ex 49
mvaa 92,212,184
la 128,252,50,0
mvaa 92,204,180
s_waitcnt lgkmcnt(0)
s_barrier
mvaa 92,196,176
s_mov_b32 m0,s72
ex 50
cvaa 120,224,176,232
s_lshl_b32 s72,s22,11
ex 51
mvaa 120,232,180
s_add_u32 s72,s72,s71
ex 52
mvaa 120,240,184
ds 255,8,76,0
mvaa 120,248,188
s_add_u32 s71,s70,131072
ex 53
cvaa 124,252,188,232
ex 54
mvaa 124,244,184
ex 55
mvaa 124,236,180
ex 24
mvaa 124,228,176
ds 255,8,76,1024
maav 0,64,0
s_cmp_lg_u32 s19,7
s_cbranch_scc1 L_j7e
s_nop 1
s_lshl_b32 s86,s12,12
s_add_u32 s86,s86,131072
v_add_u32_e32 v236,s86,v244
ra 160,236,0
ra 164,236,1024
ra 168,236,2048
ra 172,236,3072
ra 176,236,16384
ra 180,236,17408
ra 184,236,18432
ra 188,236,19456
v_add_u32_e32 v224,s94,v224
s_waitcnt lgkmcnt(0)
L_j7e:
ex 25
maav 0,96,4
ex 26
maav 4,68,0
ex 27
maav 4,100,4
cv 11,14,15
cv 12,40,41
maav 8,72,0
ds 255,8,76,2048
maav 8,104,4
cv 13,42,43
cv 14,44,45
maav 12,76,0
cv 15,46,47
cv 16,16,17
maav 12,108,4
ex 28
v_smfmac_f32_16x16x64_bf16 v[144:147],v[228:231],v[0:7],v248
cv 17,18,19
maav 16,80,0
ds 255,8,76,3072
maav 16,112,4
s_mov_b32 m0,s73
ex 29
maav 20,84,0
s_add_u32 s73,s72,131072
ex 30
maav 20,116,4
ex 31
maav 24,88,0
ex 56
maav 24,120,4
ds 255,8,77,0
maav 28,92,0
ex 57
maav 28,124,4
ex 58
maav 32,64,8
ex 59
maav 32,96,12
ex 60
maav 36,68,8
ds 255,8,77,1024
maav 36,100,12
cv 18,20,21
cv 19,22,23
maav 40,72,8
cv 20,48,49
cv 21,50,51
maav 40,104,12
cv 22,52,53
cv 23,54,55
maav 44,76,8
ex 61
maav 44,108,12
ds 255,8,77,2048
v_smfmac_f32_16x16x64_bf16 v[148:151],v[228:231],v[8:15],v248
cv 24,24,25
maav 48,80,8
ex 62
maav 48,112,12
ex 63
maav 52,84,8
ra 192,244,0
cv 25,26,27
maav 52,116,12
L_vs17:
s_cbranch_execz L_vs21
ds 255,8,77,3072
maav 56,88,8
cv 26,28,29
cv 27,30,31
maav 56,120,12
cv 28,56,57
cv 29,58,59
maav 60,92,8
cv 30,60,61
cv 31,62,63
maav 60,124,12
ex 64
mvav 160,64,16
la 132,252,50,2048
mvav 160,96,20
ex 65
mvav 164,68,16
ex 66
mvav 164,100,20
ex 67
mvav 168,72,16
ex 68
mvav 168,104,20
la 136,250,50,0
mvav 172,76,16
ex 69
mvav 172,108,20
ex 70
v_smfmac_f32_16x16x64_bf16 v[152:155],v[228:231],v[16:23],v248
ra 200,244,2048
mvav 176,80,16
ra 208,244,4096
cv 64,64,65
mvav 176,112,20
la 140,250,50,2048
mvav 180,84,16
ra 216,244,6144
cv 65,66,67
mvav 180,116,20
ex 71
mvav 184,88,16
ex 96
mvav 184,120,20
s_barrier
ex 97
mvav 188,92,16
la 156,250,50,3072
mvav 188,124,20
s_add_u32 m0,s52,131072
ra 196,244,1024
s_xor_b32 s51,s62,1048576
mvav 192,64,24
ra 204,244,3072
cv 66,68,69
mvav 192,96,28
ra 212,244,5120
cv 67,70,71
mvav 196,68,24
ra 220,244,7168
cv 68,96,97
mvav 196,100,28
la 152,250,50,1024
mvav 200,72,24
ex 98
mvav 200,104,28
ex 99
mvav 204,76,24
ra 224,244,8192
cv 69,98,99
mvav 204,108,28
ra 232,244,10240
v_add_u32_e32 v6,s87,v244
v_smfmac_f32_16x16x64_bf16 v[156:159],v[228:231],v[24:31],v248
ra 240,244,12288
mvav 208,80,24
la 148,252,50,3072
mvav 208,112,28
s_xor_b32 s87,s52,4096
ra 248,244,14336
v_add_u32_e32 v7,98304,v6
mvav 212,84,24
s_add_u32 s87,s87,150528
ra 252,244,15360
v_add_u32_e32 v5,65536,v244
mvav 212,116,28
ra 244,244,13312
v_add_u32_e32 v32,s87,v244
mvav 216,88,24
ra 228,244,9216
v_and_b32_e32 v224,960,v244
mvav 216,120,28
la 144,252,50,1024
mvav 220,92,24
s_add_u32 s50,s50,s55
ra 236,244,11264
mvav 220,124,28
ra 64,225,32768
s_waitcnt lgkmcnt(8)
cvaa 24,192,176,232
dl 245,24,51,0
mvaa 24,200,180
ra 96,225,40960
mvaa 24,208,184
ra 68,225,33792
mvaa 24,216,188
dl 246,24,51,1024
cvaa 16,192,160,128
ra 100,225,41984
mvaa 16,200,164
ra 72,225,34816
mvaa 16,208,168
dl 249,24,51,2048
mvaa 16,216,172
ra 104,225,43008
cvaa 28,196,176,232
ra 76,225,35840
mvaa 28,204,180
dl 247,24,51,3072
mvaa 28,212,184
s_add_u32 m0,s52,147456
s_xor_b32 s51,s63,1048576
ra 108,225,44032
mvaa 28,220,188
ra 80,225,36864
cvaa 20,196,160,132
dl 245,24,51,0
mvaa 20,204,164
ra 112,225,45056
mvaa 20,212,168
ra 192,244,16384
mvaa 20,220,172
dl 246,24,51,1024
cvaa 56,224,176,232
ra 200,244,18432
s_waitcnt lgkmcnt(7)
mvaa 56,232,180
ra 208,244,20480
mvaa 56,240,184
dl 249,24,51,2048
mvaa 56,248,188
ra 216,244,22528
cvaa 48,224,160,136
ra 196,244,17408
mvaa 48,232,164
ra 204,244,19456
mvaa 48,240,168
ra 212,244,21504
mvaa 48,248,172
ra 220,244,23552
cvaa 60,228,176,232
ra 224,244,24576
mvaa 60,236,180
ra 232,244,26624
mvaa 60,244,184
ra 240,244,28672
mvaa 60,252,188
ra 248,244,30720
cvaa 52,228,160,140
ra 84,225,37888
mvaa 52,236,164
ra 116,225,46080
mvaa 52,244,168
la 160,252,50,0
ra 228,244,25600
mvaa 52,252,172
ra 236,244,27648
s_waitcnt lgkmcnt(10)
cvaa 8,192,176,128
ra 244,244,29696
mvaa 8,200,180
la 164,252,50,2048
ra 252,244,31744
mvaa 8,208,184
ra 88,225,38912
mvaa 8,216,188
ra 120,225,47104
cvaa 12,196,176,132
la 168,250,50,0
mvaa 12,204,180
ra 92,225,39936
ra 124,225,48128
s_waitcnt lgkmcnt(9)
mvaa 12,212,184
ex 100
mvaa 12,220,188
la 172,250,50,2048
cvaa 40,224,176,136
ex 101
mvaa 40,232,180
ex 102
mvaa 40,240,184
ex 103
mvaa 40,248,188
cv 70,100,101
cv 71,102,103
s_waitcnt lgkmcnt(4)
cvaa 44,228,176,140
ex 72
mvaa 44,236,180
s_waitcnt vmcnt(18) lgkmcnt(0)
s_barrier
ex 73
mvaa 44,244,184
la 176,252,50,1024
mvaa 44,252,188
ex 74
maav 0,64,64
ex 75
maav 0,96,68
la 188,250,50,3072
maav 4,68,64
ra 192,5,0
cv 72,72,73
maav 4,100,68
ra 196,5,1024
cv 73,74,75
maav 8,72,64
la 184,250,50,1024
ds_write_b128 v6,a[0:3] offset:0
maav 8,104,68
ex 76
maav 12,76,64
ex 77
maav 12,108,68
la 180,252,50,3072
ds_write_b128 v6,a[4:7] offset:1024
v_smfmac_f32_16x16x64_bf16 v[144:147],v[228:231],v[64:71],v248
cv 74,76,77
maav 16,80,64
ex 78
maav 16,112,68
ds_write_b128 v6,a[8:11] offset:2048
ex 79
maav 20,84,64
ex 104
maav 20,116,68
ds_write_b128 v6,a[12:15] offset:3072
ex 105
maav 24,88,64
ex 106
ex 107
ex 108
maav 24,120,68
ds_write_b128 v6,a[16:19] offset:4096
cv 75,78,79
maav 28,92,64
ex 109
ex 110
ex 111
maav 28,124,68
cv 76,104,105
cv 77,106,107
cv 78,108,109
cv 79,110,111
maav 32,64,72
ds_write_b128 v6,a[20:23] offset:5120
maav 32,96,76
ex 80
maav 36,68,72
ex 81
maav 36,100,76
ds_write_b128 v6,a[24:27] offset:6144
maav 40,72,72
ex 82
maav 40,104,76
ex 83
maav 44,76,72
ds_write_b128 v6,a[28:31] offset:7168
maav 44,108,76
cv 80,80,81
cv 81,82,83
v_smfmac_f32_16x16x64_bf16 v[148:151],v[228:231],v[72:79],v248
ex 84
maav 48,80,72
ds_write_b128 v7,a[32:35] offset:0
maav 48,112,76
ex 85
ex 86
ex 87
maav 52,84,72
ex 112
ex 113
maav 52,116,76
ds_write_b128 v7,a[36:39] offset:1024
cv 82,84,85
maav 56,88,72
ex 114
ex 115
ex 116
maav 56,120,76
ex 117
ex 118
ex 119
maav 60,92,72
ds_write_b128 v7,a[40:43] offset:2048
cv 83,86,87
maav 60,124,76
cv 84,112,113
cv 85,114,115
cv 86,116,117
cv 87,118,119
mvav 160,64,80
ex 88
mvav 160,96,84
)"
R"(ds_write_b128 v7,a[44:47] offset:3072
mvav 164,68,80
v_add_f32_e32 v130,v144,v145
v_add_f32_e32 v131,v148,v149
mvav 164,100,84
ex 89
mvav 168,72,80
ds_write_b128 v7,a[48:51] offset:4096
mvav 168,104,84
ex 90
mvav 172,76,80
ex 91
mvav 172,108,84
ds_write_b128 v32,v[128:131]
v_smfmac_f32_16x16x64_bf16 v[152:155],v[228:231],v[80:87],v248
ex 92
ex 93
mvav 176,80,80
ds_write_b128 v7,a[52:55] offset:5120
ex 94
mvav 176,112,84
ra 200,5,2048
ra 204,5,3072
ex 95
cv 88,88,89
mvav 180,84,80
ra 208,5,4096
ra 212,5,5120
cv 89,90,91
cv 90,92,93
ex 120
mvav 180,116,84
ds_write_b128 v7,a[56:59] offset:6144
ex 121
mvav 184,88,80
ex 122
ex 123
cv 91,94,95
mvav 184,120,84
ex 124
ex 125
mvav 188,92,80
ds_write_b128 v7,a[60:63] offset:7168
ex 126
mvav 188,124,84
ra 216,5,6144
ra 220,5,7168
ra 224,5,8192
ra 232,5,10240
ex 127
cv 92,120,121
mvav 192,64,88
ra 240,5,12288
ra 248,5,14336
ra 228,5,9216
ra 236,5,11264
ra 244,5,13312
ra 252,5,15360
cv 93,122,123
cv 94,124,125
cv 95,126,127
v_xor_b32_e32 v6,8192,v6
s_nop 0
mvav 192,96,92
ex 16
mvav 196,68,88
s_waitcnt lgkmcnt(10)
s_xor_b32 s87,s87,0
mvav 196,100,92
s_xor_b32 s50,s66,294912
s_barrier
v_xor_b32_e32 v7,8192,v7
mvav 200,72,88
la 4,252,50,0
ra 64,5,16384
ra 96,5,24576
rv 64,6,0
rv 68,6,1024
rv 72,6,2048
mvav 200,104,92
rv 76,6,3072
rv 80,6,4096
rv 84,6,5120
ex 17
ex 18
mvav 204,76,88
rv 96,7,0
rv 100,7,1024
ex 19
mvav 204,108,92
la 12,252,50,2048
rv 104,7,2048
rv 108,7,3072
cv 16,16,17
v_smfmac_f32_16x16x64_bf16 v[156:159],v[228:231],v[88:95],v248
rv 112,7,4096
rv 116,7,5120
ex 20
ex 21
mvav 208,80,88
rv 120,7,6144
rv 124,7,7168
ex 22
ex 23
mvav 208,112,92
la 20,250,50,0
ra 68,5,17408
ra 100,5,25600
cv 17,18,19
cv 18,20,21
mvav 212,84,88
ra 72,5,18432
ra 104,5,26624
ex 48
ex 49
mvav 212,116,92
ra 76,5,19456
ra 108,5,27648
ex 50
ex 51
mvav 216,88,88
la 28,250,50,2048
ra 80,5,20480
ra 112,5,28672
ra 84,5,21504
mvav 216,120,92
ra 116,5,29696
cv 19,22,23
ex 52
ex 53
mvav 220,92,88
ra 88,5,22528
cv 20,48,49
ex 54
ex 55
mvav 220,124,92
la 60,250,50,3072
ra 120,5,30720
cv 21,50,51
cv 22,52,53
cv 23,54,55
s_bitcmp1_b32 s82,s81
s_cbranch_scc1 L_pend2
L_ret_eb4:
mvav 160,192,16
ra 92,5,23552
rv 88,6,6144
ra 124,5,31744
rv 92,6,7168
ex 24
s_waitcnt lgkmcnt(10)
mvav 160,224,20
ex 25
ex 26
v_smfmac_f32_16x16x64_bf16 v[152:155],v[228:231],v[16:23],v248
ex 27
v_xor_b32_e32 v32,4096,v32
mvav 164,196,16
la 52,250,50,1024
ds_read_b128 v[236:239],v32
cv 24,24,25
ex 28
mvav 164,228,20
ex 29
cv 25,26,27
ex 30
ex 31
mvav 168,200,16
ex 56
ex 57
mvav 168,232,20
la 44,252,50,3072
cv 26,28,29
ex 58
ex 59
mvav 172,204,16
ex 60
ex 61
ex 62
mvav 172,236,20
s_waitcnt lgkmcnt(0)
s_barrier
cv 27,30,31
ex 63
mvav 176,208,16
la 36,252,50,1024
cv 28,56,57
cv 29,58,59
cv 30,60,61
cv 31,62,63
mvav 176,240,20
s_add_u32 m0,s52,147456
ex 8
s_xor_b32 s51,s63,1048576
ex 9
ex 10
mvav 180,212,16
s_add_u32 s50,s50,s55
ex 11
cv 8,8,9
cv 9,10,11
ex 12
mvav 180,244,20
dl 247,24,51,3072
ex 13
ex 14
mvav 184,216,16
s_add_u32 m0,s52,0
ex 15
ex 40
ex 41
mvav 184,248,20
s_xor_b32 s51,s64,0
ex 42
ex 43
ex 44
mvav 188,220,16
la 0,252,50,0
ex 45
ex 46
mvav 188,252,20
ex 47
cv 10,12,13
cv 11,14,15
cv 12,40,41
cv 13,42,43
cv 14,44,45
v_smfmac_f32_16x16x64_bf16 v[156:159],v[228:231],v[24:31],v248
cv 15,46,47
v_bfe_u32 v225,v244,1,5
s_nop 0
v_smfmac_f32_16x16x64_bf16 v[156:159],v[228:231],v[8:15],v248
v_lshl_add_u32 v224,v224,1,v225
v_bfe_u32 v225,v244,7,2
v_lshl_add_u32 v224,v225,5,v224
mvav 192,192,24
la 8,252,50,2048
v_xor_b32_e32 v224,s69,v224
v_add_f32_e32 v152,v152,v153
v_add_f32_e32 v137,v152,v238
v_subrev_u32_e32 v236,0x23800000,v137
mvav 192,224,28
v_rcp_f32_e32 v137,v137
mvav 192,64,8
tv 128,224,32768
v_mul_f32_e32 v232,v240,v137
v_mul_f32_e32 v64,v232,v64
v_mul_f32_e32 v65,v232,v65
v_mul_f32_e32 v66,v232,v66
mvav 192,96,12
la 16,250,50,0
v_mul_f32_e32 v67,v232,v67
v_mul_f32_e32 v68,v232,v68
v_mul_f32_e32 v69,v232,v69
v_mul_f32_e32 v70,v232,v70
mvav 196,196,24
v_fmac_f32_e32 v64,v232,v160
v_fmac_f32_e32 v65,v232,v161
v_mul_f32_e32 v71,v232,v71
mvav 196,228,28
v_fmac_f32_e32 v66,v232,v162
v_fmac_f32_e32 v67,v232,v163
v_fmac_f32_e32 v68,v232,v164
v_fmac_f32_e32 v69,v232,v165
v_fmac_f32_e32 v70,v232,v166
mvav 196,68,8
la 24,250,50,2048
v_fmac_f32_e32 v71,v232,v167
cv 160,64,65
cv 161,66,67
cv 162,68,69
cv 163,70,71
mvav 196,100,12
tv 130,224,34816
tv 132,224,36864
mvav 200,200,24
tv 134,224,38912
tv 144,224,40960
v_mul_f32_e32 v72,v232,v72
v_mul_f32_e32 v73,v232,v73
v_mul_f32_e32 v74,v232,v74
mvav 200,232,28
buffer_store_dwordx4 v[160:163],v244,s[8:11],s14 offen offset:0 sc0 sc1
v_mul_f32_e32 v75,v232,v75
v_mul_f32_e32 v76,v232,v76
v_mul_f32_e32 v77,v232,v77
v_mul_f32_e32 v78,v232,v78
mvav 200,72,8
v_fmac_f32_e32 v72,v232,v168
v_fmac_f32_e32 v73,v232,v169
v_mul_f32_e32 v79,v232,v79
mvav 200,104,12
L_vs18:
s_cbranch_execz L_vs22
v_fmac_f32_e32 v74,v232,v170
v_fmac_f32_e32 v75,v232,v171
v_fmac_f32_e32 v76,v232,v172
v_fmac_f32_e32 v77,v232,v173
v_fmac_f32_e32 v78,v232,v174
mvav 204,204,24
la 56,250,50,3072
v_fmac_f32_e32 v79,v232,v175
cv 168,72,73
cv 169,74,75
cv 170,76,77
cv 171,78,79
mvav 204,236,28
tv 146,224,43008
tv 148,224,45056
mvav 204,76,8
tv 150,224,47104
v_mul_f32_e32 v80,v232,v80
v_mul_f32_e32 v81,v232,v81
v_mul_f32_e32 v82,v232,v82
mvav 204,108,12
buffer_store_dwordx4 v[168:171],v244,s[8:11],s14 offen offset:2048 sc0 sc1
v_mul_f32_e32 v83,v232,v83
v_mul_f32_e32 v84,v232,v84
v_mul_f32_e32 v85,v232,v85
v_mul_f32_e32 v86,v232,v86
v_mul_f32_e32 v87,v232,v87
mvav 208,208,24
v_fmac_f32_e32 v80,v232,v176
v_fmac_f32_e32 v81,v232,v177
v_fmac_f32_e32 v82,v232,v178
mvav 208,240,28
v_fmac_f32_e32 v83,v232,v179
v_fmac_f32_e32 v84,v232,v180
v_fmac_f32_e32 v85,v232,v181
cv 176,80,81
cv 177,82,83
mvav 208,80,8
la 48,250,50,1024
v_fmac_f32_e32 v86,v232,v182
v_fmac_f32_e32 v87,v232,v183
cv 178,84,85
cv 179,86,87
mvav 208,112,12
v_mul_f32_e32 v88,v232,v88
v_mul_f32_e32 v89,v232,v89
v_mul_f32_e32 v90,v232,v90
mvav 212,212,24
v_mul_f32_e32 v91,v232,v91
v_mul_f32_e32 v92,v232,v92
v_mul_f32_e32 v93,v232,v93
v_mul_f32_e32 v94,v232,v94
v_mul_f32_e32 v95,v232,v95
mvav 212,244,28
buffer_store_dwordx4 v[176:179],v244,s[8:11],s88 offen offset:0 sc0 sc1
v_fmac_f32_e32 v88,v232,v184
v_fmac_f32_e32 v89,v232,v185
v_fmac_f32_e32 v90,v232,v186
v_fmac_f32_e32 v91,v232,v187
cv 184,88,89
mvav 212,84,8
v_fmac_f32_e32 v92,v232,v188
cv 185,90,91
mvav 212,116,12
v_fmac_f32_e32 v93,v232,v189
v_fmac_f32_e32 v94,v232,v190
v_fmac_f32_e32 v95,v232,v191
cv 186,92,93
cv 187,94,95
mvav 216,216,24
la 40,252,50,3072
v_add_f32_e32 v156,v156,v157
v_add_f32_e32 v141,v156,v239
v_subrev_u32_e32 v237,0x23800000,v141
v_rcp_f32_e32 v141,v141
mvav 216,248,28
v_mul_f32_e32 v233,v241,v141
v_mul_f32_e32 v96,v233,v96
mvav 216,88,8
v_mul_f32_e32 v97,v233,v97
v_mul_f32_e32 v98,v233,v98
v_mul_f32_e32 v99,v233,v99
v_mul_f32_e32 v100,v233,v100
mvav 216,120,12
buffer_store_dwordx4 v[184:187],v244,s[8:11],s88 offen offset:2048 sc0 sc1
v_fmac_f32_e32 v96,v233,v192
v_fmac_f32_e32 v97,v233,v193
v_mul_f32_e32 v101,v233,v101
v_mul_f32_e32 v102,v233,v102
v_mul_f32_e32 v103,v233,v103
mvav 220,220,24
v_fmac_f32_e32 v98,v233,v194
v_fmac_f32_e32 v99,v233,v195
mvav 220,252,28
v_fmac_f32_e32 v100,v233,v196
v_fmac_f32_e32 v101,v233,v197
cv 192,96,97
cv 193,98,99
mvav 220,92,8
la 32,252,50,1024
v_fmac_f32_e32 v102,v233,v198
v_fmac_f32_e32 v103,v233,v199
cv 194,100,101
cv 195,102,103
mvav 220,124,12
v_xor_b32_e32 v225,32,v224
v_xor_b32_e32 v226,64,v224
s_waitcnt lgkmcnt(0)
zvva 0,128,128
tv 138,225,34816
tv 142,225,38912
tv 152,225,40960
s_waitcnt vmcnt(36)
mvva 0,132,132
tv 136,225,32768
tv 154,225,43008
mvva 0,144,136
tv 140,225,36864
tv 158,225,47104
mvva 0,148,140
tv 156,225,45056
v_mul_f32_e32 v104,v233,v104
v_mul_f32_e32 v105,v233,v105
v_mul_f32_e32 v106,v233,v106
zvva 4,148,156
buffer_store_dwordx4 v[192:195],v244,s[8:11],s14 offen offset:1024 sc0 sc1
v_mul_f32_e32 v107,v233,v107
v_mul_f32_e32 v108,v233,v108
v_mul_f32_e32 v109,v233,v109
v_mul_f32_e32 v110,v233,v110
mvva 4,144,152
v_fmac_f32_e32 v104,v233,v200
v_fmac_f32_e32 v105,v233,v201
v_fmac_f32_e32 v106,v233,v202
v_mul_f32_e32 v111,v233,v111
mvva 4,132,148
v_fmac_f32_e32 v107,v233,v203
v_fmac_f32_e32 v108,v233,v204
v_fmac_f32_e32 v109,v233,v205
cv 200,104,105
cv 201,106,107
mvva 4,128,144
v_fmac_f32_e32 v110,v233,v206
v_fmac_f32_e32 v111,v233,v207
cv 202,108,109
cv 203,110,111
s_waitcnt vmcnt(16)
zvva 8,128,160
v_cmp_lt_u32_e64 s[42:43],s48,v236
v_mul_f32_e32 v112,v233,v112
s_or_b64 s[40:41],s[40:41],s[42:43]
v_cmp_lt_u32_e64 s[42:43],s48,v237
mvva 8,132,164
s_or_b64 s[40:41],s[40:41],s[42:43]
v_mul_f32_e32 v113,v233,v113
v_mul_f32_e32 v114,v233,v114
v_mul_f32_e32 v115,v233,v115
mvva 8,144,168
dl 245,36,51,0
v_mul_f32_e32 v116,v233,v116
v_mul_f32_e32 v117,v233,v117
v_mul_f32_e32 v118,v233,v118
v_mul_f32_e32 v119,v233,v119
v_mul_f32_e32 v120,v233,v120
mvva 8,148,172
v_fmac_f32_e32 v112,v233,v208
v_fmac_f32_e32 v113,v233,v209
v_fmac_f32_e32 v114,v233,v210
v_fmac_f32_e32 v115,v233,v211
zvva 12,148,188
v_fmac_f32_e32 v116,v233,v212
v_fmac_f32_e32 v117,v233,v213
cv 208,112,113
cv 209,114,115
mvva 12,144,184
v_fmac_f32_e32 v118,v233,v214
v_fmac_f32_e32 v119,v233,v215
cv 210,116,117
cv 211,118,119
mvva 12,132,180
v_mul_f32_e32 v121,v233,v121
v_mul_f32_e32 v122,v233,v122
v_mul_f32_e32 v123,v233,v123
mvva 12,128,176
v_mul_f32_e32 v124,v233,v124
v_mul_f32_e32 v125,v233,v125
v_mul_f32_e32 v126,v233,v126
v_mul_f32_e32 v127,v233,v127
s_waitcnt lgkmcnt(0)
zvva 28,136,176
buffer_store_dwordx4 v[200:203],v244,s[8:11],s14 offen offset:3072 sc0 sc1
tv 144,226,40960
v_fmac_f32_e32 v120,v233,v216
v_fmac_f32_e32 v121,v233,v217
v_fmac_f32_e32 v122,v233,v218
v_fmac_f32_e32 v123,v233,v219
mvva 28,140,180
tv 128,226,32768
tv 130,226,34816
cv 216,120,121
cv 217,122,123
mvva 28,152,184
tv 132,226,36864
tv 134,226,38912
v_fmac_f32_e32 v124,v233,v220
v_fmac_f32_e32 v125,v233,v221
v_fmac_f32_e32 v126,v233,v222
mvva 28,156,188
tv 146,226,43008
v_fmac_f32_e32 v127,v233,v223
cv 218,124,125
cv 219,126,127
zvva 24,156,172
tv 148,226,45056
tv 150,226,47104
mvva 24,152,168
v_xor_b32_e32 v227,96,v224
v_add_u32_e32 v232,131072,v224
mvva 24,140,164
buffer_store_dwordx4 v[208:211],v244,s[8:11],s88 offen offset:1024 sc0 sc1
mvva 24,136,160
v_add_u32_e32 v233,131072,v225
v_add_u32_e32 v234,131072,v226
zvva 20,136,144
v_add_u32_e32 v235,131072,v227
v_mov_b32_e32 v242,83914752
mvva 20,140,148
v_add_u32_e32 v228,65536,v224
v_add_u32_e32 v229,65536,v225
mvva 20,152,152
v_add_u32_e32 v230,65536,v226
v_add_u32_e32 v231,65536,v227
mvva 20,156,156
v_and_b32_e32 v243,240,v244
v_lshlrev_b32_e32 v243,7,v243
zvva 16,156,140
buffer_store_dwordx4 v[216:219],v244,s[8:11],s88 offen offset:3072 sc0 sc1
s_cmp_lg_u64 s[40:41],0
s_cbranch_scc1 L_redo
L_redo_r4:
mvva 16,152,136
tv 156,227,45056
v_mov_b32_e32 v236,16256
mvva 16,140,132
tv 158,227,47104
v_and_b32_e32 v248,256,v244
mvva 16,136,128
tv 152,227,40960
v_lshrrev_b32_e32 v248,3,v248
s_waitcnt lgkmcnt(3)
zvva 32,128,128
tv 140,227,36864
v_add_u32_e32 v243,v243,v248
mvva 32,132,132
tv 136,227,32768
tv 138,227,34816
mvva 32,144,136
dl 246,36,51,1024
tv 142,227,38912
mvva 32,148,140
tv 154,227,43008
v_and_b32_e32 v248,512,v244
zvva 36,148,156
v_lshrrev_b32_e32 v248,5,v248
v_add_u32_e32 v243,v243,v248
mvva 36,144,152
v_and_b32_e32 v248,16,v244
v_lshlrev_b32_e32 v248,v248,v236
mvva 36,132,148
v_cndmask_b32_e64 v238,0,v248,s[74:75]
s_mov_b32 s74,0xc0000c
mvva 36,128,144
s_mov_b32 s75,0xc0000c00
v_mov_b32_e32 v240,0
v_cndmask_b32_e64 v239,0,v248,s[74:75]
zvva 40,128,160
dl 249,36,51,2048
mvva 40,132,164
v_mov_b32_e32 v241,0
v_mov_b32_e32 v236,0
mvva 40,144,168
v_mov_b32_e32 v237,0
mvva 40,148,172
zvva 44,148,188
mvva 44,144,184
mvva 44,132,180
dl 247,36,51,3072
s_add_u32 m0,s52,16384
s_xor_b32 s51,s65,0
mvva 44,128,176
tv 144,224,57344
s_waitcnt lgkmcnt(1)
zvva 60,136,176
tv 132,224,53248
tv 134,224,55296
mvva 60,140,180
tv 128,224,49152
tv 130,224,51200
mvva 60,152,184
mvva 60,156,188
tv 146,224,59392
zvva 56,156,172
dl 245,36,51,0
tv 148,224,61440
tv 150,224,63488
mvva 56,152,168
mvva 56,140,164
mvva 56,136,160
zvva 52,136,144
mvva 52,140,148
mvva 52,152,152
dl 246,36,51,1024
mvva 52,156,156
zvva 48,156,140
mvva 48,152,136
mvva 48,140,132
tv 156,225,61440
mvva 48,136,128
tv 152,225,57344
s_waitcnt lgkmcnt(2)
zvva 64,128,128
dl 249,36,51,2048
tv 140,225,53248
mvva 64,132,132
tv 136,225,49152
tv 138,225,51200
mvva 64,144,136
tv 142,225,55296
mvva 64,148,140
tv 154,225,59392
zvva 68,148,156
tv 158,225,63488
mvva 68,144,152
mvva 68,132,148
dl 247,36,51,3072
s_lshl_b32 s51,s33,5
s_add_u32 m0,s52,98304
s_xor_b32 s51,s64,s51
mvva 68,128,144
zvva 72,128,160
mvva 72,132,164
s_waitcnt vmcnt(18)
s_mov_b32 s90,0
mvva 72,144,168
s_xor_b32 s50,s67,0
mvva 72,148,172
zvva 76,148,188
dl 245,36,51,0
mvva 76,144,184
mvva 76,132,180
tv 148,226,61440
mvva 76,128,176
tv 144,226,57344
s_waitcnt lgkmcnt(2)
zvva 92,136,176
tv 132,226,53248
mvva 92,140,180
tv 128,226,49152
tv 130,226,51200
mvva 92,152,184
dl 246,36,51,1024
tv 134,226,55296
mvva 92,156,188
tv 146,226,59392
zvva 88,156,172
tv 150,226,63488
mvva 88,152,168
mvva 88,140,164
mvva 88,136,160
zvva 84,136,144
dl 249,36,51,2048
mvva 84,140,148
mvva 84,152,152
mvva 84,156,156
zvva 80,156,140
mvva 80,152,136
mvva 80,140,132
dl 247,36,51,3072
mvva 80,136,128
s_lshl_b32 s51,s33,5
tv 152,227,57344
s_add_u32 m0,s52,114688
s_waitcnt lgkmcnt(1)
zvva 96,128,128
s_xor_b32 s51,s65,s51
tv 140,227,53248
tv 142,227,55296
mvva 96,132,132
tv 136,227,49152
tv 138,227,51200
mvva 96,144,136
mvva 96,148,140
tv 154,227,59392
zvva 100,148,156
lv 164,252,50,0
tv 156,227,61440
tv 158,227,63488
mvva 100,144,152
mvva 100,132,148
mvva 100,128,144
zvva 104,128,160
mvva 104,132,164
mvva 104,144,168
lv 172,252,50,2048
mvva 104,148,172
zvva 108,148,188
mvva 108,144,184
mvva 108,132,180
mvva 108,128,176
s_barrier
s_waitcnt lgkmcnt(0)
zvva 124,136,176
lv 180,250,50,0
tv 132,232,4096
tv 144,232,8192
tv 146,232,10240
mvva 124,140,180
tv 128,232,0
tv 130,232,2048
mvva 124,152,184
tv 134,232,6144
mvva 124,156,188
tv 148,232,12288
tv 150,232,14336
zvva 120,156,172
mvva 120,152,168
mvva 120,140,164
lv 188,250,50,2048
mvva 120,136,160
zvva 116,136,144
mvva 116,140,148
mvva 116,152,152
mvva 116,156,156
zvva 112,156,140
lv 220,250,50,3072
mvva 112,152,136
mvva 112,140,132
tv 156,233,12288
mvva 112,136,128
tv 152,233,8192
s_waitcnt lgkmcnt(2)
mvva 0,128,4
tv 140,233,4096
mvva 0,132,12
tv 136,233,0
tv 138,233,2048
mvva 0,144,20
lv 212,250,50,1024
tv 142,233,6144
mvva 0,148,28
tv 154,233,10240
mvva 4,148,60
tv 158,233,14336
mvva 4,144,52
mvva 4,132,44
mvva 4,128,36
mvva 8,128,0
lv 204,252,50,3072
mvva 8,132,8
mvva 8,144,16
mvva 8,148,24
mvva 12,148,56
s_waitcnt vmcnt(27)
mvva 12,144,48
s_waitcnt vmcnt(25)
mvva 12,132,40
lv 196,252,50,1024
s_waitcnt vmcnt(24)
mvva 12,128,32
tv 144,234,8192
s_waitcnt lgkmcnt(1)
mvva 28,136,32
tv 132,234,4096
tv 134,234,6144
mvva 28,140,40
tv 128,234,0
tv 130,234,2048
mvva 28,152,48
mvva 28,156,56
tv 146,234,10240
mvva 24,156,24
dl 245,36,51,0
s_waitcnt vmcnt(20)
s_barrier
tv 148,234,12288
tv 150,234,14336
mvva 24,152,16
mvva 24,140,8
mvva 24,136,0
mvva 20,136,36
mvva 20,140,44
mvva 20,152,52
s_cmp_eq_u32 s12,0
s_cselect_b64 exec,1,0
buffer_store_dword v251,off,s[8:11],s61 sc0 sc1
s_mov_b64 exec,-1
mvva 20,156,60
mvva 16,156,28
mvva 16,152,20
mvva 16,140,12
tv 156,235,12288
mvva 16,136,4
tv 152,235,8192
s_waitcnt lgkmcnt(2)
mvva 32,128,4
dl 246,36,51,1024
tv 140,235,4096
mvva 32,132,12
tv 136,235,0
tv 138,235,2048
mvva 32,144,20
tv 142,235,6144
mvva 32,148,28
tv 154,235,10240
mvva 36,148,60
tv 158,235,14336
mvva 36,144,52
mvva 36,132,44
dl 249,36,51,2048
mvva 36,128,36
mvva 40,128,0
mvva 40,132,8
mvva 40,144,16
mvva 40,148,24
mvva 44,148,56
dl 247,36,51,3072
s_lshl_b32 s51,s34,5
s_add_u32 m0,s52,65536
s_xor_b32 s51,s64,s51
mvva 44,144,48
tv 148,232,28672
mvva 44,132,40
s_xor_b32 s53,s60,s33
tv 150,232,30720
mvva 44,128,32
tv 144,232,24576
s_waitcnt lgkmcnt(3)
mvva 60,136,32
tv 132,232,20480
mvva 60,140,40
tv 128,232,16384
tv 130,232,18432
mvva 60,152,48
dl 245,36,51,0
tv 134,232,22528
mvva 60,156,56
tv 146,232,26624
mvva 56,156,24
mvva 56,152,16
mvva 56,140,8
mvva 56,136,0
mvva 52,136,36
buffer_load_dword v253,v242,s[8:11],s53 offen sc1
mvva 52,140,44
mvva 52,152,52
mvva 52,156,60
mvva 48,156,28
mvva 48,152,20
mvva 48,140,12
dl 246,36,51,1024
mvva 48,136,4
tv 152,233,24576
s_waitcnt lgkmcnt(1)
mvva 64,128,4
tv 140,233,20480
tv 142,233,22528
mvva 64,132,12
tv 136,233,16384
tv 138,233,18432
mvva 64,144,20
mvva 64,148,28
tv 154,233,26624
mvva 68,148,60
dl 249,36,51,2048
tv 156,233,28672
tv 158,233,30720
mvva 68,144,52
mvva 68,132,44
mvva 68,128,36
mvva 72,128,0
mvva 72,132,8
mvva 72,144,16
dl 247,36,51,3072
s_lshl_b32 s51,s34,5
s_add_u32 m0,s52,81920
s_xor_b32 s51,s65,s51
mvva 72,148,24
mvva 76,148,56
mvva 76,144,48
mvva 76,132,40
tv 148,234,28672
mvva 76,128,32
L_vs19:
s_cbranch_execz L_vs23
tv 144,234,24576
s_waitcnt lgkmcnt(2)
mvva 92,136,32
dl 245,36,51,0
tv 132,234,20480
mvva 92,140,40
tv 128,234,16384
tv 130,234,18432
mvva 92,152,48
tv 134,234,22528
mvva 92,156,56
tv 146,234,26624
mvva 88,156,24
tv 150,234,30720
mvva 88,152,16
mvva 88,140,8
dl 246,36,51,1024
mvva 88,136,0
mvva 84,136,36
mvva 84,140,44
mvva 84,152,52
mvva 84,156,60
mvva 80,156,28
dl 249,36,51,2048
mvva 80,152,20
mvva 80,140,12
tv 156,235,28672
mvva 80,136,4
tv 152,235,24576
s_waitcnt lgkmcnt(2)
mvva 96,128,4
tv 140,235,20480
mvva 96,132,12
tv 136,235,16384
tv 138,235,18432
mvva 96,144,20
dl 247,36,51,3072
s_lshl_b32 s51,s35,5
s_add_u32 m0,s52,32768
s_xor_b32 s51,s64,s51
tv 142,235,22528
mvva 96,148,28
tv 154,235,26624
mvva 100,148,60
tv 158,235,30720
mvva 100,144,52
mvva 100,132,44
mvva 100,128,36
mvva 104,128,0
dl 245,36,51,0
mvva 104,132,8
mvva 104,144,16
mvva 104,148,24
mvva 108,148,56
mvva 108,144,48
mvva 108,132,40
dl 246,36,51,1024
mvva 108,128,32
s_waitcnt vmcnt(27)
s_barrier
s_waitcnt lgkmcnt(0)
mvva 124,136,32
tv 132,224,4096
tv 134,224,6144
tv 144,224,8192
mvva 124,140,40
tv 128,224,0
tv 130,224,2048
mvva 124,152,48
mvva 124,156,56
tv 146,224,10240
mvva 120,156,24
dl 249,36,51,2048
tv 148,224,12288
tv 150,224,14336
mvva 120,152,16
mvva 120,140,8
mvva 120,136,0
mvva 116,136,36
mvva 116,140,44
mvva 116,152,52
dl 247,36,51,3072
s_lshl_b32 s51,s35,5
s_add_u32 m0,s52,49152
s_xor_b32 s51,s65,s51
mvva 116,156,60
mvva 112,156,28
mvva 112,152,20
s_waitcnt vmcnt(11)
v_cmp_ne_u32_e32 vcc,4,v253
mvva 112,140,12
dl 245,36,51,0
s_cbranch_vccnz L_spa3e
L_spra3e:
mvva 112,136,4
s_xor_b32 s50,s67,s33
tv 152,225,8192
s_waitcnt lgkmcnt(1)
mvvv 0,128,164
tv 140,225,4096
tv 142,225,6144
mvvv 0,132,172
tv 136,225,0
tv 138,225,2048
mvvv 0,144,180
mvvv 0,148,188
dl 246,36,51,1024
tv 154,225,10240
mvvv 4,148,220
tv 156,225,12288
tv 158,225,14336
mvvv 4,144,212
)"
R"(mvvv 4,132,204
mvvv 4,128,196
mvvv 8,128,160
la 196,252,50,0
s_xor_b32 s53,s60,s34
mvvv 8,132,168
mvvv 8,144,176
mvvv 8,148,184
mvvv 12,148,216
mvvv 12,144,208
la 204,252,50,2048
mvvv 12,132,200
tv 148,226,12288
mvvv 12,128,192
tv 144,226,8192
s_waitcnt lgkmcnt(2)
mvvv 28,136,192
tv 132,226,4096
mvvv 28,140,200
tv 128,226,0
tv 130,226,2048
mvvv 28,152,208
buffer_load_dword v253,v242,s[8:11],s53 offen sc1
tv 134,226,6144
mvvv 28,156,216
tv 146,226,10240
mvvv 24,156,184
tv 150,226,14336
mvvv 24,152,176
mvvv 24,140,168
mvvv 24,136,160
la 212,250,50,0
mvvv 20,136,196
mvvv 20,140,204
mvvv 20,152,212
mvvv 20,156,220
la 220,250,50,2048
mvvv 16,156,188
mvvv 16,152,180
mvvv 16,140,172
tv 156,227,12288
mvvv 16,136,164
la 252,250,50,3072
tv 152,227,8192
s_waitcnt lgkmcnt(2)
mvvv 32,128,164
tv 140,227,4096
mvvv 32,132,172
tv 136,227,0
tv 138,227,2048
mvvv 32,144,180
tv 142,227,6144
mvvv 32,148,188
la 244,250,50,1024
tv 154,227,10240
mvvv 36,148,220
tv 158,227,14336
mvvv 36,144,212
mvvv 36,132,204
mvvv 36,128,196
la 236,252,50,3072
mvvv 40,128,160
mvvv 40,132,168
mvvv 40,144,176
mvvv 40,148,184
mvvv 44,148,216
la 228,252,50,1024
s_add_u32 s50,s50,s55
mvvv 44,144,208
mvvv 44,132,200
tv 148,224,28672
tv 150,224,30720
mvvv 44,128,192
s_waitcnt lgkmcnt(2)
mvvv 60,136,192
la 192,252,50,0
tv 132,224,20480
mvvv 60,140,200
tv 128,224,16384
tv 130,224,18432
mvvv 60,152,208
tv 134,224,22528
mvvv 60,156,216
tv 144,224,24576
tv 146,224,26624
mvvv 56,156,184
la 200,252,50,2048
mvvv 56,152,176
mvvv 56,140,168
mvvv 56,136,160
mvvv 52,136,196
la 208,250,50,0
mvvv 52,140,204
mvvv 52,152,212
mvvv 52,156,220
mvvv 48,156,188
la 216,250,50,2048
mvvv 48,152,180
mvvv 48,140,172
tv 156,225,28672
mvvv 48,136,164
tv 152,225,24576
s_waitcnt lgkmcnt(2)
mvvv 64,128,164
tv 140,225,20480
mvvv 64,132,172
la 248,250,50,3072
tv 136,225,16384
tv 138,225,18432
mvvv 64,144,180
tv 142,225,22528
mvvv 64,148,188
tv 154,225,26624
mvvv 68,148,220
tv 158,225,30720
mvvv 68,144,212
la 240,250,50,1024
mvvv 68,132,204
mvvv 68,128,196
mvvv 72,128,160
mvvv 72,132,168
la 232,252,50,3072
mvvv 72,144,176
mvvv 72,148,184
mvvv 76,148,216
mvvv 76,144,208
mvvv 76,132,200
la 224,252,50,1024
mvvv 76,128,192
tv 144,226,24576
s_waitcnt lgkmcnt(1)
mvvv 92,136,192
tv 132,226,20480
tv 134,226,22528
mvvv 92,140,200
tv 128,226,16384
tv 130,226,18432
mvvv 92,152,208
dl 249,36,51,2048
mvvv 92,156,216
tv 146,226,26624
mvvv 88,156,184
tv 148,226,28672
tv 150,226,30720
mvvv 88,152,176
mvvv 88,140,168
dl 247,36,51,3072
s_lshl_b32 s51,s98,5
s_xor_b32 s51,s64,s51
s_add_u32 m0,s52,131072
s_xor_b32 s51,s51,4194304
mvvv 88,136,160
s_waitcnt vmcnt(16)
v_cmp_ne_u32_e32 vcc,4,v253
mvvv 84,136,196
s_cbranch_vccnz L_spa4e
L_spra4e:
mvvv 84,140,204
s_xor_b32 s50,s67,s34
mvvv 84,152,212
dl 245,36,51,0
mvvv 84,156,220
mvvv 80,156,188
mvvv 80,152,180
mvvv 80,140,172
la 68,252,50,0
mvvv 80,136,164
tv 152,227,24576
s_waitcnt lgkmcnt(1)
mvvv 96,128,164
tv 140,227,20480
tv 142,227,22528
mvvv 96,132,172
tv 136,227,16384
tv 138,227,18432
mvvv 96,144,180
s_xor_b32 s53,s60,s35
mvvv 96,148,188
la 76,252,50,2048
tv 154,227,26624
mvvv 100,148,220
tv 156,227,28672
tv 158,227,30720
mvvv 100,144,212
mvvv 100,132,204
mvvv 100,128,196
la 84,250,50,0
mvvv 104,128,160
mvvv 104,132,168
mvvv 104,144,176
mvvv 104,148,184
buffer_load_dword v253,v242,s[8:11],s53 offen sc1
mvvv 108,148,216
mvvv 108,144,208
mvvv 108,132,200
mvvv 108,128,192
s_barrier
s_waitcnt lgkmcnt(0)
mvvv 124,136,192
la 92,250,50,2048
tv 132,228,36864
tv 144,228,40960
tv 146,228,43008
mvvv 124,140,200
tv 128,228,32768
tv 130,228,34816
mvvv 124,152,208
tv 134,228,38912
mvvv 124,156,216
tv 148,228,45056
tv 150,228,47104
mvvv 120,156,184
la 124,250,50,3072
mvvv 120,152,176
mvvv 120,140,168
mvvv 120,136,160
mvvv 116,136,196
la 116,250,50,1024
mvvv 116,140,204
mvvv 116,152,212
mvvv 116,156,220
mvvv 112,156,188
la 108,252,50,3072
mvvv 112,152,180
mvvv 112,140,172
tv 156,229,45056
tv 158,229,47104
mvvv 112,136,164
s_waitcnt lgkmcnt(2)
mvva 0,128,196
la 100,252,50,1024
s_add_u32 s50,s50,s55
tv 140,229,36864
mvva 0,132,204
tv 136,229,32768
tv 138,229,34816
s_waitcnt vmcnt(21)
mvva 0,144,212
tv 142,229,38912
mvva 0,148,220
tv 152,229,40960
tv 154,229,43008
mvva 4,148,252
mvva 4,144,244
la 64,252,50,0
mvva 4,132,236
s_waitcnt vmcnt(21)
mvva 4,128,228
s_waitcnt vmcnt(20)
mvva 8,128,192
s_waitcnt vmcnt(19)
mvva 8,132,200
la 72,252,50,2048
s_waitcnt vmcnt(19)
mvva 8,144,208
s_waitcnt vmcnt(18)
mvva 8,148,216
s_waitcnt vmcnt(17)
mvva 12,148,248
s_waitcnt vmcnt(16)
mvva 12,144,240
la 80,250,50,0
s_waitcnt vmcnt(16)
mvva 12,132,232
tv 148,230,45056
s_waitcnt vmcnt(15)
mvva 12,128,224
tv 144,230,40960
s_waitcnt lgkmcnt(2)
mvva 28,136,224
tv 132,230,36864
mvva 28,140,232
la 88,250,50,2048
tv 128,230,32768
tv 130,230,34816
mvva 28,152,240
tv 134,230,38912
mvva 28,156,248
tv 146,230,43008
mvva 24,156,216
tv 150,230,47104
mvva 24,152,208
la 120,250,50,3072
mvva 24,140,200
mvva 24,136,192
mvva 20,136,228
mvva 20,140,236
mvva 20,152,244
la 112,250,50,1024
mvva 20,156,252
mvva 16,156,220
mvva 16,152,212
mvva 16,140,204
la 104,252,50,3072
mvva 16,136,196
tv 152,231,40960
s_waitcnt lgkmcnt(1)
mvva 32,128,196
tv 140,231,36864
tv 142,231,38912
mvva 32,132,204
tv 136,231,32768
tv 138,231,34816
mvva 32,144,212
la 96,252,50,1024
mvva 32,148,220
tv 154,231,43008
mvva 36,148,252
tv 156,231,45056
tv 158,231,47104
mvva 36,144,244
mvva 36,132,236
mvva 36,128,228
dl 246,36,51,1024
mvva 40,128,192
mvva 40,132,200
mvva 40,144,208
mvva 40,148,216
dl 249,36,51,2048
mvva 44,148,248
mvva 44,144,240
mvva 44,132,232
tv 148,228,61440
mvva 44,128,224
dl 247,36,51,3072
s_lshl_b32 s51,s98,5
s_xor_b32 s51,s65,s51
s_add_u32 m0,s52,147456
s_xor_b32 s51,s51,4194304
tv 144,228,57344
s_waitcnt lgkmcnt(2)
mvva 60,136,224
tv 132,228,53248
mvva 60,140,232
tv 128,228,49152
tv 130,228,51200
s_waitcnt vmcnt(16)
v_cmp_ne_u32_e32 vcc,4,v253
mvva 60,152,240
s_cbranch_vccnz L_spa5e
L_spra5e:
tv 134,228,55296
s_xor_b32 s50,s67,s35
mvva 60,156,248
dl 245,36,51,0
tv 146,228,59392
mvva 56,156,216
tv 150,228,63488
mvva 56,152,208
mvva 56,140,200
mvva 56,136,192
la 132,252,50,0
mvva 52,136,228
mvva 52,140,236
mvva 52,152,244
mvva 52,156,252
mvva 48,156,220
la 140,252,50,2048
s_xor_b32 s53,s60,s98
s_xor_b32 s53,s53,512
mvva 48,152,212
tv 156,229,61440
mvva 48,140,204
tv 158,229,63488
mvva 48,136,196
tv 152,229,57344
s_waitcnt lgkmcnt(3)
mvva 64,128,196
la 148,250,50,0
tv 140,229,53248
mvva 64,132,204
tv 136,229,49152
tv 138,229,51200
mvva 64,144,212
tv 142,229,55296
mvva 64,148,220
tv 154,229,59392
mvva 68,148,252
buffer_load_dword v253,v242,s[8:11],s53 offen sc1
mvva 68,144,244
mvva 68,132,236
mvva 68,128,228
mvva 72,128,192
mvva 72,132,200
la 156,250,50,2048
mvva 72,144,208
mvva 72,148,216
mvva 76,148,248
mvva 76,144,240
la 188,250,50,3072
mvva 76,132,232
tv 148,230,61440
mvva 76,128,224
tv 144,230,57344
s_waitcnt lgkmcnt(2)
mvva 92,136,224
tv 132,230,53248
mvva 92,140,232
la 180,250,50,1024
tv 128,230,49152
tv 130,230,51200
mvva 92,152,240
tv 134,230,55296
mvva 92,156,248
tv 146,230,59392
mvva 88,156,216
tv 150,230,63488
mvva 88,152,208
la 172,252,50,3072
mvva 88,140,200
mvva 88,136,192
mvva 84,136,228
mvva 84,140,236
la 164,252,50,1024
s_add_u32 s50,s50,s55
mvva 84,152,244
mvva 84,156,252
mvva 80,156,220
mvva 80,152,212
mvva 80,140,204
la 128,252,50,0
mvva 80,136,196
tv 152,231,57344
s_waitcnt lgkmcnt(1)
mvva 96,128,196
tv 140,231,53248
tv 142,231,55296
mvva 96,132,204
tv 136,231,49152
tv 138,231,51200
mvva 96,144,212
L_vs20:
s_cbranch_execz L_vs16
la 136,252,50,2048
mvva 96,148,220
tv 154,231,59392
mvva 100,148,252
tv 156,231,61440
tv 158,231,63488
mvva 100,144,244
mvva 100,132,236
la 144,250,50,0
mvva 100,128,228
mvva 104,128,192
mvva 104,132,200
mvva 104,144,208
la 152,250,50,2048
mvva 104,148,216
mvva 108,148,248
mvva 108,144,240
mvva 108,132,232
la 184,250,50,3072
mvva 108,128,224
s_barrier
s_waitcnt lgkmcnt(0)
mvva 124,136,224
tv 132,228,4096
tv 134,228,6144
tv 144,228,8192
mvva 124,140,232
tv 128,228,0
tv 130,228,2048
mvva 124,152,240
mvva 124,156,248
la 176,250,50,1024
tv 146,228,10240
mvva 120,156,216
tv 148,228,12288
tv 150,228,14336
mvva 120,152,208
mvva 120,140,200
mvva 120,136,192
la 168,252,50,3072
mvva 116,136,228
mvva 116,140,236
mvva 116,152,244
mvva 116,156,252
la 160,252,50,1024
mvva 112,156,220
mvva 112,152,212
mvva 112,140,204
tv 156,229,12288
tv 158,229,14336
mvva 112,136,196
s_waitcnt lgkmcnt(2)
mvva 0,128,68
dl 246,36,51,1024
tv 140,229,4096
mvva 0,132,76
tv 136,229,0
tv 138,229,2048
mvva 0,144,84
tv 142,229,6144
s_waitcnt vmcnt(22)
mvva 0,148,92
tv 152,229,8192
tv 154,229,10240
mvva 4,148,124
dl 249,36,51,2048
mvva 4,144,116
mvva 4,132,108
mvva 4,128,100
mvva 8,128,64
dl 247,36,51,3072
s_lshl_b32 s51,s98,5
s_xor_b32 s51,s64,s51
s_add_u32 m0,s52,0
s_xor_b32 s51,s51,4456448
mvva 8,132,72
mvva 8,144,80
mvva 8,148,88
s_waitcnt vmcnt(16)
v_cmp_ne_u32_e32 vcc,4,v253
mvva 12,148,120
dl 245,36,51,0
s_cbranch_vccnz L_spa6e
L_spra6e:
mvva 12,144,112
s_xor_b32 s50,s67,s98
mvva 12,132,104
s_xor_b32 s50,s50,131072
tv 148,230,12288
tv 150,230,14336
mvva 12,128,96
s_waitcnt lgkmcnt(2)
mvva 28,136,96
dl 246,36,51,1024
tv 132,230,4096
mvva 28,140,104
tv 128,230,0
tv 130,230,2048
mvva 28,152,112
tv 134,230,6144
mvva 28,156,120
tv 144,230,8192
tv 146,230,10240
mvva 24,156,88
mvva 24,152,80
dl 249,36,51,2048
mvva 24,140,72
s_xor_b32 s53,s60,s98
mvva 24,136,64
s_xor_b32 s53,s53,8704
mvva 20,136,100
mvva 20,140,108
dl 247,36,51,3072
mvva 20,152,116
s_lshl_b32 s51,s98,5
s_add_u32 m0,s52,16384
s_xor_b32 s51,s65,s51
mvva 20,156,124
s_xor_b32 s51,s51,4456448
mvva 16,156,92
mvva 16,152,84
buffer_load_dword v253,v242,s[8:11],s53 offen sc1
mvva 16,140,76
tv 156,231,12288
mvva 16,136,68
tv 152,231,8192
s_waitcnt lgkmcnt(2)
mvva 32,128,68
tv 140,231,4096
mvva 32,132,76
la 4,252,50,0
tv 136,231,0
tv 138,231,2048
mvva 32,144,84
tv 142,231,6144
mvva 32,148,92
tv 154,231,10240
mvva 36,148,124
tv 158,231,14336
mvva 36,144,116
la 12,252,50,2048
mvva 36,132,108
mvva 36,128,100
mvva 40,128,64
mvva 40,132,72
mvva 40,144,80
la 20,250,50,0
mvva 40,148,88
mvva 44,148,120
mvva 44,144,112
mvva 44,132,104
la 28,250,50,2048
mvva 44,128,96
tv 144,228,24576
s_waitcnt lgkmcnt(1)
mvva 60,136,96
tv 132,228,20480
tv 134,228,22528
mvva 60,140,104
tv 128,228,16384
tv 130,228,18432
mvva 60,152,112
la 60,250,50,3072
mvva 60,156,120
tv 146,228,26624
mvva 56,156,88
tv 148,228,28672
tv 150,228,30720
mvva 56,152,80
mvva 56,140,72
mvva 56,136,64
la 52,250,50,1024
mvva 52,136,100
mvva 52,140,108
mvva 52,152,116
mvva 52,156,124
la 44,252,50,3072
mvva 48,156,92
mvva 48,152,84
mvva 48,140,76
tv 156,229,28672
mvva 48,136,68
la 36,252,50,1024
s_add_u32 s50,s50,s55
tv 152,229,24576
s_waitcnt lgkmcnt(2)
mvva 64,128,68
tv 140,229,20480
mvva 64,132,76
tv 136,229,16384
tv 138,229,18432
mvva 64,144,84
tv 142,229,22528
mvva 64,148,92
la 0,252,50,0
tv 154,229,26624
mvva 68,148,124
tv 158,229,30720
mvva 68,144,116
mvva 68,132,108
mvva 68,128,100
la 8,252,50,2048
mvva 72,128,64
mvva 72,132,72
mvva 72,144,80
mvva 72,148,88
mvva 76,148,120
la 16,250,50,0
mvva 76,144,112
mvva 76,132,104
tv 148,230,28672
tv 150,230,30720
mvva 76,128,96
s_waitcnt lgkmcnt(2)
mvva 92,136,96
la 24,250,50,2048
tv 132,230,20480
mvva 92,140,104
tv 128,230,16384
tv 130,230,18432
mvva 92,152,112
tv 134,230,22528
mvva 92,156,120
tv 144,230,24576
tv 146,230,26624
mvva 88,156,88
la 56,250,50,3072
mvva 88,152,80
mvva 88,140,72
mvva 88,136,64
mvva 84,136,100
mvva 84,140,108
la 48,250,50,1024
mvva 84,152,116
mvva 84,156,124
mvva 80,156,92
mvva 80,152,84
la 40,252,50,3072
mvva 80,140,76
tv 156,231,28672
mvva 80,136,68
tv 152,231,24576
s_waitcnt lgkmcnt(2)
mvva 96,128,68
tv 140,231,20480
mvva 96,132,76
la 32,252,50,1024
tv 136,231,16384
tv 138,231,18432
mvva 96,144,84
tv 142,231,22528
mvva 96,148,92
tv 154,231,26624
mvva 100,148,124
tv 158,231,30720
mvva 100,144,116
dl 245,36,51,0
s_waitcnt vmcnt(17)
v_cmp_ne_u32_e32 vcc,4,v253
mvva 100,132,108
s_cbranch_vccnz L_spa7e
L_spra7e:
mvva 100,128,100
s_xor_b32 s50,s67,s98
mvva 104,128,64
s_xor_b32 s50,s50,139264
mvva 104,132,72
dl 246,36,51,1024
mvva 104,144,80
mvva 104,148,88
mvva 108,148,120
mvva 108,144,112
mvva 108,132,104
lv 164,252,50,0
mvva 108,128,96
s_barrier
s_waitcnt lgkmcnt(0)
mvva 124,136,96
s_xor_b32 s53,s60,s98
tv 132,224,36864
tv 134,224,38912
tv 144,224,40960
s_xor_b32 s53,s53,16896
mvva 124,140,104
tv 128,224,32768
tv 130,224,34816
mvva 124,152,112
lv 172,252,50,2048
mvva 124,156,120
tv 146,224,43008
mvva 120,156,88
tv 148,224,45056
tv 150,224,47104
mvva 120,152,80
mvva 120,140,72
buffer_load_dword v253,v242,s[8:11],s53 offen sc1
mvva 120,136,64
mvva 116,136,100
mvva 116,140,108
mvva 116,152,116
lv 180,250,50,0
mvva 116,156,124
mvva 112,156,92
mvva 112,152,84
mvva 112,140,76
lv 188,250,50,2048
mvva 112,136,68
tv 152,225,40960
s_waitcnt lgkmcnt(1)
mvva 0,128,132
tv 140,225,36864
tv 142,225,38912
mvva 0,132,140
tv 136,225,32768
tv 138,225,34816
mvva 0,144,148
mvva 0,148,156
lv 220,250,50,3072
tv 154,225,43008
mvva 4,148,188
tv 156,225,45056
tv 158,225,47104
mvva 4,144,180
mvva 4,132,172
mvva 4,128,164
lv 212,250,50,1024
mvva 8,128,128
mvva 8,132,136
mvva 8,144,144
mvva 8,148,152
lv 204,252,50,3072
mvva 12,148,184
mvva 12,144,176
mvva 12,132,168
tv 148,226,45056
tv 150,226,47104
mvva 12,128,160
s_waitcnt lgkmcnt(2)
mvva 28,136,160
lv 196,252,50,1024
s_add_u32 s50,s50,s55
tv 132,226,36864
mvva 28,140,168
tv 128,226,32768
tv 130,226,34816
mvva 28,152,176
tv 134,226,38912
mvva 28,156,184
tv 144,226,40960
tv 146,226,43008
mvva 24,156,152
lv 160,252,50,0
mvva 24,152,144
mvva 24,140,136
mvva 24,136,128
mvva 20,136,164
lv 168,252,50,2048
mvva 20,140,172
mvva 20,152,180
mvva 20,156,188
mvva 16,156,156
lv 176,250,50,0
mvva 16,152,148
mvva 16,140,140
tv 156,227,45056
tv 158,227,47104
mvva 16,136,132
s_waitcnt lgkmcnt(2)
mvva 32,128,132
lv 184,250,50,2048
tv 140,227,36864
mvva 32,132,140
tv 136,227,32768
tv 138,227,34816
mvva 32,144,148
tv 142,227,38912
mvva 32,148,156
tv 152,227,40960
tv 154,227,43008
mvva 36,148,188
mvva 36,144,180
lv 216,250,50,3072
mvva 36,132,172
mvva 36,128,164
mvva 40,128,128
mvva 40,132,136
lv 208,250,50,1024
mvva 40,144,144
mvva 40,148,152
mvva 44,148,184
mvva 44,144,176
lv 200,252,50,3072
mvva 44,132,168
tv 148,224,61440
mvva 44,128,160
tv 144,224,57344
s_waitcnt lgkmcnt(2)
mvva 60,136,160
tv 132,224,53248
mvva 60,140,168
tv 128,224,49152
tv 130,224,51200
mvva 60,152,176
lv 192,252,50,1024
tv 134,224,55296
mvva 60,156,184
tv 146,224,59392
mvva 56,156,152
tv 150,224,63488
mvva 56,152,144
mvva 56,140,136
dl 249,36,51,2048
mvva 56,136,128
mvva 52,136,164
mvva 52,140,172
mvva 52,152,180
dl 247,36,51,3072
s_lshl_b32 s51,s98,5
s_xor_b32 s51,s64,s51
s_add_u32 m0,s52,98304
s_xor_b32 s51,s51,4718592
mvva 52,156,188
mvva 48,156,156
mvva 48,152,148
mvva 48,140,140
dl 245,36,51,0
mvva 48,136,132
tv 152,225,57344
s_waitcnt vmcnt(17)
v_cmp_ne_u32_e32 vcc,4,v253
s_waitcnt lgkmcnt(1)
mvva 64,128,132
s_cbranch_vccnz L_spa8e
L_spra8e:
tv 140,225,53248
tv 142,225,55296
mvva 64,132,140
s_xor_b32 s50,s67,s98
tv 136,225,49152
tv 138,225,51200
mvva 64,144,148
dl 246,36,51,1024
mvva 64,148,156
s_xor_b32 s50,s50,147456
tv 154,225,59392
mvva 68,148,188
tv 156,225,61440
tv 158,225,63488
mvva 68,144,180
mvva 68,132,172
mvva 68,128,164
dl 249,36,51,2048
mvva 72,128,128
mvva 72,132,136
mvva 72,144,144
s_xor_b32 s53,s60,s98
mvva 72,148,152
dl 247,36,51,3072
s_xor_b32 s53,s53,25088
mvva 76,148,184
s_lshl_b32 s51,s98,5
s_add_u32 m0,s52,114688
s_xor_b32 s51,s65,s51
mvva 76,144,176
s_xor_b32 s51,s51,4718592
mvva 76,132,168
tv 148,226,61440
mvva 76,128,160
buffer_load_dword v253,v242,s[8:11],s53 offen sc1
tv 144,226,57344
s_waitcnt lgkmcnt(2)
mvva 92,136,160
tv 132,226,53248
mvva 92,140,168
tv 128,226,49152
tv 130,226,51200
mvva 92,152,176
tv 134,226,55296
mvva 92,156,184
la 196,252,50,0
tv 146,226,59392
mvva 88,156,152
tv 150,226,63488
mvva 88,152,144
mvva 88,140,136
mvva 88,136,128
la 204,252,50,2048
mvva 84,136,164
mvva 84,140,172
mvva 84,152,180
mvva 84,156,188
mvva 80,156,156
la 212,250,50,0
mvva 80,152,148
mvva 80,140,140
tv 156,227,61440
tv 158,227,63488
mvva 80,136,132
s_waitcnt lgkmcnt(2)
mvva 96,128,132
la 220,250,50,2048
tv 140,227,53248
mvva 96,132,140
tv 136,227,49152
tv 138,227,51200
mvva 96,144,148
tv 142,227,55296
mvva 96,148,156
tv 152,227,57344
tv 154,227,59392
mvva 100,148,188
la 252,250,50,3072
mvva 100,144,180
mvva 100,132,172
mvva 100,128,164
mvva 104,128,128
mvva 104,132,136
la 244,250,50,1024
mvva 104,144,144
mvva 104,148,152
mvva 108,148,184
mvva 108,144,176
la 236,252,50,3072
mvva 108,132,168
mvva 108,128,160
s_barrier
s_waitcnt lgkmcnt(0)
mvva 124,136,160
tv 132,232,4096
tv 144,232,8192
mvva 124,140,168
la 228,252,50,1024
s_add_u32 s50,s50,s55
tv 128,232,0
tv 130,232,2048
mvva 124,152,176
tv 134,232,6144
mvva 124,156,184
tv 146,232,10240
mvva 120,156,152
L_vs21:
s_cbranch_execz L_vr1
tv 148,232,12288
tv 150,232,14336
mvva 120,152,144
la 192,252,50,0
mvva 120,140,136
mvva 120,136,128
mvva 116,136,164
mvva 116,140,172
la 200,252,50,2048
mvva 116,152,180
mvva 116,156,188
mvva 112,156,156
mvva 112,152,148
mvva 112,140,140
la 208,250,50,0
mvva 112,136,132
tv 152,233,8192
s_waitcnt lgkmcnt(1)
mvva 0,128,4
tv 140,233,4096
tv 142,233,6144
mvva 0,132,12
tv 136,233,0
tv 138,233,2048
mvva 0,144,20
)"
R"(la 216,250,50,2048
mvva 0,148,28
tv 154,233,10240
mvva 4,148,60
tv 156,233,12288
tv 158,233,14336
mvva 4,144,52
mvva 4,132,44
la 248,250,50,3072
mvva 4,128,36
mvva 8,128,0
mvva 8,132,8
mvva 8,144,16
la 240,250,50,1024
mvva 8,148,24
mvva 12,148,56
mvva 12,144,48
mvva 12,132,40
la 232,252,50,3072
mvva 12,128,32
tv 144,234,8192
s_waitcnt lgkmcnt(1)
mvva 28,136,32
tv 132,234,4096
tv 134,234,6144
mvva 28,140,40
tv 128,234,0
tv 130,234,2048
mvva 28,152,48
mvva 28,156,56
la 224,252,50,1024
tv 146,234,10240
mvva 24,156,24
tv 148,234,12288
tv 150,234,14336
mvva 24,152,16
mvva 24,140,8
mvva 24,136,0
dl 245,36,51,0
mvva 20,136,36
mvva 20,140,44
s_waitcnt vmcnt(17)
v_cmp_ne_u32_e32 vcc,4,v253
mvva 20,152,52
s_cbranch_vccnz L_spa9e
L_spra9e:
mvva 20,156,60
dl 246,36,51,1024
mvva 16,156,28
s_xor_b32 s50,s67,s98
mvva 16,152,20
s_xor_b32 s50,s50,155648
mvva 16,140,12
tv 156,235,12288
tv 158,235,14336
mvva 16,136,4
s_waitcnt lgkmcnt(2)
mvva 32,128,4
dl 249,36,51,2048
tv 140,235,4096
mvva 32,132,12
tv 136,235,0
tv 138,235,2048
mvva 32,144,20
tv 142,235,6144
mvva 32,148,28
tv 152,235,8192
tv 154,235,10240
mvva 36,148,60
dl 247,36,51,3072
s_lshl_b32 s51,s98,5
s_xor_b32 s51,s64,s51
s_add_u32 m0,s52,65536
s_xor_b32 s51,s51,4980736
s_xor_b32 s53,s60,s98
mvva 36,144,52
s_xor_b32 s53,s53,25216
mvva 36,132,44
mvva 36,128,36
mvva 40,128,0
dl 245,36,51,0
mvva 40,132,8
mvva 40,144,16
mvva 40,148,24
mvva 44,148,56
buffer_load_dword v253,v242,s[8:11],s53 offen sc1
mvva 44,144,48
mvva 44,132,40
tv 148,232,28672
tv 150,232,30720
mvva 44,128,32
s_waitcnt lgkmcnt(2)
mvva 60,136,32
dl 246,36,51,1024
tv 132,232,20480
mvva 60,140,40
tv 128,232,16384
tv 130,232,18432
mvva 60,152,48
tv 134,232,22528
mvva 60,156,56
tv 144,232,24576
tv 146,232,26624
mvva 56,156,24
mvva 56,152,16
dl 249,36,51,2048
mvva 56,140,8
mvva 56,136,0
mvva 52,136,36
mvva 52,140,44
dl 247,36,51,3072
s_lshl_b32 s51,s98,5
s_xor_b32 s51,s64,s51
s_add_u32 m0,s52,81920
s_xor_b32 s51,s51,6029312
mvva 52,152,52
mvva 52,156,60
mvva 48,156,28
mvva 48,152,20
dl 245,36,51,0
mvva 48,140,12
tv 156,233,28672
mvva 48,136,4
tv 152,233,24576
s_waitcnt lgkmcnt(2)
mvva 64,128,4
tv 140,233,20480
mvva 64,132,12
tv 136,233,16384
tv 138,233,18432
mvva 64,144,20
dl 246,36,51,1024
tv 142,233,22528
mvva 64,148,28
tv 154,233,26624
mvva 68,148,60
tv 158,233,30720
mvva 68,144,52
mvva 68,132,44
dl 249,36,51,2048
mvva 68,128,36
mvva 72,128,0
mvva 72,132,8
mvva 72,144,16
dl 247,36,51,3072
mvva 72,148,24
s_lshl_b32 s51,s98,5
s_add_u32 m0,s52,32768
s_xor_b32 s51,s65,s51
mvva 76,148,56
s_xor_b32 s51,s51,4980736
mvva 76,144,48
mvva 76,132,40
la 68,252,50,0
mvva 76,128,32
tv 144,224,8192
s_waitcnt lgkmcnt(1)
mvva 92,136,32
tv 132,224,4096
tv 134,224,6144
mvva 92,140,40
tv 128,224,0
tv 130,224,2048
mvva 92,152,48
la 76,252,50,2048
mvva 92,156,56
tv 146,224,10240
mvva 88,156,24
tv 148,224,12288
tv 150,224,14336
mvva 88,152,16
mvva 88,140,8
mvva 88,136,0
la 84,250,50,0
mvva 84,136,36
mvva 84,140,44
mvva 84,152,52
mvva 84,156,60
la 92,250,50,2048
mvva 80,156,28
mvva 80,152,20
mvva 80,140,12
tv 156,225,12288
mvva 80,136,4
la 124,250,50,3072
tv 152,225,8192
s_waitcnt lgkmcnt(2)
mvvv 0,128,164
tv 140,225,4096
mvvv 0,132,172
tv 136,225,0
tv 138,225,2048
mvvv 0,144,180
tv 142,225,6144
mvvv 0,148,188
la 116,250,50,1024
tv 154,225,10240
mvvv 4,148,220
tv 158,225,14336
mvvv 4,144,212
mvvv 4,132,204
mvvv 4,128,196
la 108,252,50,3072
mvvv 8,128,160
mvvv 8,132,168
mvvv 8,144,176
mvvv 8,148,184
mvvv 12,148,216
la 100,252,50,1024
s_add_u32 s50,s50,s55
mvvv 12,144,208
mvvv 12,132,200
tv 148,226,12288
tv 150,226,14336
mvvv 12,128,192
s_barrier
tv 144,226,8192
s_waitcnt lgkmcnt(3)
mvvv 28,136,192
la 64,252,50,0
mvvv 28,140,200
tv 128,226,0
tv 130,226,2048
mvvv 28,152,208
tv 132,226,4096
tv 134,226,6144
mvvv 28,156,216
tv 146,226,10240
s_waitcnt vmcnt(16)
v_cmp_ne_u32_e32 vcc,4,v253
mvvv 24,156,184
la 72,252,50,2048
s_cbranch_vccnz L_spa10e
L_spra10e:
mvvv 24,152,176
v_and_b32_e32 v242,48,v244
v_lshlrev_b32_e32 v242,3,v242
mvvv 24,140,168
mvvv 24,136,160
mvvv 20,136,196
mvvv 20,140,204
la 80,250,50,0
mvvv 20,152,212
mvvv 20,156,220
mvvv 16,156,188
s_andn2_b32 s53,s61,384
mvvv 16,152,180
la 88,250,50,2048
mvvv 16,140,172
tv 156,227,12288
mvvv 16,136,164
tv 152,227,8192
s_waitcnt lgkmcnt(2)
mvvv 32,128,164
tv 140,227,4096
mvvv 32,132,172
buffer_load_dword v253,v242,s[8:11],s53 offen sc1
tv 136,227,0
tv 138,227,2048
mvvv 32,144,180
tv 142,227,6144
mvvv 32,148,188
tv 154,227,10240
mvvv 36,148,220
tv 158,227,14336
mvvv 36,144,212
la 120,250,50,3072
mvvv 36,132,204
mvvv 36,128,196
mvvv 40,128,160
mvvv 40,132,168
la 112,250,50,1024
mvvv 40,144,176
mvvv 40,148,184
mvvv 44,148,216
mvvv 44,144,208
mvvv 44,132,200
la 104,252,50,3072
mvvv 44,128,192
tv 144,224,24576
s_waitcnt lgkmcnt(1)
mvvv 60,136,192
tv 132,224,20480
tv 134,224,22528
mvvv 60,140,200
tv 128,224,16384
tv 130,224,18432
mvvv 60,152,208
la 96,252,50,1024
s_xor_b32 s50,s67,s98
s_xor_b32 s50,s50,188416
mvvv 60,156,216
tv 146,224,26624
mvvv 56,156,184
tv 148,224,28672
tv 150,224,30720
mvvv 56,152,176
mvvv 56,140,168
la 132,252,50,0
mvvv 56,136,160
mvvv 52,136,196
mvvv 52,140,204
mvvv 52,152,212
la 140,252,50,2048
mvvv 52,156,220
mvvv 48,156,188
mvvv 48,152,180
mvvv 48,140,172
la 148,250,50,0
mvvv 48,136,164
tv 152,225,24576
s_waitcnt lgkmcnt(1)
mvvv 64,128,164
tv 140,225,20480
tv 142,225,22528
mvvv 64,132,172
tv 136,225,16384
tv 138,225,18432
mvvv 64,144,180
mvvv 64,148,188
la 156,250,50,2048
tv 154,225,26624
mvvv 68,148,220
tv 156,225,28672
tv 158,225,30720
mvvv 68,144,212
mvvv 68,132,204
mvvv 68,128,196
la 188,250,50,3072
mvvv 72,128,160
mvvv 72,132,168
mvvv 72,144,176
mvvv 72,148,184
la 180,250,50,1024
mvvv 76,148,216
mvvv 76,144,208
mvvv 76,132,200
tv 148,228,45056
tv 150,228,47104
mvvv 76,128,192
s_waitcnt lgkmcnt(2)
mvvv 92,136,192
la 172,252,50,3072
tv 132,228,36864
mvvv 92,140,200
tv 128,228,32768
tv 130,228,34816
mvvv 92,152,208
tv 134,228,38912
mvvv 92,156,216
tv 144,228,40960
tv 146,228,43008
mvvv 88,156,184
la 164,252,50,1024
s_add_u32 s50,s50,s55
mvvv 88,152,176
mvvv 88,140,168
mvvv 88,136,160
mvvv 84,136,196
la 128,252,50,0
mvvv 84,140,204
mvvv 84,152,212
mvvv 84,156,220
mvvv 80,156,188
la 136,252,50,2048
mvvv 80,152,180
mvvv 80,140,172
tv 156,229,45056
tv 158,229,47104
mvvv 80,136,164
s_waitcnt lgkmcnt(2)
mvva 0,128,196
la 144,250,50,0
tv 140,229,36864
mvva 0,132,204
tv 136,229,32768
tv 138,229,34816
mvva 0,144,212
tv 142,229,38912
mvva 0,148,220
tv 152,229,40960
tv 154,229,43008
mvva 4,148,252
mvva 4,144,244
la 152,250,50,2048
mvva 4,132,236
mvva 4,128,228
mvva 8,128,192
mvva 8,132,200
la 184,250,50,3072
mvva 8,144,208
mvva 8,148,216
s_waitcnt vmcnt(17)
v_cmp_ne_u32_e32 vcc,4,v253
mvva 12,148,248
s_cbranch_vccnz L_sppe
L_sprpe:
mvva 12,144,240
la 176,250,50,1024
mvva 12,132,232
tv 148,230,45056
mvva 12,128,224
tv 144,230,40960
s_waitcnt lgkmcnt(2)
mvva 28,136,224
tv 132,230,36864
mvva 28,140,232
tv 128,230,32768
tv 130,230,34816
mvva 28,152,240
la 168,252,50,3072
tv 134,230,38912
mvva 28,156,248
tv 146,230,43008
mvva 24,156,216
tv 150,230,47104
mvva 24,152,208
mvva 24,140,200
la 160,252,50,1024
mvva 24,136,192
mvva 20,136,228
mvva 20,140,236
mvva 20,152,244
dl 245,36,51,0
mvva 20,156,252
mvva 16,156,220
mvva 16,152,212
mvva 16,140,204
dl 246,36,51,1024
mvva 16,136,196
tv 152,231,40960
s_waitcnt lgkmcnt(1)
mvva 32,128,196
tv 140,231,36864
tv 142,231,38912
mvva 32,132,204
tv 136,231,32768
tv 138,231,34816
mvva 32,144,212
dl 249,36,51,2048
mvva 32,148,220
tv 154,231,43008
mvva 36,148,252
tv 156,231,45056
tv 158,231,47104
mvva 36,144,244
mvva 36,132,236
mvva 36,128,228
dl 247,36,51,3072
s_lshl_b32 s51,s98,5
s_xor_b32 s51,s65,s51
s_add_u32 m0,s52,49152
s_xor_b32 s51,s51,6029312
mvva 40,128,192
mvva 40,132,200
mvva 40,144,208
mvva 40,148,216
dl 245,36,51,0
mvva 44,148,248
mvva 44,144,240
mvva 44,132,232
mvva 44,128,224
dl 246,36,51,1024
s_waitcnt lgkmcnt(0)
mvva 60,136,224
s_barrier
mvva 60,140,232
tv 144,228,57344
tv 146,228,59392
tv 128,228,49152
tv 130,228,51200
mvva 60,152,240
tv 132,228,53248
tv 134,228,55296
mvva 60,156,248
dl 249,36,51,2048
mvva 56,156,216
tv 148,228,61440
tv 150,228,63488
mvva 56,152,208
mvva 56,140,200
mvva 56,136,192
dl 247,36,51,3072
mvva 52,136,228
mvva 52,140,236
mvva 52,152,244
mvva 52,156,252
mvva 48,156,220
mvva 48,152,212
mvva 48,140,204
tv 156,229,61440
mvva 48,136,196
tv 152,229,57344
s_waitcnt lgkmcnt(2)
mvva 64,128,196
tv 140,229,53248
mvva 64,132,204
tv 136,229,49152
tv 138,229,51200
mvva 64,144,212
tv 142,229,55296
mvva 64,148,220
tv 154,229,59392
mvva 68,148,252
tv 158,229,63488
mvva 68,144,244
mvva 68,132,236
mvva 68,128,228
mvva 72,128,192
mvva 72,132,200
L_vs22:
s_cbranch_execz L_vr1
mvva 72,144,208
mvva 72,148,216
mvva 76,148,248
mvva 76,144,240
mvva 76,132,232
tv 148,228,12288
mvva 76,128,224
tv 144,228,8192
s_waitcnt lgkmcnt(2)
mvva 92,136,224
tv 132,228,4096
mvva 92,140,232
tv 128,228,0
tv 130,228,2048
mvva 92,152,240
tv 134,228,6144
mvva 92,156,248
tv 146,228,10240
mvva 88,156,216
tv 150,228,14336
mvva 88,152,208
mvva 88,140,200
mvva 88,136,192
mvva 84,136,228
mvva 84,140,236
mvva 84,152,244
mvva 84,156,252
mvva 80,156,220
mvva 80,152,212
mvva 80,140,204
tv 156,229,12288
mvva 80,136,196
tv 152,229,8192
s_waitcnt lgkmcnt(2)
mvva 0,128,68
tv 140,229,4096
mvva 0,132,76
tv 136,229,0
tv 138,229,2048
mvva 0,144,84
tv 142,229,6144
mvva 0,148,92
tv 154,229,10240
mvva 4,148,124
tv 158,229,14336
mvva 4,144,116
mvva 4,132,108
mvva 4,128,100
mvva 8,128,64
mvva 8,132,72
mvva 8,144,80
mvva 8,148,88
s_waitcnt vmcnt(10)
mvva 12,148,120
mvva 12,144,112
mvva 12,132,104
tv 148,228,28672
mvva 12,128,96
tv 144,228,24576
s_waitcnt lgkmcnt(2)
mvva 28,136,96
tv 132,228,20480
mvva 28,140,104
tv 128,228,16384
tv 130,228,18432
mvva 28,152,112
tv 134,228,22528
mvva 28,156,120
tv 146,228,26624
mvva 24,156,88
tv 150,228,30720
mvva 24,152,80
mvva 24,140,72
mvva 24,136,64
mvva 20,136,100
mvva 20,140,108
mvva 20,152,116
mvva 20,156,124
mvva 16,156,92
mvva 16,152,84
mvva 16,140,76
tv 156,229,28672
mvva 16,136,68
tv 152,229,24576
s_waitcnt lgkmcnt(2)
mvva 0,128,132
tv 140,229,20480
mvva 0,132,140
tv 136,229,16384
tv 138,229,18432
mvva 0,144,148
tv 142,229,22528
mvva 0,148,156
tv 154,229,26624
mvva 4,148,188
tv 158,229,30720
mvva 4,144,180
mvva 4,132,172
mvva 4,128,164
mvva 8,128,128
mvva 8,132,136
cv 0,0,1
cv 1,2,3
mvva 8,144,144
mvva 8,148,152
mvva 12,148,184
mvva 12,144,176
cv 4,4,5
cv 5,6,7
s_waitcnt vmcnt(8)
mvva 12,132,168
tv 148,230,12288
mvva 12,128,160
tv 144,230,8192
cv 8,8,9
s_waitcnt lgkmcnt(2)
mvva 28,136,160
tv 132,230,4096
cv 9,10,11
mvva 28,140,168
tv 128,230,0
tv 130,230,2048
mvva 28,152,176
tv 134,230,6144
cv 12,12,13
mvva 28,156,184
tv 146,230,10240
cv 13,14,15
mvva 24,156,152
tv 150,230,14336
mvva 24,152,144
mvva 24,140,136
mvva 24,136,128
mvva 20,136,164
cv 14,28,29
cv 15,30,31
mvva 20,140,172
buffer_store_dwordx4 v[12:15],v244,s[8:11],s56 offen offset:3072 sc0 sc1
mvva 20,152,180
mvva 20,156,188
mvva 16,156,156
cv 10,24,25
cv 11,26,27
mvva 16,152,148
buffer_store_dwordx4 v[8:11],v244,s[8:11],s56 offen offset:2048 sc0 sc1
mvva 16,140,140
tv 156,231,12288
mvva 16,136,132
cv 6,20,21
cv 7,22,23
s_waitcnt lgkmcnt(1)
mvva 32,128,68
tv 140,231,4096
mvva 32,132,76
tv 136,231,0
tv 138,231,2048
mvva 32,144,84
buffer_store_dwordx4 v[4:7],v244,s[8:11],s56 offen offset:1024 sc0 sc1
tv 142,231,6144
mvva 32,148,92
tv 152,231,8192
tv 154,231,10240
mvva 36,148,124
tv 158,231,14336
cv 2,16,17
cv 3,18,19
mvva 36,144,116
mvva 36,132,108
buffer_store_dwordx4 v[0:3],v244,s[8:11],s56 offen offset:0 sc0 sc1
mvva 36,128,100
mvva 40,128,64
mvva 40,132,72
mvva 40,144,80
mvva 40,148,88
mvva 44,148,120
mvva 44,144,112
mvva 44,132,104
tv 148,230,28672
mvva 44,128,96
s_waitcnt vmcnt(4)
s_barrier
tv 144,230,24576
s_waitcnt lgkmcnt(2)
mvva 60,136,96
tv 132,230,20480
mvva 60,140,104
tv 128,230,16384
tv 130,230,18432
mvva 60,152,112
tv 134,230,22528
mvva 60,156,120
tv 146,230,26624
mvva 56,156,88
tv 150,230,30720
mvva 56,152,80
mvva 56,140,72
mvva 56,136,64
mvva 52,136,100
mvva 52,140,108
mvva 52,152,116
mvva 52,156,124
mvva 48,156,92
mvva 48,152,84
mvva 48,140,76
tv 156,231,28672
mvva 48,136,68
tv 152,231,24576
s_waitcnt lgkmcnt(2)
mvva 32,128,132
tv 140,231,20480
mvva 32,132,140
tv 136,231,16384
tv 138,231,18432
mvva 32,144,148
tv 142,231,22528
mvva 32,148,156
tv 154,231,26624
mvva 36,148,188
tv 158,231,30720
mvva 36,144,180
mvva 36,132,172
mvva 36,128,164
mvva 40,128,128
mvva 40,132,136
cv 32,32,33
cv 33,34,35
mvva 40,144,144
mvva 40,148,152
mvva 44,148,184
mvva 44,144,176
cv 36,36,37
cv 37,38,39
mvva 44,132,168
tv 148,224,45056
mvva 44,128,160
tv 144,224,40960
cv 40,40,41
s_waitcnt lgkmcnt(2)
mvva 60,136,160
tv 132,224,36864
cv 41,42,43
mvva 60,140,168
tv 128,224,32768
tv 130,224,34816
mvva 60,152,176
tv 134,224,38912
cv 44,44,45
mvva 60,156,184
tv 146,224,43008
cv 45,46,47
mvva 56,156,152
tv 150,224,47104
mvva 56,152,144
mvva 56,140,136
mvva 56,136,128
mvva 52,136,164
cv 46,60,61
cv 47,62,63
mvva 52,140,172
buffer_store_dwordx4 v[44:47],v244,s[8:11],s57 offen offset:3072 sc0 sc1
mvva 52,152,180
mvva 52,156,188
mvva 48,156,156
cv 42,56,57
cv 43,58,59
mvva 48,152,148
buffer_store_dwordx4 v[40:43],v244,s[8:11],s57 offen offset:2048 sc0 sc1
mvva 48,140,140
tv 156,225,45056
mvva 48,136,132
cv 38,52,53
cv 39,54,55
s_waitcnt lgkmcnt(1)
mvva 64,128,68
tv 140,225,36864
mvva 64,132,76
buffer_store_dwordx4 v[36:39],v244,s[8:11],s57 offen offset:1024 sc0 sc1
tv 136,225,32768
tv 138,225,34816
mvva 64,144,84
tv 142,225,38912
cv 34,48,49
mvva 64,148,92
tv 152,225,40960
tv 154,225,43008
mvva 68,148,124
tv 158,225,47104
cv 35,50,51
mvva 68,144,116
buffer_store_dwordx4 v[32:35],v244,s[8:11],s57 offen offset:0 sc0 sc1
mvva 68,132,108
mvva 68,128,100
mvva 72,128,64
mvva 72,132,72
mvva 72,144,80
mvva 72,148,88
mvva 76,148,120
mvva 76,144,112
mvva 76,132,104
tv 148,224,61440
mvva 76,128,96
tv 144,224,57344
s_waitcnt lgkmcnt(2)
mvva 92,136,96
tv 132,224,53248
mvva 92,140,104
tv 128,224,49152
tv 130,224,51200
mvva 92,152,112
tv 134,224,55296
mvva 92,156,120
tv 146,224,59392
mvva 88,156,88
tv 150,224,63488
mvva 88,152,80
mvva 88,140,72
mvva 88,136,64
mvva 84,136,100
mvva 84,140,108
mvva 84,152,116
mvva 84,156,124
mvva 80,156,92
mvva 80,152,84
mvva 80,140,76
tv 156,225,61440
mvva 80,136,68
tv 152,225,57344
s_waitcnt lgkmcnt(2)
mvva 64,128,132
tv 140,225,53248
mvva 64,132,140
tv 136,225,49152
tv 138,225,51200
mvva 64,144,148
tv 142,225,55296
mvva 64,148,156
tv 154,225,59392
mvva 68,148,188
tv 158,225,63488
mvva 68,144,180
mvva 68,132,172
mvva 68,128,164
s_xor_b32 s82,s59,12
mvva 72,128,128
s_mul_i32 s82,s82,12288
cv 64,64,65
s_add_u32 s82,s82,0
mvva 72,132,136
lv 16,244,82,0
mvva 72,144,144
cv 65,66,67
cv 68,68,69
mvva 72,148,152
cv 69,70,71
mvva 76,148,184
mvva 76,144,176
mvva 76,132,168
lv 20,244,82,1024
mvva 76,128,160
tv 144,234,24576
s_waitcnt lgkmcnt(1)
mvva 92,136,160
tv 132,234,20480
tv 134,234,22528
mvva 92,140,168
tv 128,234,16384
tv 130,234,18432
mvva 92,152,176
cv 72,72,73
cv 73,74,75
mvva 92,156,184
lv 24,244,82,2048
tv 146,234,26624
mvva 88,156,152
tv 148,234,28672
tv 150,234,30720
mvva 88,152,144
cv 76,76,77
cv 77,78,79
mvva 88,140,136
cv 78,92,93
cv 79,94,95
mvva 88,136,128
mvva 84,136,164
lv 28,244,82,3072
mvva 84,140,172
mvva 84,152,180
mvva 84,156,188
mvva 80,156,156
buffer_store_dwordx4 v[76:79],v244,s[8:11],s58 offen offset:3072 sc0 sc1
mvva 80,152,148
cv 74,88,89
cv 75,90,91
mvva 80,140,140
tv 156,235,28672
mvva 80,136,132
cv 70,84,85
cv 71,86,87
s_waitcnt lgkmcnt(1)
mvva 96,128,4
buffer_store_dwordx4 v[72:75],v244,s[8:11],s58 offen offset:2048 sc0 sc1
tv 140,235,20480
mvva 96,132,12
tv 136,235,16384
tv 138,235,18432
mvva 96,144,20
tv 142,235,22528
cv 66,80,81
mvva 96,148,28
tv 152,235,24576
tv 154,235,26624
mvva 100,148,60
tv 158,235,30720
cv 67,82,83
mvva 100,144,52
buffer_store_dwordx4 v[68:71],v244,s[8:11],s58 offen offset:1024 sc0 sc1
mvva 100,132,44
mvva 100,128,36
mvva 104,128,0
mvva 104,132,8
buffer_store_dwordx4 v[64:67],v244,s[8:11],s58 offen offset:0 sc0 sc1
mvva 104,144,16
mvva 104,148,24
mvva 108,148,56
mvva 108,144,48
mvva 108,132,40
tv 148,226,28672
mvva 108,128,32
tv 144,226,24576
s_waitcnt lgkmcnt(2)
mvva 124,136,32
tv 132,226,20480
mvva 124,140,40
tv 128,226,16384
tv 130,226,18432
mvva 124,152,48
tv 134,226,22528
mvva 124,156,56
tv 146,226,26624
mvva 120,156,24
tv 150,226,30720
mvva 120,152,16
mvva 120,140,8
mvva 120,136,0
mvva 116,136,36
mvva 116,140,44
s_waitcnt vmcnt(7)
v_max_u32_e32 v248,v16,v17
mvva 116,152,52
v_max3_u32 v248,v248,v18,v19
s_waitcnt vmcnt(6)
v_max3_u32 v248,v248,v20,v21
v_max3_u32 v248,v248,v22,v23
mvva 116,156,60
s_waitcnt vmcnt(5)
v_max3_u32 v248,v248,v24,v25
v_max3_u32 v248,v248,v26,v27
s_waitcnt vmcnt(4)
v_max3_u32 v248,v248,v28,v29
mvva 112,156,28
v_max3_u32 v248,v248,v30,v31
v_cmp_eq_u32_e32 vcc,-1,v248
mvva 112,152,20
s_cbranch_vccnz L_spx0e
L_sprx0e:
mvva 112,140,12
tv 156,227,28672
tv 158,227,30720
v_lshlrev_b32_e32 v251,16,v18
v_and_b32_e32 v255,4294901760,v18
mvva 112,136,4
tv 152,227,24576
tv 154,227,26624
s_waitcnt lgkmcnt(4)
mvvv 96,128,164
v_lshlrev_b32_e32 v253,16,v19
v_and_b32_e32 v242,4294901760,v19
mvvv 96,132,172
tv 136,227,16384
tv 138,227,18432
mvvv 96,144,180
tv 140,227,20480
tv 142,227,22528
v_add_f32_e32 v112,v112,v251
mvvv 96,148,188
v_add_f32_e32 v113,v113,v255
v_add_f32_e32 v114,v114,v253
mvvv 100,148,220
)"
R"(v_add_f32_e32 v115,v115,v242
v_lshlrev_b32_e32 v251,16,v22
mvvv 100,144,212
v_and_b32_e32 v255,4294901760,v22
v_lshlrev_b32_e32 v253,16,v23
mvvv 100,132,204
v_and_b32_e32 v242,4294901760,v23
v_add_f32_e32 v116,v116,v251
v_add_f32_e32 v117,v117,v255
mvvv 100,128,196
v_add_f32_e32 v118,v118,v253
v_add_f32_e32 v119,v119,v242
mvvv 104,128,160
v_lshlrev_b32_e32 v251,16,v26
v_and_b32_e32 v255,4294901760,v26
mvvv 104,132,168
v_lshlrev_b32_e32 v253,16,v27
v_and_b32_e32 v242,4294901760,v27
mvvv 104,144,176
v_add_f32_e32 v120,v120,v251
v_add_f32_e32 v121,v121,v255
v_add_f32_e32 v122,v122,v253
mvvv 104,148,184
v_add_f32_e32 v123,v123,v242
v_lshlrev_b32_e32 v251,16,v30
mvvv 108,148,216
v_and_b32_e32 v255,4294901760,v30
v_lshlrev_b32_e32 v253,16,v31
mvvv 108,144,208
v_and_b32_e32 v242,4294901760,v31
v_add_f32_e32 v124,v124,v251
mvvv 108,132,200
v_add_f32_e32 v125,v125,v255
v_add_f32_e32 v126,v126,v253
v_add_f32_e32 v127,v127,v242
mvvv 108,128,192
tv 144,230,57344
v_lshlrev_b32_e32 v251,16,v16
s_waitcnt lgkmcnt(1)
mvvv 124,136,192
tv 132,230,53248
v_and_b32_e32 v255,4294901760,v16
mvvv 124,140,200
tv 128,230,49152
tv 130,230,51200
mvvv 124,152,208
tv 134,230,55296
v_lshlrev_b32_e32 v253,16,v17
mvvv 124,156,216
tv 146,230,59392
v_and_b32_e32 v242,4294901760,v17
mvvv 120,156,184
L_vs23:
s_cbranch_execz L_vr1
tv 148,230,61440
tv 150,230,63488
mvvv 120,152,176
v_add_f32_e32 v96,v96,v251
v_add_f32_e32 v97,v97,v255
mvvv 120,140,168
v_add_f32_e32 v98,v98,v253
v_add_f32_e32 v99,v99,v242
mvvv 120,136,160
v_lshlrev_b32_e32 v251,16,v20
v_and_b32_e32 v255,4294901760,v20
mvvv 116,136,196
v_lshlrev_b32_e32 v253,16,v21
v_and_b32_e32 v242,4294901760,v21
mvvv 116,140,204
v_add_f32_e32 v100,v100,v251
v_add_f32_e32 v101,v101,v255
mvvv 116,152,212
v_add_f32_e32 v102,v102,v253
v_add_f32_e32 v103,v103,v242
mvvv 116,156,220
v_lshlrev_b32_e32 v251,16,v24
v_and_b32_e32 v255,4294901760,v24
mvvv 112,156,188
v_lshlrev_b32_e32 v253,16,v25
v_and_b32_e32 v242,4294901760,v25
mvvv 112,152,180
v_add_f32_e32 v104,v104,v251
v_add_f32_e32 v105,v105,v255
mvvv 112,140,172
tv 156,231,61440
v_add_f32_e32 v106,v106,v253
mvvv 112,136,164
tv 152,231,57344
v_add_f32_e32 v107,v107,v242
s_waitcnt lgkmcnt(2)
mvva 96,128,196
tv 140,231,53248
v_lshlrev_b32_e32 v251,16,v28
mvva 96,132,204
tv 136,231,49152
tv 138,231,51200
mvva 96,144,212
tv 142,231,55296
v_and_b32_e32 v255,4294901760,v28
mvva 96,148,220
tv 154,231,59392
v_lshlrev_b32_e32 v253,16,v29
mvva 100,148,252
tv 158,231,63488
v_and_b32_e32 v242,4294901760,v29
mvva 100,144,244
v_add_f32_e32 v108,v108,v251
v_add_f32_e32 v109,v109,v255
mvva 100,132,236
v_add_f32_e32 v110,v110,v253
v_add_f32_e32 v111,v111,v242
mvva 100,128,228
mvva 104,128,192
mvva 104,132,200
mvva 104,144,208
mvva 104,148,216
mvva 108,148,248
mvva 108,144,240
mvva 108,132,232
tv 148,226,45056
mvva 108,128,224
tv 144,226,40960
s_waitcnt lgkmcnt(2)
mvva 124,136,224
s_xor_b32 s83,s59,8
tv 132,226,36864
s_mul_i32 s83,s83,12288
mvva 124,140,232
s_add_u32 s83,s83,4096
tv 128,226,32768
tv 130,226,34816
mvva 124,152,240
lv 48,244,83,0
tv 134,226,38912
mvva 124,156,248
tv 146,226,43008
mvva 120,156,216
lv 52,244,83,1024
tv 150,226,47104
mvva 120,152,208
mvva 120,140,200
lv 56,244,83,2048
mvva 120,136,192
mvva 116,136,228
lv 60,244,83,3072
mvva 116,140,236
mvva 116,152,244
s_xor_b32 s84,s59,4
mvva 116,156,252
s_mul_i32 s84,s84,12288
mvva 112,156,220
s_add_u32 s84,s84,8192
mvva 112,152,212
lv 80,244,84,0
mvva 112,140,204
tv 156,227,45056
mvva 112,136,196
lv 84,244,84,1024
tv 152,227,40960
s_waitcnt lgkmcnt(2)
mvva 96,128,68
tv 140,227,36864
mvva 96,132,76
lv 88,244,84,2048
tv 136,227,32768
tv 138,227,34816
mvva 96,144,84
tv 142,227,38912
mvva 96,148,92
lv 92,244,84,3072
tv 154,227,43008
mvva 100,148,124
tv 158,227,47104
mvva 100,144,116
mvva 100,132,108
mvva 100,128,100
mvva 104,128,64
mvva 104,132,72
mvva 104,144,80
mvva 104,148,88
mvva 108,148,120
mvva 108,144,112
mvva 108,132,104
tv 148,226,61440
mvva 108,128,96
tv 144,226,57344
s_waitcnt lgkmcnt(2)
mvva 124,136,96
tv 132,226,53248
mvva 124,140,104
tv 128,226,49152
tv 130,226,51200
mvva 124,152,112
tv 134,226,55296
mvva 124,156,120
tv 146,226,59392
mvva 120,156,88
tv 150,226,63488
mvva 120,152,80
mvva 120,140,72
mvva 120,136,64
mvva 116,136,100
mvva 116,140,108
mvva 116,152,116
mvva 116,156,124
mvva 112,156,92
mvva 112,152,84
mvva 112,140,76
tv 156,227,61440
mvva 112,136,68
tv 152,227,57344
s_waitcnt lgkmcnt(2)
mvva 96,128,132
tv 140,227,53248
mvva 96,132,140
tv 136,227,49152
tv 138,227,51200
mvva 96,144,148
tv 142,227,55296
mvva 96,148,156
tv 154,227,59392
mvva 100,148,188
tv 158,227,63488
mvva 100,144,180
mvva 100,132,172
mvva 100,128,164
mvva 104,128,128
mvva 104,132,136
mvva 104,144,144
mvva 104,148,152
s_waitcnt vmcnt(7)
v_max_u32_e32 v248,v48,v49
mvva 108,148,184
v_max3_u32 v248,v248,v50,v51
s_waitcnt vmcnt(6)
v_max3_u32 v248,v248,v52,v53
mvva 108,144,176
v_max3_u32 v248,v248,v54,v55
s_waitcnt vmcnt(5)
v_max3_u32 v248,v248,v56,v57
mvva 108,132,168
v_max3_u32 v248,v248,v58,v59
s_waitcnt vmcnt(4)
v_max3_u32 v248,v248,v60,v61
mvva 108,128,160
v_max3_u32 v248,v248,v62,v63
v_cmp_eq_u32_e32 vcc,-1,v248
s_waitcnt lgkmcnt(0)
mvva 124,136,160
s_cbranch_vccnz L_spx1e
L_sprx1e:
mvva 124,140,168
mvva 124,152,176
mvva 124,156,184
s_waitcnt vmcnt(3)
v_max_u32_e32 v248,v80,v81
mvva 120,156,152
v_max3_u32 v248,v248,v82,v83
s_waitcnt vmcnt(2)
v_max3_u32 v248,v248,v84,v85
mvva 120,152,144
v_max3_u32 v248,v248,v86,v87
s_waitcnt vmcnt(1)
v_max3_u32 v248,v248,v88,v89
mvva 120,140,136
v_max3_u32 v248,v248,v90,v91
s_waitcnt vmcnt(0)
v_max3_u32 v248,v248,v92,v93
mvva 120,136,128
v_max3_u32 v248,v248,v94,v95
v_cmp_eq_u32_e32 vcc,-1,v248
mvva 116,136,164
s_cbranch_vccnz L_spx2e
L_sprx2e:
mvva 116,140,172
mvva 116,152,180
mvva 116,156,188
mvva 112,156,156
mvva 112,152,148
mvva 112,140,140
mvva 112,136,132
mvvv 108,238,60
mvvv 108,238,92
mvvv 124,236,60
mvvv 124,236,92
mvvv 104,238,56
mvvv 104,238,88
mvvv 120,236,56
mvvv 120,236,88
mvvv 100,238,52
mvvv 100,238,84
cv 108,108,109
cv 109,110,111
cv 110,124,125
cv 111,126,127
cv 104,104,105
v_permlane16_swap_b32_e32 v108,v110
mvvv 116,236,52
v_permlane16_swap_b32_e32 v109,v111
cv 105,106,107
cv 106,120,121
cv 107,122,123
mvvv 116,236,84
buffer_store_dwordx4 v[108:111],v243,s[44:47],s73 offen sc0 sc1
v_permlane16_swap_b32_e32 v104,v106
v_permlane16_swap_b32_e32 v105,v107
mvvv 96,238,48
cv 100,100,101
cv 101,102,103
mvvv 96,238,80
buffer_store_dwordx4 v[104:107],v243,s[44:47],s72 offen sc0 sc1
cv 102,116,117
cv 103,118,119
mvvv 112,236,48
v_permlane16_swap_b32_e32 v100,v102
v_permlane16_swap_b32_e32 v101,v103
mvvv 112,236,80
buffer_store_dwordx4 v[100:103],v243,s[44:47],s71 offen sc0 sc1
cv 96,96,97
cv 97,98,99
s_nop 4
cv 98,112,113
cv 99,114,115
s_nop 0
v_permlane16_swap_b32_e32 v96,v98
v_permlane16_swap_b32_e32 v97,v99
buffer_store_dwordx4 v[96:99],v243,s[44:47],s70 offen sc0 sc1
s_endpgm
L_odd:
L_vs24:
s_cbranch_execz L_vs28
s_lshl_b32 s86,s21,2
s_sub_u32 s87,s13,s67
s_add_u32 s86,s86,s12
mvaa 64,200,132
s_lshl_b32 s86,s86,8
v_add_u32_e32 v252,s87,v244
s_add_u32 s86,s86,L_odd-L_att_pc+1024
v_lshl_add_u32 v226,v244,3,s86
s_lshl_b32 s86,s12,1
s_bfe_u32 s87,s21,65539
mvaa 64,208,136
s_add_u32 s86,s86,s87
v_min_u32_e32 v226,L_att_end-L_att_pc-4,v226
s_lshl_b32 s86,s86,7
s_add_u32 s86,s86,L_pend2-L_att_pc
v_writelane_b32 v226,s86,2
mvaa 64,216,140
s_mov_b64 exec,7
global_load_dword v253,v226,s[96:97]
s_mov_b64 exec,-1
cvaa 68,220,140,232
s_add_u32 m0,s52,32768
v_and_b32_e32 v224,16,v244
v_lshlrev_b32_e32 v224,5,v224
mvaa 68,212,136
s_xor_b32 s51,s62,0
v_lshrrev_b32_e32 v239,4,v244
v_lshrrev_b32_e32 v236,3,v239
mvaa 68,204,132
s_lshl_b32 s87,s12,13
v_and_b32_e32 v237,112,v244
v_and_b32_e32 v238,48,v239
mvaa 68,196,128
s_xor_b32 s87,s87,8192
ra 64,225,0
v_lshlrev_b32_e32 v238,1,v238
cvaa 96,224,128,232
buffer_load_dword v253,v224,s[8:11],s53 offen sc1
mvaa 96,232,132
s_mov_b32 s84,7
ra 68,225,1024
v_xor_b32_e32 v237,v237,v238
mvaa 96,240,136
s_add_u32 s81,s81,1
ra 72,225,2048
v_lshl_add_u32 v245,v236,11,v237
mvaa 96,248,140
s_mov_b32 s84,5
v_add_u32_e32 v246,15360,v245
v_add_u32_e32 v249,30720,v245
cvaa 100,252,140,232
s_andn2_b32 s69,64,s54
ra 76,225,3072
s_waitcnt vmcnt(9)
v_add_u32_e32 v247,46080,v245
mvaa 100,244,136
dl 245,24,51,0
mvaa 100,236,132
s_add_u32 s88,s14,4096
ra 80,225,4096
cv 8,8,9
mvaa 100,228,128
s_lshl_b32 s35,s19,1
ra 84,225,5120
cv 9,10,11
cvaa 72,192,144,232
s_lshr_b32 s33,40569,s35
ra 88,225,6144
cv 10,12,13
mvaa 72,200,148
s_and_b32 s33,s33,3
ra 92,225,7168
v_add_u32_e32 v250,4096,v252
mvaa 72,208,152
dl 246,24,51,1024
mvaa 72,216,156
s_lshl_b32 s33,s33,13
ra 96,225,8192
v_mov_b32_e32 v251,4
cvaa 76,220,156,232
s_lshr_b32 s34,59294,s35
ra 100,225,9216
s_and_b32 s34,s34,3
mvaa 76,212,152
s_lshl_b32 s34,s34,13
ra 104,225,10240
s_xor_b32 s35,s33,s34
mvaa 76,204,148
s_and_b32 s98,s19,3
ra 108,225,11264
s_lshl_b32 s98,s98,13
mvaa 76,196,144
dl 249,24,51,2048
cvaa 104,224,144,232
s_mov_b32 s74,0x300003
ra 112,225,12288
s_mov_b32 s75,0x30000300
mvaa 104,232,148
s_lshl_b32 s71,s12,15
ra 116,225,13312
s_add_u32 s71,s71,s54
mvaa 104,240,152
s_mov_b64 s[44:45],s[30:31]
ra 120,225,14336
s_mov_b32 s46,4194304
mvaa 104,248,156
s_mov_b32 s47,131072
ex 14
cvaa 108,252,156,232
dl 247,24,51,3072
mvaa 108,244,152
s_add_u32 m0,s52,49152
s_xor_b32 s51,s63,0
ra 124,225,15360
mvaa 108,236,148
s_lshl_b32 s70,s23,11
ex 15
mvaa 108,228,144
s_add_u32 s70,s70,s71
ex 40
cvaa 80,192,160,232
ex 41
mvaa 80,200,164
dl 245,24,51,0
mvaa 80,208,168
ex 42
mvaa 80,216,172
ex 43
cvaa 84,220,172,232
ex 44
mvaa 84,212,168
ex 45
mvaa 84,204,164
dl 246,24,51,1024
mvaa 84,196,160
ex 46
cvaa 112,224,160,232
ex 47
mvaa 112,232,164
ex 16
mvaa 112,240,168
ex 17
mvaa 112,248,172
dl 249,24,51,2048
cvaa 116,252,172,232
ex 18
mvaa 116,244,168
ex 19
mvaa 116,236,164
ex 20
mvaa 116,228,160
ex 21
cvaa 88,192,176,232
dl 247,24,51,3072
s_waitcnt vmcnt(8)
v_cmp_ne_u32_e32 vcc,4,v253
mvaa 88,200,180
s_cbranch_vccnz L_spbo
L_sprbo:
ex 22
mvaa 88,208,184
s_xor_b32 s50,s66,262144
ex 23
mvaa 88,216,188
ex 48
cvaa 92,220,188,232
ex 49
mvaa 92,212,184
la 128,252,50,0
mvaa 92,204,180
s_waitcnt lgkmcnt(0)
s_barrier
mvaa 92,196,176
s_mov_b32 m0,s72
ex 50
cvaa 120,224,176,232
s_lshl_b32 s72,s22,11
ex 51
mvaa 120,232,180
s_add_u32 s72,s72,s71
ex 52
mvaa 120,240,184
ds 255,8,76,0
mvaa 120,248,188
s_add_u32 s71,s70,131072
ex 53
cvaa 124,252,188,232
ex 54
mvaa 124,244,184
ex 55
mvaa 124,236,180
ex 24
mvaa 124,228,176
ds 255,8,76,1024
maav 0,64,0
s_cmp_lg_u32 s19,7
s_cbranch_scc1 L_j7o
s_nop 1
s_lshl_b32 s86,s12,12
s_add_u32 s86,s86,131072
v_add_u32_e32 v236,s86,v244
ra 160,236,0
ra 164,236,1024
ra 168,236,2048
ra 172,236,3072
ra 176,236,16384
ra 180,236,17408
ra 184,236,18432
ra 188,236,19456
v_add_u32_e32 v224,s94,v224
s_waitcnt lgkmcnt(0)
L_j7o:
ex 25
maav 0,96,4
ex 26
maav 4,68,0
ex 27
maav 4,100,4
cv 11,14,15
cv 12,40,41
maav 8,72,0
ds 255,8,76,2048
maav 8,104,4
cv 13,42,43
cv 14,44,45
maav 12,76,0
cv 15,46,47
cv 16,16,17
maav 12,108,4
ex 28
v_smfmac_f32_16x16x64_bf16 v[144:147],v[228:231],v[0:7],v248
cv 17,18,19
maav 16,80,0
ds 255,8,76,3072
maav 16,112,4
s_mov_b32 m0,s73
ex 29
maav 20,84,0
s_add_u32 s73,s72,131072
ex 30
maav 20,116,4
ex 31
maav 24,88,0
ex 56
maav 24,120,4
ds 255,8,77,0
maav 28,92,0
ex 57
maav 28,124,4
ex 58
maav 32,64,8
ex 59
maav 32,96,12
ex 60
maav 36,68,8
ds 255,8,77,1024
maav 36,100,12
cv 18,20,21
cv 19,22,23
maav 40,72,8
cv 20,48,49
cv 21,50,51
maav 40,104,12
cv 22,52,53
cv 23,54,55
maav 44,76,8
ex 61
maav 44,108,12
ds 255,8,77,2048
v_smfmac_f32_16x16x64_bf16 v[148:151],v[228:231],v[8:15],v248
cv 24,24,25
maav 48,80,8
ex 62
maav 48,112,12
ex 63
maav 52,84,8
ra 192,244,0
cv 25,26,27
maav 52,116,12
ds 255,8,77,3072
maav 56,88,8
cv 26,28,29
cv 27,30,31
maav 56,120,12
cv 28,56,57
cv 29,58,59
maav 60,92,8
cv 30,60,61
cv 31,62,63
maav 60,124,12
ex 64
mvav 160,64,16
la 132,252,50,2048
mvav 160,96,20
ex 65
mvav 164,68,16
ex 66
mvav 164,100,20
ex 67
mvav 168,72,16
ex 68
mvav 168,104,20
la 136,250,50,0
mvav 172,76,16
ex 69
mvav 172,108,20
ex 70
v_smfmac_f32_16x16x64_bf16 v[152:155],v[228:231],v[16:23],v248
ra 200,244,2048
mvav 176,80,16
ra 208,244,4096
cv 64,64,65
mvav 176,112,20
la 140,250,50,2048
mvav 180,84,16
ra 216,244,6144
cv 65,66,67
mvav 180,116,20
ex 71
mvav 184,88,16
ex 96
mvav 184,120,20
L_vs25:
s_cbranch_execz L_vs29
s_barrier
ex 97
mvav 188,92,16
la 156,250,50,3072
mvav 188,124,20
s_add_u32 m0,s52,131072
ra 196,244,1024
s_xor_b32 s51,s62,1048576
mvav 192,64,24
ra 204,244,3072
cv 66,68,69
mvav 192,96,28
ra 212,244,5120
cv 67,70,71
mvav 196,68,24
ra 220,244,7168
cv 68,96,97
mvav 196,100,28
la 152,250,50,1024
mvav 200,72,24
ex 98
mvav 200,104,28
ex 99
mvav 204,76,24
ra 224,244,8192
cv 69,98,99
mvav 204,108,28
ra 232,244,10240
v_add_u32_e32 v6,s87,v244
v_smfmac_f32_16x16x64_bf16 v[156:159],v[228:231],v[24:31],v248
ra 240,244,12288
mvav 208,80,24
la 148,252,50,3072
mvav 208,112,28
s_xor_b32 s87,s52,4096
ra 248,244,14336
v_add_u32_e32 v7,98304,v6
mvav 212,84,24
s_add_u32 s87,s87,150528
ra 252,244,15360
v_add_u32_e32 v5,65536,v244
mvav 212,116,28
ra 244,244,13312
v_add_u32_e32 v32,s87,v244
mvav 216,88,24
ra 228,244,9216
v_and_b32_e32 v224,960,v244
mvav 216,120,28
la 144,252,50,1024
mvav 220,92,24
s_add_u32 s50,s50,s55
ra 236,244,11264
mvav 220,124,28
ra 64,225,32768
s_waitcnt lgkmcnt(8)
cvaa 24,192,176,232
dl 245,24,51,0
mvaa 24,200,180
ra 96,225,40960
mvaa 24,208,184
ra 68,225,33792
mvaa 24,216,188
dl 246,24,51,1024
cvaa 16,192,160,128
ra 100,225,41984
mvaa 16,200,164
ra 72,225,34816
mvaa 16,208,168
dl 249,24,51,2048
mvaa 16,216,172
ra 104,225,43008
cvaa 28,196,176,232
ra 76,225,35840
mvaa 28,204,180
dl 247,24,51,3072
mvaa 28,212,184
s_add_u32 m0,s52,147456
s_xor_b32 s51,s63,1048576
ra 108,225,44032
mvaa 28,220,188
ra 80,225,36864
cvaa 20,196,160,132
dl 245,24,51,0
mvaa 20,204,164
ra 112,225,45056
mvaa 20,212,168
ra 192,244,16384
mvaa 20,220,172
dl 246,24,51,1024
cvaa 56,224,176,232
ra 200,244,18432
s_waitcnt lgkmcnt(7)
mvaa 56,232,180
ra 208,244,20480
mvaa 56,240,184
dl 249,24,51,2048
mvaa 56,248,188
ra 216,244,22528
cvaa 48,224,160,136
ra 196,244,17408
mvaa 48,232,164
ra 204,244,19456
mvaa 48,240,168
ra 212,244,21504
mvaa 48,248,172
ra 220,244,23552
cvaa 60,228,176,232
ra 224,244,24576
mvaa 60,236,180
ra 232,244,26624
mvaa 60,244,184
ra 240,244,28672
mvaa 60,252,188
ra 248,244,30720
cvaa 52,228,160,140
ra 84,225,37888
mvaa 52,236,164
ra 116,225,46080
mvaa 52,244,168
la 160,252,50,0
ra 228,244,25600
mvaa 52,252,172
ra 236,244,27648
s_waitcnt lgkmcnt(10)
cvaa 8,192,176,128
ra 244,244,29696
mvaa 8,200,180
la 164,252,50,2048
ra 252,244,31744
mvaa 8,208,184
ra 88,225,38912
mvaa 8,216,188
ra 120,225,47104
cvaa 12,196,176,132
la 168,250,50,0
mvaa 12,204,180
ra 92,225,39936
ra 124,225,48128
s_waitcnt lgkmcnt(9)
mvaa 12,212,184
ex 100
mvaa 12,220,188
la 172,250,50,2048
cvaa 40,224,176,136
ex 101
mvaa 40,232,180
ex 102
mvaa 40,240,184
ex 103
mvaa 40,248,188
cv 70,100,101
cv 71,102,103
s_waitcnt lgkmcnt(4)
cvaa 44,228,176,140
ex 72
mvaa 44,236,180
s_waitcnt vmcnt(18) lgkmcnt(0)
s_barrier
ex 73
mvaa 44,244,184
la 176,252,50,1024
mvaa 44,252,188
ex 74
maav 0,64,64
ex 75
maav 0,96,68
la 188,250,50,3072
maav 4,68,64
ra 192,5,0
cv 72,72,73
maav 4,100,68
ra 196,5,1024
cv 73,74,75
maav 8,72,64
la 184,250,50,1024
ds_write_b128 v6,a[0:3] offset:0
maav 8,104,68
ex 76
maav 12,76,64
ex 77
maav 12,108,68
la 180,252,50,3072
ds_write_b128 v6,a[4:7] offset:1024
v_smfmac_f32_16x16x64_bf16 v[144:147],v[228:231],v[64:71],v248
cv 74,76,77
maav 16,80,64
ex 78
maav 16,112,68
ds_write_b128 v6,a[8:11] offset:2048
ex 79
maav 20,84,64
ex 104
maav 20,116,68
ds_write_b128 v6,a[12:15] offset:3072
ex 105
maav 24,88,64
ex 106
ex 107
ex 108
maav 24,120,68
ds_write_b128 v6,a[16:19] offset:4096
cv 75,78,79
maav 28,92,64
ex 109
ex 110
ex 111
maav 28,124,68
cv 76,104,105
cv 77,106,107
cv 78,108,109
cv 79,110,111
maav 32,64,72
ds_write_b128 v6,a[20:23] offset:5120
maav 32,96,76
ex 80
maav 36,68,72
ex 81
maav 36,100,76
ds_write_b128 v6,a[24:27] offset:6144
maav 40,72,72
ex 82
maav 40,104,76
ex 83
maav 44,76,72
ds_write_b128 v6,a[28:31] offset:7168
maav 44,108,76
cv 80,80,81
cv 81,82,83
v_smfmac_f32_16x16x64_bf16 v[148:151],v[228:231],v[72:79],v248
ex 84
maav 48,80,72
ds_write_b128 v7,a[32:35] offset:0
maav 48,112,76
ex 85
ex 86
ex 87
maav 52,84,72
ex 112
ex 113
maav 52,116,76
ds_write_b128 v7,a[36:39] offset:1024
cv 82,84,85
maav 56,88,72
ex 114
ex 115
ex 116
maav 56,120,76
ex 117
ex 118
ex 119
maav 60,92,72
ds_write_b128 v7,a[40:43] offset:2048
cv 83,86,87
maav 60,124,76
cv 84,112,113
cv 85,114,115
cv 86,116,117
cv 87,118,119
mvav 160,64,80
ex 88
mvav 160,96,84
ds_write_b128 v7,a[44:47] offset:3072
mvav 164,68,80
v_add_f32_e32 v130,v144,v145
v_add_f32_e32 v131,v148,v149
mvav 164,100,84
ex 89
mvav 168,72,80
ds_write_b128 v7,a[48:51] offset:4096
mvav 168,104,84
ex 90
mvav 172,76,80
ex 91
mvav 172,108,84
ds_write_b128 v32,v[128:131]
v_smfmac_f32_16x16x64_bf16 v[152:155],v[228:231],v[80:87],v248
ex 92
ex 93
mvav 176,80,80
ds_write_b128 v7,a[52:55] offset:5120
ex 94
mvav 176,112,84
ra 200,5,2048
ra 204,5,3072
ex 95
cv 88,88,89
mvav 180,84,80
ra 208,5,4096
ra 212,5,5120
cv 89,90,91
cv 90,92,93
ex 120
mvav 180,116,84
ds_write_b128 v7,a[56:59] offset:6144
ex 121
mvav 184,88,80
ex 122
ex 123
cv 91,94,95
mvav 184,120,84
ex 124
ex 125
mvav 188,92,80
ds_write_b128 v7,a[60:63] offset:7168
ex 126
mvav 188,124,84
ra 216,5,6144
ra 220,5,7168
ra 224,5,8192
ra 232,5,10240
ex 127
cv 92,120,121
mvav 192,64,88
ra 240,5,12288
ra 248,5,14336
ra 228,5,9216
ra 236,5,11264
ra 244,5,13312
ra 252,5,15360
cv 93,122,123
cv 94,124,125
cv 95,126,127
v_xor_b32_e32 v6,8192,v6
s_nop 0
mvav 192,96,92
ex 16
mvav 196,68,88
s_waitcnt lgkmcnt(10)
s_xor_b32 s87,s87,0
mvav 196,100,92
s_xor_b32 s50,s66,294912
s_barrier
v_xor_b32_e32 v7,8192,v7
mvav 200,72,88
la 4,252,50,0
ra 64,5,16384
ra 96,5,24576
rv 64,6,0
rv 68,6,1024
rv 72,6,2048
mvav 200,104,92
rv 76,6,3072
rv 80,6,4096
rv 84,6,5120
ex 17
)"
R"(ex 18
mvav 204,76,88
rv 96,7,0
rv 100,7,1024
ex 19
mvav 204,108,92
la 12,252,50,2048
rv 104,7,2048
rv 108,7,3072
cv 16,16,17
v_smfmac_f32_16x16x64_bf16 v[156:159],v[228:231],v[88:95],v248
rv 112,7,4096
rv 116,7,5120
ex 20
ex 21
mvav 208,80,88
rv 120,7,6144
rv 124,7,7168
ex 22
ex 23
mvav 208,112,92
la 20,250,50,0
ra 68,5,17408
ra 100,5,25600
cv 17,18,19
cv 18,20,21
mvav 212,84,88
ra 72,5,18432
ra 104,5,26624
ex 48
ex 49
mvav 212,116,92
ra 76,5,19456
ra 108,5,27648
ex 50
ex 51
mvav 216,88,88
la 28,250,50,2048
ra 80,5,20480
ra 112,5,28672
ra 84,5,21504
mvav 216,120,92
ra 116,5,29696
cv 19,22,23
ex 52
ex 53
mvav 220,92,88
ra 88,5,22528
cv 20,48,49
ex 54
ex 55
mvav 220,124,92
la 60,250,50,3072
ra 120,5,30720
cv 21,50,51
cv 22,52,53
cv 23,54,55
s_bitcmp1_b32 s82,s81
s_cbranch_scc1 L_pend2
L_ret_eb5:
mvav 160,192,16
ra 92,5,23552
rv 88,6,6144
ra 124,5,31744
rv 92,6,7168
ex 24
s_waitcnt lgkmcnt(10)
mvav 160,224,20
ex 25
ex 26
v_smfmac_f32_16x16x64_bf16 v[152:155],v[228:231],v[16:23],v248
ex 27
v_xor_b32_e32 v32,4096,v32
mvav 164,196,16
la 52,250,50,1024
ds_read_b128 v[236:239],v32
cv 24,24,25
ex 28
mvav 164,228,20
ex 29
cv 25,26,27
ex 30
ex 31
mvav 168,200,16
ex 56
ex 57
mvav 168,232,20
la 44,252,50,3072
cv 26,28,29
ex 58
ex 59
mvav 172,204,16
ex 60
ex 61
ex 62
mvav 172,236,20
s_waitcnt lgkmcnt(0)
s_barrier
cv 27,30,31
ex 63
mvav 176,208,16
la 36,252,50,1024
cv 28,56,57
cv 29,58,59
cv 30,60,61
cv 31,62,63
mvav 176,240,20
s_add_u32 m0,s52,147456
ex 8
s_xor_b32 s51,s63,1048576
ex 9
ex 10
mvav 180,212,16
s_add_u32 s50,s50,s55
ex 11
cv 8,8,9
cv 9,10,11
ex 12
mvav 180,244,20
dl 247,24,51,3072
ex 13
ex 14
mvav 184,216,16
s_add_u32 m0,s52,0
ex 15
ex 40
ex 41
mvav 184,248,20
s_xor_b32 s51,s64,0
ex 42
ex 43
ex 44
mvav 188,220,16
la 0,252,50,0
ex 45
ex 46
mvav 188,252,20
ex 47
cv 10,12,13
cv 11,14,15
cv 12,40,41
cv 13,42,43
cv 14,44,45
v_smfmac_f32_16x16x64_bf16 v[156:159],v[228:231],v[24:31],v248
cv 15,46,47
v_bfe_u32 v225,v244,1,5
s_nop 0
v_smfmac_f32_16x16x64_bf16 v[156:159],v[228:231],v[8:15],v248
v_lshl_add_u32 v224,v224,1,v225
v_bfe_u32 v225,v244,7,2
v_lshl_add_u32 v224,v225,5,v224
mvav 192,192,24
la 8,252,50,2048
v_xor_b32_e32 v224,s69,v224
v_add_f32_e32 v152,v152,v153
v_add_f32_e32 v137,v152,v238
v_subrev_u32_e32 v236,0x23800000,v137
mvav 192,224,28
v_rcp_f32_e32 v137,v137
mvav 192,64,8
tv 128,224,32768
v_mul_f32_e32 v232,v240,v137
v_mul_f32_e32 v64,v232,v64
v_mul_f32_e32 v65,v232,v65
v_mul_f32_e32 v66,v232,v66
mvav 192,96,12
la 16,250,50,0
v_mul_f32_e32 v67,v232,v67
v_mul_f32_e32 v68,v232,v68
v_mul_f32_e32 v69,v232,v69
v_mul_f32_e32 v70,v232,v70
mvav 196,196,24
v_fmac_f32_e32 v64,v232,v160
v_fmac_f32_e32 v65,v232,v161
v_mul_f32_e32 v71,v232,v71
mvav 196,228,28
v_fmac_f32_e32 v66,v232,v162
v_fmac_f32_e32 v67,v232,v163
v_fmac_f32_e32 v68,v232,v164
v_fmac_f32_e32 v69,v232,v165
v_fmac_f32_e32 v70,v232,v166
mvav 196,68,8
la 24,250,50,2048
v_fmac_f32_e32 v71,v232,v167
cv 160,64,65
cv 161,66,67
cv 162,68,69
cv 163,70,71
mvav 196,100,12
tv 130,224,34816
tv 132,224,36864
mvav 200,200,24
tv 134,224,38912
tv 144,224,40960
v_mul_f32_e32 v72,v232,v72
v_mul_f32_e32 v73,v232,v73
v_mul_f32_e32 v74,v232,v74
mvav 200,232,28
buffer_store_dwordx4 v[160:163],v244,s[8:11],s14 offen offset:0 sc0 sc1
v_mul_f32_e32 v75,v232,v75
v_mul_f32_e32 v76,v232,v76
v_mul_f32_e32 v77,v232,v77
v_mul_f32_e32 v78,v232,v78
mvav 200,72,8
v_fmac_f32_e32 v72,v232,v168
v_fmac_f32_e32 v73,v232,v169
v_mul_f32_e32 v79,v232,v79
mvav 200,104,12
v_fmac_f32_e32 v74,v232,v170
v_fmac_f32_e32 v75,v232,v171
v_fmac_f32_e32 v76,v232,v172
v_fmac_f32_e32 v77,v232,v173
v_fmac_f32_e32 v78,v232,v174
mvav 204,204,24
la 56,250,50,3072
v_fmac_f32_e32 v79,v232,v175
cv 168,72,73
cv 169,74,75
cv 170,76,77
cv 171,78,79
mvav 204,236,28
tv 146,224,43008
tv 148,224,45056
mvav 204,76,8
tv 150,224,47104
v_mul_f32_e32 v80,v232,v80
v_mul_f32_e32 v81,v232,v81
v_mul_f32_e32 v82,v232,v82
mvav 204,108,12
buffer_store_dwordx4 v[168:171],v244,s[8:11],s14 offen offset:2048 sc0 sc1
v_mul_f32_e32 v83,v232,v83
v_mul_f32_e32 v84,v232,v84
v_mul_f32_e32 v85,v232,v85
v_mul_f32_e32 v86,v232,v86
v_mul_f32_e32 v87,v232,v87
mvav 208,208,24
v_fmac_f32_e32 v80,v232,v176
v_fmac_f32_e32 v81,v232,v177
v_fmac_f32_e32 v82,v232,v178
mvav 208,240,28
v_fmac_f32_e32 v83,v232,v179
v_fmac_f32_e32 v84,v232,v180
v_fmac_f32_e32 v85,v232,v181
cv 176,80,81
cv 177,82,83
mvav 208,80,8
la 48,250,50,1024
v_fmac_f32_e32 v86,v232,v182
v_fmac_f32_e32 v87,v232,v183
cv 178,84,85
cv 179,86,87
mvav 208,112,12
v_mul_f32_e32 v88,v232,v88
v_mul_f32_e32 v89,v232,v89
v_mul_f32_e32 v90,v232,v90
mvav 212,212,24
v_mul_f32_e32 v91,v232,v91
v_mul_f32_e32 v92,v232,v92
v_mul_f32_e32 v93,v232,v93
v_mul_f32_e32 v94,v232,v94
v_mul_f32_e32 v95,v232,v95
mvav 212,244,28
L_vs26:
s_cbranch_execz L_vs30
buffer_store_dwordx4 v[176:179],v244,s[8:11],s88 offen offset:0 sc0 sc1
v_fmac_f32_e32 v88,v232,v184
v_fmac_f32_e32 v89,v232,v185
v_fmac_f32_e32 v90,v232,v186
v_fmac_f32_e32 v91,v232,v187
cv 184,88,89
mvav 212,84,8
v_fmac_f32_e32 v92,v232,v188
cv 185,90,91
mvav 212,116,12
v_fmac_f32_e32 v93,v232,v189
v_fmac_f32_e32 v94,v232,v190
v_fmac_f32_e32 v95,v232,v191
cv 186,92,93
cv 187,94,95
mvav 216,216,24
la 40,252,50,3072
v_add_f32_e32 v156,v156,v157
v_add_f32_e32 v141,v156,v239
v_subrev_u32_e32 v237,0x23800000,v141
v_rcp_f32_e32 v141,v141
mvav 216,248,28
v_mul_f32_e32 v233,v241,v141
v_mul_f32_e32 v96,v233,v96
mvav 216,88,8
v_mul_f32_e32 v97,v233,v97
v_mul_f32_e32 v98,v233,v98
v_mul_f32_e32 v99,v233,v99
v_mul_f32_e32 v100,v233,v100
mvav 216,120,12
buffer_store_dwordx4 v[184:187],v244,s[8:11],s88 offen offset:2048 sc0 sc1
v_fmac_f32_e32 v96,v233,v192
v_fmac_f32_e32 v97,v233,v193
v_mul_f32_e32 v101,v233,v101
v_mul_f32_e32 v102,v233,v102
v_mul_f32_e32 v103,v233,v103
mvav 220,220,24
v_fmac_f32_e32 v98,v233,v194
v_fmac_f32_e32 v99,v233,v195
mvav 220,252,28
v_fmac_f32_e32 v100,v233,v196
v_fmac_f32_e32 v101,v233,v197
cv 192,96,97
cv 193,98,99
mvav 220,92,8
la 32,252,50,1024
v_fmac_f32_e32 v102,v233,v198
v_fmac_f32_e32 v103,v233,v199
cv 194,100,101
cv 195,102,103
mvav 220,124,12
v_xor_b32_e32 v225,32,v224
v_xor_b32_e32 v226,64,v224
s_waitcnt lgkmcnt(0)
zvva 0,128,128
tv 138,225,34816
tv 142,225,38912
tv 152,225,40960
s_waitcnt vmcnt(36)
mvva 0,132,132
tv 136,225,32768
tv 154,225,43008
mvva 0,144,136
tv 140,225,36864
tv 158,225,47104
mvva 0,148,140
tv 156,225,45056
v_mul_f32_e32 v104,v233,v104
v_mul_f32_e32 v105,v233,v105
v_mul_f32_e32 v106,v233,v106
zvva 4,148,156
buffer_store_dwordx4 v[192:195],v244,s[8:11],s14 offen offset:1024 sc0 sc1
v_mul_f32_e32 v107,v233,v107
v_mul_f32_e32 v108,v233,v108
v_mul_f32_e32 v109,v233,v109
v_mul_f32_e32 v110,v233,v110
mvva 4,144,152
v_fmac_f32_e32 v104,v233,v200
v_fmac_f32_e32 v105,v233,v201
v_fmac_f32_e32 v106,v233,v202
v_mul_f32_e32 v111,v233,v111
mvva 4,132,148
v_fmac_f32_e32 v107,v233,v203
v_fmac_f32_e32 v108,v233,v204
v_fmac_f32_e32 v109,v233,v205
cv 200,104,105
cv 201,106,107
mvva 4,128,144
v_fmac_f32_e32 v110,v233,v206
v_fmac_f32_e32 v111,v233,v207
cv 202,108,109
cv 203,110,111
s_waitcnt vmcnt(16)
zvva 8,128,160
v_cmp_lt_u32_e64 s[42:43],s48,v236
v_mul_f32_e32 v112,v233,v112
s_or_b64 s[40:41],s[40:41],s[42:43]
v_cmp_lt_u32_e64 s[42:43],s48,v237
mvva 8,132,164
s_or_b64 s[40:41],s[40:41],s[42:43]
v_mul_f32_e32 v113,v233,v113
v_mul_f32_e32 v114,v233,v114
v_mul_f32_e32 v115,v233,v115
mvva 8,144,168
dl 245,36,51,0
v_mul_f32_e32 v116,v233,v116
v_mul_f32_e32 v117,v233,v117
v_mul_f32_e32 v118,v233,v118
v_mul_f32_e32 v119,v233,v119
v_mul_f32_e32 v120,v233,v120
mvva 8,148,172
v_fmac_f32_e32 v112,v233,v208
v_fmac_f32_e32 v113,v233,v209
v_fmac_f32_e32 v114,v233,v210
v_fmac_f32_e32 v115,v233,v211
zvva 12,148,188
v_fmac_f32_e32 v116,v233,v212
v_fmac_f32_e32 v117,v233,v213
cv 208,112,113
cv 209,114,115
mvva 12,144,184
v_fmac_f32_e32 v118,v233,v214
v_fmac_f32_e32 v119,v233,v215
cv 210,116,117
cv 211,118,119
mvva 12,132,180
v_mul_f32_e32 v121,v233,v121
v_mul_f32_e32 v122,v233,v122
v_mul_f32_e32 v123,v233,v123
mvva 12,128,176
v_mul_f32_e32 v124,v233,v124
v_mul_f32_e32 v125,v233,v125
v_mul_f32_e32 v126,v233,v126
v_mul_f32_e32 v127,v233,v127
s_waitcnt lgkmcnt(0)
zvva 28,136,176
buffer_store_dwordx4 v[200:203],v244,s[8:11],s14 offen offset:3072 sc0 sc1
tv 144,226,40960
v_fmac_f32_e32 v120,v233,v216
v_fmac_f32_e32 v121,v233,v217
v_fmac_f32_e32 v122,v233,v218
v_fmac_f32_e32 v123,v233,v219
mvva 28,140,180
tv 128,226,32768
tv 130,226,34816
cv 216,120,121
cv 217,122,123
mvva 28,152,184
tv 132,226,36864
tv 134,226,38912
v_fmac_f32_e32 v124,v233,v220
v_fmac_f32_e32 v125,v233,v221
v_fmac_f32_e32 v126,v233,v222
mvva 28,156,188
tv 146,226,43008
v_fmac_f32_e32 v127,v233,v223
cv 218,124,125
cv 219,126,127
zvva 24,156,172
tv 148,226,45056
tv 150,226,47104
mvva 24,152,168
v_xor_b32_e32 v227,96,v224
v_add_u32_e32 v232,131072,v224
mvva 24,140,164
buffer_store_dwordx4 v[208:211],v244,s[8:11],s88 offen offset:1024 sc0 sc1
mvva 24,136,160
v_add_u32_e32 v233,131072,v225
v_add_u32_e32 v234,131072,v226
zvva 20,136,144
v_add_u32_e32 v235,131072,v227
v_mov_b32_e32 v242,83914752
mvva 20,140,148
v_add_u32_e32 v228,65536,v224
v_add_u32_e32 v229,65536,v225
mvva 20,152,152
v_add_u32_e32 v230,65536,v226
v_add_u32_e32 v231,65536,v227
mvva 20,156,156
v_and_b32_e32 v243,240,v244
v_lshlrev_b32_e32 v243,7,v243
zvva 16,156,140
buffer_store_dwordx4 v[216:219],v244,s[8:11],s88 offen offset:3072 sc0 sc1
s_cmp_lg_u64 s[40:41],0
s_cbranch_scc1 L_redo
L_redo_r5:
mvva 16,152,136
tv 156,227,45056
v_mov_b32_e32 v236,16256
mvva 16,140,132
tv 158,227,47104
v_and_b32_e32 v248,256,v244
mvva 16,136,128
tv 152,227,40960
v_lshrrev_b32_e32 v248,3,v248
s_waitcnt lgkmcnt(3)
zvva 32,128,128
tv 140,227,36864
v_add_u32_e32 v243,v243,v248
mvva 32,132,132
tv 136,227,32768
tv 138,227,34816
mvva 32,144,136
dl 246,36,51,1024
tv 142,227,38912
mvva 32,148,140
tv 154,227,43008
v_and_b32_e32 v248,512,v244
zvva 36,148,156
v_lshrrev_b32_e32 v248,5,v248
v_add_u32_e32 v243,v243,v248
mvva 36,144,152
v_and_b32_e32 v248,16,v244
v_lshlrev_b32_e32 v248,v248,v236
mvva 36,132,148
v_cndmask_b32_e64 v238,0,v248,s[74:75]
s_mov_b32 s74,0xc0000c
mvva 36,128,144
s_mov_b32 s75,0xc0000c00
v_mov_b32_e32 v240,0
v_cndmask_b32_e64 v239,0,v248,s[74:75]
zvva 40,128,160
dl 249,36,51,2048
mvva 40,132,164
v_mov_b32_e32 v241,0
v_mov_b32_e32 v236,0
mvva 40,144,168
v_mov_b32_e32 v237,0
mvva 40,148,172
zvva 44,148,188
mvva 44,144,184
mvva 44,132,180
dl 247,36,51,3072
s_add_u32 m0,s52,16384
s_xor_b32 s51,s65,0
mvva 44,128,176
tv 144,224,57344
s_waitcnt lgkmcnt(1)
zvva 60,136,176
tv 132,224,53248
tv 134,224,55296
mvva 60,140,180
tv 128,224,49152
tv 130,224,51200
mvva 60,152,184
mvva 60,156,188
tv 146,224,59392
zvva 56,156,172
dl 245,36,51,0
tv 148,224,61440
tv 150,224,63488
mvva 56,152,168
mvva 56,140,164
mvva 56,136,160
zvva 52,136,144
mvva 52,140,148
mvva 52,152,152
dl 246,36,51,1024
mvva 52,156,156
zvva 48,156,140
mvva 48,152,136
mvva 48,140,132
tv 156,225,61440
mvva 48,136,128
tv 152,225,57344
s_waitcnt lgkmcnt(2)
zvva 64,128,128
dl 249,36,51,2048
tv 140,225,53248
mvva 64,132,132
tv 136,225,49152
tv 138,225,51200
mvva 64,144,136
tv 142,225,55296
mvva 64,148,140
tv 154,225,59392
zvva 68,148,156
tv 158,225,63488
mvva 68,144,152
mvva 68,132,148
dl 247,36,51,3072
s_lshl_b32 s51,s33,5
s_add_u32 m0,s52,98304
s_xor_b32 s51,s64,s51
mvva 68,128,144
zvva 72,128,160
mvva 72,132,164
s_waitcnt vmcnt(18)
s_mov_b32 s90,0
mvva 72,144,168
s_xor_b32 s50,s67,0
mvva 72,148,172
zvva 76,148,188
dl 245,36,51,0
mvva 76,144,184
mvva 76,132,180
tv 148,226,61440
mvva 76,128,176
tv 144,226,57344
s_waitcnt lgkmcnt(2)
zvva 92,136,176
tv 132,226,53248
mvva 92,140,180
tv 128,226,49152
tv 130,226,51200
mvva 92,152,184
dl 246,36,51,1024
tv 134,226,55296
mvva 92,156,188
tv 146,226,59392
zvva 88,156,172
tv 150,226,63488
mvva 88,152,168
mvva 88,140,164
mvva 88,136,160
zvva 84,136,144
dl 249,36,51,2048
mvva 84,140,148
mvva 84,152,152
mvva 84,156,156
zvva 80,156,140
mvva 80,152,136
mvva 80,140,132
dl 247,36,51,3072
mvva 80,136,128
s_lshl_b32 s51,s33,5
tv 152,227,57344
s_add_u32 m0,s52,114688
s_waitcnt lgkmcnt(1)
zvva 96,128,128
s_xor_b32 s51,s65,s51
tv 140,227,53248
tv 142,227,55296
mvva 96,132,132
tv 136,227,49152
tv 138,227,51200
mvva 96,144,136
mvva 96,148,140
tv 154,227,59392
zvva 100,148,156
lv 164,252,50,0
tv 156,227,61440
tv 158,227,63488
mvva 100,144,152
mvva 100,132,148
mvva 100,128,144
zvva 104,128,160
mvva 104,132,164
mvva 104,144,168
lv 172,252,50,2048
mvva 104,148,172
zvva 108,148,188
mvva 108,144,184
mvva 108,132,180
mvva 108,128,176
s_barrier
s_waitcnt lgkmcnt(0)
zvva 124,136,176
lv 180,250,50,0
tv 132,232,4096
tv 144,232,8192
tv 146,232,10240
mvva 124,140,180
tv 128,232,0
tv 130,232,2048
mvva 124,152,184
tv 134,232,6144
mvva 124,156,188
tv 148,232,12288
tv 150,232,14336
zvva 120,156,172
mvva 120,152,168
mvva 120,140,164
lv 188,250,50,2048
mvva 120,136,160
zvva 116,136,144
mvva 116,140,148
mvva 116,152,152
mvva 116,156,156
zvva 112,156,140
lv 220,250,50,3072
mvva 112,152,136
mvva 112,140,132
tv 156,233,12288
mvva 112,136,128
tv 152,233,8192
s_waitcnt lgkmcnt(2)
mvva 0,128,4
tv 140,233,4096
mvva 0,132,12
tv 136,233,0
tv 138,233,2048
mvva 0,144,20
lv 212,250,50,1024
tv 142,233,6144
mvva 0,148,28
tv 154,233,10240
mvva 4,148,60
tv 158,233,14336
mvva 4,144,52
mvva 4,132,44
mvva 4,128,36
mvva 8,128,0
lv 204,252,50,3072
mvva 8,132,8
mvva 8,144,16
mvva 8,148,24
mvva 12,148,56
s_waitcnt vmcnt(27)
mvva 12,144,48
s_waitcnt vmcnt(25)
mvva 12,132,40
lv 196,252,50,1024
s_waitcnt vmcnt(24)
mvva 12,128,32
tv 144,234,8192
s_waitcnt lgkmcnt(1)
mvva 28,136,32
tv 132,234,4096
tv 134,234,6144
mvva 28,140,40
tv 128,234,0
tv 130,234,2048
mvva 28,152,48
mvva 28,156,56
tv 146,234,10240
mvva 24,156,24
dl 245,36,51,0
s_waitcnt vmcnt(20)
s_barrier
tv 148,234,12288
tv 150,234,14336
mvva 24,152,16
mvva 24,140,8
mvva 24,136,0
mvva 20,136,36
mvva 20,140,44
mvva 20,152,52
s_cmp_eq_u32 s12,0
s_cselect_b64 exec,1,0
buffer_store_dword v251,off,s[8:11],s61 sc0 sc1
s_mov_b64 exec,-1
mvva 20,156,60
mvva 16,156,28
mvva 16,152,20
mvva 16,140,12
tv 156,235,12288
mvva 16,136,4
tv 152,235,8192
s_waitcnt lgkmcnt(2)
mvva 32,128,4
dl 246,36,51,1024
tv 140,235,4096
mvva 32,132,12
tv 136,235,0
tv 138,235,2048
mvva 32,144,20
tv 142,235,6144
mvva 32,148,28
tv 154,235,10240
mvva 36,148,60
tv 158,235,14336
mvva 36,144,52
mvva 36,132,44
dl 249,36,51,2048
mvva 36,128,36
mvva 40,128,0
mvva 40,132,8
mvva 40,144,16
mvva 40,148,24
mvva 44,148,56
dl 247,36,51,3072
s_lshl_b32 s51,s34,5
s_add_u32 m0,s52,65536
s_xor_b32 s51,s64,s51
mvva 44,144,48
tv 148,232,28672
mvva 44,132,40
s_xor_b32 s53,s60,s33
tv 150,232,30720
mvva 44,128,32
tv 144,232,24576
s_waitcnt lgkmcnt(3)
mvva 60,136,32
tv 132,232,20480
mvva 60,140,40
tv 128,232,16384
tv 130,232,18432
mvva 60,152,48
dl 245,36,51,0
tv 134,232,22528
mvva 60,156,56
tv 146,232,26624
mvva 56,156,24
mvva 56,152,16
mvva 56,140,8
mvva 56,136,0
mvva 52,136,36
buffer_load_dword v253,v242,s[8:11],s53 offen sc1
mvva 52,140,44
mvva 52,152,52
mvva 52,156,60
mvva 48,156,28
mvva 48,152,20
mvva 48,140,12
dl 246,36,51,1024
mvva 48,136,4
tv 152,233,24576
s_waitcnt lgkmcnt(1)
mvva 64,128,4
tv 140,233,20480
tv 142,233,22528
mvva 64,132,12
tv 136,233,16384
tv 138,233,18432
mvva 64,144,20
mvva 64,148,28
tv 154,233,26624
mvva 68,148,60
dl 249,36,51,2048
tv 156,233,28672
tv 158,233,30720
mvva 68,144,52
mvva 68,132,44
mvva 68,128,36
mvva 72,128,0
mvva 72,132,8
mvva 72,144,16
dl 247,36,51,3072
s_lshl_b32 s51,s34,5
s_add_u32 m0,s52,81920
s_xor_b32 s51,s65,s51
mvva 72,148,24
mvva 76,148,56
mvva 76,144,48
mvva 76,132,40
tv 148,234,28672
mvva 76,128,32
tv 144,234,24576
s_waitcnt lgkmcnt(2)
mvva 92,136,32
dl 245,36,51,0
tv 132,234,20480
mvva 92,140,40
tv 128,234,16384
tv 130,234,18432
mvva 92,152,48
tv 134,234,22528
mvva 92,156,56
tv 146,234,26624
mvva 88,156,24
tv 150,234,30720
mvva 88,152,16
mvva 88,140,8
dl 246,36,51,1024
mvva 88,136,0
mvva 84,136,36
mvva 84,140,44
mvva 84,152,52
mvva 84,156,60
mvva 80,156,28
dl 249,36,51,2048
mvva 80,152,20
mvva 80,140,12
tv 156,235,28672
mvva 80,136,4
tv 152,235,24576
s_waitcnt lgkmcnt(2)
mvva 96,128,4
tv 140,235,20480
mvva 96,132,12
tv 136,235,16384
tv 138,235,18432
mvva 96,144,20
dl 247,36,51,3072
s_lshl_b32 s51,s35,5
s_add_u32 m0,s52,32768
s_xor_b32 s51,s64,s51
tv 142,235,22528
mvva 96,148,28
L_vs27:
s_cbranch_execz L_vs16
tv 154,235,26624
mvva 100,148,60
tv 158,235,30720
mvva 100,144,52
mvva 100,132,44
mvva 100,128,36
mvva 104,128,0
dl 245,36,51,0
mvva 104,132,8
mvva 104,144,16
mvva 104,148,24
mvva 108,148,56
mvva 108,144,48
mvva 108,132,40
dl 246,36,51,1024
mvva 108,128,32
s_waitcnt vmcnt(27)
s_barrier
s_waitcnt lgkmcnt(0)
mvva 124,136,32
tv 132,224,4096
tv 134,224,6144
tv 144,224,8192
mvva 124,140,40
tv 128,224,0
tv 130,224,2048
mvva 124,152,48
mvva 124,156,56
tv 146,224,10240
mvva 120,156,24
dl 249,36,51,2048
tv 148,224,12288
tv 150,224,14336
mvva 120,152,16
mvva 120,140,8
mvva 120,136,0
mvva 116,136,36
mvva 116,140,44
mvva 116,152,52
dl 247,36,51,3072
s_lshl_b32 s51,s35,5
s_add_u32 m0,s52,49152
s_xor_b32 s51,s65,s51
mvva 116,156,60
mvva 112,156,28
mvva 112,152,20
s_waitcnt vmcnt(11)
v_cmp_ne_u32_e32 vcc,4,v253
mvva 112,140,12
dl 245,36,51,0
s_cbranch_vccnz L_spa3o
L_spra3o:
mvva 112,136,4
s_xor_b32 s50,s67,s33
tv 152,225,8192
s_waitcnt lgkmcnt(1)
mvvv 0,128,164
tv 140,225,4096
tv 142,225,6144
mvvv 0,132,172
tv 136,225,0
tv 138,225,2048
mvvv 0,144,180
dl 246,36,51,1024
mvvv 0,148,188
tv 154,225,10240
mvvv 4,148,220
tv 156,225,12288
tv 158,225,14336
mvvv 4,144,212
mvvv 4,132,204
la 196,252,50,0
mvvv 4,128,196
mvvv 8,128,160
s_xor_b32 s53,s60,s34
mvvv 8,132,168
mvvv 8,144,176
la 204,252,50,2048
mvvv 8,148,184
mvvv 12,148,216
mvvv 12,144,208
mvvv 12,132,200
buffer_load_dword v253,v242,s[8:11],s53 offen sc1
mvvv 12,128,192
tv 144,226,8192
s_waitcnt lgkmcnt(1)
mvvv 28,136,192
tv 132,226,4096
tv 134,226,6144
mvvv 28,140,200
tv 128,226,0
tv 130,226,2048
mvvv 28,152,208
la 212,250,50,0
mvvv 28,156,216
tv 146,226,10240
mvvv 24,156,184
tv 148,226,12288
tv 150,226,14336
mvvv 24,152,176
mvvv 24,140,168
mvvv 24,136,160
la 220,250,50,2048
mvvv 20,136,196
mvvv 20,140,204
mvvv 20,152,212
mvvv 20,156,220
la 252,250,50,3072
mvvv 16,156,188
mvvv 16,152,180
mvvv 16,140,172
tv 156,227,12288
mvvv 16,136,164
la 244,250,50,1024
tv 152,227,8192
s_waitcnt lgkmcnt(2)
mvvv 32,128,164
tv 140,227,4096
mvvv 32,132,172
tv 136,227,0
tv 138,227,2048
mvvv 32,144,180
tv 142,227,6144
mvvv 32,148,188
la 236,252,50,3072
tv 154,227,10240
mvvv 36,148,220
tv 158,227,14336
mvvv 36,144,212
mvvv 36,132,204
mvvv 36,128,196
la 228,252,50,1024
s_add_u32 s50,s50,s55
mvvv 40,128,160
mvvv 40,132,168
mvvv 40,144,176
mvvv 40,148,184
mvvv 44,148,216
la 192,252,50,0
mvvv 44,144,208
mvvv 44,132,200
tv 148,224,28672
tv 150,224,30720
mvvv 44,128,192
s_waitcnt lgkmcnt(2)
mvvv 60,136,192
la 200,252,50,2048
tv 132,224,20480
mvvv 60,140,200
tv 128,224,16384
tv 130,224,18432
mvvv 60,152,208
tv 134,224,22528
mvvv 60,156,216
)"
R"(tv 144,224,24576
tv 146,224,26624
mvvv 56,156,184
la 208,250,50,0
mvvv 56,152,176
mvvv 56,140,168
mvvv 56,136,160
mvvv 52,136,196
la 216,250,50,2048
mvvv 52,140,204
mvvv 52,152,212
mvvv 52,156,220
mvvv 48,156,188
la 248,250,50,3072
mvvv 48,152,180
mvvv 48,140,172
tv 156,225,28672
mvvv 48,136,164
tv 152,225,24576
s_waitcnt lgkmcnt(2)
mvvv 64,128,164
tv 140,225,20480
mvvv 64,132,172
la 240,250,50,1024
tv 136,225,16384
tv 138,225,18432
mvvv 64,144,180
tv 142,225,22528
mvvv 64,148,188
tv 154,225,26624
mvvv 68,148,220
tv 158,225,30720
mvvv 68,144,212
la 232,252,50,3072
mvvv 68,132,204
mvvv 68,128,196
mvvv 72,128,160
mvvv 72,132,168
la 224,252,50,1024
mvvv 72,144,176
mvvv 72,148,184
mvvv 76,148,216
mvvv 76,144,208
mvvv 76,132,200
dl 249,36,51,2048
mvvv 76,128,192
tv 144,226,24576
s_waitcnt lgkmcnt(1)
mvvv 92,136,192
tv 132,226,20480
tv 134,226,22528
mvvv 92,140,200
tv 128,226,16384
tv 130,226,18432
mvvv 92,152,208
dl 247,36,51,3072
s_lshl_b32 s51,s98,5
s_xor_b32 s51,s64,s51
s_add_u32 m0,s52,131072
s_xor_b32 s51,s51,4194304
mvvv 92,156,216
tv 146,226,26624
mvvv 88,156,184
tv 148,226,28672
tv 150,226,30720
mvvv 88,152,176
mvvv 88,140,168
dl 245,36,51,0
mvvv 88,136,160
s_waitcnt vmcnt(17)
v_cmp_ne_u32_e32 vcc,4,v253
mvvv 84,136,196
s_cbranch_vccnz L_spa4o
L_spra4o:
mvvv 84,140,204
s_xor_b32 s50,s67,s34
mvvv 84,152,212
dl 246,36,51,1024
mvvv 84,156,220
mvvv 80,156,188
mvvv 80,152,180
mvvv 80,140,172
la 68,252,50,0
mvvv 80,136,164
tv 152,227,24576
s_waitcnt lgkmcnt(1)
mvvv 96,128,164
tv 140,227,20480
tv 142,227,22528
mvvv 96,132,172
tv 136,227,16384
tv 138,227,18432
mvvv 96,144,180
s_xor_b32 s53,s60,s35
mvvv 96,148,188
la 76,252,50,2048
tv 154,227,26624
mvvv 100,148,220
tv 156,227,28672
tv 158,227,30720
mvvv 100,144,212
mvvv 100,132,204
mvvv 100,128,196
la 84,250,50,0
mvvv 104,128,160
mvvv 104,132,168
mvvv 104,144,176
mvvv 104,148,184
buffer_load_dword v253,v242,s[8:11],s53 offen sc1
mvvv 108,148,216
mvvv 108,144,208
mvvv 108,132,200
mvvv 108,128,192
s_barrier
s_waitcnt lgkmcnt(0)
mvvv 124,136,192
la 92,250,50,2048
tv 132,228,36864
tv 144,228,40960
tv 146,228,43008
mvvv 124,140,200
tv 128,228,32768
tv 130,228,34816
mvvv 124,152,208
tv 134,228,38912
mvvv 124,156,216
tv 148,228,45056
tv 150,228,47104
mvvv 120,156,184
la 124,250,50,3072
mvvv 120,152,176
mvvv 120,140,168
mvvv 120,136,160
mvvv 116,136,196
la 116,250,50,1024
mvvv 116,140,204
mvvv 116,152,212
mvvv 116,156,220
mvvv 112,156,188
la 108,252,50,3072
mvvv 112,152,180
mvvv 112,140,172
tv 156,229,45056
tv 158,229,47104
mvvv 112,136,164
s_waitcnt lgkmcnt(2)
mvva 0,128,196
la 100,252,50,1024
s_add_u32 s50,s50,s55
tv 140,229,36864
mvva 0,132,204
tv 136,229,32768
tv 138,229,34816
s_waitcnt vmcnt(21)
mvva 0,144,212
tv 142,229,38912
mvva 0,148,220
tv 152,229,40960
tv 154,229,43008
mvva 4,148,252
mvva 4,144,244
la 64,252,50,0
mvva 4,132,236
mvva 4,128,228
s_waitcnt vmcnt(21)
mvva 8,128,192
s_waitcnt vmcnt(20)
mvva 8,132,200
la 72,252,50,2048
s_waitcnt vmcnt(20)
mvva 8,144,208
s_waitcnt vmcnt(19)
mvva 8,148,216
s_waitcnt vmcnt(18)
mvva 12,148,248
s_waitcnt vmcnt(17)
mvva 12,144,240
la 80,250,50,0
s_waitcnt vmcnt(17)
mvva 12,132,232
tv 148,230,45056
s_waitcnt vmcnt(16)
mvva 12,128,224
tv 144,230,40960
s_waitcnt lgkmcnt(2)
mvva 28,136,224
tv 132,230,36864
mvva 28,140,232
la 88,250,50,2048
tv 128,230,32768
tv 130,230,34816
mvva 28,152,240
tv 134,230,38912
mvva 28,156,248
tv 146,230,43008
mvva 24,156,216
tv 150,230,47104
mvva 24,152,208
la 120,250,50,3072
mvva 24,140,200
mvva 24,136,192
mvva 20,136,228
mvva 20,140,236
mvva 20,152,244
la 112,250,50,1024
mvva 20,156,252
mvva 16,156,220
mvva 16,152,212
mvva 16,140,204
la 104,252,50,3072
mvva 16,136,196
tv 152,231,40960
s_waitcnt lgkmcnt(1)
mvva 32,128,196
tv 140,231,36864
tv 142,231,38912
mvva 32,132,204
tv 136,231,32768
tv 138,231,34816
mvva 32,144,212
la 96,252,50,1024
mvva 32,148,220
tv 154,231,43008
mvva 36,148,252
tv 156,231,45056
tv 158,231,47104
mvva 36,144,244
mvva 36,132,236
mvva 36,128,228
dl 249,36,51,2048
mvva 40,128,192
mvva 40,132,200
mvva 40,144,208
mvva 40,148,216
dl 247,36,51,3072
s_lshl_b32 s51,s98,5
s_xor_b32 s51,s65,s51
s_add_u32 m0,s52,147456
s_xor_b32 s51,s51,4194304
mvva 44,148,248
mvva 44,144,240
mvva 44,132,232
tv 148,228,61440
mvva 44,128,224
dl 245,36,51,0
tv 144,228,57344
s_waitcnt lgkmcnt(2)
mvva 60,136,224
tv 132,228,53248
mvva 60,140,232
tv 128,228,49152
tv 130,228,51200
s_waitcnt vmcnt(16)
v_cmp_ne_u32_e32 vcc,4,v253
mvva 60,152,240
s_cbranch_vccnz L_spa5o
L_spra5o:
tv 134,228,55296
s_xor_b32 s50,s67,s35
mvva 60,156,248
dl 246,36,51,1024
tv 146,228,59392
mvva 56,156,216
tv 150,228,63488
mvva 56,152,208
mvva 56,140,200
mvva 56,136,192
la 132,252,50,0
mvva 52,136,228
mvva 52,140,236
mvva 52,152,244
mvva 52,156,252
mvva 48,156,220
la 140,252,50,2048
s_xor_b32 s53,s60,s98
s_xor_b32 s53,s53,512
mvva 48,152,212
tv 156,229,61440
mvva 48,140,204
tv 158,229,63488
mvva 48,136,196
tv 152,229,57344
s_waitcnt lgkmcnt(3)
mvva 64,128,196
la 148,250,50,0
tv 140,229,53248
mvva 64,132,204
tv 136,229,49152
tv 138,229,51200
mvva 64,144,212
tv 142,229,55296
mvva 64,148,220
tv 154,229,59392
mvva 68,148,252
buffer_load_dword v253,v242,s[8:11],s53 offen sc1
mvva 68,144,244
mvva 68,132,236
mvva 68,128,228
mvva 72,128,192
mvva 72,132,200
la 156,250,50,2048
mvva 72,144,208
mvva 72,148,216
mvva 76,148,248
mvva 76,144,240
la 188,250,50,3072
mvva 76,132,232
tv 148,230,61440
mvva 76,128,224
tv 144,230,57344
s_waitcnt lgkmcnt(2)
mvva 92,136,224
tv 132,230,53248
mvva 92,140,232
la 180,250,50,1024
tv 128,230,49152
tv 130,230,51200
mvva 92,152,240
tv 134,230,55296
mvva 92,156,248
tv 146,230,59392
mvva 88,156,216
tv 150,230,63488
mvva 88,152,208
la 172,252,50,3072
mvva 88,140,200
mvva 88,136,192
mvva 84,136,228
mvva 84,140,236
la 164,252,50,1024
s_add_u32 s50,s50,s55
mvva 84,152,244
mvva 84,156,252
mvva 80,156,220
mvva 80,152,212
mvva 80,140,204
la 128,252,50,0
mvva 80,136,196
tv 152,231,57344
s_waitcnt lgkmcnt(1)
mvva 96,128,196
tv 140,231,53248
tv 142,231,55296
mvva 96,132,204
tv 136,231,49152
tv 138,231,51200
mvva 96,144,212
la 136,252,50,2048
mvva 96,148,220
tv 154,231,59392
mvva 100,148,252
tv 156,231,61440
tv 158,231,63488
mvva 100,144,244
mvva 100,132,236
la 144,250,50,0
mvva 100,128,228
mvva 104,128,192
mvva 104,132,200
mvva 104,144,208
la 152,250,50,2048
mvva 104,148,216
mvva 108,148,248
mvva 108,144,240
mvva 108,132,232
la 184,250,50,3072
mvva 108,128,224
s_barrier
s_waitcnt lgkmcnt(0)
mvva 124,136,224
tv 132,228,4096
tv 134,228,6144
tv 144,228,8192
mvva 124,140,232
tv 128,228,0
tv 130,228,2048
mvva 124,152,240
mvva 124,156,248
la 176,250,50,1024
tv 146,228,10240
mvva 120,156,216
tv 148,228,12288
tv 150,228,14336
mvva 120,152,208
mvva 120,140,200
mvva 120,136,192
la 168,252,50,3072
mvva 116,136,228
L_vs28:
s_cbranch_execz L_vr1
mvva 116,140,236
mvva 116,152,244
mvva 116,156,252
la 160,252,50,1024
mvva 112,156,220
mvva 112,152,212
mvva 112,140,204
tv 156,229,12288
tv 158,229,14336
mvva 112,136,196
s_waitcnt lgkmcnt(2)
mvva 0,128,68
dl 249,36,51,2048
tv 140,229,4096
mvva 0,132,76
tv 136,229,0
tv 138,229,2048
mvva 0,144,84
tv 142,229,6144
s_waitcnt vmcnt(22)
mvva 0,148,92
tv 152,229,8192
tv 154,229,10240
mvva 4,148,124
dl 247,36,51,3072
s_lshl_b32 s51,s98,5
s_xor_b32 s51,s64,s51
s_add_u32 m0,s52,0
s_xor_b32 s51,s51,4456448
mvva 4,144,116
mvva 4,132,108
mvva 4,128,100
mvva 8,128,64
dl 245,36,51,0
mvva 8,132,72
mvva 8,144,80
mvva 8,148,88
s_waitcnt vmcnt(16)
v_cmp_ne_u32_e32 vcc,4,v253
mvva 12,148,120
dl 246,36,51,1024
s_cbranch_vccnz L_spa6o
L_spra6o:
mvva 12,144,112
s_xor_b32 s50,s67,s98
mvva 12,132,104
s_xor_b32 s50,s50,131072
tv 148,230,12288
tv 150,230,14336
mvva 12,128,96
s_waitcnt lgkmcnt(2)
mvva 28,136,96
dl 249,36,51,2048
tv 132,230,4096
mvva 28,140,104
tv 128,230,0
tv 130,230,2048
mvva 28,152,112
tv 134,230,6144
mvva 28,156,120
tv 144,230,8192
tv 146,230,10240
mvva 24,156,88
mvva 24,152,80
la 4,252,50,0
mvva 24,140,72
s_xor_b32 s53,s60,s98
mvva 24,136,64
s_xor_b32 s53,s53,8704
mvva 20,136,100
mvva 20,140,108
la 12,252,50,2048
mvva 20,152,116
mvva 20,156,124
mvva 16,156,92
mvva 16,152,84
buffer_load_dword v253,v242,s[8:11],s53 offen sc1
mvva 16,140,76
tv 156,231,12288
mvva 16,136,68
tv 152,231,8192
s_waitcnt lgkmcnt(2)
mvva 32,128,68
tv 140,231,4096
mvva 32,132,76
la 20,250,50,0
tv 136,231,0
tv 138,231,2048
mvva 32,144,84
tv 142,231,6144
mvva 32,148,92
tv 154,231,10240
mvva 36,148,124
tv 158,231,14336
mvva 36,144,116
la 28,250,50,2048
mvva 36,132,108
mvva 36,128,100
mvva 40,128,64
mvva 40,132,72
mvva 40,144,80
la 60,250,50,3072
mvva 40,148,88
mvva 44,148,120
mvva 44,144,112
mvva 44,132,104
la 52,250,50,1024
mvva 44,128,96
tv 144,228,24576
s_waitcnt lgkmcnt(1)
mvva 60,136,96
tv 132,228,20480
tv 134,228,22528
mvva 60,140,104
tv 128,228,16384
tv 130,228,18432
mvva 60,152,112
la 44,252,50,3072
mvva 60,156,120
tv 146,228,26624
mvva 56,156,88
tv 148,228,28672
tv 150,228,30720
mvva 56,152,80
mvva 56,140,72
mvva 56,136,64
la 36,252,50,1024
s_add_u32 s50,s50,s55
mvva 52,136,100
mvva 52,140,108
mvva 52,152,116
mvva 52,156,124
la 0,252,50,0
mvva 48,156,92
mvva 48,152,84
mvva 48,140,76
tv 156,229,28672
mvva 48,136,68
la 8,252,50,2048
tv 152,229,24576
s_waitcnt lgkmcnt(2)
mvva 64,128,68
tv 140,229,20480
mvva 64,132,76
tv 136,229,16384
tv 138,229,18432
mvva 64,144,84
tv 142,229,22528
mvva 64,148,92
la 16,250,50,0
tv 154,229,26624
mvva 68,148,124
tv 158,229,30720
mvva 68,144,116
mvva 68,132,108
mvva 68,128,100
la 24,250,50,2048
mvva 72,128,64
mvva 72,132,72
mvva 72,144,80
mvva 72,148,88
mvva 76,148,120
la 56,250,50,3072
mvva 76,144,112
mvva 76,132,104
tv 148,224,45056
tv 150,224,47104
mvva 76,128,96
s_waitcnt lgkmcnt(2)
mvva 92,136,96
la 48,250,50,1024
tv 132,224,36864
mvva 92,140,104
tv 128,224,32768
tv 130,224,34816
mvva 92,152,112
tv 134,224,38912
mvva 92,156,120
tv 144,224,40960
tv 146,224,43008
mvva 88,156,88
la 40,252,50,3072
mvva 88,152,80
mvva 88,140,72
mvva 88,136,64
mvva 84,136,100
mvva 84,140,108
la 32,252,50,1024
mvva 84,152,116
mvva 84,156,124
mvva 80,156,92
mvva 80,152,84
dl 247,36,51,3072
s_lshl_b32 s51,s98,5
s_xor_b32 s51,s64,s51
s_add_u32 m0,s52,16384
s_xor_b32 s51,s51,4718592
mvva 80,140,76
tv 152,225,40960
mvva 80,136,68
tv 154,225,43008
s_waitcnt lgkmcnt(2)
mvva 0,128,132
tv 140,225,36864
mvva 0,132,140
dl 245,36,51,0
tv 136,225,32768
tv 138,225,34816
mvva 0,144,148
tv 142,225,38912
s_waitcnt vmcnt(24)
mvva 0,148,156
mvva 4,148,188
tv 156,225,45056
tv 158,225,47104
mvva 4,144,180
dl 246,36,51,1024
s_waitcnt vmcnt(17)
v_cmp_ne_u32_e32 vcc,4,v253
mvva 4,132,172
s_cbranch_vccnz L_spa7o
L_spra7o:
mvva 4,128,164
s_xor_b32 s50,s67,s98
mvva 8,128,128
s_xor_b32 s50,s50,139264
mvva 8,132,136
dl 249,36,51,2048
mvva 8,144,144
mvva 8,148,152
mvva 12,148,184
mvva 12,144,176
mvva 12,132,168
dl 247,36,51,3072
mvva 12,128,160
s_barrier
tv 144,226,40960
s_lshl_b32 s51,s98,5
s_waitcnt lgkmcnt(1)
mvva 28,136,160
s_xor_b32 s53,s60,s98
tv 132,226,36864
tv 134,226,38912
s_xor_b32 s53,s53,16896
mvva 28,140,168
s_xor_b32 s51,s65,s51
tv 128,226,32768
tv 130,226,34816
mvva 28,152,176
lv 164,252,50,0
mvva 28,156,184
s_add_u32 m0,s52,98304
tv 146,226,43008
s_xor_b32 s51,s51,4456448
mvva 24,156,152
tv 148,226,45056
tv 150,226,47104
mvva 24,152,144
mvva 24,140,136
buffer_load_dword v253,v242,s[8:11],s53 offen sc1
mvva 24,136,128
mvva 20,136,164
mvva 20,140,172
mvva 20,152,180
lv 172,252,50,2048
mvva 20,156,188
mvva 16,156,156
mvva 16,152,148
mvva 16,140,140
lv 180,250,50,0
mvva 16,136,132
tv 152,227,40960
s_waitcnt lgkmcnt(1)
mvva 32,128,132
tv 140,227,36864
tv 142,227,38912
mvva 32,132,140
tv 136,227,32768
tv 138,227,34816
mvva 32,144,148
mvva 32,148,156
lv 188,250,50,2048
tv 154,227,43008
mvva 36,148,188
tv 156,227,45056
tv 158,227,47104
mvva 36,144,180
mvva 36,132,172
mvva 36,128,164
lv 220,250,50,3072
mvva 40,128,128
mvva 40,132,136
mvva 40,144,144
mvva 40,148,152
lv 212,250,50,1024
mvva 44,148,184
mvva 44,144,176
mvva 44,132,168
tv 148,224,61440
tv 150,224,63488
mvva 44,128,160
s_waitcnt lgkmcnt(2)
mvva 60,136,160
lv 204,252,50,3072
tv 132,224,53248
mvva 60,140,168
tv 128,224,49152
tv 130,224,51200
mvva 60,152,176
tv 134,224,55296
mvva 60,156,184
tv 144,224,57344
tv 146,224,59392
mvva 56,156,152
lv 196,252,50,1024
s_add_u32 s50,s50,s55
mvva 56,152,144
mvva 56,140,136
mvva 56,136,128
mvva 52,136,164
lv 160,252,50,0
mvva 52,140,172
mvva 52,152,180
mvva 52,156,188
mvva 48,156,156
lv 168,252,50,2048
mvva 48,152,148
mvva 48,140,140
tv 156,225,61440
tv 158,225,63488
mvva 48,136,132
s_waitcnt lgkmcnt(2)
mvva 64,128,132
lv 176,250,50,0
tv 140,225,53248
mvva 64,132,140
tv 136,225,49152
tv 138,225,51200
mvva 64,144,148
tv 142,225,55296
mvva 64,148,156
tv 152,225,57344
tv 154,225,59392
mvva 68,148,188
mvva 68,144,180
lv 184,250,50,2048
mvva 68,132,172
mvva 68,128,164
mvva 72,128,128
mvva 72,132,136
lv 216,250,50,3072
mvva 72,144,144
mvva 72,148,152
mvva 76,148,184
mvva 76,144,176
lv 208,250,50,1024
mvva 76,132,168
tv 148,232,12288
mvva 76,128,160
tv 144,232,8192
s_waitcnt lgkmcnt(2)
mvva 92,136,160
tv 132,232,4096
mvva 92,140,168
tv 128,232,0
tv 130,232,2048
mvva 92,152,176
lv 200,252,50,3072
tv 134,232,6144
mvva 92,156,184
tv 146,232,10240
mvva 88,156,152
tv 150,232,14336
mvva 88,152,144
mvva 88,140,136
lv 192,252,50,1024
mvva 88,136,128
mvva 84,136,164
mvva 84,140,172
mvva 84,152,180
dl 245,36,51,0
mvva 84,156,188
mvva 80,156,156
mvva 80,152,148
mvva 80,140,140
dl 246,36,51,1024
mvva 80,136,132
tv 152,233,8192
s_waitcnt vmcnt(17)
v_cmp_ne_u32_e32 vcc,4,v253
s_waitcnt lgkmcnt(1)
mvva 0,128,4
s_cbranch_vccnz L_spa8o
L_spra8o:
tv 140,233,4096
tv 142,233,6144
mvva 0,132,12
s_xor_b32 s50,s67,s98
tv 136,233,0
s_xor_b32 s50,s50,147456
tv 138,233,2048
mvva 0,144,20
dl 249,36,51,2048
mvva 0,148,28
tv 154,233,10240
v_and_b32_e32 v242,48,v244
mvva 4,148,60
tv 156,233,12288
tv 158,233,14336
mvva 4,144,52
v_lshlrev_b32_e32 v242,3,v242
mvva 4,132,44
mvva 4,128,36
la 196,252,50,0
mvva 8,128,0
s_andn2_b32 s53,s61,384
mvva 8,132,8
mvva 8,144,16
mvva 8,148,24
la 204,252,50,2048
mvva 12,148,56
mvva 12,144,48
mvva 12,132,40
tv 148,234,12288
mvva 12,128,32
buffer_load_dword v253,v242,s[8:11],s53 offen sc1
tv 144,234,8192
s_waitcnt lgkmcnt(2)
mvva 28,136,32
tv 132,234,4096
mvva 28,140,40
tv 128,234,0
tv 130,234,2048
mvva 28,152,48
tv 134,234,6144
mvva 28,156,56
la 212,250,50,0
tv 146,234,10240
mvva 24,156,24
tv 150,234,14336
mvva 24,152,16
mvva 24,140,8
mvva 24,136,0
la 220,250,50,2048
mvva 20,136,36
mvva 20,140,44
mvva 20,152,52
mvva 20,156,60
mvva 16,156,28
la 252,250,50,3072
mvva 16,152,20
mvva 16,140,12
tv 156,235,12288
tv 158,235,14336
mvva 16,136,4
s_waitcnt lgkmcnt(2)
mvva 32,128,4
la 244,250,50,1024
tv 140,235,4096
mvva 32,132,12
tv 136,235,0
tv 138,235,2048
mvva 32,144,20
tv 142,235,6144
mvva 32,148,28
tv 152,235,8192
tv 154,235,10240
mvva 36,148,60
la 236,252,50,3072
mvva 36,144,52
mvva 36,132,44
mvva 36,128,36
mvva 40,128,0
mvva 40,132,8
la 228,252,50,1024
s_add_u32 s50,s50,s55
mvva 40,144,16
mvva 40,148,24
mvva 44,148,56
mvva 44,144,48
la 192,252,50,0
mvva 44,132,40
mvva 44,128,32
s_barrier
s_waitcnt lgkmcnt(0)
mvva 60,136,32
tv 132,232,20480
tv 144,232,24576
mvva 60,140,40
la 200,252,50,2048
tv 128,232,16384
tv 130,232,18432
mvva 60,152,48
tv 134,232,22528
mvva 60,156,56
tv 146,232,26624
mvva 56,156,24
tv 148,232,28672
tv 150,232,30720
mvva 56,152,16
la 208,250,50,0
mvva 56,140,8
mvva 56,136,0
mvva 52,136,36
mvva 52,140,44
la 216,250,50,2048
mvva 52,152,52
mvva 52,156,60
mvva 48,156,28
mvva 48,152,20
mvva 48,140,12
la 248,250,50,3072
mvva 48,136,4
tv 152,233,24576
s_waitcnt lgkmcnt(1)
mvva 64,128,4
tv 140,233,20480
tv 142,233,22528
mvva 64,132,12
tv 136,233,16384
tv 138,233,18432
mvva 64,144,20
la 240,250,50,1024
mvva 64,148,28
tv 154,233,26624
mvva 68,148,60
tv 156,233,28672
tv 158,233,30720
mvva 68,144,52
mvva 68,132,44
la 232,252,50,3072
mvva 68,128,36
mvva 72,128,0
mvva 72,132,8
mvva 72,144,16
la 224,252,50,1024
mvva 72,148,24
L_vs29:
s_cbranch_execz L_vr1
mvva 76,148,56
mvva 76,144,48
mvva 76,132,40
dl 247,36,51,3072
s_lshl_b32 s51,s98,5
s_xor_b32 s51,s65,s51
s_add_u32 m0,s52,114688
s_xor_b32 s51,s51,4718592
mvva 76,128,32
tv 144,224,8192
s_waitcnt lgkmcnt(1)
mvva 92,136,32
tv 132,224,4096
tv 134,224,6144
mvva 92,140,40
tv 128,224,0
tv 130,224,2048
mvva 92,152,48
mvva 92,156,56
dl 245,36,51,0
tv 146,224,10240
mvva 88,156,24
tv 148,224,12288
tv 150,224,14336
mvva 88,152,16
mvva 88,140,8
mvva 88,136,0
dl 246,36,51,1024
s_waitcnt vmcnt(17)
v_cmp_ne_u32_e32 vcc,4,v253
mvva 84,136,36
s_cbranch_vccnz L_sppo
L_sprpo:
mvva 84,140,44
mvva 84,152,52
mvva 84,156,60
dl 249,36,51,2048
mvva 80,156,28
mvva 80,152,20
mvva 80,140,12
tv 156,225,12288
mvva 80,136,4
tv 152,225,8192
s_waitcnt lgkmcnt(2)
mvvv 0,128,164
dl 247,36,51,3072
tv 140,225,4096
mvvv 0,132,172
tv 136,225,0
tv 138,225,2048
mvvv 0,144,180
tv 142,225,6144
mvvv 0,148,188
tv 154,225,10240
mvvv 4,148,220
tv 158,225,14336
mvvv 4,144,212
mvvv 4,132,204
mvvv 4,128,196
mvvv 8,128,160
mvvv 8,132,168
mvvv 8,144,176
mvvv 8,148,184
mvvv 12,148,216
mvvv 12,144,208
mvvv 12,132,200
tv 148,224,28672
mvvv 12,128,192
tv 144,224,24576
s_waitcnt lgkmcnt(2)
mvvv 28,136,192
)"
R"(tv 132,224,20480
mvvv 28,140,200
tv 128,224,16384
tv 130,224,18432
mvvv 28,152,208
tv 134,224,22528
mvvv 28,156,216
tv 146,224,26624
mvvv 24,156,184
tv 150,224,30720
mvvv 24,152,176
mvvv 24,140,168
mvvv 24,136,160
mvvv 20,136,196
mvvv 20,140,204
mvvv 20,152,212
mvvv 20,156,220
mvvv 16,156,188
mvvv 16,152,180
mvvv 16,140,172
tv 156,225,28672
mvvv 16,136,164
tv 152,225,24576
s_waitcnt lgkmcnt(2)
mvva 0,128,196
tv 140,225,20480
mvva 0,132,204
tv 136,225,16384
tv 138,225,18432
s_waitcnt vmcnt(14)
mvva 0,144,212
tv 142,225,22528
mvva 0,148,220
tv 154,225,26624
mvva 4,148,252
tv 158,225,30720
mvva 4,144,244
mvva 4,132,236
s_waitcnt vmcnt(13)
mvva 4,128,228
s_waitcnt vmcnt(12)
mvva 8,128,192
cv 0,0,1
cv 1,2,3
s_waitcnt vmcnt(11)
mvva 8,132,200
s_waitcnt vmcnt(10)
mvva 8,144,208
cv 4,4,5
cv 5,6,7
s_waitcnt vmcnt(9)
mvva 8,148,216
s_waitcnt vmcnt(8)
mvva 12,148,248
s_waitcnt vmcnt(7)
mvva 12,144,240
s_waitcnt vmcnt(6)
mvva 12,132,232
tv 148,226,12288
s_waitcnt vmcnt(5)
mvva 12,128,224
tv 144,226,8192
cv 8,8,9
s_waitcnt lgkmcnt(2)
mvva 28,136,224
tv 132,226,4096
cv 9,10,11
mvva 28,140,232
tv 128,226,0
tv 130,226,2048
mvva 28,152,240
tv 134,226,6144
cv 12,12,13
mvva 28,156,248
tv 146,226,10240
cv 13,14,15
mvva 24,156,216
tv 150,226,14336
mvva 24,152,208
mvva 24,140,200
mvva 24,136,192
mvva 20,136,228
cv 14,28,29
cv 15,30,31
mvva 20,140,236
buffer_store_dwordx4 v[12:15],v244,s[8:11],s56 offen offset:3072 sc0 sc1
mvva 20,152,244
mvva 20,156,252
mvva 16,156,220
cv 10,24,25
cv 11,26,27
mvva 16,152,212
buffer_store_dwordx4 v[8:11],v244,s[8:11],s56 offen offset:2048 sc0 sc1
mvva 16,140,204
tv 156,227,12288
mvva 16,136,196
cv 6,20,21
cv 7,22,23
s_waitcnt lgkmcnt(1)
mvvv 32,128,164
tv 140,227,4096
mvvv 32,132,172
buffer_store_dwordx4 v[4:7],v244,s[8:11],s56 offen offset:1024 sc0 sc1
tv 136,227,0
tv 138,227,2048
mvvv 32,144,180
tv 142,227,6144
cv 2,16,17
mvvv 32,148,188
tv 152,227,8192
tv 154,227,10240
mvvv 36,148,220
tv 158,227,14336
cv 3,18,19
mvvv 36,144,212
buffer_store_dwordx4 v[0:3],v244,s[8:11],s56 offen offset:0 sc0 sc1
mvvv 36,132,204
mvvv 36,128,196
mvvv 40,128,160
mvvv 40,132,168
mvvv 40,144,176
mvvv 40,148,184
mvvv 44,148,216
mvvv 44,144,208
mvvv 44,132,200
tv 148,226,28672
mvvv 44,128,192
s_waitcnt vmcnt(4)
s_barrier
tv 144,226,24576
s_waitcnt lgkmcnt(2)
mvvv 60,136,192
tv 132,226,20480
mvvv 60,140,200
tv 128,226,16384
tv 130,226,18432
mvvv 60,152,208
tv 134,226,22528
mvvv 60,156,216
tv 146,226,26624
mvvv 56,156,184
tv 150,226,30720
mvvv 56,152,176
mvvv 56,140,168
mvvv 56,136,160
mvvv 52,136,196
mvvv 52,140,204
mvvv 52,152,212
mvvv 52,156,220
mvvv 48,156,188
mvvv 48,152,180
mvvv 48,140,172
tv 156,227,28672
mvvv 48,136,164
tv 152,227,24576
s_waitcnt lgkmcnt(2)
mvva 32,128,196
tv 140,227,20480
mvva 32,132,204
tv 136,227,16384
tv 138,227,18432
mvva 32,144,212
tv 142,227,22528
mvva 32,148,220
tv 154,227,26624
mvva 36,148,252
tv 158,227,30720
mvva 36,144,244
mvva 36,132,236
mvva 36,128,228
mvva 40,128,192
mvva 40,132,200
cv 32,32,33
cv 33,34,35
mvva 40,144,208
mvva 40,148,216
mvva 44,148,248
mvva 44,144,240
cv 36,36,37
cv 37,38,39
mvva 44,132,232
tv 148,228,45056
mvva 44,128,224
tv 144,228,40960
cv 40,40,41
s_waitcnt lgkmcnt(2)
mvva 60,136,224
tv 132,228,36864
cv 41,42,43
mvva 60,140,232
tv 128,228,32768
tv 130,228,34816
mvva 60,152,240
tv 134,228,38912
cv 44,44,45
mvva 60,156,248
tv 146,228,43008
cv 45,46,47
mvva 56,156,216
tv 150,228,47104
mvva 56,152,208
mvva 56,140,200
mvva 56,136,192
mvva 52,136,228
cv 46,60,61
cv 47,62,63
mvva 52,140,236
buffer_store_dwordx4 v[44:47],v244,s[8:11],s57 offen offset:3072 sc0 sc1
mvva 52,152,244
mvva 52,156,252
mvva 48,156,220
cv 42,56,57
cv 43,58,59
mvva 48,152,212
buffer_store_dwordx4 v[40:43],v244,s[8:11],s57 offen offset:2048 sc0 sc1
mvva 48,140,204
tv 156,229,45056
mvva 48,136,196
cv 38,52,53
cv 39,54,55
s_waitcnt lgkmcnt(1)
mvvv 64,128,164
tv 140,229,36864
mvvv 64,132,172
buffer_store_dwordx4 v[36:39],v244,s[8:11],s57 offen offset:1024 sc0 sc1
tv 136,229,32768
tv 138,229,34816
mvvv 64,144,180
tv 142,229,38912
cv 34,48,49
mvvv 64,148,188
tv 152,229,40960
tv 154,229,43008
mvvv 68,148,220
tv 158,229,47104
cv 35,50,51
mvvv 68,144,212
buffer_store_dwordx4 v[32:35],v244,s[8:11],s57 offen offset:0 sc0 sc1
mvvv 68,132,204
mvvv 68,128,196
mvvv 72,128,160
mvvv 72,132,168
mvvv 72,144,176
mvvv 72,148,184
mvvv 76,148,216
mvvv 76,144,208
mvvv 76,132,200
tv 148,228,61440
mvvv 76,128,192
tv 144,228,57344
s_waitcnt lgkmcnt(2)
mvvv 92,136,192
tv 132,228,53248
mvvv 92,140,200
tv 128,228,49152
tv 130,228,51200
mvvv 92,152,208
tv 134,228,55296
mvvv 92,156,216
tv 146,228,59392
mvvv 88,156,184
tv 150,228,63488
mvvv 88,152,176
mvvv 88,140,168
mvvv 88,136,160
mvvv 84,136,196
mvvv 84,140,204
mvvv 84,152,212
mvvv 84,156,220
mvvv 80,156,188
mvvv 80,152,180
mvvv 80,140,172
tv 156,229,61440
mvvv 80,136,164
tv 152,229,57344
s_waitcnt lgkmcnt(2)
mvva 64,128,196
tv 140,229,53248
mvva 64,132,204
tv 136,229,49152
tv 138,229,51200
mvva 64,144,212
tv 142,229,55296
mvva 64,148,220
tv 154,229,59392
mvva 68,148,252
tv 158,229,63488
mvva 68,144,244
mvva 68,132,236
mvva 68,128,228
s_xor_b32 s82,s59,12
mvva 72,128,192
s_mul_i32 s82,s82,12288
cv 64,64,65
s_add_u32 s82,s82,0
mvva 72,132,200
lv 16,244,82,0
mvva 72,144,208
cv 65,66,67
cv 68,68,69
mvva 72,148,216
cv 69,70,71
mvva 76,148,248
mvva 76,144,240
lv 20,244,82,1024
mvva 76,132,232
tv 148,230,28672
mvva 76,128,224
tv 144,230,24576
cv 72,72,73
s_waitcnt lgkmcnt(2)
mvva 92,136,224
tv 132,230,20480
cv 73,74,75
mvva 92,140,232
tv 128,230,16384
tv 130,230,18432
mvva 92,152,240
lv 24,244,82,2048
tv 134,230,22528
mvva 92,156,248
tv 146,230,26624
cv 76,76,77
mvva 88,156,216
tv 150,230,30720
cv 77,78,79
mvva 88,152,208
mvva 88,140,200
lv 28,244,82,3072
mvva 88,136,192
cv 78,92,93
cv 79,94,95
mvva 84,136,228
mvva 84,140,236
mvva 84,152,244
buffer_store_dwordx4 v[76:79],v244,s[8:11],s58 offen offset:3072 sc0 sc1
mvva 84,156,252
mvva 80,156,220
cv 74,88,89
cv 75,90,91
mvva 80,152,212
mvva 80,140,204
buffer_store_dwordx4 v[72:75],v244,s[8:11],s58 offen offset:2048 sc0 sc1
tv 156,231,28672
mvva 80,136,196
cv 70,84,85
cv 71,86,87
s_waitcnt lgkmcnt(1)
mvva 96,128,68
tv 140,231,20480
mvva 96,132,76
tv 136,231,16384
tv 138,231,18432
mvva 96,144,84
buffer_store_dwordx4 v[68:71],v244,s[8:11],s58 offen offset:1024 sc0 sc1
tv 142,231,22528
mvva 96,148,92
tv 152,231,24576
tv 154,231,26624
mvva 100,148,124
tv 158,231,30720
cv 66,80,81
cv 67,82,83
mvva 100,144,116
mvva 100,132,108
mvva 100,128,100
buffer_store_dwordx4 v[64:67],v244,s[8:11],s58 offen offset:0 sc0 sc1
mvva 104,128,64
mvva 104,132,72
mvva 104,144,80
mvva 104,148,88
mvva 108,148,120
mvva 108,144,112
mvva 108,132,104
tv 148,226,61440
mvva 108,128,96
tv 144,226,57344
s_waitcnt lgkmcnt(2)
mvva 124,136,96
tv 132,226,53248
mvva 124,140,104
tv 128,226,49152
tv 130,226,51200
mvva 124,152,112
tv 134,226,55296
mvva 124,156,120
tv 146,226,59392
mvva 120,156,88
tv 150,226,63488
mvva 120,152,80
mvva 120,140,72
mvva 120,136,64
mvva 116,136,100
mvva 116,140,108
s_waitcnt vmcnt(7)
v_max_u32_e32 v248,v16,v17
mvva 116,152,116
v_max3_u32 v248,v248,v18,v19
s_waitcnt vmcnt(6)
v_max3_u32 v248,v248,v20,v21
v_max3_u32 v248,v248,v22,v23
mvva 116,156,124
s_waitcnt vmcnt(5)
v_max3_u32 v248,v248,v24,v25
v_max3_u32 v248,v248,v26,v27
s_waitcnt vmcnt(4)
v_max3_u32 v248,v248,v28,v29
mvva 112,156,92
v_max3_u32 v248,v248,v30,v31
v_cmp_eq_u32_e32 vcc,-1,v248
mvva 112,152,84
s_cbranch_vccnz L_spx0o
L_sprx0o:
mvva 112,140,76
tv 156,227,61440
tv 158,227,63488
v_lshlrev_b32_e32 v251,16,v18
v_and_b32_e32 v255,4294901760,v18
mvva 112,136,68
tv 152,227,57344
tv 154,227,59392
s_waitcnt lgkmcnt(4)
mvva 96,128,132
v_lshlrev_b32_e32 v253,16,v19
v_and_b32_e32 v242,4294901760,v19
mvva 96,132,140
tv 136,227,49152
tv 138,227,51200
mvva 96,144,148
tv 140,227,53248
tv 142,227,55296
v_add_f32_e32 v112,v112,v251
mvva 96,148,156
v_add_f32_e32 v113,v113,v255
v_add_f32_e32 v114,v114,v253
mvva 100,148,188
v_add_f32_e32 v115,v115,v242
v_lshlrev_b32_e32 v251,16,v22
mvva 100,144,180
v_and_b32_e32 v255,4294901760,v22
v_lshlrev_b32_e32 v253,16,v23
mvva 100,132,172
v_and_b32_e32 v242,4294901760,v23
v_add_f32_e32 v116,v116,v251
v_add_f32_e32 v117,v117,v255
mvva 100,128,164
v_add_f32_e32 v118,v118,v253
v_add_f32_e32 v119,v119,v242
mvva 104,128,128
v_lshlrev_b32_e32 v251,16,v26
v_and_b32_e32 v255,4294901760,v26
mvva 104,132,136
v_lshlrev_b32_e32 v253,16,v27
v_and_b32_e32 v242,4294901760,v27
mvva 104,144,144
v_add_f32_e32 v120,v120,v251
v_add_f32_e32 v121,v121,v255
v_add_f32_e32 v122,v122,v253
mvva 104,148,152
v_add_f32_e32 v123,v123,v242
v_lshlrev_b32_e32 v251,16,v30
mvva 108,148,184
v_and_b32_e32 v255,4294901760,v30
v_lshlrev_b32_e32 v253,16,v31
mvva 108,144,176
v_and_b32_e32 v242,4294901760,v31
v_add_f32_e32 v124,v124,v251
mvva 108,132,168
v_add_f32_e32 v125,v125,v255
v_add_f32_e32 v126,v126,v253
v_add_f32_e32 v127,v127,v242
mvva 108,128,160
tv 144,234,24576
v_lshlrev_b32_e32 v251,16,v16
s_waitcnt lgkmcnt(1)
mvva 124,136,160
tv 132,234,20480
v_and_b32_e32 v255,4294901760,v16
mvva 124,140,168
L_vs30:
s_cbranch_execz L_vr1
tv 128,234,16384
tv 130,234,18432
mvva 124,152,176
tv 134,234,22528
v_lshlrev_b32_e32 v253,16,v17
mvva 124,156,184
tv 146,234,26624
v_and_b32_e32 v242,4294901760,v17
mvva 120,156,152
tv 148,234,28672
tv 150,234,30720
mvva 120,152,144
v_add_f32_e32 v96,v96,v251
v_add_f32_e32 v97,v97,v255
mvva 120,140,136
v_add_f32_e32 v98,v98,v253
v_add_f32_e32 v99,v99,v242
mvva 120,136,128
v_lshlrev_b32_e32 v251,16,v20
v_and_b32_e32 v255,4294901760,v20
mvva 116,136,164
v_lshlrev_b32_e32 v253,16,v21
v_and_b32_e32 v242,4294901760,v21
mvva 116,140,172
v_add_f32_e32 v100,v100,v251
v_add_f32_e32 v101,v101,v255
mvva 116,152,180
v_add_f32_e32 v102,v102,v253
v_add_f32_e32 v103,v103,v242
mvva 116,156,188
v_lshlrev_b32_e32 v251,16,v24
v_and_b32_e32 v255,4294901760,v24
mvva 112,156,156
v_lshlrev_b32_e32 v253,16,v25
v_and_b32_e32 v242,4294901760,v25
mvva 112,152,148
v_add_f32_e32 v104,v104,v251
v_add_f32_e32 v105,v105,v255
mvva 112,140,140
tv 156,235,28672
v_add_f32_e32 v106,v106,v253
mvva 112,136,132
tv 152,235,24576
v_add_f32_e32 v107,v107,v242
s_waitcnt lgkmcnt(2)
mvva 96,128,4
tv 140,235,20480
v_lshlrev_b32_e32 v251,16,v28
mvva 96,132,12
tv 136,235,16384
tv 138,235,18432
mvva 96,144,20
tv 142,235,22528
v_and_b32_e32 v255,4294901760,v28
mvva 96,148,28
tv 154,235,26624
v_lshlrev_b32_e32 v253,16,v29
mvva 100,148,60
tv 158,235,30720
v_and_b32_e32 v242,4294901760,v29
mvva 100,144,52
v_add_f32_e32 v108,v108,v251
v_add_f32_e32 v109,v109,v255
mvva 100,132,44
v_add_f32_e32 v110,v110,v253
v_add_f32_e32 v111,v111,v242
mvva 100,128,36
mvva 104,128,0
mvva 104,132,8
mvva 104,144,16
mvva 104,148,24
mvva 108,148,56
mvva 108,144,48
mvva 108,132,40
tv 148,230,45056
mvva 108,128,32
tv 144,230,40960
s_waitcnt lgkmcnt(2)
mvva 124,136,32
s_xor_b32 s83,s59,8
tv 132,230,36864
s_mul_i32 s83,s83,12288
mvva 124,140,40
s_add_u32 s83,s83,4096
tv 128,230,32768
tv 130,230,34816
mvva 124,152,48
lv 48,244,83,0
tv 134,230,38912
mvva 124,156,56
tv 146,230,43008
mvva 120,156,24
lv 52,244,83,1024
tv 150,230,47104
mvva 120,152,16
mvva 120,140,8
lv 56,244,83,2048
mvva 120,136,0
mvva 116,136,36
lv 60,244,83,3072
mvva 116,140,44
mvva 116,152,52
s_xor_b32 s84,s59,4
mvva 116,156,60
s_mul_i32 s84,s84,12288
mvva 112,156,28
s_add_u32 s84,s84,8192
mvva 112,152,20
lv 80,244,84,0
mvva 112,140,12
tv 156,231,45056
mvva 112,136,4
lv 84,244,84,1024
tv 152,231,40960
s_waitcnt lgkmcnt(2)
mvvv 96,128,164
tv 140,231,36864
mvvv 96,132,172
lv 88,244,84,2048
tv 136,231,32768
tv 138,231,34816
mvvv 96,144,180
tv 142,231,38912
mvvv 96,148,188
lv 92,244,84,3072
tv 154,231,43008
mvvv 100,148,220
tv 158,231,47104
mvvv 100,144,212
mvvv 100,132,204
mvvv 100,128,196
mvvv 104,128,160
mvvv 104,132,168
mvvv 104,144,176
mvvv 104,148,184
mvvv 108,148,216
mvvv 108,144,208
mvvv 108,132,200
tv 148,230,61440
mvvv 108,128,192
tv 144,230,57344
s_waitcnt lgkmcnt(2)
mvvv 124,136,192
tv 132,230,53248
mvvv 124,140,200
tv 128,230,49152
tv 130,230,51200
mvvv 124,152,208
tv 134,230,55296
mvvv 124,156,216
tv 146,230,59392
mvvv 120,156,184
tv 150,230,63488
mvvv 120,152,176
mvvv 120,140,168
mvvv 120,136,160
mvvv 116,136,196
mvvv 116,140,204
mvvv 116,152,212
mvvv 116,156,220
mvvv 112,156,188
mvvv 112,152,180
mvvv 112,140,172
tv 156,231,61440
mvvv 112,136,164
tv 152,231,57344
s_waitcnt lgkmcnt(2)
mvva 96,128,196
tv 140,231,53248
mvva 96,132,204
tv 136,231,49152
tv 138,231,51200
mvva 96,144,212
tv 142,231,55296
mvva 96,148,220
tv 154,231,59392
mvva 100,148,252
tv 158,231,63488
mvva 100,144,244
mvva 100,132,236
mvva 100,128,228
mvva 104,128,192
mvva 104,132,200
mvva 104,144,208
mvva 104,148,216
s_waitcnt vmcnt(7)
v_max_u32_e32 v248,v48,v49
mvva 108,148,248
v_max3_u32 v248,v248,v50,v51
s_waitcnt vmcnt(6)
v_max3_u32 v248,v248,v52,v53
mvva 108,144,240
v_max3_u32 v248,v248,v54,v55
s_waitcnt vmcnt(5)
v_max3_u32 v248,v248,v56,v57
mvva 108,132,232
v_max3_u32 v248,v248,v58,v59
s_waitcnt vmcnt(4)
v_max3_u32 v248,v248,v60,v61
mvva 108,128,224
v_max3_u32 v248,v248,v62,v63
v_cmp_eq_u32_e32 vcc,-1,v248
s_waitcnt lgkmcnt(0)
mvva 124,136,224
s_cbranch_vccnz L_spx1o
L_sprx1o:
mvva 124,140,232
mvva 124,152,240
mvva 124,156,248
s_waitcnt vmcnt(3)
v_max_u32_e32 v248,v80,v81
mvva 120,156,216
v_max3_u32 v248,v248,v82,v83
s_waitcnt vmcnt(2)
v_max3_u32 v248,v248,v84,v85
mvva 120,152,208
v_max3_u32 v248,v248,v86,v87
s_waitcnt vmcnt(1)
v_max3_u32 v248,v248,v88,v89
mvva 120,140,200
v_max3_u32 v248,v248,v90,v91
s_waitcnt vmcnt(0)
v_max3_u32 v248,v248,v92,v93
mvva 120,136,192
v_max3_u32 v248,v248,v94,v95
v_cmp_eq_u32_e32 vcc,-1,v248
mvva 116,136,228
s_cbranch_vccnz L_spx2o
L_sprx2o:
mvva 116,140,236
mvva 116,152,244
mvva 116,156,252
mvva 112,156,220
mvva 112,152,212
mvva 112,140,204
mvva 112,136,196
mvvv 108,238,60
mvvv 108,238,92
mvvv 124,236,60
mvvv 124,236,92
mvvv 104,238,56
mvvv 104,238,88
mvvv 120,236,56
mvvv 120,236,88
mvvv 100,238,52
mvvv 100,238,84
cv 108,108,109
cv 109,110,111
cv 110,124,125
cv 111,126,127
cv 104,104,105
v_permlane16_swap_b32_e32 v108,v110
mvvv 116,236,52
v_permlane16_swap_b32_e32 v109,v111
cv 105,106,107
cv 106,120,121
cv 107,122,123
mvvv 116,236,84
buffer_store_dwordx4 v[108:111],v243,s[44:47],s73 offen sc0 sc1
v_permlane16_swap_b32_e32 v104,v106
v_permlane16_swap_b32_e32 v105,v107
mvvv 96,238,48
cv 100,100,101
cv 101,102,103
mvvv 96,238,80
buffer_store_dwordx4 v[104:107],v243,s[44:47],s72 offen sc0 sc1
cv 102,116,117
cv 103,118,119
mvvv 112,236,48
v_permlane16_swap_b32_e32 v100,v102
v_permlane16_swap_b32_e32 v101,v103
mvvv 112,236,80
buffer_store_dwordx4 v[100:103],v243,s[44:47],s71 offen sc0 sc1
cv 96,96,97
cv 97,98,99
s_nop 4
cv 98,112,113
cv 99,114,115
s_nop 0
v_permlane16_swap_b32_e32 v96,v98
v_permlane16_swap_b32_e32 v97,v99
buffer_store_dwordx4 v[96:99],v243,s[44:47],s70 offen sc0 sc1
L_oddx:
s_endpgm
L_k_ec1:
buffer_store_dwordx4 a[252:255],v244,s[8:11],s35 offen offset:1024 sc0 sc1
s_branch L_k_ecr1
L_q_ack:
s_mov_b32 s86,65536
L_q_ack2:
s_sleep 8
buffer_load_dword v246,off,s[8:11],s35 offset:1024 sc1
s_waitcnt vmcnt(0)
v_readfirstlane_b32 s87,v246
s_nop 0
s_cmp_eq_u32 s87,s32
s_cbranch_scc1 L_q_acked
s_sub_u32 s86,s86,1
s_cbranch_scc0 L_q_ack2
s_branch L_q_acked
L_q_spin:
s_mov_b32 s6,65536
L_q_spin1:
s_sleep 8
buffer_load_dword v248,v250,s[8:11],0 offen sc1
s_or_b32 s7,%1,s12
s_cbranch_scc1 L_q_spin2
buffer_load_dwordx4 a[252:255],v244,s[8:11],s35 offen sc1
s_waitcnt vmcnt(0)
buffer_store_dwordx4 a[252:255],v244,s[8:11],s35 offen offset:1024 sc0 sc1
L_q_spin2:
s_waitcnt vmcnt(0)
v_and_b32_e32 v248,v242,v248
v_cmp_ne_u32_e64 s[100:101],v243,v248
s_nop 1
s_cmp_eq_u64 s[100:101],0
s_cbranch_scc1 L_q_ok
s_sub_u32 s6,s6,1
s_cbranch_scc0 L_q_spin1
s_branch L_q_ok
L_bx:
s_mov_b32 s87,65536
L_bx1:
s_sleep 2
buffer_load_dwordx4 v[176:179],v244,s[8:11],s98 offen sc1
buffer_load_dwordx2 v[168:169],v253,s[8:11],s98 offen offset:1024 sc1
buffer_load_dwordx2 v[172:173],v253,s[8:11],s98 offen offset:1536 sc1
s_waitcnt vmcnt(0)
v_max3_u32 v180,v176,v177,v178
v_max3_u32 v180,v180,v179,v168
v_max3_u32 v180,v180,v169,v172
v_max_u32_e32 v180,v180,v173
v_cmp_eq_u32_e64 s[40:41],-1,v180
s_nop 1
s_cmp_eq_u64 s[40:41],0
s_cbranch_scc1 L_bxr
s_sub_u32 s87,s87,1
s_cbranch_scc0 L_bx1
s_branch L_bxr
L_pend2:
s_nop 7
v_add_f32_e32 v226,v152,v153
v_add_f32_e32 v227,v156,v157
v_subrev_u32_e32 v236,0x23800000,v226
v_subrev_u32_e32 v237,0x23800000,v227
v_max_u32_e32 v236,v236,v237
v_cmp_lt_u32_e64 s[40:41],s48,v236
v_rcp_f32_e32 v226,v226
v_rcp_f32_e32 v227,v227
s_add_u32 s86,s13,4096
v_mov_b32_e32 v236,0
v_mov_b32_e32 v237,0
v_mov_b32_e32 v238,0
v_mov_b32_e32 v239,0
v_mov_b64_e32 v[152:153],0
v_mov_b64_e32 v[156:157],0
v_mul_f32_e32 v226,v242,v226
v_mul_f32_e32 v227,v243,v227
v_add_u32_e32 v225,s94,v225
v_pk_mul_f32 v[160:161],v[160:161],v[226:227] op_sel:[0,0] op_sel_hi:[1,0]
v_pk_mul_f32 v[162:163],v[162:163],v[226:227] op_sel:[0,0] op_sel_hi:[1,0]
v_pk_mul_f32 v[164:165],v[164:165],v[226:227] op_sel:[0,0] op_sel_hi:[1,0]
v_pk_mul_f32 v[166:167],v[166:167],v[226:227] op_sel:[0,0] op_sel_hi:[1,0]
cv 160,160,161
cv 161,162,163
cv 162,164,165
cv 163,166,167
buffer_store_dwordx4 v[160:163],v244,s[8:11],s13 offen offset:0 sc0 sc1
v_pk_mul_f32 v[168:169],v[168:169],v[226:227] op_sel:[0,0] op_sel_hi:[1,0]
v_pk_mul_f32 v[170:171],v[170:171],v[226:227] op_sel:[0,0] op_sel_hi:[1,0]
v_pk_mul_f32 v[172:173],v[172:173],v[226:227] op_sel:[0,0] op_sel_hi:[1,0]
v_pk_mul_f32 v[174:175],v[174:175],v[226:227] op_sel:[0,0] op_sel_hi:[1,0]
cv 168,168,169
cv 169,170,171
cv 170,172,173
cv 171,174,175
buffer_store_dwordx4 v[168:171],v244,s[8:11],s13 offen offset:2048 sc0 sc1
v_pk_mul_f32 v[176:177],v[176:177],v[226:227] op_sel:[0,0] op_sel_hi:[1,0]
v_pk_mul_f32 v[178:179],v[178:179],v[226:227] op_sel:[0,0] op_sel_hi:[1,0]
v_pk_mul_f32 v[180:181],v[180:181],v[226:227] op_sel:[0,0] op_sel_hi:[1,0]
v_pk_mul_f32 v[182:183],v[182:183],v[226:227] op_sel:[0,0] op_sel_hi:[1,0]
cv 176,176,177
cv 177,178,179
cv 178,180,181
v_mfma_f32_32x32x16_bf16 v[160:175],v[236:239],v[236:239],0
cv 179,182,183
buffer_store_dwordx4 v[176:179],v244,s[8:11],s86 offen offset:0 sc0 sc1
v_pk_mul_f32 v[184:185],v[184:185],v[226:227] op_sel:[0,0] op_sel_hi:[1,0]
v_pk_mul_f32 v[186:187],v[186:187],v[226:227] op_sel:[0,0] op_sel_hi:[1,0]
v_pk_mul_f32 v[188:189],v[188:189],v[226:227] op_sel:[0,0] op_sel_hi:[1,0]
v_pk_mul_f32 v[190:191],v[190:191],v[226:227] op_sel:[0,0] op_sel_hi:[1,0]
cv 184,184,185
cv 185,186,187
cv 186,188,189
cv 187,190,191
buffer_store_dwordx4 v[184:187],v244,s[8:11],s86 offen offset:2048 sc0 sc1
v_pk_mul_f32 v[192:193],v[192:193],v[226:227] op_sel:[0,1] op_sel_hi:[1,1]
v_pk_mul_f32 v[194:195],v[194:195],v[226:227] op_sel:[0,1] op_sel_hi:[1,1]
v_pk_mul_f32 v[196:197],v[196:197],v[226:227] op_sel:[0,1] op_sel_hi:[1,1]
v_pk_mul_f32 v[198:199],v[198:199],v[226:227] op_sel:[0,1] op_sel_hi:[1,1]
cv 192,192,193
cv 193,194,195
cv 194,196,197
v_mfma_f32_32x32x16_bf16 v[176:191],v[236:239],v[236:239],0
cv 195,198,199
buffer_store_dwordx4 v[192:195],v244,s[8:11],s13 offen offset:1024 sc0 sc1
v_pk_mul_f32 v[200:201],v[200:201],v[226:227] op_sel:[0,1] op_sel_hi:[1,1]
v_pk_mul_f32 v[202:203],v[202:203],v[226:227] op_sel:[0,1] op_sel_hi:[1,1]
v_pk_mul_f32 v[204:205],v[204:205],v[226:227] op_sel:[0,1] op_sel_hi:[1,1]
v_pk_mul_f32 v[206:207],v[206:207],v[226:227] op_sel:[0,1] op_sel_hi:[1,1]
cv 200,200,201
cv 201,202,203
cv 202,204,205
cv 203,206,207
buffer_store_dwordx4 v[200:203],v244,s[8:11],s13 offen offset:3072 sc0 sc1
v_pk_mul_f32 v[208:209],v[208:209],v[226:227] op_sel:[0,1] op_sel_hi:[1,1]
v_pk_mul_f32 v[210:211],v[210:211],v[226:227] op_sel:[0,1] op_sel_hi:[1,1]
v_pk_mul_f32 v[212:213],v[212:213],v[226:227] op_sel:[0,1] op_sel_hi:[1,1]
v_pk_mul_f32 v[214:215],v[214:215],v[226:227] op_sel:[0,1] op_sel_hi:[1,1]
cv 208,208,209
cv 209,210,211
cv 210,212,213
v_mfma_f32_32x32x16_bf16 v[192:207],v[236:239],v[236:239],0
cv 211,214,215
buffer_store_dwordx4 v[208:211],v244,s[8:11],s86 offen offset:1024 sc0 sc1
v_pk_mul_f32 v[216:217],v[216:217],v[226:227] op_sel:[0,1] op_sel_hi:[1,1]
v_pk_mul_f32 v[218:219],v[218:219],v[226:227] op_sel:[0,1] op_sel_hi:[1,1]
v_pk_mul_f32 v[220:221],v[220:221],v[226:227] op_sel:[0,1] op_sel_hi:[1,1]
v_pk_mul_f32 v[222:223],v[222:223],v[226:227] op_sel:[0,1] op_sel_hi:[1,1]
cv 216,216,217
cv 217,218,219
cv 218,220,221
cv 219,222,223
buffer_store_dwordx4 v[216:219],v244,s[8:11],s86 offen offset:3072 sc0 sc1
s_nop 1
v_mfma_f32_32x32x16_bf16 v[208:223],v[236:239],v[236:239],0
s_nop 7
s_nop 1
s_cmp_eq_u32 s84,4
s_cbranch_scc1 L_ret_eb4
s_cmp_lg_u32 s80,s81
s_branch L_ret_eb5
L_redo:
s_waitcnt vmcnt(0) lgkmcnt(0)
s_nop 15
s_nop 3
s_load_dwordx2 s[76:77],%0,0
s_load_dwordx2 s[80:81],%0,16
s_mov_b32 s78,4194304
s_mov_b32 s79,131072
s_mov_b32 s82,8388608
s_mov_b32 s83,131072
s_mov_b32 s86,0
s_waitcnt lgkmcnt(0)
L_rd_t:
s_nop 15
s_nop 3
s_cmp_lt_u32 s86,2
s_cselect_b32 s88,s23,s22
s_cselect_b32 s95,s13,s14
s_and_b32 s89,s86,1
s_lshl_b32 s90,s89,10
s_add_u32 s95,s95,s90
s_lshl_b32 s89,s89,6
s_add_u32 s88,s88,s89
s_lshl_b32 s89,s12,4
s_add_u32 s88,s88,s89
v_bfe_u32 v213,v244,4,4
v_lshrrev_b32_e32 v214,8,v244
v_add_u32_e32 v210,s88,v213
v_lshlrev_b32_e32 v208,11,v210
v_lshl_add_u32 v208,v214,4,v208
v_lshlrev_b32_e32 v209,16,v214
v_lshl_add_u32 v209,v213,1,v209
s_lshl_b32 s89,s18,8
v_add_u32_e32 v209,s89,v209
v_lshlrev_b32_e32 v214,2,v214
v_sub_u32_e32 v210,v210,v214
v_accvgpr_write_b32 a192,0
v_accvgpr_write_b32 a193,0
v_accvgpr_write_b32 a194,0
v_accvgpr_write_b32 a195,0
v_accvgpr_write_b32 a196,0
v_accvgpr_write_b32 a197,0
v_accvgpr_write_b32 a198,0
v_accvgpr_write_b32 a199,0
v_accvgpr_write_b32 a200,0
v_accvgpr_write_b32 a201,0
v_accvgpr_write_b32 a202,0
v_accvgpr_write_b32 a203,0
v_accvgpr_write_b32 a204,0
v_accvgpr_write_b32 a205,0
v_accvgpr_write_b32 a206,0
v_accvgpr_write_b32 a207,0
v_accvgpr_write_b32 a208,0
v_accvgpr_write_b32 a209,0
v_accvgpr_write_b32 a210,0
v_accvgpr_write_b32 a211,0
v_accvgpr_write_b32 a212,0
v_accvgpr_write_b32 a213,0
v_accvgpr_write_b32 a214,0
v_accvgpr_write_b32 a215,0
v_accvgpr_write_b32 a216,0
v_accvgpr_write_b32 a217,0
v_accvgpr_write_b32 a218,0
v_accvgpr_write_b32 a219,0
v_accvgpr_write_b32 a220,0
v_accvgpr_write_b32 a221,0
v_accvgpr_write_b32 a222,0
v_accvgpr_write_b32 a223,0
s_mov_b32 s87,0
L_rd_q:
s_nop 15
s_nop 3
s_lshl_b32 s89,s87,6
s_lshl_b32 s90,s87,18
buffer_load_dwordx4 v[200:203],v208,s[76:79],s89 offen
buffer_load_ushort v160,v209,s[80:83],s90 offen offset:0
buffer_load_ushort v164,v209,s[80:83],s90 offen offset:32
)"
R"(buffer_load_ushort v168,v209,s[80:83],s90 offen offset:64
buffer_load_ushort v172,v209,s[80:83],s90 offen offset:96
buffer_load_ushort v176,v209,s[80:83],s90 offen offset:128
buffer_load_ushort v180,v209,s[80:83],s90 offen offset:160
buffer_load_ushort v184,v209,s[80:83],s90 offen offset:192
buffer_load_ushort v188,v209,s[80:83],s90 offen offset:224
s_add_u32 s90,s90,8192
buffer_load_ushort v192,v209,s[80:83],s90 offen offset:0
buffer_load_ushort v193,v209,s[80:83],s90 offen offset:32
buffer_load_ushort v194,v209,s[80:83],s90 offen offset:64
buffer_load_ushort v195,v209,s[80:83],s90 offen offset:96
buffer_load_ushort v196,v209,s[80:83],s90 offen offset:128
buffer_load_ushort v197,v209,s[80:83],s90 offen offset:160
buffer_load_ushort v198,v209,s[80:83],s90 offen offset:192
buffer_load_ushort v199,v209,s[80:83],s90 offen offset:224
s_add_u32 s90,s90,8192
s_waitcnt vmcnt(0)
v_lshl_or_b32 v160,v192,16,v160
v_lshl_or_b32 v164,v193,16,v164
v_lshl_or_b32 v168,v194,16,v168
v_lshl_or_b32 v172,v195,16,v172
v_lshl_or_b32 v176,v196,16,v176
v_lshl_or_b32 v180,v197,16,v180
v_lshl_or_b32 v184,v198,16,v184
v_lshl_or_b32 v188,v199,16,v188
buffer_load_ushort v161,v209,s[80:83],s90 offen offset:0
buffer_load_ushort v165,v209,s[80:83],s90 offen offset:32
buffer_load_ushort v169,v209,s[80:83],s90 offen offset:64
buffer_load_ushort v173,v209,s[80:83],s90 offen offset:96
buffer_load_ushort v177,v209,s[80:83],s90 offen offset:128
buffer_load_ushort v181,v209,s[80:83],s90 offen offset:160
buffer_load_ushort v185,v209,s[80:83],s90 offen offset:192
buffer_load_ushort v189,v209,s[80:83],s90 offen offset:224
s_add_u32 s90,s90,8192
buffer_load_ushort v192,v209,s[80:83],s90 offen offset:0
buffer_load_ushort v193,v209,s[80:83],s90 offen offset:32
buffer_load_ushort v194,v209,s[80:83],s90 offen offset:64
buffer_load_ushort v195,v209,s[80:83],s90 offen offset:96
buffer_load_ushort v196,v209,s[80:83],s90 offen offset:128
buffer_load_ushort v197,v209,s[80:83],s90 offen offset:160
buffer_load_ushort v198,v209,s[80:83],s90 offen offset:192
buffer_load_ushort v199,v209,s[80:83],s90 offen offset:224
s_add_u32 s90,s90,8192
s_waitcnt vmcnt(0)
v_lshl_or_b32 v161,v192,16,v161
v_lshl_or_b32 v165,v193,16,v165
v_lshl_or_b32 v169,v194,16,v169
v_lshl_or_b32 v173,v195,16,v173
v_lshl_or_b32 v177,v196,16,v177
v_lshl_or_b32 v181,v197,16,v181
v_lshl_or_b32 v185,v198,16,v185
v_lshl_or_b32 v189,v199,16,v189
buffer_load_ushort v162,v209,s[80:83],s90 offen offset:0
buffer_load_ushort v166,v209,s[80:83],s90 offen offset:32
buffer_load_ushort v170,v209,s[80:83],s90 offen offset:64
buffer_load_ushort v174,v209,s[80:83],s90 offen offset:96
buffer_load_ushort v178,v209,s[80:83],s90 offen offset:128
buffer_load_ushort v182,v209,s[80:83],s90 offen offset:160
buffer_load_ushort v186,v209,s[80:83],s90 offen offset:192
buffer_load_ushort v190,v209,s[80:83],s90 offen offset:224
s_add_u32 s90,s90,8192
buffer_load_ushort v192,v209,s[80:83],s90 offen offset:0
buffer_load_ushort v193,v209,s[80:83],s90 offen offset:32
buffer_load_ushort v194,v209,s[80:83],s90 offen offset:64
buffer_load_ushort v195,v209,s[80:83],s90 offen offset:96
buffer_load_ushort v196,v209,s[80:83],s90 offen offset:128
buffer_load_ushort v197,v209,s[80:83],s90 offen offset:160
buffer_load_ushort v198,v209,s[80:83],s90 offen offset:192
buffer_load_ushort v199,v209,s[80:83],s90 offen offset:224
s_add_u32 s90,s90,8192
s_waitcnt vmcnt(0)
v_lshl_or_b32 v162,v192,16,v162
v_lshl_or_b32 v166,v193,16,v166
v_lshl_or_b32 v170,v194,16,v170
v_lshl_or_b32 v174,v195,16,v174
v_lshl_or_b32 v178,v196,16,v178
v_lshl_or_b32 v182,v197,16,v182
v_lshl_or_b32 v186,v198,16,v186
v_lshl_or_b32 v190,v199,16,v190
buffer_load_ushort v163,v209,s[80:83],s90 offen offset:0
buffer_load_ushort v167,v209,s[80:83],s90 offen offset:32
buffer_load_ushort v171,v209,s[80:83],s90 offen offset:64
buffer_load_ushort v175,v209,s[80:83],s90 offen offset:96
buffer_load_ushort v179,v209,s[80:83],s90 offen offset:128
buffer_load_ushort v183,v209,s[80:83],s90 offen offset:160
buffer_load_ushort v187,v209,s[80:83],s90 offen offset:192
buffer_load_ushort v191,v209,s[80:83],s90 offen offset:224
s_add_u32 s90,s90,8192
buffer_load_ushort v192,v209,s[80:83],s90 offen offset:0
buffer_load_ushort v193,v209,s[80:83],s90 offen offset:32
buffer_load_ushort v194,v209,s[80:83],s90 offen offset:64
buffer_load_ushort v195,v209,s[80:83],s90 offen offset:96
buffer_load_ushort v196,v209,s[80:83],s90 offen offset:128
buffer_load_ushort v197,v209,s[80:83],s90 offen offset:160
buffer_load_ushort v198,v209,s[80:83],s90 offen offset:192
buffer_load_ushort v199,v209,s[80:83],s90 offen offset:224
s_add_u32 s90,s90,8192
s_waitcnt vmcnt(0)
v_lshl_or_b32 v163,v192,16,v163
v_lshl_or_b32 v167,v193,16,v167
v_lshl_or_b32 v171,v194,16,v171
v_lshl_or_b32 v175,v195,16,v175
v_lshl_or_b32 v179,v196,16,v179
v_lshl_or_b32 v183,v197,16,v183
v_lshl_or_b32 v187,v198,16,v187
v_lshl_or_b32 v191,v199,16,v191
mavv 192,160,200
mavv 196,164,200
mavv 200,168,200
mavv 204,172,200
mavv 208,176,200
mavv 212,180,200
mavv 216,184,200
mavv 220,188,200
s_add_u32 s87,s87,1
s_cmp_lt_u32 s87,32
s_cbranch_scc1 L_rd_q
s_nop 15
s_nop 3
v_accvgpr_read_b32 v192,a192
v_accvgpr_read_b32 v193,a193
v_accvgpr_read_b32 v194,a194
v_accvgpr_read_b32 v195,a195
v_accvgpr_read_b32 v196,a196
v_accvgpr_read_b32 v197,a197
v_accvgpr_read_b32 v198,a198
v_accvgpr_read_b32 v199,a199
v_mul_f32_e32 v192,0x3e0293ee,v192
v_mul_f32_e32 v193,0x3e0293ee,v193
v_mul_f32_e32 v194,0x3e0293ee,v194
v_mul_f32_e32 v195,0x3e0293ee,v195
v_mul_f32_e32 v196,0x3e0293ee,v196
v_mul_f32_e32 v197,0x3e0293ee,v197
v_mul_f32_e32 v198,0x3e0293ee,v198
v_mul_f32_e32 v199,0x3e0293ee,v199
cv 192,192,193
cv 193,194,195
cv 194,196,197
cv 195,198,199
v_accvgpr_write_b32 a224,v192
v_accvgpr_write_b32 a225,v193
v_accvgpr_write_b32 a226,v194
v_accvgpr_write_b32 a227,v195
v_accvgpr_read_b32 v192,a200
v_accvgpr_read_b32 v193,a201
v_accvgpr_read_b32 v194,a202
v_accvgpr_read_b32 v195,a203
v_accvgpr_read_b32 v196,a204
v_accvgpr_read_b32 v197,a205
v_accvgpr_read_b32 v198,a206
v_accvgpr_read_b32 v199,a207
v_mul_f32_e32 v192,0x3e0293ee,v192
v_mul_f32_e32 v193,0x3e0293ee,v193
v_mul_f32_e32 v194,0x3e0293ee,v194
v_mul_f32_e32 v195,0x3e0293ee,v195
v_mul_f32_e32 v196,0x3e0293ee,v196
v_mul_f32_e32 v197,0x3e0293ee,v197
v_mul_f32_e32 v198,0x3e0293ee,v198
v_mul_f32_e32 v199,0x3e0293ee,v199
cv 192,192,193
cv 193,194,195
cv 194,196,197
cv 195,198,199
v_accvgpr_write_b32 a228,v192
v_accvgpr_write_b32 a229,v193
v_accvgpr_write_b32 a230,v194
v_accvgpr_write_b32 a231,v195
v_accvgpr_read_b32 v192,a208
v_accvgpr_read_b32 v193,a209
v_accvgpr_read_b32 v194,a210
v_accvgpr_read_b32 v195,a211
v_accvgpr_read_b32 v196,a212
v_accvgpr_read_b32 v197,a213
v_accvgpr_read_b32 v198,a214
v_accvgpr_read_b32 v199,a215
v_mul_f32_e32 v192,0x3e0293ee,v192
v_mul_f32_e32 v193,0x3e0293ee,v193
v_mul_f32_e32 v194,0x3e0293ee,v194
v_mul_f32_e32 v195,0x3e0293ee,v195
v_mul_f32_e32 v196,0x3e0293ee,v196
v_mul_f32_e32 v197,0x3e0293ee,v197
v_mul_f32_e32 v198,0x3e0293ee,v198
v_mul_f32_e32 v199,0x3e0293ee,v199
cv 192,192,193
cv 193,194,195
cv 194,196,197
cv 195,198,199
v_accvgpr_write_b32 a232,v192
v_accvgpr_write_b32 a233,v193
v_accvgpr_write_b32 a234,v194
v_accvgpr_write_b32 a235,v195
v_accvgpr_read_b32 v192,a216
v_accvgpr_read_b32 v193,a217
v_accvgpr_read_b32 v194,a218
v_accvgpr_read_b32 v195,a219
v_accvgpr_read_b32 v196,a220
v_accvgpr_read_b32 v197,a221
v_accvgpr_read_b32 v198,a222
v_accvgpr_read_b32 v199,a223
v_mul_f32_e32 v192,0x3e0293ee,v192
v_mul_f32_e32 v193,0x3e0293ee,v193
v_mul_f32_e32 v194,0x3e0293ee,v194
v_mul_f32_e32 v195,0x3e0293ee,v195
v_mul_f32_e32 v196,0x3e0293ee,v196
v_mul_f32_e32 v197,0x3e0293ee,v197
v_mul_f32_e32 v198,0x3e0293ee,v198
v_mul_f32_e32 v199,0x3e0293ee,v199
cv 192,192,193
cv 193,194,195
cv 194,196,197
cv 195,198,199
v_accvgpr_write_b32 a236,v192
v_accvgpr_write_b32 a237,v193
v_accvgpr_write_b32 a238,v194
v_accvgpr_write_b32 a239,v195
v_mov_b32_e32 v160,0
v_mov_b32_e32 v161,0
v_mov_b32_e32 v162,0
v_mov_b32_e32 v163,0
v_mov_b32_e32 v164,0
v_mov_b32_e32 v165,0
v_mov_b32_e32 v166,0
v_mov_b32_e32 v167,0
v_mov_b32_e32 v168,0
v_mov_b32_e32 v169,0
v_mov_b32_e32 v170,0
v_mov_b32_e32 v171,0
v_mov_b32_e32 v172,0
v_mov_b32_e32 v173,0
v_mov_b32_e32 v174,0
v_mov_b32_e32 v175,0
v_mov_b32_e32 v176,0
v_mov_b32_e32 v177,0
v_mov_b32_e32 v178,0
v_mov_b32_e32 v179,0
v_mov_b32_e32 v180,0
v_mov_b32_e32 v181,0
v_mov_b32_e32 v182,0
v_mov_b32_e32 v183,0
v_mov_b32_e32 v184,0
v_mov_b32_e32 v185,0
v_mov_b32_e32 v186,0
v_mov_b32_e32 v187,0
v_mov_b32_e32 v188,0
v_mov_b32_e32 v189,0
v_mov_b32_e32 v190,0
v_mov_b32_e32 v191,0
v_mov_b32_e32 v216,0xf149f2ca
v_mov_b32_e32 v211,v216
v_mov_b32_e32 v212,0
s_lshr_b32 s91,s88,5
s_mov_b32 s87,0
L_rd_a:
s_nop 15
s_nop 3
s_mul_i32 s89,s18,524288
s_lshl_b32 s90,s87,13
s_add_u32 s89,s89,s90
s_add_u32 s90,s89,16777216
la 64,244,90,0
la 68,244,90,1024
la 72,244,90,2048
la 76,244,90,3072
s_add_u32 s90,s89,16781312
la 80,244,90,0
la 84,244,90,1024
la 88,244,90,2048
la 92,244,90,3072
s_add_u32 s90,s89,33554432
la 96,244,90,0
la 100,244,90,1024
la 104,244,90,2048
la 108,244,90,3072
s_add_u32 s90,s89,33558528
la 112,244,90,0
la 116,244,90,1024
la 120,244,90,2048
la 124,244,90,3072
s_waitcnt vmcnt(0)
zvaa 192,64,224
mvaa 192,72,228
mvaa 192,80,232
mvaa 192,88,236
zvaa 196,68,224
mvaa 196,76,228
mvaa 196,84,232
mvaa 196,92,236
v_cmp_gt_i32_e32 vcc,0,v210
s_nop 2
v_cndmask_b32_e32 v192,v192,v216,vcc
v_cmp_gt_i32_e32 vcc,1,v210
s_nop 1
v_cndmask_b32_e32 v193,v193,v216,vcc
v_cmp_gt_i32_e32 vcc,2,v210
s_nop 1
v_cndmask_b32_e32 v194,v194,v216,vcc
v_cmp_gt_i32_e32 vcc,3,v210
s_nop 1
v_cndmask_b32_e32 v195,v195,v216,vcc
v_cmp_gt_i32_e32 vcc,16,v210
s_nop 1
v_cndmask_b32_e32 v196,v196,v216,vcc
v_cmp_gt_i32_e32 vcc,17,v210
s_nop 1
v_cndmask_b32_e32 v197,v197,v216,vcc
v_cmp_gt_i32_e32 vcc,18,v210
s_nop 1
v_cndmask_b32_e32 v198,v198,v216,vcc
v_cmp_gt_i32_e32 vcc,19,v210
s_nop 1
v_cndmask_b32_e32 v199,v199,v216,vcc
v_max3_f32 v213,v192,v193,v194
v_max3_f32 v214,v195,v196,v197
v_max3_f32 v213,v213,v198,v199
v_max_f32_e32 v213,v213,v214
v_mov_b32_e32 v214,v213
s_nop 1
v_permlane16_swap_b32_e32 v213,v214
v_max_f32_e32 v213,v213,v214
v_mov_b32_e32 v214,v213
s_nop 1
v_permlane32_swap_b32_e32 v213,v214
v_max_f32_e32 v213,v213,v214
v_max_f32_e32 v213,v213,v211
v_sub_f32_e32 v215,v211,v213
ex 215
v_mov_b32_e32 v211,v213
v_sub_f32_e32 v192,v192,v211
v_sub_f32_e32 v193,v193,v211
v_sub_f32_e32 v194,v194,v211
v_sub_f32_e32 v195,v195,v211
v_sub_f32_e32 v196,v196,v211
v_sub_f32_e32 v197,v197,v211
v_sub_f32_e32 v198,v198,v211
v_sub_f32_e32 v199,v199,v211
ex 192
ex 193
ex 194
ex 195
ex 196
ex 197
ex 198
ex 199
v_mul_f32_e32 v212,v215,v212
v_add_f32_e32 v212,v212,v192
v_add_f32_e32 v212,v212,v193
v_add_f32_e32 v212,v212,v194
v_add_f32_e32 v212,v212,v195
v_add_f32_e32 v212,v212,v196
v_add_f32_e32 v212,v212,v197
v_add_f32_e32 v212,v212,v198
v_add_f32_e32 v212,v212,v199
cv 192,192,193
cv 193,194,195
cv 194,196,197
cv 195,198,199
v_mul_f32_e32 v160,v215,v160
v_mul_f32_e32 v161,v215,v161
v_mul_f32_e32 v162,v215,v162
v_mul_f32_e32 v163,v215,v163
v_mul_f32_e32 v164,v215,v164
v_mul_f32_e32 v165,v215,v165
v_mul_f32_e32 v166,v215,v166
v_mul_f32_e32 v167,v215,v167
v_mul_f32_e32 v168,v215,v168
v_mul_f32_e32 v169,v215,v169
v_mul_f32_e32 v170,v215,v170
v_mul_f32_e32 v171,v215,v171
v_mul_f32_e32 v172,v215,v172
v_mul_f32_e32 v173,v215,v173
v_mul_f32_e32 v174,v215,v174
v_mul_f32_e32 v175,v215,v175
v_mul_f32_e32 v176,v215,v176
v_mul_f32_e32 v177,v215,v177
v_mul_f32_e32 v178,v215,v178
v_mul_f32_e32 v179,v215,v179
v_mul_f32_e32 v180,v215,v180
v_mul_f32_e32 v181,v215,v181
v_mul_f32_e32 v182,v215,v182
v_mul_f32_e32 v183,v215,v183
v_mul_f32_e32 v184,v215,v184
v_mul_f32_e32 v185,v215,v185
v_mul_f32_e32 v186,v215,v186
v_mul_f32_e32 v187,v215,v187
v_mul_f32_e32 v188,v215,v188
v_mul_f32_e32 v189,v215,v189
v_mul_f32_e32 v190,v215,v190
v_mul_f32_e32 v191,v215,v191
mvav 160,96,192
mvav 164,100,192
mvav 168,104,192
mvav 172,108,192
mvav 176,112,192
mvav 180,116,192
mvav 184,120,192
mvav 188,124,192
v_subrev_u32_e32 v210,32,v210
s_add_u32 s87,s87,1
s_cmp_le_u32 s87,s91
s_cbranch_scc1 L_rd_a
s_nop 15
s_nop 3
s_lshl_b32 s89,s88,2
s_add_u32 s89,s89,83886080
v_bfe_u32 v213,v244,2,6
buffer_load_dword v215,v213,s[8:11],s89 offen sc1
v_mov_b32_e32 v214,v212
s_nop 1
v_permlane16_swap_b32_e32 v212,v214
v_add_f32_e32 v212,v212,v214
v_mov_b32_e32 v214,v212
s_nop 1
v_permlane32_swap_b32_e32 v212,v214
v_add_f32_e32 v212,v212,v214
v_rcp_f32_e32 v212,v212
s_waitcnt vmcnt(0)
v_mul_f32_e32 v215,v215,v212
v_mul_f32_e32 v160,v215,v160
v_mul_f32_e32 v161,v215,v161
v_mul_f32_e32 v162,v215,v162
v_mul_f32_e32 v163,v215,v163
v_mul_f32_e32 v164,v215,v164
v_mul_f32_e32 v165,v215,v165
v_mul_f32_e32 v166,v215,v166
v_mul_f32_e32 v167,v215,v167
v_mul_f32_e32 v168,v215,v168
v_mul_f32_e32 v169,v215,v169
v_mul_f32_e32 v170,v215,v170
v_mul_f32_e32 v171,v215,v171
v_mul_f32_e32 v172,v215,v172
v_mul_f32_e32 v173,v215,v173
v_mul_f32_e32 v174,v215,v174
v_mul_f32_e32 v175,v215,v175
v_mul_f32_e32 v176,v215,v176
v_mul_f32_e32 v177,v215,v177
v_mul_f32_e32 v178,v215,v178
v_mul_f32_e32 v179,v215,v179
v_mul_f32_e32 v180,v215,v180
v_mul_f32_e32 v181,v215,v181
v_mul_f32_e32 v182,v215,v182
v_mul_f32_e32 v183,v215,v183
v_mul_f32_e32 v184,v215,v184
v_mul_f32_e32 v185,v215,v185
v_mul_f32_e32 v186,v215,v186
v_mul_f32_e32 v187,v215,v187
v_mul_f32_e32 v188,v215,v188
v_mul_f32_e32 v189,v215,v189
v_mul_f32_e32 v190,v215,v190
v_mul_f32_e32 v191,v215,v191
s_add_u32 s89,s95,4096
cv 160,160,161
cv 161,162,163
cv 162,164,165
cv 163,166,167
buffer_store_dwordx4 v[160:163],v244,s[8:11],s95 offen offset:0 sc0 sc1
cv 168,168,169
cv 169,170,171
cv 170,172,173
cv 171,174,175
buffer_store_dwordx4 v[168:171],v244,s[8:11],s95 offen offset:2048 sc0 sc1
cv 176,176,177
cv 177,178,179
cv 178,180,181
cv 179,182,183
buffer_store_dwordx4 v[176:179],v244,s[8:11],s89 offen offset:0 sc0 sc1
cv 184,184,185
cv 185,186,187
cv 186,188,189
cv 187,190,191
buffer_store_dwordx4 v[184:187],v244,s[8:11],s89 offen offset:2048 sc0 sc1
s_waitcnt vmcnt(0)
v_mov_b32_e32 v192,v160
v_accvgpr_read_b32 v160,a240
v_accvgpr_write_b32 a240,v192
v_mov_b32_e32 v193,v161
v_accvgpr_read_b32 v161,a241
v_accvgpr_write_b32 a241,v193
v_mov_b32_e32 v194,v162
v_accvgpr_read_b32 v162,a242
v_accvgpr_write_b32 a242,v194
v_mov_b32_e32 v195,v163
v_accvgpr_read_b32 v163,a243
v_accvgpr_write_b32 a243,v195
v_mov_b32_e32 v200,v168
v_accvgpr_read_b32 v168,a244
v_accvgpr_write_b32 a244,v200
v_mov_b32_e32 v201,v169
v_accvgpr_read_b32 v169,a245
v_accvgpr_write_b32 a245,v201
v_mov_b32_e32 v202,v170
v_accvgpr_read_b32 v170,a246
v_accvgpr_write_b32 a246,v202
v_mov_b32_e32 v203,v171
v_accvgpr_read_b32 v171,a247
v_accvgpr_write_b32 a247,v203
v_mov_b32_e32 v208,v176
v_accvgpr_read_b32 v176,a248
v_accvgpr_write_b32 a248,v208
v_mov_b32_e32 v209,v177
v_accvgpr_read_b32 v177,a249
v_accvgpr_write_b32 a249,v209
v_mov_b32_e32 v210,v178
v_accvgpr_read_b32 v178,a250
v_accvgpr_write_b32 a250,v210
v_mov_b32_e32 v211,v179
v_accvgpr_read_b32 v179,a251
v_accvgpr_write_b32 a251,v211
v_mov_b32_e32 v216,v184
v_accvgpr_read_b32 v184,a252
v_accvgpr_write_b32 a252,v216
v_mov_b32_e32 v217,v185
v_accvgpr_read_b32 v185,a253
v_accvgpr_write_b32 a253,v217
v_mov_b32_e32 v218,v186
v_accvgpr_read_b32 v186,a254
v_accvgpr_write_b32 a254,v218
v_mov_b32_e32 v219,v187
v_accvgpr_read_b32 v187,a255
v_accvgpr_write_b32 a255,v219
s_add_u32 s86,s86,1
s_cmp_lt_u32 s86,4
s_cbranch_scc1 L_rd_t
s_nop 15
s_nop 3
s_nop 1
s_cmp_eq_u32 s84,4
s_cbranch_scc1 L_redo_r4
s_cmp_lg_u32 s80,s81
s_branch L_redo_r5
L_k_ev:
L_v_ev:
buffer_load_dwordx4 a[252:255],v244,s[8:11],s35 offen sc1
s_branch L_k_evr
L_spbe:
s_mov_b32 s91,65536
L_splbe:
s_sleep 2
buffer_load_dword v253,v224,s[8:11],s53 offen sc1
s_waitcnt vmcnt(0)
v_cmp_ne_u32_e32 vcc,4,v253
s_nop 1
s_cbranch_vccz L_sprbe
s_sub_u32 s91,s91,1
s_cbranch_scc0 L_splbe
s_branch L_sprbe
L_spa3e:
s_mov_b32 s91,65536
L_spla3e:
s_sleep 2
buffer_load_dword v253,v242,s[8:11],s53 offen sc1
s_waitcnt vmcnt(0)
v_cmp_ne_u32_e32 vcc,4,v253
s_nop 1
s_cbranch_vccz L_spra3e
s_sub_u32 s91,s91,1
s_cbranch_scc0 L_spla3e
s_branch L_spra3e
L_spa4e:
s_mov_b32 s91,65536
L_spla4e:
s_sleep 2
buffer_load_dword v253,v242,s[8:11],s53 offen sc1
s_waitcnt vmcnt(0)
v_cmp_ne_u32_e32 vcc,4,v253
s_nop 1
s_cbranch_vccz L_spra4e
s_sub_u32 s91,s91,1
s_cbranch_scc0 L_spla4e
s_branch L_spra4e
L_spa5e:
s_mov_b32 s91,65536
L_spla5e:
s_sleep 2
buffer_load_dword v253,v242,s[8:11],s53 offen sc1
s_waitcnt vmcnt(0)
v_cmp_ne_u32_e32 vcc,4,v253
s_nop 1
s_cbranch_vccz L_spra5e
s_sub_u32 s91,s91,1
s_cbranch_scc0 L_spla5e
s_branch L_spra5e
L_spa6e:
s_mov_b32 s91,65536
L_spla6e:
s_sleep 2
buffer_load_dword v253,v242,s[8:11],s53 offen sc1
s_waitcnt vmcnt(0)
v_cmp_ne_u32_e32 vcc,4,v253
s_nop 1
s_cbranch_vccz L_spra6e
s_sub_u32 s91,s91,1
s_cbranch_scc0 L_spla6e
s_branch L_spra6e
L_spa7e:
s_mov_b32 s91,65536
L_spla7e:
s_sleep 2
buffer_load_dword v253,v242,s[8:11],s53 offen sc1
s_waitcnt vmcnt(0)
v_cmp_ne_u32_e32 vcc,4,v253
s_nop 1
s_cbranch_vccz L_spra7e
s_sub_u32 s91,s91,1
s_cbranch_scc0 L_spla7e
s_branch L_spra7e
L_spa8e:
s_mov_b32 s91,65536
L_spla8e:
s_sleep 2
buffer_load_dword v253,v242,s[8:11],s53 offen sc1
s_waitcnt vmcnt(0)
v_cmp_ne_u32_e32 vcc,4,v253
s_nop 1
s_cbranch_vccz L_spra8e
s_sub_u32 s91,s91,1
s_cbranch_scc0 L_spla8e
s_branch L_spra8e
L_spa9e:
s_mov_b32 s91,65536
L_spla9e:
s_sleep 2
buffer_load_dword v253,v242,s[8:11],s53 offen sc1
s_waitcnt vmcnt(0)
v_cmp_ne_u32_e32 vcc,4,v253
s_nop 1
s_cbranch_vccz L_spra9e
s_sub_u32 s91,s91,1
s_cbranch_scc0 L_spla9e
s_branch L_spra9e
L_spa10e:
s_mov_b32 s91,65536
L_spla10e:
s_sleep 2
buffer_load_dword v253,v242,s[8:11],s53 offen sc1
s_waitcnt vmcnt(0)
v_cmp_ne_u32_e32 vcc,4,v253
s_nop 1
s_cbranch_vccz L_spra10e
s_sub_u32 s91,s91,1
s_cbranch_scc0 L_spla10e
s_branch L_spra10e
L_sppe:
s_mov_b32 s91,65536
L_splpe:
s_sleep 2
buffer_load_dword v253,v242,s[8:11],s53 offen sc1
s_waitcnt vmcnt(0)
v_cmp_ne_u32_e32 vcc,4,v253
s_nop 1
s_cbranch_vccz L_sprpe
s_sub_u32 s91,s91,1
s_cbranch_scc0 L_splpe
s_branch L_sprpe
L_spx0e:
s_mov_b32 s91,65536
L_splx0e:
lv 16,244,82,0
lv 20,244,82,1024
lv 24,244,82,2048
lv 28,244,82,3072
s_waitcnt vmcnt(0)
v_max_u32_e32 v248,v16,v17
v_max3_u32 v248,v248,v18,v19
v_max3_u32 v248,v248,v20,v21
v_max3_u32 v248,v248,v22,v23
v_max3_u32 v248,v248,v24,v25
v_max3_u32 v248,v248,v26,v27
v_max3_u32 v248,v248,v28,v29
v_max3_u32 v248,v248,v30,v31
v_cmp_eq_u32_e32 vcc,-1,v248
s_nop 1
s_cbranch_vccz L_sprx0e
s_sub_u32 s91,s91,1
s_cbranch_scc0 L_splx0e
s_branch L_sprx0e
L_spx1e:
s_mov_b32 s91,65536
L_splx1e:
lv 48,244,83,0
lv 52,244,83,1024
lv 56,244,83,2048
lv 60,244,83,3072
s_waitcnt vmcnt(0)
v_max_u32_e32 v248,v48,v49
v_max3_u32 v248,v248,v50,v51
v_max3_u32 v248,v248,v52,v53
v_max3_u32 v248,v248,v54,v55
v_max3_u32 v248,v248,v56,v57
v_max3_u32 v248,v248,v58,v59
v_max3_u32 v248,v248,v60,v61
v_max3_u32 v248,v248,v62,v63
v_cmp_eq_u32_e32 vcc,-1,v248
s_nop 1
s_cbranch_vccz L_sprx1e
s_sub_u32 s91,s91,1
s_cbranch_scc0 L_splx1e
s_branch L_sprx1e
L_spx2e:
s_mov_b32 s91,65536
L_splx2e:
lv 80,244,84,0
lv 84,244,84,1024
lv 88,244,84,2048
lv 92,244,84,3072
s_waitcnt vmcnt(0)
v_max_u32_e32 v248,v80,v81
v_max3_u32 v248,v248,v82,v83
v_max3_u32 v248,v248,v84,v85
v_max3_u32 v248,v248,v86,v87
v_max3_u32 v248,v248,v88,v89
v_max3_u32 v248,v248,v90,v91
v_max3_u32 v248,v248,v92,v93
v_max3_u32 v248,v248,v94,v95
v_cmp_eq_u32_e32 vcc,-1,v248
s_nop 1
s_cbranch_vccz L_sprx2e
s_sub_u32 s91,s91,1
s_cbranch_scc0 L_splx2e
s_branch L_sprx2e
L_spbo:
s_mov_b32 s91,65536
L_splbo:
s_sleep 2
buffer_load_dword v253,v224,s[8:11],s53 offen sc1
s_waitcnt vmcnt(0)
v_cmp_ne_u32_e32 vcc,4,v253
s_nop 1
s_cbranch_vccz L_sprbo
s_sub_u32 s91,s91,1
s_cbranch_scc0 L_splbo
s_branch L_sprbo
L_spa3o:
s_mov_b32 s91,65536
L_spla3o:
s_sleep 2
buffer_load_dword v253,v242,s[8:11],s53 offen sc1
s_waitcnt vmcnt(0)
v_cmp_ne_u32_e32 vcc,4,v253
s_nop 1
s_cbranch_vccz L_spra3o
s_sub_u32 s91,s91,1
s_cbranch_scc0 L_spla3o
s_branch L_spra3o
L_spa4o:
s_mov_b32 s91,65536
L_spla4o:
s_sleep 2
buffer_load_dword v253,v242,s[8:11],s53 offen sc1
s_waitcnt vmcnt(0)
v_cmp_ne_u32_e32 vcc,4,v253
s_nop 1
s_cbranch_vccz L_spra4o
s_sub_u32 s91,s91,1
s_cbranch_scc0 L_spla4o
s_branch L_spra4o
L_spa5o:
s_mov_b32 s91,65536
L_spla5o:
s_sleep 2
buffer_load_dword v253,v242,s[8:11],s53 offen sc1
s_waitcnt vmcnt(0)
v_cmp_ne_u32_e32 vcc,4,v253
s_nop 1
s_cbranch_vccz L_spra5o
s_sub_u32 s91,s91,1
s_cbranch_scc0 L_spla5o
s_branch L_spra5o
L_spa6o:
s_mov_b32 s91,65536
L_spla6o:
s_sleep 2
buffer_load_dword v253,v242,s[8:11],s53 offen sc1
s_waitcnt vmcnt(0)
v_cmp_ne_u32_e32 vcc,4,v253
s_nop 1
s_cbranch_vccz L_spra6o
s_sub_u32 s91,s91,1
s_cbranch_scc0 L_spla6o
s_branch L_spra6o
L_spa7o:
s_mov_b32 s91,65536
L_spla7o:
s_sleep 2
buffer_load_dword v253,v242,s[8:11],s53 offen sc1
s_waitcnt vmcnt(0)
v_cmp_ne_u32_e32 vcc,4,v253
s_nop 1
s_cbranch_vccz L_spra7o
s_sub_u32 s91,s91,1
s_cbranch_scc0 L_spla7o
s_branch L_spra7o
L_spa8o:
s_mov_b32 s91,65536
L_spla8o:
s_sleep 2
buffer_load_dword v253,v242,s[8:11],s53 offen sc1
s_waitcnt vmcnt(0)
v_cmp_ne_u32_e32 vcc,4,v253
s_nop 1
s_cbranch_vccz L_spra8o
s_sub_u32 s91,s91,1
s_cbranch_scc0 L_spla8o
s_branch L_spra8o
L_sppo:
s_mov_b32 s91,65536
L_splpo:
s_sleep 2
buffer_load_dword v253,v242,s[8:11],s53 offen sc1
s_waitcnt vmcnt(0)
v_cmp_ne_u32_e32 vcc,4,v253
s_nop 1
s_cbranch_vccz L_sprpo
s_sub_u32 s91,s91,1
s_cbranch_scc0 L_splpo
s_branch L_sprpo
L_spx0o:
s_mov_b32 s91,65536
L_splx0o:
lv 16,244,82,0
lv 20,244,82,1024
lv 24,244,82,2048
lv 28,244,82,3072
s_waitcnt vmcnt(0)
v_max_u32_e32 v248,v16,v17
v_max3_u32 v248,v248,v18,v19
v_max3_u32 v248,v248,v20,v21
v_max3_u32 v248,v248,v22,v23
v_max3_u32 v248,v248,v24,v25
v_max3_u32 v248,v248,v26,v27
v_max3_u32 v248,v248,v28,v29
v_max3_u32 v248,v248,v30,v31
v_cmp_eq_u32_e32 vcc,-1,v248
s_nop 1
s_cbranch_vccz L_sprx0o
s_sub_u32 s91,s91,1
s_cbranch_scc0 L_splx0o
s_branch L_sprx0o
L_spx1o:
s_mov_b32 s91,65536
L_splx1o:
lv 48,244,83,0
lv 52,244,83,1024
lv 56,244,83,2048
lv 60,244,83,3072
s_waitcnt vmcnt(0)
v_max_u32_e32 v248,v48,v49
v_max3_u32 v248,v248,v50,v51
v_max3_u32 v248,v248,v52,v53
v_max3_u32 v248,v248,v54,v55
v_max3_u32 v248,v248,v56,v57
v_max3_u32 v248,v248,v58,v59
v_max3_u32 v248,v248,v60,v61
v_max3_u32 v248,v248,v62,v63
v_cmp_eq_u32_e32 vcc,-1,v248
s_nop 1
s_cbranch_vccz L_sprx1o
s_sub_u32 s91,s91,1
s_cbranch_scc0 L_splx1o
s_branch L_sprx1o
L_spx2o:
s_mov_b32 s91,65536
L_splx2o:
lv 80,244,84,0
lv 84,244,84,1024
lv 88,244,84,2048
lv 92,244,84,3072
s_waitcnt vmcnt(0)
v_max_u32_e32 v248,v80,v81
v_max3_u32 v248,v248,v82,v83
v_max3_u32 v248,v248,v84,v85
v_max3_u32 v248,v248,v86,v87
v_max3_u32 v248,v248,v88,v89
v_max3_u32 v248,v248,v90,v91
v_max3_u32 v248,v248,v92,v93
v_max3_u32 v248,v248,v94,v95
v_cmp_eq_u32_e32 vcc,-1,v248
s_nop 1
s_cbranch_vccz L_sprx2o
s_sub_u32 s91,s91,1
s_cbranch_scc0 L_splx2o
s_branch L_sprx2o
L_att_end:
s_endpgm
.p2align 12
.purgem cv
.purgem cvaa
.purgem cvvv
.purgem dl
.purgem ds
.purgem ex
.purgem la
.purgem lv
.purgem maaa
.purgem maav
.purgem mavv
.purgem mvaa
.purgem mvav
.purgem mvva
.purgem mvvv
.purgem ra
.purgem rv
.purgem ta
.purgem tv
.purgem zaaa
.purgem zaav
.purgem zvaa
.purgem zvav
.purgem zvva
.purgem zvvv
)"
        :: "s"(__builtin_amdgcn_kernarg_segment_ptr()), "s"(blockIdx.x), "v"(tid)
        : "v0", "v1", "v2", "v3", "v4", "v5", "v6", "v7", "v8", "v9", "v10", "v11", "v12", "v13", "v14", "v15", "v16", "v17", "v18", "v19", "v20", "v21", "v22", "v23", "v24", "v25", "v26", "v27", "v28", "v29", "v30", "v31", "v32", "v33", "v34", "v35", "v36", "v37", "v38", "v39", "v40", "v41", "v42", "v43", "v44", "v45", "v46", "v47", "v48", "v49", "v50", "v51", "v52", "v53", "v54", "v55", "v56", "v57", "v58", "v59", "v60", "v61", "v62", "v63", "v64", "v65", "v66", "v67", "v68", "v69", "v70", "v71", "v72", "v73", "v74", "v75", "v76", "v77", "v78", "v79", "v80", "v81", "v82", "v83", "v84", "v85", "v86", "v87", "v88", "v89", "v90", "v91", "v92", "v93", "v94", "v95", "v96", "v97", "v98", "v99", "v100", "v101", "v102", "v103", "v104", "v105", "v106", "v107", "v108", "v109", "v110", "v111", "v112", "v113", "v114", "v115", "v116", "v117", "v118", "v119", "v120", "v121", "v122", "v123", "v124", "v125", "v126", "v127", "v128", "v129", "v130", "v131", "v132", "v133", "v134", "v135", "v136", "v137", "v138", "v139", "v140", "v141", "v142", "v143", "v144", "v145", "v146", "v147", "v148", "v149", "v150", "v151", "v152", "v153", "v154", "v155", "v156", "v157", "v158", "v159", "v160", "v161", "v162", "v163", "v164", "v165", "v166", "v167", "v168", "v169", "v170", "v171", "v172", "v173", "v174", "v175", "v176", "v177", "v178", "v179", "v180", "v181", "v182", "v183", "v184", "v185", "v186", "v187", "v188", "v189", "v190", "v191", "v192", "v193", "v194", "v195", "v196", "v197", "v198", "v199", "v200", "v201", "v202", "v203", "v204", "v205", "v206", "v207", "v208", "v209", "v210", "v211", "v212", "v213", "v214", "v215", "v216", "v217", "v218", "v219", "v220", "v221", "v222", "v223", "v224", "v225", "v226", "v227", "v228", "v229", "v230", "v231", "v232", "v233", "v234", "v235", "v236", "v237", "v238", "v239", "v240", "v241", "v242", "v243", "v244", "v245", "v246", "v247", "v248", "v249", "v250", "v251", "v252", "v253", "v254", "a0", "a1", "a2", "a3", "a4", "a5", "a6", "a7", "a8", "a9", "a10", "a11", "a12", "a13", "a14", "a15", "a16", "a17", "a18", "a19", "a20", "a21", "a22", "a23", "a24", "a25", "a26", "a27", "a28", "a29", "a30", "a31", "a32", "a33", "a34", "a35", "a36", "a37", "a38", "a39", "a40", "a41", "a42", "a43", "a44", "a45", "a46", "a47", "a48", "a49", "a50", "a51", "a52", "a53", "a54", "a55", "a56", "a57", "a58", "a59", "a60", "a61", "a62", "a63", "a64", "a65", "a66", "a67", "a68", "a69", "a70", "a71", "a72", "a73", "a74", "a75", "a76", "a77", "a78", "a79", "a80", "a81", "a82", "a83", "a84", "a85", "a86", "a87", "a88", "a89", "a90", "a91", "a92", "a93", "a94", "a95", "a96", "a97", "a98", "a99", "a100", "a101", "a102", "a103", "a104", "a105", "a106", "a107", "a108", "a109", "a110", "a111", "a112", "a113", "a114", "a115", "a116", "a117", "a118", "a119", "a120", "a121", "a122", "a123", "a124", "a125", "a126", "a127", "a128", "a129", "a130", "a131", "a132", "a133", "a134", "a135", "a136", "a137", "a138", "a139", "a140", "a141", "a142", "a143", "a144", "a145", "a146", "a147", "a148", "a149", "a150", "a151", "a152", "a153", "a154", "a155", "a156", "a157", "a158", "a159", "a160", "a161", "a162", "a163", "a164", "a165", "a166", "a167", "a168", "a169", "a170", "a171", "a172", "a173", "a174", "a175", "a176", "a177", "a178", "a179", "a180", "a181", "a182", "a183", "a184", "a185", "a186", "a187", "a188", "a189", "a190", "a191", "a192", "a193", "a194", "a195", "a196", "a197", "a198", "a199", "a200", "a201", "a202", "a203", "a204", "a205", "a206", "a207", "a208", "a209", "a210", "a211", "a212", "a213", "a214", "a215", "a216", "a217", "a218", "a219", "a220", "a221", "a222", "a223", "a224", "a225", "a226", "a227", "a228", "a229", "a230", "a231", "a232", "a233", "a234", "a235", "a236", "a237", "a238", "a239", "a240", "a241", "a242", "a243", "a244", "a245", "a246", "a247", "a248", "a249", "a250", "a251", "a252", "a253", "a254", "a255", "s4", "s5", "s6", "s7", "s8", "s9", "s10", "s11", "s12", "s13", "s14", "s15", "s16", "s17", "s18", "s19", "s20", "s21", "s22", "s23", "s24", "s25", "s26", "s27", "s28", "s29", "s30", "s31", "s32", "s33", "s34", "s35", "s36", "s37", "s38", "s39", "s40", "s41", "s42", "s43", "s44", "s45", "s46", "s47", "s48", "s49", "s50", "s51", "s52", "s53", "s54", "s55", "s56", "s57", "s58", "s59", "s60", "s61", "s62", "s63", "s64", "s65", "s66", "s67", "s68", "s69", "s70", "s71", "s72", "s73", "s74", "s75", "s76", "s77", "s78", "s79", "s80", "s81", "s82", "s83", "s84", "s85", "s86", "s87", "s88", "s89", "s90", "s91", "s92", "s93", "s94", "s95", "s96", "s97", "s98", "s99", "s100", "s101", "vcc", "scc", "m0", "memory");
}

extern "C" void kernel_launch(void* const* d_in, const int* in_sizes, int n_in, void* d_out, int out_size, void* d_ws, size_t ws_size, hipStream_t stream) {
    (void)in_sizes; (void)n_in; (void)out_size; (void)ws_size;
    char* ws = (char*)d_ws + 0ull;
    Args a;
    a.p[0] = d_in[0]; a.p[1] = d_in[1]; a.p[2] = d_in[2]; a.p[3] = d_in[3]; a.p[4] = d_in[4]; a.p[5] = d_in[5]; a.p[6] = d_in[7]; a.p[7] = d_in[8]; a.p[8] = d_in[9]; a.p[9] = d_out; a.p[10] = ws;
    mega<<<256, 256, 0, stream>>>(a);
}
```

```cpp
#include <hip/hip_runtime.h>
#include <hip/hip_bf16.h>
#include <stdint.h>
#include <type_traits>

#define LAS __attribute__((address_space(3)))
struct Args { const void* p[11]; };
__global__ __launch_bounds__(256, 1)
void mega(Args args) {
    __shared__ __attribute__((aligned(1024))) char smem[163840];
    const unsigned tid = threadIdx.x + (unsigned)(uintptr_t)(LAS char*)smem;
    asm volatile(
R"(.macro cv a,b,c
v_cvt_pk_bf16_f32 v\a,v\b,v\c
.endm
.macro cvaa a,b,c,d
v_mfma_f32_16x16x32_bf16 v[\a:\a+3],a[\b:\b+3],a[\c:\c+3],v[\d:\d+3]
.endm
.macro cvvv a,b,c,d
v_mfma_f32_16x16x32_bf16 v[\a:\a+3],v[\b:\b+3],v[\c:\c+3],v[\d:\d+3]
.endm
.macro dl a,b,c,d
buffer_load_dwordx4 v\a,s[\b:\b+3],s\c offen offset:\d lds
.endm
.macro ds a,b,c,d
buffer_load_dwordx4 v\a,s[\b:\b+3],s\c offen offset:\d sc1 lds
.endm
.macro ex a
v_exp_f32_e32 v\a,v\a
.endm
.macro la a,b,c,d
buffer_load_dwordx4 a[\a:\a+3],v\b,s[8:11],s\c offen offset:\d sc1
.endm
.macro lv a,b,c,d
buffer_load_dwordx4 v[\a:\a+3],v\b,s[8:11],s\c offen offset:\d sc1
.endm
.macro maaa a,b,c
v_mfma_f32_16x16x32_bf16 a[\a:\a+3],a[\b:\b+3],a[\c:\c+3],a[\a:\a+3]
.endm
.macro maav a,b,c
v_mfma_f32_16x16x32_bf16 a[\a:\a+3],a[\b:\b+3],v[\c:\c+3],a[\a:\a+3]
.endm
.macro mavv a,b,c
v_mfma_f32_16x16x32_bf16 a[\a:\a+3],v[\b:\b+3],v[\c:\c+3],a[\a:\a+3]
.endm
.macro mvaa a,b,c
v_mfma_f32_16x16x32_bf16 v[\a:\a+3],a[\b:\b+3],a[\c:\c+3],v[\a:\a+3]
.endm
.macro mvav a,b,c
v_mfma_f32_16x16x32_bf16 v[\a:\a+3],a[\b:\b+3],v[\c:\c+3],v[\a:\a+3]
.endm
.macro mvva a,b,c
v_mfma_f32_16x16x32_bf16 v[\a:\a+3],v[\b:\b+3],a[\c:\c+3],v[\a:\a+3]
.endm
.macro mvvv a,b,c
v_mfma_f32_16x16x32_bf16 v[\a:\a+3],v[\b:\b+3],v[\c:\c+3],v[\a:\a+3]
.endm
.macro ra a,b,c
ds_read_b128 a[\a:\a+3],v\b offset:\c
.endm
.macro rv a,b,c
ds_read_b128 v[\a:\a+3],v\b offset:\c
.endm
.macro ta a,b,c
ds_read_b64_tr_b16 a[\a:\a+1],v\b offset:\c
.endm
.macro tv a,b,c
ds_read_b64_tr_b16 v[\a:\a+1],v\b offset:\c
.endm
.macro zaaa a,b,c
v_mfma_f32_16x16x32_bf16 a[\a:\a+3],a[\b:\b+3],a[\c:\c+3],0
.endm
.macro zaav a,b,c
v_mfma_f32_16x16x32_bf16 a[\a:\a+3],a[\b:\b+3],v[\c:\c+3],0
.endm
.macro zvaa a,b,c
v_mfma_f32_16x16x32_bf16 v[\a:\a+3],a[\b:\b+3],a[\c:\c+3],0
.endm
.macro zvav a,b,c
v_mfma_f32_16x16x32_bf16 v[\a:\a+3],a[\b:\b+3],v[\c:\c+3],0
.endm
.macro zvva a,b,c
v_mfma_f32_16x16x32_bf16 v[\a:\a+3],v[\b:\b+3],a[\c:\c+3],0
.endm
.macro zvvv a,b,c
v_mfma_f32_16x16x32_bf16 v[\a:\a+3],v[\b:\b+3],v[\c:\c+3],0
.endm
L_begin:
s_load_dwordx2 s[28:29],%0,72
s_load_dwordx16 s[68:83],%0,0
s_load_dwordx2 s[84:85],%0,64
s_load_dwordx2 s[8:9],%0,80
v_lshrrev_b32_e32 v246,6,%2
v_and_b32_e32 v247,63,%2
v_lshlrev_b32_e32 v244,4,v247
v_readfirstlane_b32 s12,v246
s_and_b32 s20,%1,7
s_lshr_b32 s21,%1,3
s_getpc_b64 s[96:97]
L_att_pc:
s_lshl_b32 s86,s21,2
s_add_u32 s86,s86,s12
s_mul_i32 s86,s86,512
v_lshl_add_u32 v246,v247,7,s86
v_min_u32_e32 v246,L_att_end-L_att_pc-4,v246
s_mov_b64 exec,15
global_load_dword a188,v246,s[96:97]
s_mov_b64 exec,-1
s_and_b32 s19,s21,7
s_lshr_b32 s86,s21,3
s_lshl_b32 s18,s20,2
s_add_u32 s18,s18,s86
v_lshrrev_b32_e32 v248,3,v247
v_and_b32_e32 v250,112,v244
v_and_b32_e32 v249,48,v247
v_xor_b32_e32 v249,v249,v250
v_and_b32_e32 v251,16,v247
v_lshlrev_b32_e32 v251,1,v251
v_xor_b32_e32 v250,v250,v251
v_lshl_add_u32 v224,v248,10,v249
v_xor_b32_e32 v225,64,v224
v_add_u32_e32 v227,21504,v225
v_add_u32_e32 v226,14336,v224
v_add_u32_e32 v225,7168,v225
v_lshl_add_u32 v228,v248,13,v250
v_xor_b32_e32 v229,64,v228
v_add_u32_e32 v231,193536,v229
v_add_u32_e32 v230,129024,v228
v_add_u32_e32 v229,64512,v229
s_lshl_b32 s86,s19,2
s_add_u32 s86,s86,s12
s_lshl_b32 s52,s86,16
s_bfe_u32 s87,s21,65539
s_lshl_b32 s88,s20,1
s_add_u32 s87,s87,s88
s_lshl_b32 s87,s87,2
s_add_u32 s87,s87,s12
s_lshl_b32 s53,s87,7
s_mov_b32 s54,s53
s_mov_b32 s39,131072
s_mov_b32 s43,131072
s_mov_b32 s47,131072
s_mov_b32 s51,131072
s_mov_b32 s38,2097152
s_mov_b32 s42,4194304
s_mov_b32 s46,4194304
s_mov_b32 s56,32768
s_mov_b32 s60,-32640
s_mov_b32 s57,262144
s_mov_b32 s58,262144
s_lshl_b32 s98,s12,13
s_lshl_b32 s62,s12,12
s_add_u32 s62,s62,98304
s_lshl_b32 s35,%1,2
s_add_u32 s35,s35,83896320
v_lshrrev_b32_e32 v48,2,v247
v_and_b32_e32 v49,3,v247
v_lshlrev_b32_e32 v49,7,v49
v_lshl_add_u32 v48,v48,13,v49
s_lshl_b32 s89,s86,14
s_and_b32 s90,s53,7680
s_add_u32 s90,s90,s89
s_lshr_b32 s89,s21,3
s_lshl_b32 s89,s89,2
s_add_u32 s89,s89,s12
s_lshl_b32 s89,s89,14
s_lshl_b32 s91,s19,18
s_add_u32 s89,s89,s91
s_cmp_ge_u32 s21,16
s_waitcnt lgkmcnt(0)
s_mov_b64 s[36:37],s[70:71]
s_cselect_b64 s[40:41],s[76:77],s[74:75]
s_mov_b64 s[44:45],s[40:41]
s_bfm_b64 exec,8,0
buffer_load_dword a189,v48,s[40:43],s90 offen
s_mov_b64 exec,-1
s_and_b32 s9,s9,65535
s_mov_b32 s10,83951616
s_mov_b32 s11,131072
buffer_load_dword v44,off,s[8:11],s35 sc1
buffer_load_dword v45,off,s[8:11],s35 offset:1024 sc1
s_add_u32 m0,s62,0
s_nop 0
dl 228,40,53,0
dl 229,40,53,1024
s_add_u32 s53,s53,s57
dl 230,44,54,2048
dl 231,44,54,3072
s_add_u32 s54,s54,s57
s_add_u32 m0,s98,0
s_nop 0
dl 224,36,52,0
dl 225,36,52,1024
dl 226,36,52,2048
dl 227,36,52,3072
s_add_u32 s52,s52,s56
s_add_u32 m0,s98,4096
s_nop 0
dl 224,36,52,0
dl 225,36,52,1024
dl 226,36,52,2048
dl 227,36,52,3072
s_add_u32 s52,s52,s60
v_and_b32_e32 v246,15,v247
v_lshlrev_b32_e32 v248,3,v247
v_xor_b32_e32 v248,v248,v247
v_and_b32_e32 v248,112,v248
v_lshl_add_u32 v232,v246,7,v248
s_and_b32 s86,s98,16384
v_add_u32_e32 v232,s86,v232
v_xor_b32_e32 v233,64,v232
v_and_b32_e32 v246,12,v247
v_lshlrev_b32_e32 v246,5,v246
v_and_b32_e32 v248,3,v247
v_lshl_add_u32 v246,v248,3,v246
v_and_b32_e32 v248,24,v247
v_lshl_add_u32 v246,v248,2,v246
s_and_b32 s87,s98,8192
s_add_u32 s87,s87,98304
v_add_u32_e32 v246,s87,v246
v_and_b32_e32 v248,48,v247
v_lshl_add_u32 v234,v248,6,v246
v_xor_b32_e32 v235,32,v234
v_xor_b32_e32 v236,64,v234
v_xor_b32_e32 v237,96,v234
s_add_u32 m0,s62,16384
s_nop 0
dl 228,40,53,0
dl 229,40,53,1024
s_add_u32 s53,s53,s58
dl 230,44,54,2048
dl 231,44,54,3072
s_add_u32 s54,s54,s58
s_add_u32 m0,s98,32768
s_nop 0
dl 224,36,52,0
dl 225,36,52,1024
dl 226,36,52,2048
dl 227,36,52,3072
s_add_u32 s52,s52,s56
s_waitcnt vmcnt(20)
s_cmp_eq_u32 s12,0
s_cbranch_scc1 L_rq0
s_branch L_rqx
L_vr0:
s_setpc_b64 s[4:5]
L_rq0:
v_readfirstlane_b32 s32,v44
v_readfirstlane_b32 s86,v45
s_add_u32 s32,s32,1
s_cmp_eq_u32 s32,s86
s_addc_u32 s32,s32,0
v_mov_b32_e32 v44,s32
v_mov_b32_e32 v45,0
v_lshlrev_b32_e32 v46,3,v244
v_mov_b32_e32 v47,0
s_lshl_b32 s86,%1,7
s_add_u32 s87,s86,83914752
s_and_b32 s86,s86,16383
s_add_u32 s86,s86,83898368
s_mov_b64 exec,1
buffer_store_dword v44,off,s[8:11],s35 sc0 sc1
v_mov_b32_e32 v48,0
v_mov_b32_e32 v49,0
buffer_store_dwordx4 v[46:49],off,s[8:11],s86 sc0 sc1
buffer_store_dwordx4 v[46:49],off,s[8:11],s87 sc0 sc1
s_cmp_lg_u32 %1,0
s_cbranch_scc1 L_rqy
s_mov_b64 exec,65535
s_mov_b32 s86,83894272
buffer_store_dword v45,v46,s[8:11],s86 offen sc0 sc1
L_rqy:
s_mov_b64 exec,-1
L_rqx:
s_cmp_ge_u32 s21,16
s_cbranch_scc1 L_vtile
s_waitcnt vmcnt(8)
s_barrier
ta 64,234,0
ta 66,234,512
ra 0,232,0
ta 68,235,0
ta 70,235,512
ra 4,232,2048
ra 8,232,4096
ra 12,232,6144
ra 16,232,8192
ra 20,232,10240
ra 24,232,12288
ra 28,232,14336
s_add_u32 m0,s62,32768
v_mov_b32_e32 v216,0
dl 228,40,53,0
v_mov_b32_e32 v217,0
v_mov_b32_e32 v218,0
v_mov_b32_e32 v219,0
v_mov_b32_e32 v220,0
v_mov_b32_e32 v221,0
v_mov_b32_e32 v222,0
v_mov_b32_e32 v223,0
v_mov_b32_e32 v212,0
v_mov_b32_e32 v213,0
v_mov_b32_e32 v214,0
v_mov_b32_e32 v215,0
v_mov_b32_e32 v208,0
v_mov_b32_e32 v209,0
v_mov_b32_e32 v210,0
v_mov_b32_e32 v211,0
v_mov_b32_e32 v152,0
s_waitcnt lgkmcnt(9)
zaaa 128,64,0
s_lshl_b32 s4,%1,1
ta 72,236,0
v_mov_b32_e32 v153,0
s_waitcnt lgkmcnt(8)
zaaa 132,68,0
s_add_u32 s4,s4,s12
ta 74,236,512
v_mov_b32_e32 v154,0
s_waitcnt lgkmcnt(8)
zaaa 140,68,4
s_mul_i32 s4,s4,1280
ta 76,237,0
ta 78,237,512
zaaa 136,64,4
dl 229,40,53,1024
s_waitcnt lgkmcnt(4)
zvaa 32,64,8
s_add_u32 s53,s53,s57
v_mov_b32_e32 v155,0
v_mov_b32_e32 v156,0
zvaa 36,68,8
s_mov_b32 s55,s4
v_mov_b32_e32 v157,0
v_mov_b32_e32 v158,0
zvaa 44,68,12
s_mul_i32 s4,%1,1792
ta 80,234,4096
v_mov_b32_e32 v159,0
zvaa 40,64,12
dl 230,44,54,2048
zvaa 96,64,16
s_lshl_b32 s88,%1,2
ta 82,234,4608
v_mov_b32_e32 v148,0
zvaa 100,68,16
s_cmp_eq_u32 s12,2
s_cselect_b32 s55,s4,s55
ta 84,235,4096
v_mov_b32_e32 v149,0
zvaa 108,68,20
s_lshl_b32 s4,%1,14
ta 86,235,4608
v_mov_b32_e32 v150,0
zvaa 104,64,20
dl 231,44,54,3072
zvaa 160,64,24
s_add_u32 m0,s98,36864
v_mov_b32_e32 v151,0
v_mov_b32_e32 v144,0
zvaa 164,68,24
s_add_u32 s54,s54,s57
v_mov_b32_e32 v145,0
v_mov_b32_e32 v146,0
zvaa 172,68,28
s_add_u32 s88,s88,s12
v_mov_b32_e32 v147,0
v_mov_b32_e32 v88,0
zvaa 168,64,28
dl 224,36,52,0
s_waitcnt lgkmcnt(4)
zvaa 184,72,28
s_add_u32 s5,s4,16384
v_mov_b32_e32 v89,0
v_mov_b32_e32 v90,0
zvaa 188,76,28
s_cmp_eq_u32 s12,3
s_cselect_b32 s55,s4,s55
ra 32,233,0
v_mov_b32_e32 v91,0
zvaa 180,76,24
s_mov_b32 s4,10
v_mov_b32_e32 v92,0
v_mov_b32_e32 v93,0
zvaa 176,72,24
dl 225,36,52,1024
zvaa 120,72,20
s_lshl_b32 s88,s88,12
ra 36,233,2048
v_mov_b32_e32 v94,0
zvaa 124,76,20
s_xor_b32 s87,s19,15
ra 40,233,4096
v_mov_b32_e32 v95,0
zvaa 116,76,16
s_cmp_eq_u32 s12,2
s_cselect_b32 s4,14,s4
v_mov_b32_e32 v84,0
v_mov_b32_e32 v85,0
zvaa 112,72,16
dl 226,36,52,2048
zvaa 56,72,12
s_waitcnt vmcnt(11)
s_barrier
zvaa 60,76,12
s_add_u32 s30,s68,s88
s_addc_u32 s31,s69,0
ta 96,234,16384
v_mov_b32_e32 v86,0
zvaa 52,76,8
s_lshl_b32 s22,s87,7
ta 98,234,16896
v_mov_b32_e32 v87,0
zvaa 48,72,8
dl 227,36,52,3072
zaaa 152,72,4
s_add_u32 m0,s62,49152
ta 100,235,16384
v_mov_b32_e32 v80,0
zaaa 156,76,4
s_add_u32 s52,s52,s60
ta 102,235,16896
v_mov_b32_e32 v81,0
zaaa 148,76,0
s_lshl_b32 s87,s87,2
ra 44,233,6144
v_mov_b32_e32 v82,0
zaaa 144,72,0
dl 228,40,53,0
s_waitcnt lgkmcnt(5)
zvaa 0,80,0
s_lshl_b32 s88,s19,2
ra 48,233,8192
v_mov_b32_e32 v83,0
zvaa 4,84,0
s_cmp_eq_u32 s12,3
s_cselect_b32 s4,10,s4
ra 52,233,10240
v_mov_b32_e32 v24,0
zvaa 12,84,4
s_add_u32 s87,s87,s12
ra 56,233,12288
v_mov_b32_e32 v25,0
zvaa 8,80,4
dl 229,40,53,1024
zvaa 64,80,8
s_add_u32 s88,s88,s12
v_mov_b32_e32 v26,0
v_mov_b32_e32 v27,0
zvaa 68,84,8
s_lshl_b32 s90,s18,13
ra 60,233,14336
v_mov_b32_e32 v28,0
zvaa 76,84,12
s_add_u32 s90,s90,62914560
ta 104,236,16384
v_mov_b32_e32 v29,0
zvaa 72,80,12
dl 230,44,54,2048
zvaa 128,80,16
s_mul_i32 s14,s87,327680
ta 106,236,16896
v_mov_b32_e32 v30,0
zvaa 132,84,16
s_mul_i32 s13,s88,327680
ta 108,237,16384
v_mov_b32_e32 v31,0
zvaa 140,84,20
s_add_u32 s14,s14,s90
ta 110,237,16896
v_mov_b32_e32 v20,0
zvaa 136,80,20
dl 231,44,54,3072
zvaa 192,80,24
s_add_u32 m0,s98,65536
v_mov_b32_e32 v21,0
v_mov_b32_e32 v22,0
zvaa 196,84,24
s_add_u32 s13,s13,s90
v_mov_b32_e32 v23,0
v_mov_b32_e32 v16,0
zvaa 204,84,28
s_mul_i32 s90,s18,524288
v_mov_b32_e32 v17,0
v_mov_b32_e32 v18,0
zvaa 200,80,28
dl 224,36,52,0
s_waitcnt lgkmcnt(4)
maaa 128,96,32
s_lshl_b32 s91,s12,12
ta 208,236,4096
v_mov_b32_e32 v19,0
maaa 132,100,32
s_add_u32 s90,s90,s91
ta 210,236,4608
v_lshlrev_b32_e32 v241,7,v247
maaa 140,100,36
s_add_u32 s90,s90,16777216
ta 216,237,4096
v_mov_b32_e32 v242,134217728
maaa 136,96,36
dl 225,36,52,1024
mvaa 32,96,40
s_cmp_eq_u32 s12,3
s_cselect_b64 s[26:27],-1,0
ta 218,237,4608
v_lshrrev_b32_e32 v248,3,v247
mvaa 36,100,40
s_lshl_b32 s86,s19,1
v_and_b32_e32 v250,112,v244
v_and_b32_e32 v251,16,v247
mvaa 44,100,44
s_mov_b64 s[48:49],s[72:73]
v_lshlrev_b32_e32 v251,1,v251
v_cmp_gt_u32_e64 s[24:25],s4,v247
mvaa 40,96,44
dl 226,36,52,2048
mvaa 96,96,48
s_mov_b32 s50,8388608
v_mul_u32_u24_e32 v246,6144,v248
v_add_u32_e32 v249,s90,v244
mvaa 100,100,48
s_mov_b32 s59,655360
ta 112,234,20480
v_cndmask_b32_e64 v241,v242,v241,s[24:25]
mvaa 108,100,52
s_cmp_eq_u32 s12,2
s_cselect_b64 s[24:25],-1,0
ta 114,234,20992
v_add_u32_e32 v255,16777216,v249
mvaa 104,96,52
dl 227,36,52,3072
mvaa 160,96,56
s_add_u32 s52,s52,s56
s_add_u32 m0,s98,69632
ta 116,235,20480
mvaa 164,100,56
s_add_u32 s86,s86,2
ta 118,235,20992
v_accvgpr_write_b32 a240,v249
mvaa 172,100,60
s_cmp_eq_u32 s12,2
s_cselect_b64 s[48:49],s[80:81],s[48:49]
v_and_b32_e32 v249,48,v247
v_accvgpr_write_b32 a241,v255
mvaa 168,96,60
dl 224,36,52,0
s_waitcnt lgkmcnt(4)
mvaa 184,104,60
s_cmp_eq_u32 s12,2
s_cselect_b32 s50,6291456,s50
ta 212,236,20480
v_xor_b32_e32 v249,v249,v250
mvaa 188,108,60
s_cmp_eq_u32 s12,2
s_cselect_b32 s59,458752,s59
v_xor_b32_e32 v250,v250,v251
v_lshl_add_u32 v252,v248,11,v249
mvaa 180,108,56
s_lshl_b32 s15,1,s86
v_and_b32_e32 v251,48,v250
v_xor_b32_e32 v253,64,v252
mvaa 176,104,56
dl 225,36,52,1024
mvaa 120,104,52
s_lshl_b32 s23,s19,7
v_accvgpr_write_b32 a242,v252
v_add_u32_e32 v246,v246,v251
mvaa 124,108,52
s_or_b32 s86,%1,s12
s_cselect_b32 s65,0,4
v_add_u32_e32 v242,15360,v253
v_and_b32_e32 v251,64,v250
mvaa 116,108,48
s_cmp_eq_u32 s12,3
s_cselect_b64 s[48:49],s[68:69],s[48:49]
v_lshl_add_u32 v248,v251,5,v246
v_xor_b32_e32 v249,64,v251
mvaa 112,104,48
dl 226,36,52,2048
mvaa 56,104,44
s_cmp_eq_u32 s12,3
s_cselect_b32 s50,s5,s50
v_accvgpr_write_b32 a243,v242
v_mul_u32_u24_e32 v251,768,v251
mvaa 60,108,44
s_cmp_eq_u32 s12,3
s_cselect_b32 s59,1280,s59
v_add_u32_e32 v242,30720,v252
v_lshl_add_u32 v249,v249,5,v246
mvaa 52,108,40
s_add_u32 s53,s53,s58
v_add_u32_e32 v246,v246,v251
v_accvgpr_write_b32 a244,v242
mvaa 48,104,40
dl 227,36,52,3072
maaa 152,104,36
s_add_u32 s54,s54,s58
v_add_u32_e32 v242,46080,v253
v_accvgpr_write_b32 a245,v242
maaa 156,108,36
s_add_u32 s52,s52,s60
v_add_u32_e32 v243,4096,v246
v_add_u32_e32 v242,0,v248
maaa 148,108,32
s_mov_b32 s63,32768
ta 214,236,20992
v_cndmask_b32_e64 v242,v228,v242,s[24:25]
maaa 144,104,32
s_mov_b32 s61,s98
ta 220,237,20480
v_cndmask_b32_e64 v242,v242,v243,s[26:27]
s_waitcnt lgkmcnt(2)
mvaa 0,112,32
s_mov_b32 s64,1
v_add_u32_e32 v243,101376,v246
v_and_b32_e32 v246,15,v247
mvaa 4,116,32
v_and_b32_e32 v246,12,v247
v_accvgpr_write_b32 a246,v242
mvaa 12,116,36
ta 222,237,20992
v_add_u32_e32 v242,48128,v249
mvaa 8,112,36
v_cndmask_b32_e64 v242,v229,v242,s[24:25]
v_lshlrev_b32_e32 v246,5,v246
mvaa 64,112,40
v_cndmask_b32_e64 v242,v242,v243,s[26:27]
v_accvgpr_write_b32 a247,v242
mvaa 68,116,40
v_add_u32_e32 v242,96256,v248
v_and_b32_e32 v248,3,v247
mvaa 76,116,44
v_cndmask_b32_e64 v242,v230,v242,s[24:25]
v_lshl_add_u32 v246,v248,3,v246
mvaa 72,112,44
v_and_b32_e32 v248,24,v247
v_accvgpr_write_b32 a248,v242
mvaa 128,112,48
v_add_u32_e32 v242,144384,v249
v_lshl_add_u32 v246,v248,2,v246
mvaa 132,116,48
v_and_b32_e32 v248,32,v247
v_cndmask_b32_e64 v242,v231,v242,s[24:25]
mvaa 140,116,52
v_add_u32_e32 v246,98304,v246
v_accvgpr_write_b32 a249,v242
mvaa 136,112,52
v_lshl_add_u32 v238,v248,5,v246
v_xor_b32_e32 v239,32,v238
mvaa 192,112,56
v_add_u32_e32 v232,32768,v232
v_add_u32_e32 v233,32768,v233
mvaa 196,116,56
mvaa 204,116,60
mvaa 200,112,60
s_add_u32 s4,s96,L_vrk-L_att_pc
s_addc_u32 s5,s97,0
s_mov_b64 exec,0
s_cmp_eq_u32 s12,0
s_cbranch_scc1 L_vs1
s_cmp_eq_u32 s12,1
s_cbranch_scc1 L_vs2
s_cmp_eq_u32 s12,2
s_cbranch_scc1 L_vs3
s_branch L_vs7
L_vrk:
s_mov_b64 exec,-1
s_waitcnt vmcnt(8)
s_waitcnt lgkmcnt(0)
s_barrier
L_k_head:
mvaa 216,208,28
ra 64,232,0
mvaa 216,212,60
s_add_u32 m0,s62,0
ta 192,234,32768
mvaa 220,220,60
dl 228,40,53,0
mvaa 220,216,28
ta 194,234,33280
s_nop 3
mvaa 212,216,24
ra 96,233,0
mvaa 212,220,56
ta 196,234,49152
mvaa 208,212,56
ta 198,234,49664
mvaa 208,208,24
ta 204,235,49152
mvaa 152,208,20
dl 229,40,53,1024
mvaa 152,212,52
s_add_u32 s53,s53,s57
ta 206,235,49664
mvaa 156,220,52
ta 200,235,32768
mvaa 156,216,20
ta 202,235,33280
mvaa 148,216,16
ra 68,232,2048
mvaa 148,220,48
ra 100,233,2048
mvaa 144,212,48
ra 72,232,4096
mvaa 144,208,16
dl 230,44,54,2048
mvaa 88,208,12
ra 104,233,4096
mvaa 88,212,44
ra 76,232,6144
mvaa 92,220,44
ra 108,233,6144
mvaa 92,216,12
ra 80,232,8192
mvaa 84,216,8
ra 112,233,8192
mvaa 84,220,40
dl 231,44,54,3072
mvaa 80,212,40
s_add_u32 s54,s54,s57
ra 84,232,10240
mvaa 80,208,8
ra 116,233,10240
mvaa 24,208,4
ra 88,232,12288
mvaa 24,212,36
ra 120,233,12288
mvaa 28,220,36
ra 92,232,14336
mvaa 28,216,4
s_add_u32 m0,s62,16384
ra 124,233,14336
mvaa 20,216,0
dl 228,40,53,0
mvaa 20,220,32
mvaa 16,212,32
mvaa 16,208,0
s_waitcnt lgkmcnt(8)
maaa 128,192,64
maaa 128,196,96
maaa 132,204,96
dl 229,40,53,1024
maaa 132,200,64
s_add_u32 s53,s53,s58
maaa 140,200,68
L_vs1:
s_cbranch_execz L_vs8
maaa 140,204,100
maaa 136,196,100
maaa 136,192,68
mvaa 32,192,72
mvaa 32,196,104
dl 230,44,54,2048
mvaa 36,204,104
mvaa 36,200,72
s_waitcnt lgkmcnt(0)
mvaa 44,200,76
ta 208,236,32768
mvaa 44,204,108
ta 210,236,33280
mvaa 40,196,108
ta 212,236,49152
mvaa 40,192,76
dl 231,44,54,3072
mvaa 96,192,80
s_add_u32 s54,s54,s58
ta 214,236,49664
mvaa 96,196,112
ta 220,237,49152
mvaa 100,204,112
ta 222,237,49664
mvaa 100,200,80
ta 216,237,32768
mvaa 108,200,84
ta 218,237,33280
mvaa 108,204,116
mvaa 104,196,116
s_add_u32 m0,s61,0
mvaa 104,192,84
dl 224,36,52,0
mvaa 160,192,88
mvaa 160,196,120
mvaa 164,204,120
mvaa 164,200,88
mvaa 172,200,92
mvaa 172,204,124
mvaa 168,196,124
dl 225,36,52,1024
mvaa 168,192,92
s_waitcnt lgkmcnt(0)
mvaa 184,208,92
mvaa 184,212,124
mvaa 188,220,124
ta 192,234,36864
mvaa 188,216,92
ta 194,234,37376
mvaa 180,216,88
ta 196,234,53248
mvaa 180,220,120
dl 226,36,52,2048
mvaa 176,212,120
ta 198,234,53760
mvaa 176,208,88
ta 204,235,53248
mvaa 120,208,84
ta 206,235,53760
mvaa 120,212,116
ta 200,235,36864
mvaa 124,220,116
ta 202,235,37376
mvaa 124,216,84
mvaa 116,216,80
dl 227,36,52,3072
mvaa 116,220,112
s_add_u32 s52,s52,s56
mvaa 112,212,112
mvaa 112,208,80
mvaa 56,208,76
mvaa 56,212,108
mvaa 60,220,108
mvaa 60,216,76
mvaa 52,216,72
s_add_u32 m0,s61,4096
mvaa 52,220,104
dl 224,36,52,0
mvaa 48,212,104
s_add_u32 s61,s63,s98
mvaa 48,208,72
s_cmp_ge_u32 s63,65536
s_cselect_b32 s99,-2,1
s_lshl_b32 s99,s99,15
maaa 152,208,68
s_add_u32 s63,s63,s99
v_add_u32_e32 v232,s99,v232
v_add_u32_e32 v233,s99,v233
maaa 152,212,100
maaa 156,220,100
maaa 156,216,68
maaa 148,216,64
dl 225,36,52,1024
maaa 148,220,96
maaa 144,212,96
s_waitcnt lgkmcnt(0)
maaa 144,208,64
mvaa 0,192,64
mvaa 0,196,96
mvaa 4,204,96
ta 208,236,36864
mvaa 4,200,64
ta 210,236,37376
mvaa 12,200,68
dl 226,36,52,2048
mvaa 12,204,100
ta 212,236,53248
mvaa 8,196,100
ta 214,236,53760
mvaa 8,192,68
ta 220,237,53248
mvaa 64,192,72
ta 222,237,53760
mvaa 64,196,104
ta 216,237,36864
mvaa 68,204,104
ta 218,237,37376
mvaa 68,200,72
dl 227,36,52,3072
mvaa 76,200,76
s_add_u32 s52,s52,s60
mvaa 76,204,108
mvaa 72,196,108
mvaa 72,192,76
mvaa 128,192,80
mvaa 128,196,112
buffer_load_dword a184,v241,s[48:51],s55 offen
mvaa 132,204,112
s_add_u32 s55,s55,s59
mvaa 132,200,80
mvaa 140,200,84
mvaa 140,204,116
mvaa 136,196,116
mvaa 136,192,84
buffer_load_dword a185,v241,s[48:51],s55 offen
mvaa 192,192,88
s_add_u32 s55,s55,s59
mvaa 192,196,120
mvaa 196,204,120
mvaa 196,200,88
mvaa 204,200,92
mvaa 204,204,124
s_cmp_eq_u32 s64,3
mvaa 200,196,124
s_waitcnt vmcnt(10) lgkmcnt(0)
s_barrier
mvaa 200,192,92
s_cbranch_scc1 L_k_exit
mvaa 216,208,92
ra 0,232,0
mvaa 216,212,124
s_add_u32 m0,s62,32768
ta 192,234,0
mvaa 220,220,124
dl 228,40,53,0
mvaa 220,216,92
ta 194,234,512
s_nop 3
mvaa 212,216,88
ra 32,233,0
mvaa 212,220,120
ta 196,234,16384
mvaa 208,212,120
ta 198,234,16896
mvaa 208,208,88
ta 204,235,16384
mvaa 152,208,84
dl 229,40,53,1024
mvaa 152,212,116
s_add_u32 s53,s53,s57
ta 206,235,16896
mvaa 156,220,116
ta 200,235,0
mvaa 156,216,84
ta 202,235,512
mvaa 148,216,80
ra 4,232,2048
mvaa 148,220,112
ra 36,233,2048
mvaa 144,212,112
ra 8,232,4096
mvaa 144,208,80
dl 230,44,54,2048
mvaa 88,208,76
ra 40,233,4096
mvaa 88,212,108
ra 12,232,6144
mvaa 92,220,108
ra 44,233,6144
mvaa 92,216,76
ra 16,232,8192
mvaa 84,216,72
ra 48,233,8192
mvaa 84,220,104
dl 231,44,54,3072
mvaa 80,212,104
s_add_u32 s54,s54,s57
ra 20,232,10240
mvaa 80,208,72
ra 52,233,10240
mvaa 24,208,68
ra 24,232,12288
mvaa 24,212,100
ra 56,233,12288
mvaa 28,220,100
ra 28,232,14336
mvaa 28,216,68
s_add_u32 m0,s62,49152
ra 60,233,14336
mvaa 20,216,64
dl 228,40,53,0
mvaa 20,220,96
mvaa 16,212,96
mvaa 16,208,64
s_waitcnt lgkmcnt(8)
maaa 128,192,0
maaa 128,196,32
maaa 132,204,32
dl 229,40,53,1024
maaa 132,200,0
s_add_u32 s53,s53,s58
maaa 140,200,4
maaa 140,204,36
maaa 136,196,36
maaa 136,192,4
mvaa 32,192,8
mvaa 32,196,40
dl 230,44,54,2048
mvaa 36,204,40
mvaa 36,200,8
s_waitcnt lgkmcnt(0)
mvaa 44,200,12
ta 208,236,0
mvaa 44,204,44
ta 210,236,512
mvaa 40,196,44
ta 212,236,16384
mvaa 40,192,12
dl 231,44,54,3072
mvaa 96,192,16
s_add_u32 s54,s54,s58
ta 214,236,16896
mvaa 96,196,48
ta 220,237,16384
mvaa 100,204,48
ta 222,237,16896
mvaa 100,200,16
ta 216,237,0
mvaa 108,200,20
ta 218,237,512
mvaa 108,204,52
mvaa 104,196,52
s_add_u32 m0,s61,0
mvaa 104,192,20
dl 224,36,52,0
mvaa 160,192,24
s_lshl_b32 s86,s21,2
s_add_u32 s86,s86,s12
s_mul_i32 s86,s86,384
mvaa 160,196,56
s_add_u32 s86,s86,4096
v_lshl_add_u32 v253,v244,3,s86
mvaa 164,204,56
s_mov_b64 exec,7
)"
R"(global_load_dword a188,v253,s[96:97]
s_mov_b64 exec,-1
mvaa 164,200,24
mvaa 172,200,28
mvaa 172,204,60
mvaa 168,196,60
dl 225,36,52,1024
mvaa 168,192,28
s_waitcnt lgkmcnt(0)
mvaa 184,208,28
mvaa 184,212,60
mvaa 188,220,60
ta 192,234,4096
mvaa 188,216,28
ta 194,234,4608
mvaa 180,216,24
ta 196,234,20480
mvaa 180,220,56
dl 226,36,52,2048
mvaa 176,212,56
ta 198,234,20992
mvaa 176,208,24
ta 204,235,20480
mvaa 120,208,20
ta 206,235,20992
mvaa 120,212,52
ta 200,235,4096
mvaa 124,220,52
ta 202,235,4608
mvaa 124,216,20
mvaa 116,216,16
dl 227,36,52,3072
mvaa 116,220,48
s_add_u32 s52,s52,s56
mvaa 112,212,48
mvaa 112,208,16
mvaa 56,208,12
mvaa 56,212,44
s_bitcmp1_b32 s65,s64
s_cbranch_scc1 L_k_ev
L_k_evr:
mvaa 60,220,44
mvaa 60,216,12
mvaa 52,216,8
s_add_u32 m0,s61,4096
mvaa 52,220,40
dl 224,36,52,0
mvaa 48,212,40
s_add_u32 s61,s63,s98
mvaa 48,208,8
s_cmp_ge_u32 s63,65536
s_cselect_b32 s99,-2,1
s_lshl_b32 s99,s99,15
maaa 152,208,4
s_add_u32 s63,s63,s99
v_add_u32_e32 v232,s99,v232
v_add_u32_e32 v233,s99,v233
maaa 152,212,36
maaa 156,220,36
maaa 156,216,4
maaa 148,216,0
dl 225,36,52,1024
maaa 148,220,32
maaa 144,212,32
s_waitcnt lgkmcnt(0)
maaa 144,208,0
mvaa 0,192,0
mvaa 0,196,32
mvaa 4,204,32
ta 208,236,4096
mvaa 4,200,0
ta 210,236,4608
mvaa 12,200,4
dl 226,36,52,2048
mvaa 12,204,36
ta 212,236,20480
mvaa 8,196,36
ta 214,236,20992
mvaa 8,192,4
ta 220,237,20480
mvaa 64,192,8
ta 222,237,20992
mvaa 64,196,40
ta 216,237,4096
mvaa 68,204,40
ta 218,237,4608
mvaa 68,200,8
dl 227,36,52,3072
mvaa 76,200,12
s_add_u32 s52,s52,s60
mvaa 76,204,44
mvaa 72,196,44
mvaa 72,192,12
mvaa 128,192,16
mvaa 128,196,48
buffer_load_dword a187,v241,s[48:51],s55 offen
mvaa 132,204,48
s_add_u32 s55,s55,s59
mvaa 132,200,16
mvaa 140,200,20
mvaa 140,204,52
mvaa 136,196,52
mvaa 136,192,20
buffer_load_dword a190,v241,s[48:51],s55 offen
mvaa 192,192,24
s_add_u32 s55,s55,s59
mvaa 192,196,56
mvaa 196,204,56
s_add_u32 s64,s64,1
mvaa 196,200,24
mvaa 204,200,28
mvaa 204,204,60
mvaa 200,196,60
s_waitcnt vmcnt(10) lgkmcnt(0)
s_barrier
mvaa 200,192,28
s_branch L_k_head
L_k_exit:
global_load_dwordx4 a[160:163],v244,s[30:31] offset:0
s_add_u32 m0,s62,32768
mvaa 216,208,92
s_cmp_lt_u32 s12,2
s_cselect_b32 s86,s22,s23
ta 32,234,0
s_and_b32 s87,s12,1
mvaa 216,212,124
s_lshl_b32 s86,s86,11
ta 34,234,512
s_lshl_b32 s87,s87,17
mvaa 220,220,124
s_add_u32 s86,s86,s87
ra 0,232,0
s_mov_b64 s[36:37],s[68:69]
mvaa 220,216,92
s_mov_b32 s38,4194304
ta 36,235,0
s_mov_b32 s52,s86
mvaa 212,216,88
s_mov_b32 s56,65536
ta 38,235,512
v_accvgpr_read_b32 v224,a242
mvaa 212,220,120
dl 228,40,53,0
mvaa 208,212,120
s_lshl_b32 s88,s18,1
ra 4,232,2048
v_accvgpr_read_b32 v225,a243
mvaa 208,208,88
s_add_u32 s88,s88,s12
ra 8,232,4096
v_accvgpr_read_b32 v226,a244
mvaa 152,208,84
s_mov_b32 s91,196608
ra 12,232,6144
v_accvgpr_read_b32 v227,a245
mvaa 152,212,116
s_lshl_b32 s88,s88,7
ra 16,232,8192
s_lshl_b32 s89,s18,6
mvaa 156,220,116
s_mov_b32 s90,6291456
ra 20,232,10240
s_mov_b32 s60,-65408
mvaa 156,216,84
dl 229,40,53,1024
mvaa 148,216,80
s_add_u32 s53,s53,s57
ra 24,232,12288
s_bfe_u32 s4,s21,65536
mvaa 148,220,112
s_lshl_b32 s4,s4,6
ra 28,232,14336
s_lshl_b32 s87,s20,2
mvaa 144,212,112
s_mov_b32 s26,32768
ta 40,236,0
s_mov_b32 s27,131072
mvaa 144,208,80
s_add_u32 s61,s98,32768
ta 42,236,512
s_mov_b32 s63,65536
mvaa 88,208,76
s_cmp_ge_u32 s63,65536
s_cselect_b32 s99,-2,1
ta 44,237,0
s_lshl_b32 s99,s99,15
mvaa 88,212,108
dl 230,44,54,2048
mvaa 92,220,108
s_mov_b32 s67,2048
ta 46,237,512
s_mov_b32 s66,8
mvaa 92,216,76
s_mov_b32 s64,1
ta 48,234,4096
mvaa 84,216,72
s_bitcmp1_b32 s12,0
s_cselect_b64 vcc,-1,0
ta 50,234,4608
mvaa 84,220,104
ta 52,235,4096
mvaa 80,212,104
ta 54,235,4608
mvaa 80,208,72
dl 231,44,54,3072
s_add_u32 m0,s62,49152
mvaa 24,208,68
s_add_u32 s54,s54,s57
ta 56,236,4096
mvaa 24,212,100
s_cmp_lt_u32 s12,2
s_cselect_b32 s57,262144,s91
ta 58,236,4608
mvaa 28,220,100
ta 60,237,4096
mvaa 28,216,68
ta 62,237,4608
mvaa 20,216,64
mvaa 20,220,96
dl 228,40,53,0
mvaa 16,212,96
v_accvgpr_read_b32 v228,a246
mvaa 16,208,64
s_waitcnt lgkmcnt(5)
maaa 128,32,0
maaa 132,36,0
maaa 140,36,4
dl 229,40,53,1024
maaa 136,32,4
s_waitcnt lgkmcnt(0)
s_barrier
mvaa 32,32,8
s_add_u32 s53,s53,s58
ta 96,234,16384
v_accvgpr_read_b32 v229,a247
mvaa 36,36,8
s_cmp_lt_u32 s12,2
s_cselect_b32 s42,8388608,s90
ta 98,234,16896
mvaa 44,36,12
s_cmp_lt_u32 s12,2
s_cselect_b32 s53,s88,s89
ra 64,233,0
s_and_b32 s89,s98,8192
mvaa 40,32,12
dl 230,44,54,2048
mvaa 96,32,16
s_bitcmp1_b32 s21,0
s_cselect_b32 s90,s57,0
ta 100,235,16384
s_add_u32 s53,s53,s90
mvaa 100,36,16
s_cmp_lt_u32 s12,2
s_cselect_b64 s[40:41],s[72:73],s[80:81]
ta 102,235,16896
v_accvgpr_read_b32 v230,a248
mvaa 108,36,20
s_lshl_b32 s90,s90,1
ra 68,233,2048
s_add_u32 s91,s89,65536
mvaa 104,32,20
s_bfe_u32 s88,s21,65539
ra 72,233,4096
s_lshl_b32 s88,s88,1
mvaa 160,32,24
dl 231,44,54,3072
mvaa 164,36,24
s_add_u32 m0,s98,65536
ra 76,233,6144
s_add_u32 s54,s54,s58
mvaa 172,36,28
s_cmp_eq_u32 s12,3
s_cselect_b64 s[44:45],s[68:69],s[40:41]
ra 80,233,8192
v_accvgpr_read_b32 v231,a249
mvaa 168,32,28
s_cmp_eq_u32 s12,3
s_cselect_b32 s46,0,s42
ra 84,233,10240
s_add_u32 s58,s57,s90
mvaa 184,40,28
s_cmp_eq_u32 s12,3
s_cselect_b32 s54,0,s53
ra 88,233,12288
s_sub_u32 s57,s57,s90
mvaa 188,44,28
global_load_dwordx4 a[168:171],v244,s[30:31] offset:2048
mvaa 180,44,24
s_add_u32 s90,s89,65536
ta 104,236,16384
s_add_u32 s88,s88,s87
mvaa 176,40,24
s_and_b32 s87,s12,1
ra 92,233,14336
s_add_u32 s88,s88,s87
mvaa 120,40,20
s_mul_i32 s88,s88,524288
ta 106,236,16896
s_and_b32 s87,s21,7
mvaa 124,44,20
L_vs2:
s_cbranch_execz L_vs9
s_add_u32 s86,s88,16777216
ta 108,237,16384
s_lshl_b32 s87,s87,16
mvaa 116,44,16
global_load_dwordx4 a[176:179],v244,s[84:85] offset:0
mvaa 112,40,16
s_add_u32 s86,s86,s87
ta 110,237,16896
s_lshr_b32 s87,s12,1
mvaa 56,40,12
s_lshl_b32 s87,s87,15
ta 112,234,20480
s_add_u32 s86,s86,s87
mvaa 60,44,12
s_add_u32 s24,s8,s86
s_addc_u32 s25,s9,0
ta 114,234,20992
s_lshl_b32 s86,%1,5
mvaa 52,44,8
s_lshl_b32 s87,s12,3
ta 116,235,20480
s_add_u32 s86,s86,s87
mvaa 48,40,8
global_load_dwordx4 a[164:167],v244,s[30:31] offset:1024
maaa 152,40,4
s_add_u32 s86,s86,83886080
ta 118,235,20992
s_mov_b32 s88,4096
maaa 156,44,4
s_cmp_lg_u32 s65,0
s_cbranch_scc1 L_k_ec1
L_k_ecr1:
ta 120,236,20480
maaa 148,44,0
s_cmp_eq_u32 s12,0
s_cselect_b32 s65,96,0
ta 122,236,20992
s_or_b32 s65,s65,128
maaa 144,40,0
ta 124,237,20480
mvaa 0,48,0
global_load_dwordx4 a[172:175],v244,s[30:31] offset:3072
mvaa 4,52,0
s_mov_b32 s30,1048577
ta 126,237,20992
s_mov_b32 s31,268435712
mvaa 12,52,4
mvaa 8,48,4
mvaa 64,48,8
mvaa 68,52,8
global_load_dwordx4 a[180:183],v244,s[84:85] offset:1024
mvaa 76,52,12
mvaa 72,48,12
mvaa 128,48,16
mvaa 132,52,16
mvaa 140,52,20
dl 224,36,52,0
mvaa 136,48,20
mvaa 192,48,24
mvaa 196,52,24
mvaa 204,52,28
mvaa 200,48,28
dl 225,36,52,1024
mvaa 216,56,28
mvaa 220,60,28
mvaa 212,60,24
mvaa 208,56,24
mvaa 152,56,20
dl 226,36,52,2048
mvaa 156,60,20
mvaa 148,60,16
mvaa 144,56,16
mvaa 88,56,12
s_waitcnt vmcnt(12) lgkmcnt(0)
s_barrier
mvaa 92,60,12
dl 227,36,52,3072
mvaa 84,60,8
s_add_u32 s52,s52,s56
s_add_u32 m0,s98,69632
ta 32,234,32768
mvaa 80,56,8
ta 34,234,33280
mvaa 24,56,4
ta 36,235,32768
mvaa 28,60,4
ta 38,235,33280
mvaa 20,60,0
dl 224,36,52,0
mvaa 16,56,0
ta 40,236,32768
maaa 128,96,64
ta 42,236,33280
maaa 132,100,64
ta 44,237,32768
maaa 140,100,68
ta 46,237,33280
maaa 136,96,68
dl 225,36,52,1024
mvaa 32,96,72
ta 48,234,36864
mvaa 36,100,72
ta 50,234,37376
mvaa 44,100,76
ta 52,235,36864
mvaa 40,96,76
ta 54,235,37376
mvaa 96,96,80
dl 226,36,52,2048
mvaa 100,100,80
ra 20,232,43008
mvaa 108,100,84
ra 24,232,45056
mvaa 104,96,84
ra 0,232,32768
mvaa 160,96,88
ra 28,232,47104
mvaa 164,100,88
dl 227,36,52,3072
mvaa 172,100,92
s_add_u32 m0,s62,0
ra 4,232,34816
s_add_u32 s52,s52,s60
mvaa 168,96,92
ra 16,232,40960
mvaa 184,104,92
ra 8,232,36864
mvaa 188,108,92
ra 12,232,38912
v_add_u32_e32 v232,s90,v232
mvaa 180,108,88
dl 228,40,53,0
mvaa 176,104,88
ta 60,237,36864
v_xor_b32_e32 v232,s4,v232
mvaa 120,104,84
ta 62,237,37376
mvaa 124,108,84
ta 56,236,36864
mvaa 116,108,80
ta 58,236,37376
mvaa 112,104,80
dl 229,40,53,1024
mvaa 56,104,76
s_add_u32 s53,s53,s57
mvaa 60,108,76
mvaa 52,108,72
mvaa 48,104,72
maaa 152,104,68
dl 230,44,54,2048
maaa 156,108,68
maaa 148,108,64
maaa 144,104,64
mvaa 0,112,64
mvaa 4,116,64
dl 231,44,54,3072
mvaa 12,116,68
s_add_u32 m0,s62,16384
s_add_u32 s54,s54,s57
mvaa 8,112,68
mvaa 64,112,72
mvaa 68,116,72
mvaa 76,116,76
dl 228,40,53,0
mvaa 72,112,76
mvaa 128,112,80
mvaa 132,116,80
mvaa 140,116,84
mvaa 136,112,84
dl 229,40,53,1024
mvaa 192,112,88
s_add_u32 s53,s53,s58
mvaa 196,116,88
mvaa 204,116,92
mvaa 200,112,92
mvaa 216,120,92
dl 230,44,54,2048
mvaa 220,124,92
mvaa 212,124,88
s_waitcnt vmcnt(20) lgkmcnt(0)
s_barrier
mvaa 208,120,88
ra 92,233,47104
mvaa 152,120,84
ta 96,234,49152
mvaa 156,124,84
dl 231,44,54,3072
mvaa 148,124,80
s_add_u32 m0,s98,0
ra 88,233,45056
s_add_u32 s54,s54,s58
mvaa 144,120,80
ta 98,234,49664
mvaa 88,120,76
ta 112,234,53248
mvaa 92,124,76
ta 116,235,53248
mvaa 84,124,72
dl 224,36,52,0
mvaa 80,120,72
ta 118,235,53760
mvaa 24,120,68
ta 114,234,53760
v_subrev_u32_e32 v234,s89,v234
mvaa 28,124,68
ta 100,235,49152
mvaa 20,124,64
ta 102,235,49664
v_subrev_u32_e32 v235,s89,v235
mvaa 16,120,64
dl 225,36,52,1024
maaa 128,32,0
ta 124,237,53248
maaa 132,36,0
ta 126,237,53760
maaa 140,36,4
ta 120,236,53248
maaa 136,32,4
ta 122,236,53760
mvaa 32,32,8
dl 226,36,52,2048
mvaa 36,36,8
ta 104,236,49152
mvaa 44,36,12
ta 106,236,49664
v_subrev_u32_e32 v236,s89,v236
mvaa 40,32,12
ta 108,237,49152
mvaa 56,40,12
ta 110,237,49664
v_subrev_u32_e32 v237,s89,v237
mvaa 60,44,12
dl 227,36,52,3072
mvaa 52,44,8
s_add_u32 s52,s52,s56
s_add_u32 m0,s98,4096
ra 84,233,43008
mvaa 48,40,8
ra 80,233,40960
maaa 152,40,4
ra 72,233,36864
maaa 156,44,4
ra 76,233,38912
maaa 148,44,0
dl 224,36,52,0
maaa 144,40,0
ra 68,233,34816
mvaa 0,48,0
ra 64,233,32768
v_add_u32_e32 v233,s91,v233
mvaa 4,52,0
v_xor_b32_e32 v233,s4,v233
mvaa 12,52,4
mvaa 8,48,4
dl 225,36,52,1024
mvaa 24,56,4
mvaa 28,60,4
mvaa 20,60,0
mvaa 16,56,0
mvaa 220,60,28
dl 226,36,52,2048
s_waitcnt lgkmcnt(2)
mvaa 220,124,92
mvaa 216,120,92
mvaa 216,56,28
mvaa 212,60,24
mvaa 212,124,88
dl 227,36,52,3072
mvaa 208,120,88
s_add_u32 m0,s62,32768
s_add_u32 s52,s52,s60
s_waitcnt vmcnt(25)
zvaa 246,160,176
mvaa 208,56,24
cv 223,222,223
cv 222,220,221
mvaa 204,52,28
cv 221,218,219
zvaa 250,168,176
mvaa 204,116,92
cv 220,216,217
cv 219,214,215
mvaa 200,112,92
dl 228,40,53,0
s_waitcnt vmcnt(25)
mvaa 246,164,180
mvaa 200,48,28
cv 218,212,213
mvaa 250,172,180
mvaa 196,52,24
cv 217,210,211
cv 216,208,209
mvaa 196,116,88
cv 215,206,207
v_cndmask_b32_e64 v246,0,v246,s[30:31]
mvaa 192,112,88
cv 214,204,205
v_cndmask_b32_e64 v250,0,v250,s[30:31]
mvaa 192,48,24
dl 229,40,53,1024
s_lshl_b64 s[30:31],s[30:31],1
v_cndmask_b32_e64 v246,v246,v247,s[30:31]
v_cndmask_b32_e64 v250,v250,v251,s[30:31]
mvaa 188,44,28
s_lshl_b64 s[30:31],s[30:31],1
s_add_u32 s53,s53,s57
v_cndmask_b32_e64 v246,v246,v248,s[30:31]
v_cndmask_b32_e64 v250,v250,v252,s[30:31]
mvaa 188,108,92
s_lshl_b64 s[30:31],s[30:31],1
cv 213,202,203
v_cndmask_b32_e64 v246,v246,v249,s[30:31]
mvaa 184,104,92
v_cndmask_b32_e64 v250,v250,v253,s[30:31]
v_add_f32_dpp v246,v246,v246 quad_perm:[1,0,3,2] row_mask:0xf bank_mask:0xf
mvaa 184,40,28
v_add_f32_dpp v250,v250,v250 quad_perm:[1,0,3,2] row_mask:0xf bank_mask:0xf
v_add_f32_dpp v246,v246,v246 quad_perm:[2,3,0,1] row_mask:0xf bank_mask:0xf
mvaa 180,44,24
dl 230,44,54,2048
v_add_f32_dpp v250,v250,v250 quad_perm:[2,3,0,1] row_mask:0xf bank_mask:0xf
mvaa 180,108,88
v_add_f32_dpp v246,v246,v246 row_half_mirror row_mask:0xf bank_mask:0xf
v_add_f32_dpp v250,v250,v250 row_half_mirror row_mask:0xf bank_mask:0xf
mvaa 176,104,88
v_add_f32_dpp v246,v246,v246 row_mirror row_mask:0xf bank_mask:0xf
v_add_f32_dpp v250,v250,v250 row_mirror row_mask:0xf bank_mask:0xf
mvaa 176,40,24
cv 212,200,201
cv 211,198,199
mvaa 172,36,28
v_permlane16_swap_b32_e32 v246,v250
mvaa 172,100,92
dl 231,44,54,3072
v_add_f32_e32 v246,v246,v250
v_mov_b32_e32 v250,v246
mvaa 168,96,92
s_add_u32 s54,s54,s57
v_permlane32_swap_b32_e32 v246,v250
mvaa 168,32,28
v_add_f32_e32 v246,v246,v250
v_lshrrev_b32_e32 v250,6,v244
mvaa 164,36,24
v_mul_f32_e32 v246,0xbfb8aa3b,v246
cv 210,196,197
mvaa 164,100,88
ex 246
mvaa 160,96,88
buffer_load_dword a242,v241,s[48:51],s55 offen
mvaa 160,32,24
s_add_u32 s55,s55,s59
v_add_f32_e32 v246,1.0,v246
cv 209,194,195
mvaa 156,60,20
v_rcp_f32_e32 v246,v246
mvaa 156,124,84
s_waitcnt vmcnt(17) lgkmcnt(0)
s_barrier
mvaa 152,120,84
s_add_u32 m0,s62,49152
cv 208,192,193
cv 207,190,191
mvaa 152,56,20
dl 228,40,53,0
mvaa 148,60,16
cv 206,188,189
cv 205,186,187
mvaa 148,124,80
cv 204,184,185
cv 203,182,183
mvaa 144,120,80
cv 202,180,181
cv 201,178,179
mvaa 144,56,16
cv 200,176,177
cv 199,174,175
mvaa 140,52,20
dl 229,40,53,1024
mvaa 140,116,84
s_add_u32 s53,s53,s58
cv 198,172,173
cv 197,170,171
mvaa 136,112,84
ra 4,232,0
cv 196,168,169
mvaa 136,48,20
ta 92,239,12288
cv 195,166,167
mvaa 132,52,16
ta 94,239,12800
cv 194,164,165
mvaa 132,116,80
dl 230,44,54,2048
mvaa 128,112,80
ta 88,238,12288
cv 193,162,163
mvaa 128,48,16
ta 90,238,12800
cv 192,160,161
mvaa 124,44,20
cv 191,158,159
cv 190,156,157
mvaa 124,108,84
cv 189,154,155
cv 188,152,153
mvaa 120,104,84
dl 231,44,54,3072
mvaa 120,40,20
s_add_u32 m0,s98,32768
cv 187,150,151
cv 186,148,149
mvaa 116,44,16
s_add_u32 s54,s54,s58
cv 185,146,147
cv 184,144,145
mvaa 116,108,80
cv 183,142,143
cv 182,140,141
mvaa 112,104,80
cv 181,138,139
cv 180,136,137
mvaa 112,40,16
buffer_load_dword a243,v241,s[48:51],s55 offen
mvaa 108,36,20
s_add_u32 s55,s55,s59
cv 179,134,135
cv 178,132,133
mvaa 108,100,84
cv 177,130,131
cv 176,128,129
mvaa 104,96,84
cv 175,126,127
cv 174,124,125
mvaa 104,32,20
cv 173,122,123
cv 172,120,121
mvaa 100,36,16
dl 224,36,52,0
mvaa 100,100,80
ta 44,235,0
cv 171,118,119
mvaa 96,96,80
ta 46,235,512
cv 170,116,117
mvaa 96,32,16
ta 40,234,0
cv 169,114,115
mvaa 92,60,12
ta 42,234,512
cv 168,112,113
mvaa 92,124,76
dl 225,36,52,1024
mvaa 88,120,76
cv 167,110,111
cv 166,108,109
mvaa 88,56,12
ta 84,237,8192
cv 165,106,107
mvaa 84,60,8
ta 86,237,8704
cv 164,104,105
mvaa 84,124,72
ta 80,236,8192
cv 163,102,103
mvaa 80,120,72
dl 226,36,52,2048
mvaa 80,56,8
ta 82,236,8704
cv 162,100,101
mvaa 76,52,12
ra 16,232,6144
cv 161,98,99
mvaa 76,116,76
ta 60,235,4096
cv 160,96,97
mvaa 72,112,76
ta 56,234,4096
cv 159,94,95
mvaa 72,48,12
dl 227,36,52,3072
mvaa 68,52,8
s_add_u32 m0,s62,0
ta 62,235,4608
s_add_u32 s52,s52,s56
mvaa 68,116,72
ta 58,234,4608
cv 151,78,79
mvaa 64,112,72
ra 12,232,4096
cv 150,76,77
mvaa 64,48,8
ta 52,237,0
cv 158,92,93
mvaa 60,108,76
buffer_load_dword a244,v241,s[48:51],s55 offen
mvaa 56,104,76
s_add_u32 s55,s55,s59
ta 54,237,512
cv 147,70,71
mvaa 52,108,72
ra 8,232,2048
cv 146,68,69
mvaa 48,104,72
ta 48,236,0
cv 145,66,67
mvaa 44,100,76
ta 50,236,512
cv 144,64,65
mvaa 40,96,76
buffer_load_dword a245,v241,s[48:51],s55 offen
mvaa 36,100,72
s_add_u32 s55,s55,s59
cv 143,62,63
cv 142,60,61
mvaa 32,96,72
cv 141,58,59
cv 140,56,57
mvaa 28,124,68
cv 139,54,55
cv 138,52,53
mvaa 24,120,68
cv 137,50,51
cv 136,48,49
mvaa 20,124,64
s_mov_b64 exec,65537
buffer_store_dword v246,v250,s[8:11],s86 offen sc0 sc1
s_mov_b64 exec,-1
mvaa 16,120,64
cv 135,46,47
cv 134,44,45
mvaa 12,116,68
ta 76,235,8192
cv 133,42,43
mvaa 8,112,68
ta 78,235,8704
cv 132,40,41
mvaa 4,116,64
ta 72,234,8192
cv 131,38,39
mvaa 0,112,64
ta 74,234,8704
cv 130,36,37
maaa 156,108,68
L_vs3:
s_cbranch_execz L_vs10
cv 129,34,35
cv 128,32,33
maaa 152,104,68
cv 127,30,31
cv 126,28,29
maaa 148,108,64
cv 125,26,27
cv 124,24,25
maaa 144,104,64
v_accvgpr_read_b32 v63,a159
v_accvgpr_read_b32 v62,a158
maaa 140,100,68
v_accvgpr_read_b32 v61,a157
v_accvgpr_read_b32 v60,a156
maaa 136,96,68
v_accvgpr_read_b32 v59,a155
v_accvgpr_read_b32 v58,a154
maaa 132,100,64
v_accvgpr_read_b32 v57,a153
v_accvgpr_read_b32 v56,a152
maaa 128,96,64
ta 68,237,4096
ta 70,237,4608
s_waitcnt lgkmcnt(4)
zaaa 236,40,4
cv 56,56,57
cv 57,58,59
zaaa 220,44,4
ta 64,236,4096
ta 66,236,4608
zaaa 216,44,8
cv 58,60,61
cv 59,62,63
zaaa 232,40,8
buffer_store_dwordx4 v[56:59],v244,s[24:27],0 offen offset:3072 sc0 sc1
zaaa 228,40,12
v_accvgpr_read_b32 v55,a151
v_accvgpr_read_b32 v54,a150
zaaa 212,44,12
v_accvgpr_read_b32 v53,a149
v_accvgpr_read_b32 v52,a148
zaaa 208,44,16
v_accvgpr_read_b32 v51,a147
v_accvgpr_read_b32 v50,a146
zaaa 224,40,16
v_accvgpr_read_b32 v49,a145
v_accvgpr_read_b32 v48,a144
zaaa 192,48,16
cv 48,48,49
cv 49,50,51
zaaa 176,52,16
cv 50,52,53
s_waitcnt vmcnt(26) lgkmcnt(0)
s_barrier
zaaa 180,52,12
dl 228,40,53,0
cv 51,54,55
zaaa 196,48,12
ra 24,233,0
v_accvgpr_read_b32 v47,a143
zaaa 200,48,8
ra 28,233,2048
v_accvgpr_read_b32 v46,a142
v_accvgpr_read_b32 v45,a141
zaaa 184,52,8
ra 32,233,4096
ra 36,233,6144
zaaa 188,52,4
v_accvgpr_read_b32 v43,a139
v_accvgpr_read_b32 v42,a138
zaaa 204,48,4
dl 229,40,53,1024
ta 96,236,24576
zaaa 172,56,4
s_add_u32 s53,s53,s57
ta 98,236,25088
v_accvgpr_read_b32 v41,a137
zaaa 156,60,4
ta 100,237,24576
v_accvgpr_read_b32 v39,a135
zaaa 152,60,8
ta 102,237,25088
ta 104,238,28672
zaaa 168,56,8
v_accvgpr_read_b32 v38,a134
v_accvgpr_read_b32 v35,a131
zaaa 164,56,12
dl 230,44,54,2048
ta 106,238,29184
zaaa 148,60,12
ta 108,239,28672
v_accvgpr_read_b32 v34,a130
zaaa 144,60,16
ta 110,239,29184
v_accvgpr_read_b32 v33,a129
zaaa 160,56,16
ta 44,235,16384
ta 46,235,16896
ta 40,234,16384
ta 42,234,16896
ta 52,237,16384
ta 54,237,16896
ta 48,236,16384
ta 50,236,16896
ta 60,235,20480
ta 62,235,20992
ta 56,234,20480
ta 58,234,20992
v_accvgpr_read_b32 v32,a128
zaaa 128,64,16
v_accvgpr_read_b32 v37,a133
v_accvgpr_read_b32 v36,a132
zaaa 112,68,16
dl 231,44,54,3072
zaaa 116,68,12
s_add_u32 m0,s61,4096
v_accvgpr_read_b32 v40,a136
v_accvgpr_read_b32 v44,a140
zaaa 132,64,12
s_add_u32 s54,s54,s57
cv 251,42,43
cv 250,40,41
zaaa 136,64,8
s_add_u32 s61,s63,s98
cv 253,46,47
cv 252,44,45
zaaa 120,68,8
s_add_u32 s63,s63,s99
cv 249,38,39
cv 247,34,35
zaaa 124,68,4
buffer_store_dwordx4 v[48:51],v244,s[24:27],0 offen offset:2048 sc0 sc1
zaaa 140,64,4
cv 246,32,33
cv 248,36,37
zvaa 40,72,4
ta 68,237,20480
cv 123,22,23
zvaa 44,76,4
ta 70,237,20992
cv 121,18,19
zvaa 52,76,8
ta 64,236,20480
cv 120,16,17
zvaa 48,72,8
dl 224,36,52,0
zvaa 56,72,12
ta 66,236,20992
cv 122,20,21
zvaa 60,76,12
cv 119,14,15
cv 118,12,13
zvaa 68,76,16
)"
R"(cv 117,10,11
cv 116,8,9
zvaa 64,72,16
cv 157,90,91
cv 156,88,89
zvaa 32,80,16
dl 225,36,52,1024
ta 76,235,24576
ta 78,235,25088
zvaa 36,84,16
ta 72,234,24576
ta 74,234,25088
zvaa 28,84,12
cv 153,82,83
cv 152,80,81
zvaa 24,80,12
cv 155,86,87
cv 154,84,85
zvaa 16,80,8
cv 149,74,75
cv 148,72,73
zvaa 20,84,8
dl 226,36,52,2048
zvaa 12,84,4
v_add_u32_e32 v232,s99,v232
cv 115,6,7
zvaa 8,80,4
cv 114,4,5
cv 113,2,3
zvaa 76,4,88
cv 112,0,1
zvaa 96,4,92
zvaa 100,8,92
dl 227,36,52,3072
zvaa 80,8,88
s_add_u32 s52,s52,s60
zvaa 84,12,88
zvaa 104,12,92
zvaa 108,16,92
s_waitcnt vmcnt(23) lgkmcnt(0)
s_barrier
zvaa 88,16,88
s_branch L_kvx
L_vtile:
s_waitcnt vmcnt(8)
s_barrier
ta 64,234,0
ta 66,234,512
ra 0,232,0
ta 68,235,0
ta 70,235,512
ra 4,232,2048
ra 8,232,4096
ra 12,232,6144
ra 16,232,8192
ra 20,232,10240
ra 24,232,12288
ra 28,232,14336
s_add_u32 m0,s62,32768
v_mov_b32_e32 v212,0
dl 228,40,53,0
v_mov_b32_e32 v213,0
v_mov_b32_e32 v214,0
v_mov_b32_e32 v215,0
v_mov_b32_e32 v220,0
v_mov_b32_e32 v221,0
v_mov_b32_e32 v222,0
v_mov_b32_e32 v223,0
v_mov_b32_e32 v216,0
v_mov_b32_e32 v217,0
v_mov_b32_e32 v218,0
v_mov_b32_e32 v219,0
v_mov_b32_e32 v208,0
v_mov_b32_e32 v209,0
v_mov_b32_e32 v210,0
v_mov_b32_e32 v211,0
v_mov_b32_e32 v148,0
s_waitcnt lgkmcnt(9)
zaaa 128,0,64
s_lshl_b32 s4,%1,1
ta 72,236,0
v_mov_b32_e32 v149,0
s_waitcnt lgkmcnt(8)
zaaa 136,0,68
s_add_u32 s4,s4,s12
ta 74,236,512
v_mov_b32_e32 v150,0
s_waitcnt lgkmcnt(8)
zaaa 140,4,68
s_mul_i32 s4,s4,1280
ta 76,237,0
ta 78,237,512
zaaa 132,4,64
dl 229,40,53,1024
s_waitcnt lgkmcnt(4)
zvaa 32,8,64
s_add_u32 s53,s53,s57
v_mov_b32_e32 v151,0
v_mov_b32_e32 v156,0
zvaa 40,8,68
s_mov_b32 s55,s4
v_mov_b32_e32 v157,0
v_mov_b32_e32 v158,0
zvaa 44,12,68
s_mul_i32 s4,%1,1792
ta 80,234,4096
v_mov_b32_e32 v159,0
zvaa 36,12,64
dl 230,44,54,2048
zvaa 96,16,64
s_lshl_b32 s88,%1,2
ta 82,234,4608
v_mov_b32_e32 v152,0
zvaa 104,16,68
s_cmp_eq_u32 s12,2
s_cselect_b32 s55,s4,s55
ta 84,235,4096
v_mov_b32_e32 v153,0
zvaa 108,20,68
s_lshl_b32 s4,%1,14
ta 86,235,4608
v_mov_b32_e32 v154,0
zvaa 100,20,64
dl 231,44,54,3072
zvaa 160,24,64
s_add_u32 m0,s98,36864
v_mov_b32_e32 v155,0
v_mov_b32_e32 v144,0
zvaa 168,24,68
s_add_u32 s54,s54,s57
v_mov_b32_e32 v145,0
v_mov_b32_e32 v146,0
zvaa 172,28,68
s_add_u32 s88,s88,s12
v_mov_b32_e32 v147,0
v_mov_b32_e32 v84,0
zvaa 164,28,64
dl 224,36,52,0
s_waitcnt lgkmcnt(4)
zvaa 180,28,72
s_add_u32 s5,s4,16384
v_mov_b32_e32 v85,0
v_mov_b32_e32 v86,0
zvaa 188,28,76
s_cmp_eq_u32 s12,3
s_cselect_b32 s55,s4,s55
ra 32,233,0
v_mov_b32_e32 v87,0
zvaa 184,24,76
s_mov_b32 s4,10
v_mov_b32_e32 v92,0
v_mov_b32_e32 v93,0
zvaa 176,24,72
dl 225,36,52,1024
zvaa 116,20,72
s_lshl_b32 s88,s88,12
ra 36,233,2048
v_mov_b32_e32 v94,0
zvaa 124,20,76
s_xor_b32 s87,s19,15
ra 40,233,4096
v_mov_b32_e32 v95,0
zvaa 120,16,76
s_cmp_eq_u32 s12,2
s_cselect_b32 s4,14,s4
v_mov_b32_e32 v88,0
v_mov_b32_e32 v89,0
zvaa 112,16,72
dl 226,36,52,2048
zvaa 52,12,72
s_waitcnt vmcnt(11)
s_barrier
zvaa 60,12,76
s_add_u32 s30,s68,s88
s_addc_u32 s31,s69,0
ta 96,234,16384
v_mov_b32_e32 v90,0
zvaa 56,8,76
s_lshl_b32 s22,s87,7
ta 98,234,16896
v_mov_b32_e32 v91,0
zvaa 48,8,72
dl 227,36,52,3072
zaaa 148,4,72
s_add_u32 m0,s62,49152
ta 100,235,16384
v_mov_b32_e32 v80,0
zaaa 156,4,76
s_add_u32 s52,s52,s60
ta 102,235,16896
v_mov_b32_e32 v81,0
zaaa 152,0,76
s_lshl_b32 s87,s87,2
ra 44,233,6144
v_mov_b32_e32 v82,0
zaaa 144,0,72
dl 228,40,53,0
s_waitcnt lgkmcnt(5)
zvaa 0,0,80
s_lshl_b32 s88,s19,2
ra 48,233,8192
v_mov_b32_e32 v83,0
zvaa 8,0,84
s_cmp_eq_u32 s12,3
s_cselect_b32 s4,10,s4
ra 52,233,10240
v_mov_b32_e32 v20,0
zvaa 12,4,84
s_add_u32 s87,s87,s12
ra 56,233,12288
v_mov_b32_e32 v21,0
zvaa 4,4,80
dl 229,40,53,1024
zvaa 64,8,80
s_add_u32 s88,s88,s12
v_mov_b32_e32 v22,0
v_mov_b32_e32 v23,0
zvaa 72,8,84
s_lshl_b32 s90,s18,13
ra 60,233,14336
v_mov_b32_e32 v28,0
zvaa 76,12,84
s_add_u32 s90,s90,62914560
ta 104,236,16384
v_mov_b32_e32 v29,0
zvaa 68,12,80
dl 230,44,54,2048
zvaa 128,16,80
s_mul_i32 s14,s87,327680
ta 106,236,16896
v_mov_b32_e32 v30,0
zvaa 136,16,84
s_mul_i32 s13,s88,327680
ta 108,237,16384
v_mov_b32_e32 v31,0
zvaa 140,20,84
s_add_u32 s14,s14,s90
ta 110,237,16896
v_mov_b32_e32 v24,0
zvaa 132,20,80
dl 231,44,54,3072
zvaa 192,24,80
s_add_u32 m0,s98,65536
v_mov_b32_e32 v25,0
v_mov_b32_e32 v26,0
zvaa 200,24,84
s_add_u32 s13,s13,s90
v_mov_b32_e32 v27,0
v_mov_b32_e32 v16,0
zvaa 204,28,84
s_mul_i32 s90,s18,524288
v_mov_b32_e32 v17,0
v_mov_b32_e32 v18,0
zvaa 196,28,80
dl 224,36,52,0
s_waitcnt lgkmcnt(4)
maaa 128,32,96
s_lshl_b32 s91,s12,12
ta 208,236,4096
v_mov_b32_e32 v19,0
maaa 136,32,100
s_add_u32 s90,s90,s91
ta 210,236,4608
v_lshlrev_b32_e32 v241,7,v247
maaa 140,36,100
s_add_u32 s90,s90,16777216
ta 216,237,4096
v_mov_b32_e32 v242,134217728
maaa 132,36,96
dl 225,36,52,1024
mvaa 32,40,96
s_cmp_eq_u32 s12,3
s_cselect_b64 s[26:27],-1,0
ta 218,237,4608
v_lshrrev_b32_e32 v248,3,v247
mvaa 40,40,100
s_lshl_b32 s86,s19,1
v_and_b32_e32 v250,112,v244
v_and_b32_e32 v251,16,v247
mvaa 44,44,100
s_mov_b64 s[48:49],s[72:73]
v_lshlrev_b32_e32 v251,1,v251
v_cmp_gt_u32_e64 s[24:25],s4,v247
mvaa 36,44,96
dl 226,36,52,2048
mvaa 96,48,96
s_mov_b32 s50,8388608
v_mul_u32_u24_e32 v246,6144,v248
v_add_u32_e32 v249,s90,v244
mvaa 104,48,100
s_mov_b32 s59,655360
ta 112,234,20480
v_cndmask_b32_e64 v241,v242,v241,s[24:25]
mvaa 108,52,100
s_cmp_eq_u32 s12,2
s_cselect_b64 s[24:25],-1,0
ta 114,234,20992
v_add_u32_e32 v255,16777216,v249
mvaa 100,52,96
dl 227,36,52,3072
mvaa 160,56,96
s_add_u32 s52,s52,s56
s_add_u32 m0,s98,69632
ta 116,235,20480
mvaa 168,56,100
s_add_u32 s86,s86,2
ta 118,235,20992
v_accvgpr_write_b32 a240,v249
mvaa 172,60,100
s_cmp_eq_u32 s12,2
s_cselect_b64 s[48:49],s[80:81],s[48:49]
v_and_b32_e32 v249,48,v247
v_accvgpr_write_b32 a241,v255
mvaa 164,60,96
dl 224,36,52,0
s_waitcnt lgkmcnt(4)
mvaa 180,60,104
s_cmp_eq_u32 s12,2
s_cselect_b32 s50,6291456,s50
ta 212,236,20480
v_xor_b32_e32 v249,v249,v250
mvaa 188,60,108
s_cmp_eq_u32 s12,2
s_cselect_b32 s59,458752,s59
v_xor_b32_e32 v250,v250,v251
v_lshl_add_u32 v252,v248,11,v249
mvaa 184,56,108
s_lshl_b32 s15,1,s86
v_and_b32_e32 v251,48,v250
v_xor_b32_e32 v253,64,v252
mvaa 176,56,104
dl 225,36,52,1024
mvaa 116,52,104
s_lshl_b32 s23,s19,7
v_accvgpr_write_b32 a242,v252
v_add_u32_e32 v246,v246,v251
mvaa 124,52,108
s_or_b32 s86,%1,s12
s_cselect_b32 s65,0,4
v_add_u32_e32 v242,15360,v253
v_and_b32_e32 v251,64,v250
mvaa 120,48,108
s_cmp_eq_u32 s12,3
s_cselect_b64 s[48:49],s[68:69],s[48:49]
v_lshl_add_u32 v248,v251,5,v246
v_xor_b32_e32 v249,64,v251
mvaa 112,48,104
dl 226,36,52,2048
mvaa 52,44,104
s_cmp_eq_u32 s12,3
s_cselect_b32 s50,s5,s50
v_accvgpr_write_b32 a243,v242
v_mul_u32_u24_e32 v251,768,v251
mvaa 60,44,108
s_cmp_eq_u32 s12,3
s_cselect_b32 s59,1280,s59
v_add_u32_e32 v242,30720,v252
v_lshl_add_u32 v249,v249,5,v246
mvaa 56,40,108
s_add_u32 s53,s53,s58
v_add_u32_e32 v246,v246,v251
v_accvgpr_write_b32 a244,v242
mvaa 48,40,104
dl 227,36,52,3072
maaa 148,36,104
s_add_u32 s54,s54,s58
v_add_u32_e32 v242,46080,v253
v_accvgpr_write_b32 a245,v242
maaa 156,36,108
s_add_u32 s52,s52,s60
v_add_u32_e32 v243,4096,v246
v_add_u32_e32 v242,0,v248
maaa 152,32,108
s_mov_b32 s63,32768
ta 214,236,20992
v_cndmask_b32_e64 v242,v228,v242,s[24:25]
maaa 144,32,104
s_mov_b32 s61,s98
ta 220,237,20480
v_cndmask_b32_e64 v242,v242,v243,s[26:27]
s_waitcnt lgkmcnt(2)
mvaa 0,32,112
s_mov_b32 s64,1
v_add_u32_e32 v243,101376,v246
v_and_b32_e32 v246,15,v247
mvaa 8,32,116
v_and_b32_e32 v246,12,v247
v_accvgpr_write_b32 a246,v242
mvaa 12,36,116
ta 222,237,20992
v_add_u32_e32 v242,48128,v249
mvaa 4,36,112
v_cndmask_b32_e64 v242,v229,v242,s[24:25]
v_lshlrev_b32_e32 v246,5,v246
mvaa 64,40,112
v_cndmask_b32_e64 v242,v242,v243,s[26:27]
v_accvgpr_write_b32 a247,v242
mvaa 72,40,116
v_add_u32_e32 v242,96256,v248
v_and_b32_e32 v248,3,v247
mvaa 76,44,116
v_cndmask_b32_e64 v242,v230,v242,s[24:25]
v_lshl_add_u32 v246,v248,3,v246
mvaa 68,44,112
v_and_b32_e32 v248,24,v247
v_accvgpr_write_b32 a248,v242
mvaa 128,48,112
v_add_u32_e32 v242,144384,v249
v_lshl_add_u32 v246,v248,2,v246
mvaa 136,48,116
v_and_b32_e32 v248,32,v247
v_cndmask_b32_e64 v242,v231,v242,s[24:25]
mvaa 140,52,116
v_add_u32_e32 v246,98304,v246
v_accvgpr_write_b32 a249,v242
mvaa 132,52,112
v_lshl_add_u32 v238,v248,5,v246
v_xor_b32_e32 v239,32,v238
mvaa 192,56,112
v_add_u32_e32 v232,32768,v232
v_add_u32_e32 v233,32768,v233
mvaa 200,56,116
mvaa 204,60,116
mvaa 196,60,112
s_add_u32 s4,s96,L_vrv-L_att_pc
s_addc_u32 s5,s97,0
s_mov_b64 exec,0
s_cmp_eq_u32 s12,0
s_cbranch_scc1 L_vs4
s_cmp_eq_u32 s12,1
s_cbranch_scc1 L_vs5
s_cmp_eq_u32 s12,2
s_cbranch_scc1 L_vs6
s_branch L_vs7
L_vrv:
s_mov_b64 exec,-1
s_waitcnt vmcnt(8)
s_waitcnt lgkmcnt(0)
s_barrier
L_v_head:
mvaa 212,28,208
ra 64,232,0
mvaa 212,60,212
s_add_u32 m0,s62,0
ta 192,234,32768
mvaa 220,60,220
dl 228,40,53,0
mvaa 220,28,216
ta 194,234,33280
s_nop 3
mvaa 216,24,216
ra 96,233,0
mvaa 216,56,220
ta 196,234,49152
mvaa 208,56,212
L_vs4:
s_cbranch_execz L_vs8
ta 198,234,49664
mvaa 208,24,208
ta 204,235,49152
mvaa 148,20,208
dl 229,40,53,1024
mvaa 148,52,212
s_add_u32 s53,s53,s57
ta 206,235,49664
mvaa 156,52,220
ta 200,235,32768
mvaa 156,20,216
ta 202,235,33280
mvaa 152,16,216
ra 68,232,2048
mvaa 152,48,220
ra 100,233,2048
mvaa 144,48,212
ra 72,232,4096
mvaa 144,16,208
dl 230,44,54,2048
mvaa 84,12,208
ra 104,233,4096
mvaa 84,44,212
ra 76,232,6144
mvaa 92,44,220
ra 108,233,6144
mvaa 92,12,216
ra 80,232,8192
mvaa 88,8,216
ra 112,233,8192
mvaa 88,40,220
dl 231,44,54,3072
mvaa 80,40,212
s_add_u32 s54,s54,s57
ra 84,232,10240
mvaa 80,8,208
ra 116,233,10240
mvaa 20,4,208
ra 88,232,12288
mvaa 20,36,212
ra 120,233,12288
mvaa 28,36,220
ra 92,232,14336
mvaa 28,4,216
s_add_u32 m0,s62,16384
ra 124,233,14336
mvaa 24,0,216
dl 228,40,53,0
mvaa 24,32,220
mvaa 16,32,212
mvaa 16,0,208
s_waitcnt lgkmcnt(8)
maaa 128,64,192
maaa 128,96,196
maaa 136,96,204
dl 229,40,53,1024
maaa 136,64,200
s_add_u32 s53,s53,s58
maaa 140,68,200
maaa 140,100,204
maaa 132,100,196
maaa 132,68,192
mvaa 32,72,192
mvaa 32,104,196
dl 230,44,54,2048
mvaa 40,104,204
mvaa 40,72,200
s_waitcnt lgkmcnt(0)
mvaa 44,76,200
ta 208,236,32768
mvaa 44,108,204
ta 210,236,33280
mvaa 36,108,196
ta 212,236,49152
mvaa 36,76,192
dl 231,44,54,3072
mvaa 96,80,192
s_add_u32 s54,s54,s58
ta 214,236,49664
mvaa 96,112,196
ta 220,237,49152
mvaa 104,112,204
ta 222,237,49664
mvaa 104,80,200
ta 216,237,32768
mvaa 108,84,200
ta 218,237,33280
mvaa 108,116,204
mvaa 100,116,196
s_add_u32 m0,s61,0
mvaa 100,84,192
dl 224,36,52,0
mvaa 160,88,192
mvaa 160,120,196
mvaa 168,120,204
mvaa 168,88,200
mvaa 172,92,200
mvaa 172,124,204
mvaa 164,124,196
dl 225,36,52,1024
mvaa 164,92,192
s_waitcnt lgkmcnt(0)
mvaa 180,92,208
mvaa 180,124,212
mvaa 188,124,220
ta 192,234,36864
mvaa 188,92,216
ta 194,234,37376
mvaa 184,88,216
ta 196,234,53248
mvaa 184,120,220
dl 226,36,52,2048
mvaa 176,120,212
ta 198,234,53760
mvaa 176,88,208
ta 204,235,53248
mvaa 116,84,208
ta 206,235,53760
mvaa 116,116,212
ta 200,235,36864
mvaa 124,116,220
ta 202,235,37376
mvaa 124,84,216
mvaa 120,80,216
dl 227,36,52,3072
mvaa 120,112,220
s_add_u32 s52,s52,s56
mvaa 112,112,212
mvaa 112,80,208
mvaa 52,76,208
mvaa 52,108,212
mvaa 60,108,220
mvaa 60,76,216
mvaa 56,72,216
s_add_u32 m0,s61,4096
mvaa 56,104,220
dl 224,36,52,0
mvaa 48,104,212
s_add_u32 s61,s63,s98
mvaa 48,72,208
s_cmp_ge_u32 s63,65536
s_cselect_b32 s99,-2,1
s_lshl_b32 s99,s99,15
maaa 148,68,208
s_add_u32 s63,s63,s99
v_add_u32_e32 v232,s99,v232
v_add_u32_e32 v233,s99,v233
maaa 148,100,212
maaa 156,100,220
maaa 156,68,216
maaa 152,64,216
dl 225,36,52,1024
maaa 152,96,220
maaa 144,96,212
s_waitcnt lgkmcnt(0)
maaa 144,64,208
mvaa 0,64,192
mvaa 0,96,196
mvaa 8,96,204
ta 208,236,36864
mvaa 8,64,200
ta 210,236,37376
mvaa 12,68,200
dl 226,36,52,2048
mvaa 12,100,204
ta 212,236,53248
mvaa 4,100,196
ta 214,236,53760
mvaa 4,68,192
ta 220,237,53248
mvaa 64,72,192
ta 222,237,53760
mvaa 64,104,196
ta 216,237,36864
mvaa 72,104,204
ta 218,237,37376
mvaa 72,72,200
dl 227,36,52,3072
mvaa 76,76,200
s_add_u32 s52,s52,s60
mvaa 76,108,204
mvaa 68,108,196
mvaa 68,76,192
mvaa 128,80,192
mvaa 128,112,196
buffer_load_dword a184,v241,s[48:51],s55 offen
mvaa 136,112,204
s_add_u32 s55,s55,s59
mvaa 136,80,200
mvaa 140,84,200
mvaa 140,116,204
mvaa 132,116,196
mvaa 132,84,192
buffer_load_dword a185,v241,s[48:51],s55 offen
mvaa 192,88,192
s_add_u32 s55,s55,s59
mvaa 192,120,196
mvaa 200,120,204
mvaa 200,88,200
mvaa 204,92,200
mvaa 204,124,204
s_cmp_eq_u32 s64,3
mvaa 196,124,196
s_waitcnt vmcnt(10) lgkmcnt(0)
s_barrier
mvaa 196,92,192
s_cbranch_scc1 L_v_exit
mvaa 212,92,208
ra 0,232,0
mvaa 212,124,212
s_add_u32 m0,s62,32768
ta 192,234,0
mvaa 220,124,220
dl 228,40,53,0
mvaa 220,92,216
ta 194,234,512
s_nop 3
mvaa 216,88,216
ra 32,233,0
mvaa 216,120,220
ta 196,234,16384
mvaa 208,120,212
ta 198,234,16896
mvaa 208,88,208
ta 204,235,16384
mvaa 148,84,208
dl 229,40,53,1024
mvaa 148,116,212
s_add_u32 s53,s53,s57
ta 206,235,16896
mvaa 156,116,220
ta 200,235,0
mvaa 156,84,216
ta 202,235,512
mvaa 152,80,216
ra 4,232,2048
mvaa 152,112,220
ra 36,233,2048
mvaa 144,112,212
ra 8,232,4096
mvaa 144,80,208
dl 230,44,54,2048
mvaa 84,76,208
ra 40,233,4096
mvaa 84,108,212
ra 12,232,6144
mvaa 92,108,220
ra 44,233,6144
mvaa 92,76,216
ra 16,232,8192
mvaa 88,72,216
ra 48,233,8192
mvaa 88,104,220
dl 231,44,54,3072
mvaa 80,104,212
s_add_u32 s54,s54,s57
ra 20,232,10240
mvaa 80,72,208
ra 52,233,10240
mvaa 20,68,208
ra 24,232,12288
mvaa 20,100,212
ra 56,233,12288
mvaa 28,100,220
ra 28,232,14336
mvaa 28,68,216
s_add_u32 m0,s62,49152
ra 60,233,14336
mvaa 24,64,216
dl 228,40,53,0
mvaa 24,96,220
mvaa 16,96,212
mvaa 16,64,208
s_waitcnt lgkmcnt(8)
maaa 128,0,192
maaa 128,32,196
maaa 136,32,204
dl 229,40,53,1024
maaa 136,0,200
s_add_u32 s53,s53,s58
maaa 140,4,200
maaa 140,36,204
maaa 132,36,196
maaa 132,4,192
mvaa 32,8,192
mvaa 32,40,196
dl 230,44,54,2048
mvaa 40,40,204
mvaa 40,8,200
s_waitcnt lgkmcnt(0)
mvaa 44,12,200
ta 208,236,0
mvaa 44,44,204
ta 210,236,512
mvaa 36,44,196
ta 212,236,16384
mvaa 36,12,192
dl 231,44,54,3072
mvaa 96,16,192
s_add_u32 s54,s54,s58
ta 214,236,16896
mvaa 96,48,196
ta 220,237,16384
mvaa 104,48,204
ta 222,237,16896
mvaa 104,16,200
ta 216,237,0
mvaa 108,20,200
ta 218,237,512
mvaa 108,52,204
mvaa 100,52,196
s_add_u32 m0,s61,0
mvaa 100,20,192
dl 224,36,52,0
mvaa 160,24,192
s_lshl_b32 s86,s21,2
s_add_u32 s86,s86,s12
s_mul_i32 s86,s86,384
mvaa 160,56,196
s_add_u32 s86,s86,4096
v_lshl_add_u32 v253,v244,3,s86
mvaa 168,56,204
s_mov_b64 exec,7
global_load_dword a188,v253,s[96:97]
s_mov_b64 exec,-1
mvaa 168,24,200
mvaa 172,28,200
mvaa 172,60,204
mvaa 164,60,196
dl 225,36,52,1024
mvaa 164,28,192
s_waitcnt lgkmcnt(0)
mvaa 180,28,208
mvaa 180,60,212
mvaa 188,60,220
ta 192,234,4096
mvaa 188,28,216
ta 194,234,4608
mvaa 184,24,216
ta 196,234,20480
mvaa 184,56,220
dl 226,36,52,2048
mvaa 176,56,212
ta 198,234,20992
mvaa 176,24,208
ta 204,235,20480
mvaa 116,20,208
ta 206,235,20992
mvaa 116,52,212
ta 200,235,4096
mvaa 124,52,220
ta 202,235,4608
mvaa 124,20,216
mvaa 120,16,216
dl 227,36,52,3072
mvaa 120,48,220
s_add_u32 s52,s52,s56
mvaa 112,48,212
mvaa 112,16,208
mvaa 52,12,208
mvaa 52,44,212
s_bitcmp1_b32 s65,s64
s_cbranch_scc1 L_v_ev
L_v_evr:
mvaa 60,44,220
mvaa 60,12,216
mvaa 56,8,216
s_add_u32 m0,s61,4096
mvaa 56,40,220
dl 224,36,52,0
mvaa 48,40,212
s_add_u32 s61,s63,s98
mvaa 48,8,208
s_cmp_ge_u32 s63,65536
s_cselect_b32 s99,-2,1
s_lshl_b32 s99,s99,15
maaa 148,4,208
s_add_u32 s63,s63,s99
v_add_u32_e32 v232,s99,v232
v_add_u32_e32 v233,s99,v233
maaa 148,36,212
maaa 156,36,220
maaa 156,4,216
maaa 152,0,216
dl 225,36,52,1024
maaa 152,32,220
maaa 144,32,212
s_waitcnt lgkmcnt(0)
maaa 144,0,208
mvaa 0,0,192
mvaa 0,32,196
mvaa 8,32,204
ta 208,236,4096
mvaa 8,0,200
ta 210,236,4608
mvaa 12,4,200
dl 226,36,52,2048
mvaa 12,36,204
ta 212,236,20480
mvaa 4,36,196
ta 214,236,20992
mvaa 4,4,192
ta 220,237,20480
mvaa 64,8,192
ta 222,237,20992
mvaa 64,40,196
ta 216,237,4096
mvaa 72,40,204
ta 218,237,4608
mvaa 72,8,200
dl 227,36,52,3072
mvaa 76,12,200
s_add_u32 s52,s52,s60
mvaa 76,44,204
mvaa 68,44,196
mvaa 68,12,192
mvaa 128,16,192
mvaa 128,48,196
buffer_load_dword a187,v241,s[48:51],s55 offen
mvaa 136,48,204
s_add_u32 s55,s55,s59
mvaa 136,16,200
mvaa 140,20,200
mvaa 140,52,204
mvaa 132,52,196
mvaa 132,20,192
buffer_load_dword a190,v241,s[48:51],s55 offen
mvaa 192,24,192
s_add_u32 s55,s55,s59
mvaa 192,56,196
mvaa 200,56,204
s_add_u32 s64,s64,1
mvaa 200,24,200
mvaa 204,28,200
mvaa 204,60,204
mvaa 196,60,196
s_waitcnt vmcnt(10) lgkmcnt(0)
s_barrier
mvaa 196,28,192
s_branch L_v_head
L_v_exit:
global_load_dwordx4 a[160:163],v244,s[30:31] offset:0
s_add_u32 m0,s62,32768
mvaa 212,92,208
s_cmp_lt_u32 s12,2
s_cselect_b32 s86,s22,s23
ta 32,234,0
s_and_b32 s87,s12,1
mvaa 212,124,212
s_lshl_b32 s86,s86,11
ta 34,234,512
s_lshl_b32 s87,s87,17
mvaa 220,124,220
s_add_u32 s86,s86,s87
ra 0,232,0
s_mov_b64 s[36:37],s[68:69]
mvaa 220,92,216
s_mov_b32 s38,4194304
ta 36,235,0
s_mov_b32 s52,s86
mvaa 216,88,216
s_mov_b32 s56,65536
ta 38,235,512
v_accvgpr_read_b32 v224,a242
mvaa 216,120,220
dl 228,40,53,0
mvaa 208,120,212
s_lshl_b32 s88,s18,1
ra 4,232,2048
v_accvgpr_read_b32 v225,a243
mvaa 208,88,208
s_add_u32 s88,s88,s12
ra 8,232,4096
v_accvgpr_read_b32 v226,a244
mvaa 148,84,208
s_mov_b32 s91,196608
ra 12,232,6144
v_accvgpr_read_b32 v227,a245
mvaa 148,116,212
s_lshl_b32 s88,s88,7
ra 16,232,8192
s_lshl_b32 s89,s18,6
mvaa 156,116,220
s_mov_b32 s90,6291456
ra 20,232,10240
s_mov_b32 s60,-65408
mvaa 156,84,216
dl 229,40,53,1024
mvaa 152,80,216
s_add_u32 s53,s53,s57
ra 24,232,12288
s_bfe_u32 s4,s21,65536
mvaa 152,112,220
s_lshl_b32 s4,s4,6
ra 28,232,14336
s_lshl_b32 s87,s20,2
mvaa 144,112,212
s_mov_b32 s26,32768
ta 40,236,0
s_mov_b32 s27,131072
mvaa 144,80,208
s_add_u32 s61,s98,32768
ta 42,236,512
s_mov_b32 s63,65536
mvaa 84,76,208
s_cmp_ge_u32 s63,65536
s_cselect_b32 s99,-2,1
ta 44,237,0
s_lshl_b32 s99,s99,15
mvaa 84,108,212
dl 230,44,54,2048
mvaa 92,108,220
s_cmp_eq_u32 s12,0
s_cselect_b32 s65,96,0
ta 46,237,512
s_mov_b32 s67,2048
mvaa 92,76,216
s_mov_b32 s66,8
ta 48,234,4096
s_mov_b32 s64,1
mvaa 88,72,216
s_or_b32 s65,s65,128
ta 50,234,4608
mvaa 88,104,220
s_bitcmp1_b32 s12,0
s_cselect_b64 vcc,-1,0
ta 52,235,4096
mvaa 80,104,212
ta 54,235,4608
mvaa 80,72,208
dl 231,44,54,3072
s_add_u32 m0,s62,49152
mvaa 20,68,208
s_add_u32 s54,s54,s57
ta 56,236,4096
mvaa 20,100,212
s_cmp_lt_u32 s12,2
s_cselect_b32 s57,262144,s91
ta 58,236,4608
mvaa 28,100,220
ta 60,237,4096
mvaa 28,68,216
ta 62,237,4608
)"
R"(mvaa 24,64,216
mvaa 24,96,220
dl 228,40,53,0
mvaa 16,96,212
v_accvgpr_read_b32 v228,a246
mvaa 16,64,208
s_waitcnt lgkmcnt(5)
maaa 128,0,32
L_vs5:
s_cbranch_execz L_vs9
maaa 136,0,36
maaa 140,4,36
dl 229,40,53,1024
maaa 132,4,32
s_waitcnt lgkmcnt(0)
s_barrier
mvaa 32,8,32
s_add_u32 s53,s53,s58
ta 96,234,16384
v_accvgpr_read_b32 v229,a247
mvaa 40,8,36
s_cmp_lt_u32 s12,2
s_cselect_b32 s42,8388608,s90
ta 98,234,16896
mvaa 44,12,36
s_cmp_lt_u32 s12,2
s_cselect_b32 s53,s88,s89
ra 64,233,0
s_and_b32 s89,s98,8192
mvaa 36,12,32
dl 230,44,54,2048
mvaa 96,16,32
s_bitcmp1_b32 s21,0
s_cselect_b32 s90,s57,0
ta 100,235,16384
s_add_u32 s53,s53,s90
mvaa 104,16,36
s_cmp_lt_u32 s12,2
s_cselect_b64 s[40:41],s[72:73],s[80:81]
ta 102,235,16896
v_accvgpr_read_b32 v230,a248
mvaa 108,20,36
s_lshl_b32 s90,s90,1
ra 68,233,2048
s_add_u32 s91,s89,65536
mvaa 100,20,32
s_bfe_u32 s88,s21,65539
ra 72,233,4096
s_lshl_b32 s88,s88,1
mvaa 160,24,32
dl 231,44,54,3072
mvaa 168,24,36
s_add_u32 m0,s98,65536
ra 76,233,6144
s_add_u32 s54,s54,s58
mvaa 172,28,36
s_cmp_eq_u32 s12,3
s_cselect_b64 s[44:45],s[68:69],s[40:41]
ra 80,233,8192
v_accvgpr_read_b32 v231,a249
mvaa 164,28,32
s_cmp_eq_u32 s12,3
s_cselect_b32 s46,0,s42
ra 84,233,10240
s_add_u32 s58,s57,s90
mvaa 180,28,40
s_cmp_eq_u32 s12,3
s_cselect_b32 s54,0,s53
ra 88,233,12288
s_sub_u32 s57,s57,s90
mvaa 188,28,44
global_load_dwordx4 a[168:171],v244,s[30:31] offset:2048
mvaa 184,24,44
s_add_u32 s90,s89,65536
ta 104,236,16384
s_add_u32 s88,s88,s87
mvaa 176,24,40
s_and_b32 s87,s12,1
ra 92,233,14336
s_add_u32 s88,s88,s87
mvaa 116,20,40
s_mul_i32 s88,s88,524288
ta 106,236,16896
s_and_b32 s87,s21,7
mvaa 124,20,44
s_add_u32 s86,s88,33554432
ta 108,237,16384
s_lshl_b32 s87,s87,16
mvaa 120,16,44
global_load_dwordx4 a[176:179],v244,s[84:85] offset:0
mvaa 112,16,40
s_add_u32 s86,s86,s87
ta 110,237,16896
s_lshr_b32 s87,s12,1
mvaa 52,12,40
s_lshl_b32 s87,s87,15
ta 112,234,20480
s_add_u32 s86,s86,s87
mvaa 60,12,44
s_add_u32 s24,s8,s86
s_addc_u32 s25,s9,0
ta 114,234,20992
s_lshl_b32 s86,%1,5
mvaa 56,8,44
s_lshl_b32 s87,s12,3
ta 116,235,20480
s_add_u32 s86,s86,s87
mvaa 48,8,40
global_load_dwordx4 a[164:167],v244,s[30:31] offset:1024
maaa 148,4,40
s_add_u32 s86,s86,83886080
ta 118,235,20992
s_mov_b32 s88,4096
maaa 156,4,44
ta 120,236,20480
maaa 152,0,44
ta 122,236,20992
maaa 144,0,40
ta 124,237,20480
mvaa 0,0,48
global_load_dwordx4 a[172:175],v244,s[30:31] offset:3072
mvaa 8,0,52
s_mov_b32 s30,1048577
ta 126,237,20992
s_mov_b32 s31,268435712
mvaa 12,4,52
mvaa 4,4,48
mvaa 64,8,48
mvaa 72,8,52
global_load_dwordx4 a[180:183],v244,s[84:85] offset:1024
mvaa 76,12,52
mvaa 68,12,48
mvaa 128,16,48
mvaa 136,16,52
mvaa 140,20,52
dl 224,36,52,0
mvaa 132,20,48
mvaa 192,24,48
mvaa 200,24,52
mvaa 204,28,52
mvaa 196,28,48
dl 225,36,52,1024
mvaa 212,28,56
mvaa 220,28,60
mvaa 216,24,60
mvaa 208,24,56
mvaa 148,20,56
dl 226,36,52,2048
mvaa 156,20,60
mvaa 152,16,60
mvaa 144,16,56
mvaa 84,12,56
s_waitcnt vmcnt(12) lgkmcnt(0)
s_barrier
mvaa 92,12,60
dl 227,36,52,3072
mvaa 88,8,60
s_add_u32 s52,s52,s56
s_add_u32 m0,s98,69632
ta 32,234,32768
mvaa 80,8,56
ta 34,234,33280
mvaa 20,4,56
ta 36,235,32768
mvaa 28,4,60
ta 38,235,33280
mvaa 24,0,60
dl 224,36,52,0
mvaa 16,0,56
ta 40,236,32768
maaa 128,64,96
ta 42,236,33280
maaa 136,64,100
ta 44,237,32768
maaa 140,68,100
ta 46,237,33280
maaa 132,68,96
dl 225,36,52,1024
mvaa 32,72,96
ta 48,234,36864
mvaa 40,72,100
ta 50,234,37376
mvaa 44,76,100
ta 52,235,36864
mvaa 36,76,96
ta 54,235,37376
mvaa 96,80,96
dl 226,36,52,2048
mvaa 104,80,100
ra 20,232,43008
mvaa 108,84,100
ra 0,232,32768
mvaa 100,84,96
ra 24,232,45056
mvaa 160,88,96
ra 4,232,34816
mvaa 168,88,100
dl 227,36,52,3072
mvaa 172,92,100
s_add_u32 m0,s62,0
ra 28,232,47104
s_add_u32 s52,s52,s60
mvaa 164,92,96
ra 8,232,36864
mvaa 180,92,104
ra 12,232,38912
mvaa 188,92,108
ra 16,232,40960
v_add_u32_e32 v232,s90,v232
mvaa 184,88,108
dl 228,40,53,0
mvaa 176,88,104
ta 60,237,36864
v_xor_b32_e32 v232,s4,v232
mvaa 116,84,104
ta 62,237,37376
mvaa 124,84,108
ta 56,236,36864
mvaa 120,80,108
ta 58,236,37376
mvaa 112,80,104
dl 229,40,53,1024
mvaa 52,76,104
s_add_u32 s53,s53,s57
mvaa 60,76,108
mvaa 56,72,108
mvaa 48,72,104
maaa 148,68,104
dl 230,44,54,2048
maaa 156,68,108
maaa 152,64,108
maaa 144,64,104
mvaa 0,64,112
mvaa 8,64,116
dl 231,44,54,3072
mvaa 12,68,116
s_add_u32 m0,s62,16384
s_add_u32 s54,s54,s57
mvaa 4,68,112
mvaa 64,72,112
mvaa 72,72,116
mvaa 76,76,116
dl 228,40,53,0
mvaa 68,76,112
mvaa 128,80,112
mvaa 136,80,116
mvaa 140,84,116
mvaa 132,84,112
dl 229,40,53,1024
mvaa 192,88,112
s_add_u32 s53,s53,s58
mvaa 200,88,116
mvaa 204,92,116
mvaa 196,92,112
mvaa 212,92,120
dl 230,44,54,2048
mvaa 220,92,124
mvaa 216,88,124
s_waitcnt vmcnt(20) lgkmcnt(0)
s_barrier
mvaa 208,88,120
ra 92,233,47104
mvaa 148,84,120
ta 96,234,49152
mvaa 156,84,124
dl 231,44,54,3072
mvaa 152,80,124
s_add_u32 m0,s98,0
ra 88,233,45056
s_add_u32 s54,s54,s58
mvaa 144,80,120
ta 98,234,49664
mvaa 84,76,120
ta 100,235,49152
mvaa 92,76,124
ta 116,235,53248
mvaa 88,72,124
dl 224,36,52,0
mvaa 80,72,120
ta 118,235,53760
mvaa 20,68,120
ta 102,235,49664
v_subrev_u32_e32 v235,s89,v235
mvaa 28,68,124
ta 104,236,49152
mvaa 24,64,124
ta 112,234,53248
mvaa 16,64,120
dl 225,36,52,1024
maaa 128,0,32
ta 124,237,53248
maaa 136,0,36
ta 126,237,53760
maaa 140,4,36
ta 120,236,53248
maaa 132,4,32
ta 122,236,53760
mvaa 32,8,32
dl 226,36,52,2048
mvaa 40,8,36
ta 114,234,53760
v_subrev_u32_e32 v234,s89,v234
mvaa 44,12,36
ta 106,236,49664
v_subrev_u32_e32 v236,s89,v236
mvaa 36,12,32
ta 108,237,49152
mvaa 52,12,40
ta 110,237,49664
v_subrev_u32_e32 v237,s89,v237
mvaa 60,12,44
dl 227,36,52,3072
mvaa 56,8,44
s_add_u32 s52,s52,s56
s_add_u32 m0,s98,4096
ra 84,233,43008
mvaa 48,8,40
ra 80,233,40960
maaa 148,4,40
ra 72,233,36864
maaa 156,4,44
ra 76,233,38912
maaa 152,0,44
dl 224,36,52,0
maaa 144,0,40
ra 68,233,34816
mvaa 0,0,48
ra 64,233,32768
v_add_u32_e32 v233,s91,v233
mvaa 8,0,52
v_xor_b32_e32 v233,s4,v233
mvaa 12,4,52
mvaa 4,4,48
dl 225,36,52,1024
mvaa 20,4,56
mvaa 28,4,60
mvaa 24,0,60
mvaa 16,0,56
mvaa 220,28,60
dl 226,36,52,2048
s_waitcnt lgkmcnt(2)
mvaa 220,92,124
mvaa 216,88,124
mvaa 216,24,60
mvaa 212,28,56
mvaa 212,92,120
dl 227,36,52,3072
mvaa 208,88,120
s_add_u32 m0,s62,32768
s_add_u32 s52,s52,s60
s_waitcnt vmcnt(25)
zvaa 246,160,176
mvaa 208,24,56
cv 223,222,223
cv 222,220,221
mvaa 204,28,52
cv 221,218,219
zvaa 250,168,176
mvaa 204,92,116
cv 220,216,217
cv 219,214,215
mvaa 200,88,116
dl 228,40,53,0
s_waitcnt vmcnt(25)
mvaa 246,164,180
mvaa 200,24,52
cv 218,212,213
mvaa 250,172,180
mvaa 196,28,48
cv 217,210,211
cv 216,208,209
mvaa 196,92,112
cv 215,206,207
v_cndmask_b32_e64 v246,0,v246,s[30:31]
mvaa 192,88,112
cv 214,204,205
v_cndmask_b32_e64 v250,0,v250,s[30:31]
mvaa 192,24,48
dl 229,40,53,1024
s_lshl_b64 s[30:31],s[30:31],1
v_cndmask_b32_e64 v246,v246,v247,s[30:31]
v_cndmask_b32_e64 v250,v250,v251,s[30:31]
mvaa 188,28,44
s_lshl_b64 s[30:31],s[30:31],1
s_add_u32 s53,s53,s57
v_cndmask_b32_e64 v246,v246,v248,s[30:31]
v_cndmask_b32_e64 v250,v250,v252,s[30:31]
mvaa 188,92,108
s_lshl_b64 s[30:31],s[30:31],1
cv 213,202,203
v_cndmask_b32_e64 v246,v246,v249,s[30:31]
mvaa 184,88,108
v_cndmask_b32_e64 v250,v250,v253,s[30:31]
v_add_f32_dpp v246,v246,v246 quad_perm:[1,0,3,2] row_mask:0xf bank_mask:0xf
mvaa 184,24,44
v_add_f32_dpp v250,v250,v250 quad_perm:[1,0,3,2] row_mask:0xf bank_mask:0xf
v_add_f32_dpp v246,v246,v246 quad_perm:[2,3,0,1] row_mask:0xf bank_mask:0xf
mvaa 180,28,40
dl 230,44,54,2048
v_add_f32_dpp v250,v250,v250 quad_perm:[2,3,0,1] row_mask:0xf bank_mask:0xf
mvaa 180,92,104
v_add_f32_dpp v246,v246,v246 row_half_mirror row_mask:0xf bank_mask:0xf
v_add_f32_dpp v250,v250,v250 row_half_mirror row_mask:0xf bank_mask:0xf
mvaa 176,88,104
v_add_f32_dpp v246,v246,v246 row_mirror row_mask:0xf bank_mask:0xf
v_add_f32_dpp v250,v250,v250 row_mirror row_mask:0xf bank_mask:0xf
mvaa 176,24,40
cv 212,200,201
cv 211,198,199
mvaa 172,28,36
v_permlane16_swap_b32_e32 v246,v250
mvaa 172,92,100
dl 231,44,54,3072
v_add_f32_e32 v246,v246,v250
v_mov_b32_e32 v250,v246
mvaa 168,88,100
s_add_u32 s54,s54,s57
v_permlane32_swap_b32_e32 v246,v250
mvaa 168,24,36
v_add_f32_e32 v246,v246,v250
v_lshrrev_b32_e32 v250,6,v244
mvaa 164,28,32
v_mul_f32_e32 v246,0xbfb8aa3b,v246
cv 210,196,197
mvaa 164,92,96
ex 246
mvaa 160,88,96
buffer_load_dword a242,v241,s[48:51],s55 offen
mvaa 160,24,32
s_add_u32 s55,s55,s59
v_add_f32_e32 v246,1.0,v246
cv 209,194,195
mvaa 156,20,60
v_rcp_f32_e32 v246,v246
mvaa 156,84,124
s_waitcnt vmcnt(17) lgkmcnt(0)
s_barrier
mvaa 152,80,124
s_add_u32 m0,s62,49152
cv 208,192,193
cv 207,190,191
mvaa 152,16,60
dl 228,40,53,0
mvaa 148,20,56
cv 206,188,189
cv 205,186,187
mvaa 148,84,120
cv 204,184,185
cv 203,182,183
mvaa 144,80,120
cv 202,180,181
cv 201,178,179
mvaa 144,16,56
cv 200,176,177
cv 199,174,175
mvaa 140,20,52
dl 229,40,53,1024
mvaa 140,84,116
s_add_u32 s53,s53,s58
cv 198,172,173
cv 197,170,171
mvaa 136,80,116
ra 4,232,0
cv 196,168,169
mvaa 136,16,52
ta 92,239,12288
cv 195,166,167
mvaa 132,20,48
ta 94,239,12800
cv 194,164,165
mvaa 132,84,112
dl 230,44,54,2048
mvaa 128,80,112
ta 88,238,12288
cv 193,162,163
mvaa 128,16,48
ta 90,238,12800
cv 192,160,161
mvaa 124,20,44
cv 191,158,159
cv 190,156,157
mvaa 124,84,108
cv 189,154,155
cv 188,152,153
mvaa 120,80,108
dl 231,44,54,3072
mvaa 120,16,44
s_add_u32 m0,s98,32768
cv 187,150,151
cv 186,148,149
mvaa 116,20,40
s_add_u32 s54,s54,s58
cv 185,146,147
cv 184,144,145
mvaa 116,84,104
cv 183,142,143
cv 182,140,141
mvaa 112,80,104
cv 181,138,139
cv 180,136,137
mvaa 112,16,40
buffer_load_dword a243,v241,s[48:51],s55 offen
mvaa 108,20,36
s_add_u32 s55,s55,s59
cv 179,134,135
cv 178,132,133
mvaa 108,84,100
cv 177,130,131
cv 176,128,129
mvaa 104,80,100
cv 175,126,127
cv 174,124,125
mvaa 104,16,36
ta 44,235,0
cv 173,122,123
mvaa 100,20,32
dl 224,36,52,0
mvaa 100,84,96
ta 46,235,512
cv 172,120,121
mvaa 96,80,96
cv 171,118,119
cv 170,116,117
mvaa 96,16,32
ta 40,234,0
cv 169,114,115
mvaa 92,12,60
ta 42,234,512
cv 168,112,113
mvaa 92,76,124
dl 225,36,52,1024
mvaa 88,72,124
cv 167,110,111
cv 166,108,109
mvaa 88,8,60
cv 165,106,107
cv 164,104,105
mvaa 84,12,56
ta 84,237,8192
cv 163,102,103
mvaa 84,76,120
ta 80,236,8192
cv 162,100,101
mvaa 80,72,120
dl 226,36,52,2048
mvaa 80,8,56
ta 86,237,8704
cv 161,98,99
mvaa 76,12,52
ta 82,236,8704
cv 160,96,97
mvaa 76,76,116
ra 16,232,6144
cv 159,94,95
mvaa 72,72,116
ta 60,235,4096
cv 158,92,93
mvaa 72,8,52
dl 227,36,52,3072
mvaa 68,12,48
s_add_u32 m0,s62,0
ta 56,234,4096
s_add_u32 s52,s52,s56
mvaa 68,76,112
L_vs6:
s_cbranch_execz L_vs10
ta 62,235,4608
cv 151,78,79
mvaa 64,72,112
ta 58,234,4608
cv 150,76,77
mvaa 64,8,48
ta 52,237,0
cv 157,90,91
mvaa 60,76,108
buffer_load_dword a244,v241,s[48:51],s55 offen
mvaa 56,72,108
s_add_u32 s55,s55,s59
ra 12,232,4096
cv 147,70,71
mvaa 52,76,104
ta 54,237,512
cv 146,68,69
mvaa 48,72,104
ra 8,232,2048
cv 145,66,67
mvaa 44,76,100
ta 48,236,0
cv 144,64,65
mvaa 40,72,100
buffer_load_dword a245,v241,s[48:51],s55 offen
mvaa 36,76,96
s_add_u32 s55,s55,s59
ta 50,236,512
cv 143,62,63
mvaa 32,72,96
cv 142,60,61
cv 141,58,59
mvaa 28,68,124
cv 140,56,57
cv 139,54,55
mvaa 24,64,124
cv 138,52,53
cv 137,50,51
mvaa 20,68,120
s_mov_b64 exec,65537
buffer_store_dword v246,v250,s[8:11],s86 offen sc0 sc1
s_mov_b64 exec,-1
mvaa 16,64,120
cv 136,48,49
cv 135,46,47
mvaa 12,68,116
ta 76,235,8192
cv 134,44,45
mvaa 8,64,116
ta 78,235,8704
cv 133,42,43
mvaa 4,68,112
ta 72,234,8192
cv 132,40,41
mvaa 0,64,112
ta 74,234,8704
cv 131,38,39
maaa 156,68,108
cv 130,36,37
cv 129,34,35
maaa 152,64,108
cv 128,32,33
cv 127,30,31
maaa 148,68,104
cv 126,28,29
cv 125,26,27
maaa 144,64,104
v_accvgpr_read_b32 v63,a159
v_accvgpr_read_b32 v62,a158
maaa 140,68,100
v_accvgpr_read_b32 v61,a157
v_accvgpr_read_b32 v60,a156
maaa 136,64,100
v_accvgpr_read_b32 v59,a155
v_accvgpr_read_b32 v58,a154
maaa 132,68,96
v_accvgpr_read_b32 v57,a153
v_accvgpr_read_b32 v56,a152
maaa 128,64,96
cv 56,56,57
cv 57,58,59
s_waitcnt lgkmcnt(2)
zaaa 236,40,4
ta 68,237,4096
ta 70,237,4608
zaaa 220,44,4
ta 64,236,4096
ta 66,236,4608
zaaa 216,44,8
cv 58,60,61
cv 59,62,63
zaaa 232,40,8
buffer_store_dwordx4 v[56:59],v244,s[24:27],0 offen offset:3072 sc0 sc1
zaaa 228,40,12
v_accvgpr_read_b32 v55,a151
v_accvgpr_read_b32 v54,a150
zaaa 212,44,12
v_accvgpr_read_b32 v53,a149
v_accvgpr_read_b32 v52,a148
zaaa 208,44,16
v_accvgpr_read_b32 v51,a147
v_accvgpr_read_b32 v50,a146
zaaa 224,40,16
v_accvgpr_read_b32 v49,a145
v_accvgpr_read_b32 v48,a144
zaaa 192,48,16
cv 48,48,49
cv 49,50,51
zaaa 176,52,16
cv 50,52,53
s_waitcnt vmcnt(26) lgkmcnt(0)
s_barrier
zaaa 180,52,12
dl 228,40,53,0
cv 51,54,55
zaaa 196,48,12
ra 24,233,0
v_accvgpr_read_b32 v47,a143
zaaa 200,48,8
ra 28,233,2048
v_accvgpr_read_b32 v46,a142
v_accvgpr_read_b32 v45,a141
zaaa 184,52,8
ra 32,233,4096
ra 36,233,6144
zaaa 188,52,4
v_accvgpr_read_b32 v43,a139
v_accvgpr_read_b32 v42,a138
zaaa 204,48,4
dl 229,40,53,1024
ta 96,236,24576
zaaa 172,56,4
s_add_u32 s53,s53,s57
ta 98,236,25088
v_accvgpr_read_b32 v41,a137
zaaa 156,60,4
ta 100,237,24576
v_accvgpr_read_b32 v39,a135
zaaa 152,60,8
ta 102,237,25088
ta 104,238,28672
zaaa 168,56,8
v_accvgpr_read_b32 v38,a134
v_accvgpr_read_b32 v35,a131
zaaa 164,56,12
dl 230,44,54,2048
ta 106,238,29184
zaaa 148,60,12
ta 108,239,28672
v_accvgpr_read_b32 v34,a130
zaaa 144,60,16
ta 110,239,29184
v_accvgpr_read_b32 v33,a129
zaaa 160,56,16
ta 44,235,16384
ta 46,235,16896
ta 40,234,16384
ta 42,234,16896
ta 52,237,16384
ta 54,237,16896
ta 48,236,16384
ta 50,236,16896
ta 60,235,20480
ta 62,235,20992
ta 56,234,20480
ta 58,234,20992
v_accvgpr_read_b32 v32,a128
zaaa 128,64,16
v_accvgpr_read_b32 v37,a133
v_accvgpr_read_b32 v36,a132
zaaa 112,68,16
dl 231,44,54,3072
zaaa 116,68,12
s_add_u32 m0,s61,4096
v_accvgpr_read_b32 v40,a136
v_accvgpr_read_b32 v44,a140
zaaa 132,64,12
s_add_u32 s54,s54,s57
cv 251,42,43
cv 250,40,41
zaaa 136,64,8
s_add_u32 s61,s63,s98
cv 253,46,47
cv 252,44,45
zaaa 120,68,8
s_add_u32 s63,s63,s99
cv 249,38,39
cv 247,34,35
zaaa 124,68,4
buffer_store_dwordx4 v[48:51],v244,s[24:27],0 offen offset:2048 sc0 sc1
zaaa 140,64,4
cv 246,32,33
cv 248,36,37
zvaa 40,72,4
ta 68,237,20480
cv 124,24,25
zvaa 44,76,4
ta 70,237,20992
cv 123,22,23
zvaa 52,76,8
ta 64,236,20480
cv 121,18,19
zvaa 48,72,8
dl 224,36,52,0
zvaa 56,72,12
ta 66,236,20992
cv 120,16,17
zvaa 60,76,12
cv 122,20,21
cv 119,14,15
zvaa 68,76,16
cv 118,12,13
cv 117,10,11
zvaa 64,72,16
cv 116,8,9
cv 156,88,89
zvaa 32,80,16
dl 225,36,52,1024
ta 76,235,24576
ta 78,235,25088
zvaa 36,84,16
ta 72,234,24576
ta 74,234,25088
zvaa 28,84,12
cv 153,82,83
cv 152,80,81
zvaa 24,80,12
cv 155,86,87
cv 154,84,85
zvaa 16,80,8
cv 149,74,75
cv 148,72,73
zvaa 20,84,8
dl 226,36,52,2048
zvaa 12,84,4
v_add_u32_e32 v232,s99,v232
cv 115,6,7
zvaa 8,80,4
cv 114,4,5
cv 113,2,3
zvaa 76,4,88
cv 112,0,1
zvaa 96,4,92
zvaa 100,8,92
dl 227,36,52,3072
zvaa 80,8,88
s_add_u32 s52,s52,s60
zvaa 84,12,88
zvaa 104,12,92
zvaa 108,16,92
s_waitcnt vmcnt(23) lgkmcnt(0)
s_barrier
zvaa 88,16,88
L_kvx:
L_vs7:
s_cbranch_execz L_vs11
L_p_exit:
s_add_u32 m0,s62,16384
ra 4,232,0
v_add_u32_e32 v233,0xffff0000,v233
maaa 236,40,24
dl 228,40,53,0
maaa 220,44,24
s_lshl_b32 s86,s21,2
ra 8,232,2048
s_add_u32 s86,s86,s12
maaa 216,44,28
s_lshl_b32 s86,s86,8
ra 12,232,4096
s_add_u32 s86,s86,L_p_exit-L_att_pc+2048
maaa 232,40,28
s_mov_b32 s67,4096
ra 16,232,6144
s_and_b32 s87,s12,1
maaa 228,40,32
s_cmp_lt_u32 s12,2
s_cselect_b32 s94,s22,s23
ra 80,233,0
s_lshr_b32 s94,s94,6
maaa 212,44,32
dl 229,40,53,1024
maaa 208,44,36
s_add_u32 s94,s94,s87
ra 84,233,2048
s_lshl_b32 s87,s18,5
maaa 224,40,36
s_add_u32 s94,s94,s87
ra 88,233,4096
s_lshl_b32 s94,s94,11
maaa 192,48,36
s_add_u32 s94,s94,12582912
ra 92,233,6144
s_add_u32 s53,s53,s58
maaa 176,52,36
s_bfe_u32 s88,s21,65539
ta 0,234,32768
s_lshl_b32 s87,s20,1
maaa 180,52,32
dl 230,44,54,2048
maaa 196,48,32
s_add_u32 s88,s88,s87
ta 2,234,33280
s_lshl_b32 s88,s88,7
maaa 200,48,28
s_add_u32 s85,s88,83894272
ta 40,235,32768
v_bfe_u32 v241,v244,6,4
maaa 184,52,28
s_lshl_b32 s91,s12,4
ta 42,235,33280
v_and_b32_e32 v241,12,v241
maaa 188,52,24
s_cmp_lt_u32 s12,2
s_cselect_b32 s90,s22,s23
s_mul_i32 s33,s90,10240
v_accvgpr_read_b32 v255,a241
maaa 204,48,24
dl 231,44,54,3072
maaa 172,56,24
s_add_u32 m0,s98,65536
ta 52,236,32768
s_add_u32 s54,s54,s58
maaa 156,60,24
s_mov_b32 s64,0x55505555
ta 54,236,33280
s_mov_b32 s65,0x50005500
maaa 152,60,28
s_lshl_b32 s95,s12,12
s_mov_b32 s66,0x50000
s_add_u32 s70,s95,32768
maaa 168,56,28
s_add_u32 s72,s95,65536
s_lshl_b32 s4,s21,2
s_add_u32 s4,s4,s12
maaa 164,56,32
buffer_store_dwordx4 v[250:253],v244,s[24:27],0 offen offset:1024 sc0 sc1
maaa 148,60,32
s_lshl_b32 s4,s4,8
v_lshl_add_u32 v253,v244,3,s86
v_mov_b32_e32 v250,-1
maaa 144,60,36
s_bitcmp1_b32 170,s20
s_cselect_b32 s5,L_odd-L_last,0
v_mov_b32_e32 v251,-1
v_bfe_u32 v252,v244,4,4
maaa 160,56,36
s_add_u32 s4,s4,s5
v_sub_u32_e32 v252,v252,v241
v_add_u32_e32 v252,s91,v252
maaa 128,64,36
s_cmp_lt_u32 s12,2
s_cselect_b32 s91,32768,0
s_add_u32 s91,s91,98304
ta 56,237,32768
maaa 112,68,36
s_mov_b64 exec,3
global_load_dword a250,v253,s[96:97]
s_mov_b64 exec,-1
maaa 116,68,32
s_add_u32 s4,s4,L_last-L_att_pc
ta 58,237,33280
v_add_u32_e32 v253,10240,v253
maaa 132,64,32
s_mov_b64 s[30:31],s[28:29]
v_mov_b32_e32 v241,0xf149f2ca
s_mov_b64 s[28:29],s[78:79]
maaa 136,64,28
s_add_u32 s68,s95,0
v_cvt_f32_i32_e32 v252,v252
v_mul_f32_e32 v252,0x7149f2ca,v252
maaa 120,68,28
s_add_u32 s69,s95,16384
s_add_u32 s74,s95,98304
s_add_u32 s71,s95,49152
maaa 124,68,24
dl 224,36,52,0
maaa 140,64,24
s_add_u32 s73,s95,81920
v_and_b32_e32 v245,48,v244
v_lshlrev_b32_e32 v245,3,v245
mvaa 40,72,24
s_waitcnt vmcnt(15) lgkmcnt(2)
s_barrier
mvaa 44,76,24
s_lshl_b32 s59,%1,2
ta 20,234,49152
s_lshl_b32 s84,%1,2
mvaa 52,76,28
s_add_u32 s59,s59,s12
ta 22,234,49664
s_add_u32 s75,s95,114688
mvaa 48,72,28
s_add_u32 s84,s84,s12
ta 44,235,49152
s_mul_i32 s84,s84,12288
mvaa 56,72,32
dl 225,36,52,1024
mvaa 60,76,32
s_sub_u32 s55,s14,s13
ta 46,235,49664
s_mov_b32 s48,0x38000000
mvaa 68,76,36
s_mov_b32 s81,1
ta 48,236,49152
s_mov_b32 s84,0
mvaa 64,72,36
ta 50,236,49664
mvaa 32,96,36
ta 60,237,49152
mvaa 36,100,36
dl 226,36,52,2048
mvaa 28,100,32
ta 62,237,49664
mvaa 24,96,32
ta 68,234,36864
mvaa 16,96,28
ta 70,234,37376
mvaa 20,100,28
ta 64,234,53248
mvaa 12,100,24
dl 227,36,52,3072
mvaa 8,96,24
s_add_u32 s52,s52,s56
s_add_u32 m0,s98,69632
ta 66,234,53760
mvaa 76,24,104
ta 76,235,53248
mvaa 96,24,108
ta 78,235,53760
mvaa 100,28,108
ta 72,235,36864
mvaa 80,28,104
buffer_store_dwordx4 v[246:249],v244,s[24:27],0 offen offset:0 sc0 sc1
mvaa 84,32,104
)"
R"(ta 74,235,37376
v_mov_b32_e32 v248,-1
mvaa 104,32,108
v_mov_b32_e32 v249,-1
v_mov_b32_e32 v247,1
mvaa 108,36,108
mvaa 88,36,104
maaa 236,0,4
s_waitcnt lgkmcnt(5)
maaa 236,20,80
dl 224,36,52,0
maaa 220,44,80
maaa 220,40,4
maaa 216,40,8
maaa 216,44,84
maaa 232,20,84
dl 225,36,52,1024
maaa 232,0,8
maaa 228,0,12
maaa 228,20,88
maaa 212,44,88
maaa 212,40,12
dl 226,36,52,2048
maaa 208,40,16
maaa 208,44,92
maaa 224,20,92
ta 40,236,36864
maaa 224,0,16
ta 42,236,37376
maaa 192,52,16
dl 227,36,52,3072
maaa 192,48,92
s_add_u32 s52,s52,s60
s_add_u32 m0,s98,0
ta 44,236,53248
maaa 176,60,92
ta 46,236,53760
maaa 176,56,16
ta 20,237,53248
maaa 180,56,12
ta 22,237,53760
maaa 180,60,88
dl 224,36,52,0
maaa 196,48,88
ta 0,237,36864
maaa 196,52,12
ta 2,237,37376
maaa 200,52,8
s_waitcnt vmcnt(16)
ra 24,232,32768
maaa 200,48,84
ra 28,232,34816
maaa 184,60,84
ra 32,232,36864
maaa 184,56,8
buffer_store_dwordx4 v[112:115],v244,s[24:27],s67 offen offset:0 sc0 sc1
maaa 188,56,4
ra 36,232,38912
v_add_u32_e32 v232,0x10000,v232
maaa 188,60,80
ra 96,233,32768
v_cndmask_b32_e64 v112,v241,0,s[64:65]
maaa 204,48,80
s_mov_b32 s64,0xaaa0aaaa
ra 100,233,34816
s_mov_b32 s65,0xa000aa00
maaa 204,52,4
ra 104,233,36864
v_cndmask_b32_e64 v113,v241,0,s[64:65]
maaa 172,68,4
dl 225,36,52,1024
s_waitcnt lgkmcnt(8)
maaa 172,64,80
s_mov_b32 s64,0x55405554
ra 108,233,38912
v_add_u32_e32 v233,0x10000,v233
maaa 156,76,80
s_mov_b32 s65,0x40005400
ta 56,234,40960
v_cndmask_b32_e64 v114,v241,0,s[64:65]
maaa 156,72,4
s_mov_b32 s64,0xaa80aaa8
ta 58,234,41472
s_mov_b32 s65,0x8000a800
maaa 152,72,8
ta 60,234,57344
v_cndmask_b32_e64 v115,v241,0,s[64:65]
maaa 152,76,84
dl 226,36,52,2048
maaa 168,64,84
ta 62,234,57856
maaa 168,68,8
ta 48,235,57344
maaa 164,68,12
ta 50,235,57856
maaa 164,64,88
ta 52,235,40960
maaa 148,76,88
buffer_store_dwordx4 v[248:251],v244,s[8:11],s94 offen offset:0 sc0 sc1
maaa 148,72,12
ta 54,235,41472
maaa 144,72,16
maaa 144,76,92
maaa 160,64,92
ta 72,236,40960
maaa 160,68,16
ta 74,236,41472
maaa 128,40,16
dl 227,36,52,3072
maaa 128,44,92
s_add_u32 s52,s52,s56
ta 76,236,57344
s_add_u32 m0,s98,4096
maaa 112,20,92
ta 78,236,57856
s_waitcnt lgkmcnt(6)
maaa 112,0,16
L_vs8:
s_cbranch_execz L_vs12
ta 64,237,57344
maaa 116,0,12
ta 66,237,57856
maaa 116,20,88
dl 224,36,52,0
maaa 132,44,88
ta 68,237,40960
maaa 132,40,12
ta 70,237,41472
maaa 136,40,8
maaa 136,44,84
maaa 120,20,84
dl 225,36,52,1024
maaa 120,0,8
maaa 124,0,4
maaa 124,20,80
maaa 140,44,80
ta 0,238,45056
ta 2,238,45568
maaa 140,40,4
buffer_store_dwordx4 v[248:251],v244,s[8:11],s94 offen offset:1024 sc0 sc1
ta 20,238,61440
ta 22,238,61952
mvaa 40,56,4
ta 44,239,61440
ta 46,239,61952
mvaa 40,60,80
ta 40,239,45056
ta 42,239,45568
mvaa 44,48,80
v_and_b32_e32 v250,240,v244
v_accvgpr_read_b32 v249,a240
s_waitcnt lgkmcnt(8)
mvaa 44,52,4
mvaa 52,52,8
mvaa 52,48,84
dl 226,36,52,2048
mvaa 48,60,84
mvaa 48,56,8
mvaa 56,56,12
mvaa 56,60,88
mvaa 60,48,88
dl 227,36,52,3072
mvaa 60,52,12
s_add_u32 s52,s52,s60
mvaa 68,52,16
mvaa 68,48,92
mvaa 64,60,92
mvaa 64,56,16
mvaa 32,72,16
s_waitcnt vmcnt(11) lgkmcnt(0)
s_barrier
s_add_u32 m0,s62,32768
mvaa 32,76,92
dl 228,40,53,0
mvaa 36,64,92
ta 52,234,0
mvaa 36,68,16
ta 54,234,512
mvaa 28,68,12
ta 48,234,16384
mvaa 28,64,88
ta 50,234,16896
mvaa 24,76,88
ta 60,235,16384
mvaa 24,72,12
dl 229,40,53,1024
mvaa 16,72,8
s_add_u32 s53,s53,s57
ta 62,235,16896
mvaa 16,76,84
ta 56,235,0
mvaa 20,64,84
ta 58,235,512
mvaa 20,68,8
mvaa 12,68,4
dl 230,44,54,2048
mvaa 12,64,80
mvaa 8,76,80
ta 68,236,0
mvaa 8,72,4
ta 70,236,512
mvaa 76,4,0
ta 64,236,16384
mvaa 76,80,20
dl 231,44,54,3072
mvaa 96,80,44
s_add_u32 m0,s62,49152
s_add_u32 s54,s54,s57
ta 66,236,16896
mvaa 96,4,40
ta 76,237,16384
mvaa 100,8,40
ta 78,237,16896
mvaa 100,84,44
ta 72,237,0
mvaa 80,84,20
ta 74,237,512
mvaa 80,8,0
dl 228,40,53,0
mvaa 84,12,0
ra 80,233,0
mvaa 84,88,20
ra 4,232,0
mvaa 104,88,44
ra 84,233,2048
mvaa 104,12,40
ra 8,232,2048
mvaa 108,16,40
dl 229,40,53,1024
mvaa 108,92,44
s_add_u32 s53,s53,s58
ra 88,233,4096
mvaa 88,92,20
ta 40,234,4096
mvaa 88,16,0
ta 42,234,4608
s_waitcnt lgkmcnt(8)
maaa 236,52,24
ta 44,234,20480
maaa 236,48,96
dl 230,44,54,2048
maaa 220,60,96
ta 46,234,20992
maaa 220,56,24
ta 20,235,20480
maaa 216,56,28
ta 22,235,20992
maaa 216,60,100
ta 0,235,4096
maaa 232,48,100
dl 231,44,54,3072
maaa 232,52,28
s_add_u32 m0,s98,32768
ta 2,235,4608
s_add_u32 s54,s54,s58
maaa 228,52,32
ra 12,232,4096
maaa 228,48,104
ra 16,232,6144
v_add_u32_e32 v232,0xffff0000,v232
maaa 212,60,104
ra 92,233,6144
v_add_u32_e32 v233,0xffff0000,v233
maaa 212,56,32
maaa 208,56,36
dl 224,36,52,0
maaa 208,60,108
maaa 224,48,108
ta 56,236,4096
maaa 224,52,36
ta 58,236,4608
maaa 192,68,36
ta 60,236,20480
maaa 192,64,108
dl 225,36,52,1024
maaa 176,76,108
ta 62,236,20992
s_waitcnt lgkmcnt(6)
maaa 176,72,36
ta 48,237,20480
maaa 180,72,32
ta 50,237,20992
maaa 180,76,104
ta 52,237,4096
maaa 196,64,104
buffer_store_dwordx4 v[116:119],v244,s[24:27],s67 offen offset:1024 sc0 sc1
maaa 196,68,32
ta 54,237,4608
maaa 200,68,28
maaa 200,64,100
maaa 184,76,100
maaa 184,72,28
dl 226,36,52,2048
maaa 188,72,24
maaa 188,76,96
maaa 204,64,96
ta 72,234,8192
maaa 204,68,24
ta 74,234,8704
maaa 172,40,24
ta 76,234,24576
maaa 172,44,96
dl 227,36,52,3072
maaa 156,20,96
s_add_u32 s52,s52,s56
s_add_u32 m0,s98,36864
ta 78,234,25088
maaa 156,0,24
ta 64,235,24576
maaa 152,0,28
ta 66,235,25088
maaa 152,20,100
ta 68,235,8192
maaa 168,44,100
dl 224,36,52,0
maaa 168,40,28
ta 70,235,8704
maaa 164,40,32
maaa 164,44,104
maaa 148,20,104
maaa 148,0,32
buffer_store_dwordx4 v[120:123],v244,s[24:27],s67 offen offset:2048 sc0 sc1
maaa 144,0,36
maaa 144,20,108
maaa 160,44,108
ta 0,236,8192
maaa 160,40,36
ta 2,236,8704
s_waitcnt lgkmcnt(3)
maaa 128,56,36
dl 225,36,52,1024
maaa 128,60,108
ta 20,236,24576
maaa 112,48,108
ta 22,236,25088
maaa 112,52,36
ta 44,237,24576
maaa 116,52,32
ta 46,237,25088
maaa 116,48,104
dl 226,36,52,2048
maaa 132,60,104
ta 40,237,8192
maaa 132,56,32
ta 42,237,8704
maaa 136,56,28
maaa 136,60,100
maaa 120,48,100
maaa 120,52,28
buffer_load_dword v246,off,s[8:11],s35 offset:1024 sc1
maaa 124,52,24
maaa 124,48,96
maaa 140,60,96
ta 52,238,12288
ta 54,238,12800
maaa 140,56,24
ta 48,238,28672
ta 50,238,29184
mvaa 40,72,24
buffer_store_dwordx4 v[124:127],v244,s[24:27],s67 offen offset:3072 sc0 sc1
ta 60,239,28672
mvaa 40,76,96
s_mov_b32 s67,8192
ta 62,239,29184
mvaa 44,64,96
ta 56,239,12288
ta 58,239,12800
s_waitcnt lgkmcnt(8)
mvaa 44,68,24
mvaa 52,68,28
mvaa 52,64,100
dl 227,36,52,3072
mvaa 48,76,100
s_add_u32 s52,s52,s60
mvaa 48,72,28
mvaa 56,72,32
mvaa 56,76,104
mvaa 60,64,104
mvaa 60,68,32
mvaa 68,68,36
mvaa 68,64,108
mvaa 64,76,108
mvaa 64,72,36
mvaa 32,0,36
s_waitcnt vmcnt(12) lgkmcnt(0)
s_barrier
s_add_u32 m0,s62,0
mvaa 32,20,108
dl 228,40,53,0
mvaa 36,44,108
ta 68,234,32768
mvaa 36,40,36
ta 70,234,33280
mvaa 28,40,32
ta 64,234,49152
mvaa 28,44,104
ta 66,234,49664
mvaa 24,20,104
ta 76,235,49152
mvaa 24,0,32
dl 229,40,53,1024
mvaa 16,0,28
s_add_u32 s53,s53,s57
ta 78,235,49664
mvaa 16,20,100
ta 72,235,32768
mvaa 20,44,100
ta 74,235,33280
mvaa 20,40,28
mvaa 12,40,24
mvaa 12,44,96
dl 230,44,54,2048
mvaa 8,20,96
ta 40,236,32768
mvaa 8,0,24
ta 42,236,33280
mvaa 76,24,52
ta 44,236,49152
mvaa 76,96,48
ta 46,236,49664
mvaa 96,96,60
dl 231,44,54,3072
mvaa 96,24,56
s_add_u32 m0,s62,16384
s_add_u32 s54,s54,s57
ta 20,237,49152
mvaa 100,28,56
ta 22,237,49664
mvaa 100,100,60
ta 0,237,32768
mvaa 80,100,48
ta 2,237,33280
mvaa 80,28,52
dl 228,40,53,0
mvaa 84,32,52
ra 96,233,0
mvaa 84,104,48
ra 24,232,0
mvaa 104,104,60
ra 100,233,2048
mvaa 104,32,56
ra 28,232,2048
mvaa 108,36,56
dl 229,40,53,1024
mvaa 108,108,60
s_add_u32 s53,s53,s58
ra 104,233,4096
mvaa 88,108,48
ta 56,234,36864
mvaa 88,36,52
ta 58,234,37376
s_waitcnt lgkmcnt(8)
maaa 236,68,4
ta 60,234,53248
maaa 236,64,80
ta 62,234,53760
maaa 220,76,80
dl 230,44,54,2048
maaa 220,72,4
ta 48,235,53248
maaa 216,72,8
ta 50,235,53760
maaa 216,76,84
ta 52,235,36864
maaa 232,64,84
ta 54,235,37376
maaa 232,68,8
dl 231,44,54,3072
maaa 228,68,12
s_add_u32 m0,s98,65536
ra 32,232,4096
s_add_u32 s54,s54,s58
maaa 228,64,88
ra 36,232,6144
maaa 212,76,88
ra 108,233,6144
maaa 212,72,12
s_waitcnt vmcnt(10)
v_readfirstlane_b32 s87,v246
maaa 208,72,16
dl 224,36,52,0
maaa 208,76,92
s_cmp_eq_u32 s12,0
s_cselect_b32 s89,s32,s87
s_cmp_lg_u32 s87,s89
s_cbranch_scc1 L_q_ack
L_q_acked:
maaa 224,64,92
s_and_b32 s87,s12,1
ta 72,236,36864
s_lshl_b32 s88,s87,6
maaa 224,68,16
s_add_u32 s90,s90,s88
ta 74,236,37376
s_lshl_b32 s88,s87,10
maaa 192,40,16
s_lshl_b32 s87,s87,14
ta 76,236,53248
s_add_u32 s91,s91,s87
maaa 192,44,92
dl 225,36,52,1024
maaa 176,20,92
s_lshr_b32 s87,s18,1
ta 78,236,53760
s_lshl_b32 s87,s87,4
s_waitcnt lgkmcnt(5)
maaa 176,0,16
s_add_u32 s33,s33,s88
ta 64,237,53248
v_cmp_eq_u32_e64 s[100:101],s87,v250
maaa 180,0,12
s_lshl_b32 s88,s18,11
ta 66,237,53760
v_lshlrev_b32_e32 v250,3,v250
maaa 180,20,88
s_add_u32 s88,s33,s88
ta 68,237,36864
v_add_u32_e32 v250,83894272,v250
maaa 196,44,88
s_add_u32 s33,s88,63176704
ta 70,237,37376
v_add_u32_e32 v251,s91,v244
maaa 196,40,12
buffer_store_dwordx4 v[128:131],v244,s[24:27],s67 offen offset:0 sc0 sc1
maaa 200,40,8
s_add_u32 s34,s88,63504384
v_cndmask_b32_e64 v242,31,-1,s[100:101]
v_and_b32_e32 v243,528,v242
maaa 200,44,84
s_add_u32 s50,s88,63832064
s_add_u32 s51,s88,64159744
s_lshr_b32 s91,s21,3
maaa 184,20,84
s_and_b32 s88,s20,4
s_and_b32 s89,s20,3
s_add_u32 s91,s91,s88
maaa 184,0,8
s_lshl_b32 s88,s89,6
s_lshl_b32 s86,s21,2
s_add_u32 s86,s86,s12
maaa 188,0,4
dl 226,36,52,2048
maaa 188,20,80
s_cmp_eq_u32 s90,0
s_cselect_b64 s[64:65],-1,0
s_and_b32 s89,s86,1
ta 0,234,40960
maaa 204,44,80
s_lshl_b32 s89,s89,9
ta 2,234,41472
s_lshr_b32 s49,s86,3
maaa 204,40,4
s_lshl_b32 s49,s49,11
ta 20,234,57344
v_add_f32_e32 v128,0x70c9f2ca,v252
maaa 172,56,4
ta 22,234,57856
v_add_f32_e32 v129,0xf0c9f2ca,v252
maaa 172,60,80
dl 227,36,52,3072
maaa 156,48,80
s_add_u32 s52,s52,s56
s_add_u32 m0,s98,69632
ta 44,235,57344
maaa 156,52,4
ta 46,235,57856
v_add_f32_e32 v130,0xf1977617,v252
maaa 152,52,8
ta 40,235,40960
v_add_f32_e32 v131,0xf1fc6f7c,v252
maaa 152,48,84
ta 42,235,41472
v_min_f32_e32 v128,0xc1a00000,v128
maaa 168,60,84
dl 224,36,52,0
maaa 168,56,8
v_min_f32_e32 v129,0xc1a00000,v129
v_min_f32_e32 v130,0xc1a00000,v130
maaa 164,56,12
v_min_f32_e32 v131,0xc1a00000,v131
v_lshrrev_b32_e32 v246,6,v244
maaa 164,60,88
v_lshl_add_u32 v245,v246,22,v245
v_lshlrev_b32_e32 v246,16,v244
maaa 148,48,88
maaa 148,52,12
maaa 144,52,16
buffer_store_dwordx4 v[132:135],v244,s[24:27],s67 offen offset:1024 sc0 sc1
maaa 144,48,92
maaa 160,60,92
ta 52,236,40960
v_add_f32_e32 v132,0xf343a333,v252
maaa 160,56,16
ta 54,236,41472
v_add_f32_e32 v133,0xf3504260,v252
s_waitcnt lgkmcnt(2)
maaa 128,72,16
ta 48,236,57344
v_add_f32_e32 v134,0xf35ce18d,v252
maaa 128,76,92
dl 225,36,52,1024
maaa 112,64,92
ta 50,236,57856
v_min_f32_e32 v132,0xc1a00000,v132
maaa 112,68,16
ta 60,237,57344
v_add_f32_e32 v135,0xf36980b9,v252
maaa 116,68,12
ta 62,237,57856
v_min_f32_e32 v133,0xc1a00000,v133
maaa 116,64,88
ta 56,237,40960
v_min_f32_e32 v134,0xc1a00000,v134
maaa 132,76,88
dl 226,36,52,2048
maaa 132,72,12
ta 58,237,41472
v_min_f32_e32 v135,0xc1a00000,v135
maaa 136,72,8
maaa 136,76,84
maaa 120,64,84
maaa 120,68,8
dl 227,36,52,3072
maaa 124,68,4
s_add_u32 s52,s52,s60
maaa 124,64,80
maaa 140,76,80
ta 68,238,45056
ta 70,238,45568
maaa 140,72,4
ta 64,238,61440
ta 66,238,61952
mvaa 40,0,4
ta 76,239,61440
mvaa 40,20,80
buffer_store_dwordx4 v[136:139],v244,s[24:27],s67 offen offset:2048 sc0 sc1
ta 78,239,61952
mvaa 44,44,80
ta 72,239,45056
ta 74,239,45568
mvaa 44,40,4
v_add_f32_e32 v136,0xf3c6caff,v252
v_add_f32_e32 v137,0xf3cd1a95,v252
mvaa 52,40,8
v_add_f32_e32 v138,0xf3d36a2b,v252
v_add_f32_e32 v139,0xf3d9b9c2,v252
mvaa 52,44,84
v_min_f32_e32 v136,0xc1a00000,v136
v_min_f32_e32 v137,0xc1a00000,v137
mvaa 48,20,84
v_min_f32_e32 v138,0xc1a00000,v138
v_min_f32_e32 v139,0xc1a00000,v139
mvaa 48,0,8
mvaa 56,0,12
mvaa 56,20,88
mvaa 60,44,88
mvaa 60,40,12
mvaa 68,40,16
mvaa 68,44,92
mvaa 64,20,92
mvaa 64,0,16
L_vs9:
s_cbranch_execz L_vs13
s_waitcnt lgkmcnt(0)
mvaa 32,52,16
s_waitcnt vmcnt(11)
s_barrier
mvaa 32,48,92
s_cmp_eq_u32 s12,0
s_cselect_b64 exec,1,0
buffer_atomic_add v247,off,s[8:11],s85 sc1
s_mov_b64 exec,-1
mvaa 36,60,92
s_add_u32 m0,s62,32768
ta 40,234,0
v_mov_b32_e32 v247,32
mvaa 36,56,16
ta 42,234,512
mvaa 28,56,12
ta 44,234,16384
mvaa 28,60,88
ta 46,234,16896
mvaa 24,48,88
dl 228,40,53,0
mvaa 24,52,12
ta 20,235,16384
mvaa 16,52,8
ta 22,235,16896
mvaa 16,48,84
ta 0,235,0
mvaa 20,60,84
ta 2,235,512
mvaa 20,56,8
dl 229,40,53,1024
mvaa 12,56,4
s_add_u32 s53,s53,s57
mvaa 12,60,80
mvaa 8,48,80
ta 56,236,0
mvaa 8,52,4
ta 58,236,512
mvaa 76,4,68
dl 230,44,54,2048
mvaa 76,80,64
ta 60,236,16384
mvaa 96,80,76
ta 62,236,16896
mvaa 96,4,72
ta 48,237,16384
mvaa 100,8,72
ta 50,237,16896
mvaa 100,84,76
dl 231,44,54,3072
mvaa 80,84,64
s_add_u32 m0,s62,49152
s_add_u32 s54,s54,s57
ta 52,237,0
mvaa 80,8,68
ta 54,237,512
mvaa 84,12,68
ra 80,233,32768
mvaa 84,88,64
ra 4,232,32768
mvaa 104,88,76
dl 228,40,53,0
mvaa 104,12,72
ra 84,233,34816
mvaa 108,16,72
ra 8,232,34816
mvaa 108,92,76
ra 12,232,36864
mvaa 88,92,64
ta 72,234,4096
mvaa 88,16,68
ta 74,234,4608
s_waitcnt lgkmcnt(9)
maaa 236,40,24
dl 229,40,53,1024
maaa 236,44,96
s_add_u32 s53,s53,s58
ta 76,234,20480
maaa 220,20,96
ta 78,234,20992
maaa 220,0,24
ta 64,235,20480
maaa 216,0,28
ta 66,235,20992
maaa 216,20,100
dl 230,44,54,2048
maaa 232,44,100
ta 68,235,4096
maaa 232,40,28
ta 70,235,4608
maaa 228,40,32
ra 88,233,36864
maaa 228,44,104
ra 92,233,38912
v_add_u32_e32 v233,0x10000,v233
maaa 212,20,104
dl 231,44,54,3072
maaa 212,0,32
s_add_u32 m0,s98,0
ra 16,232,38912
s_add_u32 s54,s54,s58
maaa 208,0,36
v_add_u32_e32 v232,0x10000,v232
maaa 208,20,108
maaa 224,44,108
ta 0,236,4096
maaa 224,40,36
dl 224,36,52,0
maaa 192,56,36
ta 2,236,4608
maaa 192,60,108
ta 20,236,20480
maaa 176,48,108
ta 22,236,20992
s_waitcnt lgkmcnt(6)
maaa 176,52,36
ta 44,237,20480
maaa 180,52,32
ta 46,237,20992
maaa 180,48,104
dl 225,36,52,1024
maaa 196,60,104
ta 40,237,4096
maaa 196,56,32
ta 42,237,4608
maaa 200,56,28
maaa 200,60,100
maaa 184,48,100
buffer_store_dwordx4 v[140:143],v244,s[24:27],s67 offen offset:3072 sc0 sc1
maaa 184,52,28
s_mov_b32 s67,12288
v_add_f32_e32 v140,0xf415e232,v252
v_add_f32_e32 v141,0xf41909fd,v252
maaa 188,52,24
v_add_f32_e32 v142,0xf41c31c8,v252
v_min_f32_e32 v140,0xc1a00000,v140
maaa 188,48,96
v_add_f32_e32 v143,0xf41f5993,v252
v_bfe_i32 v252,v244,4,1
maaa 204,60,96
ta 52,234,8192
v_min_f32_e32 v141,0xc1a00000,v141
maaa 204,56,24
dl 226,36,52,2048
maaa 172,72,24
ta 54,234,8704
v_min_f32_e32 v142,0xc1a00000,v142
maaa 172,76,96
ta 48,234,24576
v_min_f32_e32 v143,0xc1a00000,v143
maaa 156,64,96
ta 50,234,25088
v_xor_b32_e32 v252,65535,v252
maaa 156,68,24
ta 60,235,24576
maaa 152,68,28
dl 227,36,52,3072
maaa 152,64,100
s_add_u32 s52,s52,s56
s_add_u32 m0,s98,4096
ta 62,235,25088
maaa 168,76,100
ta 56,235,8192
maaa 168,72,28
ta 58,235,8704
maaa 164,72,32
maaa 164,76,104
maaa 148,64,104
dl 224,36,52,0
maaa 148,68,32
maaa 144,68,36
maaa 144,64,108
maaa 160,76,108
ta 68,236,8192
maaa 160,72,36
buffer_store_dwordx4 v[144:147],v244,s[24:27],s67 offen offset:0 sc0 sc1
s_waitcnt lgkmcnt(3)
maaa 128,0,36
ta 70,236,8704
maaa 128,20,108
ta 64,236,24576
maaa 112,44,108
ta 66,236,25088
maaa 112,40,36
ta 76,237,24576
maaa 116,40,32
dl 225,36,52,1024
maaa 116,44,104
ta 78,237,25088
maaa 132,20,104
ta 72,237,8192
maaa 132,0,32
ta 74,237,8704
maaa 136,0,28
maaa 136,20,100
dl 226,36,52,2048
maaa 120,44,100
maaa 120,40,28
maaa 124,40,24
maaa 124,44,96
maaa 140,20,96
buffer_store_dwordx4 v[148:151],v244,s[24:27],s67 offen offset:1024 sc0 sc1
ta 40,238,12288
ta 42,238,12800
maaa 140,0,24
ta 44,238,28672
ta 46,238,29184
mvaa 40,52,24
ta 20,239,28672
mvaa 40,48,96
ta 22,239,29184
mvaa 44,60,96
ta 0,239,12288
ta 2,239,12800
s_waitcnt lgkmcnt(8)
mvaa 44,56,24
mvaa 52,56,28
dl 227,36,52,3072
mvaa 52,60,100
s_add_u32 s52,s52,s60
mvaa 48,48,100
mvaa 48,52,28
mvaa 56,52,32
mvaa 56,48,104
mvaa 60,60,104
mvaa 60,56,32
mvaa 68,56,36
mvaa 68,60,108
mvaa 64,48,108
mvaa 64,52,36
mvaa 32,68,36
s_waitcnt vmcnt(11) lgkmcnt(0)
s_barrier
s_add_u32 m0,s62,0
mvaa 32,64,108
dl 228,40,53,0
mvaa 36,76,108
ta 56,234,32768
mvaa 36,72,36
ta 58,234,33280
mvaa 28,72,32
ta 60,234,49152
mvaa 28,76,104
ta 62,234,49664
mvaa 24,64,104
dl 229,40,53,1024
mvaa 24,68,32
s_add_u32 s53,s53,s57
ta 48,235,49152
mvaa 16,68,28
ta 50,235,49664
mvaa 16,64,100
ta 52,235,32768
mvaa 20,76,100
ta 54,235,33280
mvaa 20,72,28
dl 230,44,54,2048
mvaa 12,72,24
mvaa 12,76,96
mvaa 8,64,96
ta 72,236,32768
mvaa 8,68,24
ta 74,236,33280
mvaa 76,24,40
ta 76,236,49152
mvaa 76,96,44
dl 231,44,54,3072
mvaa 96,96,20
s_add_u32 m0,s62,16384
s_add_u32 s54,s54,s57
ta 78,236,49664
mvaa 96,24,0
ta 64,237,49152
mvaa 100,28,0
ta 66,237,49664
mvaa 100,100,20
ta 68,237,32768
mvaa 80,100,44
dl 228,40,53,0
mvaa 80,28,40
ta 70,237,33280
mvaa 84,32,40
ra 96,233,0
mvaa 84,104,44
ra 24,232,0
mvaa 104,104,20
ra 100,233,2048
mvaa 104,32,0
dl 229,40,53,1024
mvaa 108,36,0
s_add_u32 s53,s53,s58
ra 28,232,2048
mvaa 108,108,20
ra 104,233,4096
mvaa 88,108,44
ta 0,234,36864
mvaa 88,36,40
ta 2,234,37376
s_waitcnt lgkmcnt(8)
maaa 236,56,4
dl 230,44,54,2048
maaa 236,60,80
ta 20,234,53248
maaa 220,48,80
ta 22,234,53760
maaa 220,52,4
ta 44,235,53248
maaa 216,52,8
ta 46,235,53760
maaa 216,48,84
dl 231,44,54,3072
maaa 232,60,84
s_add_u32 m0,s98,32768
ta 40,235,36864
s_add_u32 s54,s54,s58
maaa 232,56,8
ta 42,235,37376
maaa 228,56,12
ra 32,232,4096
maaa 228,60,88
ra 108,233,6144
v_add_u32_e32 v233,0xffff0000,v233
maaa 212,48,88
ra 36,232,6144
v_add_u32_e32 v232,0xffff0000,v232
maaa 212,52,12
dl 224,36,52,0
maaa 208,52,16
maaa 208,48,92
maaa 224,60,92
ta 52,236,36864
maaa 224,56,16
ta 54,236,37376
maaa 192,72,16
dl 225,36,52,1024
maaa 192,76,92
ta 48,236,53248
)"
R"(maaa 176,64,92
ta 50,236,53760
s_waitcnt lgkmcnt(6)
maaa 176,68,16
ta 60,237,53248
maaa 180,68,12
ta 62,237,53760
maaa 180,64,88
dl 226,36,52,2048
maaa 196,76,88
ta 56,237,36864
maaa 196,72,12
ta 58,237,37376
maaa 200,72,8
maaa 200,76,84
maaa 184,64,84
buffer_store_dwordx4 v[152:155],v244,s[24:27],s67 offen offset:2048 sc0 sc1
maaa 184,68,8
maaa 188,68,4
maaa 188,64,80
maaa 204,76,80
ta 68,234,40960
maaa 204,72,4
ta 70,234,41472
maaa 172,0,4
dl 227,36,52,3072
maaa 172,20,80
s_add_u32 s52,s52,s56
s_add_u32 m0,s98,36864
ta 64,234,57344
maaa 156,44,80
ta 66,234,57856
maaa 156,40,4
ta 76,235,57344
maaa 152,40,8
ta 78,235,57856
maaa 152,44,84
dl 224,36,52,0
maaa 168,20,84
ta 72,235,40960
maaa 168,0,8
ta 74,235,41472
maaa 164,0,12
maaa 164,20,88
maaa 148,44,88
buffer_store_dwordx4 v[156:159],v244,s[24:27],s67 offen offset:3072 sc0 sc1
maaa 148,40,12
s_mov_b32 s67,16384
maaa 144,40,16
maaa 144,44,92
maaa 160,20,92
ta 40,236,40960
maaa 160,0,16
dl 225,36,52,1024
s_waitcnt lgkmcnt(3)
maaa 128,52,16
ta 42,236,41472
maaa 128,48,92
ta 44,236,57344
maaa 112,60,92
ta 46,236,57856
maaa 112,56,16
ta 20,237,57344
maaa 116,56,12
ta 22,237,57856
maaa 116,60,88
dl 226,36,52,2048
maaa 132,48,88
ta 0,237,40960
maaa 132,52,12
ta 2,237,41472
maaa 136,52,8
maaa 136,48,84
maaa 120,60,84
dl 227,36,52,3072
maaa 120,56,8
s_add_u32 s52,s52,s60
maaa 124,56,4
maaa 124,60,80
maaa 140,48,80
ta 56,238,45056
ta 58,238,45568
maaa 140,52,4
buffer_store_dwordx4 v[160:163],v244,s[24:27],s67 offen offset:0 sc0 sc1
ta 60,238,61440
ta 62,238,61952
mvaa 40,68,4
ta 48,239,61440
mvaa 40,64,80
ta 50,239,61952
mvaa 44,76,80
ta 52,239,45056
ta 54,239,45568
s_waitcnt lgkmcnt(8)
mvaa 44,72,4
mvaa 52,72,8
mvaa 52,76,84
mvaa 48,64,84
mvaa 48,68,8
mvaa 56,68,12
mvaa 56,64,88
mvaa 60,76,88
mvaa 60,72,12
mvaa 68,72,16
mvaa 68,76,92
mvaa 64,64,92
mvaa 64,68,16
mvaa 32,40,16
s_waitcnt vmcnt(11) lgkmcnt(0)
s_barrier
s_add_u32 m0,s62,32768
mvaa 32,44,92
dl 228,40,53,0
mvaa 36,20,92
ta 72,234,0
mvaa 36,0,16
ta 74,234,512
mvaa 28,0,12
ta 76,234,16384
mvaa 28,20,88
ta 78,234,16896
mvaa 24,44,88
ta 64,235,16384
mvaa 24,40,12
dl 229,40,53,1024
mvaa 16,40,8
s_add_u32 s53,s53,s57
ta 66,235,16896
mvaa 16,44,84
ta 68,235,0
mvaa 20,20,84
ta 70,235,512
mvaa 20,0,8
mvaa 12,0,4
dl 230,44,54,2048
mvaa 12,20,80
mvaa 8,44,80
ta 0,236,0
mvaa 8,40,4
ta 2,236,512
mvaa 76,4,56
ta 20,236,16384
mvaa 76,80,60
dl 231,44,54,3072
mvaa 96,80,48
s_add_u32 m0,s62,49152
s_add_u32 s54,s54,s57
ta 22,236,16896
mvaa 96,4,52
ta 44,237,16384
mvaa 100,8,52
ta 46,237,16896
mvaa 100,84,48
ta 40,237,0
mvaa 80,84,60
dl 228,40,53,0
mvaa 80,8,56
ta 42,237,512
mvaa 84,12,56
ra 80,233,0
mvaa 84,88,60
ra 4,232,0
mvaa 104,88,48
ra 84,233,2048
mvaa 104,12,52
ra 8,232,2048
mvaa 108,16,52
dl 229,40,53,1024
mvaa 108,92,48
s_add_u32 s53,s53,s58
ra 88,233,4096
mvaa 88,92,60
ta 52,234,4096
mvaa 88,16,56
ta 54,234,4608
s_waitcnt lgkmcnt(8)
maaa 236,72,24
ta 48,234,20480
maaa 236,76,96
dl 230,44,54,2048
maaa 220,64,96
ta 50,234,20992
maaa 220,68,24
ta 60,235,20480
maaa 216,68,28
ta 62,235,20992
maaa 216,64,100
ta 56,235,4096
v_accvgpr_read_b32 v0,a236
maaa 232,76,100
dl 231,44,54,3072
maaa 232,72,28
s_add_u32 m0,s98,65536
ta 58,235,4608
s_add_u32 s54,s54,s58
maaa 228,72,32
L_vs10:
s_cbranch_execz L_vs14
ra 12,232,4096
v_accvgpr_read_b32 v1,a237
maaa 228,76,104
ra 92,233,6144
v_accvgpr_read_b32 v2,a238
maaa 212,64,104
ra 16,232,6144
v_accvgpr_read_b32 v3,a239
maaa 212,68,32
dl 224,36,52,0
maaa 208,68,36
v_accvgpr_read_b32 v4,a220
v_accvgpr_read_b32 v5,a221
maaa 208,64,108
v_accvgpr_read_b32 v6,a222
v_accvgpr_read_b32 v7,a223
maaa 224,76,108
ta 68,236,4096
v_accvgpr_read_b32 v92,a216
maaa 224,72,36
ta 70,236,4608
v_accvgpr_read_b32 v93,a217
maaa 192,0,36
dl 225,36,52,1024
maaa 192,20,108
ta 64,236,20480
v_accvgpr_read_b32 v94,a218
maaa 176,44,108
ta 66,236,20992
v_accvgpr_read_b32 v95,a219
s_waitcnt lgkmcnt(6)
maaa 176,40,36
ta 76,237,20480
v_accvgpr_read_b32 v72,a232
maaa 180,40,32
ta 78,237,20992
v_accvgpr_read_b32 v73,a233
maaa 180,44,104
ta 72,237,4096
v_accvgpr_read_b32 v74,a234
maaa 196,20,104
s_mov_b64 exec,3
global_load_dword a250,v253,s[96:97]
s_mov_b64 exec,-1
maaa 196,0,32
ta 74,237,4608
v_accvgpr_read_b32 v75,a235
maaa 200,0,28
maaa 200,20,100
maaa 184,44,100
maaa 184,40,28
buffer_store_dwordx4 v[164:167],v244,s[24:27],s67 offen offset:1024 sc0 sc1
maaa 188,40,24
maaa 188,44,96
maaa 204,20,96
ta 40,234,8192
maaa 204,0,24
ta 42,234,8704
maaa 172,52,24
dl 226,36,52,2048
maaa 172,48,96
ta 44,234,24576
maaa 156,60,96
ta 46,234,25088
maaa 156,56,24
ta 20,235,24576
maaa 152,56,28
ta 22,235,25088
maaa 152,60,100
dl 227,36,52,3072
maaa 168,48,100
s_add_u32 s52,s52,s56
s_add_u32 m0,s98,69632
ta 0,235,8192
maaa 168,52,28
ta 2,235,8704
maaa 164,52,32
maaa 164,48,104
maaa 148,60,104
maaa 148,56,32
buffer_store_dwordx4 v[168:171],v244,s[24:27],s67 offen offset:2048 sc0 sc1
maaa 144,56,36
maaa 144,60,108
maaa 160,48,108
ta 56,236,8192
maaa 160,52,36
ta 58,236,8704
s_waitcnt lgkmcnt(4)
maaa 128,68,36
dl 224,36,52,0
maaa 128,64,108
ta 60,236,24576
maaa 112,76,108
ta 62,236,25088
maaa 112,72,36
ta 48,237,24576
maaa 116,72,32
ta 50,237,25088
maaa 116,76,104
dl 225,36,52,1024
maaa 132,64,104
ta 52,237,8192
maaa 132,68,32
ta 54,237,8704
maaa 136,68,28
maaa 136,64,100
maaa 120,76,100
dl 226,36,52,2048
maaa 120,72,28
maaa 124,72,24
maaa 124,76,96
maaa 140,64,96
ta 72,238,12288
ta 74,238,12800
maaa 140,68,24
ta 76,238,28672
ta 78,238,29184
mvaa 40,40,24
buffer_store_dwordx4 v[172:175],v244,s[24:27],s67 offen offset:3072 sc0 sc1
ta 64,239,28672
mvaa 40,44,96
s_mov_b32 s67,20480
ta 66,239,29184
mvaa 44,20,96
ta 68,239,12288
ta 70,239,12800
s_waitcnt lgkmcnt(8)
mvaa 44,0,24
mvaa 52,0,28
mvaa 52,20,100
dl 227,36,52,3072
mvaa 48,44,100
s_add_u32 s52,s52,s60
mvaa 48,40,28
mvaa 56,40,32
mvaa 56,44,104
mvaa 60,20,104
mvaa 60,0,32
mvaa 68,0,36
mvaa 68,20,108
mvaa 64,44,108
mvaa 64,40,36
mvaa 32,56,36
s_waitcnt vmcnt(12) lgkmcnt(0)
s_barrier
s_add_u32 m0,s62,0
mvaa 32,60,108
dl 228,40,53,0
mvaa 36,48,108
ta 0,234,32768
mvaa 36,52,36
ta 2,234,33280
mvaa 28,52,32
ta 20,234,49152
mvaa 28,48,104
ta 22,234,49664
mvaa 24,60,104
ta 44,235,49152
mvaa 24,56,32
dl 229,40,53,1024
mvaa 16,56,28
s_add_u32 s53,s53,s57
ta 46,235,49664
mvaa 16,60,100
ta 40,235,32768
mvaa 20,48,100
ta 42,235,33280
mvaa 20,52,28
ta 236,236,36864
mvaa 12,52,24
dl 230,44,54,2048
mvaa 12,48,96
ta 238,236,37376
mvaa 8,60,96
ta 52,236,32768
mvaa 8,56,24
ta 54,236,33280
mvaa 76,24,72
ta 48,236,49152
mvaa 76,96,76
ta 50,236,49664
mvaa 96,96,64
dl 231,44,54,3072
mvaa 96,24,68
s_add_u32 m0,s62,16384
s_add_u32 s54,s54,s57
ta 60,237,49152
mvaa 100,28,68
ta 62,237,49664
mvaa 100,100,64
ta 56,237,32768
mvaa 80,100,76
ta 58,237,33280
mvaa 80,28,72
dl 228,40,53,0
mvaa 84,32,72
ta 220,236,53248
mvaa 84,104,76
ta 222,236,53760
mvaa 104,104,64
ta 232,237,53248
mvaa 104,32,68
ta 234,237,53760
mvaa 108,36,68
dl 229,40,53,1024
mvaa 108,108,64
s_add_u32 s53,s53,s58
ta 216,237,36864
mvaa 88,108,76
ta 68,234,36864
mvaa 88,36,72
ta 70,234,37376
s_waitcnt lgkmcnt(8)
mvaa 0,0,4
ta 64,234,53248
mvaa 0,20,80
dl 230,44,54,2048
mvaa 4,44,80
ta 66,234,53760
mvaa 4,40,4
ta 76,235,53248
mvaa 92,40,8
ta 78,235,53760
mvaa 92,44,84
ta 72,235,36864
mvaa 72,20,84
ta 74,235,37376
mvaa 72,0,8
dl 231,44,54,3072
maaa 228,0,12
s_add_u32 m0,s98,0
ta 218,237,37376
s_add_u32 s54,s54,s58
maaa 228,20,88
ra 96,233,32768
maaa 212,44,88
ra 24,232,32768
maaa 212,40,12
ra 100,233,34816
maaa 208,40,16
dl 224,36,52,0
maaa 208,44,92
ra 28,232,34816
v_accvgpr_read_b32 v144,a228
maaa 224,20,92
ra 104,233,36864
v_accvgpr_read_b32 v145,a229
maaa 224,0,16
ra 32,232,36864
v_accvgpr_read_b32 v146,a230
maaa 192,52,16
ra 36,232,38912
v_accvgpr_read_b32 v147,a231
maaa 192,48,92
dl 225,36,52,1024
maaa 176,60,92
ra 108,233,38912
v_accvgpr_read_b32 v148,a212
s_waitcnt lgkmcnt(8)
maaa 176,56,16
ta 40,234,40960
v_accvgpr_read_b32 v149,a213
maaa 180,56,12
ta 42,234,41472
v_accvgpr_read_b32 v150,a214
maaa 180,60,88
ta 44,234,57344
v_accvgpr_read_b32 v151,a215
maaa 196,48,88
buffer_store_dwordx4 v[176:179],v244,s[24:27],s67 offen offset:0 sc0 sc1
maaa 196,52,12
ta 46,234,57856
v_accvgpr_read_b32 v156,a208
maaa 200,52,8
ta 20,235,57344
v_accvgpr_read_b32 v157,a209
maaa 200,48,84
ta 22,235,57856
v_accvgpr_read_b32 v158,a210
maaa 184,60,84
ta 0,235,40960
v_accvgpr_read_b32 v159,a211
maaa 184,56,8
ta 2,235,41472
v_accvgpr_read_b32 v152,a224
maaa 188,56,4
dl 226,36,52,2048
maaa 188,60,80
v_accvgpr_read_b32 v153,a225
v_accvgpr_read_b32 v154,a226
maaa 204,48,80
ta 56,236,40960
v_accvgpr_read_b32 v155,a227
maaa 204,52,4
ta 58,236,41472
v_add_u32_e32 v233,0x10000,v233
maaa 172,68,4
ta 60,236,57344
v_add_u32_e32 v232,0x10000,v232
maaa 172,64,80
dl 227,36,52,3072
maaa 156,76,80
s_add_u32 s52,s52,s56
s_add_u32 m0,s98,4096
ta 62,236,57856
maaa 156,72,4
ta 48,237,57344
maaa 152,72,8
ta 50,237,57856
maaa 152,76,84
ta 52,237,40960
maaa 168,64,84
dl 224,36,52,0
maaa 168,68,8
ta 54,237,41472
maaa 164,68,12
maaa 164,64,88
maaa 148,76,88
maaa 148,72,12
buffer_store_dwordx4 v[180:183],v244,s[24:27],s67 offen offset:1024 sc0 sc1
maaa 144,72,16
maaa 144,76,92
maaa 160,64,92
ta 72,238,45056
maaa 160,68,16
ta 74,238,45568
maaa 128,236,16
dl 225,36,52,1024
maaa 128,220,92
ta 76,238,61440
maaa 112,232,92
ta 78,238,61952
maaa 112,216,16
ta 64,239,61440
maaa 116,216,12
ta 66,239,61952
maaa 116,232,88
ta 68,239,45056
maaa 132,220,88
dl 226,36,52,2048
maaa 132,236,12
ta 70,239,45568
maaa 136,236,8
maaa 136,220,84
maaa 120,232,84
maaa 120,216,8
dl 227,36,52,3072
maaa 124,216,4
s_add_u32 s52,s52,s60
maaa 124,232,80
maaa 140,220,80
maaa 140,236,4
s_waitcnt lgkmcnt(1)
mvaa 40,40,4
buffer_store_dwordx4 v[184:187],v244,s[24:27],s67 offen offset:2048 sc0 sc1
mvaa 40,44,80
mvaa 44,20,80
mvaa 44,0,4
mvaa 52,0,8
mvaa 52,20,84
mvaa 48,44,84
mvaa 48,40,8
mvaa 56,40,12
mvaa 56,44,88
mvaa 60,20,88
mvaa 60,0,12
mvaa 68,0,16
mvaa 68,20,92
mvaa 64,44,92
mvaa 64,40,16
mvaa 32,56,16
s_waitcnt vmcnt(11) lgkmcnt(0)
s_barrier
s_add_u32 m0,s62,32768
mvaa 32,60,92
dl 228,40,53,0
mvaa 36,48,92
ta 216,234,0
mvaa 36,52,16
ta 218,234,512
mvaa 28,52,12
ta 232,234,16384
mvaa 28,48,88
ta 234,234,16896
mvaa 24,60,88
dl 229,40,53,1024
mvaa 24,56,12
s_add_u32 s53,s53,s57
ta 220,235,16384
mvaa 16,56,8
ta 222,235,16896
mvaa 16,60,84
ta 236,235,0
mvaa 20,48,84
ta 238,235,512
mvaa 20,52,8
dl 230,44,54,2048
mvaa 12,52,4
ta 0,236,0
mvaa 12,48,80
ta 2,236,512
mvaa 8,60,80
ta 20,236,16384
mvaa 8,56,4
ta 22,236,16896
mvaa 76,4,72
dl 231,44,54,3072
mvaa 76,80,76
s_add_u32 m0,s62,49152
s_add_u32 s54,s54,s57
ta 44,237,16384
mvaa 96,80,64
ta 46,237,16896
mvaa 96,4,68
ta 40,237,0
mvaa 100,8,68
ta 42,237,512
mvaa 100,84,64
dl 228,40,53,0
mvaa 80,84,76
ta 52,234,4096
mvaa 80,8,72
ta 54,234,4608
mvaa 84,12,72
ta 48,234,20480
mvaa 84,88,76
ta 50,234,20992
mvaa 104,88,64
dl 229,40,53,1024
mvaa 104,12,68
s_add_u32 s53,s53,s58
ta 60,235,20480
mvaa 108,16,68
ta 62,235,20992
mvaa 108,92,64
ta 56,235,4096
mvaa 88,92,76
ta 58,235,4608
mvaa 88,16,72
dl 230,44,54,2048
s_waitcnt lgkmcnt(8)
mvaa 0,216,24
ta 228,234,8192
mvaa 0,232,96
ta 68,236,4096
mvaa 4,220,96
ta 70,236,4608
mvaa 4,236,24
ta 64,236,20480
mvaa 92,236,28
ta 66,236,20992
mvaa 92,220,100
dl 231,44,54,3072
mvaa 72,232,100
s_add_u32 m0,s98,32768
ta 76,237,20480
s_add_u32 s54,s54,s58
mvaa 72,216,28
ta 78,237,20992
mvaa 144,216,32
ta 72,237,4096
mvaa 144,232,104
ta 74,237,4608
mvaa 148,220,104
dl 224,36,52,0
mvaa 148,236,32
ta 230,234,8704
mvaa 156,236,36
ta 212,234,24576
mvaa 156,220,108
ta 214,234,25088
mvaa 152,232,108
ta 224,235,24576
mvaa 152,216,36
dl 225,36,52,1024
maaa 192,0,36
ta 226,235,25088
maaa 192,20,108
ta 208,235,8192
maaa 176,44,108
ra 80,233,0
maaa 176,40,36
ta 210,235,8704
maaa 180,40,32
dl 226,36,52,2048
maaa 180,44,104
ra 4,232,0
v_accvgpr_read_b32 v160,a192
maaa 196,20,104
ra 84,233,2048
v_accvgpr_read_b32 v161,a193
maaa 196,0,32
ra 8,232,2048
v_accvgpr_read_b32 v162,a194
maaa 200,0,28
ra 12,232,4096
v_accvgpr_read_b32 v163,a195
maaa 200,20,100
buffer_store_dwordx4 v[188:191],v244,s[24:27],s67 offen offset:3072 sc0 sc1
maaa 184,44,100
s_mov_b32 s67,24576
ra 88,233,4096
v_accvgpr_read_b32 v164,a176
maaa 184,40,28
ra 92,233,6144
v_accvgpr_read_b32 v165,a177
maaa 188,40,24
ra 16,232,6144
v_accvgpr_read_b32 v166,a178
maaa 188,44,96
ta 236,236,8192
v_accvgpr_read_b32 v167,a179
maaa 204,20,96
ta 238,236,8704
v_accvgpr_read_b32 v172,a180
maaa 204,0,24
dl 227,36,52,3072
s_waitcnt lgkmcnt(8)
maaa 172,52,24
s_add_u32 s52,s52,s56
s_add_u32 m0,s98,36864
ta 220,236,24576
maaa 172,48,96
L_vs11:
s_cbranch_execz L_vs15
ta 222,236,25088
v_accvgpr_read_b32 v173,a181
maaa 156,60,96
ta 232,237,24576
v_accvgpr_read_b32 v174,a182
maaa 156,56,24
ta 234,237,25088
v_accvgpr_read_b32 v175,a183
maaa 152,56,28
dl 224,36,52,0
maaa 152,60,100
ta 216,237,8192
v_accvgpr_read_b32 v168,a196
maaa 168,48,100
ta 218,237,8704
v_accvgpr_read_b32 v169,a197
maaa 168,52,28
ta 40,238,12288
v_accvgpr_read_b32 v170,a198
maaa 164,52,32
ta 42,238,12800
v_accvgpr_read_b32 v171,a199
maaa 164,48,104
dl 225,36,52,1024
maaa 148,60,104
ta 44,238,28672
v_add_u32_e32 v233,0xffff0000,v233
maaa 148,56,32
ta 46,238,29184
v_add_u32_e32 v232,0xffff0000,v232
maaa 144,56,36
ta 20,239,28672
maaa 144,60,108
ta 22,239,29184
maaa 160,48,108
buffer_store_dwordx4 v[192:195],v244,s[24:27],s67 offen offset:0 sc0 sc1
maaa 160,52,36
ta 0,239,12288
maaa 128,68,36
ta 2,239,12800
maaa 128,64,108
maaa 112,76,108
maaa 112,72,36
maaa 116,72,32
dl 226,36,52,2048
maaa 116,76,104
maaa 132,64,104
maaa 132,68,32
maaa 136,68,28
maaa 136,64,100
dl 227,36,52,3072
maaa 120,76,100
s_add_u32 s52,s52,s60
maaa 120,72,28
maaa 124,72,24
maaa 124,76,96
maaa 140,64,96
buffer_store_dwordx4 v[196:199],v244,s[24:27],s67 offen offset:1024 sc0 sc1
maaa 140,68,24
mvaa 40,228,24
mvaa 40,212,96
mvaa 44,224,96
mvaa 44,208,24
mvaa 52,208,28
mvaa 52,224,100
mvaa 48,212,100
mvaa 48,228,28
mvaa 56,228,32
mvaa 56,212,104
mvaa 60,224,104
mvaa 60,208,32
mvaa 68,208,36
mvaa 68,224,108
mvaa 64,212,108
mvaa 64,228,36
s_waitcnt lgkmcnt(0)
mvaa 32,236,36
s_waitcnt vmcnt(11)
s_barrier
s_add_u32 m0,s62,0
mvaa 32,220,108
dl 228,40,53,0
mvaa 36,232,108
ta 56,234,32768
mvaa 36,216,36
ta 58,234,33280
mvaa 28,216,32
ta 60,234,49152
mvaa 28,232,104
ta 62,234,49664
mvaa 24,220,104
dl 229,40,53,1024
mvaa 24,236,32
s_add_u32 s53,s53,s57
ta 48,235,49152
mvaa 16,236,28
ta 50,235,49664
mvaa 16,220,100
ta 52,235,32768
mvaa 20,232,100
ta 54,235,33280
mvaa 20,216,28
dl 230,44,54,2048
mvaa 12,216,24
ta 72,236,32768
mvaa 12,232,96
ta 74,236,33280
mvaa 8,220,96
ta 76,236,49152
mvaa 8,236,24
ta 78,236,49664
mvaa 76,24,40
dl 231,44,54,3072
mvaa 76,96,44
s_add_u32 m0,s62,16384
s_add_u32 s54,s54,s57
ta 64,237,49152
mvaa 96,96,20
ta 66,237,49664
mvaa 96,24,0
ta 68,237,32768
mvaa 100,28,0
ta 70,237,33280
mvaa 100,100,20
dl 228,40,53,0
mvaa 80,100,44
ta 208,234,36864
mvaa 80,28,40
ta 210,234,37376
mvaa 84,32,40
ta 224,234,53248
mvaa 84,104,44
ta 226,234,53760
mvaa 104,104,20
ta 212,235,53248
mvaa 104,32,0
dl 229,40,53,1024
mvaa 108,36,0
s_add_u32 s53,s53,s58
ta 214,235,53760
mvaa 108,108,20
ta 228,235,36864
mvaa 88,108,44
ta 230,235,37376
mvaa 88,36,40
ta 192,236,40960
s_waitcnt lgkmcnt(9)
mvaa 0,56,4
dl 230,44,54,2048
mvaa 0,60,80
ta 216,236,36864
mvaa 4,48,80
ta 218,236,37376
mvaa 4,52,4
ta 232,236,53248
mvaa 92,52,8
ta 234,236,53760
mvaa 92,48,84
dl 231,44,54,3072
mvaa 72,60,84
s_add_u32 m0,s98,65536
ta 220,237,53248
s_add_u32 s54,s54,s58
mvaa 72,56,8
ta 222,237,53760
mvaa 144,56,12
ta 236,237,36864
mvaa 144,60,88
ta 238,237,37376
mvaa 148,48,88
dl 224,36,52,0
mvaa 148,52,12
ta 194,236,41472
mvaa 156,52,16
ta 176,236,57344
mvaa 156,48,92
ta 178,236,57856
mvaa 152,60,92
ta 196,237,57344
mvaa 152,56,16
ta 198,237,57856
mvaa 160,72,16
dl 225,36,52,1024
mvaa 160,76,92
ta 180,237,40960
mvaa 164,64,92
ta 182,237,41472
mvaa 164,68,16
ra 96,233,0
mvaa 172,68,12
ta 0,234,40960
mvaa 172,64,88
dl 226,36,52,2048
mvaa 168,76,88
ta 2,234,41472
mvaa 168,72,12
ta 20,234,57344
maaa 200,72,8
ta 22,234,57856
maaa 200,76,84
ta 44,235,57344
maaa 184,64,84
buffer_store_dwordx4 v[200:203],v244,s[24:27],s67 offen offset:2048 sc0 sc1
maaa 184,68,8
ta 46,235,57856
maaa 188,68,4
ta 40,235,40960
maaa 188,64,80
ta 42,235,41472
v_accvgpr_read_b32 v176,a200
maaa 204,76,80
ra 24,232,0
v_accvgpr_read_b32 v177,a201
maaa 204,72,4
dl 227,36,52,3072
s_waitcnt lgkmcnt(8)
maaa 172,208,4
s_add_u32 s52,s52,s56
s_add_u32 m0,s98,69632
ra 100,233,2048
maaa 172,224,80
ra 28,232,2048
v_accvgpr_read_b32 v178,a202
maaa 156,212,80
ra 32,232,4096
v_accvgpr_read_b32 v179,a203
maaa 156,228,4
ra 104,233,4096
v_accvgpr_read_b32 v180,a184
maaa 152,228,8
ra 108,233,6144
v_accvgpr_read_b32 v181,a185
maaa 152,212,84
dl 224,36,52,0
maaa 168,224,84
ra 36,232,6144
v_accvgpr_read_b32 v182,a186
maaa 168,208,8
ta 52,238,45056
v_accvgpr_read_b32 v183,a187
maaa 164,208,12
ta 54,238,45568
v_accvgpr_read_b32 v188,a188
maaa 164,224,88
ta 48,238,61440
v_accvgpr_read_b32 v189,a189
maaa 148,212,88
buffer_store_dwordx4 v[204:207],v244,s[24:27],s67 offen offset:3072 sc0 sc1
maaa 148,228,12
s_mov_b32 s67,28672
ta 50,238,61952
v_accvgpr_read_b32 v190,a190
maaa 144,228,16
ta 60,239,61440
v_accvgpr_read_b32 v184,a204
maaa 144,212,92
ta 62,239,61952
v_accvgpr_read_b32 v191,a191
maaa 160,224,92
ta 56,239,45056
v_accvgpr_read_b32 v185,a205
maaa 160,208,16
dl 225,36,52,1024
maaa 128,216,16
ta 58,239,45568
v_accvgpr_read_b32 v186,a206
maaa 128,232,92
v_accvgpr_read_b32 v187,a207
maaa 112,220,92
maaa 112,236,16
maaa 116,236,12
dl 226,36,52,2048
maaa 116,220,88
maaa 132,232,88
maaa 132,216,12
maaa 136,216,8
maaa 136,232,84
)"
R"(dl 227,36,52,3072
maaa 120,220,84
s_add_u32 s52,s52,s60
maaa 120,236,8
maaa 124,236,4
maaa 124,220,80
maaa 140,232,80
maaa 140,216,4
buffer_store_dwordx4 v[208:211],v244,s[24:27],s67 offen offset:0 sc0 sc1
s_waitcnt lgkmcnt(0)
mvaa 40,0,4
mvaa 40,20,80
mvaa 44,44,80
mvaa 44,40,4
mvaa 52,40,8
mvaa 52,44,84
mvaa 48,20,84
mvaa 48,0,8
mvaa 56,0,12
mvaa 56,20,88
mvaa 60,44,88
mvaa 60,40,12
mvaa 68,40,16
mvaa 68,44,92
mvaa 64,20,92
mvaa 64,0,16
mvaa 32,192,16
s_waitcnt vmcnt(11)
s_barrier
s_add_u32 m0,s62,32768
mvaa 32,176,92
dl 228,40,53,0
mvaa 36,196,92
ta 68,234,0
mvaa 36,180,16
ta 70,234,512
mvaa 28,180,12
ta 64,234,16384
mvaa 28,196,88
ta 66,234,16896
mvaa 24,176,88
dl 229,40,53,1024
mvaa 24,192,12
s_add_u32 s53,s53,s57
ta 76,235,16384
mvaa 16,192,8
ta 78,235,16896
mvaa 16,176,84
ta 72,235,0
mvaa 20,196,84
ta 74,235,512
mvaa 20,180,8
ta 228,236,0
mvaa 12,180,4
dl 230,44,54,2048
mvaa 12,196,80
ta 230,236,512
mvaa 8,176,80
ta 212,236,16384
mvaa 8,192,4
ta 214,236,16896
mvaa 76,4,52
ta 224,237,16384
mvaa 76,80,48
dl 231,44,54,3072
mvaa 96,80,60
s_add_u32 m0,s62,49152
s_add_u32 s54,s54,s57
ta 226,237,16896
mvaa 96,4,56
ta 208,237,0
mvaa 100,8,56
ta 210,237,512
mvaa 100,84,60
ta 236,234,4096
mvaa 80,84,48
dl 228,40,53,0
mvaa 80,8,52
ta 238,234,4608
mvaa 84,12,52
ta 220,234,20480
mvaa 84,88,48
ta 222,234,20992
mvaa 104,88,60
ta 232,235,20480
mvaa 104,12,56
dl 229,40,53,1024
mvaa 108,16,56
s_add_u32 s53,s53,s58
ta 234,235,20992
mvaa 108,92,60
ta 216,235,4096
mvaa 88,92,48
ta 218,235,4608
mvaa 88,16,52
ta 40,236,4096
s_waitcnt lgkmcnt(8)
mvaa 0,68,24
dl 230,44,54,2048
mvaa 0,64,96
ta 42,236,4608
mvaa 4,76,96
ta 44,236,20480
mvaa 4,72,24
ta 46,236,20992
mvaa 92,72,28
ta 20,237,20480
mvaa 92,76,100
ta 22,237,20992
mvaa 72,64,100
dl 231,44,54,3072
mvaa 72,68,28
s_add_u32 m0,s98,0
ta 200,238,12288
s_add_u32 s54,s54,s58
mvaa 144,68,32
ta 202,238,12800
mvaa 144,64,104
ta 0,237,4096
mvaa 148,76,104
ta 2,237,4608
mvaa 148,72,32
dl 224,36,52,0
mvaa 156,72,36
ta 184,238,28672
mvaa 156,76,108
ta 186,238,29184
mvaa 152,64,108
ta 204,239,28672
mvaa 152,68,36
ta 206,239,29184
mvaa 160,228,36
dl 225,36,52,1024
mvaa 160,212,108
ta 188,239,12288
mvaa 164,224,108
ta 190,239,12800
mvaa 164,208,36
ta 180,234,8192
mvaa 172,208,32
ta 182,234,8704
mvaa 172,224,104
dl 226,36,52,2048
mvaa 168,212,104
ta 196,234,24576
mvaa 168,228,32
ta 198,234,25088
mvaa 176,228,28
ta 176,235,24576
mvaa 176,212,100
ta 178,235,25088
mvaa 180,224,100
ta 192,235,8192
mvaa 180,208,28
buffer_store_dwordx4 v[212:215],v244,s[24:27],s67 offen offset:1024 sc0 sc1
mvaa 188,208,24
ta 194,235,8704
mvaa 188,224,96
ra 4,232,32768
mvaa 184,212,96
ra 80,233,32768
mvaa 184,228,24
ra 84,233,34816
s_waitcnt lgkmcnt(9)
maaa 172,236,24
dl 227,36,52,3072
maaa 172,220,96
s_add_u32 s52,s52,s56
s_add_u32 m0,s98,4096
ra 8,232,34816
maaa 156,232,96
ra 88,233,36864
maaa 156,216,24
ta 56,236,8192
maaa 152,216,28
ra 12,232,36864
v_accvgpr_read_b32 v192,a172
maaa 152,232,100
dl 224,36,52,0
maaa 168,220,100
ta 58,236,8704
v_accvgpr_read_b32 v193,a173
maaa 168,236,28
ta 60,236,24576
v_accvgpr_read_b32 v194,a174
maaa 164,236,32
ta 62,236,25088
v_accvgpr_read_b32 v195,a175
maaa 164,220,104
ta 48,237,24576
v_accvgpr_read_b32 v196,a156
maaa 148,232,104
buffer_store_dwordx4 v[216:219],v244,s[24:27],s67 offen offset:2048 sc0 sc1
maaa 148,216,32
ta 50,237,25088
v_accvgpr_read_b32 v197,a157
maaa 144,216,36
ra 92,233,38912
v_accvgpr_read_b32 v198,a158
maaa 144,232,108
ra 16,232,38912
v_accvgpr_read_b32 v199,a159
maaa 160,220,108
ta 52,237,8192
v_accvgpr_read_b32 v204,a152
maaa 160,236,36
ta 54,237,8704
v_accvgpr_read_b32 v205,a153
maaa 128,40,36
dl 225,36,52,1024
maaa 128,44,108
v_accvgpr_read_b32 v206,a154
v_accvgpr_read_b32 v207,a155
maaa 112,20,108
v_accvgpr_read_b32 v200,a168
v_accvgpr_read_b32 v201,a169
maaa 112,0,36
v_accvgpr_read_b32 v202,a170
v_accvgpr_read_b32 v203,a171
maaa 116,0,32
v_add_u32_e32 v233,0x10000,v233
v_add_u32_e32 v232,0x10000,v232
maaa 116,20,104
dl 226,36,52,2048
maaa 132,44,104
maaa 132,40,32
maaa 136,40,28
maaa 136,44,100
maaa 120,20,100
dl 227,36,52,3072
maaa 120,0,28
s_add_u32 s52,s52,s60
maaa 124,0,24
maaa 124,20,96
maaa 140,44,96
maaa 140,40,24
buffer_store_dwordx4 v[220:223],v244,s[24:27],s67 offen offset:3072 sc0 sc1
mvaa 40,180,24
s_mov_b32 s67,0x5550055
s_mov_b32 s26,2097152
v_cndmask_b32_e64 v116,v241,0,s[66:67]
s_waitcnt lgkmcnt(0)
mvaa 40,196,96
s_mov_b32 s66,0xa0000
s_mov_b32 s67,0xaaa00aa
v_cndmask_b32_e64 v117,v241,0,s[66:67]
mvaa 44,176,96
s_mov_b32 s66,0x150001
s_mov_b32 s67,0x15550155
v_cndmask_b32_e64 v118,v241,0,s[66:67]
mvaa 44,192,24
L_vs12:
s_cbranch_execz L_vr0
s_mov_b32 s66,0x2a0002
s_mov_b32 s67,0x2aaa02aa
v_cndmask_b32_e64 v119,v241,0,s[66:67]
mvaa 52,192,28
s_lshr_b32 s66,s18,2
s_lshl_b32 s67,s18,13
s_lshl_b32 s66,s66,13
mvaa 52,176,100
s_mov_b32 s27,131072
v_cndmask_b32_e64 v120,v116,v241,s[64:65]
v_cndmask_b32_e64 v121,v117,v241,s[64:65]
mvaa 48,196,100
s_mov_b64 s[24:25],s[82:83]
v_cndmask_b32_e64 v122,v118,v241,s[64:65]
v_cndmask_b32_e64 v123,v119,v241,s[64:65]
mvaa 48,180,28
s_mov_b32 s83,0
s_or_b32 s82,s15,16384
mvaa 56,180,32
mvaa 56,196,104
mvaa 60,176,104
mvaa 60,192,32
mvaa 68,192,36
mvaa 68,176,108
mvaa 64,196,108
mvaa 64,180,36
mvaa 32,56,36
s_waitcnt vmcnt(11)
s_barrier
s_add_u32 m0,s62,0
mvaa 32,60,108
dl 228,40,53,0
mvaa 36,48,108
ta 72,234,32768
mvaa 36,52,36
ta 74,234,33280
mvaa 28,52,32
ta 76,234,49152
mvaa 28,48,104
ta 78,234,49664
mvaa 24,60,104
ta 64,235,49152
mvaa 24,56,32
dl 229,40,53,1024
mvaa 16,56,28
s_add_u32 s53,s53,s57
ta 66,235,49664
mvaa 16,60,100
ta 68,235,32768
mvaa 20,48,100
ta 70,235,33280
mvaa 20,52,28
ta 208,236,32768
mvaa 12,52,24
dl 230,44,54,2048
mvaa 12,48,96
ta 210,236,33280
mvaa 8,60,96
ta 224,236,49152
mvaa 8,56,24
ta 226,236,49664
mvaa 76,24,200
ta 212,237,49152
mvaa 76,96,184
dl 231,44,54,3072
mvaa 96,96,204
s_add_u32 m0,s62,16384
s_add_u32 s54,s54,s57
ta 214,237,49664
mvaa 96,24,188
ta 228,237,32768
mvaa 100,28,188
ta 230,237,33280
mvaa 100,100,204
ta 216,234,36864
mvaa 80,100,184
ta 218,234,37376
mvaa 80,28,200
dl 228,40,53,0
mvaa 84,32,200
ta 232,234,53248
mvaa 84,104,184
ta 234,234,53760
mvaa 104,104,204
ta 220,235,53248
mvaa 104,32,188
ta 222,235,53760
mvaa 108,36,188
dl 229,40,53,1024
mvaa 108,108,204
s_add_u32 s53,s53,s58
ta 236,235,36864
mvaa 88,108,184
ta 238,235,37376
mvaa 88,36,200
ta 0,236,36864
s_waitcnt lgkmcnt(8)
mvaa 0,72,4
ta 2,236,37376
mvaa 0,76,80
dl 230,44,54,2048
mvaa 4,64,80
ta 20,236,53248
mvaa 4,68,4
ta 22,236,53760
mvaa 92,68,8
ta 44,237,53248
mvaa 92,64,84
ta 46,237,53760
mvaa 72,76,84
dl 231,44,54,3072
mvaa 72,72,8
s_add_u32 m0,s98,32768
ta 40,237,36864
s_add_u32 s54,s54,s58
mvaa 144,72,12
ta 42,237,37376
mvaa 144,76,88
ta 192,234,40960
mvaa 148,64,88
ta 194,234,41472
mvaa 148,68,12
ta 176,234,57344
mvaa 156,68,16
dl 224,36,52,0
mvaa 156,64,92
ta 178,234,57856
mvaa 152,76,92
ta 196,235,57344
mvaa 152,72,16
ta 198,235,57856
mvaa 160,208,16
ta 180,235,40960
mvaa 160,224,92
dl 225,36,52,1024
mvaa 164,212,92
ta 182,235,41472
mvaa 164,228,16
ta 52,236,40960
mvaa 172,228,12
ta 54,236,41472
mvaa 172,212,88
ta 48,236,57344
mvaa 168,224,88
s_mov_b64 exec,3
global_load_dword a250,v253,s[96:97]
s_mov_b64 exec,-1
mvaa 168,208,12
ta 50,236,57856
v_lshrrev_b32_e32 v253,1,v244
mvaa 176,208,8
ta 60,237,57344
mvaa 176,224,84
ta 62,237,57856
mvaa 180,212,84
ta 56,237,40960
mvaa 180,228,8
dl 226,36,52,2048
mvaa 188,228,4
ta 58,237,41472
mvaa 188,212,80
ra 96,233,0
mvaa 184,224,80
ra 24,232,0
mvaa 184,208,4
ra 100,233,2048
s_waitcnt lgkmcnt(8)
mvaa 192,216,4
dl 227,36,52,3072
mvaa 192,232,80
s_add_u32 s52,s52,s56
s_add_u32 m0,s98,36864
ra 28,232,2048
mvaa 196,220,80
ra 104,233,4096
mvaa 196,236,4
ra 32,232,4096
mvaa 204,236,8
ta 188,238,45056
mvaa 204,220,84
ta 190,238,45568
mvaa 200,232,84
dl 224,36,52,0
mvaa 200,216,8
ta 204,238,61440
maaa 164,216,12
ta 206,238,61952
maaa 164,232,88
ta 184,239,61440
maaa 148,220,88
ta 186,239,61952
maaa 148,236,12
dl 225,36,52,1024
maaa 144,236,16
ra 108,233,6144
v_add_u32_e32 v233,0xffff0000,v233
maaa 144,220,92
ra 36,232,6144
v_add_u32_e32 v232,0xffff0000,v232
maaa 160,232,92
ta 200,239,45056
v_accvgpr_read_b32 v208,a164
maaa 160,216,16
ta 202,239,45568
v_accvgpr_read_b32 v209,a165
maaa 128,0,16
dl 226,36,52,2048
maaa 128,20,92
v_accvgpr_read_b32 v210,a166
v_accvgpr_read_b32 v211,a167
maaa 112,44,92
v_accvgpr_read_b32 v212,a148
v_accvgpr_read_b32 v213,a149
maaa 112,40,16
v_accvgpr_read_b32 v214,a150
v_accvgpr_read_b32 v215,a151
maaa 116,40,12
v_accvgpr_read_b32 v220,a144
v_accvgpr_read_b32 v221,a145
maaa 116,44,88
dl 227,36,52,3072
maaa 132,20,88
s_add_u32 s52,s52,s60
v_accvgpr_read_b32 v222,a146
v_accvgpr_read_b32 v223,a147
maaa 132,0,12
s_lshl_b32 s60,%1,7
v_accvgpr_read_b32 v216,a160
v_accvgpr_read_b32 v217,a161
maaa 136,0,8
s_and_b32 s87,s60,7552
v_accvgpr_read_b32 v218,a162
v_accvgpr_read_b32 v219,a163
maaa 136,20,84
s_add_u32 s61,s60,83914752
maaa 120,44,84
maaa 120,40,8
maaa 124,40,4
maaa 124,44,80
maaa 140,20,80
maaa 140,0,4
mvaa 40,192,4
mvaa 40,176,80
mvaa 44,196,80
mvaa 44,180,4
mvaa 52,180,8
mvaa 52,196,84
mvaa 48,176,84
mvaa 48,192,8
mvaa 56,192,12
s_waitcnt vmcnt(9) lgkmcnt(0)
s_barrier
mvaa 56,176,88
s_cmp_eq_u32 s12,0
s_cselect_b64 exec,1,0
buffer_atomic_add v247,off,s[8:11],s85 sc1
s_mov_b64 exec,-1
mvaa 60,196,88
s_add_u32 m0,s62,32768
ta 172,234,0
s_lshr_b32 s85,s86,1
mvaa 60,180,12
s_lshl_b32 s85,s85,11
ta 174,234,512
v_bfe_u32 v247,v244,2,6
mvaa 68,180,16
ta 156,234,16384
mvaa 68,196,92
ta 158,234,16896
mvaa 64,176,92
dl 228,40,53,0
mvaa 64,192,16
ta 168,235,16384
mvaa 32,52,16
ta 170,235,16896
mvaa 32,48,92
ta 152,235,0
mvaa 36,60,92
ta 154,235,512
mvaa 36,56,16
dl 229,40,53,1024
mvaa 28,56,12
s_add_u32 s53,s53,s57
ta 68,236,0
mvaa 28,60,88
ta 70,236,512
mvaa 24,48,88
ta 64,236,16384
mvaa 24,52,12
ta 66,236,16896
mvaa 16,52,8
dl 230,44,54,2048
mvaa 16,48,84
ta 76,237,16384
mvaa 20,60,84
ta 78,237,16896
mvaa 20,56,8
ta 72,237,0
mvaa 12,56,4
ta 74,237,512
mvaa 12,60,80
dl 231,44,54,3072
mvaa 8,48,80
s_add_u32 m0,s62,49152
s_add_u32 s54,s54,s57
ta 228,234,4096
mvaa 8,52,4
ta 230,234,4608
mvaa 76,4,188
ta 212,234,20480
mvaa 76,80,204
ta 214,234,20992
mvaa 96,80,184
dl 228,40,53,0
mvaa 96,4,200
ta 224,235,20480
mvaa 100,8,200
ta 226,235,20992
mvaa 100,84,184
ta 208,235,4096
mvaa 80,84,204
ta 210,235,4608
mvaa 80,8,188
dl 229,40,53,1024
mvaa 84,12,188
s_add_u32 s53,s53,s58
ta 236,236,4096
mvaa 84,88,204
ta 238,236,4608
mvaa 104,88,184
ta 220,236,20480
mvaa 104,12,200
ta 222,236,20992
mvaa 108,16,200
dl 230,44,54,2048
mvaa 108,92,184
ta 232,237,20480
mvaa 88,92,204
ta 234,237,20992
mvaa 88,16,188
ta 216,237,4096
s_waitcnt lgkmcnt(8)
mvaa 0,172,24
ta 218,237,4608
mvaa 0,156,96
dl 231,44,54,3072
mvaa 4,168,96
s_add_u32 m0,s98,65536
ta 40,234,8192
s_add_u32 s54,s54,s58
mvaa 4,152,24
ta 42,234,8704
mvaa 92,152,28
ta 44,234,24576
mvaa 92,168,100
ta 46,234,25088
mvaa 72,156,100
dl 224,36,52,0
mvaa 72,172,28
ta 20,235,24576
mvaa 144,172,32
ta 22,235,25088
mvaa 144,156,104
ta 0,235,8192
mvaa 148,168,104
ta 2,235,8704
mvaa 148,152,32
dl 225,36,52,1024
mvaa 156,152,36
ta 180,236,8192
mvaa 156,168,108
ta 182,236,8704
mvaa 152,156,108
ta 196,236,24576
mvaa 152,172,36
ta 198,236,25088
mvaa 160,68,36
dl 226,36,52,2048
mvaa 160,64,108
ta 176,237,24576
mvaa 164,76,108
ta 178,237,25088
mvaa 164,72,36
ta 192,237,8192
mvaa 172,72,32
ta 194,237,8704
mvaa 172,76,104
dl 227,36,52,3072
mvaa 168,64,104
s_add_u32 s52,s52,s56
s_add_u32 m0,s98,69632
ta 56,238,12288
mvaa 168,68,32
s_mov_b32 s98,s94
ta 58,238,12800
s_mov_b32 s99,s98
mvaa 176,68,28
s_and_b32 s94,s12,1
ta 60,238,28672
s_lshl_b32 s94,s94,14
mvaa 176,64,100
s_mul_i32 s56,s59,12288
ta 62,238,29184
mvaa 180,76,100
dl 224,36,52,0
mvaa 180,72,28
ta 48,239,28672
v_mov_b32_e32 v224,v244
mvaa 188,72,24
ta 50,239,29184
mvaa 188,76,96
ta 52,239,12288
mvaa 184,64,96
ta 54,239,12800
mvaa 184,68,24
dl 225,36,52,1024
mvaa 192,228,24
ra 80,233,0
v_add_u32_e32 v225,65536,v224
mvaa 192,212,96
ra 4,232,0
mvaa 196,224,96
ra 84,233,2048
s_waitcnt lgkmcnt(8)
mvaa 196,208,24
ra 8,232,2048
mvaa 204,208,28
dl 226,36,52,2048
mvaa 204,224,100
ra 88,233,4096
v_mov_b32_e32 v226,1065369472
mvaa 200,212,100
ra 12,232,4096
mvaa 200,228,28
ra 16,232,6144
v_add_u32_e32 v232,0x8000,v232
mvaa 208,228,32
ra 92,233,6144
v_add_u32_e32 v233,0x8000,v233
mvaa 208,212,104
dl 227,36,52,3072
mvaa 212,224,104
s_mov_b64 s[36:37],s[28:29]
s_mov_b32 s38,8388608
s_mov_b32 s39,131072
mvaa 212,208,32
s_lshl_b32 s52,s12,12
mvaa 220,208,36
mvaa 220,224,108
mvaa 216,212,108
mvaa 216,228,36
maaa 128,236,36
maaa 128,220,108
maaa 112,232,108
maaa 112,216,36
maaa 116,216,32
maaa 116,232,104
maaa 132,220,104
maaa 132,236,32
maaa 136,236,28
maaa 136,220,100
maaa 120,232,100
maaa 120,216,28
maaa 124,216,24
maaa 124,232,96
maaa 140,220,96
s_waitcnt vmcnt(8) lgkmcnt(0)
s_barrier
s_add_u32 m0,s62,0
maaa 140,236,24
dl 228,40,53,0
mvaa 40,40,24
ta 200,234,32768
mvaa 40,44,96
ta 202,234,33280
mvaa 44,20,96
ta 184,234,49152
mvaa 44,0,24
ta 186,234,49664
mvaa 52,0,28
dl 229,40,53,1024
mvaa 52,20,100
s_add_u32 s53,s53,s57
ta 204,235,49152
mvaa 48,44,100
ta 206,235,49664
mvaa 48,40,28
ta 188,235,32768
mvaa 56,40,32
ta 190,235,33280
mvaa 56,44,104
dl 230,44,54,2048
mvaa 60,20,104
ta 164,236,32768
mvaa 60,0,32
ta 166,236,33280
mvaa 68,0,36
ta 148,236,49152
mvaa 68,20,108
ta 150,236,49664
mvaa 64,44,108
dl 231,44,54,3072
mvaa 64,40,36
s_add_u32 m0,s62,16384
s_add_u32 s54,s54,s57
ta 160,237,49152
mvaa 32,180,36
ta 162,237,49664
mvaa 32,196,108
ta 144,237,32768
mvaa 36,176,108
ta 146,237,33280
mvaa 36,192,36
dl 228,40,53,0
mvaa 28,192,32
ta 152,234,36864
mvaa 28,176,104
ta 154,234,37376
mvaa 24,196,104
ta 168,234,53248
mvaa 24,180,32
ta 170,234,53760
mvaa 16,180,28
dl 229,40,53,1024
mvaa 16,196,100
s_add_u32 s53,s53,s58
ta 156,235,53248
mvaa 20,176,100
ta 158,235,53760
mvaa 20,192,28
ta 172,235,36864
mvaa 12,192,24
L_vs13:
s_cbranch_execz L_vr0
ta 174,235,37376
mvaa 12,176,96
dl 230,44,54,2048
mvaa 8,196,96
ta 208,236,36864
mvaa 8,180,24
ta 210,236,37376
mvaa 76,24,56
ta 224,236,53248
mvaa 76,96,60
ta 226,236,53760
mvaa 96,96,48
dl 231,44,54,3072
mvaa 96,24,52
s_add_u32 s54,s54,s58
ta 212,237,53248
s_add_u32 s58,s56,8192
mvaa 100,28,52
ta 214,237,53760
mvaa 100,100,48
ta 228,237,36864
mvaa 80,100,60
ta 230,237,37376
mvaa 80,28,56
ta 216,234,40960
mvaa 84,32,56
ta 218,234,41472
mvaa 84,104,60
ta 232,234,57344
mvaa 104,104,48
ta 234,234,57856
mvaa 104,32,52
ta 220,235,57344
mvaa 108,36,52
ta 222,235,57856
mvaa 108,108,48
ta 236,235,40960
mvaa 88,108,60
ta 238,235,41472
mvaa 88,36,56
ta 0,236,40960
s_waitcnt lgkmcnt(10)
mvaa 0,200,4
ta 2,236,41472
mvaa 0,184,80
ta 20,236,57344
mvaa 4,204,80
ta 22,236,57856
mvaa 4,188,4
ta 192,237,57344
mvaa 92,188,8
ta 194,237,57856
mvaa 92,204,84
ta 176,237,40960
mvaa 72,184,84
ta 178,237,41472
mvaa 72,200,8
ta 196,238,45056
mvaa 144,200,12
ta 198,238,45568
mvaa 144,184,88
ta 180,238,61440
mvaa 148,204,88
ta 182,238,61952
mvaa 148,188,12
ra 24,232,0
mvaa 156,188,16
ra 28,232,2048
mvaa 156,204,92
ra 32,232,4096
mvaa 152,184,92
ra 36,232,6144
mvaa 152,200,16
ta 188,239,61440
mvaa 160,164,16
ta 190,239,61952
mvaa 160,148,92
ta 204,239,45056
mvaa 164,160,92
ta 206,239,45568
mvaa 164,144,16
mvaa 172,144,12
mvaa 172,160,88
mvaa 168,148,88
mvaa 168,164,12
mvaa 176,164,8
mvaa 176,148,84
mvaa 180,160,84
mvaa 180,144,8
mvaa 188,144,4
mvaa 188,160,80
mvaa 184,148,80
mvaa 184,164,4
mvaa 192,152,4
mvaa 192,168,80
mvaa 196,156,80
mvaa 196,172,4
mvaa 204,172,8
mvaa 204,156,84
mvaa 200,168,84
mvaa 200,152,8
mvaa 208,152,12
mvaa 208,168,88
mvaa 212,156,88
mvaa 212,172,12
mvaa 220,172,16
mvaa 220,156,92
mvaa 216,168,92
mvaa 216,152,16
maaa 128,208,16
maaa 128,224,92
maaa 112,212,92
s_waitcnt vmcnt(0) lgkmcnt(0)
s_barrier
s_add_u32 m0,s62,32768
maaa 112,228,16
dl 228,40,53,0
maaa 116,228,12
ta 40,234,0
maaa 116,212,88
ta 42,234,512
maaa 132,224,88
ta 44,235,0
maaa 132,208,12
ta 46,235,512
maaa 136,208,8
dl 229,40,53,1024
maaa 136,224,84
s_add_u32 s53,s53,s57
ta 48,236,0
maaa 120,212,84
ta 50,236,512
maaa 120,228,8
ta 52,237,0
maaa 124,228,4
ta 54,237,512
maaa 124,212,80
dl 230,44,54,2048
maaa 140,224,80
ta 56,234,4096
maaa 140,208,4
ta 58,234,4608
mvaa 40,216,4
ta 60,235,4096
mvaa 40,232,80
ta 62,235,4608
mvaa 44,220,80
dl 231,44,54,3072
mvaa 44,236,4
s_add_u32 m0,s62,49152
ta 64,236,4096
s_add_u32 s54,s54,s57
mvaa 52,236,8
s_add_u32 s57,s56,4096
ta 66,236,4608
mvaa 52,220,84
ta 68,237,4096
mvaa 48,232,84
ta 70,237,4608
mvaa 48,216,8
buffer_load_dword v248,v250,s[8:11],0 offen sc1
mvaa 56,216,12
ta 72,234,8192
mvaa 56,232,88
ta 74,234,8704
mvaa 60,220,88
ta 76,235,8192
mvaa 60,236,12
ta 78,235,8704
mvaa 68,236,16
dl 228,40,53,0
mvaa 68,220,92
ta 96,236,8192
mvaa 64,232,92
ta 98,236,8704
mvaa 64,216,16
ta 100,237,8192
mvaa 32,0,16
ta 102,237,8704
mvaa 32,20,92
dl 229,40,53,1024
mvaa 36,192,92
s_lshl_b32 s43,s19,15
ta 104,238,12288
s_add_u32 s42,s43,16384
mvaa 36,176,16
s_add_u32 s53,s87,83898368
ta 106,238,12800
s_lshl_b32 s87,s91,8
mvaa 28,176,12
s_lshl_b32 s91,s23,2
ta 108,239,12288
s_mov_b32 s40,858993459
mvaa 28,192,88
s_mov_b32 s41,858993459
ta 110,239,12800
v_cndmask_b32_e64 v228,0,v226,s[40:41]
mvaa 24,20,88
dl 230,44,54,2048
mvaa 24,0,12
ta 144,234,16384
v_cndmask_b32_e64 v229,0,v226,s[40:41]
mvaa 16,0,8
ta 146,234,16896
v_cndmask_b32_e64 v230,0,v226,s[40:41]
mvaa 16,20,84
ta 164,235,16384
mvaa 20,192,84
ta 166,235,16896
mvaa 20,176,8
dl 231,44,54,3072
mvaa 12,176,4
s_add_u32 s54,s87,s88
)"
R"(ta 168,236,16384
s_lshl_b32 s87,s12,16
mvaa 12,192,80
s_add_u32 s87,s87,s54
ta 170,236,16896
s_andn2_b32 s87,s87,64
mvaa 8,20,80
s_lshl_b32 s88,s66,5
ta 244,235,24576
s_add_u32 s63,s87,s88
mvaa 8,0,4
s_lshl_b32 s88,s67,5
ta 246,235,25088
s_add_u32 s65,s87,s88
mvaa 76,4,196
s_and_b32 s87,s20,4
ta 212,235,20480
s_lshl_b32 s87,s87,8
mvaa 76,80,180
s_and_b32 s88,s20,3
ta 214,235,20992
s_add_u32 s85,s85,s87
mvaa 96,80,188
s_add_u32 s85,s85,s89
ta 236,237,20480
s_lshl_b32 s89,s88,20
mvaa 96,4,204
s_add_u32 s85,s85,s89
ta 238,237,20992
s_and_b32 s89,s86,7
mvaa 100,8,204
s_add_u32 s49,s49,s87
ta 216,236,20480
s_lshl_b32 s89,s89,7
mvaa 100,84,188
s_add_u32 s49,s49,s89
ta 218,236,20992
s_lshl_b32 s89,s88,18
mvaa 80,84,180
s_add_u32 s49,s49,s89
ta 192,234,20480
s_lshl_b32 s89,s12,6
mvaa 80,8,196
s_add_u32 s89,s89,83886080
ta 194,234,20992
s_add_u32 s91,s91,s89
mvaa 84,12,196
s_mov_b32 s46,16384
ra 20,233,0
s_mov_b32 s86,268435456
mvaa 84,88,180
s_bfe_u32 s87,s98,327691
s_cselect_b32 s87,2048,0
ra 0,232,32768
v_cndmask_b32_e64 v231,0,v226,s[40:41]
mvaa 104,88,188
s_mov_b32 s40,0xf0000
ra 80,233,4096
s_mov_b32 s41,0xfff00ff
mvaa 104,12,204
s_sub_u32 s98,s98,s87
ra 4,233,32768
v_cndmask_b32_e64 v226,0,v252,s[40:41]
mvaa 108,16,204
s_mov_b32 s40,0x3f0003
ra 84,232,36864
s_mov_b32 s41,0x3fff03ff
mvaa 108,92,188
s_sub_u32 s47,32768,s43
ra 8,233,2048
v_cndmask_b32_e64 v227,0,v252,s[40:41]
mvaa 88,92,180
s_mov_b32 s40,0xfff0ffff
ra 88,233,36864
s_mov_b32 s41,0xf000ff00
mvaa 88,16,196
s_add_u32 s47,s47,32768
ra 12,232,34816
s_lshr_b32 s45,s15,1
s_waitcnt lgkmcnt(10)
mvaa 0,40,24
s_xor_b32 s62,s63,128
ta 188,237,16384
s_xor_b32 s64,s65,128
mvaa 4,44,24
s_ff1_i32_b32 s80,s45
ta 190,237,16896
s_sub_u32 s44,s56,2048
mvaa 92,44,28
ra 92,233,6144
mvaa 72,40,28
ra 16,233,34816
mvaa 144,40,32
mvaa 148,44,32
mvaa 156,44,36
mvaa 152,40,36
mvaa 160,48,36
ra 44,232,38912
v_mov_b32_e32 v232,0xc1a00000
mvaa 164,52,36
ra 40,233,38912
v_mov_b32_e32 v233,0xc1a00000
mvaa 172,52,32
mvaa 168,48,32
mvaa 176,48,28
mvaa 180,52,28
mvaa 188,52,24
mvaa 184,48,24
mvaa 192,56,24
ta 52,239,28672
mvaa 196,60,24
ta 54,239,29184
mvaa 204,60,28
mvaa 200,56,28
mvaa 208,56,32
mvaa 212,60,32
mvaa 220,60,36
mvaa 216,56,36
maaa 128,64,36
maaa 112,68,36
maaa 116,68,32
maaa 132,64,32
maaa 136,64,28
maaa 120,68,28
maaa 124,68,24
maaa 140,64,24
mvaa 40,72,24
mvaa 44,76,24
ta 64,238,28672
ta 66,238,29184
mvaa 52,76,28
s_waitcnt vmcnt(4)
v_and_b32_e32 v248,v242,v248
v_cmp_ne_u32_e64 s[100:101],v243,v248
mvaa 48,72,28
s_waitcnt vmcnt(0)
s_barrier
ta 148,234,32768
ta 150,234,33280
ta 152,234,49152
ta 154,234,49664
ta 156,235,49152
mvaa 56,72,32
s_cmp_lg_u64 s[100:101],0
s_cbranch_scc1 L_q_spin
L_q_ok:
ta 158,235,49664
ta 160,235,32768
ta 162,235,33280
ta 172,236,32768
ta 174,236,33280
ta 176,236,49152
ta 178,236,49664
mvaa 60,76,32
buffer_load_dword v242,v247,s[8:11],s91 offen offset:0 sc1
ta 180,237,49152
ta 182,237,49664
ta 184,237,32768
ta 186,237,33280
ta 196,234,36864
mvaa 68,76,36
s_mov_b32 m0,s95
ta 198,234,37376
ta 200,234,53248
ta 202,234,53760
ta 204,235,53248
ta 206,235,53760
ta 208,235,36864
ta 210,235,37376
mvaa 64,72,36
s_lshl_b32 s35,s90,2
ta 220,236,36864
ta 222,236,37376
ta 224,236,53248
mvaa 32,96,36
s_lshl_b32 s90,s22,2
ta 226,236,53760
ta 228,237,53248
mvaa 36,100,36
s_add_u32 s90,s90,s89
ta 230,237,53760
ta 232,237,36864
mvaa 28,100,32
ds 249,8,42,0
ta 234,237,37376
ta 240,235,40960
mvaa 24,96,32
s_add_u32 s35,s35,83886080
ta 242,235,41472
ta 48,239,45056
mvaa 16,96,28
ta 50,239,45568
ta 60,239,61440
mvaa 20,100,28
ta 62,239,61952
ta 56,238,61440
mvaa 12,100,24
ta 58,238,61952
ta 68,238,45056
mvaa 8,96,24
ds 249,8,42,1024
ta 70,238,45568
ta 76,237,24576
mvaa 76,24,104
ta 78,237,25088
ta 72,237,40960
mvaa 96,24,108
ta 74,237,41472
ta 100,237,57344
mvaa 100,28,108
ta 102,237,57856
ta 96,236,57344
mvaa 80,28,104
ta 98,236,57856
ta 24,236,40960
mvaa 84,32,104
ds 249,8,42,2048
ta 26,236,41472
mvaa 104,32,108
ta 28,236,24576
ta 30,236,25088
mvaa 108,36,108
v_lshl_add_u32 v238,v244,3,s4
v_min_u32_e32 v238,L_att_end-L_att_pc-4,v238
mvaa 88,36,104
ta 32,234,24576
ta 34,234,25088
s_waitcnt lgkmcnt(15)
mvaa 0,144,20
ta 108,234,40960
ta 110,234,41472
mvaa 0,148,0
ds 249,8,42,3072
ta 36,234,57344
ta 38,234,57856
ta 104,235,57344
ta 106,235,57856
mvaa 0,152,4
v_mov_b32_e32 v234,0xc1a00000
v_mov_b32_e32 v235,0xc1a00000
mvaa 4,156,4
v_cndmask_b32_e64 v250,0,v252,s[40:41]
s_mov_b32 s40,0xffc0fffc
mvaa 4,160,0
s_mov_b32 s41,0xc000fc00
v_bfe_i32 v248,v244,4,1
v_mul_f32_e32 v0,0x3e0293ee,v0
mvaa 4,164,20
v_mul_f32_e32 v1,0x3e0293ee,v1
v_mul_f32_e32 v2,0x3e0293ee,v2
mvaa 92,164,8
buffer_load_dword v240,v247,s[8:11],s90 offen offset:0 sc1
mvaa 92,160,12
v_mul_f32_e32 v3,0x3e0293ee,v3
cv 0,0,1
mvaa 92,156,16
v_mul_f32_e32 v4,0x3e0293ee,v4
cv 1,2,3
mvaa 72,152,16
v_mul_f32_e32 v5,0x3e0293ee,v5
s_waitcnt lgkmcnt(0)
s_barrier
mvaa 72,148,12
s_mov_b32 m0,s70
v_mul_f32_e32 v6,0x3e0293ee,v6
v_mul_f32_e32 v7,0x3e0293ee,v7
mvaa 72,144,8
buffer_load_dword v241,v247,s[8:11],s90 offen offset:256 sc1
mvaa 144,144,80
cv 2,4,5
cv 3,6,7
mvaa 144,148,84
v_mul_f32_e32 v92,0x3e0293ee,v92
v_mul_f32_e32 v93,0x3e0293ee,v93
mvaa 144,152,88
v_mul_f32_e32 v94,0x3e0293ee,v94
v_mul_f32_e32 v72,0x3e0293ee,v72
mvaa 148,156,88
v_mul_f32_e32 v95,0x3e0293ee,v95
v_mul_f32_e32 v73,0x3e0293ee,v73
mvaa 148,160,84
ds 249,8,43,0
mvaa 148,164,80
v_mul_f32_e32 v74,0x3e0293ee,v74
v_mul_f32_e32 v75,0x3e0293ee,v75
mvaa 156,164,92
cv 72,72,73
cv 73,74,75
mvaa 156,160,44
cv 74,92,93
cv 75,94,95
mvaa 156,156,40
v_mul_f32_e32 v144,0x3e0293ee,v144
v_mul_f32_e32 v145,0x3e0293ee,v145
mvaa 152,152,40
ds 249,8,43,1024
mvaa 152,148,44
v_mul_f32_e32 v146,0x3e0293ee,v146
v_mul_f32_e32 v147,0x3e0293ee,v147
mvaa 152,144,92
cv 144,144,145
v_mul_f32_e32 v148,0x3e0293ee,v148
mvaa 160,168,92
cv 145,146,147
v_mul_f32_e32 v149,0x3e0293ee,v149
mvaa 160,172,44
v_mul_f32_e32 v150,0x3e0293ee,v150
v_mul_f32_e32 v151,0x3e0293ee,v151
mvaa 160,176,40
ds 249,8,43,2048
mvaa 164,180,40
cv 146,148,149
cv 147,150,151
mvaa 164,184,44
v_mul_f32_e32 v156,0x3e0293ee,v156
v_mul_f32_e32 v157,0x3e0293ee,v157
mvaa 164,188,92
v_mul_f32_e32 v152,0x3e0293ee,v152
v_mul_f32_e32 v158,0x3e0293ee,v158
mvaa 172,188,80
v_mul_f32_e32 v159,0x3e0293ee,v159
v_mul_f32_e32 v153,0x3e0293ee,v153
mvaa 172,184,84
ds 249,8,43,3072
s_mov_b32 m0,s72
mvaa 172,180,88
L_vs14:
s_cbranch_execz L_vr0
v_mul_f32_e32 v154,0x3e0293ee,v154
v_mul_f32_e32 v155,0x3e0293ee,v155
mvaa 168,176,88
cv 152,152,153
cv 153,154,155
mvaa 168,172,84
ds_write_b128 v251,v[0:3] offset:0
mvaa 168,168,80
cv 154,156,157
cv 155,158,159
mvaa 176,168,8
ds 255,8,42,0
mvaa 176,172,12
v_mul_f32_e32 v160,0x3e0293ee,v160
v_mul_f32_e32 v161,0x3e0293ee,v161
mvaa 176,176,16
v_mul_f32_e32 v162,0x3e0293ee,v162
v_mul_f32_e32 v163,0x3e0293ee,v163
mvaa 180,180,16
cv 160,160,161
v_mul_f32_e32 v164,0x3e0293ee,v164
mvaa 180,184,12
v_mul_f32_e32 v165,0x3e0293ee,v165
cv 161,162,163
mvaa 180,188,8
ds 255,8,42,1024
ds_write_b128 v251,v[72:75] offset:4096
mvaa 188,188,20
v_mul_f32_e32 v166,0x3e0293ee,v166
v_mul_f32_e32 v167,0x3e0293ee,v167
mvaa 188,184,0
cv 162,164,165
cv 163,166,167
mvaa 188,180,4
v_mul_f32_e32 v172,0x3e0293ee,v172
v_mul_f32_e32 v173,0x3e0293ee,v173
mvaa 184,176,4
v_mul_f32_e32 v174,0x3e0293ee,v174
v_mul_f32_e32 v175,0x3e0293ee,v175
mvaa 184,172,0
ds 255,8,42,2048
mvaa 184,168,20
v_mul_f32_e32 v168,0x3e0293ee,v168
v_mul_f32_e32 v169,0x3e0293ee,v169
mvaa 192,192,20
ds_write_b128 v251,v[144:147] offset:8192
mvaa 192,196,0
v_mul_f32_e32 v170,0x3e0293ee,v170
v_mul_f32_e32 v171,0x3e0293ee,v171
mvaa 192,200,4
cv 168,168,169
cv 169,170,171
mvaa 196,204,4
ds 255,8,42,3072
mvaa 196,208,0
cv 170,172,173
cv 171,174,175
mvaa 196,212,20
v_mul_f32_e32 v176,0x3e0293ee,v176
v_mul_f32_e32 v177,0x3e0293ee,v177
mvaa 204,212,8
v_mul_f32_e32 v178,0x3e0293ee,v178
v_mul_f32_e32 v179,0x3e0293ee,v179
mvaa 204,208,12
ds_write_b128 v251,v[152:155] offset:12288
mvaa 204,204,16
buffer_load_dword v243,v247,s[8:11],s91 offen offset:256 sc1
mvaa 200,200,16
cv 176,176,177
cv 177,178,179
mvaa 200,196,12
v_mul_f32_e32 v180,0x3e0293ee,v180
v_mul_f32_e32 v181,0x3e0293ee,v181
mvaa 200,192,8
v_mul_f32_e32 v182,0x3e0293ee,v182
v_mul_f32_e32 v183,0x3e0293ee,v183
mvaa 208,192,80
cv 178,180,181
cv 179,182,183
mvaa 208,196,84
v_mul_f32_e32 v188,0x3e0293ee,v188
v_mul_f32_e32 v189,0x3e0293ee,v189
mvaa 208,200,88
ds_write_b128 v251,v[160:163] offset:13312
mvaa 212,204,88
v_mul_f32_e32 v190,0x3e0293ee,v190
v_mul_f32_e32 v191,0x3e0293ee,v191
mvaa 212,208,84
v_mul_f32_e32 v184,0x3e0293ee,v184
v_mul_f32_e32 v185,0x3e0293ee,v185
mvaa 212,212,80
v_mul_f32_e32 v186,0x3e0293ee,v186
v_mul_f32_e32 v187,0x3e0293ee,v187
mvaa 220,212,92
cv 184,184,185
cv 185,186,187
mvaa 220,208,44
cv 186,188,189
cv 187,190,191
mvaa 220,204,40
v_mul_f32_e32 v192,0x3e0293ee,v192
v_mul_f32_e32 v193,0x3e0293ee,v193
mvaa 216,200,40
ds_write_b128 v251,v[168:171] offset:9216
mvaa 216,196,44
v_mul_f32_e32 v194,0x3e0293ee,v194
v_mul_f32_e32 v195,0x3e0293ee,v195
mvaa 216,192,92
cv 192,192,193
cv 193,194,195
maaa 128,216,92
v_mul_f32_e32 v196,0x3e0293ee,v196
v_mul_f32_e32 v197,0x3e0293ee,v197
maaa 128,220,44
v_mul_f32_e32 v198,0x3e0293ee,v198
v_mul_f32_e32 v199,0x3e0293ee,v199
maaa 128,224,40
cv 194,196,197
cv 195,198,199
maaa 112,228,40
v_mul_f32_e32 v204,0x3e0293ee,v204
v_mul_f32_e32 v205,0x3e0293ee,v205
maaa 112,232,44
ds_write_b128 v251,v[176:179] offset:5120
maaa 112,236,92
v_mul_f32_e32 v206,0x3e0293ee,v206
v_mul_f32_e32 v207,0x3e0293ee,v207
maaa 116,236,80
v_mul_f32_e32 v200,0x3e0293ee,v200
v_mul_f32_e32 v201,0x3e0293ee,v201
maaa 116,232,84
v_mul_f32_e32 v202,0x3e0293ee,v202
v_mul_f32_e32 v203,0x3e0293ee,v203
maaa 116,228,88
cv 200,200,201
cv 201,202,203
maaa 132,224,88
cv 202,204,205
cv 203,206,207
maaa 132,220,84
v_mul_f32_e32 v208,0x3e0293ee,v208
v_mul_f32_e32 v209,0x3e0293ee,v209
maaa 132,216,80
ds_write_b128 v251,v[184:187] offset:1024
maaa 136,216,8
v_mul_f32_e32 v210,0x3e0293ee,v210
v_mul_f32_e32 v211,0x3e0293ee,v211
maaa 136,220,12
cv 208,208,209
cv 209,210,211
maaa 136,224,16
v_mul_f32_e32 v212,0x3e0293ee,v212
v_mul_f32_e32 v213,0x3e0293ee,v213
maaa 120,228,16
s_waitcnt vmcnt(11)
s_barrier
maaa 120,232,12
v_mul_f32_e32 v214,0x3e0293ee,v214
v_mul_f32_e32 v215,0x3e0293ee,v215
maaa 120,236,8
cv 210,212,213
cv 211,214,215
maaa 124,236,20
ds_write_b128 v251,v[192:195] offset:2048
maaa 124,232,0
v_mul_f32_e32 v220,0x3e0293ee,v220
v_mul_f32_e32 v221,0x3e0293ee,v221
maaa 124,228,4
v_mul_f32_e32 v216,0x3e0293ee,v216
v_mul_f32_e32 v217,0x3e0293ee,v217
maaa 140,224,4
v_mul_f32_e32 v222,0x3e0293ee,v222
v_mul_f32_e32 v223,0x3e0293ee,v223
maaa 140,220,0
v_mul_f32_e32 v218,0x3e0293ee,v218
v_mul_f32_e32 v219,0x3e0293ee,v219
maaa 140,216,20
cv 216,216,217
cv 217,218,219
mvaa 88,92,64
cv 218,220,221
cv 219,222,223
mvaa 88,44,68
ds_write_b128 v251,v[200:203] offset:6144
mvaa 88,40,56
v_accvgpr_read_b32 v0,a128
v_accvgpr_read_b32 v1,a129
mvaa 108,40,60
v_accvgpr_read_b32 v2,a130
v_accvgpr_read_b32 v3,a131
mvaa 108,44,48
v_mul_f32_e32 v0,0x3e0293ee,v0
v_mul_f32_e32 v1,0x3e0293ee,v1
cv 88,88,89
cv 89,90,91
mvaa 108,92,52
buffer_store_dwordx2 v[88:89],v253,s[8:11],s99 offen offset:1024 sc0 sc1
mvaa 36,76,92
v_mul_f32_e32 v2,0x3e0293ee,v2
v_mul_f32_e32 v3,0x3e0293ee,v3
mvaa 36,72,44
cv 0,0,1
cv 1,2,3
mvaa 36,100,40
ds_write_b128 v251,v[208:211] offset:10240
mvaa 32,96,40
cv 108,108,109
cv 109,110,111
mvaa 32,24,44
buffer_store_dwordx2 v[108:109],v253,s[8:11],s99 offen offset:1536 sc0 sc1
mvaa 32,28,92
v_accvgpr_read_b32 v4,a112
v_accvgpr_read_b32 v5,a113
mvaa 64,32,92
v_accvgpr_read_b32 v6,a114
v_mul_f32_e32 v4,0x3e0293ee,v4
mvaa 64,108,44
v_mul_f32_e32 v5,0x3e0293ee,v5
v_mul_f32_e32 v6,0x3e0293ee,v6
mvaa 64,36,40
cv 32,32,33
cv 33,34,35
cv 34,36,37
cv 35,38,39
mvaa 68,104,40
buffer_store_dwordx4 v[32:35],v244,s[8:11],s99 offen sc0 sc1
ds_write_b128 v251,v[216:219] offset:14336
mvaa 68,240,44
v_accvgpr_read_b32 v7,a115
v_mul_f32_e32 v7,0x3e0293ee,v7
mvaa 68,244,92
cv 2,4,5
cv 3,6,7
mvaa 60,244,80
ra 192,224,0
v_accvgpr_read_b32 v92,a116
mvaa 60,240,84
ra 196,224,1024
v_accvgpr_read_b32 v93,a117
mvaa 60,104,88
ra 200,224,2048
v_mul_f32_e32 v92,0x3e0293ee,v92
mvaa 56,36,88
ra 204,224,3072
v_mul_f32_e32 v93,0x3e0293ee,v93
mvaa 56,108,84
ds_write_b128 v251,v[0:3] offset:15360
mvaa 56,32,80
v_accvgpr_read_b32 v94,a118
v_accvgpr_read_b32 v95,a119
mvaa 24,28,80
v_accvgpr_read_b32 v72,a132
v_accvgpr_read_b32 v73,a133
mvaa 24,24,84
v_accvgpr_read_b32 v74,a134
v_mul_f32_e32 v94,0x3e0293ee,v94
mvaa 24,96,88
v_mul_f32_e32 v95,0x3e0293ee,v95
v_mul_f32_e32 v72,0x3e0293ee,v72
mvaa 28,100,88
v_mul_f32_e32 v73,0x3e0293ee,v73
v_mul_f32_e32 v74,0x3e0293ee,v74
v_accvgpr_read_b32 v75,a135
mvaa 28,72,84
v_mul_f32_e32 v75,0x3e0293ee,v75
cv 72,72,73
cv 73,74,75
cv 74,92,93
mvaa 28,76,80
ra 208,224,4096
cv 75,94,95
mvaa 20,76,8
ds_write_b128 v251,v[72:75] offset:11264
mvaa 20,72,12
v_accvgpr_read_b32 v144,a136
v_accvgpr_read_b32 v145,a137
mvaa 20,100,16
s_mov_b64 exec,3
global_load_dword v239,v238,s[96:97]
s_mov_b64 exec,-1
ra 212,224,5120
ra 248,224,14336
ra 252,224,15360
mvaa 16,96,16
v_accvgpr_read_b32 v146,a138
v_mul_f32_e32 v144,0x3e0293ee,v144
v_mul_f32_e32 v145,0x3e0293ee,v145
v_mul_f32_e32 v146,0x3e0293ee,v146
v_accvgpr_read_b32 v147,a139
v_mul_f32_e32 v147,0x3e0293ee,v147
cv 144,144,145
mvaa 16,24,12
ra 236,224,11264
v_accvgpr_read_b32 v148,a120
v_accvgpr_read_b32 v149,a121
v_accvgpr_read_b32 v150,a122
cv 145,146,147
mvaa 16,28,8
v_mul_f32_e32 v148,0x3e0293ee,v148
v_mul_f32_e32 v149,0x3e0293ee,v149
v_mul_f32_e32 v150,0x3e0293ee,v150
v_accvgpr_read_b32 v151,a123
v_mul_f32_e32 v151,0x3e0293ee,v151
cv 146,148,149
mvaa 48,32,8
ra 228,224,9216
cv 147,150,151
mvaa 48,108,12
ds_write_b128 v251,v[144:147] offset:7168
mvaa 48,36,16
v_accvgpr_read_b32 v156,a124
v_accvgpr_read_b32 v157,a125
mvaa 52,104,16
ra 232,224,10240
ra 216,224,6144
ra 220,224,7168
ra 224,224,8192
v_accvgpr_read_b32 v158,a126
v_accvgpr_read_b32 v159,a127
mvaa 52,240,12
v_accvgpr_read_b32 v152,a140
v_mul_f32_e32 v156,0x3e0293ee,v156
v_mul_f32_e32 v157,0x3e0293ee,v157
v_mul_f32_e32 v158,0x3e0293ee,v158
v_mul_f32_e32 v159,0x3e0293ee,v159
mvaa 52,244,8
v_mul_f32_e32 v152,0x3e0293ee,v152
v_accvgpr_read_b32 v153,a141
v_mul_f32_e32 v153,0x3e0293ee,v153
v_accvgpr_read_b32 v154,a142
v_accvgpr_read_b32 v155,a143
mvaa 44,244,20
v_mul_f32_e32 v154,0x3e0293ee,v154
v_mul_f32_e32 v155,0x3e0293ee,v155
cv 152,152,153
cv 153,154,155
cv 154,156,157
mvaa 44,240,0
ra 244,224,13312
cv 155,158,159
mvaa 44,104,4
ds_write_b128 v251,v[152:155] offset:3072
mvaa 40,36,4
cv 64,64,65
cv 56,56,57
mvaa 40,108,0
cv 57,58,59
cv 58,60,61
mvaa 40,32,20
ra 240,224,12288
cv 59,62,63
mvaa 8,28,20
cv 16,16,17
cv 17,18,19
mvaa 8,24,0
cv 48,48,49
cv 194,44,45
mvaa 8,96,4
cv 195,46,47
cv 192,40,41
mvaa 12,100,4
cv 193,42,43
cv 49,50,51
mvaa 12,72,0
s_cmp_eq_u32 s19,0
s_cselect_b32 s76,32768,0
s_cselect_b32 s77,49152,s86
s_cselect_b32 s78,65536,s46
s_cselect_b32 s79,81920,s86
mvaa 12,76,20
s_add_u32 s86,s95,98304
cv 8,8,9
v_add_u32_e32 v236,s86,v244
mvaa 76,20,64
s_add_u32 s46,s46,16384
v_xor_b32_e32 v237,4096,v236
s_waitcnt lgkmcnt(0)
s_barrier
s_mov_b32 m0,s68
mvaa 76,0,68
ds 249,8,76,0
ra 128,237,32768
ra 132,237,33792
mvaa 76,4,56
s_add_u32 s95,s47,32768
ra 136,237,34816
cv 9,10,11
mvaa 96,4,60
ra 140,237,35840
ra 144,237,49152
mvaa 96,0,48
cv 10,12,13
cv 11,14,15
mvaa 96,20,52
ra 148,237,50176
cv 50,52,53
mvaa 100,8,52
ds 249,8,76,1024
ra 152,237,51200
ra 156,237,52224
mvaa 100,12,48
ra 176,236,16384
cv 51,54,55
mvaa 100,16,60
ra 180,236,17408
cv 18,20,21
mvaa 80,16,56
ra 184,236,18432
cv 19,22,23
mvaa 80,12,68
ra 188,236,19456
cv 24,24,25
mvaa 80,8,64
ds 249,8,76,2048
ra 160,236,0
mvaa 84,80,64
ra 164,236,1024
cv 25,26,27
mvaa 84,84,68
ra 168,236,2048
ra 172,236,3072
mvaa 84,88,56
cv 26,28,29
cv 27,30,31
mvaa 104,88,60
cv 65,66,67
cv 66,68,69
mvaa 104,84,48
ds 249,8,76,3072
mvaa 104,80,52
s_mov_b32 m0,s69
cv 67,70,71
v_cndmask_b32_e64 v252,0,v252,s[40:41]
cvvv 164,8,192,112
cv 70,76,77
cv 71,78,79
cvvv 168,8,48,116
ds 249,8,77,0
cvvv 172,16,48,112
cv 94,96,97
v_mov_b32_e32 v76,v88
cvvv 176,16,56,116
cv 95,98,99
s_waitcnt vmcnt(9) lgkmcnt(0)
s_barrier
cvvv 180,24,56,112
ds 249,8,77,1024
cvvv 184,24,64,116
ra 0,224,32768
cv 96,100,101
cvvv 188,32,64,112
ra 4,224,33792
cv 97,102,103
cvaa 0,192,128,232
buffer_load_dword v17,v247,s[8:11],s35 offen offset:64 sc1
mvaa 0,200,132
ra 8,224,34816
cv 72,80,81
mvaa 0,208,136
ra 12,224,35840
cv 73,82,83
mvaa 0,216,140
ds 249,8,77,2048
cvaa 4,220,140,232
ra 16,224,36864
cv 74,84,85
mvaa 4,212,136
ra 20,224,37888
v_max3_f32 v172,v168,v169,v172
mvaa 4,204,132
ds 249,8,77,3072
mvaa 4,196,128
s_mov_b32 m0,s74
ra 24,224,38912
v_max_f32_e32 v172,v172,v173
cvaa 32,224,128,232
ra 28,224,39936
v_max3_f32 v174,v170,v171,v174
mvaa 32,232,132
buffer_load_dword v19,v247,s[8:11],s35 offen offset:192 sc1
mvaa 32,240,136
ra 32,224,40960
v_max_f32_e32 v174,v174,v175
mvaa 32,248,140
ra 36,224,41984
ra 40,224,43008
cvaa 36,252,140,232
buffer_load_dword v18,v247,s[8:11],s35 offen offset:128 sc1
ra 44,224,44032
mvaa 36,244,136
ra 48,224,45056
v_max_f32_e32 v148,v172,v174
mvaa 36,236,132
ra 56,224,47104
ra 60,224,48128
mvaa 36,228,128
buffer_load_dword v16,v247,s[8:11],s35 offen offset:0 sc1
ra 52,224,46080
cvaa 8,192,144,232
v_mov_b32_e32 v149,v148
cv 75,86,87
mvaa 8,200,148
v_permlane16_swap_b32_e32 v148,v149
mvaa 8,208,152
ds 255,8,43,0
mvaa 8,216,156
v_max_f32_e32 v148,v148,v149
v_mov_b32_e32 v149,v148
cvaa 12,220,156,232
cv 98,104,105
v_max3_f32 v180,v176,v177,v180
mvaa 12,212,152
ds 255,8,43,1024
mvaa 12,204,148
v_permlane32_swap_b32_e32 v148,v149
mvaa 12,196,144
v_max_f32_e32 v148,v148,v149
s_waitcnt lgkmcnt(0)
s_barrier
cvaa 40,224,144,232
ds 255,8,43,2048
mvaa 40,232,148
ra 64,225,0
v_mul_f32_e32 v148,0xbe8293ee,v148
mvaa 40,240,152
L_vs15:
s_cbranch_execz L_vr0
ra 68,225,1024
v_fmamk_f32 v172,v172,0x3e8293ee,v148
mvaa 40,248,156
ds 255,8,43,3072
cvaa 44,252,156,232
s_mov_b32 m0,s70
ra 72,225,2048
v_fmamk_f32 v174,v174,0x3e8293ee,v148
mvaa 44,244,152
ra 76,225,3072
v_max3_f32 v182,v178,v179,v182
mvaa 44,236,148
ds 249,8,78,0
mvaa 44,228,144
ra 80,225,4096
v_max_f32_e32 v180,v180,v181
cvaa 24,192,176,128
ra 84,225,5120
v_max_f32_e32 v182,v182,v183
mvaa 24,200,180
ds 249,8,78,1024
mvaa 24,208,184
ra 88,225,6144
v_max_f32_e32 v152,v180,v182
mvaa 24,216,188
ra 92,225,7168
v_mov_b32_e32 v153,v152
cvaa 28,220,188,132
ds 249,8,78,2048
mvaa 28,212,184
ra 96,225,8192
v_max3_f32 v188,v184,v185,v188
mvaa 28,204,180
ra 100,225,9216
ra 104,225,10240
mvaa 28,196,176
ds 249,8,78,3072
ra 108,225,11264
cvaa 56,224,176,136
s_mov_b32 m0,s71
ra 112,225,12288
v_max3_f32 v190,v186,v187,v190
mvaa 56,232,180
ra 116,225,13312
ra 120,225,14336
mvaa 56,240,184
ds 249,8,79,0
ra 124,225,15360
mvaa 56,248,188
ex 172
cvaa 60,252,188,140
ex 174
mvaa 60,244,184
ds 249,8,79,1024
mvaa 60,236,180
v_permlane16_swap_b32_e32 v152,v153
mvaa 60,228,176
cv 172,172,172
cv 174,174,174
cvaa 80,0,160,128
ds 249,8,79,2048
mvaa 80,8,164
v_and_b32_e32 v168,v226,v172
v_max_f32_e32 v152,v152,v153
mvaa 80,16,168
v_and_b32_e32 v169,v227,v174
v_and_b32_e32 v170,v250,v172
v_and_b32_e32 v171,v252,v174
mvaa 80,24,172
ds 249,8,79,3072
v_mov_b32_e32 v153,v152
cvaa 84,28,172,132
v_max_f32_e32 v188,v188,v189
v_permlane32_swap_b32_e32 v152,v153
v_max_f32_e32 v152,v152,v153
mvaa 84,20,168
v_mul_f32_e32 v152,0xbe8293ee,v152
v_fmamk_f32 v180,v180,0x3e8293ee,v152
mvaa 84,12,164
v_fmamk_f32 v182,v182,0x3e8293ee,v152
v_max_f32_e32 v190,v190,v191
mvaa 84,4,160
ex 180
cvaa 112,32,160,136
ex 182
v_max_f32_e32 v156,v188,v190
mvaa 112,40,164
v_mov_b32_e32 v157,v156
cv 180,180,180
mvaa 112,48,168
v_permlane16_swap_b32_e32 v156,v157
mvaa 112,56,172
cv 182,182,182
v_and_b32_e32 v176,v226,v180
v_max_f32_e32 v156,v156,v157
s_waitcnt vmcnt(15) lgkmcnt(0)
s_barrier
s_mov_b32 m0,s72
cvaa 116,60,172,140
ds 255,8,76,0
v_and_b32_e32 v177,v227,v182
mvaa 116,52,168
v_and_b32_e32 v178,v250,v180
v_and_b32_e32 v179,v252,v182
v_mov_b32_e32 v157,v156
mvaa 116,44,164
cv 99,106,107
v_permlane32_swap_b32_e32 v156,v157
v_max_f32_e32 v156,v156,v157
mvaa 116,36,160
ds 255,8,76,1024
v_mul_f32_e32 v156,0xbe8293ee,v156
zvvv 148,228,168
v_fmamk_f32 v188,v188,0x3e8293ee,v156
ex 188
zvvv 200,70,168
v_fmamk_f32 v190,v190,0x3e8293ee,v156
ex 190
zvvv 204,94,168
ds 255,8,76,2048
zvvv 152,228,176
cv 188,188,188
cv 190,190,190
v_and_b32_e32 v184,v226,v188
zvvv 208,72,176
v_and_b32_e32 v185,v227,v190
v_and_b32_e32 v186,v250,v188
v_and_b32_e32 v187,v252,v190
zvvv 212,96,176
ds 255,8,76,3072
s_mov_b32 m0,s73
v_mov_b32_e32 v77,v89
zvvv 156,228,184
v_mov_b32_e32 v100,v108
v_mov_b32_e32 v101,v109
zvvv 216,74,184
ex 0
zvvv 220,98,184
ds 255,8,77,0
cvaa 64,0,128,232
ex 1
mvaa 64,8,132
ex 2
mvaa 64,16,136
ds 255,8,77,1024
)"
R"(mvaa 64,24,140
ds 255,8,77,2048
v_mov_b32_e32 v170,v70
v_mov_b32_e32 v171,v71
cvaa 68,28,140,232
ex 3
mvaa 68,20,136
ex 4
mvaa 68,12,132
ds 255,8,77,3072
mvaa 68,4,128
ex 5
v_and_b32_e32 v248,2863311530,v248
cvaa 96,32,128,232
ex 6
mvaa 96,40,132
v_sub_f32_e32 v17,1.0,v17
v_rcp_f32_e32 v148,v148
mvaa 96,48,136
v_mul_f32_e32 v148,v148,v17
s_waitcnt vmcnt(21)
v_sub_f32_e32 v18,1.0,v18
v_sub_f32_e32 v19,1.0,v19
mvaa 96,56,140
v_mul_f32_e32 v200,v148,v200
v_mul_f32_e32 v201,v148,v201
v_mul_f32_e32 v202,v148,v202
cvaa 100,60,140,232
v_mul_f32_e32 v203,v148,v203
cv 200,200,201
mvaa 100,52,136
v_mul_f32_e32 v204,v148,v204
v_mul_f32_e32 v205,v148,v205
cv 201,202,203
mvaa 100,44,132
v_mul_f32_e32 v206,v148,v206
v_mul_f32_e32 v207,v148,v207
cv 202,204,205
mvaa 100,36,128
buffer_load_dwordx4 v[176:179],v244,s[8:11],s98 offen sc1
cv 203,206,207
cvaa 72,0,144,232
v_rcp_f32_e32 v152,v152
mvaa 72,8,148
v_mul_f32_e32 v152,v152,v18
v_mul_f32_e32 v208,v152,v208
v_mul_f32_e32 v209,v152,v209
mvaa 72,16,152
buffer_load_dwordx2 v[168:169],v253,s[8:11],s98 offen offset:1024 sc1
v_mul_f32_e32 v210,v152,v210
mvaa 72,24,156
v_mul_f32_e32 v211,v152,v211
cv 208,208,209
cv 209,210,211
cvaa 76,28,156,232
v_mul_f32_e32 v212,v152,v212
v_mul_f32_e32 v213,v152,v213
mvaa 76,20,152
buffer_load_dwordx2 v[172:173],v253,s[8:11],s98 offen offset:1536 sc1
v_mul_f32_e32 v214,v152,v214
mvaa 76,12,148
v_mul_f32_e32 v215,v152,v215
v_rcp_f32_e32 v156,v156
mvaa 76,4,144
cv 210,212,213
cv 211,214,215
v_mul_f32_e32 v156,v156,v19
cvaa 104,32,144,232
buffer_store_dwordx4 v[200:203],v244,s[8:11],s34 offen sc0 sc1
mvaa 104,40,148
v_mul_f32_e32 v216,v156,v216
v_mul_f32_e32 v217,v156,v217
v_mul_f32_e32 v218,v156,v218
mvaa 104,48,152
v_mul_f32_e32 v219,v156,v219
cv 216,216,217
cv 217,218,219
mvaa 104,56,156
buffer_store_dwordx4 v[208:211],v244,s[8:11],s50 offen sc0 sc1
v_mul_f32_e32 v220,v156,v220
cvaa 108,60,156,232
v_mul_f32_e32 v221,v156,v221
v_mul_f32_e32 v222,v156,v222
mvaa 108,52,152
v_mul_f32_e32 v223,v156,v223
cv 218,220,221
cv 219,222,223
mvaa 108,44,148
buffer_store_dwordx4 v[216:219],v244,s[8:11],s51 offen sc0 sc1
v_mov_b32_e32 v160,v120
mvaa 108,36,144
v_mov_b32_e32 v161,v121
ex 7
cvaa 88,0,176,232
ex 32
mvaa 88,8,180
v_mov_b32_e32 v162,v122
ex 33
mvaa 88,16,184
v_mov_b32_e32 v175,v95
ex 34
mvaa 88,24,188
s_cmp_ge_i32 s47,s46
s_cselect_b32 s76,s95,s46
s_cselect_b32 s77,s95,268435456
v_mov_b32_e32 v174,v94
ex 35
cvaa 92,28,188,232
s_add_u32 s77,s77,16384
ex 36
mvaa 92,20,184
v_mov_b32_e32 v163,v123
ex 37
mvaa 92,12,180
cv 0,0,1
ex 38
mvaa 92,4,176
cv 1,2,3
ex 39
cvaa 120,32,176,232
buffer_load_dword v247,v246,s[24:27],s49 offen
mvaa 120,40,180
s_add_u32 s49,s49,32768
cv 2,4,5
ex 8
mvaa 120,48,184
cv 3,6,7
ex 9
mvaa 120,56,188
buffer_load_dword v247,v245,s[36:39],s85 offen
cv 4,32,33
cvaa 124,60,188,232
s_add_u32 s85,s85,131072
ex 10
mvaa 124,52,184
cv 5,34,35
ex 11
mvaa 124,44,180
buffer_load_dword v247,v246,s[24:27],s49 offen
cv 6,36,37
mvaa 124,36,176
s_cmp_lg_u32 s19,0
s_cbranch_scc1 L_j0
s_nop 1
s_lshl_b32 s86,s12,12
s_add_u32 s86,s86,131072
v_add_u32_e32 v236,s86,v244
ra 160,236,0
ra 164,236,1024
ra 168,236,2048
ra 172,236,3072
ra 176,236,16384
ra 180,236,17408
ra 184,236,18432
ra 188,236,19456
v_add_u32_e32 v224,s94,v224
s_waitcnt lgkmcnt(0)
L_j0:
cv 7,38,39
ex 12
zaav 0,64,0
s_add_u32 s49,s49,32768
ex 13
maav 0,96,4
ex 14
v_or_b32_e32 v248,1145324612,v248
zaav 4,68,0
ex 15
v_mov_b64_e32 v[144:145],0
maav 4,100,4
cv 8,8,9
ex 40
zaav 8,72,0
ex 41
maav 8,104,4
cv 9,10,11
ex 42
zaav 12,76,0
cv 10,12,13
ex 43
maav 12,108,4
cv 11,14,15
ex 44
v_smfmac_f32_16x16x64_bf16 v[144:147],v[228:231],v[0:7],v248
ex 45
zaav 16,80,0
cv 12,40,41
ex 46
maav 16,112,4
s_waitcnt vmcnt(8)
v_max3_u32 v180,v176,v177,v178
cv 13,42,43
zaav 20,84,0
s_waitcnt vmcnt(7)
v_max3_u32 v180,v180,v179,v168
s_waitcnt vmcnt(6)
v_max3_u32 v180,v180,v169,v172
v_max_u32_e32 v180,v180,v173
v_cmp_eq_u32_e64 s[40:41],-1,v180
maav 20,116,4
s_cmp_lg_u64 s[40:41],0
s_cbranch_scc1 L_bx
L_bxr:
cv 14,44,45
ex 47
mvvv 160,176,192
zaav 24,88,0
cv 15,46,47
v_sub_f32_e32 v16,1.0,v16
maav 24,120,4
ex 24
zaav 28,92,0
ex 25
maav 28,124,4
v_max3_f32 v164,v160,v161,v164
v_max_f32_e32 v164,v164,v165
v_max3_f32 v166,v162,v163,v166
v_max_f32_e32 v166,v166,v167
v_max_f32_e32 v180,v164,v166
v_mov_b32_e32 v181,v180
zaav 32,64,8
ex 26
v_permlane16_swap_b32_e32 v180,v181
v_max_f32_e32 v180,v180,v181
v_mov_b32_e32 v181,v180
maav 32,96,12
ex 27
v_permlane32_swap_b32_e32 v180,v181
v_max_f32_e32 v180,v180,v181
v_mul_f32_e32 v180,0xbe8293ee,v180
v_fmamk_f32 v164,v164,0x3e8293ee,v180
zaav 36,68,8
ex 164
v_fmamk_f32 v166,v166,0x3e8293ee,v180
ex 166
cv 164,164,164
cv 166,166,166
maav 36,100,12
v_and_b32_e32 v160,v226,v164
v_and_b32_e32 v161,v227,v166
v_and_b32_e32 v162,v250,v164
v_and_b32_e32 v163,v252,v166
zaav 40,72,8
v_mov_b64_e32 v[148:149],0
zvvv 180,228,160
maav 40,104,12
ra 192,224,0
zvvv 184,168,160
zaav 44,76,8
ra 196,224,1024
zvvv 188,172,160
maav 44,108,12
ra 200,224,2048
v_rcp_f32_e32 v180,v180
cv 24,24,25
v_mul_f32_e32 v180,v180,v16
v_mul_f32_e32 v184,v180,v184
v_mul_f32_e32 v185,v180,v185
v_smfmac_f32_16x16x64_bf16 v[148:151],v[228:231],v[8:15],v248
v_mul_f32_e32 v186,v180,v186
v_mul_f32_e32 v187,v180,v187
v_mul_f32_e32 v188,v180,v188
v_mul_f32_e32 v189,v180,v189
v_mul_f32_e32 v190,v180,v190
v_mul_f32_e32 v191,v180,v191
zaav 48,80,8
cv 184,184,185
cv 185,186,187
cv 186,188,189
cv 187,190,191
cv 25,26,27
ex 28
maav 48,112,12
buffer_store_dwordx4 v[184:187],v244,s[8:11],s33 offen sc0 sc1
ex 29
ex 30
cv 26,28,29
zaav 52,84,8
ex 31
ex 56
cv 27,30,31
ex 57
maav 52,116,12
ex 58
ex 59
ex 60
zaav 56,88,8
ex 61
maav 56,120,12
cv 28,56,57
ex 62
zaav 60,92,8
ra 204,224,3072
ra 208,224,4096
ex 63
maav 60,124,12
ra 212,224,5120
cv 29,58,59
cv 30,60,61
cv 31,62,63
zvav 192,64,24
ra 216,224,6144
ra 220,224,7168
ex 64
mvav 192,96,28
ra 224,224,8192
v_mov_b32_e32 v251,-1
ex 65
zvav 196,68,24
ra 228,224,9216
ra 232,224,10240
ex 66
mvav 196,100,28
ra 236,224,11264
v_mov_b64_e32 v[152:153],0
ex 67
zvav 200,72,24
ra 240,224,12288
ra 244,224,13312
v_mov_b64_e32 v[156:157],0
cv 64,64,65
mvav 200,104,28
ra 248,224,14336
ra 252,224,15360
ex 68
zvav 204,76,24
ex 69
ex 70
mvav 204,108,28
cv 65,66,67
ex 71
cv 66,68,69
v_smfmac_f32_16x16x64_bf16 v[156:159],v[228:231],v[24:31],v248
ex 96
ex 97
zvav 208,80,24
ex 98
ex 99
mvav 208,112,28
cv 67,70,71
cv 68,96,97
ex 100
zvav 212,84,24
ex 101
ex 102
mvav 212,116,28
cv 69,98,99
ex 103
cv 70,100,101
zvav 216,88,24
ex 72
ex 73
mvav 216,120,28
ex 74
ex 75
zvav 220,92,24
ex 76
ex 77
mvav 220,124,28
v_mov_b32_e32 v250,-1
v_mov_b32_e32 v252,-1
v_mov_b32_e32 v253,-1
cv 71,102,103
s_mov_b64 exec,0
s_bitcmp1_b32 170,s20
s_cbranch_scc1 L_vbO
s_cmp_eq_u32 s12,0
s_cbranch_scc1 L_vs17
s_cmp_eq_u32 s12,1
s_cbranch_scc1 L_vs18
s_cmp_eq_u32 s12,2
s_cbranch_scc1 L_vs19
s_branch L_vs20
L_vbO:
s_cmp_eq_u32 s12,0
s_cbranch_scc1 L_vs24
s_cmp_eq_u32 s12,1
s_cbranch_scc1 L_vs25
s_cmp_eq_u32 s12,2
s_cbranch_scc1 L_vs26
s_branch L_vs27
L_vr1:
s_mov_b64 exec,-1
s_waitcnt lgkmcnt(0)
s_barrier
cvaa 0,192,128,232
mvaa 0,200,132
s_add_u32 s81,s81,1
ex 78
mvaa 0,208,136
s_add_u32 s44,s44,1024
ex 79
mvaa 0,216,140
cv 72,72,73
cv 73,74,75
cvaa 4,220,140,232
s_mov_b32 m0,s68
ex 104
mvaa 4,212,136
ds 249,8,76,0
mvaa 4,204,132
ex 105
mvaa 4,196,128
ex 106
cvaa 32,224,128,232
ra 64,225,32768
cv 74,76,77
mvaa 32,232,132
ex 107
mvaa 32,240,136
ra 68,225,33792
cv 75,78,79
mvaa 32,248,140
ex 108
cvaa 36,252,140,232
ds 249,8,76,1024
mvaa 36,244,136
ex 109
mvaa 36,236,132
ra 72,225,34816
cv 76,104,105
mvaa 36,228,128
ex 110
cvaa 8,192,144,232
ra 76,225,35840
cv 77,106,107
mvaa 8,200,148
ex 111
mvaa 8,208,152
ex 80
mvaa 8,216,156
ds 249,8,76,2048
cvaa 12,220,156,232
ra 80,225,36864
cv 78,108,109
mvaa 12,212,152
ex 81
mvaa 12,204,148
ra 84,225,37888
cv 79,110,111
mvaa 12,196,144
ex 82
cvaa 40,224,144,232
ex 83
mvaa 40,232,148
ra 88,225,38912
cv 80,80,81
mvaa 40,240,152
ds 249,8,76,3072
mvaa 40,248,156
ex 84
cvaa 44,252,156,232
ra 92,225,39936
cv 81,82,83
mvaa 44,244,152
ex 85
mvaa 44,236,148
ex 86
mvaa 44,228,144
ra 96,225,40960
cv 82,84,85
cvaa 16,192,160,232
s_mov_b32 m0,s69
ex 87
mvaa 16,200,164
ra 100,225,41984
cv 83,86,87
mvaa 16,208,168
ds 249,8,77,0
mvaa 16,216,172
ex 112
cvaa 20,220,172,232
ex 113
mvaa 20,212,168
ra 104,225,43008
cv 84,112,113
mvaa 20,204,164
ex 114
mvaa 20,196,160
ra 108,225,44032
cvaa 48,224,160,232
ex 115
mvaa 48,232,164
ds 249,8,77,1024
mvaa 48,240,168
ex 116
mvaa 48,248,172
ra 112,225,45056
cv 85,114,115
cvaa 52,252,172,232
ex 117
mvaa 52,244,168
ra 116,225,46080
cv 86,116,117
mvaa 52,236,164
L_vs16:
s_cbranch_execz L_vr1
ex 118
mvaa 52,228,160
ex 119
cvaa 24,192,176,232
ds 249,8,77,2048
mvaa 24,200,180
ra 120,225,47104
cv 87,118,119
mvaa 24,208,184
ex 88
mvaa 24,216,188
ra 124,225,48128
cvaa 28,220,188,232
mvaa 28,212,184
ex 89
mvaa 28,204,180
ex 90
mvaa 28,196,176
ds 249,8,77,3072
cvaa 56,224,176,232
ex 91
mvaa 56,232,180
ex 92
mvaa 56,240,184
ex 93
mvaa 56,248,188
ex 94
cvaa 60,252,188,232
mvaa 60,244,184
ex 95
mvaa 60,236,180
cv 88,88,89
cv 89,90,91
mvaa 60,228,176
s_waitcnt vmcnt(8) lgkmcnt(0)
s_barrier
maav 0,64,64
ex 120
maav 0,96,68
s_mov_b32 m0,s74
ex 121
maav 4,68,64
ds 255,8,78,0
maav 4,100,68
ex 122
maav 8,72,64
ex 123
maav 8,104,68
cv 90,92,93
cv 91,94,95
maav 12,76,64
ex 124
maav 12,108,68
ex 125
v_smfmac_f32_16x16x64_bf16 v[144:147],v[228:231],v[64:71],v248
cv 92,120,121
maav 16,80,64
ex 126
maav 16,112,68
buffer_load_dword v247,v245,s[36:39],s85 offen
maav 20,84,64
ex 127
maav 20,116,68
cv 93,122,123
cv 94,124,125
maav 24,88,64
cv 95,126,127
maav 24,120,68
ra 192,224,32768
maav 28,92,64
ds 255,8,78,1024
maav 28,124,68
s_lshl_b32 s86,s19,3
s_lshr_b32 s87,s18,2
s_add_u32 s86,s86,s87
maav 32,64,72
ra 196,224,33792
maav 32,96,76
s_add_u32 s85,s85,131072
maav 36,68,72
ra 200,224,34816
maav 36,100,76
s_lshl_b32 s86,s86,7
s_add_u32 s86,s86,83898368
v_mov_b32_e32 v238,1
maav 40,72,72
ds 255,8,78,2048
maav 40,104,76
s_cmp_eq_u32 s12,0
s_cselect_b64 exec,1,0
buffer_atomic_add v238,off,s[8:11],s86 sc1
s_mov_b64 exec,-1
maav 44,76,72
ra 204,224,35840
maav 44,108,76
v_smfmac_f32_16x16x64_bf16 v[148:151],v[228:231],v[72:79],v248
ra 208,224,36864
maav 48,80,72
maav 48,112,76
maav 52,84,72
ds 255,8,78,3072
maav 52,116,76
ra 212,224,37888
maav 56,88,72
ex 0
maav 56,120,76
ra 216,224,38912
maav 60,92,72
ex 1
maav 60,124,76
s_mov_b32 m0,s75
ex 2
zvav 160,64,80
ra 220,224,39936
cv 0,0,1
mvav 160,96,84
ds 255,8,79,0
zvav 164,68,80
ex 3
mvav 164,100,84
ra 224,224,40960
cv 1,2,3
zvav 168,72,80
ex 4
mvav 168,104,84
ra 228,224,41984
zvav 172,76,80
ds 255,8,79,1024
mvav 172,108,84
s_add_u32 s46,s46,16384
ex 5
v_smfmac_f32_16x16x64_bf16 v[152:155],v[228:231],v[80:87],v248
ra 232,224,43008
zvav 176,80,80
s_add_u32 s47,s47,32768
ex 6
mvav 176,112,84
s_add_u32 s95,s47,32768
ex 7
zvav 180,84,80
ra 236,224,44032
cv 2,4,5
mvav 180,116,84
ex 32
zvav 184,88,80
ds 255,8,79,2048
mvav 184,120,84
ex 33
zvav 188,92,80
ra 240,224,45056
cv 3,6,7
mvav 188,124,84
ex 34
mvav 192,64,88
ra 244,224,46080
cv 4,32,33
mvav 192,96,92
ex 35
mvav 196,68,88
ex 36
mvav 196,100,92
ds 255,8,79,3072
mvav 200,72,88
ra 248,224,47104
cv 5,34,35
mvav 200,104,92
s_cmp_ge_i32 s47,s46
ex 37
mvav 204,76,88
s_cselect_b32 s78,s95,s46
ex 38
mvav 204,108,92
s_cselect_b32 s79,s95,268435456
ex 39
v_smfmac_f32_16x16x64_bf16 v[156:159],v[228:231],v[88:95],v248
ra 252,224,48128
mvav 208,80,88
s_add_u32 s79,s79,16384
ex 8
mvav 208,112,92
ex 9
mvav 212,84,88
ex 10
mvav 212,116,92
ex 11
mvav 216,88,88
cv 6,36,37
cv 7,38,39
mvav 216,120,92
ex 12
mvav 220,92,88
ex 13
mvav 220,124,92
s_waitcnt vmcnt(16) lgkmcnt(0)
s_barrier
cvaa 64,192,128,232
s_bitcmp0_b32 s82,s81
s_cbranch_scc1 L_loop
s_bitcmp1_b32 s15,s81
s_cbranch_scc1 L_short
.p2align 6
L_loop:
mvaa 64,200,132
s_add_u32 s81,s81,1
ex 14
mvaa 64,208,136
s_add_u32 s44,s44,1024
ex 15
mvaa 64,216,140
cv 8,8,9
cv 9,10,11
cvaa 68,220,140,232
s_mov_b32 m0,s70
ex 40
mvaa 68,212,136
ds 249,8,78,0
mvaa 68,204,132
ex 41
mvaa 68,196,128
ex 42
cvaa 96,224,128,232
ra 64,225,0
cv 10,12,13
mvaa 96,232,132
ex 43
mvaa 96,240,136
ra 68,225,1024
cv 11,14,15
mvaa 96,248,140
ex 44
cvaa 100,252,140,232
ds 249,8,78,1024
mvaa 100,244,136
ex 45
mvaa 100,236,132
ra 72,225,2048
cv 12,40,41
mvaa 100,228,128
ex 46
cvaa 72,192,144,232
ra 76,225,3072
cv 13,42,43
mvaa 72,200,148
ex 47
mvaa 72,208,152
ex 16
mvaa 72,216,156
ds 249,8,78,2048
cvaa 76,220,156,232
ra 80,225,4096
cv 14,44,45
mvaa 76,212,152
ex 17
mvaa 76,204,148
ra 84,225,5120
cv 15,46,47
mvaa 76,196,144
ex 18
cvaa 104,224,144,232
ex 19
mvaa 104,232,148
ra 88,225,6144
cv 16,16,17
mvaa 104,240,152
ds 249,8,78,3072
mvaa 104,248,156
ex 20
cvaa 108,252,156,232
ra 92,225,7168
cv 17,18,19
mvaa 108,244,152
ex 21
mvaa 108,236,148
ex 22
mvaa 108,228,144
ra 96,225,8192
cv 18,20,21
cvaa 80,192,160,232
s_mov_b32 m0,s71
ex 23
mvaa 80,200,164
ra 100,225,9216
cv 19,22,23
mvaa 80,208,168
ds 249,8,79,0
mvaa 80,216,172
ex 48
cvaa 84,220,172,232
ex 49
mvaa 84,212,168
ra 104,225,10240
cv 20,48,49
mvaa 84,204,164
ex 50
mvaa 84,196,160
ra 108,225,11264
cvaa 112,224,160,232
ex 51
mvaa 112,232,164
ds 249,8,79,1024
mvaa 112,240,168
ex 52
mvaa 112,248,172
ra 112,225,12288
cv 21,50,51
cvaa 116,252,172,232
ex 53
mvaa 116,244,168
ra 116,225,13312
cv 22,52,53
mvaa 116,236,164
ex 54
mvaa 116,228,160
ex 55
cvaa 88,192,176,232
ds 249,8,79,2048
mvaa 88,200,180
ra 120,225,14336
cv 23,54,55
mvaa 88,208,184
ex 24
mvaa 88,216,188
ra 124,225,15360
cvaa 92,220,188,232
buffer_store_dwordx4 v[250:253],v244,s[8:11],s44 offen offset:1024 sc0 sc1
mvaa 92,212,184
ex 25
mvaa 92,204,180
ex 26
mvaa 92,196,176
ds 249,8,79,3072
cvaa 120,224,176,232
ex 27
mvaa 120,232,180
ex 28
mvaa 120,240,184
ex 29
mvaa 120,248,188
ex 30
cvaa 124,252,188,232
buffer_store_dwordx4 v[250:253],v244,s[8:11],s44 offen sc0 sc1
mvaa 124,244,184
ex 31
mvaa 124,236,180
cv 24,24,25
cv 25,26,27
mvaa 124,228,176
s_waitcnt vmcnt(19) lgkmcnt(0)
s_barrier
maav 0,64,0
ex 56
maav 0,96,4
s_mov_b32 m0,s72
ex 57
maav 4,68,0
ds 255,8,76,0
maav 4,100,4
ex 58
maav 8,72,0
ex 59
maav 8,104,4
cv 26,28,29
cv 27,30,31
maav 12,76,0
s_cmp_lg_u32 s80,s81
ex 60
maav 12,108,4
ex 61
v_smfmac_f32_16x16x64_bf16 v[144:147],v[228:231],v[0:7],v248
cv 28,56,57
maav 16,80,0
ex 62
maav 16,112,4
buffer_load_dword v247,v246,s[24:27],s49 offen
maav 20,84,0
ex 63
maav 20,116,4
cv 29,58,59
cv 30,60,61
maav 24,88,0
s_cbranch_scc1 L_evb_r
v_add_u32_e32 v224,s94,v224
s_lshl_b32 s86,s12,12
s_add_u32 s86,s86,131072
v_add_u32_e32 v236,s86,v244
ra 160,236,0
ra 164,236,1024
ra 168,236,2048
ra 172,236,3072
ra 176,236,16384
ra 180,236,17408
ra 184,236,18432
ra 188,236,19456
L_evb_r:
cv 31,62,63
maav 24,120,4
ra 192,224,0
maav 28,92,0
ds 255,8,76,1024
maav 28,124,4
maav 32,64,8
ra 196,224,1024
maav 32,96,12
s_add_u32 s49,s49,32768
maav 36,68,8
ra 200,224,2048
maav 36,100,12
maav 40,72,8
ds 255,8,76,2048
maav 40,104,12
maav 44,76,8
ra 204,224,3072
maav 44,108,12
v_smfmac_f32_16x16x64_bf16 v[148:151],v[228:231],v[8:15],v248
ra 208,224,4096
maav 48,80,8
maav 48,112,12
maav 52,84,8
ds 255,8,76,3072
maav 52,116,12
ra 212,224,5120
maav 56,88,8
ex 64
maav 56,120,12
ra 216,224,6144
maav 60,92,8
ex 65
maav 60,124,12
s_mov_b32 m0,s73
ex 66
mvav 160,64,16
ra 220,224,7168
cv 64,64,65
mvav 160,96,20
ds 255,8,77,0
mvav 164,68,16
ex 67
mvav 164,100,20
ra 224,224,8192
cv 65,66,67
mvav 168,72,16
ex 68
mvav 168,104,20
ra 228,224,9216
mvav 172,76,16
ds 255,8,77,1024
mvav 172,108,20
s_add_u32 s46,s46,16384
ex 69
v_smfmac_f32_16x16x64_bf16 v[152:155],v[228:231],v[16:23],v248
ra 232,224,10240
mvav 176,80,16
s_add_u32 s47,s47,32768
ex 70
mvav 176,112,20
s_add_u32 s95,s47,32768
ex 71
mvav 180,84,16
ra 236,224,11264
cv 66,68,69
mvav 180,116,20
ex 96
mvav 184,88,16
ds 255,8,77,2048
mvav 184,120,20
ex 97
mvav 188,92,16
ra 240,224,12288
cv 67,70,71
mvav 188,124,20
ex 98
mvav 192,64,24
ra 244,224,13312
cv 68,96,97
mvav 192,96,28
ex 99
mvav 196,68,24
ex 100
mvav 196,100,28
ds 255,8,77,3072
mvav 200,72,24
ra 248,224,14336
cv 69,98,99
mvav 200,104,28
s_cmp_ge_i32 s47,s46
ex 101
mvav 204,76,24
s_cselect_b32 s76,s95,s46
ex 102
mvav 204,108,28
s_cselect_b32 s77,s95,268435456
ex 103
v_smfmac_f32_16x16x64_bf16 v[156:159],v[228:231],v[24:31],v248
ra 252,224,15360
mvav 208,80,24
s_add_u32 s77,s77,16384
ex 72
mvav 208,112,28
ex 73
mvav 212,84,24
ex 74
mvav 212,116,28
ex 75
)"
R"(mvav 216,88,24
cv 70,100,101
cv 71,102,103
mvav 216,120,28
ex 76
mvav 220,92,24
ex 77
mvav 220,124,28
s_waitcnt vmcnt(19) lgkmcnt(0)
s_barrier
cvaa 0,192,128,232
mvaa 0,200,132
s_add_u32 s81,s81,1
ex 78
mvaa 0,208,136
s_add_u32 s44,s44,1024
ex 79
mvaa 0,216,140
cv 72,72,73
cv 73,74,75
cvaa 4,220,140,232
s_mov_b32 m0,s68
ex 104
mvaa 4,212,136
ds 249,8,76,0
mvaa 4,204,132
ex 105
mvaa 4,196,128
ex 106
cvaa 32,224,128,232
ra 64,225,32768
cv 74,76,77
mvaa 32,232,132
ex 107
mvaa 32,240,136
ra 68,225,33792
cv 75,78,79
mvaa 32,248,140
ex 108
cvaa 36,252,140,232
ds 249,8,76,1024
mvaa 36,244,136
ex 109
mvaa 36,236,132
ra 72,225,34816
cv 76,104,105
mvaa 36,228,128
ex 110
cvaa 8,192,144,232
ra 76,225,35840
cv 77,106,107
mvaa 8,200,148
ex 111
mvaa 8,208,152
ex 80
mvaa 8,216,156
ds 249,8,76,2048
cvaa 12,220,156,232
ra 80,225,36864
cv 78,108,109
mvaa 12,212,152
ex 81
mvaa 12,204,148
ra 84,225,37888
cv 79,110,111
mvaa 12,196,144
ex 82
cvaa 40,224,144,232
ex 83
mvaa 40,232,148
ra 88,225,38912
cv 80,80,81
mvaa 40,240,152
ds 249,8,76,3072
mvaa 40,248,156
ex 84
cvaa 44,252,156,232
ra 92,225,39936
cv 81,82,83
mvaa 44,244,152
ex 85
mvaa 44,236,148
ex 86
mvaa 44,228,144
ra 96,225,40960
cv 82,84,85
cvaa 16,192,160,232
s_mov_b32 m0,s69
ex 87
mvaa 16,200,164
ra 100,225,41984
cv 83,86,87
mvaa 16,208,168
ds 249,8,77,0
mvaa 16,216,172
ex 112
cvaa 20,220,172,232
ex 113
mvaa 20,212,168
ra 104,225,43008
cv 84,112,113
mvaa 20,204,164
ex 114
mvaa 20,196,160
ra 108,225,44032
cvaa 48,224,160,232
ex 115
mvaa 48,232,164
ds 249,8,77,1024
mvaa 48,240,168
ex 116
mvaa 48,248,172
ra 112,225,45056
cv 85,114,115
cvaa 52,252,172,232
ex 117
mvaa 52,244,168
ra 116,225,46080
cv 86,116,117
mvaa 52,236,164
ex 118
mvaa 52,228,160
ex 119
cvaa 24,192,176,232
ds 249,8,77,2048
mvaa 24,200,180
ra 120,225,47104
cv 87,118,119
mvaa 24,208,184
ex 88
mvaa 24,216,188
ra 124,225,48128
cvaa 28,220,188,232
mvaa 28,212,184
ex 89
mvaa 28,204,180
ex 90
mvaa 28,196,176
ds 249,8,77,3072
cvaa 56,224,176,232
ex 91
mvaa 56,232,180
ex 92
mvaa 56,240,184
ex 93
mvaa 56,248,188
ex 94
cvaa 60,252,188,232
mvaa 60,244,184
ex 95
mvaa 60,236,180
cv 88,88,89
cv 89,90,91
mvaa 60,228,176
s_waitcnt vmcnt(18) lgkmcnt(0)
s_barrier
maav 0,64,64
ex 120
maav 0,96,68
s_mov_b32 m0,s74
ex 121
maav 4,68,64
ds 255,8,78,0
maav 4,100,68
ex 122
maav 8,72,64
ex 123
maav 8,104,68
cv 90,92,93
cv 91,94,95
maav 12,76,64
ex 124
maav 12,108,68
ex 125
v_smfmac_f32_16x16x64_bf16 v[144:147],v[228:231],v[64:71],v248
cv 92,120,121
maav 16,80,64
ex 126
maav 16,112,68
buffer_load_dword v247,v245,s[36:39],s85 offen
maav 20,84,64
ex 127
maav 20,116,68
cv 93,122,123
cv 94,124,125
maav 24,88,64
cv 95,126,127
maav 24,120,68
ra 192,224,32768
maav 28,92,64
ds 255,8,78,1024
maav 28,124,68
maav 32,64,72
ra 196,224,33792
maav 32,96,76
s_add_u32 s85,s85,131072
maav 36,68,72
ra 200,224,34816
maav 36,100,76
maav 40,72,72
ds 255,8,78,2048
maav 40,104,76
maav 44,76,72
ra 204,224,35840
maav 44,108,76
v_smfmac_f32_16x16x64_bf16 v[148:151],v[228:231],v[72:79],v248
ra 208,224,36864
maav 48,80,72
maav 48,112,76
maav 52,84,72
ds 255,8,78,3072
maav 52,116,76
ra 212,224,37888
maav 56,88,72
ex 0
maav 56,120,76
ra 216,224,38912
maav 60,92,72
ex 1
maav 60,124,76
s_mov_b32 m0,s75
ex 2
mvav 160,64,80
ra 220,224,39936
cv 0,0,1
mvav 160,96,84
ds 255,8,79,0
mvav 164,68,80
ex 3
mvav 164,100,84
ra 224,224,40960
cv 1,2,3
mvav 168,72,80
ex 4
mvav 168,104,84
ra 228,224,41984
mvav 172,76,80
ds 255,8,79,1024
mvav 172,108,84
s_add_u32 s46,s46,16384
ex 5
v_smfmac_f32_16x16x64_bf16 v[152:155],v[228:231],v[80:87],v248
ra 232,224,43008
mvav 176,80,80
s_add_u32 s47,s47,32768
ex 6
mvav 176,112,84
s_add_u32 s95,s47,32768
ex 7
mvav 180,84,80
ra 236,224,44032
cv 2,4,5
mvav 180,116,84
ex 32
mvav 184,88,80
ds 255,8,79,2048
mvav 184,120,84
ex 33
mvav 188,92,80
ra 240,224,45056
cv 3,6,7
mvav 188,124,84
ex 34
mvav 192,64,88
ra 244,224,46080
cv 4,32,33
mvav 192,96,92
ex 35
mvav 196,68,88
ex 36
mvav 196,100,92
ds 255,8,79,3072
mvav 200,72,88
ra 248,224,47104
cv 5,34,35
mvav 200,104,92
s_cmp_ge_i32 s47,s46
ex 37
mvav 204,76,88
s_cselect_b32 s78,s95,s46
ex 38
mvav 204,108,92
s_cselect_b32 s79,s95,268435456
ex 39
v_smfmac_f32_16x16x64_bf16 v[156:159],v[228:231],v[88:95],v248
ra 252,224,48128
mvav 208,80,88
s_add_u32 s79,s79,16384
ex 8
mvav 208,112,92
ex 9
mvav 212,84,88
ex 10
mvav 212,116,92
ex 11
mvav 216,88,88
cv 6,36,37
cv 7,38,39
mvav 216,120,92
ex 12
mvav 220,92,88
ex 13
mvav 220,124,92
s_waitcnt vmcnt(17) lgkmcnt(0)
s_barrier
cvaa 64,192,128,232
s_bitcmp0_b32 s82,s81
s_cbranch_scc1 L_loop
s_bitcmp0_b32 s15,s81
s_cbranch_scc1 L_eva_r
L_short:
v_add_f32_e32 v226,v152,v153
v_add_f32_e32 v227,v156,v157
v_subrev_u32_e32 v236,0x23800000,v226
v_subrev_u32_e32 v237,0x23800000,v227
v_max_u32_e32 v236,v236,v237
v_cmp_lt_u32_e64 s[40:41],s48,v236
v_rcp_f32_e32 v226,v226
v_rcp_f32_e32 v227,v227
s_add_u32 s86,s13,4096
v_mov_b32_e32 v236,0
v_mov_b32_e32 v237,0
v_mov_b32_e32 v238,0
v_mov_b32_e32 v239,0
v_mov_b64_e32 v[152:153],0
v_mov_b64_e32 v[156:157],0
v_mul_f32_e32 v226,v242,v226
v_mul_f32_e32 v227,v243,v227
v_add_u32_e32 v225,s94,v225
v_pk_mul_f32 v[160:161],v[160:161],v[226:227] op_sel:[0,0] op_sel_hi:[1,0]
v_pk_mul_f32 v[162:163],v[162:163],v[226:227] op_sel:[0,0] op_sel_hi:[1,0]
v_pk_mul_f32 v[164:165],v[164:165],v[226:227] op_sel:[0,0] op_sel_hi:[1,0]
v_pk_mul_f32 v[166:167],v[166:167],v[226:227] op_sel:[0,0] op_sel_hi:[1,0]
cv 160,160,161
cv 161,162,163
cv 162,164,165
cv 163,166,167
buffer_store_dwordx4 v[160:163],v244,s[8:11],s13 offen offset:0 sc0 sc1
v_pk_mul_f32 v[168:169],v[168:169],v[226:227] op_sel:[0,0] op_sel_hi:[1,0]
v_pk_mul_f32 v[170:171],v[170:171],v[226:227] op_sel:[0,0] op_sel_hi:[1,0]
v_pk_mul_f32 v[172:173],v[172:173],v[226:227] op_sel:[0,0] op_sel_hi:[1,0]
v_pk_mul_f32 v[174:175],v[174:175],v[226:227] op_sel:[0,0] op_sel_hi:[1,0]
cv 168,168,169
cv 169,170,171
cv 170,172,173
cv 171,174,175
buffer_store_dwordx4 v[168:171],v244,s[8:11],s13 offen offset:2048 sc0 sc1
v_pk_mul_f32 v[176:177],v[176:177],v[226:227] op_sel:[0,0] op_sel_hi:[1,0]
v_pk_mul_f32 v[178:179],v[178:179],v[226:227] op_sel:[0,0] op_sel_hi:[1,0]
v_pk_mul_f32 v[180:181],v[180:181],v[226:227] op_sel:[0,0] op_sel_hi:[1,0]
v_pk_mul_f32 v[182:183],v[182:183],v[226:227] op_sel:[0,0] op_sel_hi:[1,0]
cv 176,176,177
cv 177,178,179
cv 178,180,181
v_mfma_f32_32x32x16_bf16 v[160:175],v[236:239],v[236:239],0
cv 179,182,183
buffer_store_dwordx4 v[176:179],v244,s[8:11],s86 offen offset:0 sc0 sc1
v_pk_mul_f32 v[184:185],v[184:185],v[226:227] op_sel:[0,0] op_sel_hi:[1,0]
v_pk_mul_f32 v[186:187],v[186:187],v[226:227] op_sel:[0,0] op_sel_hi:[1,0]
v_pk_mul_f32 v[188:189],v[188:189],v[226:227] op_sel:[0,0] op_sel_hi:[1,0]
v_pk_mul_f32 v[190:191],v[190:191],v[226:227] op_sel:[0,0] op_sel_hi:[1,0]
cv 184,184,185
cv 185,186,187
cv 186,188,189
cv 187,190,191
buffer_store_dwordx4 v[184:187],v244,s[8:11],s86 offen offset:2048 sc0 sc1
v_pk_mul_f32 v[192:193],v[192:193],v[226:227] op_sel:[0,1] op_sel_hi:[1,1]
v_pk_mul_f32 v[194:195],v[194:195],v[226:227] op_sel:[0,1] op_sel_hi:[1,1]
v_pk_mul_f32 v[196:197],v[196:197],v[226:227] op_sel:[0,1] op_sel_hi:[1,1]
v_pk_mul_f32 v[198:199],v[198:199],v[226:227] op_sel:[0,1] op_sel_hi:[1,1]
cv 192,192,193
cv 193,194,195
cv 194,196,197
v_mfma_f32_32x32x16_bf16 v[176:191],v[236:239],v[236:239],0
cv 195,198,199
buffer_store_dwordx4 v[192:195],v244,s[8:11],s13 offen offset:1024 sc0 sc1
v_pk_mul_f32 v[200:201],v[200:201],v[226:227] op_sel:[0,1] op_sel_hi:[1,1]
v_pk_mul_f32 v[202:203],v[202:203],v[226:227] op_sel:[0,1] op_sel_hi:[1,1]
v_pk_mul_f32 v[204:205],v[204:205],v[226:227] op_sel:[0,1] op_sel_hi:[1,1]
v_pk_mul_f32 v[206:207],v[206:207],v[226:227] op_sel:[0,1] op_sel_hi:[1,1]
cv 200,200,201
cv 201,202,203
cv 202,204,205
cv 203,206,207
buffer_store_dwordx4 v[200:203],v244,s[8:11],s13 offen offset:3072 sc0 sc1
v_pk_mul_f32 v[208:209],v[208:209],v[226:227] op_sel:[0,1] op_sel_hi:[1,1]
v_pk_mul_f32 v[210:211],v[210:211],v[226:227] op_sel:[0,1] op_sel_hi:[1,1]
v_pk_mul_f32 v[212:213],v[212:213],v[226:227] op_sel:[0,1] op_sel_hi:[1,1]
v_pk_mul_f32 v[214:215],v[214:215],v[226:227] op_sel:[0,1] op_sel_hi:[1,1]
cv 208,208,209
cv 209,210,211
cv 210,212,213
v_mfma_f32_32x32x16_bf16 v[192:207],v[236:239],v[236:239],0
cv 211,214,215
buffer_store_dwordx4 v[208:211],v244,s[8:11],s86 offen offset:1024 sc0 sc1
v_pk_mul_f32 v[216:217],v[216:217],v[226:227] op_sel:[0,1] op_sel_hi:[1,1]
v_pk_mul_f32 v[218:219],v[218:219],v[226:227] op_sel:[0,1] op_sel_hi:[1,1]
v_pk_mul_f32 v[220:221],v[220:221],v[226:227] op_sel:[0,1] op_sel_hi:[1,1]
v_pk_mul_f32 v[222:223],v[222:223],v[226:227] op_sel:[0,1] op_sel_hi:[1,1]
cv 216,216,217
cv 217,218,219
cv 218,220,221
cv 219,222,223
buffer_store_dwordx4 v[216:219],v244,s[8:11],s86 offen offset:3072 sc0 sc1
s_nop 1
v_mfma_f32_32x32x16_bf16 v[208:223],v[236:239],v[236:239],0
L_eva_r:
s_cmp_lg_u32 s81,14
s_cbranch_scc1 L_loop
s_add_u32 s81,s81,1
s_bitcmp1_b32 170,s20
s_cbranch_scc1 L_odd
L_last:
s_lshl_b32 s86,s21,2
s_sub_u32 s87,s13,s67
s_add_u32 s86,s86,s12
mvaa 64,200,132
s_lshl_b32 s86,s86,8
v_add_u32_e32 v252,s87,v244
s_add_u32 s86,s86,L_last-L_att_pc+1024
v_lshl_add_u32 v226,v244,3,s86
s_lshl_b32 s86,s12,1
s_bfe_u32 s87,s21,65539
mvaa 64,208,136
s_add_u32 s86,s86,s87
v_min_u32_e32 v226,L_att_end-L_att_pc-4,v226
s_lshl_b32 s86,s86,7
s_add_u32 s86,s86,L_pend2-L_att_pc
v_writelane_b32 v226,s86,2
mvaa 64,216,140
s_mov_b64 exec,7
global_load_dword v253,v226,s[96:97]
s_mov_b64 exec,-1
cvaa 68,220,140,232
s_add_u32 m0,s52,32768
v_and_b32_e32 v224,16,v244
v_lshlrev_b32_e32 v224,5,v224
mvaa 68,212,136
s_xor_b32 s51,s62,0
v_lshrrev_b32_e32 v239,4,v244
v_lshrrev_b32_e32 v236,3,v239
mvaa 68,204,132
s_lshl_b32 s87,s12,13
v_and_b32_e32 v237,112,v244
v_and_b32_e32 v238,48,v239
mvaa 68,196,128
s_xor_b32 s87,s87,8192
ra 64,225,0
v_lshlrev_b32_e32 v238,1,v238
cvaa 96,224,128,232
buffer_load_dword v253,v224,s[8:11],s53 offen sc1
mvaa 96,232,132
s_mov_b32 s84,6
ra 68,225,1024
v_xor_b32_e32 v237,v237,v238
mvaa 96,240,136
s_add_u32 s81,s81,1
ra 72,225,2048
v_lshl_add_u32 v245,v236,11,v237
mvaa 96,248,140
s_mov_b32 s84,4
v_add_u32_e32 v246,15360,v245
v_add_u32_e32 v249,30720,v245
cvaa 100,252,140,232
s_andn2_b32 s69,64,s54
ra 76,225,3072
s_waitcnt vmcnt(9)
v_add_u32_e32 v247,46080,v245
mvaa 100,244,136
dl 245,24,51,0
mvaa 100,236,132
s_add_u32 s88,s14,4096
ra 80,225,4096
cv 8,8,9
mvaa 100,228,128
s_lshl_b32 s35,s19,1
ra 84,225,5120
cv 9,10,11
cvaa 72,192,144,232
s_lshr_b32 s33,40569,s35
ra 88,225,6144
cv 10,12,13
mvaa 72,200,148
s_and_b32 s33,s33,3
ra 92,225,7168
v_add_u32_e32 v250,4096,v252
mvaa 72,208,152
dl 246,24,51,1024
mvaa 72,216,156
s_lshl_b32 s33,s33,13
ra 96,225,8192
v_mov_b32_e32 v251,4
cvaa 76,220,156,232
s_lshr_b32 s34,59294,s35
ra 100,225,9216
s_and_b32 s34,s34,3
mvaa 76,212,152
s_lshl_b32 s34,s34,13
ra 104,225,10240
s_xor_b32 s35,s33,s34
mvaa 76,204,148
s_and_b32 s98,s19,3
ra 108,225,11264
s_lshl_b32 s98,s98,13
mvaa 76,196,144
dl 249,24,51,2048
cvaa 104,224,144,232
s_mov_b32 s74,0x300003
ra 112,225,12288
s_mov_b32 s75,0x30000300
mvaa 104,232,148
s_lshl_b32 s71,s12,15
ra 116,225,13312
s_add_u32 s71,s71,s54
mvaa 104,240,152
s_mov_b64 s[44:45],s[30:31]
ra 120,225,14336
s_mov_b32 s46,4194304
mvaa 104,248,156
s_mov_b32 s47,131072
ex 14
cvaa 108,252,156,232
dl 247,24,51,3072
mvaa 108,244,152
s_add_u32 m0,s52,49152
s_xor_b32 s51,s63,0
ra 124,225,15360
mvaa 108,236,148
s_lshl_b32 s70,s23,11
ex 15
mvaa 108,228,144
s_add_u32 s70,s70,s71
ex 40
cvaa 80,192,160,232
ex 41
mvaa 80,200,164
dl 245,24,51,0
mvaa 80,208,168
ex 42
mvaa 80,216,172
ex 43
cvaa 84,220,172,232
ex 44
mvaa 84,212,168
ex 45
mvaa 84,204,164
dl 246,24,51,1024
mvaa 84,196,160
ex 46
cvaa 112,224,160,232
ex 47
mvaa 112,232,164
ex 16
mvaa 112,240,168
ex 17
mvaa 112,248,172
dl 249,24,51,2048
cvaa 116,252,172,232
ex 18
mvaa 116,244,168
ex 19
mvaa 116,236,164
ex 20
mvaa 116,228,160
ex 21
cvaa 88,192,176,232
dl 247,24,51,3072
s_waitcnt vmcnt(8)
v_cmp_ne_u32_e32 vcc,4,v253
mvaa 88,200,180
s_cbranch_vccnz L_spbe
L_sprbe:
ex 22
mvaa 88,208,184
s_xor_b32 s50,s66,262144
ex 23
mvaa 88,216,188
ex 48
cvaa 92,220,188,232
ex 49
mvaa 92,212,184
la 128,252,50,0
mvaa 92,204,180
s_waitcnt lgkmcnt(0)
s_barrier
mvaa 92,196,176
s_mov_b32 m0,s72
ex 50
cvaa 120,224,176,232
s_lshl_b32 s72,s22,11
ex 51
mvaa 120,232,180
s_add_u32 s72,s72,s71
ex 52
mvaa 120,240,184
ds 255,8,76,0
mvaa 120,248,188
s_add_u32 s71,s70,131072
ex 53
cvaa 124,252,188,232
ex 54
mvaa 124,244,184
ex 55
mvaa 124,236,180
ex 24
mvaa 124,228,176
ds 255,8,76,1024
maav 0,64,0
s_cmp_lg_u32 s19,7
s_cbranch_scc1 L_j7e
s_nop 1
s_lshl_b32 s86,s12,12
s_add_u32 s86,s86,131072
v_add_u32_e32 v236,s86,v244
ra 160,236,0
ra 164,236,1024
ra 168,236,2048
ra 172,236,3072
ra 176,236,16384
ra 180,236,17408
ra 184,236,18432
ra 188,236,19456
v_add_u32_e32 v224,s94,v224
s_waitcnt lgkmcnt(0)
L_j7e:
ex 25
maav 0,96,4
ex 26
maav 4,68,0
ex 27
maav 4,100,4
cv 11,14,15
cv 12,40,41
maav 8,72,0
ds 255,8,76,2048
maav 8,104,4
cv 13,42,43
cv 14,44,45
maav 12,76,0
cv 15,46,47
cv 16,16,17
maav 12,108,4
ex 28
v_smfmac_f32_16x16x64_bf16 v[144:147],v[228:231],v[0:7],v248
cv 17,18,19
maav 16,80,0
ds 255,8,76,3072
maav 16,112,4
s_mov_b32 m0,s73
ex 29
maav 20,84,0
s_add_u32 s73,s72,131072
ex 30
maav 20,116,4
ex 31
maav 24,88,0
ex 56
maav 24,120,4
ds 255,8,77,0
maav 28,92,0
ex 57
maav 28,124,4
ex 58
maav 32,64,8
ex 59
maav 32,96,12
ex 60
maav 36,68,8
ds 255,8,77,1024
maav 36,100,12
cv 18,20,21
cv 19,22,23
maav 40,72,8
cv 20,48,49
cv 21,50,51
maav 40,104,12
cv 22,52,53
cv 23,54,55
maav 44,76,8
ex 61
maav 44,108,12
ds 255,8,77,2048
v_smfmac_f32_16x16x64_bf16 v[148:151],v[228:231],v[8:15],v248
cv 24,24,25
maav 48,80,8
ex 62
maav 48,112,12
ex 63
maav 52,84,8
ra 192,244,0
cv 25,26,27
maav 52,116,12
L_vs17:
s_cbranch_execz L_vs21
ds 255,8,77,3072
maav 56,88,8
cv 26,28,29
cv 27,30,31
maav 56,120,12
cv 28,56,57
cv 29,58,59
maav 60,92,8
cv 30,60,61
cv 31,62,63
maav 60,124,12
ex 64
mvav 160,64,16
la 132,252,50,2048
mvav 160,96,20
ex 65
mvav 164,68,16
ex 66
mvav 164,100,20
ex 67
mvav 168,72,16
ex 68
mvav 168,104,20
la 136,250,50,0
mvav 172,76,16
ex 69
mvav 172,108,20
ex 70
v_smfmac_f32_16x16x64_bf16 v[152:155],v[228:231],v[16:23],v248
ra 200,244,2048
mvav 176,80,16
ra 208,244,4096
cv 64,64,65
mvav 176,112,20
la 140,250,50,2048
mvav 180,84,16
ra 216,244,6144
cv 65,66,67
mvav 180,116,20
ex 71
mvav 184,88,16
ex 96
mvav 184,120,20
s_barrier
ex 97
mvav 188,92,16
la 156,250,50,3072
mvav 188,124,20
s_add_u32 m0,s52,131072
ra 196,244,1024
s_xor_b32 s51,s62,1048576
mvav 192,64,24
ra 204,244,3072
cv 66,68,69
mvav 192,96,28
ra 212,244,5120
cv 67,70,71
mvav 196,68,24
ra 220,244,7168
cv 68,96,97
mvav 196,100,28
la 152,250,50,1024
mvav 200,72,24
ex 98
mvav 200,104,28
ex 99
mvav 204,76,24
ra 224,244,8192
cv 69,98,99
mvav 204,108,28
ra 232,244,10240
v_add_u32_e32 v6,s87,v244
v_smfmac_f32_16x16x64_bf16 v[156:159],v[228:231],v[24:31],v248
ra 240,244,12288
mvav 208,80,24
la 148,252,50,3072
mvav 208,112,28
s_xor_b32 s87,s52,4096
ra 248,244,14336
v_add_u32_e32 v7,98304,v6
mvav 212,84,24
s_add_u32 s87,s87,150528
ra 252,244,15360
v_add_u32_e32 v5,65536,v244
mvav 212,116,28
ra 244,244,13312
v_add_u32_e32 v32,s87,v244
mvav 216,88,24
ra 228,244,9216
v_and_b32_e32 v224,960,v244
mvav 216,120,28
la 144,252,50,1024
mvav 220,92,24
s_add_u32 s50,s50,s55
ra 236,244,11264
mvav 220,124,28
ra 64,225,32768
s_waitcnt lgkmcnt(8)
cvaa 24,192,176,232
dl 245,24,51,0
mvaa 24,200,180
ra 96,225,40960
mvaa 24,208,184
ra 68,225,33792
mvaa 24,216,188
dl 246,24,51,1024
cvaa 16,192,160,128
ra 100,225,41984
mvaa 16,200,164
ra 72,225,34816
mvaa 16,208,168
dl 249,24,51,2048
mvaa 16,216,172
ra 104,225,43008
cvaa 28,196,176,232
ra 76,225,35840
mvaa 28,204,180
dl 247,24,51,3072
mvaa 28,212,184
s_add_u32 m0,s52,147456
s_xor_b32 s51,s63,1048576
ra 108,225,44032
mvaa 28,220,188
ra 80,225,36864
cvaa 20,196,160,132
dl 245,24,51,0
mvaa 20,204,164
ra 112,225,45056
mvaa 20,212,168
ra 192,244,16384
mvaa 20,220,172
dl 246,24,51,1024
cvaa 56,224,176,232
ra 200,244,18432
s_waitcnt lgkmcnt(7)
mvaa 56,232,180
ra 208,244,20480
mvaa 56,240,184
dl 249,24,51,2048
mvaa 56,248,188
ra 216,244,22528
cvaa 48,224,160,136
ra 196,244,17408
mvaa 48,232,164
ra 204,244,19456
mvaa 48,240,168
ra 212,244,21504
mvaa 48,248,172
ra 220,244,23552
cvaa 60,228,176,232
ra 224,244,24576
mvaa 60,236,180
ra 232,244,26624
mvaa 60,244,184
ra 240,244,28672
mvaa 60,252,188
ra 248,244,30720
cvaa 52,228,160,140
ra 84,225,37888
mvaa 52,236,164
ra 116,225,46080
mvaa 52,244,168
la 160,252,50,0
ra 228,244,25600
mvaa 52,252,172
ra 236,244,27648
s_waitcnt lgkmcnt(10)
cvaa 8,192,176,128
ra 244,244,29696
mvaa 8,200,180
la 164,252,50,2048
ra 252,244,31744
mvaa 8,208,184
ra 88,225,38912
mvaa 8,216,188
ra 120,225,47104
cvaa 12,196,176,132
la 168,250,50,0
mvaa 12,204,180
ra 92,225,39936
ra 124,225,48128
s_waitcnt lgkmcnt(9)
mvaa 12,212,184
ex 100
mvaa 12,220,188
la 172,250,50,2048
cvaa 40,224,176,136
ex 101
mvaa 40,232,180
ex 102
mvaa 40,240,184
ex 103
mvaa 40,248,188
cv 70,100,101
cv 71,102,103
s_waitcnt lgkmcnt(4)
cvaa 44,228,176,140
ex 72
mvaa 44,236,180
s_waitcnt vmcnt(18) lgkmcnt(0)
s_barrier
ex 73
mvaa 44,244,184
la 176,252,50,1024
mvaa 44,252,188
ex 74
maav 0,64,64
ex 75
maav 0,96,68
la 188,250,50,3072
maav 4,68,64
ra 192,5,0
cv 72,72,73
maav 4,100,68
ra 196,5,1024
cv 73,74,75
maav 8,72,64
la 184,250,50,1024
ds_write_b128 v6,a[0:3] offset:0
maav 8,104,68
ex 76
maav 12,76,64
ex 77
maav 12,108,68
la 180,252,50,3072
ds_write_b128 v6,a[4:7] offset:1024
v_smfmac_f32_16x16x64_bf16 v[144:147],v[228:231],v[64:71],v248
cv 74,76,77
maav 16,80,64
ex 78
maav 16,112,68
ds_write_b128 v6,a[8:11] offset:2048
ex 79
maav 20,84,64
ex 104
maav 20,116,68
ds_write_b128 v6,a[12:15] offset:3072
ex 105
maav 24,88,64
ex 106
ex 107
ex 108
maav 24,120,68
ds_write_b128 v6,a[16:19] offset:4096
cv 75,78,79
maav 28,92,64
ex 109
ex 110
ex 111
maav 28,124,68
cv 76,104,105
cv 77,106,107
cv 78,108,109
cv 79,110,111
maav 32,64,72
ds_write_b128 v6,a[20:23] offset:5120
maav 32,96,76
ex 80
maav 36,68,72
ex 81
maav 36,100,76
ds_write_b128 v6,a[24:27] offset:6144
maav 40,72,72
ex 82
maav 40,104,76
ex 83
maav 44,76,72
ds_write_b128 v6,a[28:31] offset:7168
maav 44,108,76
cv 80,80,81
cv 81,82,83
v_smfmac_f32_16x16x64_bf16 v[148:151],v[228:231],v[72:79],v248
ex 84
maav 48,80,72
ds_write_b128 v7,a[32:35] offset:0
maav 48,112,76
ex 85
ex 86
ex 87
maav 52,84,72
ex 112
ex 113
maav 52,116,76
ds_write_b128 v7,a[36:39] offset:1024
cv 82,84,85
maav 56,88,72
ex 114
ex 115
ex 116
maav 56,120,76
ex 117
ex 118
ex 119
maav 60,92,72
ds_write_b128 v7,a[40:43] offset:2048
cv 83,86,87
maav 60,124,76
cv 84,112,113
cv 85,114,115
cv 86,116,117
cv 87,118,119
mvav 160,64,80
ex 88
mvav 160,96,84
ds_write_b128 v7,a[44:47] offset:3072
mvav 164,68,80
)"
R"(v_add_f32_e32 v130,v144,v145
v_add_f32_e32 v131,v148,v149
mvav 164,100,84
ex 89
mvav 168,72,80
ds_write_b128 v7,a[48:51] offset:4096
mvav 168,104,84
ex 90
mvav 172,76,80
ex 91
mvav 172,108,84
ds_write_b128 v32,v[128:131]
v_smfmac_f32_16x16x64_bf16 v[152:155],v[228:231],v[80:87],v248
ex 92
ex 93
mvav 176,80,80
ds_write_b128 v7,a[52:55] offset:5120
ex 94
mvav 176,112,84
ra 200,5,2048
ra 204,5,3072
ex 95
cv 88,88,89
mvav 180,84,80
ra 208,5,4096
ra 212,5,5120
cv 89,90,91
cv 90,92,93
ex 120
mvav 180,116,84
ds_write_b128 v7,a[56:59] offset:6144
ex 121
mvav 184,88,80
ex 122
ex 123
cv 91,94,95
mvav 184,120,84
ex 124
ex 125
mvav 188,92,80
ds_write_b128 v7,a[60:63] offset:7168
ex 126
mvav 188,124,84
ra 216,5,6144
ra 220,5,7168
ra 224,5,8192
ra 232,5,10240
ex 127
cv 92,120,121
mvav 192,64,88
ra 240,5,12288
ra 248,5,14336
ra 228,5,9216
ra 236,5,11264
ra 244,5,13312
ra 252,5,15360
cv 93,122,123
cv 94,124,125
cv 95,126,127
v_xor_b32_e32 v6,8192,v6
s_nop 0
mvav 192,96,92
ex 16
mvav 196,68,88
s_waitcnt lgkmcnt(10)
s_xor_b32 s87,s87,0
mvav 196,100,92
s_xor_b32 s50,s66,294912
s_barrier
v_xor_b32_e32 v7,8192,v7
mvav 200,72,88
la 4,252,50,0
ra 64,5,16384
ra 96,5,24576
rv 64,6,0
rv 68,6,1024
rv 72,6,2048
mvav 200,104,92
rv 76,6,3072
rv 80,6,4096
rv 84,6,5120
ex 17
ex 18
mvav 204,76,88
rv 96,7,0
rv 100,7,1024
ex 19
mvav 204,108,92
la 12,252,50,2048
rv 104,7,2048
rv 108,7,3072
cv 16,16,17
v_smfmac_f32_16x16x64_bf16 v[156:159],v[228:231],v[88:95],v248
rv 112,7,4096
rv 116,7,5120
ex 20
ex 21
mvav 208,80,88
rv 120,7,6144
rv 124,7,7168
ex 22
ex 23
mvav 208,112,92
la 20,250,50,0
ra 68,5,17408
ra 100,5,25600
cv 17,18,19
cv 18,20,21
mvav 212,84,88
ra 72,5,18432
ra 104,5,26624
ex 48
ex 49
mvav 212,116,92
ra 76,5,19456
ra 108,5,27648
ex 50
ex 51
mvav 216,88,88
la 28,250,50,2048
ra 80,5,20480
ra 112,5,28672
ra 84,5,21504
mvav 216,120,92
ra 116,5,29696
cv 19,22,23
ex 52
ex 53
mvav 220,92,88
ra 88,5,22528
cv 20,48,49
ex 54
ex 55
mvav 220,124,92
la 60,250,50,3072
ra 120,5,30720
cv 21,50,51
cv 22,52,53
cv 23,54,55
s_bitcmp1_b32 s82,s81
s_cbranch_scc1 L_pend2
L_ret_eb4:
mvav 160,192,16
ra 92,5,23552
rv 88,6,6144
ra 124,5,31744
rv 92,6,7168
ex 24
s_waitcnt lgkmcnt(10)
mvav 160,224,20
ex 25
ex 26
v_smfmac_f32_16x16x64_bf16 v[152:155],v[228:231],v[16:23],v248
ex 27
v_xor_b32_e32 v32,4096,v32
mvav 164,196,16
la 52,250,50,1024
ds_read_b128 v[236:239],v32
cv 24,24,25
ex 28
mvav 164,228,20
ex 29
cv 25,26,27
ex 30
ex 31
mvav 168,200,16
ex 56
ex 57
mvav 168,232,20
la 44,252,50,3072
cv 26,28,29
ex 58
ex 59
mvav 172,204,16
ex 60
ex 61
ex 62
mvav 172,236,20
s_waitcnt lgkmcnt(0)
s_barrier
cv 27,30,31
ex 63
mvav 176,208,16
la 36,252,50,1024
cv 28,56,57
cv 29,58,59
cv 30,60,61
cv 31,62,63
mvav 176,240,20
s_add_u32 m0,s52,147456
ex 8
s_xor_b32 s51,s63,1048576
ex 9
ex 10
mvav 180,212,16
s_add_u32 s50,s50,s55
ex 11
cv 8,8,9
cv 9,10,11
ex 12
mvav 180,244,20
dl 247,24,51,3072
ex 13
ex 14
mvav 184,216,16
s_add_u32 m0,s52,0
ex 15
ex 40
ex 41
mvav 184,248,20
s_xor_b32 s51,s64,0
ex 42
ex 43
ex 44
mvav 188,220,16
la 0,252,50,0
ex 45
ex 46
mvav 188,252,20
ex 47
cv 10,12,13
cv 11,14,15
cv 12,40,41
cv 13,42,43
cv 14,44,45
v_smfmac_f32_16x16x64_bf16 v[156:159],v[228:231],v[24:31],v248
cv 15,46,47
v_bfe_u32 v225,v244,1,5
s_nop 0
v_smfmac_f32_16x16x64_bf16 v[156:159],v[228:231],v[8:15],v248
v_lshl_add_u32 v224,v224,1,v225
v_bfe_u32 v225,v244,7,2
v_lshl_add_u32 v224,v225,5,v224
mvav 192,192,24
la 8,252,50,2048
v_xor_b32_e32 v224,s69,v224
v_add_f32_e32 v152,v152,v153
v_add_f32_e32 v137,v152,v238
v_subrev_u32_e32 v236,0x23800000,v137
mvav 192,224,28
v_rcp_f32_e32 v137,v137
mvav 192,64,8
tv 128,224,32768
v_mul_f32_e32 v232,v240,v137
v_mul_f32_e32 v64,v232,v64
v_mul_f32_e32 v65,v232,v65
v_mul_f32_e32 v66,v232,v66
mvav 192,96,12
la 16,250,50,0
v_mul_f32_e32 v67,v232,v67
v_mul_f32_e32 v68,v232,v68
v_mul_f32_e32 v69,v232,v69
v_mul_f32_e32 v70,v232,v70
mvav 196,196,24
v_fmac_f32_e32 v64,v232,v160
v_fmac_f32_e32 v65,v232,v161
v_mul_f32_e32 v71,v232,v71
mvav 196,228,28
v_fmac_f32_e32 v66,v232,v162
v_fmac_f32_e32 v67,v232,v163
v_fmac_f32_e32 v68,v232,v164
v_fmac_f32_e32 v69,v232,v165
v_fmac_f32_e32 v70,v232,v166
mvav 196,68,8
la 24,250,50,2048
v_fmac_f32_e32 v71,v232,v167
cv 160,64,65
cv 161,66,67
cv 162,68,69
cv 163,70,71
mvav 196,100,12
tv 130,224,34816
tv 132,224,36864
mvav 200,200,24
tv 134,224,38912
tv 144,224,40960
v_mul_f32_e32 v72,v232,v72
v_mul_f32_e32 v73,v232,v73
v_mul_f32_e32 v74,v232,v74
mvav 200,232,28
buffer_store_dwordx4 v[160:163],v244,s[8:11],s14 offen offset:0 sc0 sc1
v_mul_f32_e32 v75,v232,v75
v_mul_f32_e32 v76,v232,v76
v_mul_f32_e32 v77,v232,v77
v_mul_f32_e32 v78,v232,v78
mvav 200,72,8
v_fmac_f32_e32 v72,v232,v168
v_fmac_f32_e32 v73,v232,v169
v_mul_f32_e32 v79,v232,v79
mvav 200,104,12
L_vs18:
s_cbranch_execz L_vs22
v_fmac_f32_e32 v74,v232,v170
v_fmac_f32_e32 v75,v232,v171
v_fmac_f32_e32 v76,v232,v172
v_fmac_f32_e32 v77,v232,v173
v_fmac_f32_e32 v78,v232,v174
mvav 204,204,24
la 56,250,50,3072
v_fmac_f32_e32 v79,v232,v175
cv 168,72,73
cv 169,74,75
cv 170,76,77
cv 171,78,79
mvav 204,236,28
tv 146,224,43008
tv 148,224,45056
mvav 204,76,8
tv 150,224,47104
v_mul_f32_e32 v80,v232,v80
v_mul_f32_e32 v81,v232,v81
v_mul_f32_e32 v82,v232,v82
mvav 204,108,12
buffer_store_dwordx4 v[168:171],v244,s[8:11],s14 offen offset:2048 sc0 sc1
v_mul_f32_e32 v83,v232,v83
v_mul_f32_e32 v84,v232,v84
v_mul_f32_e32 v85,v232,v85
v_mul_f32_e32 v86,v232,v86
v_mul_f32_e32 v87,v232,v87
mvav 208,208,24
v_fmac_f32_e32 v80,v232,v176
v_fmac_f32_e32 v81,v232,v177
v_fmac_f32_e32 v82,v232,v178
mvav 208,240,28
v_fmac_f32_e32 v83,v232,v179
v_fmac_f32_e32 v84,v232,v180
v_fmac_f32_e32 v85,v232,v181
cv 176,80,81
cv 177,82,83
mvav 208,80,8
la 48,250,50,1024
v_fmac_f32_e32 v86,v232,v182
v_fmac_f32_e32 v87,v232,v183
cv 178,84,85
cv 179,86,87
mvav 208,112,12
v_mul_f32_e32 v88,v232,v88
v_mul_f32_e32 v89,v232,v89
v_mul_f32_e32 v90,v232,v90
mvav 212,212,24
v_mul_f32_e32 v91,v232,v91
v_mul_f32_e32 v92,v232,v92
v_mul_f32_e32 v93,v232,v93
v_mul_f32_e32 v94,v232,v94
v_mul_f32_e32 v95,v232,v95
mvav 212,244,28
buffer_store_dwordx4 v[176:179],v244,s[8:11],s88 offen offset:0 sc0 sc1
v_fmac_f32_e32 v88,v232,v184
v_fmac_f32_e32 v89,v232,v185
v_fmac_f32_e32 v90,v232,v186
v_fmac_f32_e32 v91,v232,v187
cv 184,88,89
mvav 212,84,8
v_fmac_f32_e32 v92,v232,v188
cv 185,90,91
mvav 212,116,12
v_fmac_f32_e32 v93,v232,v189
v_fmac_f32_e32 v94,v232,v190
v_fmac_f32_e32 v95,v232,v191
cv 186,92,93
cv 187,94,95
mvav 216,216,24
la 40,252,50,3072
v_add_f32_e32 v156,v156,v157
v_add_f32_e32 v141,v156,v239
v_subrev_u32_e32 v237,0x23800000,v141
v_rcp_f32_e32 v141,v141
mvav 216,248,28
v_mul_f32_e32 v233,v241,v141
v_mul_f32_e32 v96,v233,v96
mvav 216,88,8
v_mul_f32_e32 v97,v233,v97
v_mul_f32_e32 v98,v233,v98
v_mul_f32_e32 v99,v233,v99
v_mul_f32_e32 v100,v233,v100
mvav 216,120,12
buffer_store_dwordx4 v[184:187],v244,s[8:11],s88 offen offset:2048 sc0 sc1
v_fmac_f32_e32 v96,v233,v192
v_fmac_f32_e32 v97,v233,v193
v_mul_f32_e32 v101,v233,v101
v_mul_f32_e32 v102,v233,v102
v_mul_f32_e32 v103,v233,v103
mvav 220,220,24
v_fmac_f32_e32 v98,v233,v194
v_fmac_f32_e32 v99,v233,v195
mvav 220,252,28
v_fmac_f32_e32 v100,v233,v196
v_fmac_f32_e32 v101,v233,v197
cv 192,96,97
cv 193,98,99
mvav 220,92,8
la 32,252,50,1024
v_fmac_f32_e32 v102,v233,v198
v_fmac_f32_e32 v103,v233,v199
cv 194,100,101
cv 195,102,103
mvav 220,124,12
v_xor_b32_e32 v225,32,v224
v_xor_b32_e32 v226,64,v224
s_waitcnt lgkmcnt(0)
zvva 0,128,128
tv 138,225,34816
tv 142,225,38912
tv 152,225,40960
s_waitcnt vmcnt(36)
mvva 0,132,132
tv 136,225,32768
tv 154,225,43008
mvva 0,144,136
tv 140,225,36864
tv 158,225,47104
mvva 0,148,140
tv 156,225,45056
v_mul_f32_e32 v104,v233,v104
v_mul_f32_e32 v105,v233,v105
v_mul_f32_e32 v106,v233,v106
zvva 4,148,156
buffer_store_dwordx4 v[192:195],v244,s[8:11],s14 offen offset:1024 sc0 sc1
v_mul_f32_e32 v107,v233,v107
v_mul_f32_e32 v108,v233,v108
v_mul_f32_e32 v109,v233,v109
v_mul_f32_e32 v110,v233,v110
mvva 4,144,152
v_fmac_f32_e32 v104,v233,v200
v_fmac_f32_e32 v105,v233,v201
v_fmac_f32_e32 v106,v233,v202
v_mul_f32_e32 v111,v233,v111
mvva 4,132,148
v_fmac_f32_e32 v107,v233,v203
v_fmac_f32_e32 v108,v233,v204
v_fmac_f32_e32 v109,v233,v205
cv 200,104,105
cv 201,106,107
mvva 4,128,144
v_fmac_f32_e32 v110,v233,v206
v_fmac_f32_e32 v111,v233,v207
cv 202,108,109
cv 203,110,111
s_waitcnt vmcnt(16)
zvva 8,128,160
v_cmp_lt_u32_e64 s[42:43],s48,v236
v_mul_f32_e32 v112,v233,v112
s_or_b64 s[40:41],s[40:41],s[42:43]
v_cmp_lt_u32_e64 s[42:43],s48,v237
mvva 8,132,164
s_or_b64 s[40:41],s[40:41],s[42:43]
v_mul_f32_e32 v113,v233,v113
v_mul_f32_e32 v114,v233,v114
v_mul_f32_e32 v115,v233,v115
mvva 8,144,168
dl 245,36,51,0
v_mul_f32_e32 v116,v233,v116
v_mul_f32_e32 v117,v233,v117
v_mul_f32_e32 v118,v233,v118
v_mul_f32_e32 v119,v233,v119
v_mul_f32_e32 v120,v233,v120
mvva 8,148,172
v_fmac_f32_e32 v112,v233,v208
v_fmac_f32_e32 v113,v233,v209
v_fmac_f32_e32 v114,v233,v210
v_fmac_f32_e32 v115,v233,v211
zvva 12,148,188
v_fmac_f32_e32 v116,v233,v212
v_fmac_f32_e32 v117,v233,v213
cv 208,112,113
cv 209,114,115
mvva 12,144,184
v_fmac_f32_e32 v118,v233,v214
v_fmac_f32_e32 v119,v233,v215
cv 210,116,117
cv 211,118,119
mvva 12,132,180
v_mul_f32_e32 v121,v233,v121
v_mul_f32_e32 v122,v233,v122
v_mul_f32_e32 v123,v233,v123
mvva 12,128,176
v_mul_f32_e32 v124,v233,v124
v_mul_f32_e32 v125,v233,v125
v_mul_f32_e32 v126,v233,v126
v_mul_f32_e32 v127,v233,v127
s_waitcnt lgkmcnt(0)
zvva 28,136,176
buffer_store_dwordx4 v[200:203],v244,s[8:11],s14 offen offset:3072 sc0 sc1
tv 144,226,40960
v_fmac_f32_e32 v120,v233,v216
v_fmac_f32_e32 v121,v233,v217
v_fmac_f32_e32 v122,v233,v218
v_fmac_f32_e32 v123,v233,v219
mvva 28,140,180
tv 128,226,32768
tv 130,226,34816
cv 216,120,121
cv 217,122,123
mvva 28,152,184
tv 132,226,36864
tv 134,226,38912
v_fmac_f32_e32 v124,v233,v220
v_fmac_f32_e32 v125,v233,v221
v_fmac_f32_e32 v126,v233,v222
mvva 28,156,188
tv 146,226,43008
v_fmac_f32_e32 v127,v233,v223
cv 218,124,125
cv 219,126,127
zvva 24,156,172
tv 148,226,45056
tv 150,226,47104
mvva 24,152,168
v_xor_b32_e32 v227,96,v224
v_add_u32_e32 v232,131072,v224
mvva 24,140,164
buffer_store_dwordx4 v[208:211],v244,s[8:11],s88 offen offset:1024 sc0 sc1
mvva 24,136,160
v_add_u32_e32 v233,131072,v225
v_add_u32_e32 v234,131072,v226
zvva 20,136,144
v_add_u32_e32 v235,131072,v227
v_mov_b32_e32 v242,83914752
mvva 20,140,148
v_add_u32_e32 v228,65536,v224
v_add_u32_e32 v229,65536,v225
mvva 20,152,152
v_add_u32_e32 v230,65536,v226
v_add_u32_e32 v231,65536,v227
mvva 20,156,156
v_and_b32_e32 v243,240,v244
v_lshlrev_b32_e32 v243,7,v243
zvva 16,156,140
buffer_store_dwordx4 v[216:219],v244,s[8:11],s88 offen offset:3072 sc0 sc1
s_cmp_lg_u64 s[40:41],0
s_cbranch_scc1 L_redo
L_redo_r4:
mvva 16,152,136
tv 156,227,45056
v_mov_b32_e32 v236,16256
mvva 16,140,132
tv 158,227,47104
v_and_b32_e32 v248,256,v244
mvva 16,136,128
tv 152,227,40960
v_lshrrev_b32_e32 v248,3,v248
s_waitcnt lgkmcnt(3)
zvva 32,128,128
tv 140,227,36864
v_add_u32_e32 v243,v243,v248
mvva 32,132,132
tv 136,227,32768
tv 138,227,34816
mvva 32,144,136
dl 246,36,51,1024
tv 142,227,38912
mvva 32,148,140
tv 154,227,43008
v_and_b32_e32 v248,512,v244
zvva 36,148,156
v_lshrrev_b32_e32 v248,5,v248
v_add_u32_e32 v243,v243,v248
mvva 36,144,152
v_and_b32_e32 v248,16,v244
v_lshlrev_b32_e32 v248,v248,v236
mvva 36,132,148
v_cndmask_b32_e64 v238,0,v248,s[74:75]
s_mov_b32 s74,0xc0000c
mvva 36,128,144
s_mov_b32 s75,0xc0000c00
v_mov_b32_e32 v240,0
v_cndmask_b32_e64 v239,0,v248,s[74:75]
zvva 40,128,160
dl 249,36,51,2048
mvva 40,132,164
v_mov_b32_e32 v241,0
v_mov_b32_e32 v236,0
mvva 40,144,168
v_mov_b32_e32 v237,0
mvva 40,148,172
zvva 44,148,188
mvva 44,144,184
mvva 44,132,180
dl 247,36,51,3072
s_add_u32 m0,s52,16384
s_xor_b32 s51,s65,0
mvva 44,128,176
tv 144,224,57344
s_waitcnt lgkmcnt(1)
zvva 60,136,176
tv 132,224,53248
tv 134,224,55296
mvva 60,140,180
tv 128,224,49152
tv 130,224,51200
mvva 60,152,184
mvva 60,156,188
tv 146,224,59392
zvva 56,156,172
dl 245,36,51,0
tv 148,224,61440
tv 150,224,63488
mvva 56,152,168
mvva 56,140,164
mvva 56,136,160
zvva 52,136,144
mvva 52,140,148
mvva 52,152,152
dl 246,36,51,1024
mvva 52,156,156
zvva 48,156,140
mvva 48,152,136
mvva 48,140,132
tv 156,225,61440
mvva 48,136,128
tv 152,225,57344
s_waitcnt lgkmcnt(2)
zvva 64,128,128
dl 249,36,51,2048
tv 140,225,53248
mvva 64,132,132
tv 136,225,49152
tv 138,225,51200
mvva 64,144,136
tv 142,225,55296
mvva 64,148,140
tv 154,225,59392
zvva 68,148,156
tv 158,225,63488
mvva 68,144,152
mvva 68,132,148
dl 247,36,51,3072
s_lshl_b32 s51,s33,5
s_add_u32 m0,s52,98304
s_xor_b32 s51,s64,s51
mvva 68,128,144
zvva 72,128,160
mvva 72,132,164
s_waitcnt vmcnt(18)
s_mov_b32 s90,0
mvva 72,144,168
s_xor_b32 s50,s67,0
mvva 72,148,172
zvva 76,148,188
dl 245,36,51,0
mvva 76,144,184
mvva 76,132,180
tv 148,226,61440
mvva 76,128,176
tv 144,226,57344
s_waitcnt lgkmcnt(2)
zvva 92,136,176
tv 132,226,53248
mvva 92,140,180
tv 128,226,49152
tv 130,226,51200
mvva 92,152,184
dl 246,36,51,1024
tv 134,226,55296
mvva 92,156,188
tv 146,226,59392
zvva 88,156,172
tv 150,226,63488
mvva 88,152,168
mvva 88,140,164
mvva 88,136,160
zvva 84,136,144
dl 249,36,51,2048
mvva 84,140,148
mvva 84,152,152
mvva 84,156,156
zvva 80,156,140
mvva 80,152,136
mvva 80,140,132
dl 247,36,51,3072
mvva 80,136,128
s_lshl_b32 s51,s33,5
tv 152,227,57344
s_add_u32 m0,s52,114688
s_waitcnt lgkmcnt(1)
zvva 96,128,128
s_xor_b32 s51,s65,s51
tv 140,227,53248
tv 142,227,55296
mvva 96,132,132
tv 136,227,49152
tv 138,227,51200
mvva 96,144,136
mvva 96,148,140
tv 154,227,59392
zvva 100,148,156
lv 164,252,50,0
tv 156,227,61440
tv 158,227,63488
mvva 100,144,152
mvva 100,132,148
mvva 100,128,144
zvva 104,128,160
mvva 104,132,164
mvva 104,144,168
lv 172,252,50,2048
mvva 104,148,172
zvva 108,148,188
mvva 108,144,184
mvva 108,132,180
mvva 108,128,176
s_barrier
s_waitcnt lgkmcnt(0)
zvva 124,136,176
lv 180,250,50,0
tv 132,232,4096
tv 144,232,8192
tv 146,232,10240
mvva 124,140,180
tv 128,232,0
tv 130,232,2048
mvva 124,152,184
tv 134,232,6144
mvva 124,156,188
tv 148,232,12288
tv 150,232,14336
zvva 120,156,172
mvva 120,152,168
mvva 120,140,164
lv 188,250,50,2048
mvva 120,136,160
zvva 116,136,144
mvva 116,140,148
mvva 116,152,152
mvva 116,156,156
zvva 112,156,140
lv 220,250,50,3072
mvva 112,152,136
mvva 112,140,132
tv 156,233,12288
mvva 112,136,128
tv 152,233,8192
s_waitcnt lgkmcnt(2)
mvva 0,128,4
tv 140,233,4096
mvva 0,132,12
tv 136,233,0
tv 138,233,2048
mvva 0,144,20
lv 212,250,50,1024
tv 142,233,6144
mvva 0,148,28
tv 154,233,10240
mvva 4,148,60
tv 158,233,14336
mvva 4,144,52
mvva 4,132,44
mvva 4,128,36
mvva 8,128,0
lv 204,252,50,3072
mvva 8,132,8
mvva 8,144,16
mvva 8,148,24
mvva 12,148,56
s_waitcnt vmcnt(27)
mvva 12,144,48
s_waitcnt vmcnt(25)
mvva 12,132,40
lv 196,252,50,1024
s_waitcnt vmcnt(24)
mvva 12,128,32
tv 144,234,8192
s_waitcnt lgkmcnt(1)
mvva 28,136,32
tv 132,234,4096
tv 134,234,6144
mvva 28,140,40
tv 128,234,0
tv 130,234,2048
mvva 28,152,48
mvva 28,156,56
tv 146,234,10240
mvva 24,156,24
dl 245,36,51,0
s_waitcnt vmcnt(20)
s_barrier
tv 148,234,12288
tv 150,234,14336
mvva 24,152,16
mvva 24,140,8
mvva 24,136,0
mvva 20,136,36
mvva 20,140,44
mvva 20,152,52
s_cmp_eq_u32 s12,0
s_cselect_b64 exec,1,0
buffer_store_dword v251,off,s[8:11],s61 sc0 sc1
s_mov_b64 exec,-1
mvva 20,156,60
mvva 16,156,28
mvva 16,152,20
mvva 16,140,12
tv 156,235,12288
mvva 16,136,4
tv 152,235,8192
s_waitcnt lgkmcnt(2)
mvva 32,128,4
dl 246,36,51,1024
tv 140,235,4096
mvva 32,132,12
tv 136,235,0
tv 138,235,2048
mvva 32,144,20
tv 142,235,6144
mvva 32,148,28
tv 154,235,10240
mvva 36,148,60
tv 158,235,14336
mvva 36,144,52
mvva 36,132,44
dl 249,36,51,2048
mvva 36,128,36
mvva 40,128,0
mvva 40,132,8
mvva 40,144,16
mvva 40,148,24
mvva 44,148,56
dl 247,36,51,3072
s_lshl_b32 s51,s34,5
s_add_u32 m0,s52,65536
s_xor_b32 s51,s64,s51
mvva 44,144,48
tv 148,232,28672
mvva 44,132,40
s_xor_b32 s53,s60,s33
tv 150,232,30720
mvva 44,128,32
tv 144,232,24576
s_waitcnt lgkmcnt(3)
mvva 60,136,32
tv 132,232,20480
mvva 60,140,40
tv 128,232,16384
tv 130,232,18432
mvva 60,152,48
dl 245,36,51,0
tv 134,232,22528
mvva 60,156,56
tv 146,232,26624
mvva 56,156,24
mvva 56,152,16
mvva 56,140,8
mvva 56,136,0
mvva 52,136,36
buffer_load_dword v253,v242,s[8:11],s53 offen sc1
mvva 52,140,44
mvva 52,152,52
mvva 52,156,60
mvva 48,156,28
mvva 48,152,20
mvva 48,140,12
dl 246,36,51,1024
mvva 48,136,4
tv 152,233,24576
s_waitcnt lgkmcnt(1)
mvva 64,128,4
tv 140,233,20480
tv 142,233,22528
mvva 64,132,12
tv 136,233,16384
tv 138,233,18432
mvva 64,144,20
mvva 64,148,28
tv 154,233,26624
mvva 68,148,60
dl 249,36,51,2048
tv 156,233,28672
tv 158,233,30720
mvva 68,144,52
mvva 68,132,44
mvva 68,128,36
mvva 72,128,0
mvva 72,132,8
mvva 72,144,16
dl 247,36,51,3072
s_lshl_b32 s51,s34,5
s_add_u32 m0,s52,81920
s_xor_b32 s51,s65,s51
mvva 72,148,24
mvva 76,148,56
mvva 76,144,48
mvva 76,132,40
tv 148,234,28672
mvva 76,128,32
L_vs19:
s_cbranch_execz L_vs23
tv 144,234,24576
s_waitcnt lgkmcnt(2)
mvva 92,136,32
dl 245,36,51,0
tv 132,234,20480
mvva 92,140,40
tv 128,234,16384
tv 130,234,18432
mvva 92,152,48
tv 134,234,22528
mvva 92,156,56
tv 146,234,26624
mvva 88,156,24
tv 150,234,30720
mvva 88,152,16
mvva 88,140,8
dl 246,36,51,1024
mvva 88,136,0
mvva 84,136,36
mvva 84,140,44
mvva 84,152,52
mvva 84,156,60
mvva 80,156,28
dl 249,36,51,2048
mvva 80,152,20
mvva 80,140,12
tv 156,235,28672
mvva 80,136,4
tv 152,235,24576
s_waitcnt lgkmcnt(2)
mvva 96,128,4
tv 140,235,20480
mvva 96,132,12
tv 136,235,16384
tv 138,235,18432
mvva 96,144,20
dl 247,36,51,3072
s_lshl_b32 s51,s35,5
s_add_u32 m0,s52,32768
s_xor_b32 s51,s64,s51
tv 142,235,22528
mvva 96,148,28
tv 154,235,26624
mvva 100,148,60
tv 158,235,30720
mvva 100,144,52
mvva 100,132,44
mvva 100,128,36
mvva 104,128,0
dl 245,36,51,0
mvva 104,132,8
mvva 104,144,16
mvva 104,148,24
mvva 108,148,56
mvva 108,144,48
mvva 108,132,40
dl 246,36,51,1024
mvva 108,128,32
s_waitcnt vmcnt(27)
s_barrier
s_waitcnt lgkmcnt(0)
mvva 124,136,32
tv 132,224,4096
tv 134,224,6144
tv 144,224,8192
mvva 124,140,40
tv 128,224,0
tv 130,224,2048
mvva 124,152,48
mvva 124,156,56
tv 146,224,10240
mvva 120,156,24
dl 249,36,51,2048
tv 148,224,12288
tv 150,224,14336
mvva 120,152,16
mvva 120,140,8
mvva 120,136,0
mvva 116,136,36
mvva 116,140,44
mvva 116,152,52
dl 247,36,51,3072
s_lshl_b32 s51,s35,5
s_add_u32 m0,s52,49152
s_xor_b32 s51,s65,s51
mvva 116,156,60
mvva 112,156,28
mvva 112,152,20
s_waitcnt vmcnt(11)
v_cmp_ne_u32_e32 vcc,4,v253
mvva 112,140,12
dl 245,36,51,0
s_cbranch_vccnz L_spa3e
L_spra3e:
mvva 112,136,4
s_xor_b32 s50,s67,s33
tv 152,225,8192
s_waitcnt lgkmcnt(1)
mvvv 0,128,164
tv 140,225,4096
tv 142,225,6144
mvvv 0,132,172
tv 136,225,0
tv 138,225,2048
mvvv 0,144,180
mvvv 0,148,188
dl 246,36,51,1024
tv 154,225,10240
mvvv 4,148,220
tv 156,225,12288
tv 158,225,14336
mvvv 4,144,212
mvvv 4,132,204
mvvv 4,128,196
)"
R"(mvvv 8,128,160
la 196,252,50,0
s_xor_b32 s53,s60,s34
mvvv 8,132,168
mvvv 8,144,176
mvvv 8,148,184
mvvv 12,148,216
mvvv 12,144,208
la 204,252,50,2048
mvvv 12,132,200
tv 148,226,12288
mvvv 12,128,192
tv 144,226,8192
s_waitcnt lgkmcnt(2)
mvvv 28,136,192
tv 132,226,4096
mvvv 28,140,200
tv 128,226,0
tv 130,226,2048
mvvv 28,152,208
buffer_load_dword v253,v242,s[8:11],s53 offen sc1
tv 134,226,6144
mvvv 28,156,216
tv 146,226,10240
mvvv 24,156,184
tv 150,226,14336
mvvv 24,152,176
mvvv 24,140,168
mvvv 24,136,160
la 212,250,50,0
mvvv 20,136,196
mvvv 20,140,204
mvvv 20,152,212
mvvv 20,156,220
la 220,250,50,2048
mvvv 16,156,188
mvvv 16,152,180
mvvv 16,140,172
tv 156,227,12288
mvvv 16,136,164
la 252,250,50,3072
tv 152,227,8192
s_waitcnt lgkmcnt(2)
mvvv 32,128,164
tv 140,227,4096
mvvv 32,132,172
tv 136,227,0
tv 138,227,2048
mvvv 32,144,180
tv 142,227,6144
mvvv 32,148,188
la 244,250,50,1024
tv 154,227,10240
mvvv 36,148,220
tv 158,227,14336
mvvv 36,144,212
mvvv 36,132,204
mvvv 36,128,196
la 236,252,50,3072
mvvv 40,128,160
mvvv 40,132,168
mvvv 40,144,176
mvvv 40,148,184
mvvv 44,148,216
la 228,252,50,1024
s_add_u32 s50,s50,s55
mvvv 44,144,208
mvvv 44,132,200
tv 148,224,28672
tv 150,224,30720
mvvv 44,128,192
s_waitcnt lgkmcnt(2)
mvvv 60,136,192
la 192,252,50,0
tv 132,224,20480
mvvv 60,140,200
tv 128,224,16384
tv 130,224,18432
mvvv 60,152,208
tv 134,224,22528
mvvv 60,156,216
tv 144,224,24576
tv 146,224,26624
mvvv 56,156,184
la 200,252,50,2048
mvvv 56,152,176
mvvv 56,140,168
mvvv 56,136,160
mvvv 52,136,196
la 208,250,50,0
mvvv 52,140,204
mvvv 52,152,212
mvvv 52,156,220
mvvv 48,156,188
la 216,250,50,2048
mvvv 48,152,180
mvvv 48,140,172
tv 156,225,28672
mvvv 48,136,164
tv 152,225,24576
s_waitcnt lgkmcnt(2)
mvvv 64,128,164
tv 140,225,20480
mvvv 64,132,172
la 248,250,50,3072
tv 136,225,16384
tv 138,225,18432
mvvv 64,144,180
tv 142,225,22528
mvvv 64,148,188
tv 154,225,26624
mvvv 68,148,220
tv 158,225,30720
mvvv 68,144,212
la 240,250,50,1024
mvvv 68,132,204
mvvv 68,128,196
mvvv 72,128,160
mvvv 72,132,168
la 232,252,50,3072
mvvv 72,144,176
mvvv 72,148,184
mvvv 76,148,216
mvvv 76,144,208
mvvv 76,132,200
la 224,252,50,1024
mvvv 76,128,192
tv 144,226,24576
s_waitcnt lgkmcnt(1)
mvvv 92,136,192
tv 132,226,20480
tv 134,226,22528
mvvv 92,140,200
tv 128,226,16384
tv 130,226,18432
mvvv 92,152,208
dl 249,36,51,2048
mvvv 92,156,216
tv 146,226,26624
mvvv 88,156,184
tv 148,226,28672
tv 150,226,30720
mvvv 88,152,176
mvvv 88,140,168
dl 247,36,51,3072
s_lshl_b32 s51,s98,5
s_xor_b32 s51,s64,s51
s_add_u32 m0,s52,131072
s_xor_b32 s51,s51,4194304
mvvv 88,136,160
s_waitcnt vmcnt(16)
v_cmp_ne_u32_e32 vcc,4,v253
mvvv 84,136,196
s_cbranch_vccnz L_spa4e
L_spra4e:
mvvv 84,140,204
s_xor_b32 s50,s67,s34
mvvv 84,152,212
dl 245,36,51,0
mvvv 84,156,220
mvvv 80,156,188
mvvv 80,152,180
mvvv 80,140,172
la 68,252,50,0
mvvv 80,136,164
tv 152,227,24576
s_waitcnt lgkmcnt(1)
mvvv 96,128,164
tv 140,227,20480
tv 142,227,22528
mvvv 96,132,172
tv 136,227,16384
tv 138,227,18432
mvvv 96,144,180
s_xor_b32 s53,s60,s35
mvvv 96,148,188
la 76,252,50,2048
tv 154,227,26624
mvvv 100,148,220
tv 156,227,28672
tv 158,227,30720
mvvv 100,144,212
mvvv 100,132,204
mvvv 100,128,196
la 84,250,50,0
mvvv 104,128,160
mvvv 104,132,168
mvvv 104,144,176
mvvv 104,148,184
buffer_load_dword v253,v242,s[8:11],s53 offen sc1
mvvv 108,148,216
mvvv 108,144,208
mvvv 108,132,200
mvvv 108,128,192
s_barrier
s_waitcnt lgkmcnt(0)
mvvv 124,136,192
la 92,250,50,2048
tv 132,228,36864
tv 144,228,40960
tv 146,228,43008
mvvv 124,140,200
tv 128,228,32768
tv 130,228,34816
mvvv 124,152,208
tv 134,228,38912
mvvv 124,156,216
tv 148,228,45056
tv 150,228,47104
mvvv 120,156,184
la 124,250,50,3072
mvvv 120,152,176
mvvv 120,140,168
mvvv 120,136,160
mvvv 116,136,196
la 116,250,50,1024
mvvv 116,140,204
mvvv 116,152,212
mvvv 116,156,220
mvvv 112,156,188
la 108,252,50,3072
mvvv 112,152,180
mvvv 112,140,172
tv 156,229,45056
tv 158,229,47104
mvvv 112,136,164
s_waitcnt lgkmcnt(2)
mvva 0,128,196
la 100,252,50,1024
s_add_u32 s50,s50,s55
tv 140,229,36864
mvva 0,132,204
tv 136,229,32768
tv 138,229,34816
s_waitcnt vmcnt(21)
mvva 0,144,212
tv 142,229,38912
mvva 0,148,220
tv 152,229,40960
tv 154,229,43008
mvva 4,148,252
mvva 4,144,244
la 64,252,50,0
mvva 4,132,236
s_waitcnt vmcnt(21)
mvva 4,128,228
s_waitcnt vmcnt(20)
mvva 8,128,192
s_waitcnt vmcnt(19)
mvva 8,132,200
la 72,252,50,2048
s_waitcnt vmcnt(19)
mvva 8,144,208
s_waitcnt vmcnt(18)
mvva 8,148,216
s_waitcnt vmcnt(17)
mvva 12,148,248
s_waitcnt vmcnt(16)
mvva 12,144,240
la 80,250,50,0
s_waitcnt vmcnt(16)
mvva 12,132,232
tv 148,230,45056
s_waitcnt vmcnt(15)
mvva 12,128,224
tv 144,230,40960
s_waitcnt lgkmcnt(2)
mvva 28,136,224
tv 132,230,36864
mvva 28,140,232
la 88,250,50,2048
tv 128,230,32768
tv 130,230,34816
mvva 28,152,240
tv 134,230,38912
mvva 28,156,248
tv 146,230,43008
mvva 24,156,216
tv 150,230,47104
mvva 24,152,208
la 120,250,50,3072
mvva 24,140,200
mvva 24,136,192
mvva 20,136,228
mvva 20,140,236
mvva 20,152,244
la 112,250,50,1024
mvva 20,156,252
mvva 16,156,220
mvva 16,152,212
mvva 16,140,204
la 104,252,50,3072
mvva 16,136,196
tv 152,231,40960
s_waitcnt lgkmcnt(1)
mvva 32,128,196
tv 140,231,36864
tv 142,231,38912
mvva 32,132,204
tv 136,231,32768
tv 138,231,34816
mvva 32,144,212
la 96,252,50,1024
mvva 32,148,220
tv 154,231,43008
mvva 36,148,252
tv 156,231,45056
tv 158,231,47104
mvva 36,144,244
mvva 36,132,236
mvva 36,128,228
dl 246,36,51,1024
mvva 40,128,192
mvva 40,132,200
mvva 40,144,208
mvva 40,148,216
dl 249,36,51,2048
mvva 44,148,248
mvva 44,144,240
mvva 44,132,232
tv 148,228,61440
mvva 44,128,224
dl 247,36,51,3072
s_lshl_b32 s51,s98,5
s_xor_b32 s51,s65,s51
s_add_u32 m0,s52,147456
s_xor_b32 s51,s51,4194304
tv 144,228,57344
s_waitcnt lgkmcnt(2)
mvva 60,136,224
tv 132,228,53248
mvva 60,140,232
tv 128,228,49152
tv 130,228,51200
s_waitcnt vmcnt(16)
v_cmp_ne_u32_e32 vcc,4,v253
mvva 60,152,240
s_cbranch_vccnz L_spa5e
L_spra5e:
tv 134,228,55296
s_xor_b32 s50,s67,s35
mvva 60,156,248
dl 245,36,51,0
tv 146,228,59392
mvva 56,156,216
tv 150,228,63488
mvva 56,152,208
mvva 56,140,200
mvva 56,136,192
la 132,252,50,0
mvva 52,136,228
mvva 52,140,236
mvva 52,152,244
mvva 52,156,252
mvva 48,156,220
la 140,252,50,2048
s_xor_b32 s53,s60,s98
s_xor_b32 s53,s53,512
mvva 48,152,212
tv 156,229,61440
mvva 48,140,204
tv 158,229,63488
mvva 48,136,196
tv 152,229,57344
s_waitcnt lgkmcnt(3)
mvva 64,128,196
la 148,250,50,0
tv 140,229,53248
mvva 64,132,204
tv 136,229,49152
tv 138,229,51200
mvva 64,144,212
tv 142,229,55296
mvva 64,148,220
tv 154,229,59392
mvva 68,148,252
buffer_load_dword v253,v242,s[8:11],s53 offen sc1
mvva 68,144,244
mvva 68,132,236
mvva 68,128,228
mvva 72,128,192
mvva 72,132,200
la 156,250,50,2048
mvva 72,144,208
mvva 72,148,216
mvva 76,148,248
mvva 76,144,240
la 188,250,50,3072
mvva 76,132,232
tv 148,230,61440
mvva 76,128,224
tv 144,230,57344
s_waitcnt lgkmcnt(2)
mvva 92,136,224
tv 132,230,53248
mvva 92,140,232
la 180,250,50,1024
tv 128,230,49152
tv 130,230,51200
mvva 92,152,240
tv 134,230,55296
mvva 92,156,248
tv 146,230,59392
mvva 88,156,216
tv 150,230,63488
mvva 88,152,208
la 172,252,50,3072
mvva 88,140,200
mvva 88,136,192
mvva 84,136,228
mvva 84,140,236
la 164,252,50,1024
s_add_u32 s50,s50,s55
mvva 84,152,244
mvva 84,156,252
mvva 80,156,220
mvva 80,152,212
mvva 80,140,204
la 128,252,50,0
mvva 80,136,196
tv 152,231,57344
s_waitcnt lgkmcnt(1)
mvva 96,128,196
tv 140,231,53248
tv 142,231,55296
mvva 96,132,204
tv 136,231,49152
tv 138,231,51200
mvva 96,144,212
L_vs20:
s_cbranch_execz L_vs16
la 136,252,50,2048
mvva 96,148,220
tv 154,231,59392
mvva 100,148,252
tv 156,231,61440
tv 158,231,63488
mvva 100,144,244
mvva 100,132,236
la 144,250,50,0
mvva 100,128,228
mvva 104,128,192
mvva 104,132,200
mvva 104,144,208
la 152,250,50,2048
mvva 104,148,216
mvva 108,148,248
mvva 108,144,240
mvva 108,132,232
la 184,250,50,3072
mvva 108,128,224
s_barrier
s_waitcnt lgkmcnt(0)
mvva 124,136,224
tv 132,228,4096
tv 134,228,6144
tv 144,228,8192
mvva 124,140,232
tv 128,228,0
tv 130,228,2048
mvva 124,152,240
mvva 124,156,248
la 176,250,50,1024
tv 146,228,10240
mvva 120,156,216
tv 148,228,12288
tv 150,228,14336
mvva 120,152,208
mvva 120,140,200
mvva 120,136,192
la 168,252,50,3072
mvva 116,136,228
mvva 116,140,236
mvva 116,152,244
mvva 116,156,252
la 160,252,50,1024
mvva 112,156,220
mvva 112,152,212
mvva 112,140,204
tv 156,229,12288
tv 158,229,14336
mvva 112,136,196
s_waitcnt lgkmcnt(2)
mvva 0,128,68
dl 246,36,51,1024
tv 140,229,4096
mvva 0,132,76
tv 136,229,0
tv 138,229,2048
mvva 0,144,84
tv 142,229,6144
s_waitcnt vmcnt(22)
mvva 0,148,92
tv 152,229,8192
tv 154,229,10240
mvva 4,148,124
dl 249,36,51,2048
mvva 4,144,116
mvva 4,132,108
mvva 4,128,100
mvva 8,128,64
dl 247,36,51,3072
s_lshl_b32 s51,s98,5
s_xor_b32 s51,s64,s51
s_add_u32 m0,s52,0
s_xor_b32 s51,s51,4456448
mvva 8,132,72
mvva 8,144,80
mvva 8,148,88
s_waitcnt vmcnt(16)
v_cmp_ne_u32_e32 vcc,4,v253
mvva 12,148,120
dl 245,36,51,0
s_cbranch_vccnz L_spa6e
L_spra6e:
mvva 12,144,112
s_xor_b32 s50,s67,s98
mvva 12,132,104
s_xor_b32 s50,s50,131072
tv 148,230,12288
tv 150,230,14336
mvva 12,128,96
s_waitcnt lgkmcnt(2)
mvva 28,136,96
dl 246,36,51,1024
tv 132,230,4096
mvva 28,140,104
tv 128,230,0
tv 130,230,2048
mvva 28,152,112
tv 134,230,6144
mvva 28,156,120
tv 144,230,8192
tv 146,230,10240
mvva 24,156,88
mvva 24,152,80
dl 249,36,51,2048
mvva 24,140,72
s_xor_b32 s53,s60,s98
mvva 24,136,64
s_xor_b32 s53,s53,8704
mvva 20,136,100
mvva 20,140,108
dl 247,36,51,3072
mvva 20,152,116
s_lshl_b32 s51,s98,5
s_add_u32 m0,s52,16384
s_xor_b32 s51,s65,s51
mvva 20,156,124
s_xor_b32 s51,s51,4456448
mvva 16,156,92
mvva 16,152,84
buffer_load_dword v253,v242,s[8:11],s53 offen sc1
mvva 16,140,76
tv 156,231,12288
mvva 16,136,68
tv 152,231,8192
s_waitcnt lgkmcnt(2)
mvva 32,128,68
tv 140,231,4096
mvva 32,132,76
la 4,252,50,0
tv 136,231,0
tv 138,231,2048
mvva 32,144,84
tv 142,231,6144
mvva 32,148,92
tv 154,231,10240
mvva 36,148,124
tv 158,231,14336
mvva 36,144,116
la 12,252,50,2048
mvva 36,132,108
mvva 36,128,100
mvva 40,128,64
mvva 40,132,72
mvva 40,144,80
la 20,250,50,0
mvva 40,148,88
mvva 44,148,120
mvva 44,144,112
mvva 44,132,104
la 28,250,50,2048
mvva 44,128,96
tv 144,228,24576
s_waitcnt lgkmcnt(1)
mvva 60,136,96
tv 132,228,20480
tv 134,228,22528
mvva 60,140,104
tv 128,228,16384
tv 130,228,18432
mvva 60,152,112
la 60,250,50,3072
mvva 60,156,120
tv 146,228,26624
mvva 56,156,88
tv 148,228,28672
tv 150,228,30720
mvva 56,152,80
mvva 56,140,72
mvva 56,136,64
la 52,250,50,1024
mvva 52,136,100
mvva 52,140,108
mvva 52,152,116
mvva 52,156,124
la 44,252,50,3072
mvva 48,156,92
mvva 48,152,84
mvva 48,140,76
tv 156,229,28672
mvva 48,136,68
la 36,252,50,1024
s_add_u32 s50,s50,s55
tv 152,229,24576
s_waitcnt lgkmcnt(2)
mvva 64,128,68
tv 140,229,20480
mvva 64,132,76
tv 136,229,16384
tv 138,229,18432
mvva 64,144,84
tv 142,229,22528
mvva 64,148,92
la 0,252,50,0
tv 154,229,26624
mvva 68,148,124
tv 158,229,30720
mvva 68,144,116
mvva 68,132,108
mvva 68,128,100
la 8,252,50,2048
mvva 72,128,64
mvva 72,132,72
mvva 72,144,80
mvva 72,148,88
mvva 76,148,120
la 16,250,50,0
mvva 76,144,112
mvva 76,132,104
tv 148,230,28672
tv 150,230,30720
mvva 76,128,96
s_waitcnt lgkmcnt(2)
mvva 92,136,96
la 24,250,50,2048
tv 132,230,20480
mvva 92,140,104
tv 128,230,16384
tv 130,230,18432
mvva 92,152,112
tv 134,230,22528
mvva 92,156,120
tv 144,230,24576
tv 146,230,26624
mvva 88,156,88
la 56,250,50,3072
mvva 88,152,80
mvva 88,140,72
mvva 88,136,64
mvva 84,136,100
mvva 84,140,108
la 48,250,50,1024
mvva 84,152,116
mvva 84,156,124
mvva 80,156,92
mvva 80,152,84
la 40,252,50,3072
mvva 80,140,76
tv 156,231,28672
mvva 80,136,68
tv 152,231,24576
s_waitcnt lgkmcnt(2)
mvva 96,128,68
tv 140,231,20480
mvva 96,132,76
la 32,252,50,1024
tv 136,231,16384
tv 138,231,18432
mvva 96,144,84
tv 142,231,22528
mvva 96,148,92
tv 154,231,26624
mvva 100,148,124
tv 158,231,30720
mvva 100,144,116
dl 245,36,51,0
s_waitcnt vmcnt(17)
v_cmp_ne_u32_e32 vcc,4,v253
mvva 100,132,108
s_cbranch_vccnz L_spa7e
L_spra7e:
mvva 100,128,100
s_xor_b32 s50,s67,s98
mvva 104,128,64
s_xor_b32 s50,s50,139264
mvva 104,132,72
dl 246,36,51,1024
mvva 104,144,80
mvva 104,148,88
mvva 108,148,120
mvva 108,144,112
mvva 108,132,104
lv 164,252,50,0
mvva 108,128,96
s_barrier
s_waitcnt lgkmcnt(0)
mvva 124,136,96
s_xor_b32 s53,s60,s98
tv 132,224,36864
tv 134,224,38912
tv 144,224,40960
s_xor_b32 s53,s53,16896
mvva 124,140,104
tv 128,224,32768
tv 130,224,34816
mvva 124,152,112
lv 172,252,50,2048
mvva 124,156,120
tv 146,224,43008
mvva 120,156,88
tv 148,224,45056
tv 150,224,47104
mvva 120,152,80
mvva 120,140,72
buffer_load_dword v253,v242,s[8:11],s53 offen sc1
mvva 120,136,64
mvva 116,136,100
mvva 116,140,108
mvva 116,152,116
lv 180,250,50,0
mvva 116,156,124
mvva 112,156,92
mvva 112,152,84
mvva 112,140,76
lv 188,250,50,2048
mvva 112,136,68
tv 152,225,40960
s_waitcnt lgkmcnt(1)
mvva 0,128,132
tv 140,225,36864
tv 142,225,38912
mvva 0,132,140
tv 136,225,32768
tv 138,225,34816
mvva 0,144,148
mvva 0,148,156
lv 220,250,50,3072
tv 154,225,43008
mvva 4,148,188
tv 156,225,45056
tv 158,225,47104
mvva 4,144,180
mvva 4,132,172
mvva 4,128,164
lv 212,250,50,1024
mvva 8,128,128
mvva 8,132,136
mvva 8,144,144
mvva 8,148,152
lv 204,252,50,3072
mvva 12,148,184
mvva 12,144,176
mvva 12,132,168
tv 148,226,45056
tv 150,226,47104
mvva 12,128,160
s_waitcnt lgkmcnt(2)
mvva 28,136,160
lv 196,252,50,1024
s_add_u32 s50,s50,s55
tv 132,226,36864
mvva 28,140,168
tv 128,226,32768
tv 130,226,34816
mvva 28,152,176
tv 134,226,38912
mvva 28,156,184
tv 144,226,40960
tv 146,226,43008
mvva 24,156,152
lv 160,252,50,0
mvva 24,152,144
mvva 24,140,136
mvva 24,136,128
mvva 20,136,164
lv 168,252,50,2048
mvva 20,140,172
mvva 20,152,180
mvva 20,156,188
mvva 16,156,156
lv 176,250,50,0
mvva 16,152,148
mvva 16,140,140
tv 156,227,45056
tv 158,227,47104
mvva 16,136,132
s_waitcnt lgkmcnt(2)
mvva 32,128,132
lv 184,250,50,2048
tv 140,227,36864
mvva 32,132,140
tv 136,227,32768
tv 138,227,34816
mvva 32,144,148
tv 142,227,38912
mvva 32,148,156
tv 152,227,40960
tv 154,227,43008
mvva 36,148,188
mvva 36,144,180
lv 216,250,50,3072
mvva 36,132,172
mvva 36,128,164
mvva 40,128,128
mvva 40,132,136
lv 208,250,50,1024
mvva 40,144,144
mvva 40,148,152
mvva 44,148,184
mvva 44,144,176
lv 200,252,50,3072
mvva 44,132,168
tv 148,224,61440
mvva 44,128,160
tv 144,224,57344
s_waitcnt lgkmcnt(2)
mvva 60,136,160
tv 132,224,53248
mvva 60,140,168
tv 128,224,49152
tv 130,224,51200
mvva 60,152,176
lv 192,252,50,1024
tv 134,224,55296
mvva 60,156,184
tv 146,224,59392
mvva 56,156,152
tv 150,224,63488
mvva 56,152,144
mvva 56,140,136
dl 249,36,51,2048
mvva 56,136,128
mvva 52,136,164
mvva 52,140,172
mvva 52,152,180
dl 247,36,51,3072
s_lshl_b32 s51,s98,5
s_xor_b32 s51,s64,s51
s_add_u32 m0,s52,98304
s_xor_b32 s51,s51,4718592
mvva 52,156,188
mvva 48,156,156
mvva 48,152,148
mvva 48,140,140
dl 245,36,51,0
mvva 48,136,132
tv 152,225,57344
s_waitcnt vmcnt(17)
v_cmp_ne_u32_e32 vcc,4,v253
s_waitcnt lgkmcnt(1)
mvva 64,128,132
s_cbranch_vccnz L_spa8e
L_spra8e:
tv 140,225,53248
tv 142,225,55296
mvva 64,132,140
s_xor_b32 s50,s67,s98
tv 136,225,49152
tv 138,225,51200
mvva 64,144,148
dl 246,36,51,1024
mvva 64,148,156
s_xor_b32 s50,s50,147456
tv 154,225,59392
mvva 68,148,188
tv 156,225,61440
tv 158,225,63488
mvva 68,144,180
mvva 68,132,172
mvva 68,128,164
dl 249,36,51,2048
mvva 72,128,128
mvva 72,132,136
mvva 72,144,144
s_xor_b32 s53,s60,s98
mvva 72,148,152
dl 247,36,51,3072
s_xor_b32 s53,s53,25088
mvva 76,148,184
s_lshl_b32 s51,s98,5
s_add_u32 m0,s52,114688
s_xor_b32 s51,s65,s51
mvva 76,144,176
s_xor_b32 s51,s51,4718592
mvva 76,132,168
tv 148,226,61440
mvva 76,128,160
buffer_load_dword v253,v242,s[8:11],s53 offen sc1
tv 144,226,57344
s_waitcnt lgkmcnt(2)
mvva 92,136,160
tv 132,226,53248
mvva 92,140,168
tv 128,226,49152
tv 130,226,51200
mvva 92,152,176
tv 134,226,55296
mvva 92,156,184
la 196,252,50,0
tv 146,226,59392
mvva 88,156,152
tv 150,226,63488
mvva 88,152,144
mvva 88,140,136
mvva 88,136,128
la 204,252,50,2048
mvva 84,136,164
mvva 84,140,172
mvva 84,152,180
mvva 84,156,188
mvva 80,156,156
la 212,250,50,0
mvva 80,152,148
mvva 80,140,140
tv 156,227,61440
tv 158,227,63488
mvva 80,136,132
s_waitcnt lgkmcnt(2)
mvva 96,128,132
la 220,250,50,2048
tv 140,227,53248
mvva 96,132,140
tv 136,227,49152
tv 138,227,51200
mvva 96,144,148
tv 142,227,55296
mvva 96,148,156
tv 152,227,57344
tv 154,227,59392
mvva 100,148,188
la 252,250,50,3072
mvva 100,144,180
mvva 100,132,172
mvva 100,128,164
mvva 104,128,128
mvva 104,132,136
la 244,250,50,1024
mvva 104,144,144
mvva 104,148,152
mvva 108,148,184
mvva 108,144,176
la 236,252,50,3072
mvva 108,132,168
mvva 108,128,160
s_barrier
s_waitcnt lgkmcnt(0)
mvva 124,136,160
tv 132,232,4096
tv 144,232,8192
mvva 124,140,168
la 228,252,50,1024
s_add_u32 s50,s50,s55
tv 128,232,0
tv 130,232,2048
mvva 124,152,176
tv 134,232,6144
mvva 124,156,184
tv 146,232,10240
mvva 120,156,152
L_vs21:
s_cbranch_execz L_vr1
tv 148,232,12288
tv 150,232,14336
mvva 120,152,144
la 192,252,50,0
mvva 120,140,136
mvva 120,136,128
mvva 116,136,164
mvva 116,140,172
la 200,252,50,2048
mvva 116,152,180
mvva 116,156,188
mvva 112,156,156
mvva 112,152,148
mvva 112,140,140
la 208,250,50,0
mvva 112,136,132
tv 152,233,8192
s_waitcnt lgkmcnt(1)
mvva 0,128,4
tv 140,233,4096
tv 142,233,6144
mvva 0,132,12
tv 136,233,0
tv 138,233,2048
mvva 0,144,20
la 216,250,50,2048
mvva 0,148,28
)"
R"(tv 154,233,10240
mvva 4,148,60
tv 156,233,12288
tv 158,233,14336
mvva 4,144,52
mvva 4,132,44
la 248,250,50,3072
mvva 4,128,36
mvva 8,128,0
mvva 8,132,8
mvva 8,144,16
la 240,250,50,1024
mvva 8,148,24
mvva 12,148,56
mvva 12,144,48
mvva 12,132,40
la 232,252,50,3072
mvva 12,128,32
tv 144,234,8192
s_waitcnt lgkmcnt(1)
mvva 28,136,32
tv 132,234,4096
tv 134,234,6144
mvva 28,140,40
tv 128,234,0
tv 130,234,2048
mvva 28,152,48
mvva 28,156,56
la 224,252,50,1024
tv 146,234,10240
mvva 24,156,24
tv 148,234,12288
tv 150,234,14336
mvva 24,152,16
mvva 24,140,8
mvva 24,136,0
dl 245,36,51,0
mvva 20,136,36
mvva 20,140,44
s_waitcnt vmcnt(17)
v_cmp_ne_u32_e32 vcc,4,v253
mvva 20,152,52
s_cbranch_vccnz L_spa9e
L_spra9e:
mvva 20,156,60
dl 246,36,51,1024
mvva 16,156,28
s_xor_b32 s50,s67,s98
mvva 16,152,20
s_xor_b32 s50,s50,155648
mvva 16,140,12
tv 156,235,12288
tv 158,235,14336
mvva 16,136,4
s_waitcnt lgkmcnt(2)
mvva 32,128,4
dl 249,36,51,2048
tv 140,235,4096
mvva 32,132,12
tv 136,235,0
tv 138,235,2048
mvva 32,144,20
tv 142,235,6144
mvva 32,148,28
tv 152,235,8192
tv 154,235,10240
mvva 36,148,60
dl 247,36,51,3072
s_lshl_b32 s51,s98,5
s_xor_b32 s51,s64,s51
s_add_u32 m0,s52,65536
s_xor_b32 s51,s51,4980736
s_xor_b32 s53,s60,s98
mvva 36,144,52
s_xor_b32 s53,s53,25216
mvva 36,132,44
mvva 36,128,36
mvva 40,128,0
dl 245,36,51,0
mvva 40,132,8
mvva 40,144,16
mvva 40,148,24
mvva 44,148,56
buffer_load_dword v253,v242,s[8:11],s53 offen sc1
mvva 44,144,48
mvva 44,132,40
tv 148,232,28672
tv 150,232,30720
mvva 44,128,32
s_waitcnt lgkmcnt(2)
mvva 60,136,32
dl 246,36,51,1024
tv 132,232,20480
mvva 60,140,40
tv 128,232,16384
tv 130,232,18432
mvva 60,152,48
tv 134,232,22528
mvva 60,156,56
tv 144,232,24576
tv 146,232,26624
mvva 56,156,24
mvva 56,152,16
dl 249,36,51,2048
mvva 56,140,8
mvva 56,136,0
mvva 52,136,36
mvva 52,140,44
dl 247,36,51,3072
s_lshl_b32 s51,s98,5
s_xor_b32 s51,s64,s51
s_add_u32 m0,s52,81920
s_xor_b32 s51,s51,6029312
mvva 52,152,52
mvva 52,156,60
mvva 48,156,28
mvva 48,152,20
dl 245,36,51,0
mvva 48,140,12
tv 156,233,28672
mvva 48,136,4
tv 152,233,24576
s_waitcnt lgkmcnt(2)
mvva 64,128,4
tv 140,233,20480
mvva 64,132,12
tv 136,233,16384
tv 138,233,18432
mvva 64,144,20
dl 246,36,51,1024
tv 142,233,22528
mvva 64,148,28
tv 154,233,26624
mvva 68,148,60
tv 158,233,30720
mvva 68,144,52
mvva 68,132,44
dl 249,36,51,2048
mvva 68,128,36
mvva 72,128,0
mvva 72,132,8
mvva 72,144,16
dl 247,36,51,3072
mvva 72,148,24
s_lshl_b32 s51,s98,5
s_add_u32 m0,s52,32768
s_xor_b32 s51,s65,s51
mvva 76,148,56
s_xor_b32 s51,s51,4980736
mvva 76,144,48
mvva 76,132,40
la 68,252,50,0
mvva 76,128,32
tv 144,224,8192
s_waitcnt lgkmcnt(1)
mvva 92,136,32
tv 132,224,4096
tv 134,224,6144
mvva 92,140,40
tv 128,224,0
tv 130,224,2048
mvva 92,152,48
la 76,252,50,2048
mvva 92,156,56
tv 146,224,10240
mvva 88,156,24
tv 148,224,12288
tv 150,224,14336
mvva 88,152,16
mvva 88,140,8
mvva 88,136,0
la 84,250,50,0
mvva 84,136,36
mvva 84,140,44
mvva 84,152,52
mvva 84,156,60
la 92,250,50,2048
mvva 80,156,28
mvva 80,152,20
mvva 80,140,12
tv 156,225,12288
mvva 80,136,4
la 124,250,50,3072
tv 152,225,8192
s_waitcnt lgkmcnt(2)
mvvv 0,128,164
tv 140,225,4096
mvvv 0,132,172
tv 136,225,0
tv 138,225,2048
mvvv 0,144,180
tv 142,225,6144
mvvv 0,148,188
la 116,250,50,1024
tv 154,225,10240
mvvv 4,148,220
tv 158,225,14336
mvvv 4,144,212
mvvv 4,132,204
mvvv 4,128,196
la 108,252,50,3072
mvvv 8,128,160
mvvv 8,132,168
mvvv 8,144,176
mvvv 8,148,184
mvvv 12,148,216
la 100,252,50,1024
s_add_u32 s50,s50,s55
mvvv 12,144,208
mvvv 12,132,200
tv 148,226,12288
tv 150,226,14336
mvvv 12,128,192
s_barrier
tv 144,226,8192
s_waitcnt lgkmcnt(3)
mvvv 28,136,192
la 64,252,50,0
mvvv 28,140,200
tv 128,226,0
tv 130,226,2048
mvvv 28,152,208
tv 132,226,4096
tv 134,226,6144
mvvv 28,156,216
tv 146,226,10240
s_waitcnt vmcnt(16)
v_cmp_ne_u32_e32 vcc,4,v253
mvvv 24,156,184
la 72,252,50,2048
s_cbranch_vccnz L_spa10e
L_spra10e:
mvvv 24,152,176
v_and_b32_e32 v242,48,v244
v_lshlrev_b32_e32 v242,3,v242
mvvv 24,140,168
mvvv 24,136,160
mvvv 20,136,196
mvvv 20,140,204
la 80,250,50,0
mvvv 20,152,212
mvvv 20,156,220
mvvv 16,156,188
s_andn2_b32 s53,s61,384
mvvv 16,152,180
la 88,250,50,2048
mvvv 16,140,172
tv 156,227,12288
mvvv 16,136,164
tv 152,227,8192
s_waitcnt lgkmcnt(2)
mvvv 32,128,164
tv 140,227,4096
mvvv 32,132,172
buffer_load_dword v253,v242,s[8:11],s53 offen sc1
tv 136,227,0
tv 138,227,2048
mvvv 32,144,180
tv 142,227,6144
mvvv 32,148,188
tv 154,227,10240
mvvv 36,148,220
tv 158,227,14336
mvvv 36,144,212
la 120,250,50,3072
mvvv 36,132,204
mvvv 36,128,196
mvvv 40,128,160
mvvv 40,132,168
la 112,250,50,1024
mvvv 40,144,176
mvvv 40,148,184
mvvv 44,148,216
mvvv 44,144,208
mvvv 44,132,200
la 104,252,50,3072
mvvv 44,128,192
tv 144,224,24576
s_waitcnt lgkmcnt(1)
mvvv 60,136,192
tv 132,224,20480
tv 134,224,22528
mvvv 60,140,200
tv 128,224,16384
tv 130,224,18432
mvvv 60,152,208
la 96,252,50,1024
s_xor_b32 s50,s67,s98
s_xor_b32 s50,s50,188416
mvvv 60,156,216
tv 146,224,26624
mvvv 56,156,184
tv 148,224,28672
tv 150,224,30720
mvvv 56,152,176
mvvv 56,140,168
la 132,252,50,0
mvvv 56,136,160
mvvv 52,136,196
mvvv 52,140,204
mvvv 52,152,212
la 140,252,50,2048
mvvv 52,156,220
mvvv 48,156,188
mvvv 48,152,180
mvvv 48,140,172
la 148,250,50,0
mvvv 48,136,164
tv 152,225,24576
s_waitcnt lgkmcnt(1)
mvvv 64,128,164
tv 140,225,20480
tv 142,225,22528
mvvv 64,132,172
tv 136,225,16384
tv 138,225,18432
mvvv 64,144,180
mvvv 64,148,188
la 156,250,50,2048
tv 154,225,26624
mvvv 68,148,220
tv 156,225,28672
tv 158,225,30720
mvvv 68,144,212
mvvv 68,132,204
mvvv 68,128,196
la 188,250,50,3072
mvvv 72,128,160
mvvv 72,132,168
mvvv 72,144,176
mvvv 72,148,184
la 180,250,50,1024
mvvv 76,148,216
mvvv 76,144,208
mvvv 76,132,200
tv 148,228,45056
tv 150,228,47104
mvvv 76,128,192
s_waitcnt lgkmcnt(2)
mvvv 92,136,192
la 172,252,50,3072
tv 132,228,36864
mvvv 92,140,200
tv 128,228,32768
tv 130,228,34816
mvvv 92,152,208
tv 134,228,38912
mvvv 92,156,216
tv 144,228,40960
tv 146,228,43008
mvvv 88,156,184
la 164,252,50,1024
s_add_u32 s50,s50,s55
mvvv 88,152,176
mvvv 88,140,168
mvvv 88,136,160
mvvv 84,136,196
la 128,252,50,0
mvvv 84,140,204
mvvv 84,152,212
mvvv 84,156,220
mvvv 80,156,188
la 136,252,50,2048
mvvv 80,152,180
mvvv 80,140,172
tv 156,229,45056
tv 158,229,47104
mvvv 80,136,164
s_waitcnt lgkmcnt(2)
mvva 0,128,196
la 144,250,50,0
tv 140,229,36864
mvva 0,132,204
tv 136,229,32768
tv 138,229,34816
mvva 0,144,212
tv 142,229,38912
mvva 0,148,220
tv 152,229,40960
tv 154,229,43008
mvva 4,148,252
mvva 4,144,244
la 152,250,50,2048
mvva 4,132,236
mvva 4,128,228
mvva 8,128,192
mvva 8,132,200
la 184,250,50,3072
mvva 8,144,208
mvva 8,148,216
s_waitcnt vmcnt(17)
v_cmp_ne_u32_e32 vcc,4,v253
mvva 12,148,248
s_cbranch_vccnz L_sppe
L_sprpe:
mvva 12,144,240
la 176,250,50,1024
mvva 12,132,232
tv 148,230,45056
mvva 12,128,224
tv 144,230,40960
s_waitcnt lgkmcnt(2)
mvva 28,136,224
tv 132,230,36864
mvva 28,140,232
tv 128,230,32768
tv 130,230,34816
mvva 28,152,240
la 168,252,50,3072
tv 134,230,38912
mvva 28,156,248
tv 146,230,43008
mvva 24,156,216
tv 150,230,47104
mvva 24,152,208
mvva 24,140,200
la 160,252,50,1024
mvva 24,136,192
mvva 20,136,228
mvva 20,140,236
mvva 20,152,244
dl 245,36,51,0
mvva 20,156,252
mvva 16,156,220
mvva 16,152,212
mvva 16,140,204
dl 246,36,51,1024
mvva 16,136,196
tv 152,231,40960
s_waitcnt lgkmcnt(1)
mvva 32,128,196
tv 140,231,36864
tv 142,231,38912
mvva 32,132,204
tv 136,231,32768
tv 138,231,34816
mvva 32,144,212
dl 249,36,51,2048
mvva 32,148,220
tv 154,231,43008
mvva 36,148,252
tv 156,231,45056
tv 158,231,47104
mvva 36,144,244
mvva 36,132,236
mvva 36,128,228
dl 247,36,51,3072
s_lshl_b32 s51,s98,5
s_xor_b32 s51,s65,s51
s_add_u32 m0,s52,49152
s_xor_b32 s51,s51,6029312
mvva 40,128,192
mvva 40,132,200
mvva 40,144,208
mvva 40,148,216
dl 245,36,51,0
mvva 44,148,248
mvva 44,144,240
mvva 44,132,232
mvva 44,128,224
dl 246,36,51,1024
s_waitcnt lgkmcnt(0)
mvva 60,136,224
s_barrier
mvva 60,140,232
tv 144,228,57344
tv 146,228,59392
tv 128,228,49152
tv 130,228,51200
mvva 60,152,240
tv 132,228,53248
tv 134,228,55296
mvva 60,156,248
dl 249,36,51,2048
mvva 56,156,216
tv 148,228,61440
tv 150,228,63488
mvva 56,152,208
mvva 56,140,200
mvva 56,136,192
dl 247,36,51,3072
mvva 52,136,228
mvva 52,140,236
mvva 52,152,244
mvva 52,156,252
mvva 48,156,220
mvva 48,152,212
mvva 48,140,204
tv 156,229,61440
mvva 48,136,196
tv 152,229,57344
s_waitcnt lgkmcnt(2)
mvva 64,128,196
tv 140,229,53248
mvva 64,132,204
tv 136,229,49152
tv 138,229,51200
mvva 64,144,212
tv 142,229,55296
mvva 64,148,220
tv 154,229,59392
mvva 68,148,252
tv 158,229,63488
mvva 68,144,244
mvva 68,132,236
mvva 68,128,228
mvva 72,128,192
mvva 72,132,200
L_vs22:
s_cbranch_execz L_vr1
mvva 72,144,208
mvva 72,148,216
mvva 76,148,248
mvva 76,144,240
mvva 76,132,232
tv 148,228,12288
mvva 76,128,224
tv 144,228,8192
s_waitcnt lgkmcnt(2)
mvva 92,136,224
tv 132,228,4096
mvva 92,140,232
tv 128,228,0
tv 130,228,2048
mvva 92,152,240
tv 134,228,6144
mvva 92,156,248
tv 146,228,10240
mvva 88,156,216
tv 150,228,14336
mvva 88,152,208
mvva 88,140,200
mvva 88,136,192
mvva 84,136,228
mvva 84,140,236
mvva 84,152,244
mvva 84,156,252
mvva 80,156,220
mvva 80,152,212
mvva 80,140,204
tv 156,229,12288
mvva 80,136,196
tv 152,229,8192
s_waitcnt lgkmcnt(2)
mvva 0,128,68
tv 140,229,4096
mvva 0,132,76
tv 136,229,0
tv 138,229,2048
mvva 0,144,84
tv 142,229,6144
mvva 0,148,92
tv 154,229,10240
mvva 4,148,124
tv 158,229,14336
mvva 4,144,116
mvva 4,132,108
mvva 4,128,100
mvva 8,128,64
mvva 8,132,72
mvva 8,144,80
mvva 8,148,88
s_waitcnt vmcnt(10)
mvva 12,148,120
mvva 12,144,112
mvva 12,132,104
tv 148,228,28672
mvva 12,128,96
tv 144,228,24576
s_waitcnt lgkmcnt(2)
mvva 28,136,96
tv 132,228,20480
mvva 28,140,104
tv 128,228,16384
tv 130,228,18432
mvva 28,152,112
tv 134,228,22528
mvva 28,156,120
tv 146,228,26624
mvva 24,156,88
tv 150,228,30720
mvva 24,152,80
mvva 24,140,72
mvva 24,136,64
mvva 20,136,100
mvva 20,140,108
mvva 20,152,116
mvva 20,156,124
mvva 16,156,92
mvva 16,152,84
mvva 16,140,76
tv 156,229,28672
mvva 16,136,68
tv 152,229,24576
s_waitcnt lgkmcnt(2)
mvva 0,128,132
tv 140,229,20480
mvva 0,132,140
tv 136,229,16384
tv 138,229,18432
mvva 0,144,148
tv 142,229,22528
mvva 0,148,156
tv 154,229,26624
mvva 4,148,188
tv 158,229,30720
mvva 4,144,180
mvva 4,132,172
mvva 4,128,164
mvva 8,128,128
mvva 8,132,136
cv 0,0,1
cv 1,2,3
mvva 8,144,144
mvva 8,148,152
mvva 12,148,184
mvva 12,144,176
cv 4,4,5
cv 5,6,7
s_waitcnt vmcnt(8)
mvva 12,132,168
tv 148,230,12288
mvva 12,128,160
tv 144,230,8192
cv 8,8,9
s_waitcnt lgkmcnt(2)
mvva 28,136,160
tv 132,230,4096
cv 9,10,11
mvva 28,140,168
tv 128,230,0
tv 130,230,2048
mvva 28,152,176
tv 134,230,6144
cv 12,12,13
mvva 28,156,184
tv 146,230,10240
cv 13,14,15
mvva 24,156,152
tv 150,230,14336
mvva 24,152,144
mvva 24,140,136
mvva 24,136,128
mvva 20,136,164
cv 14,28,29
cv 15,30,31
mvva 20,140,172
buffer_store_dwordx4 v[12:15],v244,s[8:11],s56 offen offset:3072 sc0 sc1
mvva 20,152,180
mvva 20,156,188
mvva 16,156,156
cv 10,24,25
cv 11,26,27
mvva 16,152,148
buffer_store_dwordx4 v[8:11],v244,s[8:11],s56 offen offset:2048 sc0 sc1
mvva 16,140,140
tv 156,231,12288
mvva 16,136,132
cv 6,20,21
cv 7,22,23
s_waitcnt lgkmcnt(1)
mvva 32,128,68
tv 140,231,4096
mvva 32,132,76
tv 136,231,0
tv 138,231,2048
mvva 32,144,84
buffer_store_dwordx4 v[4:7],v244,s[8:11],s56 offen offset:1024 sc0 sc1
tv 142,231,6144
mvva 32,148,92
tv 152,231,8192
tv 154,231,10240
mvva 36,148,124
tv 158,231,14336
cv 2,16,17
cv 3,18,19
mvva 36,144,116
mvva 36,132,108
buffer_store_dwordx4 v[0:3],v244,s[8:11],s56 offen offset:0 sc0 sc1
mvva 36,128,100
mvva 40,128,64
mvva 40,132,72
mvva 40,144,80
mvva 40,148,88
mvva 44,148,120
mvva 44,144,112
mvva 44,132,104
tv 148,230,28672
mvva 44,128,96
s_waitcnt vmcnt(4)
s_barrier
tv 144,230,24576
s_waitcnt lgkmcnt(2)
mvva 60,136,96
tv 132,230,20480
mvva 60,140,104
tv 128,230,16384
tv 130,230,18432
mvva 60,152,112
tv 134,230,22528
mvva 60,156,120
tv 146,230,26624
mvva 56,156,88
tv 150,230,30720
mvva 56,152,80
mvva 56,140,72
mvva 56,136,64
mvva 52,136,100
mvva 52,140,108
mvva 52,152,116
mvva 52,156,124
mvva 48,156,92
mvva 48,152,84
mvva 48,140,76
tv 156,231,28672
mvva 48,136,68
tv 152,231,24576
s_waitcnt lgkmcnt(2)
mvva 32,128,132
tv 140,231,20480
mvva 32,132,140
tv 136,231,16384
tv 138,231,18432
mvva 32,144,148
tv 142,231,22528
mvva 32,148,156
tv 154,231,26624
mvva 36,148,188
tv 158,231,30720
mvva 36,144,180
mvva 36,132,172
mvva 36,128,164
mvva 40,128,128
mvva 40,132,136
cv 32,32,33
cv 33,34,35
mvva 40,144,144
mvva 40,148,152
mvva 44,148,184
mvva 44,144,176
cv 36,36,37
cv 37,38,39
mvva 44,132,168
tv 148,224,45056
mvva 44,128,160
tv 144,224,40960
cv 40,40,41
s_waitcnt lgkmcnt(2)
mvva 60,136,160
tv 132,224,36864
cv 41,42,43
mvva 60,140,168
tv 128,224,32768
tv 130,224,34816
mvva 60,152,176
tv 134,224,38912
cv 44,44,45
mvva 60,156,184
tv 146,224,43008
cv 45,46,47
mvva 56,156,152
tv 150,224,47104
mvva 56,152,144
mvva 56,140,136
mvva 56,136,128
mvva 52,136,164
cv 46,60,61
cv 47,62,63
mvva 52,140,172
buffer_store_dwordx4 v[44:47],v244,s[8:11],s57 offen offset:3072 sc0 sc1
mvva 52,152,180
mvva 52,156,188
mvva 48,156,156
cv 42,56,57
cv 43,58,59
mvva 48,152,148
buffer_store_dwordx4 v[40:43],v244,s[8:11],s57 offen offset:2048 sc0 sc1
mvva 48,140,140
tv 156,225,45056
mvva 48,136,132
cv 38,52,53
cv 39,54,55
s_waitcnt lgkmcnt(1)
mvva 64,128,68
tv 140,225,36864
mvva 64,132,76
buffer_store_dwordx4 v[36:39],v244,s[8:11],s57 offen offset:1024 sc0 sc1
tv 136,225,32768
tv 138,225,34816
mvva 64,144,84
tv 142,225,38912
cv 34,48,49
mvva 64,148,92
tv 152,225,40960
tv 154,225,43008
mvva 68,148,124
tv 158,225,47104
cv 35,50,51
mvva 68,144,116
buffer_store_dwordx4 v[32:35],v244,s[8:11],s57 offen offset:0 sc0 sc1
mvva 68,132,108
mvva 68,128,100
mvva 72,128,64
mvva 72,132,72
mvva 72,144,80
mvva 72,148,88
mvva 76,148,120
mvva 76,144,112
mvva 76,132,104
tv 148,224,61440
mvva 76,128,96
tv 144,224,57344
s_waitcnt lgkmcnt(2)
mvva 92,136,96
tv 132,224,53248
mvva 92,140,104
tv 128,224,49152
tv 130,224,51200
mvva 92,152,112
tv 134,224,55296
mvva 92,156,120
tv 146,224,59392
mvva 88,156,88
tv 150,224,63488
mvva 88,152,80
mvva 88,140,72
mvva 88,136,64
mvva 84,136,100
mvva 84,140,108
mvva 84,152,116
mvva 84,156,124
mvva 80,156,92
mvva 80,152,84
mvva 80,140,76
tv 156,225,61440
mvva 80,136,68
tv 152,225,57344
s_waitcnt lgkmcnt(2)
mvva 64,128,132
tv 140,225,53248
mvva 64,132,140
tv 136,225,49152
tv 138,225,51200
mvva 64,144,148
tv 142,225,55296
mvva 64,148,156
tv 154,225,59392
mvva 68,148,188
tv 158,225,63488
mvva 68,144,180
mvva 68,132,172
mvva 68,128,164
s_xor_b32 s82,s59,12
mvva 72,128,128
s_mul_i32 s82,s82,12288
cv 64,64,65
s_add_u32 s82,s82,0
mvva 72,132,136
lv 16,244,82,0
mvva 72,144,144
cv 65,66,67
cv 68,68,69
mvva 72,148,152
cv 69,70,71
mvva 76,148,184
mvva 76,144,176
mvva 76,132,168
lv 20,244,82,1024
mvva 76,128,160
tv 144,234,24576
s_waitcnt lgkmcnt(1)
mvva 92,136,160
tv 132,234,20480
tv 134,234,22528
mvva 92,140,168
tv 128,234,16384
tv 130,234,18432
mvva 92,152,176
cv 72,72,73
cv 73,74,75
mvva 92,156,184
lv 24,244,82,2048
tv 146,234,26624
mvva 88,156,152
tv 148,234,28672
tv 150,234,30720
mvva 88,152,144
cv 76,76,77
cv 77,78,79
mvva 88,140,136
cv 78,92,93
cv 79,94,95
mvva 88,136,128
mvva 84,136,164
lv 28,244,82,3072
mvva 84,140,172
mvva 84,152,180
mvva 84,156,188
mvva 80,156,156
buffer_store_dwordx4 v[76:79],v244,s[8:11],s58 offen offset:3072 sc0 sc1
mvva 80,152,148
cv 74,88,89
cv 75,90,91
mvva 80,140,140
tv 156,235,28672
mvva 80,136,132
cv 70,84,85
cv 71,86,87
s_waitcnt lgkmcnt(1)
mvva 96,128,4
buffer_store_dwordx4 v[72:75],v244,s[8:11],s58 offen offset:2048 sc0 sc1
tv 140,235,20480
mvva 96,132,12
tv 136,235,16384
tv 138,235,18432
mvva 96,144,20
tv 142,235,22528
cv 66,80,81
mvva 96,148,28
tv 152,235,24576
tv 154,235,26624
mvva 100,148,60
tv 158,235,30720
cv 67,82,83
mvva 100,144,52
buffer_store_dwordx4 v[68:71],v244,s[8:11],s58 offen offset:1024 sc0 sc1
mvva 100,132,44
mvva 100,128,36
mvva 104,128,0
mvva 104,132,8
buffer_store_dwordx4 v[64:67],v244,s[8:11],s58 offen offset:0 sc0 sc1
mvva 104,144,16
mvva 104,148,24
mvva 108,148,56
mvva 108,144,48
mvva 108,132,40
tv 148,226,28672
mvva 108,128,32
tv 144,226,24576
s_waitcnt lgkmcnt(2)
mvva 124,136,32
tv 132,226,20480
mvva 124,140,40
tv 128,226,16384
tv 130,226,18432
mvva 124,152,48
tv 134,226,22528
mvva 124,156,56
tv 146,226,26624
mvva 120,156,24
tv 150,226,30720
mvva 120,152,16
mvva 120,140,8
mvva 120,136,0
mvva 116,136,36
mvva 116,140,44
s_waitcnt vmcnt(7)
v_max_u32_e32 v248,v16,v17
mvva 116,152,52
v_max3_u32 v248,v248,v18,v19
s_waitcnt vmcnt(6)
v_max3_u32 v248,v248,v20,v21
v_max3_u32 v248,v248,v22,v23
mvva 116,156,60
s_waitcnt vmcnt(5)
v_max3_u32 v248,v248,v24,v25
v_max3_u32 v248,v248,v26,v27
s_waitcnt vmcnt(4)
v_max3_u32 v248,v248,v28,v29
mvva 112,156,28
v_max3_u32 v248,v248,v30,v31
v_cmp_eq_u32_e32 vcc,-1,v248
mvva 112,152,20
s_cbranch_vccnz L_spx0e
L_sprx0e:
mvva 112,140,12
tv 156,227,28672
tv 158,227,30720
v_lshlrev_b32_e32 v251,16,v18
v_and_b32_e32 v255,4294901760,v18
mvva 112,136,4
tv 152,227,24576
tv 154,227,26624
s_waitcnt lgkmcnt(4)
mvvv 96,128,164
v_lshlrev_b32_e32 v253,16,v19
v_and_b32_e32 v242,4294901760,v19
mvvv 96,132,172
tv 136,227,16384
tv 138,227,18432
mvvv 96,144,180
tv 140,227,20480
tv 142,227,22528
v_add_f32_e32 v112,v112,v251
mvvv 96,148,188
v_add_f32_e32 v113,v113,v255
v_add_f32_e32 v114,v114,v253
mvvv 100,148,220
v_add_f32_e32 v115,v115,v242
v_lshlrev_b32_e32 v251,16,v22
)"
R"(mvvv 100,144,212
v_and_b32_e32 v255,4294901760,v22
v_lshlrev_b32_e32 v253,16,v23
mvvv 100,132,204
v_and_b32_e32 v242,4294901760,v23
v_add_f32_e32 v116,v116,v251
v_add_f32_e32 v117,v117,v255
mvvv 100,128,196
v_add_f32_e32 v118,v118,v253
v_add_f32_e32 v119,v119,v242
mvvv 104,128,160
v_lshlrev_b32_e32 v251,16,v26
v_and_b32_e32 v255,4294901760,v26
mvvv 104,132,168
v_lshlrev_b32_e32 v253,16,v27
v_and_b32_e32 v242,4294901760,v27
mvvv 104,144,176
v_add_f32_e32 v120,v120,v251
v_add_f32_e32 v121,v121,v255
v_add_f32_e32 v122,v122,v253
mvvv 104,148,184
v_add_f32_e32 v123,v123,v242
v_lshlrev_b32_e32 v251,16,v30
mvvv 108,148,216
v_and_b32_e32 v255,4294901760,v30
v_lshlrev_b32_e32 v253,16,v31
mvvv 108,144,208
v_and_b32_e32 v242,4294901760,v31
v_add_f32_e32 v124,v124,v251
mvvv 108,132,200
v_add_f32_e32 v125,v125,v255
v_add_f32_e32 v126,v126,v253
v_add_f32_e32 v127,v127,v242
mvvv 108,128,192
tv 144,230,57344
v_lshlrev_b32_e32 v251,16,v16
s_waitcnt lgkmcnt(1)
mvvv 124,136,192
tv 132,230,53248
v_and_b32_e32 v255,4294901760,v16
mvvv 124,140,200
tv 128,230,49152
tv 130,230,51200
mvvv 124,152,208
tv 134,230,55296
v_lshlrev_b32_e32 v253,16,v17
mvvv 124,156,216
tv 146,230,59392
v_and_b32_e32 v242,4294901760,v17
mvvv 120,156,184
L_vs23:
s_cbranch_execz L_vr1
tv 148,230,61440
tv 150,230,63488
mvvv 120,152,176
v_add_f32_e32 v96,v96,v251
v_add_f32_e32 v97,v97,v255
mvvv 120,140,168
v_add_f32_e32 v98,v98,v253
v_add_f32_e32 v99,v99,v242
mvvv 120,136,160
v_lshlrev_b32_e32 v251,16,v20
v_and_b32_e32 v255,4294901760,v20
mvvv 116,136,196
v_lshlrev_b32_e32 v253,16,v21
v_and_b32_e32 v242,4294901760,v21
mvvv 116,140,204
v_add_f32_e32 v100,v100,v251
v_add_f32_e32 v101,v101,v255
mvvv 116,152,212
v_add_f32_e32 v102,v102,v253
v_add_f32_e32 v103,v103,v242
mvvv 116,156,220
v_lshlrev_b32_e32 v251,16,v24
v_and_b32_e32 v255,4294901760,v24
mvvv 112,156,188
v_lshlrev_b32_e32 v253,16,v25
v_and_b32_e32 v242,4294901760,v25
mvvv 112,152,180
v_add_f32_e32 v104,v104,v251
v_add_f32_e32 v105,v105,v255
mvvv 112,140,172
tv 156,231,61440
v_add_f32_e32 v106,v106,v253
mvvv 112,136,164
tv 152,231,57344
v_add_f32_e32 v107,v107,v242
s_waitcnt lgkmcnt(2)
mvva 96,128,196
tv 140,231,53248
v_lshlrev_b32_e32 v251,16,v28
mvva 96,132,204
tv 136,231,49152
tv 138,231,51200
mvva 96,144,212
tv 142,231,55296
v_and_b32_e32 v255,4294901760,v28
mvva 96,148,220
tv 154,231,59392
v_lshlrev_b32_e32 v253,16,v29
mvva 100,148,252
tv 158,231,63488
v_and_b32_e32 v242,4294901760,v29
mvva 100,144,244
v_add_f32_e32 v108,v108,v251
v_add_f32_e32 v109,v109,v255
mvva 100,132,236
v_add_f32_e32 v110,v110,v253
v_add_f32_e32 v111,v111,v242
mvva 100,128,228
mvva 104,128,192
mvva 104,132,200
mvva 104,144,208
mvva 104,148,216
mvva 108,148,248
mvva 108,144,240
mvva 108,132,232
tv 148,226,45056
mvva 108,128,224
tv 144,226,40960
s_waitcnt lgkmcnt(2)
mvva 124,136,224
s_xor_b32 s83,s59,8
tv 132,226,36864
s_mul_i32 s83,s83,12288
mvva 124,140,232
s_add_u32 s83,s83,4096
tv 128,226,32768
tv 130,226,34816
mvva 124,152,240
lv 48,244,83,0
tv 134,226,38912
mvva 124,156,248
tv 146,226,43008
mvva 120,156,216
lv 52,244,83,1024
tv 150,226,47104
mvva 120,152,208
mvva 120,140,200
lv 56,244,83,2048
mvva 120,136,192
mvva 116,136,228
lv 60,244,83,3072
mvva 116,140,236
mvva 116,152,244
s_xor_b32 s84,s59,4
mvva 116,156,252
s_mul_i32 s84,s84,12288
mvva 112,156,220
s_add_u32 s84,s84,8192
mvva 112,152,212
lv 80,244,84,0
mvva 112,140,204
tv 156,227,45056
mvva 112,136,196
lv 84,244,84,1024
tv 152,227,40960
s_waitcnt lgkmcnt(2)
mvva 96,128,68
tv 140,227,36864
mvva 96,132,76
lv 88,244,84,2048
tv 136,227,32768
tv 138,227,34816
mvva 96,144,84
tv 142,227,38912
mvva 96,148,92
lv 92,244,84,3072
tv 154,227,43008
mvva 100,148,124
tv 158,227,47104
mvva 100,144,116
mvva 100,132,108
mvva 100,128,100
mvva 104,128,64
mvva 104,132,72
mvva 104,144,80
mvva 104,148,88
mvva 108,148,120
mvva 108,144,112
mvva 108,132,104
tv 148,226,61440
mvva 108,128,96
tv 144,226,57344
s_waitcnt lgkmcnt(2)
mvva 124,136,96
tv 132,226,53248
mvva 124,140,104
tv 128,226,49152
tv 130,226,51200
mvva 124,152,112
tv 134,226,55296
mvva 124,156,120
tv 146,226,59392
mvva 120,156,88
tv 150,226,63488
mvva 120,152,80
mvva 120,140,72
mvva 120,136,64
mvva 116,136,100
mvva 116,140,108
mvva 116,152,116
mvva 116,156,124
mvva 112,156,92
mvva 112,152,84
mvva 112,140,76
tv 156,227,61440
mvva 112,136,68
tv 152,227,57344
s_waitcnt lgkmcnt(2)
mvva 96,128,132
tv 140,227,53248
mvva 96,132,140
tv 136,227,49152
tv 138,227,51200
mvva 96,144,148
tv 142,227,55296
mvva 96,148,156
tv 154,227,59392
mvva 100,148,188
tv 158,227,63488
mvva 100,144,180
mvva 100,132,172
mvva 100,128,164
mvva 104,128,128
mvva 104,132,136
mvva 104,144,144
mvva 104,148,152
s_waitcnt vmcnt(7)
v_max_u32_e32 v248,v48,v49
mvva 108,148,184
v_max3_u32 v248,v248,v50,v51
s_waitcnt vmcnt(6)
v_max3_u32 v248,v248,v52,v53
mvva 108,144,176
v_max3_u32 v248,v248,v54,v55
s_waitcnt vmcnt(5)
v_max3_u32 v248,v248,v56,v57
mvva 108,132,168
v_max3_u32 v248,v248,v58,v59
s_waitcnt vmcnt(4)
v_max3_u32 v248,v248,v60,v61
mvva 108,128,160
v_max3_u32 v248,v248,v62,v63
v_cmp_eq_u32_e32 vcc,-1,v248
s_waitcnt lgkmcnt(0)
mvva 124,136,160
s_cbranch_vccnz L_spx1e
L_sprx1e:
mvva 124,140,168
mvva 124,152,176
mvva 124,156,184
s_waitcnt vmcnt(3)
v_max_u32_e32 v248,v80,v81
mvva 120,156,152
v_max3_u32 v248,v248,v82,v83
s_waitcnt vmcnt(2)
v_max3_u32 v248,v248,v84,v85
mvva 120,152,144
v_max3_u32 v248,v248,v86,v87
s_waitcnt vmcnt(1)
v_max3_u32 v248,v248,v88,v89
mvva 120,140,136
v_max3_u32 v248,v248,v90,v91
s_waitcnt vmcnt(0)
v_max3_u32 v248,v248,v92,v93
mvva 120,136,128
v_max3_u32 v248,v248,v94,v95
v_cmp_eq_u32_e32 vcc,-1,v248
mvva 116,136,164
s_cbranch_vccnz L_spx2e
L_sprx2e:
mvva 116,140,172
mvva 116,152,180
mvva 116,156,188
mvva 112,156,156
mvva 112,152,148
mvva 112,140,140
mvva 112,136,132
mvvv 108,238,60
mvvv 108,238,92
mvvv 124,236,60
mvvv 124,236,92
mvvv 104,238,56
mvvv 104,238,88
mvvv 120,236,56
mvvv 120,236,88
mvvv 100,238,52
mvvv 100,238,84
cv 108,108,109
cv 109,110,111
cv 110,124,125
cv 111,126,127
cv 104,104,105
v_permlane16_swap_b32_e32 v108,v110
mvvv 116,236,52
v_permlane16_swap_b32_e32 v109,v111
cv 105,106,107
cv 106,120,121
cv 107,122,123
mvvv 116,236,84
buffer_store_dwordx4 v[108:111],v243,s[44:47],s73 offen sc0 sc1
v_permlane16_swap_b32_e32 v104,v106
v_permlane16_swap_b32_e32 v105,v107
mvvv 96,238,48
cv 100,100,101
cv 101,102,103
mvvv 96,238,80
buffer_store_dwordx4 v[104:107],v243,s[44:47],s72 offen sc0 sc1
cv 102,116,117
cv 103,118,119
mvvv 112,236,48
v_permlane16_swap_b32_e32 v100,v102
v_permlane16_swap_b32_e32 v101,v103
mvvv 112,236,80
buffer_store_dwordx4 v[100:103],v243,s[44:47],s71 offen sc0 sc1
cv 96,96,97
cv 97,98,99
s_nop 4
cv 98,112,113
cv 99,114,115
s_nop 0
v_permlane16_swap_b32_e32 v96,v98
v_permlane16_swap_b32_e32 v97,v99
buffer_store_dwordx4 v[96:99],v243,s[44:47],s70 offen sc0 sc1
s_endpgm
L_odd:
L_vs24:
s_cbranch_execz L_vs28
s_lshl_b32 s86,s21,2
s_sub_u32 s87,s13,s67
s_add_u32 s86,s86,s12
mvaa 64,200,132
s_lshl_b32 s86,s86,8
v_add_u32_e32 v252,s87,v244
s_add_u32 s86,s86,L_odd-L_att_pc+1024
v_lshl_add_u32 v226,v244,3,s86
s_lshl_b32 s86,s12,1
s_bfe_u32 s87,s21,65539
mvaa 64,208,136
s_add_u32 s86,s86,s87
v_min_u32_e32 v226,L_att_end-L_att_pc-4,v226
s_lshl_b32 s86,s86,7
s_add_u32 s86,s86,L_pend2-L_att_pc
v_writelane_b32 v226,s86,2
mvaa 64,216,140
s_mov_b64 exec,7
global_load_dword v253,v226,s[96:97]
s_mov_b64 exec,-1
cvaa 68,220,140,232
s_add_u32 m0,s52,32768
v_and_b32_e32 v224,16,v244
v_lshlrev_b32_e32 v224,5,v224
mvaa 68,212,136
s_xor_b32 s51,s62,0
v_lshrrev_b32_e32 v239,4,v244
v_lshrrev_b32_e32 v236,3,v239
mvaa 68,204,132
s_lshl_b32 s87,s12,13
v_and_b32_e32 v237,112,v244
v_and_b32_e32 v238,48,v239
mvaa 68,196,128
s_xor_b32 s87,s87,8192
ra 64,225,0
v_lshlrev_b32_e32 v238,1,v238
cvaa 96,224,128,232
buffer_load_dword v253,v224,s[8:11],s53 offen sc1
mvaa 96,232,132
s_mov_b32 s84,7
ra 68,225,1024
v_xor_b32_e32 v237,v237,v238
mvaa 96,240,136
s_add_u32 s81,s81,1
ra 72,225,2048
v_lshl_add_u32 v245,v236,11,v237
mvaa 96,248,140
s_mov_b32 s84,5
v_add_u32_e32 v246,15360,v245
v_add_u32_e32 v249,30720,v245
cvaa 100,252,140,232
s_andn2_b32 s69,64,s54
ra 76,225,3072
s_waitcnt vmcnt(9)
v_add_u32_e32 v247,46080,v245
mvaa 100,244,136
dl 245,24,51,0
mvaa 100,236,132
s_add_u32 s88,s14,4096
ra 80,225,4096
cv 8,8,9
mvaa 100,228,128
s_lshl_b32 s35,s19,1
ra 84,225,5120
cv 9,10,11
cvaa 72,192,144,232
s_lshr_b32 s33,40569,s35
ra 88,225,6144
cv 10,12,13
mvaa 72,200,148
s_and_b32 s33,s33,3
ra 92,225,7168
v_add_u32_e32 v250,4096,v252
mvaa 72,208,152
dl 246,24,51,1024
mvaa 72,216,156
s_lshl_b32 s33,s33,13
ra 96,225,8192
v_mov_b32_e32 v251,4
cvaa 76,220,156,232
s_lshr_b32 s34,59294,s35
ra 100,225,9216
s_and_b32 s34,s34,3
mvaa 76,212,152
s_lshl_b32 s34,s34,13
ra 104,225,10240
s_xor_b32 s35,s33,s34
mvaa 76,204,148
s_and_b32 s98,s19,3
ra 108,225,11264
s_lshl_b32 s98,s98,13
mvaa 76,196,144
dl 249,24,51,2048
cvaa 104,224,144,232
s_mov_b32 s74,0x300003
ra 112,225,12288
s_mov_b32 s75,0x30000300
mvaa 104,232,148
s_lshl_b32 s71,s12,15
ra 116,225,13312
s_add_u32 s71,s71,s54
mvaa 104,240,152
s_mov_b64 s[44:45],s[30:31]
ra 120,225,14336
s_mov_b32 s46,4194304
mvaa 104,248,156
s_mov_b32 s47,131072
ex 14
cvaa 108,252,156,232
dl 247,24,51,3072
mvaa 108,244,152
s_add_u32 m0,s52,49152
s_xor_b32 s51,s63,0
ra 124,225,15360
mvaa 108,236,148
s_lshl_b32 s70,s23,11
ex 15
mvaa 108,228,144
s_add_u32 s70,s70,s71
ex 40
cvaa 80,192,160,232
ex 41
mvaa 80,200,164
dl 245,24,51,0
mvaa 80,208,168
ex 42
mvaa 80,216,172
ex 43
cvaa 84,220,172,232
ex 44
mvaa 84,212,168
ex 45
mvaa 84,204,164
dl 246,24,51,1024
mvaa 84,196,160
ex 46
cvaa 112,224,160,232
ex 47
mvaa 112,232,164
ex 16
mvaa 112,240,168
ex 17
mvaa 112,248,172
dl 249,24,51,2048
cvaa 116,252,172,232
ex 18
mvaa 116,244,168
ex 19
mvaa 116,236,164
ex 20
mvaa 116,228,160
ex 21
cvaa 88,192,176,232
dl 247,24,51,3072
s_waitcnt vmcnt(8)
v_cmp_ne_u32_e32 vcc,4,v253
mvaa 88,200,180
s_cbranch_vccnz L_spbo
L_sprbo:
ex 22
mvaa 88,208,184
s_xor_b32 s50,s66,262144
ex 23
mvaa 88,216,188
ex 48
cvaa 92,220,188,232
ex 49
mvaa 92,212,184
la 128,252,50,0
mvaa 92,204,180
s_waitcnt lgkmcnt(0)
s_barrier
mvaa 92,196,176
s_mov_b32 m0,s72
ex 50
cvaa 120,224,176,232
s_lshl_b32 s72,s22,11
ex 51
mvaa 120,232,180
s_add_u32 s72,s72,s71
ex 52
mvaa 120,240,184
ds 255,8,76,0
mvaa 120,248,188
s_add_u32 s71,s70,131072
ex 53
cvaa 124,252,188,232
ex 54
mvaa 124,244,184
ex 55
mvaa 124,236,180
ex 24
mvaa 124,228,176
ds 255,8,76,1024
maav 0,64,0
s_cmp_lg_u32 s19,7
s_cbranch_scc1 L_j7o
s_nop 1
s_lshl_b32 s86,s12,12
s_add_u32 s86,s86,131072
v_add_u32_e32 v236,s86,v244
ra 160,236,0
ra 164,236,1024
ra 168,236,2048
ra 172,236,3072
ra 176,236,16384
ra 180,236,17408
ra 184,236,18432
ra 188,236,19456
v_add_u32_e32 v224,s94,v224
s_waitcnt lgkmcnt(0)
L_j7o:
ex 25
maav 0,96,4
ex 26
maav 4,68,0
ex 27
maav 4,100,4
cv 11,14,15
cv 12,40,41
maav 8,72,0
ds 255,8,76,2048
maav 8,104,4
cv 13,42,43
cv 14,44,45
maav 12,76,0
cv 15,46,47
cv 16,16,17
maav 12,108,4
ex 28
v_smfmac_f32_16x16x64_bf16 v[144:147],v[228:231],v[0:7],v248
cv 17,18,19
maav 16,80,0
ds 255,8,76,3072
maav 16,112,4
s_mov_b32 m0,s73
ex 29
maav 20,84,0
s_add_u32 s73,s72,131072
ex 30
maav 20,116,4
ex 31
maav 24,88,0
ex 56
maav 24,120,4
ds 255,8,77,0
maav 28,92,0
ex 57
maav 28,124,4
ex 58
maav 32,64,8
ex 59
maav 32,96,12
ex 60
maav 36,68,8
ds 255,8,77,1024
maav 36,100,12
cv 18,20,21
cv 19,22,23
maav 40,72,8
cv 20,48,49
cv 21,50,51
maav 40,104,12
cv 22,52,53
cv 23,54,55
maav 44,76,8
ex 61
maav 44,108,12
ds 255,8,77,2048
v_smfmac_f32_16x16x64_bf16 v[148:151],v[228:231],v[8:15],v248
cv 24,24,25
maav 48,80,8
ex 62
maav 48,112,12
ex 63
maav 52,84,8
ra 192,244,0
cv 25,26,27
maav 52,116,12
ds 255,8,77,3072
maav 56,88,8
cv 26,28,29
cv 27,30,31
maav 56,120,12
cv 28,56,57
cv 29,58,59
maav 60,92,8
cv 30,60,61
cv 31,62,63
maav 60,124,12
ex 64
mvav 160,64,16
la 132,252,50,2048
mvav 160,96,20
ex 65
mvav 164,68,16
ex 66
mvav 164,100,20
ex 67
mvav 168,72,16
ex 68
mvav 168,104,20
la 136,250,50,0
mvav 172,76,16
ex 69
mvav 172,108,20
ex 70
v_smfmac_f32_16x16x64_bf16 v[152:155],v[228:231],v[16:23],v248
ra 200,244,2048
mvav 176,80,16
ra 208,244,4096
cv 64,64,65
mvav 176,112,20
la 140,250,50,2048
mvav 180,84,16
ra 216,244,6144
cv 65,66,67
mvav 180,116,20
ex 71
mvav 184,88,16
ex 96
mvav 184,120,20
L_vs25:
s_cbranch_execz L_vs29
s_barrier
ex 97
mvav 188,92,16
la 156,250,50,3072
mvav 188,124,20
s_add_u32 m0,s52,131072
ra 196,244,1024
s_xor_b32 s51,s62,1048576
mvav 192,64,24
ra 204,244,3072
cv 66,68,69
mvav 192,96,28
ra 212,244,5120
cv 67,70,71
mvav 196,68,24
ra 220,244,7168
cv 68,96,97
mvav 196,100,28
la 152,250,50,1024
mvav 200,72,24
ex 98
mvav 200,104,28
ex 99
mvav 204,76,24
ra 224,244,8192
cv 69,98,99
mvav 204,108,28
ra 232,244,10240
v_add_u32_e32 v6,s87,v244
v_smfmac_f32_16x16x64_bf16 v[156:159],v[228:231],v[24:31],v248
ra 240,244,12288
mvav 208,80,24
la 148,252,50,3072
mvav 208,112,28
s_xor_b32 s87,s52,4096
ra 248,244,14336
v_add_u32_e32 v7,98304,v6
mvav 212,84,24
s_add_u32 s87,s87,150528
ra 252,244,15360
v_add_u32_e32 v5,65536,v244
mvav 212,116,28
ra 244,244,13312
v_add_u32_e32 v32,s87,v244
mvav 216,88,24
ra 228,244,9216
v_and_b32_e32 v224,960,v244
mvav 216,120,28
la 144,252,50,1024
mvav 220,92,24
s_add_u32 s50,s50,s55
ra 236,244,11264
mvav 220,124,28
ra 64,225,32768
s_waitcnt lgkmcnt(8)
cvaa 24,192,176,232
dl 245,24,51,0
mvaa 24,200,180
ra 96,225,40960
mvaa 24,208,184
ra 68,225,33792
mvaa 24,216,188
dl 246,24,51,1024
cvaa 16,192,160,128
ra 100,225,41984
mvaa 16,200,164
ra 72,225,34816
mvaa 16,208,168
dl 249,24,51,2048
mvaa 16,216,172
ra 104,225,43008
cvaa 28,196,176,232
ra 76,225,35840
mvaa 28,204,180
dl 247,24,51,3072
mvaa 28,212,184
s_add_u32 m0,s52,147456
s_xor_b32 s51,s63,1048576
ra 108,225,44032
mvaa 28,220,188
ra 80,225,36864
cvaa 20,196,160,132
dl 245,24,51,0
mvaa 20,204,164
ra 112,225,45056
mvaa 20,212,168
ra 192,244,16384
mvaa 20,220,172
dl 246,24,51,1024
cvaa 56,224,176,232
ra 200,244,18432
s_waitcnt lgkmcnt(7)
mvaa 56,232,180
ra 208,244,20480
mvaa 56,240,184
dl 249,24,51,2048
mvaa 56,248,188
ra 216,244,22528
cvaa 48,224,160,136
ra 196,244,17408
mvaa 48,232,164
ra 204,244,19456
mvaa 48,240,168
ra 212,244,21504
mvaa 48,248,172
ra 220,244,23552
cvaa 60,228,176,232
ra 224,244,24576
mvaa 60,236,180
ra 232,244,26624
mvaa 60,244,184
ra 240,244,28672
mvaa 60,252,188
ra 248,244,30720
cvaa 52,228,160,140
ra 84,225,37888
mvaa 52,236,164
ra 116,225,46080
mvaa 52,244,168
la 160,252,50,0
ra 228,244,25600
mvaa 52,252,172
ra 236,244,27648
s_waitcnt lgkmcnt(10)
cvaa 8,192,176,128
ra 244,244,29696
mvaa 8,200,180
la 164,252,50,2048
ra 252,244,31744
mvaa 8,208,184
ra 88,225,38912
mvaa 8,216,188
ra 120,225,47104
cvaa 12,196,176,132
la 168,250,50,0
mvaa 12,204,180
ra 92,225,39936
ra 124,225,48128
s_waitcnt lgkmcnt(9)
mvaa 12,212,184
ex 100
mvaa 12,220,188
la 172,250,50,2048
cvaa 40,224,176,136
ex 101
mvaa 40,232,180
ex 102
mvaa 40,240,184
ex 103
mvaa 40,248,188
cv 70,100,101
cv 71,102,103
s_waitcnt lgkmcnt(4)
cvaa 44,228,176,140
ex 72
mvaa 44,236,180
s_waitcnt vmcnt(18) lgkmcnt(0)
s_barrier
ex 73
mvaa 44,244,184
la 176,252,50,1024
mvaa 44,252,188
ex 74
maav 0,64,64
ex 75
maav 0,96,68
la 188,250,50,3072
maav 4,68,64
ra 192,5,0
cv 72,72,73
maav 4,100,68
ra 196,5,1024
cv 73,74,75
maav 8,72,64
la 184,250,50,1024
ds_write_b128 v6,a[0:3] offset:0
maav 8,104,68
ex 76
maav 12,76,64
ex 77
maav 12,108,68
la 180,252,50,3072
ds_write_b128 v6,a[4:7] offset:1024
v_smfmac_f32_16x16x64_bf16 v[144:147],v[228:231],v[64:71],v248
cv 74,76,77
maav 16,80,64
ex 78
maav 16,112,68
ds_write_b128 v6,a[8:11] offset:2048
ex 79
maav 20,84,64
ex 104
maav 20,116,68
ds_write_b128 v6,a[12:15] offset:3072
ex 105
maav 24,88,64
ex 106
ex 107
ex 108
maav 24,120,68
ds_write_b128 v6,a[16:19] offset:4096
cv 75,78,79
maav 28,92,64
ex 109
ex 110
ex 111
maav 28,124,68
cv 76,104,105
cv 77,106,107
cv 78,108,109
cv 79,110,111
maav 32,64,72
ds_write_b128 v6,a[20:23] offset:5120
maav 32,96,76
ex 80
maav 36,68,72
ex 81
maav 36,100,76
ds_write_b128 v6,a[24:27] offset:6144
maav 40,72,72
ex 82
maav 40,104,76
ex 83
maav 44,76,72
ds_write_b128 v6,a[28:31] offset:7168
maav 44,108,76
cv 80,80,81
cv 81,82,83
v_smfmac_f32_16x16x64_bf16 v[148:151],v[228:231],v[72:79],v248
ex 84
maav 48,80,72
ds_write_b128 v7,a[32:35] offset:0
maav 48,112,76
ex 85
ex 86
ex 87
maav 52,84,72
ex 112
ex 113
maav 52,116,76
ds_write_b128 v7,a[36:39] offset:1024
cv 82,84,85
maav 56,88,72
ex 114
ex 115
ex 116
maav 56,120,76
ex 117
ex 118
ex 119
maav 60,92,72
ds_write_b128 v7,a[40:43] offset:2048
cv 83,86,87
maav 60,124,76
cv 84,112,113
cv 85,114,115
cv 86,116,117
cv 87,118,119
mvav 160,64,80
ex 88
mvav 160,96,84
ds_write_b128 v7,a[44:47] offset:3072
mvav 164,68,80
v_add_f32_e32 v130,v144,v145
v_add_f32_e32 v131,v148,v149
mvav 164,100,84
ex 89
mvav 168,72,80
ds_write_b128 v7,a[48:51] offset:4096
mvav 168,104,84
ex 90
mvav 172,76,80
ex 91
mvav 172,108,84
ds_write_b128 v32,v[128:131]
v_smfmac_f32_16x16x64_bf16 v[152:155],v[228:231],v[80:87],v248
ex 92
ex 93
mvav 176,80,80
ds_write_b128 v7,a[52:55] offset:5120
ex 94
mvav 176,112,84
ra 200,5,2048
ra 204,5,3072
ex 95
cv 88,88,89
mvav 180,84,80
ra 208,5,4096
ra 212,5,5120
cv 89,90,91
cv 90,92,93
ex 120
mvav 180,116,84
ds_write_b128 v7,a[56:59] offset:6144
ex 121
mvav 184,88,80
ex 122
ex 123
cv 91,94,95
mvav 184,120,84
ex 124
ex 125
mvav 188,92,80
ds_write_b128 v7,a[60:63] offset:7168
ex 126
mvav 188,124,84
ra 216,5,6144
ra 220,5,7168
ra 224,5,8192
ra 232,5,10240
ex 127
cv 92,120,121
mvav 192,64,88
ra 240,5,12288
ra 248,5,14336
ra 228,5,9216
ra 236,5,11264
ra 244,5,13312
ra 252,5,15360
cv 93,122,123
cv 94,124,125
cv 95,126,127
v_xor_b32_e32 v6,8192,v6
s_nop 0
mvav 192,96,92
ex 16
mvav 196,68,88
s_waitcnt lgkmcnt(10)
s_xor_b32 s87,s87,0
mvav 196,100,92
s_xor_b32 s50,s66,294912
s_barrier
v_xor_b32_e32 v7,8192,v7
mvav 200,72,88
la 4,252,50,0
ra 64,5,16384
ra 96,5,24576
rv 64,6,0
rv 68,6,1024
rv 72,6,2048
mvav 200,104,92
rv 76,6,3072
rv 80,6,4096
rv 84,6,5120
ex 17
ex 18
mvav 204,76,88
)"
R"(rv 96,7,0
rv 100,7,1024
ex 19
mvav 204,108,92
la 12,252,50,2048
rv 104,7,2048
rv 108,7,3072
cv 16,16,17
v_smfmac_f32_16x16x64_bf16 v[156:159],v[228:231],v[88:95],v248
rv 112,7,4096
rv 116,7,5120
ex 20
ex 21
mvav 208,80,88
rv 120,7,6144
rv 124,7,7168
ex 22
ex 23
mvav 208,112,92
la 20,250,50,0
ra 68,5,17408
ra 100,5,25600
cv 17,18,19
cv 18,20,21
mvav 212,84,88
ra 72,5,18432
ra 104,5,26624
ex 48
ex 49
mvav 212,116,92
ra 76,5,19456
ra 108,5,27648
ex 50
ex 51
mvav 216,88,88
la 28,250,50,2048
ra 80,5,20480
ra 112,5,28672
ra 84,5,21504
mvav 216,120,92
ra 116,5,29696
cv 19,22,23
ex 52
ex 53
mvav 220,92,88
ra 88,5,22528
cv 20,48,49
ex 54
ex 55
mvav 220,124,92
la 60,250,50,3072
ra 120,5,30720
cv 21,50,51
cv 22,52,53
cv 23,54,55
s_bitcmp1_b32 s82,s81
s_cbranch_scc1 L_pend2
L_ret_eb5:
mvav 160,192,16
ra 92,5,23552
rv 88,6,6144
ra 124,5,31744
rv 92,6,7168
ex 24
s_waitcnt lgkmcnt(10)
mvav 160,224,20
ex 25
ex 26
v_smfmac_f32_16x16x64_bf16 v[152:155],v[228:231],v[16:23],v248
ex 27
v_xor_b32_e32 v32,4096,v32
mvav 164,196,16
la 52,250,50,1024
ds_read_b128 v[236:239],v32
cv 24,24,25
ex 28
mvav 164,228,20
ex 29
cv 25,26,27
ex 30
ex 31
mvav 168,200,16
ex 56
ex 57
mvav 168,232,20
la 44,252,50,3072
cv 26,28,29
ex 58
ex 59
mvav 172,204,16
ex 60
ex 61
ex 62
mvav 172,236,20
s_waitcnt lgkmcnt(0)
s_barrier
cv 27,30,31
ex 63
mvav 176,208,16
la 36,252,50,1024
cv 28,56,57
cv 29,58,59
cv 30,60,61
cv 31,62,63
mvav 176,240,20
s_add_u32 m0,s52,147456
ex 8
s_xor_b32 s51,s63,1048576
ex 9
ex 10
mvav 180,212,16
s_add_u32 s50,s50,s55
ex 11
cv 8,8,9
cv 9,10,11
ex 12
mvav 180,244,20
dl 247,24,51,3072
ex 13
ex 14
mvav 184,216,16
s_add_u32 m0,s52,0
ex 15
ex 40
ex 41
mvav 184,248,20
s_xor_b32 s51,s64,0
ex 42
ex 43
ex 44
mvav 188,220,16
la 0,252,50,0
ex 45
ex 46
mvav 188,252,20
ex 47
cv 10,12,13
cv 11,14,15
cv 12,40,41
cv 13,42,43
cv 14,44,45
v_smfmac_f32_16x16x64_bf16 v[156:159],v[228:231],v[24:31],v248
cv 15,46,47
v_bfe_u32 v225,v244,1,5
s_nop 0
v_smfmac_f32_16x16x64_bf16 v[156:159],v[228:231],v[8:15],v248
v_lshl_add_u32 v224,v224,1,v225
v_bfe_u32 v225,v244,7,2
v_lshl_add_u32 v224,v225,5,v224
mvav 192,192,24
la 8,252,50,2048
v_xor_b32_e32 v224,s69,v224
v_add_f32_e32 v152,v152,v153
v_add_f32_e32 v137,v152,v238
v_subrev_u32_e32 v236,0x23800000,v137
mvav 192,224,28
v_rcp_f32_e32 v137,v137
mvav 192,64,8
tv 128,224,32768
v_mul_f32_e32 v232,v240,v137
v_mul_f32_e32 v64,v232,v64
v_mul_f32_e32 v65,v232,v65
v_mul_f32_e32 v66,v232,v66
mvav 192,96,12
la 16,250,50,0
v_mul_f32_e32 v67,v232,v67
v_mul_f32_e32 v68,v232,v68
v_mul_f32_e32 v69,v232,v69
v_mul_f32_e32 v70,v232,v70
mvav 196,196,24
v_fmac_f32_e32 v64,v232,v160
v_fmac_f32_e32 v65,v232,v161
v_mul_f32_e32 v71,v232,v71
mvav 196,228,28
v_fmac_f32_e32 v66,v232,v162
v_fmac_f32_e32 v67,v232,v163
v_fmac_f32_e32 v68,v232,v164
v_fmac_f32_e32 v69,v232,v165
v_fmac_f32_e32 v70,v232,v166
mvav 196,68,8
la 24,250,50,2048
v_fmac_f32_e32 v71,v232,v167
cv 160,64,65
cv 161,66,67
cv 162,68,69
cv 163,70,71
mvav 196,100,12
tv 130,224,34816
tv 132,224,36864
mvav 200,200,24
tv 134,224,38912
tv 144,224,40960
v_mul_f32_e32 v72,v232,v72
v_mul_f32_e32 v73,v232,v73
v_mul_f32_e32 v74,v232,v74
mvav 200,232,28
buffer_store_dwordx4 v[160:163],v244,s[8:11],s14 offen offset:0 sc0 sc1
v_mul_f32_e32 v75,v232,v75
v_mul_f32_e32 v76,v232,v76
v_mul_f32_e32 v77,v232,v77
v_mul_f32_e32 v78,v232,v78
mvav 200,72,8
v_fmac_f32_e32 v72,v232,v168
v_fmac_f32_e32 v73,v232,v169
v_mul_f32_e32 v79,v232,v79
mvav 200,104,12
v_fmac_f32_e32 v74,v232,v170
v_fmac_f32_e32 v75,v232,v171
v_fmac_f32_e32 v76,v232,v172
v_fmac_f32_e32 v77,v232,v173
v_fmac_f32_e32 v78,v232,v174
mvav 204,204,24
la 56,250,50,3072
v_fmac_f32_e32 v79,v232,v175
cv 168,72,73
cv 169,74,75
cv 170,76,77
cv 171,78,79
mvav 204,236,28
tv 146,224,43008
tv 148,224,45056
mvav 204,76,8
tv 150,224,47104
v_mul_f32_e32 v80,v232,v80
v_mul_f32_e32 v81,v232,v81
v_mul_f32_e32 v82,v232,v82
mvav 204,108,12
buffer_store_dwordx4 v[168:171],v244,s[8:11],s14 offen offset:2048 sc0 sc1
v_mul_f32_e32 v83,v232,v83
v_mul_f32_e32 v84,v232,v84
v_mul_f32_e32 v85,v232,v85
v_mul_f32_e32 v86,v232,v86
v_mul_f32_e32 v87,v232,v87
mvav 208,208,24
v_fmac_f32_e32 v80,v232,v176
v_fmac_f32_e32 v81,v232,v177
v_fmac_f32_e32 v82,v232,v178
mvav 208,240,28
v_fmac_f32_e32 v83,v232,v179
v_fmac_f32_e32 v84,v232,v180
v_fmac_f32_e32 v85,v232,v181
cv 176,80,81
cv 177,82,83
mvav 208,80,8
la 48,250,50,1024
v_fmac_f32_e32 v86,v232,v182
v_fmac_f32_e32 v87,v232,v183
cv 178,84,85
cv 179,86,87
mvav 208,112,12
v_mul_f32_e32 v88,v232,v88
v_mul_f32_e32 v89,v232,v89
v_mul_f32_e32 v90,v232,v90
mvav 212,212,24
v_mul_f32_e32 v91,v232,v91
v_mul_f32_e32 v92,v232,v92
v_mul_f32_e32 v93,v232,v93
v_mul_f32_e32 v94,v232,v94
v_mul_f32_e32 v95,v232,v95
mvav 212,244,28
L_vs26:
s_cbranch_execz L_vs30
buffer_store_dwordx4 v[176:179],v244,s[8:11],s88 offen offset:0 sc0 sc1
v_fmac_f32_e32 v88,v232,v184
v_fmac_f32_e32 v89,v232,v185
v_fmac_f32_e32 v90,v232,v186
v_fmac_f32_e32 v91,v232,v187
cv 184,88,89
mvav 212,84,8
v_fmac_f32_e32 v92,v232,v188
cv 185,90,91
mvav 212,116,12
v_fmac_f32_e32 v93,v232,v189
v_fmac_f32_e32 v94,v232,v190
v_fmac_f32_e32 v95,v232,v191
cv 186,92,93
cv 187,94,95
mvav 216,216,24
la 40,252,50,3072
v_add_f32_e32 v156,v156,v157
v_add_f32_e32 v141,v156,v239
v_subrev_u32_e32 v237,0x23800000,v141
v_rcp_f32_e32 v141,v141
mvav 216,248,28
v_mul_f32_e32 v233,v241,v141
v_mul_f32_e32 v96,v233,v96
mvav 216,88,8
v_mul_f32_e32 v97,v233,v97
v_mul_f32_e32 v98,v233,v98
v_mul_f32_e32 v99,v233,v99
v_mul_f32_e32 v100,v233,v100
mvav 216,120,12
buffer_store_dwordx4 v[184:187],v244,s[8:11],s88 offen offset:2048 sc0 sc1
v_fmac_f32_e32 v96,v233,v192
v_fmac_f32_e32 v97,v233,v193
v_mul_f32_e32 v101,v233,v101
v_mul_f32_e32 v102,v233,v102
v_mul_f32_e32 v103,v233,v103
mvav 220,220,24
v_fmac_f32_e32 v98,v233,v194
v_fmac_f32_e32 v99,v233,v195
mvav 220,252,28
v_fmac_f32_e32 v100,v233,v196
v_fmac_f32_e32 v101,v233,v197
cv 192,96,97
cv 193,98,99
mvav 220,92,8
la 32,252,50,1024
v_fmac_f32_e32 v102,v233,v198
v_fmac_f32_e32 v103,v233,v199
cv 194,100,101
cv 195,102,103
mvav 220,124,12
v_xor_b32_e32 v225,32,v224
v_xor_b32_e32 v226,64,v224
s_waitcnt lgkmcnt(0)
zvva 0,128,128
tv 138,225,34816
tv 142,225,38912
tv 152,225,40960
s_waitcnt vmcnt(36)
mvva 0,132,132
tv 136,225,32768
tv 154,225,43008
mvva 0,144,136
tv 140,225,36864
tv 158,225,47104
mvva 0,148,140
tv 156,225,45056
v_mul_f32_e32 v104,v233,v104
v_mul_f32_e32 v105,v233,v105
v_mul_f32_e32 v106,v233,v106
zvva 4,148,156
buffer_store_dwordx4 v[192:195],v244,s[8:11],s14 offen offset:1024 sc0 sc1
v_mul_f32_e32 v107,v233,v107
v_mul_f32_e32 v108,v233,v108
v_mul_f32_e32 v109,v233,v109
v_mul_f32_e32 v110,v233,v110
mvva 4,144,152
v_fmac_f32_e32 v104,v233,v200
v_fmac_f32_e32 v105,v233,v201
v_fmac_f32_e32 v106,v233,v202
v_mul_f32_e32 v111,v233,v111
mvva 4,132,148
v_fmac_f32_e32 v107,v233,v203
v_fmac_f32_e32 v108,v233,v204
v_fmac_f32_e32 v109,v233,v205
cv 200,104,105
cv 201,106,107
mvva 4,128,144
v_fmac_f32_e32 v110,v233,v206
v_fmac_f32_e32 v111,v233,v207
cv 202,108,109
cv 203,110,111
s_waitcnt vmcnt(16)
zvva 8,128,160
v_cmp_lt_u32_e64 s[42:43],s48,v236
v_mul_f32_e32 v112,v233,v112
s_or_b64 s[40:41],s[40:41],s[42:43]
v_cmp_lt_u32_e64 s[42:43],s48,v237
mvva 8,132,164
s_or_b64 s[40:41],s[40:41],s[42:43]
v_mul_f32_e32 v113,v233,v113
v_mul_f32_e32 v114,v233,v114
v_mul_f32_e32 v115,v233,v115
mvva 8,144,168
dl 245,36,51,0
v_mul_f32_e32 v116,v233,v116
v_mul_f32_e32 v117,v233,v117
v_mul_f32_e32 v118,v233,v118
v_mul_f32_e32 v119,v233,v119
v_mul_f32_e32 v120,v233,v120
mvva 8,148,172
v_fmac_f32_e32 v112,v233,v208
v_fmac_f32_e32 v113,v233,v209
v_fmac_f32_e32 v114,v233,v210
v_fmac_f32_e32 v115,v233,v211
zvva 12,148,188
v_fmac_f32_e32 v116,v233,v212
v_fmac_f32_e32 v117,v233,v213
cv 208,112,113
cv 209,114,115
mvva 12,144,184
v_fmac_f32_e32 v118,v233,v214
v_fmac_f32_e32 v119,v233,v215
cv 210,116,117
cv 211,118,119
mvva 12,132,180
v_mul_f32_e32 v121,v233,v121
v_mul_f32_e32 v122,v233,v122
v_mul_f32_e32 v123,v233,v123
mvva 12,128,176
v_mul_f32_e32 v124,v233,v124
v_mul_f32_e32 v125,v233,v125
v_mul_f32_e32 v126,v233,v126
v_mul_f32_e32 v127,v233,v127
s_waitcnt lgkmcnt(0)
zvva 28,136,176
buffer_store_dwordx4 v[200:203],v244,s[8:11],s14 offen offset:3072 sc0 sc1
tv 144,226,40960
v_fmac_f32_e32 v120,v233,v216
v_fmac_f32_e32 v121,v233,v217
v_fmac_f32_e32 v122,v233,v218
v_fmac_f32_e32 v123,v233,v219
mvva 28,140,180
tv 128,226,32768
tv 130,226,34816
cv 216,120,121
cv 217,122,123
mvva 28,152,184
tv 132,226,36864
tv 134,226,38912
v_fmac_f32_e32 v124,v233,v220
v_fmac_f32_e32 v125,v233,v221
v_fmac_f32_e32 v126,v233,v222
mvva 28,156,188
tv 146,226,43008
v_fmac_f32_e32 v127,v233,v223
cv 218,124,125
cv 219,126,127
zvva 24,156,172
tv 148,226,45056
tv 150,226,47104
mvva 24,152,168
v_xor_b32_e32 v227,96,v224
v_add_u32_e32 v232,131072,v224
mvva 24,140,164
buffer_store_dwordx4 v[208:211],v244,s[8:11],s88 offen offset:1024 sc0 sc1
mvva 24,136,160
v_add_u32_e32 v233,131072,v225
v_add_u32_e32 v234,131072,v226
zvva 20,136,144
v_add_u32_e32 v235,131072,v227
v_mov_b32_e32 v242,83914752
mvva 20,140,148
v_add_u32_e32 v228,65536,v224
v_add_u32_e32 v229,65536,v225
mvva 20,152,152
v_add_u32_e32 v230,65536,v226
v_add_u32_e32 v231,65536,v227
mvva 20,156,156
v_and_b32_e32 v243,240,v244
v_lshlrev_b32_e32 v243,7,v243
zvva 16,156,140
buffer_store_dwordx4 v[216:219],v244,s[8:11],s88 offen offset:3072 sc0 sc1
s_cmp_lg_u64 s[40:41],0
s_cbranch_scc1 L_redo
L_redo_r5:
mvva 16,152,136
tv 156,227,45056
v_mov_b32_e32 v236,16256
mvva 16,140,132
tv 158,227,47104
v_and_b32_e32 v248,256,v244
mvva 16,136,128
tv 152,227,40960
v_lshrrev_b32_e32 v248,3,v248
s_waitcnt lgkmcnt(3)
zvva 32,128,128
tv 140,227,36864
v_add_u32_e32 v243,v243,v248
mvva 32,132,132
tv 136,227,32768
tv 138,227,34816
mvva 32,144,136
dl 246,36,51,1024
tv 142,227,38912
mvva 32,148,140
tv 154,227,43008
v_and_b32_e32 v248,512,v244
zvva 36,148,156
v_lshrrev_b32_e32 v248,5,v248
v_add_u32_e32 v243,v243,v248
mvva 36,144,152
v_and_b32_e32 v248,16,v244
v_lshlrev_b32_e32 v248,v248,v236
mvva 36,132,148
v_cndmask_b32_e64 v238,0,v248,s[74:75]
s_mov_b32 s74,0xc0000c
mvva 36,128,144
s_mov_b32 s75,0xc0000c00
v_mov_b32_e32 v240,0
v_cndmask_b32_e64 v239,0,v248,s[74:75]
zvva 40,128,160
dl 249,36,51,2048
mvva 40,132,164
v_mov_b32_e32 v241,0
v_mov_b32_e32 v236,0
mvva 40,144,168
v_mov_b32_e32 v237,0
mvva 40,148,172
zvva 44,148,188
mvva 44,144,184
mvva 44,132,180
dl 247,36,51,3072
s_add_u32 m0,s52,16384
s_xor_b32 s51,s65,0
mvva 44,128,176
tv 144,224,57344
s_waitcnt lgkmcnt(1)
zvva 60,136,176
tv 132,224,53248
tv 134,224,55296
mvva 60,140,180
tv 128,224,49152
tv 130,224,51200
mvva 60,152,184
mvva 60,156,188
tv 146,224,59392
zvva 56,156,172
dl 245,36,51,0
tv 148,224,61440
tv 150,224,63488
mvva 56,152,168
mvva 56,140,164
mvva 56,136,160
zvva 52,136,144
mvva 52,140,148
mvva 52,152,152
dl 246,36,51,1024
mvva 52,156,156
zvva 48,156,140
mvva 48,152,136
mvva 48,140,132
tv 156,225,61440
mvva 48,136,128
tv 152,225,57344
s_waitcnt lgkmcnt(2)
zvva 64,128,128
dl 249,36,51,2048
tv 140,225,53248
mvva 64,132,132
tv 136,225,49152
tv 138,225,51200
mvva 64,144,136
tv 142,225,55296
mvva 64,148,140
tv 154,225,59392
zvva 68,148,156
tv 158,225,63488
mvva 68,144,152
mvva 68,132,148
dl 247,36,51,3072
s_lshl_b32 s51,s33,5
s_add_u32 m0,s52,98304
s_xor_b32 s51,s64,s51
mvva 68,128,144
zvva 72,128,160
mvva 72,132,164
s_waitcnt vmcnt(18)
s_mov_b32 s90,0
mvva 72,144,168
s_xor_b32 s50,s67,0
mvva 72,148,172
zvva 76,148,188
dl 245,36,51,0
mvva 76,144,184
mvva 76,132,180
tv 148,226,61440
mvva 76,128,176
tv 144,226,57344
s_waitcnt lgkmcnt(2)
zvva 92,136,176
tv 132,226,53248
mvva 92,140,180
tv 128,226,49152
tv 130,226,51200
mvva 92,152,184
dl 246,36,51,1024
tv 134,226,55296
mvva 92,156,188
tv 146,226,59392
zvva 88,156,172
tv 150,226,63488
mvva 88,152,168
mvva 88,140,164
mvva 88,136,160
zvva 84,136,144
dl 249,36,51,2048
mvva 84,140,148
mvva 84,152,152
mvva 84,156,156
zvva 80,156,140
mvva 80,152,136
mvva 80,140,132
dl 247,36,51,3072
mvva 80,136,128
s_lshl_b32 s51,s33,5
tv 152,227,57344
s_add_u32 m0,s52,114688
s_waitcnt lgkmcnt(1)
zvva 96,128,128
s_xor_b32 s51,s65,s51
tv 140,227,53248
tv 142,227,55296
mvva 96,132,132
tv 136,227,49152
tv 138,227,51200
mvva 96,144,136
mvva 96,148,140
tv 154,227,59392
zvva 100,148,156
lv 164,252,50,0
tv 156,227,61440
tv 158,227,63488
mvva 100,144,152
mvva 100,132,148
mvva 100,128,144
zvva 104,128,160
mvva 104,132,164
mvva 104,144,168
lv 172,252,50,2048
mvva 104,148,172
zvva 108,148,188
mvva 108,144,184
mvva 108,132,180
mvva 108,128,176
s_barrier
s_waitcnt lgkmcnt(0)
zvva 124,136,176
lv 180,250,50,0
tv 132,232,4096
tv 144,232,8192
tv 146,232,10240
mvva 124,140,180
tv 128,232,0
tv 130,232,2048
mvva 124,152,184
tv 134,232,6144
mvva 124,156,188
tv 148,232,12288
tv 150,232,14336
zvva 120,156,172
mvva 120,152,168
mvva 120,140,164
lv 188,250,50,2048
mvva 120,136,160
zvva 116,136,144
mvva 116,140,148
mvva 116,152,152
mvva 116,156,156
zvva 112,156,140
lv 220,250,50,3072
mvva 112,152,136
mvva 112,140,132
tv 156,233,12288
mvva 112,136,128
tv 152,233,8192
s_waitcnt lgkmcnt(2)
mvva 0,128,4
tv 140,233,4096
mvva 0,132,12
tv 136,233,0
tv 138,233,2048
mvva 0,144,20
lv 212,250,50,1024
tv 142,233,6144
mvva 0,148,28
tv 154,233,10240
mvva 4,148,60
tv 158,233,14336
mvva 4,144,52
mvva 4,132,44
mvva 4,128,36
mvva 8,128,0
lv 204,252,50,3072
mvva 8,132,8
mvva 8,144,16
mvva 8,148,24
mvva 12,148,56
s_waitcnt vmcnt(27)
mvva 12,144,48
s_waitcnt vmcnt(25)
mvva 12,132,40
lv 196,252,50,1024
s_waitcnt vmcnt(24)
mvva 12,128,32
tv 144,234,8192
s_waitcnt lgkmcnt(1)
mvva 28,136,32
tv 132,234,4096
tv 134,234,6144
mvva 28,140,40
tv 128,234,0
tv 130,234,2048
mvva 28,152,48
mvva 28,156,56
tv 146,234,10240
mvva 24,156,24
dl 245,36,51,0
s_waitcnt vmcnt(20)
s_barrier
tv 148,234,12288
tv 150,234,14336
mvva 24,152,16
mvva 24,140,8
mvva 24,136,0
mvva 20,136,36
mvva 20,140,44
mvva 20,152,52
s_cmp_eq_u32 s12,0
s_cselect_b64 exec,1,0
buffer_store_dword v251,off,s[8:11],s61 sc0 sc1
s_mov_b64 exec,-1
mvva 20,156,60
mvva 16,156,28
mvva 16,152,20
mvva 16,140,12
tv 156,235,12288
mvva 16,136,4
tv 152,235,8192
s_waitcnt lgkmcnt(2)
mvva 32,128,4
dl 246,36,51,1024
tv 140,235,4096
mvva 32,132,12
tv 136,235,0
tv 138,235,2048
mvva 32,144,20
tv 142,235,6144
mvva 32,148,28
tv 154,235,10240
mvva 36,148,60
tv 158,235,14336
mvva 36,144,52
mvva 36,132,44
dl 249,36,51,2048
mvva 36,128,36
mvva 40,128,0
mvva 40,132,8
mvva 40,144,16
mvva 40,148,24
mvva 44,148,56
dl 247,36,51,3072
s_lshl_b32 s51,s34,5
s_add_u32 m0,s52,65536
s_xor_b32 s51,s64,s51
mvva 44,144,48
tv 148,232,28672
mvva 44,132,40
s_xor_b32 s53,s60,s33
tv 150,232,30720
mvva 44,128,32
tv 144,232,24576
s_waitcnt lgkmcnt(3)
mvva 60,136,32
tv 132,232,20480
mvva 60,140,40
tv 128,232,16384
tv 130,232,18432
mvva 60,152,48
dl 245,36,51,0
tv 134,232,22528
mvva 60,156,56
tv 146,232,26624
mvva 56,156,24
mvva 56,152,16
mvva 56,140,8
mvva 56,136,0
mvva 52,136,36
buffer_load_dword v253,v242,s[8:11],s53 offen sc1
mvva 52,140,44
mvva 52,152,52
mvva 52,156,60
mvva 48,156,28
mvva 48,152,20
mvva 48,140,12
dl 246,36,51,1024
mvva 48,136,4
tv 152,233,24576
s_waitcnt lgkmcnt(1)
mvva 64,128,4
tv 140,233,20480
tv 142,233,22528
mvva 64,132,12
tv 136,233,16384
tv 138,233,18432
mvva 64,144,20
mvva 64,148,28
tv 154,233,26624
mvva 68,148,60
dl 249,36,51,2048
tv 156,233,28672
tv 158,233,30720
mvva 68,144,52
mvva 68,132,44
mvva 68,128,36
mvva 72,128,0
mvva 72,132,8
mvva 72,144,16
dl 247,36,51,3072
s_lshl_b32 s51,s34,5
s_add_u32 m0,s52,81920
s_xor_b32 s51,s65,s51
mvva 72,148,24
mvva 76,148,56
mvva 76,144,48
mvva 76,132,40
tv 148,234,28672
mvva 76,128,32
tv 144,234,24576
s_waitcnt lgkmcnt(2)
mvva 92,136,32
dl 245,36,51,0
tv 132,234,20480
mvva 92,140,40
tv 128,234,16384
tv 130,234,18432
mvva 92,152,48
tv 134,234,22528
mvva 92,156,56
tv 146,234,26624
mvva 88,156,24
tv 150,234,30720
mvva 88,152,16
mvva 88,140,8
dl 246,36,51,1024
mvva 88,136,0
mvva 84,136,36
mvva 84,140,44
mvva 84,152,52
mvva 84,156,60
mvva 80,156,28
dl 249,36,51,2048
mvva 80,152,20
mvva 80,140,12
tv 156,235,28672
mvva 80,136,4
tv 152,235,24576
s_waitcnt lgkmcnt(2)
mvva 96,128,4
tv 140,235,20480
mvva 96,132,12
tv 136,235,16384
tv 138,235,18432
mvva 96,144,20
dl 247,36,51,3072
s_lshl_b32 s51,s35,5
s_add_u32 m0,s52,32768
s_xor_b32 s51,s64,s51
tv 142,235,22528
mvva 96,148,28
L_vs27:
s_cbranch_execz L_vs16
tv 154,235,26624
mvva 100,148,60
tv 158,235,30720
mvva 100,144,52
mvva 100,132,44
mvva 100,128,36
mvva 104,128,0
dl 245,36,51,0
mvva 104,132,8
mvva 104,144,16
mvva 104,148,24
mvva 108,148,56
mvva 108,144,48
mvva 108,132,40
dl 246,36,51,1024
mvva 108,128,32
s_waitcnt vmcnt(27)
s_barrier
s_waitcnt lgkmcnt(0)
mvva 124,136,32
tv 132,224,4096
tv 134,224,6144
tv 144,224,8192
mvva 124,140,40
tv 128,224,0
tv 130,224,2048
mvva 124,152,48
mvva 124,156,56
tv 146,224,10240
mvva 120,156,24
dl 249,36,51,2048
tv 148,224,12288
tv 150,224,14336
mvva 120,152,16
mvva 120,140,8
mvva 120,136,0
mvva 116,136,36
mvva 116,140,44
mvva 116,152,52
dl 247,36,51,3072
s_lshl_b32 s51,s35,5
s_add_u32 m0,s52,49152
s_xor_b32 s51,s65,s51
mvva 116,156,60
mvva 112,156,28
mvva 112,152,20
s_waitcnt vmcnt(11)
v_cmp_ne_u32_e32 vcc,4,v253
mvva 112,140,12
dl 245,36,51,0
s_cbranch_vccnz L_spa3o
L_spra3o:
mvva 112,136,4
s_xor_b32 s50,s67,s33
tv 152,225,8192
s_waitcnt lgkmcnt(1)
mvvv 0,128,164
tv 140,225,4096
tv 142,225,6144
mvvv 0,132,172
tv 136,225,0
tv 138,225,2048
mvvv 0,144,180
dl 246,36,51,1024
mvvv 0,148,188
tv 154,225,10240
mvvv 4,148,220
tv 156,225,12288
tv 158,225,14336
mvvv 4,144,212
mvvv 4,132,204
la 196,252,50,0
mvvv 4,128,196
mvvv 8,128,160
s_xor_b32 s53,s60,s34
mvvv 8,132,168
mvvv 8,144,176
la 204,252,50,2048
mvvv 8,148,184
mvvv 12,148,216
mvvv 12,144,208
mvvv 12,132,200
buffer_load_dword v253,v242,s[8:11],s53 offen sc1
mvvv 12,128,192
tv 144,226,8192
s_waitcnt lgkmcnt(1)
mvvv 28,136,192
tv 132,226,4096
tv 134,226,6144
mvvv 28,140,200
tv 128,226,0
tv 130,226,2048
mvvv 28,152,208
la 212,250,50,0
mvvv 28,156,216
tv 146,226,10240
mvvv 24,156,184
tv 148,226,12288
tv 150,226,14336
mvvv 24,152,176
mvvv 24,140,168
mvvv 24,136,160
la 220,250,50,2048
mvvv 20,136,196
mvvv 20,140,204
mvvv 20,152,212
mvvv 20,156,220
la 252,250,50,3072
mvvv 16,156,188
mvvv 16,152,180
mvvv 16,140,172
tv 156,227,12288
mvvv 16,136,164
la 244,250,50,1024
tv 152,227,8192
s_waitcnt lgkmcnt(2)
mvvv 32,128,164
tv 140,227,4096
mvvv 32,132,172
tv 136,227,0
tv 138,227,2048
mvvv 32,144,180
tv 142,227,6144
mvvv 32,148,188
la 236,252,50,3072
tv 154,227,10240
mvvv 36,148,220
tv 158,227,14336
mvvv 36,144,212
mvvv 36,132,204
mvvv 36,128,196
la 228,252,50,1024
s_add_u32 s50,s50,s55
mvvv 40,128,160
mvvv 40,132,168
mvvv 40,144,176
mvvv 40,148,184
mvvv 44,148,216
la 192,252,50,0
mvvv 44,144,208
mvvv 44,132,200
tv 148,224,28672
tv 150,224,30720
mvvv 44,128,192
s_waitcnt lgkmcnt(2)
mvvv 60,136,192
la 200,252,50,2048
tv 132,224,20480
mvvv 60,140,200
tv 128,224,16384
tv 130,224,18432
mvvv 60,152,208
tv 134,224,22528
mvvv 60,156,216
tv 144,224,24576
tv 146,224,26624
)"
R"(mvvv 56,156,184
la 208,250,50,0
mvvv 56,152,176
mvvv 56,140,168
mvvv 56,136,160
mvvv 52,136,196
la 216,250,50,2048
mvvv 52,140,204
mvvv 52,152,212
mvvv 52,156,220
mvvv 48,156,188
la 248,250,50,3072
mvvv 48,152,180
mvvv 48,140,172
tv 156,225,28672
mvvv 48,136,164
tv 152,225,24576
s_waitcnt lgkmcnt(2)
mvvv 64,128,164
tv 140,225,20480
mvvv 64,132,172
la 240,250,50,1024
tv 136,225,16384
tv 138,225,18432
mvvv 64,144,180
tv 142,225,22528
mvvv 64,148,188
tv 154,225,26624
mvvv 68,148,220
tv 158,225,30720
mvvv 68,144,212
la 232,252,50,3072
mvvv 68,132,204
mvvv 68,128,196
mvvv 72,128,160
mvvv 72,132,168
la 224,252,50,1024
mvvv 72,144,176
mvvv 72,148,184
mvvv 76,148,216
mvvv 76,144,208
mvvv 76,132,200
dl 249,36,51,2048
mvvv 76,128,192
tv 144,226,24576
s_waitcnt lgkmcnt(1)
mvvv 92,136,192
tv 132,226,20480
tv 134,226,22528
mvvv 92,140,200
tv 128,226,16384
tv 130,226,18432
mvvv 92,152,208
dl 247,36,51,3072
s_lshl_b32 s51,s98,5
s_xor_b32 s51,s64,s51
s_add_u32 m0,s52,131072
s_xor_b32 s51,s51,4194304
mvvv 92,156,216
tv 146,226,26624
mvvv 88,156,184
tv 148,226,28672
tv 150,226,30720
mvvv 88,152,176
mvvv 88,140,168
dl 245,36,51,0
mvvv 88,136,160
s_waitcnt vmcnt(17)
v_cmp_ne_u32_e32 vcc,4,v253
mvvv 84,136,196
s_cbranch_vccnz L_spa4o
L_spra4o:
mvvv 84,140,204
s_xor_b32 s50,s67,s34
mvvv 84,152,212
dl 246,36,51,1024
mvvv 84,156,220
mvvv 80,156,188
mvvv 80,152,180
mvvv 80,140,172
la 68,252,50,0
mvvv 80,136,164
tv 152,227,24576
s_waitcnt lgkmcnt(1)
mvvv 96,128,164
tv 140,227,20480
tv 142,227,22528
mvvv 96,132,172
tv 136,227,16384
tv 138,227,18432
mvvv 96,144,180
s_xor_b32 s53,s60,s35
mvvv 96,148,188
la 76,252,50,2048
tv 154,227,26624
mvvv 100,148,220
tv 156,227,28672
tv 158,227,30720
mvvv 100,144,212
mvvv 100,132,204
mvvv 100,128,196
la 84,250,50,0
mvvv 104,128,160
mvvv 104,132,168
mvvv 104,144,176
mvvv 104,148,184
buffer_load_dword v253,v242,s[8:11],s53 offen sc1
mvvv 108,148,216
mvvv 108,144,208
mvvv 108,132,200
mvvv 108,128,192
s_barrier
s_waitcnt lgkmcnt(0)
mvvv 124,136,192
la 92,250,50,2048
tv 132,228,36864
tv 144,228,40960
tv 146,228,43008
mvvv 124,140,200
tv 128,228,32768
tv 130,228,34816
mvvv 124,152,208
tv 134,228,38912
mvvv 124,156,216
tv 148,228,45056
tv 150,228,47104
mvvv 120,156,184
la 124,250,50,3072
mvvv 120,152,176
mvvv 120,140,168
mvvv 120,136,160
mvvv 116,136,196
la 116,250,50,1024
mvvv 116,140,204
mvvv 116,152,212
mvvv 116,156,220
mvvv 112,156,188
la 108,252,50,3072
mvvv 112,152,180
mvvv 112,140,172
tv 156,229,45056
tv 158,229,47104
mvvv 112,136,164
s_waitcnt lgkmcnt(2)
mvva 0,128,196
la 100,252,50,1024
s_add_u32 s50,s50,s55
tv 140,229,36864
mvva 0,132,204
tv 136,229,32768
tv 138,229,34816
s_waitcnt vmcnt(21)
mvva 0,144,212
tv 142,229,38912
mvva 0,148,220
tv 152,229,40960
tv 154,229,43008
mvva 4,148,252
mvva 4,144,244
la 64,252,50,0
mvva 4,132,236
mvva 4,128,228
s_waitcnt vmcnt(21)
mvva 8,128,192
s_waitcnt vmcnt(20)
mvva 8,132,200
la 72,252,50,2048
s_waitcnt vmcnt(20)
mvva 8,144,208
s_waitcnt vmcnt(19)
mvva 8,148,216
s_waitcnt vmcnt(18)
mvva 12,148,248
s_waitcnt vmcnt(17)
mvva 12,144,240
la 80,250,50,0
s_waitcnt vmcnt(17)
mvva 12,132,232
tv 148,230,45056
s_waitcnt vmcnt(16)
mvva 12,128,224
tv 144,230,40960
s_waitcnt lgkmcnt(2)
mvva 28,136,224
tv 132,230,36864
mvva 28,140,232
la 88,250,50,2048
tv 128,230,32768
tv 130,230,34816
mvva 28,152,240
tv 134,230,38912
mvva 28,156,248
tv 146,230,43008
mvva 24,156,216
tv 150,230,47104
mvva 24,152,208
la 120,250,50,3072
mvva 24,140,200
mvva 24,136,192
mvva 20,136,228
mvva 20,140,236
mvva 20,152,244
la 112,250,50,1024
mvva 20,156,252
mvva 16,156,220
mvva 16,152,212
mvva 16,140,204
la 104,252,50,3072
mvva 16,136,196
tv 152,231,40960
s_waitcnt lgkmcnt(1)
mvva 32,128,196
tv 140,231,36864
tv 142,231,38912
mvva 32,132,204
tv 136,231,32768
tv 138,231,34816
mvva 32,144,212
la 96,252,50,1024
mvva 32,148,220
tv 154,231,43008
mvva 36,148,252
tv 156,231,45056
tv 158,231,47104
mvva 36,144,244
mvva 36,132,236
mvva 36,128,228
dl 249,36,51,2048
mvva 40,128,192
mvva 40,132,200
mvva 40,144,208
mvva 40,148,216
dl 247,36,51,3072
s_lshl_b32 s51,s98,5
s_xor_b32 s51,s65,s51
s_add_u32 m0,s52,147456
s_xor_b32 s51,s51,4194304
mvva 44,148,248
mvva 44,144,240
mvva 44,132,232
tv 148,228,61440
mvva 44,128,224
dl 245,36,51,0
tv 144,228,57344
s_waitcnt lgkmcnt(2)
mvva 60,136,224
tv 132,228,53248
mvva 60,140,232
tv 128,228,49152
tv 130,228,51200
s_waitcnt vmcnt(16)
v_cmp_ne_u32_e32 vcc,4,v253
mvva 60,152,240
s_cbranch_vccnz L_spa5o
L_spra5o:
tv 134,228,55296
s_xor_b32 s50,s67,s35
mvva 60,156,248
dl 246,36,51,1024
tv 146,228,59392
mvva 56,156,216
tv 150,228,63488
mvva 56,152,208
mvva 56,140,200
mvva 56,136,192
la 132,252,50,0
mvva 52,136,228
mvva 52,140,236
mvva 52,152,244
mvva 52,156,252
mvva 48,156,220
la 140,252,50,2048
s_xor_b32 s53,s60,s98
s_xor_b32 s53,s53,512
mvva 48,152,212
tv 156,229,61440
mvva 48,140,204
tv 158,229,63488
mvva 48,136,196
tv 152,229,57344
s_waitcnt lgkmcnt(3)
mvva 64,128,196
la 148,250,50,0
tv 140,229,53248
mvva 64,132,204
tv 136,229,49152
tv 138,229,51200
mvva 64,144,212
tv 142,229,55296
mvva 64,148,220
tv 154,229,59392
mvva 68,148,252
buffer_load_dword v253,v242,s[8:11],s53 offen sc1
mvva 68,144,244
mvva 68,132,236
mvva 68,128,228
mvva 72,128,192
mvva 72,132,200
la 156,250,50,2048
mvva 72,144,208
mvva 72,148,216
mvva 76,148,248
mvva 76,144,240
la 188,250,50,3072
mvva 76,132,232
tv 148,230,61440
mvva 76,128,224
tv 144,230,57344
s_waitcnt lgkmcnt(2)
mvva 92,136,224
tv 132,230,53248
mvva 92,140,232
la 180,250,50,1024
tv 128,230,49152
tv 130,230,51200
mvva 92,152,240
tv 134,230,55296
mvva 92,156,248
tv 146,230,59392
mvva 88,156,216
tv 150,230,63488
mvva 88,152,208
la 172,252,50,3072
mvva 88,140,200
mvva 88,136,192
mvva 84,136,228
mvva 84,140,236
la 164,252,50,1024
s_add_u32 s50,s50,s55
mvva 84,152,244
mvva 84,156,252
mvva 80,156,220
mvva 80,152,212
mvva 80,140,204
la 128,252,50,0
mvva 80,136,196
tv 152,231,57344
s_waitcnt lgkmcnt(1)
mvva 96,128,196
tv 140,231,53248
tv 142,231,55296
mvva 96,132,204
tv 136,231,49152
tv 138,231,51200
mvva 96,144,212
la 136,252,50,2048
mvva 96,148,220
tv 154,231,59392
mvva 100,148,252
tv 156,231,61440
tv 158,231,63488
mvva 100,144,244
mvva 100,132,236
la 144,250,50,0
mvva 100,128,228
mvva 104,128,192
mvva 104,132,200
mvva 104,144,208
la 152,250,50,2048
mvva 104,148,216
mvva 108,148,248
mvva 108,144,240
mvva 108,132,232
la 184,250,50,3072
mvva 108,128,224
s_barrier
s_waitcnt lgkmcnt(0)
mvva 124,136,224
tv 132,228,4096
tv 134,228,6144
tv 144,228,8192
mvva 124,140,232
tv 128,228,0
tv 130,228,2048
mvva 124,152,240
mvva 124,156,248
la 176,250,50,1024
tv 146,228,10240
mvva 120,156,216
tv 148,228,12288
tv 150,228,14336
mvva 120,152,208
mvva 120,140,200
mvva 120,136,192
la 168,252,50,3072
mvva 116,136,228
L_vs28:
s_cbranch_execz L_vr1
mvva 116,140,236
mvva 116,152,244
mvva 116,156,252
la 160,252,50,1024
mvva 112,156,220
mvva 112,152,212
mvva 112,140,204
tv 156,229,12288
tv 158,229,14336
mvva 112,136,196
s_waitcnt lgkmcnt(2)
mvva 0,128,68
dl 249,36,51,2048
tv 140,229,4096
mvva 0,132,76
tv 136,229,0
tv 138,229,2048
mvva 0,144,84
tv 142,229,6144
s_waitcnt vmcnt(22)
mvva 0,148,92
tv 152,229,8192
tv 154,229,10240
mvva 4,148,124
dl 247,36,51,3072
s_lshl_b32 s51,s98,5
s_xor_b32 s51,s64,s51
s_add_u32 m0,s52,0
s_xor_b32 s51,s51,4456448
mvva 4,144,116
mvva 4,132,108
mvva 4,128,100
mvva 8,128,64
dl 245,36,51,0
mvva 8,132,72
mvva 8,144,80
mvva 8,148,88
s_waitcnt vmcnt(16)
v_cmp_ne_u32_e32 vcc,4,v253
mvva 12,148,120
dl 246,36,51,1024
s_cbranch_vccnz L_spa6o
L_spra6o:
mvva 12,144,112
s_xor_b32 s50,s67,s98
mvva 12,132,104
s_xor_b32 s50,s50,131072
tv 148,230,12288
tv 150,230,14336
mvva 12,128,96
s_waitcnt lgkmcnt(2)
mvva 28,136,96
dl 249,36,51,2048
tv 132,230,4096
mvva 28,140,104
tv 128,230,0
tv 130,230,2048
mvva 28,152,112
tv 134,230,6144
mvva 28,156,120
tv 144,230,8192
tv 146,230,10240
mvva 24,156,88
mvva 24,152,80
la 4,252,50,0
mvva 24,140,72
s_xor_b32 s53,s60,s98
mvva 24,136,64
s_xor_b32 s53,s53,8704
mvva 20,136,100
mvva 20,140,108
la 12,252,50,2048
mvva 20,152,116
mvva 20,156,124
mvva 16,156,92
mvva 16,152,84
buffer_load_dword v253,v242,s[8:11],s53 offen sc1
mvva 16,140,76
tv 156,231,12288
mvva 16,136,68
tv 152,231,8192
s_waitcnt lgkmcnt(2)
mvva 32,128,68
tv 140,231,4096
mvva 32,132,76
la 20,250,50,0
tv 136,231,0
tv 138,231,2048
mvva 32,144,84
tv 142,231,6144
mvva 32,148,92
tv 154,231,10240
mvva 36,148,124
tv 158,231,14336
mvva 36,144,116
la 28,250,50,2048
mvva 36,132,108
mvva 36,128,100
mvva 40,128,64
mvva 40,132,72
mvva 40,144,80
la 60,250,50,3072
mvva 40,148,88
mvva 44,148,120
mvva 44,144,112
mvva 44,132,104
la 52,250,50,1024
mvva 44,128,96
tv 144,228,24576
s_waitcnt lgkmcnt(1)
mvva 60,136,96
tv 132,228,20480
tv 134,228,22528
mvva 60,140,104
tv 128,228,16384
tv 130,228,18432
mvva 60,152,112
la 44,252,50,3072
mvva 60,156,120
tv 146,228,26624
mvva 56,156,88
tv 148,228,28672
tv 150,228,30720
mvva 56,152,80
mvva 56,140,72
mvva 56,136,64
la 36,252,50,1024
s_add_u32 s50,s50,s55
mvva 52,136,100
mvva 52,140,108
mvva 52,152,116
mvva 52,156,124
la 0,252,50,0
mvva 48,156,92
mvva 48,152,84
mvva 48,140,76
tv 156,229,28672
mvva 48,136,68
la 8,252,50,2048
tv 152,229,24576
s_waitcnt lgkmcnt(2)
mvva 64,128,68
tv 140,229,20480
mvva 64,132,76
tv 136,229,16384
tv 138,229,18432
mvva 64,144,84
tv 142,229,22528
mvva 64,148,92
la 16,250,50,0
tv 154,229,26624
mvva 68,148,124
tv 158,229,30720
mvva 68,144,116
mvva 68,132,108
mvva 68,128,100
la 24,250,50,2048
mvva 72,128,64
mvva 72,132,72
mvva 72,144,80
mvva 72,148,88
mvva 76,148,120
la 56,250,50,3072
mvva 76,144,112
mvva 76,132,104
tv 148,224,45056
tv 150,224,47104
mvva 76,128,96
s_waitcnt lgkmcnt(2)
mvva 92,136,96
la 48,250,50,1024
tv 132,224,36864
mvva 92,140,104
tv 128,224,32768
tv 130,224,34816
mvva 92,152,112
tv 134,224,38912
mvva 92,156,120
tv 144,224,40960
tv 146,224,43008
mvva 88,156,88
la 40,252,50,3072
mvva 88,152,80
mvva 88,140,72
mvva 88,136,64
mvva 84,136,100
mvva 84,140,108
la 32,252,50,1024
mvva 84,152,116
mvva 84,156,124
mvva 80,156,92
mvva 80,152,84
dl 247,36,51,3072
s_lshl_b32 s51,s98,5
s_xor_b32 s51,s64,s51
s_add_u32 m0,s52,16384
s_xor_b32 s51,s51,4718592
mvva 80,140,76
tv 152,225,40960
mvva 80,136,68
tv 154,225,43008
s_waitcnt lgkmcnt(2)
mvva 0,128,132
tv 140,225,36864
mvva 0,132,140
dl 245,36,51,0
tv 136,225,32768
tv 138,225,34816
mvva 0,144,148
tv 142,225,38912
s_waitcnt vmcnt(24)
mvva 0,148,156
mvva 4,148,188
tv 156,225,45056
tv 158,225,47104
mvva 4,144,180
dl 246,36,51,1024
s_waitcnt vmcnt(17)
v_cmp_ne_u32_e32 vcc,4,v253
mvva 4,132,172
s_cbranch_vccnz L_spa7o
L_spra7o:
mvva 4,128,164
s_xor_b32 s50,s67,s98
mvva 8,128,128
s_xor_b32 s50,s50,139264
mvva 8,132,136
dl 249,36,51,2048
mvva 8,144,144
mvva 8,148,152
mvva 12,148,184
mvva 12,144,176
mvva 12,132,168
dl 247,36,51,3072
mvva 12,128,160
s_barrier
tv 144,226,40960
s_lshl_b32 s51,s98,5
s_waitcnt lgkmcnt(1)
mvva 28,136,160
s_xor_b32 s53,s60,s98
tv 132,226,36864
tv 134,226,38912
s_xor_b32 s53,s53,16896
mvva 28,140,168
s_xor_b32 s51,s65,s51
tv 128,226,32768
tv 130,226,34816
mvva 28,152,176
lv 164,252,50,0
mvva 28,156,184
s_add_u32 m0,s52,98304
tv 146,226,43008
s_xor_b32 s51,s51,4456448
mvva 24,156,152
tv 148,226,45056
tv 150,226,47104
mvva 24,152,144
mvva 24,140,136
buffer_load_dword v253,v242,s[8:11],s53 offen sc1
mvva 24,136,128
mvva 20,136,164
mvva 20,140,172
mvva 20,152,180
lv 172,252,50,2048
mvva 20,156,188
mvva 16,156,156
mvva 16,152,148
mvva 16,140,140
lv 180,250,50,0
mvva 16,136,132
tv 152,227,40960
s_waitcnt lgkmcnt(1)
mvva 32,128,132
tv 140,227,36864
tv 142,227,38912
mvva 32,132,140
tv 136,227,32768
tv 138,227,34816
mvva 32,144,148
mvva 32,148,156
lv 188,250,50,2048
tv 154,227,43008
mvva 36,148,188
tv 156,227,45056
tv 158,227,47104
mvva 36,144,180
mvva 36,132,172
mvva 36,128,164
lv 220,250,50,3072
mvva 40,128,128
mvva 40,132,136
mvva 40,144,144
mvva 40,148,152
lv 212,250,50,1024
mvva 44,148,184
mvva 44,144,176
mvva 44,132,168
tv 148,224,61440
tv 150,224,63488
mvva 44,128,160
s_waitcnt lgkmcnt(2)
mvva 60,136,160
lv 204,252,50,3072
tv 132,224,53248
mvva 60,140,168
tv 128,224,49152
tv 130,224,51200
mvva 60,152,176
tv 134,224,55296
mvva 60,156,184
tv 144,224,57344
tv 146,224,59392
mvva 56,156,152
lv 196,252,50,1024
s_add_u32 s50,s50,s55
mvva 56,152,144
mvva 56,140,136
mvva 56,136,128
mvva 52,136,164
lv 160,252,50,0
mvva 52,140,172
mvva 52,152,180
mvva 52,156,188
mvva 48,156,156
lv 168,252,50,2048
mvva 48,152,148
mvva 48,140,140
tv 156,225,61440
tv 158,225,63488
mvva 48,136,132
s_waitcnt lgkmcnt(2)
mvva 64,128,132
lv 176,250,50,0
tv 140,225,53248
mvva 64,132,140
tv 136,225,49152
tv 138,225,51200
mvva 64,144,148
tv 142,225,55296
mvva 64,148,156
tv 152,225,57344
tv 154,225,59392
mvva 68,148,188
mvva 68,144,180
lv 184,250,50,2048
mvva 68,132,172
mvva 68,128,164
mvva 72,128,128
mvva 72,132,136
lv 216,250,50,3072
mvva 72,144,144
mvva 72,148,152
mvva 76,148,184
mvva 76,144,176
lv 208,250,50,1024
mvva 76,132,168
tv 148,232,12288
mvva 76,128,160
tv 144,232,8192
s_waitcnt lgkmcnt(2)
mvva 92,136,160
tv 132,232,4096
mvva 92,140,168
tv 128,232,0
tv 130,232,2048
mvva 92,152,176
lv 200,252,50,3072
tv 134,232,6144
mvva 92,156,184
tv 146,232,10240
mvva 88,156,152
tv 150,232,14336
mvva 88,152,144
mvva 88,140,136
lv 192,252,50,1024
mvva 88,136,128
mvva 84,136,164
mvva 84,140,172
mvva 84,152,180
dl 245,36,51,0
mvva 84,156,188
mvva 80,156,156
mvva 80,152,148
mvva 80,140,140
dl 246,36,51,1024
mvva 80,136,132
tv 152,233,8192
s_waitcnt vmcnt(17)
v_cmp_ne_u32_e32 vcc,4,v253
s_waitcnt lgkmcnt(1)
mvva 0,128,4
s_cbranch_vccnz L_spa8o
L_spra8o:
tv 140,233,4096
tv 142,233,6144
mvva 0,132,12
s_xor_b32 s50,s67,s98
tv 136,233,0
s_xor_b32 s50,s50,147456
tv 138,233,2048
mvva 0,144,20
dl 249,36,51,2048
mvva 0,148,28
tv 154,233,10240
v_and_b32_e32 v242,48,v244
mvva 4,148,60
tv 156,233,12288
tv 158,233,14336
mvva 4,144,52
v_lshlrev_b32_e32 v242,3,v242
mvva 4,132,44
mvva 4,128,36
la 196,252,50,0
mvva 8,128,0
s_andn2_b32 s53,s61,384
mvva 8,132,8
mvva 8,144,16
mvva 8,148,24
la 204,252,50,2048
mvva 12,148,56
mvva 12,144,48
mvva 12,132,40
tv 148,234,12288
mvva 12,128,32
buffer_load_dword v253,v242,s[8:11],s53 offen sc1
tv 144,234,8192
s_waitcnt lgkmcnt(2)
mvva 28,136,32
tv 132,234,4096
mvva 28,140,40
tv 128,234,0
tv 130,234,2048
mvva 28,152,48
tv 134,234,6144
mvva 28,156,56
la 212,250,50,0
tv 146,234,10240
mvva 24,156,24
tv 150,234,14336
mvva 24,152,16
mvva 24,140,8
mvva 24,136,0
la 220,250,50,2048
mvva 20,136,36
mvva 20,140,44
mvva 20,152,52
mvva 20,156,60
mvva 16,156,28
la 252,250,50,3072
mvva 16,152,20
mvva 16,140,12
tv 156,235,12288
tv 158,235,14336
mvva 16,136,4
s_waitcnt lgkmcnt(2)
mvva 32,128,4
la 244,250,50,1024
tv 140,235,4096
mvva 32,132,12
tv 136,235,0
tv 138,235,2048
mvva 32,144,20
tv 142,235,6144
mvva 32,148,28
tv 152,235,8192
tv 154,235,10240
mvva 36,148,60
la 236,252,50,3072
mvva 36,144,52
mvva 36,132,44
mvva 36,128,36
mvva 40,128,0
mvva 40,132,8
la 228,252,50,1024
s_add_u32 s50,s50,s55
mvva 40,144,16
mvva 40,148,24
mvva 44,148,56
mvva 44,144,48
la 192,252,50,0
mvva 44,132,40
mvva 44,128,32
s_barrier
s_waitcnt lgkmcnt(0)
mvva 60,136,32
tv 132,232,20480
tv 144,232,24576
mvva 60,140,40
la 200,252,50,2048
tv 128,232,16384
tv 130,232,18432
mvva 60,152,48
tv 134,232,22528
mvva 60,156,56
tv 146,232,26624
mvva 56,156,24
tv 148,232,28672
tv 150,232,30720
mvva 56,152,16
la 208,250,50,0
mvva 56,140,8
mvva 56,136,0
mvva 52,136,36
mvva 52,140,44
la 216,250,50,2048
mvva 52,152,52
mvva 52,156,60
mvva 48,156,28
mvva 48,152,20
mvva 48,140,12
la 248,250,50,3072
mvva 48,136,4
tv 152,233,24576
s_waitcnt lgkmcnt(1)
mvva 64,128,4
tv 140,233,20480
tv 142,233,22528
mvva 64,132,12
tv 136,233,16384
tv 138,233,18432
mvva 64,144,20
la 240,250,50,1024
mvva 64,148,28
tv 154,233,26624
mvva 68,148,60
tv 156,233,28672
tv 158,233,30720
mvva 68,144,52
mvva 68,132,44
la 232,252,50,3072
mvva 68,128,36
mvva 72,128,0
mvva 72,132,8
mvva 72,144,16
la 224,252,50,1024
mvva 72,148,24
L_vs29:
s_cbranch_execz L_vr1
mvva 76,148,56
mvva 76,144,48
mvva 76,132,40
dl 247,36,51,3072
s_lshl_b32 s51,s98,5
s_xor_b32 s51,s65,s51
s_add_u32 m0,s52,114688
s_xor_b32 s51,s51,4718592
mvva 76,128,32
tv 144,224,8192
s_waitcnt lgkmcnt(1)
mvva 92,136,32
tv 132,224,4096
tv 134,224,6144
mvva 92,140,40
tv 128,224,0
tv 130,224,2048
mvva 92,152,48
mvva 92,156,56
dl 245,36,51,0
tv 146,224,10240
mvva 88,156,24
tv 148,224,12288
tv 150,224,14336
mvva 88,152,16
mvva 88,140,8
mvva 88,136,0
dl 246,36,51,1024
s_waitcnt vmcnt(17)
v_cmp_ne_u32_e32 vcc,4,v253
mvva 84,136,36
s_cbranch_vccnz L_sppo
L_sprpo:
mvva 84,140,44
mvva 84,152,52
mvva 84,156,60
dl 249,36,51,2048
mvva 80,156,28
mvva 80,152,20
mvva 80,140,12
tv 156,225,12288
mvva 80,136,4
tv 152,225,8192
s_waitcnt lgkmcnt(2)
mvvv 0,128,164
dl 247,36,51,3072
tv 140,225,4096
mvvv 0,132,172
tv 136,225,0
tv 138,225,2048
mvvv 0,144,180
tv 142,225,6144
mvvv 0,148,188
tv 154,225,10240
mvvv 4,148,220
tv 158,225,14336
mvvv 4,144,212
mvvv 4,132,204
mvvv 4,128,196
mvvv 8,128,160
mvvv 8,132,168
mvvv 8,144,176
mvvv 8,148,184
mvvv 12,148,216
mvvv 12,144,208
mvvv 12,132,200
tv 148,224,28672
mvvv 12,128,192
tv 144,224,24576
s_waitcnt lgkmcnt(2)
mvvv 28,136,192
tv 132,224,20480
mvvv 28,140,200
)"
R"(tv 128,224,16384
tv 130,224,18432
mvvv 28,152,208
tv 134,224,22528
mvvv 28,156,216
tv 146,224,26624
mvvv 24,156,184
tv 150,224,30720
mvvv 24,152,176
mvvv 24,140,168
mvvv 24,136,160
mvvv 20,136,196
mvvv 20,140,204
mvvv 20,152,212
mvvv 20,156,220
mvvv 16,156,188
mvvv 16,152,180
mvvv 16,140,172
tv 156,225,28672
mvvv 16,136,164
tv 152,225,24576
s_waitcnt lgkmcnt(2)
mvva 0,128,196
tv 140,225,20480
mvva 0,132,204
tv 136,225,16384
tv 138,225,18432
s_waitcnt vmcnt(14)
mvva 0,144,212
tv 142,225,22528
mvva 0,148,220
tv 154,225,26624
mvva 4,148,252
tv 158,225,30720
mvva 4,144,244
mvva 4,132,236
s_waitcnt vmcnt(13)
mvva 4,128,228
s_waitcnt vmcnt(12)
mvva 8,128,192
cv 0,0,1
cv 1,2,3
s_waitcnt vmcnt(11)
mvva 8,132,200
s_waitcnt vmcnt(10)
mvva 8,144,208
cv 4,4,5
cv 5,6,7
s_waitcnt vmcnt(9)
mvva 8,148,216
s_waitcnt vmcnt(8)
mvva 12,148,248
s_waitcnt vmcnt(7)
mvva 12,144,240
s_waitcnt vmcnt(6)
mvva 12,132,232
tv 148,226,12288
s_waitcnt vmcnt(5)
mvva 12,128,224
tv 144,226,8192
cv 8,8,9
s_waitcnt lgkmcnt(2)
mvva 28,136,224
tv 132,226,4096
cv 9,10,11
mvva 28,140,232
tv 128,226,0
tv 130,226,2048
mvva 28,152,240
tv 134,226,6144
cv 12,12,13
mvva 28,156,248
tv 146,226,10240
cv 13,14,15
mvva 24,156,216
tv 150,226,14336
mvva 24,152,208
mvva 24,140,200
mvva 24,136,192
mvva 20,136,228
cv 14,28,29
cv 15,30,31
mvva 20,140,236
buffer_store_dwordx4 v[12:15],v244,s[8:11],s56 offen offset:3072 sc0 sc1
mvva 20,152,244
mvva 20,156,252
mvva 16,156,220
cv 10,24,25
cv 11,26,27
mvva 16,152,212
buffer_store_dwordx4 v[8:11],v244,s[8:11],s56 offen offset:2048 sc0 sc1
mvva 16,140,204
tv 156,227,12288
mvva 16,136,196
cv 6,20,21
cv 7,22,23
s_waitcnt lgkmcnt(1)
mvvv 32,128,164
tv 140,227,4096
mvvv 32,132,172
buffer_store_dwordx4 v[4:7],v244,s[8:11],s56 offen offset:1024 sc0 sc1
tv 136,227,0
tv 138,227,2048
mvvv 32,144,180
tv 142,227,6144
cv 2,16,17
mvvv 32,148,188
tv 152,227,8192
tv 154,227,10240
mvvv 36,148,220
tv 158,227,14336
cv 3,18,19
mvvv 36,144,212
buffer_store_dwordx4 v[0:3],v244,s[8:11],s56 offen offset:0 sc0 sc1
mvvv 36,132,204
mvvv 36,128,196
mvvv 40,128,160
mvvv 40,132,168
mvvv 40,144,176
mvvv 40,148,184
mvvv 44,148,216
mvvv 44,144,208
mvvv 44,132,200
tv 148,226,28672
mvvv 44,128,192
s_waitcnt vmcnt(4)
s_barrier
tv 144,226,24576
s_waitcnt lgkmcnt(2)
mvvv 60,136,192
tv 132,226,20480
mvvv 60,140,200
tv 128,226,16384
tv 130,226,18432
mvvv 60,152,208
tv 134,226,22528
mvvv 60,156,216
tv 146,226,26624
mvvv 56,156,184
tv 150,226,30720
mvvv 56,152,176
mvvv 56,140,168
mvvv 56,136,160
mvvv 52,136,196
mvvv 52,140,204
mvvv 52,152,212
mvvv 52,156,220
mvvv 48,156,188
mvvv 48,152,180
mvvv 48,140,172
tv 156,227,28672
mvvv 48,136,164
tv 152,227,24576
s_waitcnt lgkmcnt(2)
mvva 32,128,196
tv 140,227,20480
mvva 32,132,204
tv 136,227,16384
tv 138,227,18432
mvva 32,144,212
tv 142,227,22528
mvva 32,148,220
tv 154,227,26624
mvva 36,148,252
tv 158,227,30720
mvva 36,144,244
mvva 36,132,236
mvva 36,128,228
mvva 40,128,192
mvva 40,132,200
cv 32,32,33
cv 33,34,35
mvva 40,144,208
mvva 40,148,216
mvva 44,148,248
mvva 44,144,240
cv 36,36,37
cv 37,38,39
mvva 44,132,232
tv 148,228,45056
mvva 44,128,224
tv 144,228,40960
cv 40,40,41
s_waitcnt lgkmcnt(2)
mvva 60,136,224
tv 132,228,36864
cv 41,42,43
mvva 60,140,232
tv 128,228,32768
tv 130,228,34816
mvva 60,152,240
tv 134,228,38912
cv 44,44,45
mvva 60,156,248
tv 146,228,43008
cv 45,46,47
mvva 56,156,216
tv 150,228,47104
mvva 56,152,208
mvva 56,140,200
mvva 56,136,192
mvva 52,136,228
cv 46,60,61
cv 47,62,63
mvva 52,140,236
buffer_store_dwordx4 v[44:47],v244,s[8:11],s57 offen offset:3072 sc0 sc1
mvva 52,152,244
mvva 52,156,252
mvva 48,156,220
cv 42,56,57
cv 43,58,59
mvva 48,152,212
buffer_store_dwordx4 v[40:43],v244,s[8:11],s57 offen offset:2048 sc0 sc1
mvva 48,140,204
tv 156,229,45056
mvva 48,136,196
cv 38,52,53
cv 39,54,55
s_waitcnt lgkmcnt(1)
mvvv 64,128,164
tv 140,229,36864
mvvv 64,132,172
buffer_store_dwordx4 v[36:39],v244,s[8:11],s57 offen offset:1024 sc0 sc1
tv 136,229,32768
tv 138,229,34816
mvvv 64,144,180
tv 142,229,38912
cv 34,48,49
mvvv 64,148,188
tv 152,229,40960
tv 154,229,43008
mvvv 68,148,220
tv 158,229,47104
cv 35,50,51
mvvv 68,144,212
buffer_store_dwordx4 v[32:35],v244,s[8:11],s57 offen offset:0 sc0 sc1
mvvv 68,132,204
mvvv 68,128,196
mvvv 72,128,160
mvvv 72,132,168
mvvv 72,144,176
mvvv 72,148,184
mvvv 76,148,216
mvvv 76,144,208
mvvv 76,132,200
tv 148,228,61440
mvvv 76,128,192
tv 144,228,57344
s_waitcnt lgkmcnt(2)
mvvv 92,136,192
tv 132,228,53248
mvvv 92,140,200
tv 128,228,49152
tv 130,228,51200
mvvv 92,152,208
tv 134,228,55296
mvvv 92,156,216
tv 146,228,59392
mvvv 88,156,184
tv 150,228,63488
mvvv 88,152,176
mvvv 88,140,168
mvvv 88,136,160
mvvv 84,136,196
mvvv 84,140,204
mvvv 84,152,212
mvvv 84,156,220
mvvv 80,156,188
mvvv 80,152,180
mvvv 80,140,172
tv 156,229,61440
mvvv 80,136,164
tv 152,229,57344
s_waitcnt lgkmcnt(2)
mvva 64,128,196
tv 140,229,53248
mvva 64,132,204
tv 136,229,49152
tv 138,229,51200
mvva 64,144,212
tv 142,229,55296
mvva 64,148,220
tv 154,229,59392
mvva 68,148,252
tv 158,229,63488
mvva 68,144,244
mvva 68,132,236
mvva 68,128,228
s_xor_b32 s82,s59,12
mvva 72,128,192
s_mul_i32 s82,s82,12288
cv 64,64,65
s_add_u32 s82,s82,0
mvva 72,132,200
lv 16,244,82,0
mvva 72,144,208
cv 65,66,67
cv 68,68,69
mvva 72,148,216
cv 69,70,71
mvva 76,148,248
mvva 76,144,240
lv 20,244,82,1024
mvva 76,132,232
tv 148,230,28672
mvva 76,128,224
tv 144,230,24576
cv 72,72,73
s_waitcnt lgkmcnt(2)
mvva 92,136,224
tv 132,230,20480
cv 73,74,75
mvva 92,140,232
tv 128,230,16384
tv 130,230,18432
mvva 92,152,240
lv 24,244,82,2048
tv 134,230,22528
mvva 92,156,248
tv 146,230,26624
cv 76,76,77
mvva 88,156,216
tv 150,230,30720
cv 77,78,79
mvva 88,152,208
mvva 88,140,200
lv 28,244,82,3072
mvva 88,136,192
cv 78,92,93
cv 79,94,95
mvva 84,136,228
mvva 84,140,236
mvva 84,152,244
buffer_store_dwordx4 v[76:79],v244,s[8:11],s58 offen offset:3072 sc0 sc1
mvva 84,156,252
mvva 80,156,220
cv 74,88,89
cv 75,90,91
mvva 80,152,212
mvva 80,140,204
buffer_store_dwordx4 v[72:75],v244,s[8:11],s58 offen offset:2048 sc0 sc1
tv 156,231,28672
mvva 80,136,196
cv 70,84,85
cv 71,86,87
s_waitcnt lgkmcnt(1)
mvva 96,128,68
tv 140,231,20480
mvva 96,132,76
tv 136,231,16384
tv 138,231,18432
mvva 96,144,84
buffer_store_dwordx4 v[68:71],v244,s[8:11],s58 offen offset:1024 sc0 sc1
tv 142,231,22528
mvva 96,148,92
tv 152,231,24576
tv 154,231,26624
mvva 100,148,124
tv 158,231,30720
cv 66,80,81
cv 67,82,83
mvva 100,144,116
mvva 100,132,108
mvva 100,128,100
buffer_store_dwordx4 v[64:67],v244,s[8:11],s58 offen offset:0 sc0 sc1
mvva 104,128,64
mvva 104,132,72
mvva 104,144,80
mvva 104,148,88
mvva 108,148,120
mvva 108,144,112
mvva 108,132,104
tv 148,226,61440
mvva 108,128,96
tv 144,226,57344
s_waitcnt lgkmcnt(2)
mvva 124,136,96
tv 132,226,53248
mvva 124,140,104
tv 128,226,49152
tv 130,226,51200
mvva 124,152,112
tv 134,226,55296
mvva 124,156,120
tv 146,226,59392
mvva 120,156,88
tv 150,226,63488
mvva 120,152,80
mvva 120,140,72
mvva 120,136,64
mvva 116,136,100
mvva 116,140,108
s_waitcnt vmcnt(7)
v_max_u32_e32 v248,v16,v17
mvva 116,152,116
v_max3_u32 v248,v248,v18,v19
s_waitcnt vmcnt(6)
v_max3_u32 v248,v248,v20,v21
v_max3_u32 v248,v248,v22,v23
mvva 116,156,124
s_waitcnt vmcnt(5)
v_max3_u32 v248,v248,v24,v25
v_max3_u32 v248,v248,v26,v27
s_waitcnt vmcnt(4)
v_max3_u32 v248,v248,v28,v29
mvva 112,156,92
v_max3_u32 v248,v248,v30,v31
v_cmp_eq_u32_e32 vcc,-1,v248
mvva 112,152,84
s_cbranch_vccnz L_spx0o
L_sprx0o:
mvva 112,140,76
tv 156,227,61440
tv 158,227,63488
v_lshlrev_b32_e32 v251,16,v18
v_and_b32_e32 v255,4294901760,v18
mvva 112,136,68
tv 152,227,57344
tv 154,227,59392
s_waitcnt lgkmcnt(4)
mvva 96,128,132
v_lshlrev_b32_e32 v253,16,v19
v_and_b32_e32 v242,4294901760,v19
mvva 96,132,140
tv 136,227,49152
tv 138,227,51200
mvva 96,144,148
tv 140,227,53248
tv 142,227,55296
v_add_f32_e32 v112,v112,v251
mvva 96,148,156
v_add_f32_e32 v113,v113,v255
v_add_f32_e32 v114,v114,v253
mvva 100,148,188
v_add_f32_e32 v115,v115,v242
v_lshlrev_b32_e32 v251,16,v22
mvva 100,144,180
v_and_b32_e32 v255,4294901760,v22
v_lshlrev_b32_e32 v253,16,v23
mvva 100,132,172
v_and_b32_e32 v242,4294901760,v23
v_add_f32_e32 v116,v116,v251
v_add_f32_e32 v117,v117,v255
mvva 100,128,164
v_add_f32_e32 v118,v118,v253
v_add_f32_e32 v119,v119,v242
mvva 104,128,128
v_lshlrev_b32_e32 v251,16,v26
v_and_b32_e32 v255,4294901760,v26
mvva 104,132,136
v_lshlrev_b32_e32 v253,16,v27
v_and_b32_e32 v242,4294901760,v27
mvva 104,144,144
v_add_f32_e32 v120,v120,v251
v_add_f32_e32 v121,v121,v255
v_add_f32_e32 v122,v122,v253
mvva 104,148,152
v_add_f32_e32 v123,v123,v242
v_lshlrev_b32_e32 v251,16,v30
mvva 108,148,184
v_and_b32_e32 v255,4294901760,v30
v_lshlrev_b32_e32 v253,16,v31
mvva 108,144,176
v_and_b32_e32 v242,4294901760,v31
v_add_f32_e32 v124,v124,v251
mvva 108,132,168
v_add_f32_e32 v125,v125,v255
v_add_f32_e32 v126,v126,v253
v_add_f32_e32 v127,v127,v242
mvva 108,128,160
tv 144,234,24576
v_lshlrev_b32_e32 v251,16,v16
s_waitcnt lgkmcnt(1)
mvva 124,136,160
tv 132,234,20480
v_and_b32_e32 v255,4294901760,v16
mvva 124,140,168
L_vs30:
s_cbranch_execz L_vr1
tv 128,234,16384
tv 130,234,18432
mvva 124,152,176
tv 134,234,22528
v_lshlrev_b32_e32 v253,16,v17
mvva 124,156,184
tv 146,234,26624
v_and_b32_e32 v242,4294901760,v17
mvva 120,156,152
tv 148,234,28672
tv 150,234,30720
mvva 120,152,144
v_add_f32_e32 v96,v96,v251
v_add_f32_e32 v97,v97,v255
mvva 120,140,136
v_add_f32_e32 v98,v98,v253
v_add_f32_e32 v99,v99,v242
mvva 120,136,128
v_lshlrev_b32_e32 v251,16,v20
v_and_b32_e32 v255,4294901760,v20
mvva 116,136,164
v_lshlrev_b32_e32 v253,16,v21
v_and_b32_e32 v242,4294901760,v21
mvva 116,140,172
v_add_f32_e32 v100,v100,v251
v_add_f32_e32 v101,v101,v255
mvva 116,152,180
v_add_f32_e32 v102,v102,v253
v_add_f32_e32 v103,v103,v242
mvva 116,156,188
v_lshlrev_b32_e32 v251,16,v24
v_and_b32_e32 v255,4294901760,v24
mvva 112,156,156
v_lshlrev_b32_e32 v253,16,v25
v_and_b32_e32 v242,4294901760,v25
mvva 112,152,148
v_add_f32_e32 v104,v104,v251
v_add_f32_e32 v105,v105,v255
mvva 112,140,140
tv 156,235,28672
v_add_f32_e32 v106,v106,v253
mvva 112,136,132
tv 152,235,24576
v_add_f32_e32 v107,v107,v242
s_waitcnt lgkmcnt(2)
mvva 96,128,4
tv 140,235,20480
v_lshlrev_b32_e32 v251,16,v28
mvva 96,132,12
tv 136,235,16384
tv 138,235,18432
mvva 96,144,20
tv 142,235,22528
v_and_b32_e32 v255,4294901760,v28
mvva 96,148,28
tv 154,235,26624
v_lshlrev_b32_e32 v253,16,v29
mvva 100,148,60
tv 158,235,30720
v_and_b32_e32 v242,4294901760,v29
mvva 100,144,52
v_add_f32_e32 v108,v108,v251
v_add_f32_e32 v109,v109,v255
mvva 100,132,44
v_add_f32_e32 v110,v110,v253
v_add_f32_e32 v111,v111,v242
mvva 100,128,36
mvva 104,128,0
mvva 104,132,8
mvva 104,144,16
mvva 104,148,24
mvva 108,148,56
mvva 108,144,48
mvva 108,132,40
tv 148,230,45056
mvva 108,128,32
tv 144,230,40960
s_waitcnt lgkmcnt(2)
mvva 124,136,32
s_xor_b32 s83,s59,8
tv 132,230,36864
s_mul_i32 s83,s83,12288
mvva 124,140,40
s_add_u32 s83,s83,4096
tv 128,230,32768
tv 130,230,34816
mvva 124,152,48
lv 48,244,83,0
tv 134,230,38912
mvva 124,156,56
tv 146,230,43008
mvva 120,156,24
lv 52,244,83,1024
tv 150,230,47104
mvva 120,152,16
mvva 120,140,8
lv 56,244,83,2048
mvva 120,136,0
mvva 116,136,36
lv 60,244,83,3072
mvva 116,140,44
mvva 116,152,52
s_xor_b32 s84,s59,4
mvva 116,156,60
s_mul_i32 s84,s84,12288
mvva 112,156,28
s_add_u32 s84,s84,8192
mvva 112,152,20
lv 80,244,84,0
mvva 112,140,12
tv 156,231,45056
mvva 112,136,4
lv 84,244,84,1024
tv 152,231,40960
s_waitcnt lgkmcnt(2)
mvvv 96,128,164
tv 140,231,36864
mvvv 96,132,172
lv 88,244,84,2048
tv 136,231,32768
tv 138,231,34816
mvvv 96,144,180
tv 142,231,38912
mvvv 96,148,188
lv 92,244,84,3072
tv 154,231,43008
mvvv 100,148,220
tv 158,231,47104
mvvv 100,144,212
mvvv 100,132,204
mvvv 100,128,196
mvvv 104,128,160
mvvv 104,132,168
mvvv 104,144,176
mvvv 104,148,184
mvvv 108,148,216
mvvv 108,144,208
mvvv 108,132,200
tv 148,230,61440
mvvv 108,128,192
tv 144,230,57344
s_waitcnt lgkmcnt(2)
mvvv 124,136,192
tv 132,230,53248
mvvv 124,140,200
tv 128,230,49152
tv 130,230,51200
mvvv 124,152,208
tv 134,230,55296
mvvv 124,156,216
tv 146,230,59392
mvvv 120,156,184
tv 150,230,63488
mvvv 120,152,176
mvvv 120,140,168
mvvv 120,136,160
mvvv 116,136,196
mvvv 116,140,204
mvvv 116,152,212
mvvv 116,156,220
mvvv 112,156,188
mvvv 112,152,180
mvvv 112,140,172
tv 156,231,61440
mvvv 112,136,164
tv 152,231,57344
s_waitcnt lgkmcnt(2)
mvva 96,128,196
tv 140,231,53248
mvva 96,132,204
tv 136,231,49152
tv 138,231,51200
mvva 96,144,212
tv 142,231,55296
mvva 96,148,220
tv 154,231,59392
mvva 100,148,252
tv 158,231,63488
mvva 100,144,244
mvva 100,132,236
mvva 100,128,228
mvva 104,128,192
mvva 104,132,200
mvva 104,144,208
mvva 104,148,216
s_waitcnt vmcnt(7)
v_max_u32_e32 v248,v48,v49
mvva 108,148,248
v_max3_u32 v248,v248,v50,v51
s_waitcnt vmcnt(6)
v_max3_u32 v248,v248,v52,v53
mvva 108,144,240
v_max3_u32 v248,v248,v54,v55
s_waitcnt vmcnt(5)
v_max3_u32 v248,v248,v56,v57
mvva 108,132,232
v_max3_u32 v248,v248,v58,v59
s_waitcnt vmcnt(4)
v_max3_u32 v248,v248,v60,v61
mvva 108,128,224
v_max3_u32 v248,v248,v62,v63
v_cmp_eq_u32_e32 vcc,-1,v248
s_waitcnt lgkmcnt(0)
mvva 124,136,224
s_cbranch_vccnz L_spx1o
L_sprx1o:
mvva 124,140,232
mvva 124,152,240
mvva 124,156,248
s_waitcnt vmcnt(3)
v_max_u32_e32 v248,v80,v81
mvva 120,156,216
v_max3_u32 v248,v248,v82,v83
s_waitcnt vmcnt(2)
v_max3_u32 v248,v248,v84,v85
mvva 120,152,208
v_max3_u32 v248,v248,v86,v87
s_waitcnt vmcnt(1)
v_max3_u32 v248,v248,v88,v89
mvva 120,140,200
v_max3_u32 v248,v248,v90,v91
s_waitcnt vmcnt(0)
v_max3_u32 v248,v248,v92,v93
mvva 120,136,192
v_max3_u32 v248,v248,v94,v95
v_cmp_eq_u32_e32 vcc,-1,v248
mvva 116,136,228
s_cbranch_vccnz L_spx2o
L_sprx2o:
mvva 116,140,236
mvva 116,152,244
mvva 116,156,252
mvva 112,156,220
mvva 112,152,212
mvva 112,140,204
mvva 112,136,196
mvvv 108,238,60
mvvv 108,238,92
mvvv 124,236,60
mvvv 124,236,92
mvvv 104,238,56
mvvv 104,238,88
mvvv 120,236,56
mvvv 120,236,88
mvvv 100,238,52
mvvv 100,238,84
cv 108,108,109
cv 109,110,111
cv 110,124,125
cv 111,126,127
cv 104,104,105
v_permlane16_swap_b32_e32 v108,v110
mvvv 116,236,52
v_permlane16_swap_b32_e32 v109,v111
cv 105,106,107
cv 106,120,121
cv 107,122,123
mvvv 116,236,84
buffer_store_dwordx4 v[108:111],v243,s[44:47],s73 offen sc0 sc1
v_permlane16_swap_b32_e32 v104,v106
v_permlane16_swap_b32_e32 v105,v107
mvvv 96,238,48
cv 100,100,101
cv 101,102,103
mvvv 96,238,80
buffer_store_dwordx4 v[104:107],v243,s[44:47],s72 offen sc0 sc1
cv 102,116,117
cv 103,118,119
mvvv 112,236,48
v_permlane16_swap_b32_e32 v100,v102
v_permlane16_swap_b32_e32 v101,v103
mvvv 112,236,80
buffer_store_dwordx4 v[100:103],v243,s[44:47],s71 offen sc0 sc1
cv 96,96,97
cv 97,98,99
s_nop 4
cv 98,112,113
cv 99,114,115
s_nop 0
v_permlane16_swap_b32_e32 v96,v98
v_permlane16_swap_b32_e32 v97,v99
buffer_store_dwordx4 v[96:99],v243,s[44:47],s70 offen sc0 sc1
L_oddx:
s_endpgm
L_k_ec1:
buffer_store_dwordx4 a[252:255],v244,s[8:11],s35 offen offset:1024 sc0 sc1
s_branch L_k_ecr1
L_q_ack:
s_mov_b32 s86,65536
L_q_ack2:
s_sleep 8
buffer_load_dword v246,off,s[8:11],s35 offset:1024 sc1
s_waitcnt vmcnt(0)
v_readfirstlane_b32 s87,v246
s_nop 0
s_cmp_eq_u32 s87,s32
s_cbranch_scc1 L_q_acked
s_sub_u32 s86,s86,1
s_cbranch_scc0 L_q_ack2
s_branch L_q_acked
L_q_spin:
s_mov_b32 s6,65536
L_q_spin1:
s_sleep 8
buffer_load_dword v248,v250,s[8:11],0 offen sc1
s_or_b32 s7,%1,s12
s_cbranch_scc1 L_q_spin2
buffer_load_dwordx4 a[252:255],v244,s[8:11],s35 offen sc1
s_waitcnt vmcnt(0)
buffer_store_dwordx4 a[252:255],v244,s[8:11],s35 offen offset:1024 sc0 sc1
L_q_spin2:
s_waitcnt vmcnt(0)
v_and_b32_e32 v248,v242,v248
v_cmp_ne_u32_e64 s[100:101],v243,v248
s_nop 1
s_cmp_eq_u64 s[100:101],0
s_cbranch_scc1 L_q_ok
s_sub_u32 s6,s6,1
s_cbranch_scc0 L_q_spin1
s_branch L_q_ok
L_bx:
s_mov_b32 s87,65536
L_bx1:
s_sleep 2
buffer_load_dwordx4 v[176:179],v244,s[8:11],s98 offen sc1
buffer_load_dwordx2 v[168:169],v253,s[8:11],s98 offen offset:1024 sc1
buffer_load_dwordx2 v[172:173],v253,s[8:11],s98 offen offset:1536 sc1
s_waitcnt vmcnt(0)
v_max3_u32 v180,v176,v177,v178
v_max3_u32 v180,v180,v179,v168
v_max3_u32 v180,v180,v169,v172
v_max_u32_e32 v180,v180,v173
v_cmp_eq_u32_e64 s[40:41],-1,v180
s_nop 1
s_cmp_eq_u64 s[40:41],0
s_cbranch_scc1 L_bxr
s_sub_u32 s87,s87,1
s_cbranch_scc0 L_bx1
s_branch L_bxr
L_pend2:
s_nop 7
v_add_f32_e32 v226,v152,v153
v_add_f32_e32 v227,v156,v157
v_subrev_u32_e32 v236,0x23800000,v226
v_subrev_u32_e32 v237,0x23800000,v227
v_max_u32_e32 v236,v236,v237
v_cmp_lt_u32_e64 s[40:41],s48,v236
v_rcp_f32_e32 v226,v226
v_rcp_f32_e32 v227,v227
s_add_u32 s86,s13,4096
v_mov_b32_e32 v236,0
v_mov_b32_e32 v237,0
v_mov_b32_e32 v238,0
v_mov_b32_e32 v239,0
v_mov_b64_e32 v[152:153],0
v_mov_b64_e32 v[156:157],0
v_mul_f32_e32 v226,v242,v226
v_mul_f32_e32 v227,v243,v227
v_add_u32_e32 v225,s94,v225
v_pk_mul_f32 v[160:161],v[160:161],v[226:227] op_sel:[0,0] op_sel_hi:[1,0]
v_pk_mul_f32 v[162:163],v[162:163],v[226:227] op_sel:[0,0] op_sel_hi:[1,0]
v_pk_mul_f32 v[164:165],v[164:165],v[226:227] op_sel:[0,0] op_sel_hi:[1,0]
v_pk_mul_f32 v[166:167],v[166:167],v[226:227] op_sel:[0,0] op_sel_hi:[1,0]
cv 160,160,161
cv 161,162,163
cv 162,164,165
cv 163,166,167
buffer_store_dwordx4 v[160:163],v244,s[8:11],s13 offen offset:0 sc0 sc1
v_pk_mul_f32 v[168:169],v[168:169],v[226:227] op_sel:[0,0] op_sel_hi:[1,0]
v_pk_mul_f32 v[170:171],v[170:171],v[226:227] op_sel:[0,0] op_sel_hi:[1,0]
v_pk_mul_f32 v[172:173],v[172:173],v[226:227] op_sel:[0,0] op_sel_hi:[1,0]
v_pk_mul_f32 v[174:175],v[174:175],v[226:227] op_sel:[0,0] op_sel_hi:[1,0]
cv 168,168,169
cv 169,170,171
cv 170,172,173
cv 171,174,175
buffer_store_dwordx4 v[168:171],v244,s[8:11],s13 offen offset:2048 sc0 sc1
v_pk_mul_f32 v[176:177],v[176:177],v[226:227] op_sel:[0,0] op_sel_hi:[1,0]
v_pk_mul_f32 v[178:179],v[178:179],v[226:227] op_sel:[0,0] op_sel_hi:[1,0]
v_pk_mul_f32 v[180:181],v[180:181],v[226:227] op_sel:[0,0] op_sel_hi:[1,0]
v_pk_mul_f32 v[182:183],v[182:183],v[226:227] op_sel:[0,0] op_sel_hi:[1,0]
cv 176,176,177
cv 177,178,179
cv 178,180,181
v_mfma_f32_32x32x16_bf16 v[160:175],v[236:239],v[236:239],0
cv 179,182,183
buffer_store_dwordx4 v[176:179],v244,s[8:11],s86 offen offset:0 sc0 sc1
v_pk_mul_f32 v[184:185],v[184:185],v[226:227] op_sel:[0,0] op_sel_hi:[1,0]
v_pk_mul_f32 v[186:187],v[186:187],v[226:227] op_sel:[0,0] op_sel_hi:[1,0]
v_pk_mul_f32 v[188:189],v[188:189],v[226:227] op_sel:[0,0] op_sel_hi:[1,0]
v_pk_mul_f32 v[190:191],v[190:191],v[226:227] op_sel:[0,0] op_sel_hi:[1,0]
cv 184,184,185
cv 185,186,187
cv 186,188,189
cv 187,190,191
buffer_store_dwordx4 v[184:187],v244,s[8:11],s86 offen offset:2048 sc0 sc1
v_pk_mul_f32 v[192:193],v[192:193],v[226:227] op_sel:[0,1] op_sel_hi:[1,1]
v_pk_mul_f32 v[194:195],v[194:195],v[226:227] op_sel:[0,1] op_sel_hi:[1,1]
v_pk_mul_f32 v[196:197],v[196:197],v[226:227] op_sel:[0,1] op_sel_hi:[1,1]
v_pk_mul_f32 v[198:199],v[198:199],v[226:227] op_sel:[0,1] op_sel_hi:[1,1]
cv 192,192,193
cv 193,194,195
cv 194,196,197
v_mfma_f32_32x32x16_bf16 v[176:191],v[236:239],v[236:239],0
cv 195,198,199
buffer_store_dwordx4 v[192:195],v244,s[8:11],s13 offen offset:1024 sc0 sc1
v_pk_mul_f32 v[200:201],v[200:201],v[226:227] op_sel:[0,1] op_sel_hi:[1,1]
v_pk_mul_f32 v[202:203],v[202:203],v[226:227] op_sel:[0,1] op_sel_hi:[1,1]
v_pk_mul_f32 v[204:205],v[204:205],v[226:227] op_sel:[0,1] op_sel_hi:[1,1]
v_pk_mul_f32 v[206:207],v[206:207],v[226:227] op_sel:[0,1] op_sel_hi:[1,1]
cv 200,200,201
cv 201,202,203
cv 202,204,205
cv 203,206,207
buffer_store_dwordx4 v[200:203],v244,s[8:11],s13 offen offset:3072 sc0 sc1
v_pk_mul_f32 v[208:209],v[208:209],v[226:227] op_sel:[0,1] op_sel_hi:[1,1]
v_pk_mul_f32 v[210:211],v[210:211],v[226:227] op_sel:[0,1] op_sel_hi:[1,1]
v_pk_mul_f32 v[212:213],v[212:213],v[226:227] op_sel:[0,1] op_sel_hi:[1,1]
v_pk_mul_f32 v[214:215],v[214:215],v[226:227] op_sel:[0,1] op_sel_hi:[1,1]
cv 208,208,209
cv 209,210,211
cv 210,212,213
v_mfma_f32_32x32x16_bf16 v[192:207],v[236:239],v[236:239],0
cv 211,214,215
buffer_store_dwordx4 v[208:211],v244,s[8:11],s86 offen offset:1024 sc0 sc1
v_pk_mul_f32 v[216:217],v[216:217],v[226:227] op_sel:[0,1] op_sel_hi:[1,1]
v_pk_mul_f32 v[218:219],v[218:219],v[226:227] op_sel:[0,1] op_sel_hi:[1,1]
v_pk_mul_f32 v[220:221],v[220:221],v[226:227] op_sel:[0,1] op_sel_hi:[1,1]
v_pk_mul_f32 v[222:223],v[222:223],v[226:227] op_sel:[0,1] op_sel_hi:[1,1]
cv 216,216,217
cv 217,218,219
cv 218,220,221
cv 219,222,223
buffer_store_dwordx4 v[216:219],v244,s[8:11],s86 offen offset:3072 sc0 sc1
s_nop 1
v_mfma_f32_32x32x16_bf16 v[208:223],v[236:239],v[236:239],0
s_nop 7
s_nop 1
s_cmp_eq_u32 s84,4
s_cbranch_scc1 L_ret_eb4
s_cmp_lg_u32 s80,s81
s_branch L_ret_eb5
L_redo:
s_waitcnt vmcnt(0) lgkmcnt(0)
s_nop 15
s_nop 3
s_load_dwordx2 s[76:77],%0,0
s_load_dwordx2 s[80:81],%0,16
s_mov_b32 s78,4194304
s_mov_b32 s79,131072
s_mov_b32 s82,8388608
s_mov_b32 s83,131072
s_mov_b32 s86,0
s_waitcnt lgkmcnt(0)
L_rd_t:
s_nop 15
s_nop 3
s_cmp_lt_u32 s86,2
s_cselect_b32 s88,s23,s22
s_cselect_b32 s95,s13,s14
s_and_b32 s89,s86,1
s_lshl_b32 s90,s89,10
s_add_u32 s95,s95,s90
s_lshl_b32 s89,s89,6
s_add_u32 s88,s88,s89
s_lshl_b32 s89,s12,4
s_add_u32 s88,s88,s89
v_bfe_u32 v213,v244,4,4
v_lshrrev_b32_e32 v214,8,v244
v_add_u32_e32 v210,s88,v213
v_lshlrev_b32_e32 v208,11,v210
v_lshl_add_u32 v208,v214,4,v208
v_lshlrev_b32_e32 v209,16,v214
v_lshl_add_u32 v209,v213,1,v209
s_lshl_b32 s89,s18,8
v_add_u32_e32 v209,s89,v209
v_lshlrev_b32_e32 v214,2,v214
v_sub_u32_e32 v210,v210,v214
v_accvgpr_write_b32 a192,0
v_accvgpr_write_b32 a193,0
v_accvgpr_write_b32 a194,0
v_accvgpr_write_b32 a195,0
v_accvgpr_write_b32 a196,0
v_accvgpr_write_b32 a197,0
v_accvgpr_write_b32 a198,0
v_accvgpr_write_b32 a199,0
v_accvgpr_write_b32 a200,0
v_accvgpr_write_b32 a201,0
v_accvgpr_write_b32 a202,0
v_accvgpr_write_b32 a203,0
v_accvgpr_write_b32 a204,0
v_accvgpr_write_b32 a205,0
v_accvgpr_write_b32 a206,0
v_accvgpr_write_b32 a207,0
v_accvgpr_write_b32 a208,0
v_accvgpr_write_b32 a209,0
v_accvgpr_write_b32 a210,0
v_accvgpr_write_b32 a211,0
v_accvgpr_write_b32 a212,0
v_accvgpr_write_b32 a213,0
v_accvgpr_write_b32 a214,0
v_accvgpr_write_b32 a215,0
v_accvgpr_write_b32 a216,0
v_accvgpr_write_b32 a217,0
v_accvgpr_write_b32 a218,0
v_accvgpr_write_b32 a219,0
v_accvgpr_write_b32 a220,0
v_accvgpr_write_b32 a221,0
v_accvgpr_write_b32 a222,0
v_accvgpr_write_b32 a223,0
s_mov_b32 s87,0
L_rd_q:
s_nop 15
s_nop 3
s_lshl_b32 s89,s87,6
s_lshl_b32 s90,s87,18
buffer_load_dwordx4 v[200:203],v208,s[76:79],s89 offen
buffer_load_ushort v160,v209,s[80:83],s90 offen offset:0
buffer_load_ushort v164,v209,s[80:83],s90 offen offset:32
buffer_load_ushort v168,v209,s[80:83],s90 offen offset:64
buffer_load_ushort v172,v209,s[80:83],s90 offen offset:96
)"
R"(buffer_load_ushort v176,v209,s[80:83],s90 offen offset:128
buffer_load_ushort v180,v209,s[80:83],s90 offen offset:160
buffer_load_ushort v184,v209,s[80:83],s90 offen offset:192
buffer_load_ushort v188,v209,s[80:83],s90 offen offset:224
s_add_u32 s90,s90,8192
buffer_load_ushort v192,v209,s[80:83],s90 offen offset:0
buffer_load_ushort v193,v209,s[80:83],s90 offen offset:32
buffer_load_ushort v194,v209,s[80:83],s90 offen offset:64
buffer_load_ushort v195,v209,s[80:83],s90 offen offset:96
buffer_load_ushort v196,v209,s[80:83],s90 offen offset:128
buffer_load_ushort v197,v209,s[80:83],s90 offen offset:160
buffer_load_ushort v198,v209,s[80:83],s90 offen offset:192
buffer_load_ushort v199,v209,s[80:83],s90 offen offset:224
s_add_u32 s90,s90,8192
s_waitcnt vmcnt(0)
v_lshl_or_b32 v160,v192,16,v160
v_lshl_or_b32 v164,v193,16,v164
v_lshl_or_b32 v168,v194,16,v168
v_lshl_or_b32 v172,v195,16,v172
v_lshl_or_b32 v176,v196,16,v176
v_lshl_or_b32 v180,v197,16,v180
v_lshl_or_b32 v184,v198,16,v184
v_lshl_or_b32 v188,v199,16,v188
buffer_load_ushort v161,v209,s[80:83],s90 offen offset:0
buffer_load_ushort v165,v209,s[80:83],s90 offen offset:32
buffer_load_ushort v169,v209,s[80:83],s90 offen offset:64
buffer_load_ushort v173,v209,s[80:83],s90 offen offset:96
buffer_load_ushort v177,v209,s[80:83],s90 offen offset:128
buffer_load_ushort v181,v209,s[80:83],s90 offen offset:160
buffer_load_ushort v185,v209,s[80:83],s90 offen offset:192
buffer_load_ushort v189,v209,s[80:83],s90 offen offset:224
s_add_u32 s90,s90,8192
buffer_load_ushort v192,v209,s[80:83],s90 offen offset:0
buffer_load_ushort v193,v209,s[80:83],s90 offen offset:32
buffer_load_ushort v194,v209,s[80:83],s90 offen offset:64
buffer_load_ushort v195,v209,s[80:83],s90 offen offset:96
buffer_load_ushort v196,v209,s[80:83],s90 offen offset:128
buffer_load_ushort v197,v209,s[80:83],s90 offen offset:160
buffer_load_ushort v198,v209,s[80:83],s90 offen offset:192
buffer_load_ushort v199,v209,s[80:83],s90 offen offset:224
s_add_u32 s90,s90,8192
s_waitcnt vmcnt(0)
v_lshl_or_b32 v161,v192,16,v161
v_lshl_or_b32 v165,v193,16,v165
v_lshl_or_b32 v169,v194,16,v169
v_lshl_or_b32 v173,v195,16,v173
v_lshl_or_b32 v177,v196,16,v177
v_lshl_or_b32 v181,v197,16,v181
v_lshl_or_b32 v185,v198,16,v185
v_lshl_or_b32 v189,v199,16,v189
buffer_load_ushort v162,v209,s[80:83],s90 offen offset:0
buffer_load_ushort v166,v209,s[80:83],s90 offen offset:32
buffer_load_ushort v170,v209,s[80:83],s90 offen offset:64
buffer_load_ushort v174,v209,s[80:83],s90 offen offset:96
buffer_load_ushort v178,v209,s[80:83],s90 offen offset:128
buffer_load_ushort v182,v209,s[80:83],s90 offen offset:160
buffer_load_ushort v186,v209,s[80:83],s90 offen offset:192
buffer_load_ushort v190,v209,s[80:83],s90 offen offset:224
s_add_u32 s90,s90,8192
buffer_load_ushort v192,v209,s[80:83],s90 offen offset:0
buffer_load_ushort v193,v209,s[80:83],s90 offen offset:32
buffer_load_ushort v194,v209,s[80:83],s90 offen offset:64
buffer_load_ushort v195,v209,s[80:83],s90 offen offset:96
buffer_load_ushort v196,v209,s[80:83],s90 offen offset:128
buffer_load_ushort v197,v209,s[80:83],s90 offen offset:160
buffer_load_ushort v198,v209,s[80:83],s90 offen offset:192
buffer_load_ushort v199,v209,s[80:83],s90 offen offset:224
s_add_u32 s90,s90,8192
s_waitcnt vmcnt(0)
v_lshl_or_b32 v162,v192,16,v162
v_lshl_or_b32 v166,v193,16,v166
v_lshl_or_b32 v170,v194,16,v170
v_lshl_or_b32 v174,v195,16,v174
v_lshl_or_b32 v178,v196,16,v178
v_lshl_or_b32 v182,v197,16,v182
v_lshl_or_b32 v186,v198,16,v186
v_lshl_or_b32 v190,v199,16,v190
buffer_load_ushort v163,v209,s[80:83],s90 offen offset:0
buffer_load_ushort v167,v209,s[80:83],s90 offen offset:32
buffer_load_ushort v171,v209,s[80:83],s90 offen offset:64
buffer_load_ushort v175,v209,s[80:83],s90 offen offset:96
buffer_load_ushort v179,v209,s[80:83],s90 offen offset:128
buffer_load_ushort v183,v209,s[80:83],s90 offen offset:160
buffer_load_ushort v187,v209,s[80:83],s90 offen offset:192
buffer_load_ushort v191,v209,s[80:83],s90 offen offset:224
s_add_u32 s90,s90,8192
buffer_load_ushort v192,v209,s[80:83],s90 offen offset:0
buffer_load_ushort v193,v209,s[80:83],s90 offen offset:32
buffer_load_ushort v194,v209,s[80:83],s90 offen offset:64
buffer_load_ushort v195,v209,s[80:83],s90 offen offset:96
buffer_load_ushort v196,v209,s[80:83],s90 offen offset:128
buffer_load_ushort v197,v209,s[80:83],s90 offen offset:160
buffer_load_ushort v198,v209,s[80:83],s90 offen offset:192
buffer_load_ushort v199,v209,s[80:83],s90 offen offset:224
s_add_u32 s90,s90,8192
s_waitcnt vmcnt(0)
v_lshl_or_b32 v163,v192,16,v163
v_lshl_or_b32 v167,v193,16,v167
v_lshl_or_b32 v171,v194,16,v171
v_lshl_or_b32 v175,v195,16,v175
v_lshl_or_b32 v179,v196,16,v179
v_lshl_or_b32 v183,v197,16,v183
v_lshl_or_b32 v187,v198,16,v187
v_lshl_or_b32 v191,v199,16,v191
mavv 192,160,200
mavv 196,164,200
mavv 200,168,200
mavv 204,172,200
mavv 208,176,200
mavv 212,180,200
mavv 216,184,200
mavv 220,188,200
s_add_u32 s87,s87,1
s_cmp_lt_u32 s87,32
s_cbranch_scc1 L_rd_q
s_nop 15
s_nop 3
v_accvgpr_read_b32 v192,a192
v_accvgpr_read_b32 v193,a193
v_accvgpr_read_b32 v194,a194
v_accvgpr_read_b32 v195,a195
v_accvgpr_read_b32 v196,a196
v_accvgpr_read_b32 v197,a197
v_accvgpr_read_b32 v198,a198
v_accvgpr_read_b32 v199,a199
v_mul_f32_e32 v192,0x3e0293ee,v192
v_mul_f32_e32 v193,0x3e0293ee,v193
v_mul_f32_e32 v194,0x3e0293ee,v194
v_mul_f32_e32 v195,0x3e0293ee,v195
v_mul_f32_e32 v196,0x3e0293ee,v196
v_mul_f32_e32 v197,0x3e0293ee,v197
v_mul_f32_e32 v198,0x3e0293ee,v198
v_mul_f32_e32 v199,0x3e0293ee,v199
cv 192,192,193
cv 193,194,195
cv 194,196,197
cv 195,198,199
v_accvgpr_write_b32 a224,v192
v_accvgpr_write_b32 a225,v193
v_accvgpr_write_b32 a226,v194
v_accvgpr_write_b32 a227,v195
v_accvgpr_read_b32 v192,a200
v_accvgpr_read_b32 v193,a201
v_accvgpr_read_b32 v194,a202
v_accvgpr_read_b32 v195,a203
v_accvgpr_read_b32 v196,a204
v_accvgpr_read_b32 v197,a205
v_accvgpr_read_b32 v198,a206
v_accvgpr_read_b32 v199,a207
v_mul_f32_e32 v192,0x3e0293ee,v192
v_mul_f32_e32 v193,0x3e0293ee,v193
v_mul_f32_e32 v194,0x3e0293ee,v194
v_mul_f32_e32 v195,0x3e0293ee,v195
v_mul_f32_e32 v196,0x3e0293ee,v196
v_mul_f32_e32 v197,0x3e0293ee,v197
v_mul_f32_e32 v198,0x3e0293ee,v198
v_mul_f32_e32 v199,0x3e0293ee,v199
cv 192,192,193
cv 193,194,195
cv 194,196,197
cv 195,198,199
v_accvgpr_write_b32 a228,v192
v_accvgpr_write_b32 a229,v193
v_accvgpr_write_b32 a230,v194
v_accvgpr_write_b32 a231,v195
v_accvgpr_read_b32 v192,a208
v_accvgpr_read_b32 v193,a209
v_accvgpr_read_b32 v194,a210
v_accvgpr_read_b32 v195,a211
v_accvgpr_read_b32 v196,a212
v_accvgpr_read_b32 v197,a213
v_accvgpr_read_b32 v198,a214
v_accvgpr_read_b32 v199,a215
v_mul_f32_e32 v192,0x3e0293ee,v192
v_mul_f32_e32 v193,0x3e0293ee,v193
v_mul_f32_e32 v194,0x3e0293ee,v194
v_mul_f32_e32 v195,0x3e0293ee,v195
v_mul_f32_e32 v196,0x3e0293ee,v196
v_mul_f32_e32 v197,0x3e0293ee,v197
v_mul_f32_e32 v198,0x3e0293ee,v198
v_mul_f32_e32 v199,0x3e0293ee,v199
cv 192,192,193
cv 193,194,195
cv 194,196,197
cv 195,198,199
v_accvgpr_write_b32 a232,v192
v_accvgpr_write_b32 a233,v193
v_accvgpr_write_b32 a234,v194
v_accvgpr_write_b32 a235,v195
v_accvgpr_read_b32 v192,a216
v_accvgpr_read_b32 v193,a217
v_accvgpr_read_b32 v194,a218
v_accvgpr_read_b32 v195,a219
v_accvgpr_read_b32 v196,a220
v_accvgpr_read_b32 v197,a221
v_accvgpr_read_b32 v198,a222
v_accvgpr_read_b32 v199,a223
v_mul_f32_e32 v192,0x3e0293ee,v192
v_mul_f32_e32 v193,0x3e0293ee,v193
v_mul_f32_e32 v194,0x3e0293ee,v194
v_mul_f32_e32 v195,0x3e0293ee,v195
v_mul_f32_e32 v196,0x3e0293ee,v196
v_mul_f32_e32 v197,0x3e0293ee,v197
v_mul_f32_e32 v198,0x3e0293ee,v198
v_mul_f32_e32 v199,0x3e0293ee,v199
cv 192,192,193
cv 193,194,195
cv 194,196,197
cv 195,198,199
v_accvgpr_write_b32 a236,v192
v_accvgpr_write_b32 a237,v193
v_accvgpr_write_b32 a238,v194
v_accvgpr_write_b32 a239,v195
v_mov_b32_e32 v160,0
v_mov_b32_e32 v161,0
v_mov_b32_e32 v162,0
v_mov_b32_e32 v163,0
v_mov_b32_e32 v164,0
v_mov_b32_e32 v165,0
v_mov_b32_e32 v166,0
v_mov_b32_e32 v167,0
v_mov_b32_e32 v168,0
v_mov_b32_e32 v169,0
v_mov_b32_e32 v170,0
v_mov_b32_e32 v171,0
v_mov_b32_e32 v172,0
v_mov_b32_e32 v173,0
v_mov_b32_e32 v174,0
v_mov_b32_e32 v175,0
v_mov_b32_e32 v176,0
v_mov_b32_e32 v177,0
v_mov_b32_e32 v178,0
v_mov_b32_e32 v179,0
v_mov_b32_e32 v180,0
v_mov_b32_e32 v181,0
v_mov_b32_e32 v182,0
v_mov_b32_e32 v183,0
v_mov_b32_e32 v184,0
v_mov_b32_e32 v185,0
v_mov_b32_e32 v186,0
v_mov_b32_e32 v187,0
v_mov_b32_e32 v188,0
v_mov_b32_e32 v189,0
v_mov_b32_e32 v190,0
v_mov_b32_e32 v191,0
v_mov_b32_e32 v216,0xf149f2ca
v_mov_b32_e32 v211,v216
v_mov_b32_e32 v212,0
s_lshr_b32 s91,s88,5
s_mov_b32 s87,0
L_rd_a:
s_nop 15
s_nop 3
s_mul_i32 s89,s18,524288
s_lshl_b32 s90,s87,13
s_add_u32 s89,s89,s90
s_add_u32 s90,s89,16777216
la 64,244,90,0
la 68,244,90,1024
la 72,244,90,2048
la 76,244,90,3072
s_add_u32 s90,s89,16781312
la 80,244,90,0
la 84,244,90,1024
la 88,244,90,2048
la 92,244,90,3072
s_add_u32 s90,s89,33554432
la 96,244,90,0
la 100,244,90,1024
la 104,244,90,2048
la 108,244,90,3072
s_add_u32 s90,s89,33558528
la 112,244,90,0
la 116,244,90,1024
la 120,244,90,2048
la 124,244,90,3072
s_waitcnt vmcnt(0)
zvaa 192,64,224
mvaa 192,72,228
mvaa 192,80,232
mvaa 192,88,236
zvaa 196,68,224
mvaa 196,76,228
mvaa 196,84,232
mvaa 196,92,236
v_cmp_gt_i32_e32 vcc,0,v210
s_nop 2
v_cndmask_b32_e32 v192,v192,v216,vcc
v_cmp_gt_i32_e32 vcc,1,v210
s_nop 1
v_cndmask_b32_e32 v193,v193,v216,vcc
v_cmp_gt_i32_e32 vcc,2,v210
s_nop 1
v_cndmask_b32_e32 v194,v194,v216,vcc
v_cmp_gt_i32_e32 vcc,3,v210
s_nop 1
v_cndmask_b32_e32 v195,v195,v216,vcc
v_cmp_gt_i32_e32 vcc,16,v210
s_nop 1
v_cndmask_b32_e32 v196,v196,v216,vcc
v_cmp_gt_i32_e32 vcc,17,v210
s_nop 1
v_cndmask_b32_e32 v197,v197,v216,vcc
v_cmp_gt_i32_e32 vcc,18,v210
s_nop 1
v_cndmask_b32_e32 v198,v198,v216,vcc
v_cmp_gt_i32_e32 vcc,19,v210
s_nop 1
v_cndmask_b32_e32 v199,v199,v216,vcc
v_max3_f32 v213,v192,v193,v194
v_max3_f32 v214,v195,v196,v197
v_max3_f32 v213,v213,v198,v199
v_max_f32_e32 v213,v213,v214
v_mov_b32_e32 v214,v213
s_nop 1
v_permlane16_swap_b32_e32 v213,v214
v_max_f32_e32 v213,v213,v214
v_mov_b32_e32 v214,v213
s_nop 1
v_permlane32_swap_b32_e32 v213,v214
v_max_f32_e32 v213,v213,v214
v_max_f32_e32 v213,v213,v211
v_sub_f32_e32 v215,v211,v213
ex 215
v_mov_b32_e32 v211,v213
v_sub_f32_e32 v192,v192,v211
v_sub_f32_e32 v193,v193,v211
v_sub_f32_e32 v194,v194,v211
v_sub_f32_e32 v195,v195,v211
v_sub_f32_e32 v196,v196,v211
v_sub_f32_e32 v197,v197,v211
v_sub_f32_e32 v198,v198,v211
v_sub_f32_e32 v199,v199,v211
ex 192
ex 193
ex 194
ex 195
ex 196
ex 197
ex 198
ex 199
v_mul_f32_e32 v212,v215,v212
v_add_f32_e32 v212,v212,v192
v_add_f32_e32 v212,v212,v193
v_add_f32_e32 v212,v212,v194
v_add_f32_e32 v212,v212,v195
v_add_f32_e32 v212,v212,v196
v_add_f32_e32 v212,v212,v197
v_add_f32_e32 v212,v212,v198
v_add_f32_e32 v212,v212,v199
cv 192,192,193
cv 193,194,195
cv 194,196,197
cv 195,198,199
v_mul_f32_e32 v160,v215,v160
v_mul_f32_e32 v161,v215,v161
v_mul_f32_e32 v162,v215,v162
v_mul_f32_e32 v163,v215,v163
v_mul_f32_e32 v164,v215,v164
v_mul_f32_e32 v165,v215,v165
v_mul_f32_e32 v166,v215,v166
v_mul_f32_e32 v167,v215,v167
v_mul_f32_e32 v168,v215,v168
v_mul_f32_e32 v169,v215,v169
v_mul_f32_e32 v170,v215,v170
v_mul_f32_e32 v171,v215,v171
v_mul_f32_e32 v172,v215,v172
v_mul_f32_e32 v173,v215,v173
v_mul_f32_e32 v174,v215,v174
v_mul_f32_e32 v175,v215,v175
v_mul_f32_e32 v176,v215,v176
v_mul_f32_e32 v177,v215,v177
v_mul_f32_e32 v178,v215,v178
v_mul_f32_e32 v179,v215,v179
v_mul_f32_e32 v180,v215,v180
v_mul_f32_e32 v181,v215,v181
v_mul_f32_e32 v182,v215,v182
v_mul_f32_e32 v183,v215,v183
v_mul_f32_e32 v184,v215,v184
v_mul_f32_e32 v185,v215,v185
v_mul_f32_e32 v186,v215,v186
v_mul_f32_e32 v187,v215,v187
v_mul_f32_e32 v188,v215,v188
v_mul_f32_e32 v189,v215,v189
v_mul_f32_e32 v190,v215,v190
v_mul_f32_e32 v191,v215,v191
mvav 160,96,192
mvav 164,100,192
mvav 168,104,192
mvav 172,108,192
mvav 176,112,192
mvav 180,116,192
mvav 184,120,192
mvav 188,124,192
v_subrev_u32_e32 v210,32,v210
s_add_u32 s87,s87,1
s_cmp_le_u32 s87,s91
s_cbranch_scc1 L_rd_a
s_nop 15
s_nop 3
s_lshl_b32 s89,s88,2
s_add_u32 s89,s89,83886080
v_bfe_u32 v213,v244,2,6
buffer_load_dword v215,v213,s[8:11],s89 offen sc1
v_mov_b32_e32 v214,v212
s_nop 1
v_permlane16_swap_b32_e32 v212,v214
v_add_f32_e32 v212,v212,v214
v_mov_b32_e32 v214,v212
s_nop 1
v_permlane32_swap_b32_e32 v212,v214
v_add_f32_e32 v212,v212,v214
v_rcp_f32_e32 v212,v212
s_waitcnt vmcnt(0)
v_mul_f32_e32 v215,v215,v212
v_mul_f32_e32 v160,v215,v160
v_mul_f32_e32 v161,v215,v161
v_mul_f32_e32 v162,v215,v162
v_mul_f32_e32 v163,v215,v163
v_mul_f32_e32 v164,v215,v164
v_mul_f32_e32 v165,v215,v165
v_mul_f32_e32 v166,v215,v166
v_mul_f32_e32 v167,v215,v167
v_mul_f32_e32 v168,v215,v168
v_mul_f32_e32 v169,v215,v169
v_mul_f32_e32 v170,v215,v170
v_mul_f32_e32 v171,v215,v171
v_mul_f32_e32 v172,v215,v172
v_mul_f32_e32 v173,v215,v173
v_mul_f32_e32 v174,v215,v174
v_mul_f32_e32 v175,v215,v175
v_mul_f32_e32 v176,v215,v176
v_mul_f32_e32 v177,v215,v177
v_mul_f32_e32 v178,v215,v178
v_mul_f32_e32 v179,v215,v179
v_mul_f32_e32 v180,v215,v180
v_mul_f32_e32 v181,v215,v181
v_mul_f32_e32 v182,v215,v182
v_mul_f32_e32 v183,v215,v183
v_mul_f32_e32 v184,v215,v184
v_mul_f32_e32 v185,v215,v185
v_mul_f32_e32 v186,v215,v186
v_mul_f32_e32 v187,v215,v187
v_mul_f32_e32 v188,v215,v188
v_mul_f32_e32 v189,v215,v189
v_mul_f32_e32 v190,v215,v190
v_mul_f32_e32 v191,v215,v191
s_add_u32 s89,s95,4096
cv 160,160,161
cv 161,162,163
cv 162,164,165
cv 163,166,167
buffer_store_dwordx4 v[160:163],v244,s[8:11],s95 offen offset:0 sc0 sc1
cv 168,168,169
cv 169,170,171
cv 170,172,173
cv 171,174,175
buffer_store_dwordx4 v[168:171],v244,s[8:11],s95 offen offset:2048 sc0 sc1
cv 176,176,177
cv 177,178,179
cv 178,180,181
cv 179,182,183
buffer_store_dwordx4 v[176:179],v244,s[8:11],s89 offen offset:0 sc0 sc1
cv 184,184,185
cv 185,186,187
cv 186,188,189
cv 187,190,191
buffer_store_dwordx4 v[184:187],v244,s[8:11],s89 offen offset:2048 sc0 sc1
s_waitcnt vmcnt(0)
v_mov_b32_e32 v192,v160
v_accvgpr_read_b32 v160,a240
v_accvgpr_write_b32 a240,v192
v_mov_b32_e32 v193,v161
v_accvgpr_read_b32 v161,a241
v_accvgpr_write_b32 a241,v193
v_mov_b32_e32 v194,v162
v_accvgpr_read_b32 v162,a242
v_accvgpr_write_b32 a242,v194
v_mov_b32_e32 v195,v163
v_accvgpr_read_b32 v163,a243
v_accvgpr_write_b32 a243,v195
v_mov_b32_e32 v200,v168
v_accvgpr_read_b32 v168,a244
v_accvgpr_write_b32 a244,v200
v_mov_b32_e32 v201,v169
v_accvgpr_read_b32 v169,a245
v_accvgpr_write_b32 a245,v201
v_mov_b32_e32 v202,v170
v_accvgpr_read_b32 v170,a246
v_accvgpr_write_b32 a246,v202
v_mov_b32_e32 v203,v171
v_accvgpr_read_b32 v171,a247
v_accvgpr_write_b32 a247,v203
v_mov_b32_e32 v208,v176
v_accvgpr_read_b32 v176,a248
v_accvgpr_write_b32 a248,v208
v_mov_b32_e32 v209,v177
v_accvgpr_read_b32 v177,a249
v_accvgpr_write_b32 a249,v209
v_mov_b32_e32 v210,v178
v_accvgpr_read_b32 v178,a250
v_accvgpr_write_b32 a250,v210
v_mov_b32_e32 v211,v179
v_accvgpr_read_b32 v179,a251
v_accvgpr_write_b32 a251,v211
v_mov_b32_e32 v216,v184
v_accvgpr_read_b32 v184,a252
v_accvgpr_write_b32 a252,v216
v_mov_b32_e32 v217,v185
v_accvgpr_read_b32 v185,a253
v_accvgpr_write_b32 a253,v217
v_mov_b32_e32 v218,v186
v_accvgpr_read_b32 v186,a254
v_accvgpr_write_b32 a254,v218
v_mov_b32_e32 v219,v187
v_accvgpr_read_b32 v187,a255
v_accvgpr_write_b32 a255,v219
s_add_u32 s86,s86,1
s_cmp_lt_u32 s86,4
s_cbranch_scc1 L_rd_t
s_nop 15
s_nop 3
s_nop 1
s_cmp_eq_u32 s84,4
s_cbranch_scc1 L_redo_r4
s_cmp_lg_u32 s80,s81
s_branch L_redo_r5
L_k_ev:
L_v_ev:
buffer_load_dwordx4 a[252:255],v244,s[8:11],s35 offen sc1
s_branch L_k_evr
L_spbe:
s_mov_b32 s91,65536
L_splbe:
s_sleep 2
buffer_load_dword v253,v224,s[8:11],s53 offen sc1
s_waitcnt vmcnt(0)
v_cmp_ne_u32_e32 vcc,4,v253
s_nop 1
s_cbranch_vccz L_sprbe
s_sub_u32 s91,s91,1
s_cbranch_scc0 L_splbe
s_branch L_sprbe
L_spa3e:
s_mov_b32 s91,65536
L_spla3e:
s_sleep 2
buffer_load_dword v253,v242,s[8:11],s53 offen sc1
s_waitcnt vmcnt(0)
v_cmp_ne_u32_e32 vcc,4,v253
s_nop 1
s_cbranch_vccz L_spra3e
s_sub_u32 s91,s91,1
s_cbranch_scc0 L_spla3e
s_branch L_spra3e
L_spa4e:
s_mov_b32 s91,65536
L_spla4e:
s_sleep 2
buffer_load_dword v253,v242,s[8:11],s53 offen sc1
s_waitcnt vmcnt(0)
v_cmp_ne_u32_e32 vcc,4,v253
s_nop 1
s_cbranch_vccz L_spra4e
s_sub_u32 s91,s91,1
s_cbranch_scc0 L_spla4e
s_branch L_spra4e
L_spa5e:
s_mov_b32 s91,65536
L_spla5e:
s_sleep 2
buffer_load_dword v253,v242,s[8:11],s53 offen sc1
s_waitcnt vmcnt(0)
v_cmp_ne_u32_e32 vcc,4,v253
s_nop 1
s_cbranch_vccz L_spra5e
s_sub_u32 s91,s91,1
s_cbranch_scc0 L_spla5e
s_branch L_spra5e
L_spa6e:
s_mov_b32 s91,65536
L_spla6e:
s_sleep 2
buffer_load_dword v253,v242,s[8:11],s53 offen sc1
s_waitcnt vmcnt(0)
v_cmp_ne_u32_e32 vcc,4,v253
s_nop 1
s_cbranch_vccz L_spra6e
s_sub_u32 s91,s91,1
s_cbranch_scc0 L_spla6e
s_branch L_spra6e
L_spa7e:
s_mov_b32 s91,65536
L_spla7e:
s_sleep 2
buffer_load_dword v253,v242,s[8:11],s53 offen sc1
s_waitcnt vmcnt(0)
v_cmp_ne_u32_e32 vcc,4,v253
s_nop 1
s_cbranch_vccz L_spra7e
s_sub_u32 s91,s91,1
s_cbranch_scc0 L_spla7e
s_branch L_spra7e
L_spa8e:
s_mov_b32 s91,65536
L_spla8e:
s_sleep 2
buffer_load_dword v253,v242,s[8:11],s53 offen sc1
s_waitcnt vmcnt(0)
v_cmp_ne_u32_e32 vcc,4,v253
s_nop 1
s_cbranch_vccz L_spra8e
s_sub_u32 s91,s91,1
s_cbranch_scc0 L_spla8e
s_branch L_spra8e
L_spa9e:
s_mov_b32 s91,65536
L_spla9e:
s_sleep 2
buffer_load_dword v253,v242,s[8:11],s53 offen sc1
s_waitcnt vmcnt(0)
v_cmp_ne_u32_e32 vcc,4,v253
s_nop 1
s_cbranch_vccz L_spra9e
s_sub_u32 s91,s91,1
s_cbranch_scc0 L_spla9e
s_branch L_spra9e
L_spa10e:
s_mov_b32 s91,65536
L_spla10e:
s_sleep 2
buffer_load_dword v253,v242,s[8:11],s53 offen sc1
s_waitcnt vmcnt(0)
v_cmp_ne_u32_e32 vcc,4,v253
s_nop 1
s_cbranch_vccz L_spra10e
s_sub_u32 s91,s91,1
s_cbranch_scc0 L_spla10e
s_branch L_spra10e
L_sppe:
s_mov_b32 s91,65536
L_splpe:
s_sleep 2
buffer_load_dword v253,v242,s[8:11],s53 offen sc1
s_waitcnt vmcnt(0)
v_cmp_ne_u32_e32 vcc,4,v253
s_nop 1
s_cbranch_vccz L_sprpe
s_sub_u32 s91,s91,1
s_cbranch_scc0 L_splpe
s_branch L_sprpe
L_spx0e:
s_mov_b32 s91,65536
L_splx0e:
lv 16,244,82,0
lv 20,244,82,1024
lv 24,244,82,2048
lv 28,244,82,3072
s_waitcnt vmcnt(0)
v_max_u32_e32 v248,v16,v17
v_max3_u32 v248,v248,v18,v19
v_max3_u32 v248,v248,v20,v21
v_max3_u32 v248,v248,v22,v23
v_max3_u32 v248,v248,v24,v25
v_max3_u32 v248,v248,v26,v27
v_max3_u32 v248,v248,v28,v29
v_max3_u32 v248,v248,v30,v31
v_cmp_eq_u32_e32 vcc,-1,v248
s_nop 1
s_cbranch_vccz L_sprx0e
s_sub_u32 s91,s91,1
s_cbranch_scc0 L_splx0e
s_branch L_sprx0e
L_spx1e:
s_mov_b32 s91,65536
L_splx1e:
lv 48,244,83,0
lv 52,244,83,1024
lv 56,244,83,2048
lv 60,244,83,3072
s_waitcnt vmcnt(0)
v_max_u32_e32 v248,v48,v49
v_max3_u32 v248,v248,v50,v51
v_max3_u32 v248,v248,v52,v53
v_max3_u32 v248,v248,v54,v55
v_max3_u32 v248,v248,v56,v57
v_max3_u32 v248,v248,v58,v59
v_max3_u32 v248,v248,v60,v61
v_max3_u32 v248,v248,v62,v63
v_cmp_eq_u32_e32 vcc,-1,v248
s_nop 1
s_cbranch_vccz L_sprx1e
s_sub_u32 s91,s91,1
s_cbranch_scc0 L_splx1e
s_branch L_sprx1e
L_spx2e:
s_mov_b32 s91,65536
L_splx2e:
lv 80,244,84,0
lv 84,244,84,1024
lv 88,244,84,2048
lv 92,244,84,3072
s_waitcnt vmcnt(0)
v_max_u32_e32 v248,v80,v81
v_max3_u32 v248,v248,v82,v83
v_max3_u32 v248,v248,v84,v85
v_max3_u32 v248,v248,v86,v87
v_max3_u32 v248,v248,v88,v89
v_max3_u32 v248,v248,v90,v91
v_max3_u32 v248,v248,v92,v93
v_max3_u32 v248,v248,v94,v95
v_cmp_eq_u32_e32 vcc,-1,v248
s_nop 1
s_cbranch_vccz L_sprx2e
s_sub_u32 s91,s91,1
s_cbranch_scc0 L_splx2e
s_branch L_sprx2e
L_spbo:
s_mov_b32 s91,65536
L_splbo:
s_sleep 2
buffer_load_dword v253,v224,s[8:11],s53 offen sc1
s_waitcnt vmcnt(0)
v_cmp_ne_u32_e32 vcc,4,v253
s_nop 1
s_cbranch_vccz L_sprbo
s_sub_u32 s91,s91,1
s_cbranch_scc0 L_splbo
s_branch L_sprbo
L_spa3o:
s_mov_b32 s91,65536
L_spla3o:
s_sleep 2
buffer_load_dword v253,v242,s[8:11],s53 offen sc1
s_waitcnt vmcnt(0)
v_cmp_ne_u32_e32 vcc,4,v253
s_nop 1
s_cbranch_vccz L_spra3o
s_sub_u32 s91,s91,1
s_cbranch_scc0 L_spla3o
s_branch L_spra3o
L_spa4o:
s_mov_b32 s91,65536
L_spla4o:
s_sleep 2
buffer_load_dword v253,v242,s[8:11],s53 offen sc1
s_waitcnt vmcnt(0)
v_cmp_ne_u32_e32 vcc,4,v253
s_nop 1
s_cbranch_vccz L_spra4o
s_sub_u32 s91,s91,1
s_cbranch_scc0 L_spla4o
s_branch L_spra4o
L_spa5o:
s_mov_b32 s91,65536
L_spla5o:
s_sleep 2
buffer_load_dword v253,v242,s[8:11],s53 offen sc1
s_waitcnt vmcnt(0)
v_cmp_ne_u32_e32 vcc,4,v253
s_nop 1
s_cbranch_vccz L_spra5o
s_sub_u32 s91,s91,1
s_cbranch_scc0 L_spla5o
s_branch L_spra5o
L_spa6o:
s_mov_b32 s91,65536
L_spla6o:
s_sleep 2
buffer_load_dword v253,v242,s[8:11],s53 offen sc1
s_waitcnt vmcnt(0)
v_cmp_ne_u32_e32 vcc,4,v253
s_nop 1
s_cbranch_vccz L_spra6o
s_sub_u32 s91,s91,1
s_cbranch_scc0 L_spla6o
s_branch L_spra6o
L_spa7o:
s_mov_b32 s91,65536
L_spla7o:
s_sleep 2
buffer_load_dword v253,v242,s[8:11],s53 offen sc1
s_waitcnt vmcnt(0)
v_cmp_ne_u32_e32 vcc,4,v253
s_nop 1
s_cbranch_vccz L_spra7o
s_sub_u32 s91,s91,1
s_cbranch_scc0 L_spla7o
s_branch L_spra7o
L_spa8o:
s_mov_b32 s91,65536
L_spla8o:
s_sleep 2
buffer_load_dword v253,v242,s[8:11],s53 offen sc1
s_waitcnt vmcnt(0)
v_cmp_ne_u32_e32 vcc,4,v253
s_nop 1
s_cbranch_vccz L_spra8o
s_sub_u32 s91,s91,1
s_cbranch_scc0 L_spla8o
s_branch L_spra8o
L_sppo:
s_mov_b32 s91,65536
L_splpo:
s_sleep 2
buffer_load_dword v253,v242,s[8:11],s53 offen sc1
s_waitcnt vmcnt(0)
v_cmp_ne_u32_e32 vcc,4,v253
s_nop 1
s_cbranch_vccz L_sprpo
s_sub_u32 s91,s91,1
s_cbranch_scc0 L_splpo
s_branch L_sprpo
L_spx0o:
s_mov_b32 s91,65536
L_splx0o:
lv 16,244,82,0
lv 20,244,82,1024
lv 24,244,82,2048
lv 28,244,82,3072
s_waitcnt vmcnt(0)
v_max_u32_e32 v248,v16,v17
v_max3_u32 v248,v248,v18,v19
v_max3_u32 v248,v248,v20,v21
v_max3_u32 v248,v248,v22,v23
v_max3_u32 v248,v248,v24,v25
v_max3_u32 v248,v248,v26,v27
v_max3_u32 v248,v248,v28,v29
v_max3_u32 v248,v248,v30,v31
v_cmp_eq_u32_e32 vcc,-1,v248
s_nop 1
s_cbranch_vccz L_sprx0o
s_sub_u32 s91,s91,1
s_cbranch_scc0 L_splx0o
s_branch L_sprx0o
L_spx1o:
s_mov_b32 s91,65536
L_splx1o:
lv 48,244,83,0
lv 52,244,83,1024
lv 56,244,83,2048
lv 60,244,83,3072
s_waitcnt vmcnt(0)
v_max_u32_e32 v248,v48,v49
v_max3_u32 v248,v248,v50,v51
v_max3_u32 v248,v248,v52,v53
v_max3_u32 v248,v248,v54,v55
v_max3_u32 v248,v248,v56,v57
v_max3_u32 v248,v248,v58,v59
v_max3_u32 v248,v248,v60,v61
v_max3_u32 v248,v248,v62,v63
v_cmp_eq_u32_e32 vcc,-1,v248
s_nop 1
s_cbranch_vccz L_sprx1o
s_sub_u32 s91,s91,1
s_cbranch_scc0 L_splx1o
s_branch L_sprx1o
L_spx2o:
s_mov_b32 s91,65536
L_splx2o:
lv 80,244,84,0
lv 84,244,84,1024
lv 88,244,84,2048
lv 92,244,84,3072
s_waitcnt vmcnt(0)
v_max_u32_e32 v248,v80,v81
v_max3_u32 v248,v248,v82,v83
v_max3_u32 v248,v248,v84,v85
v_max3_u32 v248,v248,v86,v87
v_max3_u32 v248,v248,v88,v89
v_max3_u32 v248,v248,v90,v91
v_max3_u32 v248,v248,v92,v93
v_max3_u32 v248,v248,v94,v95
v_cmp_eq_u32_e32 vcc,-1,v248
s_nop 1
s_cbranch_vccz L_sprx2o
s_sub_u32 s91,s91,1
s_cbranch_scc0 L_splx2o
s_branch L_sprx2o
L_att_end:
s_endpgm
.p2align 12
.purgem cv
.purgem cvaa
.purgem cvvv
.purgem dl
.purgem ds
.purgem ex
.purgem la
.purgem lv
.purgem maaa
.purgem maav
.purgem mavv
.purgem mvaa
.purgem mvav
.purgem mvva
.purgem mvvv
.purgem ra
.purgem rv
.purgem ta
.purgem tv
.purgem zaaa
.purgem zaav
.purgem zvaa
.purgem zvav
.purgem zvva
.purgem zvvv
)"
        :: "s"(__builtin_amdgcn_kernarg_segment_ptr()), "s"(blockIdx.x), "v"(tid)
        : "v0", "v1", "v2", "v3", "v4", "v5", "v6", "v7", "v8", "v9", "v10", "v11", "v12", "v13", "v14", "v15", "v16", "v17", "v18", "v19", "v20", "v21", "v22", "v23", "v24", "v25", "v26", "v27", "v28", "v29", "v30", "v31", "v32", "v33", "v34", "v35", "v36", "v37", "v38", "v39", "v40", "v41", "v42", "v43", "v44", "v45", "v46", "v47", "v48", "v49", "v50", "v51", "v52", "v53", "v54", "v55", "v56", "v57", "v58", "v59", "v60", "v61", "v62", "v63", "v64", "v65", "v66", "v67", "v68", "v69", "v70", "v71", "v72", "v73", "v74", "v75", "v76", "v77", "v78", "v79", "v80", "v81", "v82", "v83", "v84", "v85", "v86", "v87", "v88", "v89", "v90", "v91", "v92", "v93", "v94", "v95", "v96", "v97", "v98", "v99", "v100", "v101", "v102", "v103", "v104", "v105", "v106", "v107", "v108", "v109", "v110", "v111", "v112", "v113", "v114", "v115", "v116", "v117", "v118", "v119", "v120", "v121", "v122", "v123", "v124", "v125", "v126", "v127", "v128", "v129", "v130", "v131", "v132", "v133", "v134", "v135", "v136", "v137", "v138", "v139", "v140", "v141", "v142", "v143", "v144", "v145", "v146", "v147", "v148", "v149", "v150", "v151", "v152", "v153", "v154", "v155", "v156", "v157", "v158", "v159", "v160", "v161", "v162", "v163", "v164", "v165", "v166", "v167", "v168", "v169", "v170", "v171", "v172", "v173", "v174", "v175", "v176", "v177", "v178", "v179", "v180", "v181", "v182", "v183", "v184", "v185", "v186", "v187", "v188", "v189", "v190", "v191", "v192", "v193", "v194", "v195", "v196", "v197", "v198", "v199", "v200", "v201", "v202", "v203", "v204", "v205", "v206", "v207", "v208", "v209", "v210", "v211", "v212", "v213", "v214", "v215", "v216", "v217", "v218", "v219", "v220", "v221", "v222", "v223", "v224", "v225", "v226", "v227", "v228", "v229", "v230", "v231", "v232", "v233", "v234", "v235", "v236", "v237", "v238", "v239", "v240", "v241", "v242", "v243", "v244", "v245", "v246", "v247", "v248", "v249", "v250", "v251", "v252", "v253", "v254", "a0", "a1", "a2", "a3", "a4", "a5", "a6", "a7", "a8", "a9", "a10", "a11", "a12", "a13", "a14", "a15", "a16", "a17", "a18", "a19", "a20", "a21", "a22", "a23", "a24", "a25", "a26", "a27", "a28", "a29", "a30", "a31", "a32", "a33", "a34", "a35", "a36", "a37", "a38", "a39", "a40", "a41", "a42", "a43", "a44", "a45", "a46", "a47", "a48", "a49", "a50", "a51", "a52", "a53", "a54", "a55", "a56", "a57", "a58", "a59", "a60", "a61", "a62", "a63", "a64", "a65", "a66", "a67", "a68", "a69", "a70", "a71", "a72", "a73", "a74", "a75", "a76", "a77", "a78", "a79", "a80", "a81", "a82", "a83", "a84", "a85", "a86", "a87", "a88", "a89", "a90", "a91", "a92", "a93", "a94", "a95", "a96", "a97", "a98", "a99", "a100", "a101", "a102", "a103", "a104", "a105", "a106", "a107", "a108", "a109", "a110", "a111", "a112", "a113", "a114", "a115", "a116", "a117", "a118", "a119", "a120", "a121", "a122", "a123", "a124", "a125", "a126", "a127", "a128", "a129", "a130", "a131", "a132", "a133", "a134", "a135", "a136", "a137", "a138", "a139", "a140", "a141", "a142", "a143", "a144", "a145", "a146", "a147", "a148", "a149", "a150", "a151", "a152", "a153", "a154", "a155", "a156", "a157", "a158", "a159", "a160", "a161", "a162", "a163", "a164", "a165", "a166", "a167", "a168", "a169", "a170", "a171", "a172", "a173", "a174", "a175", "a176", "a177", "a178", "a179", "a180", "a181", "a182", "a183", "a184", "a185", "a186", "a187", "a188", "a189", "a190", "a191", "a192", "a193", "a194", "a195", "a196", "a197", "a198", "a199", "a200", "a201", "a202", "a203", "a204", "a205", "a206", "a207", "a208", "a209", "a210", "a211", "a212", "a213", "a214", "a215", "a216", "a217", "a218", "a219", "a220", "a221", "a222", "a223", "a224", "a225", "a226", "a227", "a228", "a229", "a230", "a231", "a232", "a233", "a234", "a235", "a236", "a237", "a238", "a239", "a240", "a241", "a242", "a243", "a244", "a245", "a246", "a247", "a248", "a249", "a250", "a251", "a252", "a253", "a254", "a255", "s4", "s5", "s6", "s7", "s8", "s9", "s10", "s11", "s12", "s13", "s14", "s15", "s16", "s17", "s18", "s19", "s20", "s21", "s22", "s23", "s24", "s25", "s26", "s27", "s28", "s29", "s30", "s31", "s32", "s33", "s34", "s35", "s36", "s37", "s38", "s39", "s40", "s41", "s42", "s43", "s44", "s45", "s46", "s47", "s48", "s49", "s50", "s51", "s52", "s53", "s54", "s55", "s56", "s57", "s58", "s59", "s60", "s61", "s62", "s63", "s64", "s65", "s66", "s67", "s68", "s69", "s70", "s71", "s72", "s73", "s74", "s75", "s76", "s77", "s78", "s79", "s80", "s81", "s82", "s83", "s84", "s85", "s86", "s87", "s88", "s89", "s90", "s91", "s92", "s93", "s94", "s95", "s96", "s97", "s98", "s99", "s100", "s101", "vcc", "scc", "m0", "memory");
}

extern "C" void kernel_launch(void* const* d_in, const int* in_sizes, int n_in, void* d_out, int out_size, void* d_ws, size_t ws_size, hipStream_t stream) {
    (void)in_sizes; (void)n_in; (void)out_size; (void)ws_size;
    char* ws = (char*)d_ws + 0ull;
    Args a;
    a.p[0] = d_in[0]; a.p[1] = d_in[1]; a.p[2] = d_in[2]; a.p[3] = d_in[3]; a.p[4] = d_in[4]; a.p[5] = d_in[5]; a.p[6] = d_in[7]; a.p[7] = d_in[8]; a.p[8] = d_in[9]; a.p[9] = d_out; a.p[10] = ws;
    mega<<<256, 256, 0, stream>>>(a);
}
```

```cpp
#include <hip/hip_runtime.h>
#include <hip/hip_bf16.h>
#include <stdint.h>
#include <type_traits>

#define LAS __attribute__((address_space(3)))
struct Args { const void* p[11]; };
__global__ __launch_bounds__(256, 1)
void mega(Args args) {
    __shared__ __attribute__((aligned(1024))) char smem[163840];
    const unsigned tid = threadIdx.x + (unsigned)(uintptr_t)(LAS char*)smem;
    asm volatile(
R"(.macro cv a,b,c
v_cvt_pk_bf16_f32 v\a,v\b,v\c
.endm
.macro cvaa a,b,c,d
v_mfma_f32_16x16x32_bf16 v[\a:\a+3],a[\b:\b+3],a[\c:\c+3],v[\d:\d+3]
.endm
.macro cvvv a,b,c,d
v_mfma_f32_16x16x32_bf16 v[\a:\a+3],v[\b:\b+3],v[\c:\c+3],v[\d:\d+3]
.endm
.macro dl a,b,c,d
buffer_load_dwordx4 v\a,s[\b:\b+3],s\c offen offset:\d lds
.endm
.macro ds a,b,c,d
buffer_load_dwordx4 v\a,s[\b:\b+3],s\c offen offset:\d sc1 lds
.endm
.macro ex a
v_exp_f32_e32 v\a,v\a
.endm
.macro la a,b,c,d
buffer_load_dwordx4 a[\a:\a+3],v\b,s[8:11],s\c offen offset:\d sc1
.endm
.macro lv a,b,c,d
buffer_load_dwordx4 v[\a:\a+3],v\b,s[8:11],s\c offen offset:\d sc1
.endm
.macro maaa a,b,c
v_mfma_f32_16x16x32_bf16 a[\a:\a+3],a[\b:\b+3],a[\c:\c+3],a[\a:\a+3]
.endm
.macro maav a,b,c
v_mfma_f32_16x16x32_bf16 a[\a:\a+3],a[\b:\b+3],v[\c:\c+3],a[\a:\a+3]
.endm
.macro mavv a,b,c
v_mfma_f32_16x16x32_bf16 a[\a:\a+3],v[\b:\b+3],v[\c:\c+3],a[\a:\a+3]
.endm
.macro mvaa a,b,c
v_mfma_f32_16x16x32_bf16 v[\a:\a+3],a[\b:\b+3],a[\c:\c+3],v[\a:\a+3]
.endm
.macro mvav a,b,c
v_mfma_f32_16x16x32_bf16 v[\a:\a+3],a[\b:\b+3],v[\c:\c+3],v[\a:\a+3]
.endm
.macro mvva a,b,c
v_mfma_f32_16x16x32_bf16 v[\a:\a+3],v[\b:\b+3],a[\c:\c+3],v[\a:\a+3]
.endm
.macro mvvv a,b,c
v_mfma_f32_16x16x32_bf16 v[\a:\a+3],v[\b:\b+3],v[\c:\c+3],v[\a:\a+3]
.endm
.macro ra a,b,c
ds_read_b128 a[\a:\a+3],v\b offset:\c
.endm
.macro rv a,b,c
ds_read_b128 v[\a:\a+3],v\b offset:\c
.endm
.macro ta a,b,c
ds_read_b64_tr_b16 a[\a:\a+1],v\b offset:\c
.endm
.macro tv a,b,c
ds_read_b64_tr_b16 v[\a:\a+1],v\b offset:\c
.endm
.macro zaaa a,b,c
v_mfma_f32_16x16x32_bf16 a[\a:\a+3],a[\b:\b+3],a[\c:\c+3],0
.endm
.macro zaav a,b,c
v_mfma_f32_16x16x32_bf16 a[\a:\a+3],a[\b:\b+3],v[\c:\c+3],0
.endm
.macro zvaa a,b,c
v_mfma_f32_16x16x32_bf16 v[\a:\a+3],a[\b:\b+3],a[\c:\c+3],0
.endm
.macro zvav a,b,c
v_mfma_f32_16x16x32_bf16 v[\a:\a+3],a[\b:\b+3],v[\c:\c+3],0
.endm
.macro zvva a,b,c
v_mfma_f32_16x16x32_bf16 v[\a:\a+3],v[\b:\b+3],a[\c:\c+3],0
.endm
.macro zvvv a,b,c
v_mfma_f32_16x16x32_bf16 v[\a:\a+3],v[\b:\b+3],v[\c:\c+3],0
.endm
L_begin:
s_load_dwordx2 s[28:29],%0,72
s_load_dwordx16 s[68:83],%0,0
s_load_dwordx2 s[84:85],%0,64
s_load_dwordx2 s[8:9],%0,80
v_lshrrev_b32_e32 v246,6,%2
v_and_b32_e32 v247,63,%2
v_lshlrev_b32_e32 v244,4,v247
v_readfirstlane_b32 s12,v246
s_and_b32 s20,%1,7
s_lshr_b32 s21,%1,3
s_getpc_b64 s[96:97]
L_att_pc:
s_lshl_b32 s86,s21,2
s_add_u32 s86,s86,s12
s_mul_i32 s86,s86,512
v_lshl_add_u32 v246,v247,7,s86
v_min_u32_e32 v246,L_att_end-L_att_pc-4,v246
s_mov_b64 exec,15
global_load_dword a188,v246,s[96:97]
s_mov_b64 exec,-1
s_and_b32 s19,s21,7
s_lshr_b32 s86,s21,3
s_lshl_b32 s18,s20,2
s_add_u32 s18,s18,s86
v_lshrrev_b32_e32 v248,3,v247
v_and_b32_e32 v250,112,v244
v_and_b32_e32 v249,48,v247
v_xor_b32_e32 v249,v249,v250
v_and_b32_e32 v251,16,v247
v_lshlrev_b32_e32 v251,1,v251
v_xor_b32_e32 v250,v250,v251
v_lshl_add_u32 v224,v248,10,v249
v_xor_b32_e32 v225,64,v224
v_add_u32_e32 v227,21504,v225
v_add_u32_e32 v226,14336,v224
v_add_u32_e32 v225,7168,v225
v_lshl_add_u32 v228,v248,13,v250
v_xor_b32_e32 v229,64,v228
v_add_u32_e32 v231,193536,v229
v_add_u32_e32 v230,129024,v228
v_add_u32_e32 v229,64512,v229
s_lshl_b32 s86,s19,2
s_add_u32 s86,s86,s12
s_lshl_b32 s52,s86,16
s_bfe_u32 s87,s21,65539
s_lshl_b32 s88,s20,1
s_add_u32 s87,s87,s88
s_lshl_b32 s87,s87,2
s_add_u32 s87,s87,s12
s_lshl_b32 s53,s87,7
s_mov_b32 s54,s53
s_mov_b32 s39,131072
s_mov_b32 s43,131072
s_mov_b32 s47,131072
s_mov_b32 s51,131072
s_mov_b32 s38,2097152
s_mov_b32 s42,4194304
s_mov_b32 s46,4194304
s_mov_b32 s56,32768
s_mov_b32 s60,-32640
s_mov_b32 s57,262144
s_mov_b32 s58,262144
s_lshl_b32 s98,s12,13
s_lshl_b32 s62,s12,12
s_add_u32 s62,s62,98304
s_lshl_b32 s35,%1,2
s_add_u32 s35,s35,83896320
v_lshrrev_b32_e32 v48,2,v247
v_and_b32_e32 v49,3,v247
v_lshlrev_b32_e32 v49,7,v49
v_lshl_add_u32 v48,v48,13,v49
s_lshl_b32 s89,s86,14
s_and_b32 s90,s53,7680
s_add_u32 s90,s90,s89
s_lshr_b32 s89,s21,3
s_lshl_b32 s89,s89,2
s_add_u32 s89,s89,s12
s_lshl_b32 s89,s89,14
s_lshl_b32 s91,s19,18
s_add_u32 s89,s89,s91
s_cmp_ge_u32 s21,16
s_waitcnt lgkmcnt(0)
s_mov_b64 s[36:37],s[70:71]
s_cselect_b64 s[40:41],s[76:77],s[74:75]
s_mov_b64 s[44:45],s[40:41]
s_bfm_b64 exec,8,0
buffer_load_dword a189,v48,s[40:43],s90 offen
s_mov_b64 exec,-1
s_and_b32 s9,s9,65535
s_mov_b32 s10,83951616
s_mov_b32 s11,131072
buffer_load_dword v44,off,s[8:11],s35 sc1
buffer_load_dword v45,off,s[8:11],s35 offset:1024 sc1
s_add_u32 m0,s62,0
s_nop 0
dl 228,40,53,0
dl 229,40,53,1024
s_add_u32 s53,s53,s57
dl 230,44,54,2048
dl 231,44,54,3072
s_add_u32 s54,s54,s57
s_add_u32 m0,s98,0
s_nop 0
dl 224,36,52,0
dl 225,36,52,1024
dl 226,36,52,2048
dl 227,36,52,3072
s_add_u32 s52,s52,s56
s_add_u32 m0,s98,4096
s_nop 0
dl 224,36,52,0
dl 225,36,52,1024
dl 226,36,52,2048
dl 227,36,52,3072
s_add_u32 s52,s52,s60
v_and_b32_e32 v246,15,v247
v_lshlrev_b32_e32 v248,3,v247
v_xor_b32_e32 v248,v248,v247
v_and_b32_e32 v248,112,v248
v_lshl_add_u32 v232,v246,7,v248
s_and_b32 s86,s98,16384
v_add_u32_e32 v232,s86,v232
v_xor_b32_e32 v233,64,v232
v_and_b32_e32 v246,12,v247
v_lshlrev_b32_e32 v246,5,v246
v_and_b32_e32 v248,3,v247
v_lshl_add_u32 v246,v248,3,v246
v_and_b32_e32 v248,24,v247
v_lshl_add_u32 v246,v248,2,v246
s_and_b32 s87,s98,8192
s_add_u32 s87,s87,98304
v_add_u32_e32 v246,s87,v246
v_and_b32_e32 v248,48,v247
v_lshl_add_u32 v234,v248,6,v246
v_xor_b32_e32 v235,32,v234
v_xor_b32_e32 v236,64,v234
v_xor_b32_e32 v237,96,v234
s_mov_b64 exec,0
s_cmp_ge_u32 s21,16
s_cbranch_scc1 L_vbV
s_cmp_eq_u32 s12,0
s_cbranch_scc1 L_vs1
s_cmp_eq_u32 s12,1
s_cbranch_scc1 L_vs2
s_cmp_eq_u32 s12,2
s_cbranch_scc1 L_vs3
s_branch L_vs8
L_vbV:
s_cmp_eq_u32 s12,0
s_cbranch_scc1 L_vs4
s_cmp_eq_u32 s12,1
s_cbranch_scc1 L_vs5
s_cmp_eq_u32 s12,2
s_cbranch_scc1 L_vs6
s_branch L_vs7
L_vr0:
s_mov_b64 exec,-1
s_add_u32 m0,s62,16384
s_nop 0
dl 228,40,53,0
dl 229,40,53,1024
s_add_u32 s53,s53,s58
dl 230,44,54,2048
dl 231,44,54,3072
s_add_u32 s54,s54,s58
s_add_u32 m0,s98,32768
s_nop 0
dl 224,36,52,0
dl 225,36,52,1024
dl 226,36,52,2048
dl 227,36,52,3072
s_add_u32 s52,s52,s56
s_waitcnt vmcnt(20)
s_cmp_eq_u32 s12,0
s_cbranch_scc1 L_rq0
s_branch L_rqx
L_vr0s:
s_setpc_b64 s[4:5]
L_rq0:
v_readfirstlane_b32 s32,v44
v_readfirstlane_b32 s86,v45
s_add_u32 s32,s32,1
s_cmp_eq_u32 s32,s86
s_addc_u32 s32,s32,0
v_mov_b32_e32 v44,s32
v_mov_b32_e32 v45,0
v_lshlrev_b32_e32 v46,3,v244
v_mov_b32_e32 v47,0
s_lshl_b32 s86,%1,7
s_add_u32 s87,s86,83914752
s_and_b32 s86,s86,16383
s_add_u32 s86,s86,83898368
s_mov_b64 exec,1
buffer_store_dword v44,off,s[8:11],s35 sc0 sc1
v_mov_b32_e32 v48,0
v_mov_b32_e32 v49,0
buffer_store_dwordx4 v[46:49],off,s[8:11],s86 sc0 sc1
buffer_store_dwordx4 v[46:49],off,s[8:11],s87 sc0 sc1
s_cmp_lg_u32 %1,0
s_cbranch_scc1 L_rqy
s_mov_b64 exec,65535
s_mov_b32 s86,83894272
buffer_store_dword v45,v46,s[8:11],s86 offen sc0 sc1
L_rqy:
s_mov_b64 exec,-1
L_rqx:
s_cmp_ge_u32 s21,16
s_cbranch_scc1 L_vtile
s_waitcnt vmcnt(12)
s_barrier
ta 64,234,0
ta 66,234,512
ra 0,232,0
ta 68,235,0
ta 70,235,512
ra 4,232,2048
ra 16,232,8192
ra 20,232,10240
s_add_u32 m0,s62,32768
v_mov_b32_e32 v216,0
dl 228,40,53,0
v_mov_b32_e32 v217,0
v_mov_b32_e32 v218,0
v_mov_b32_e32 v219,0
v_mov_b32_e32 v220,0
v_mov_b32_e32 v221,0
v_mov_b32_e32 v222,0
v_mov_b32_e32 v223,0
v_mov_b32_e32 v212,0
v_mov_b32_e32 v213,0
v_mov_b32_e32 v214,0
v_mov_b32_e32 v215,0
v_mov_b32_e32 v208,0
v_mov_b32_e32 v209,0
v_mov_b32_e32 v210,0
v_mov_b32_e32 v211,0
v_mov_b32_e32 v152,0
v_mov_b32_e32 v153,0
v_mov_b32_e32 v154,0
v_mov_b32_e32 v155,0
v_mov_b32_e32 v156,0
s_waitcnt lgkmcnt(5)
zaaa 128,64,0
s_lshl_b32 s4,%1,1
ta 72,236,0
ta 74,236,512
ta 76,237,0
ta 78,237,512
ta 80,234,4096
s_waitcnt lgkmcnt(8)
zaaa 132,68,0
s_add_u32 s4,s4,s12
ta 82,234,4608
v_mov_b32_e32 v157,0
s_waitcnt lgkmcnt(6)
zaaa 140,68,4
s_mul_i32 s4,s4,1280
ta 84,235,4096
ta 86,235,4608
zaaa 136,64,4
dl 229,40,53,1024
zvaa 96,64,16
s_add_u32 s53,s53,s57
v_mov_b32_e32 v158,0
v_mov_b32_e32 v159,0
zvaa 100,68,16
s_mov_b32 s55,s4
v_mov_b32_e32 v148,0
v_mov_b32_e32 v149,0
zvaa 108,68,20
s_mul_i32 s4,%1,1792
v_mov_b32_e32 v150,0
v_mov_b32_e32 v151,0
zvaa 104,64,20
dl 230,44,54,2048
s_waitcnt lgkmcnt(6)
zvaa 120,72,20
s_lshl_b32 s88,%1,2
ra 32,233,0
v_mov_b32_e32 v144,0
s_waitcnt lgkmcnt(5)
zvaa 124,76,20
s_waitcnt vmcnt(11)
s_barrier
ra 8,232,4096
zvaa 116,76,16
s_cmp_eq_u32 s12,2
s_cselect_b32 s55,s4,s55
ra 12,232,6144
v_mov_b32_e32 v145,0
zvaa 112,72,16
dl 231,44,54,3072
zaaa 152,72,4
s_add_u32 m0,s98,36864
ra 24,232,12288
s_add_u32 s54,s54,s57
zaaa 156,76,4
s_lshl_b32 s4,%1,14
ra 28,232,14336
v_mov_b32_e32 v146,0
zaaa 148,76,0
s_add_u32 s88,s88,s12
ra 36,233,2048
v_mov_b32_e32 v147,0
zaaa 144,72,0
dl 224,36,52,0
s_waitcnt lgkmcnt(6)
zvaa 0,80,0
s_add_u32 s5,s4,16384
ra 40,233,4096
v_mov_b32_e32 v88,0
zvaa 4,84,0
s_cmp_eq_u32 s12,3
s_cselect_b32 s55,s4,s55
ra 44,233,6144
v_mov_b32_e32 v89,0
zvaa 12,84,4
s_mov_b32 s4,10
ra 48,233,8192
v_mov_b32_e32 v90,0
zvaa 8,80,4
dl 225,36,52,1024
zvaa 128,80,16
s_lshl_b32 s88,s88,12
ra 52,233,10240
v_mov_b32_e32 v91,0
zvaa 132,84,16
s_xor_b32 s87,s19,15
v_mov_b32_e32 v92,0
v_mov_b32_e32 v93,0
zvaa 140,84,20
s_cmp_eq_u32 s12,2
s_cselect_b32 s4,14,s4
ra 56,233,12288
v_mov_b32_e32 v94,0
zvaa 136,80,20
dl 226,36,52,2048
s_waitcnt lgkmcnt(5)
zvaa 32,64,8
s_add_u32 s30,s68,s88
s_addc_u32 s31,s69,0
v_mov_b32_e32 v95,0
v_mov_b32_e32 v84,0
zvaa 36,68,8
s_lshl_b32 s22,s87,7
ra 60,233,14336
v_mov_b32_e32 v85,0
zvaa 44,68,12
s_lshl_b32 s87,s87,2
v_mov_b32_e32 v86,0
v_mov_b32_e32 v87,0
zvaa 40,64,12
dl 227,36,52,3072
zvaa 160,64,24
s_add_u32 m0,s62,49152
s_add_u32 s52,s52,s60
v_mov_b32_e32 v80,0
zvaa 164,68,24
s_lshl_b32 s88,s19,2
v_mov_b32_e32 v81,0
v_mov_b32_e32 v82,0
zvaa 172,68,28
s_cmp_eq_u32 s12,3
s_cselect_b32 s4,10,s4
v_mov_b32_e32 v83,0
v_mov_b32_e32 v24,0
zvaa 168,64,28
dl 228,40,53,0
zvaa 184,72,28
s_add_u32 s87,s87,s12
v_mov_b32_e32 v25,0
v_mov_b32_e32 v26,0
zvaa 188,76,28
s_add_u32 s88,s88,s12
v_mov_b32_e32 v27,0
v_mov_b32_e32 v28,0
zvaa 180,76,24
s_waitcnt vmcnt(13)
s_barrier
ta 96,234,16384
ta 98,234,16896
ta 100,235,16384
ta 102,235,16896
zvaa 176,72,24
dl 229,40,53,1024
zvaa 56,72,12
s_lshl_b32 s90,s18,13
v_mov_b32_e32 v29,0
v_mov_b32_e32 v30,0
zvaa 60,76,12
s_add_u32 s90,s90,62914560
ta 104,236,16384
v_mov_b32_e32 v31,0
zvaa 52,76,8
s_mul_i32 s14,s87,327680
ta 106,236,16896
v_mov_b32_e32 v20,0
zvaa 48,72,8
dl 230,44,54,2048
zvaa 64,80,8
s_mul_i32 s13,s88,327680
ta 108,237,16384
v_mov_b32_e32 v21,0
zvaa 68,84,8
s_add_u32 s14,s14,s90
ta 110,237,16896
v_mov_b32_e32 v22,0
zvaa 76,84,12
s_add_u32 s13,s13,s90
v_mov_b32_e32 v23,0
v_mov_b32_e32 v16,0
zvaa 72,80,12
dl 231,44,54,3072
zvaa 192,80,24
s_add_u32 m0,s98,65536
v_mov_b32_e32 v17,0
v_mov_b32_e32 v18,0
zvaa 196,84,24
s_mul_i32 s90,s18,524288
ta 208,236,4096
v_mov_b32_e32 v19,0
zvaa 204,84,28
s_lshl_b32 s91,s12,12
ta 210,236,4608
v_lshlrev_b32_e32 v241,7,v247
zvaa 200,80,28
dl 224,36,52,0
s_waitcnt lgkmcnt(5)
maaa 128,96,32
s_add_u32 s90,s90,s91
ta 216,237,4096
v_mov_b32_e32 v242,134217728
maaa 132,100,32
s_add_u32 s90,s90,16777216
ta 218,237,4608
v_lshrrev_b32_e32 v248,3,v247
maaa 140,100,36
s_cmp_eq_u32 s12,3
s_cselect_b64 s[26:27],-1,0
ta 112,234,20480
v_and_b32_e32 v250,112,v244
maaa 136,96,36
dl 225,36,52,1024
mvaa 32,96,40
s_lshl_b32 s86,s19,1
ta 114,234,20992
v_and_b32_e32 v251,16,v247
mvaa 36,100,40
s_mov_b64 s[48:49],s[72:73]
ta 116,235,20480
v_lshlrev_b32_e32 v251,1,v251
mvaa 44,100,44
s_mov_b32 s50,8388608
ta 118,235,20992
v_mul_u32_u24_e32 v246,6144,v248
mvaa 40,96,44
dl 226,36,52,2048
mvaa 96,96,48
s_mov_b32 s59,655360
v_cmp_gt_u32_e64 s[24:25],s4,v247
v_add_u32_e32 v249,s90,v244
mvaa 100,100,48
s_add_u32 s86,s86,2
v_cndmask_b32_e64 v241,v242,v241,s[24:25]
v_add_u32_e32 v255,16777216,v249
mvaa 108,100,52
s_cmp_eq_u32 s12,2
s_cselect_b64 s[24:25],-1,0
v_accvgpr_write_b32 a240,v249
v_and_b32_e32 v249,48,v247
mvaa 104,96,52
dl 227,36,52,3072
mvaa 160,96,56
s_add_u32 s52,s52,s56
s_add_u32 m0,s98,69632
v_accvgpr_write_b32 a241,v255
mvaa 164,100,56
s_cmp_eq_u32 s12,2
s_cselect_b64 s[48:49],s[80:81],s[48:49]
ta 212,236,20480
v_xor_b32_e32 v249,v249,v250
mvaa 172,100,60
s_cmp_eq_u32 s12,2
s_cselect_b32 s50,6291456,s50
v_xor_b32_e32 v250,v250,v251
v_lshl_add_u32 v252,v248,11,v249
mvaa 168,96,60
dl 224,36,52,0
s_waitcnt lgkmcnt(1)
mvaa 184,104,60
s_cmp_eq_u32 s12,2
s_cselect_b32 s59,458752,s59
v_and_b32_e32 v251,48,v250
v_xor_b32_e32 v253,64,v252
mvaa 188,108,60
s_lshl_b32 s15,1,s86
v_accvgpr_write_b32 a242,v252
v_add_u32_e32 v246,v246,v251
mvaa 180,108,56
s_lshl_b32 s23,s19,7
v_add_u32_e32 v242,15360,v253
v_and_b32_e32 v251,64,v250
mvaa 176,104,56
dl 225,36,52,1024
mvaa 120,104,52
s_or_b32 s86,%1,s12
s_cselect_b32 s65,0,4
v_lshl_add_u32 v248,v251,5,v246
v_xor_b32_e32 v249,64,v251
mvaa 124,108,52
s_cmp_eq_u32 s12,3
s_cselect_b64 s[48:49],s[68:69],s[48:49]
v_mul_u32_u24_e32 v251,768,v251
v_accvgpr_write_b32 a243,v242
mvaa 116,108,48
s_cmp_eq_u32 s12,3
s_cselect_b32 s50,s5,s50
v_add_u32_e32 v242,30720,v252
v_lshl_add_u32 v249,v249,5,v246
mvaa 112,104,48
dl 226,36,52,2048
mvaa 56,104,44
s_cmp_eq_u32 s12,3
s_cselect_b32 s59,1280,s59
v_accvgpr_write_b32 a244,v242
v_add_u32_e32 v246,v246,v251
mvaa 60,108,44
s_add_u32 s53,s53,s58
v_add_u32_e32 v242,46080,v253
v_accvgpr_write_b32 a245,v242
mvaa 52,108,40
s_add_u32 s54,s54,s58
v_add_u32_e32 v243,4096,v246
v_add_u32_e32 v242,0,v248
mvaa 48,104,40
dl 227,36,52,3072
maaa 152,104,36
s_add_u32 s52,s52,s60
ta 214,236,20992
v_cndmask_b32_e64 v242,v228,v242,s[24:25]
maaa 156,108,36
s_mov_b32 s63,32768
ta 220,237,20480
v_cndmask_b32_e64 v242,v242,v243,s[26:27]
maaa 148,108,32
s_mov_b32 s61,s98
ta 222,237,20992
v_add_u32_e32 v243,101376,v246
maaa 144,104,32
s_mov_b32 s64,1
v_and_b32_e32 v246,15,v247
v_and_b32_e32 v246,12,v247
mvaa 0,112,32
v_accvgpr_write_b32 a246,v242
v_add_u32_e32 v242,48128,v249
mvaa 4,116,32
v_cndmask_b32_e64 v242,v229,v242,s[24:25]
v_cndmask_b32_e64 v242,v242,v243,s[26:27]
mvaa 12,116,36
v_lshlrev_b32_e32 v246,5,v246
v_accvgpr_write_b32 a247,v242
mvaa 8,112,36
v_add_u32_e32 v242,96256,v248
v_cndmask_b32_e64 v242,v230,v242,s[24:25]
mvaa 64,112,40
v_and_b32_e32 v248,3,v247
v_lshl_add_u32 v246,v248,3,v246
mvaa 68,116,40
v_accvgpr_write_b32 a248,v242
v_and_b32_e32 v248,24,v247
mvaa 76,116,44
v_add_u32_e32 v242,144384,v249
v_lshl_add_u32 v246,v248,2,v246
mvaa 72,112,44
v_and_b32_e32 v248,32,v247
v_cndmask_b32_e64 v242,v231,v242,s[24:25]
mvaa 128,112,48
v_add_u32_e32 v246,98304,v246
v_accvgpr_write_b32 a249,v242
mvaa 132,116,48
v_lshl_add_u32 v238,v248,5,v246
v_xor_b32_e32 v239,32,v238
mvaa 140,116,52
v_add_u32_e32 v232,32768,v232
v_add_u32_e32 v233,32768,v233
mvaa 136,112,52
mvaa 192,112,56
mvaa 196,116,56
mvaa 204,116,60
mvaa 200,112,60
s_add_u32 s4,s96,L_vrk-L_att_pc
s_addc_u32 s5,s97,0
s_mov_b64 exec,0
s_cmp_eq_u32 s12,0
s_cbranch_scc1 L_vs13
s_cmp_eq_u32 s12,1
s_cbranch_scc1 L_vs14
s_cmp_eq_u32 s12,2
s_cbranch_scc1 L_vs15
s_branch L_vs16
L_vrk:
s_mov_b64 exec,-1
s_sleep 6
s_waitcnt vmcnt(8)
s_waitcnt lgkmcnt(0)
s_barrier
L_k_head:
mvaa 216,208,28
ra 64,232,0
mvaa 216,212,60
s_add_u32 m0,s62,0
ta 192,234,32768
mvaa 220,220,60
dl 228,40,53,0
mvaa 220,216,28
ta 194,234,33280
s_nop 3
mvaa 212,216,24
ra 96,233,0
mvaa 212,220,56
ta 196,234,49152
mvaa 208,212,56
ta 198,234,49664
mvaa 208,208,24
ta 204,235,49152
mvaa 152,208,20
dl 229,40,53,1024
mvaa 152,212,52
s_add_u32 s53,s53,s57
ta 206,235,49664
mvaa 156,220,52
ta 200,235,32768
mvaa 156,216,20
ta 202,235,33280
mvaa 148,216,16
ra 68,232,2048
mvaa 148,220,48
ra 100,233,2048
mvaa 144,212,48
ra 72,232,4096
mvaa 144,208,16
dl 230,44,54,2048
mvaa 88,208,12
ra 104,233,4096
mvaa 88,212,44
ra 76,232,6144
mvaa 92,220,44
ra 108,233,6144
mvaa 92,216,12
ra 80,232,8192
mvaa 84,216,8
ra 112,233,8192
mvaa 84,220,40
dl 231,44,54,3072
mvaa 80,212,40
s_add_u32 s54,s54,s57
ra 84,232,10240
mvaa 80,208,8
ra 116,233,10240
mvaa 24,208,4
ra 88,232,12288
mvaa 24,212,36
ra 120,233,12288
mvaa 28,220,36
ra 92,232,14336
mvaa 28,216,4
s_add_u32 m0,s62,16384
ra 124,233,14336
mvaa 20,216,0
dl 228,40,53,0
mvaa 20,220,32
L_vs1:
s_cbranch_execz L_vs9
mvaa 16,212,32
mvaa 16,208,0
s_waitcnt lgkmcnt(8)
maaa 128,192,64
maaa 128,196,96
maaa 132,204,96
dl 229,40,53,1024
maaa 132,200,64
s_add_u32 s53,s53,s58
maaa 140,200,68
maaa 140,204,100
maaa 136,196,100
maaa 136,192,68
mvaa 32,192,72
mvaa 32,196,104
dl 230,44,54,2048
mvaa 36,204,104
mvaa 36,200,72
s_waitcnt lgkmcnt(0)
mvaa 44,200,76
ta 208,236,32768
mvaa 44,204,108
ta 210,236,33280
mvaa 40,196,108
ta 212,236,49152
mvaa 40,192,76
dl 231,44,54,3072
mvaa 96,192,80
s_add_u32 s54,s54,s58
ta 214,236,49664
mvaa 96,196,112
ta 220,237,49152
mvaa 100,204,112
ta 222,237,49664
mvaa 100,200,80
ta 216,237,32768
mvaa 108,200,84
ta 218,237,33280
mvaa 108,204,116
mvaa 104,196,116
s_add_u32 m0,s61,0
mvaa 104,192,84
dl 224,36,52,0
mvaa 160,192,88
mvaa 160,196,120
mvaa 164,204,120
mvaa 164,200,88
mvaa 172,200,92
mvaa 172,204,124
mvaa 168,196,124
dl 225,36,52,1024
mvaa 168,192,92
s_waitcnt lgkmcnt(0)
mvaa 184,208,92
mvaa 184,212,124
mvaa 188,220,124
ta 192,234,36864
mvaa 188,216,92
ta 194,234,37376
mvaa 180,216,88
ta 196,234,53248
mvaa 180,220,120
dl 226,36,52,2048
mvaa 176,212,120
ta 198,234,53760
mvaa 176,208,88
ta 204,235,53248
mvaa 120,208,84
ta 206,235,53760
mvaa 120,212,116
ta 200,235,36864
mvaa 124,220,116
ta 202,235,37376
mvaa 124,216,84
mvaa 116,216,80
dl 227,36,52,3072
mvaa 116,220,112
s_add_u32 s52,s52,s56
mvaa 112,212,112
mvaa 112,208,80
mvaa 56,208,76
mvaa 56,212,108
mvaa 60,220,108
mvaa 60,216,76
mvaa 52,216,72
s_add_u32 m0,s61,4096
mvaa 52,220,104
dl 224,36,52,0
mvaa 48,212,104
s_add_u32 s61,s63,s98
mvaa 48,208,72
s_cmp_ge_u32 s63,65536
s_cselect_b32 s99,-2,1
s_lshl_b32 s99,s99,15
maaa 152,208,68
s_add_u32 s63,s63,s99
v_add_u32_e32 v232,s99,v232
v_add_u32_e32 v233,s99,v233
maaa 152,212,100
maaa 156,220,100
maaa 156,216,68
maaa 148,216,64
dl 225,36,52,1024
maaa 148,220,96
maaa 144,212,96
s_waitcnt lgkmcnt(0)
maaa 144,208,64
mvaa 0,192,64
mvaa 0,196,96
mvaa 4,204,96
ta 208,236,36864
mvaa 4,200,64
ta 210,236,37376
mvaa 12,200,68
dl 226,36,52,2048
mvaa 12,204,100
ta 212,236,53248
mvaa 8,196,100
ta 214,236,53760
mvaa 8,192,68
ta 220,237,53248
mvaa 64,192,72
ta 222,237,53760
mvaa 64,196,104
ta 216,237,36864
mvaa 68,204,104
ta 218,237,37376
mvaa 68,200,72
dl 227,36,52,3072
mvaa 76,200,76
s_add_u32 s52,s52,s60
mvaa 76,204,108
mvaa 72,196,108
mvaa 72,192,76
mvaa 128,192,80
mvaa 128,196,112
buffer_load_dword a184,v241,s[48:51],s55 offen
mvaa 132,204,112
s_add_u32 s55,s55,s59
mvaa 132,200,80
mvaa 140,200,84
mvaa 140,204,116
mvaa 136,196,116
mvaa 136,192,84
buffer_load_dword a185,v241,s[48:51],s55 offen
mvaa 192,192,88
s_add_u32 s55,s55,s59
mvaa 192,196,120
mvaa 196,204,120
mvaa 196,200,88
mvaa 204,200,92
mvaa 204,204,124
s_cmp_eq_u32 s64,3
mvaa 200,196,124
s_waitcnt vmcnt(10) lgkmcnt(0)
s_barrier
mvaa 200,192,92
s_cbranch_scc1 L_k_exit
mvaa 216,208,92
ra 0,232,0
mvaa 216,212,124
s_add_u32 m0,s62,32768
ta 192,234,0
mvaa 220,220,124
dl 228,40,53,0
mvaa 220,216,92
ta 194,234,512
s_nop 3
mvaa 212,216,88
ra 32,233,0
mvaa 212,220,120
ta 196,234,16384
mvaa 208,212,120
ta 198,234,16896
mvaa 208,208,88
ta 204,235,16384
mvaa 152,208,84
dl 229,40,53,1024
mvaa 152,212,116
s_add_u32 s53,s53,s57
ta 206,235,16896
mvaa 156,220,116
ta 200,235,0
mvaa 156,216,84
ta 202,235,512
mvaa 148,216,80
ra 4,232,2048
mvaa 148,220,112
ra 36,233,2048
mvaa 144,212,112
ra 8,232,4096
mvaa 144,208,80
dl 230,44,54,2048
mvaa 88,208,76
ra 40,233,4096
mvaa 88,212,108
ra 12,232,6144
mvaa 92,220,108
ra 44,233,6144
mvaa 92,216,76
ra 16,232,8192
mvaa 84,216,72
ra 48,233,8192
mvaa 84,220,104
dl 231,44,54,3072
mvaa 80,212,104
s_add_u32 s54,s54,s57
ra 20,232,10240
mvaa 80,208,72
ra 52,233,10240
mvaa 24,208,68
ra 24,232,12288
mvaa 24,212,100
ra 56,233,12288
mvaa 28,220,100
ra 28,232,14336
mvaa 28,216,68
s_add_u32 m0,s62,49152
ra 60,233,14336
mvaa 20,216,64
dl 228,40,53,0
mvaa 20,220,96
mvaa 16,212,96
mvaa 16,208,64
s_waitcnt lgkmcnt(8)
maaa 128,192,0
maaa 128,196,32
maaa 132,204,32
dl 229,40,53,1024
maaa 132,200,0
s_add_u32 s53,s53,s58
maaa 140,200,4
maaa 140,204,36
maaa 136,196,36
maaa 136,192,4
mvaa 32,192,8
mvaa 32,196,40
dl 230,44,54,2048
mvaa 36,204,40
mvaa 36,200,8
s_waitcnt lgkmcnt(0)
mvaa 44,200,12
ta 208,236,0
mvaa 44,204,44
ta 210,236,512
mvaa 40,196,44
ta 212,236,16384
mvaa 40,192,12
dl 231,44,54,3072
mvaa 96,192,16
s_add_u32 s54,s54,s58
)"
R"(ta 214,236,16896
mvaa 96,196,48
ta 220,237,16384
mvaa 100,204,48
ta 222,237,16896
mvaa 100,200,16
ta 216,237,0
mvaa 108,200,20
ta 218,237,512
mvaa 108,204,52
mvaa 104,196,52
s_add_u32 m0,s61,0
mvaa 104,192,20
dl 224,36,52,0
mvaa 160,192,24
s_lshl_b32 s86,s21,2
s_add_u32 s86,s86,s12
s_mul_i32 s86,s86,384
mvaa 160,196,56
s_add_u32 s86,s86,4096
v_lshl_add_u32 v253,v244,3,s86
mvaa 164,204,56
s_mov_b64 exec,7
global_load_dword a188,v253,s[96:97]
s_mov_b64 exec,-1
mvaa 164,200,24
mvaa 172,200,28
mvaa 172,204,60
mvaa 168,196,60
dl 225,36,52,1024
mvaa 168,192,28
s_waitcnt lgkmcnt(0)
mvaa 184,208,28
mvaa 184,212,60
mvaa 188,220,60
ta 192,234,4096
mvaa 188,216,28
ta 194,234,4608
mvaa 180,216,24
ta 196,234,20480
mvaa 180,220,56
dl 226,36,52,2048
mvaa 176,212,56
ta 198,234,20992
mvaa 176,208,24
ta 204,235,20480
mvaa 120,208,20
ta 206,235,20992
mvaa 120,212,52
ta 200,235,4096
mvaa 124,220,52
ta 202,235,4608
mvaa 124,216,20
mvaa 116,216,16
dl 227,36,52,3072
mvaa 116,220,48
s_add_u32 s52,s52,s56
mvaa 112,212,48
mvaa 112,208,16
mvaa 56,208,12
mvaa 56,212,44
s_bitcmp1_b32 s65,s64
s_cbranch_scc1 L_k_ev
L_k_evr:
mvaa 60,220,44
mvaa 60,216,12
mvaa 52,216,8
s_add_u32 m0,s61,4096
mvaa 52,220,40
dl 224,36,52,0
mvaa 48,212,40
s_add_u32 s61,s63,s98
mvaa 48,208,8
s_cmp_ge_u32 s63,65536
s_cselect_b32 s99,-2,1
s_lshl_b32 s99,s99,15
maaa 152,208,4
s_add_u32 s63,s63,s99
v_add_u32_e32 v232,s99,v232
v_add_u32_e32 v233,s99,v233
maaa 152,212,36
maaa 156,220,36
maaa 156,216,4
maaa 148,216,0
dl 225,36,52,1024
maaa 148,220,32
maaa 144,212,32
s_waitcnt lgkmcnt(0)
maaa 144,208,0
mvaa 0,192,0
mvaa 0,196,32
mvaa 4,204,32
ta 208,236,4096
mvaa 4,200,0
ta 210,236,4608
mvaa 12,200,4
dl 226,36,52,2048
mvaa 12,204,36
ta 212,236,20480
mvaa 8,196,36
ta 214,236,20992
mvaa 8,192,4
ta 220,237,20480
mvaa 64,192,8
ta 222,237,20992
mvaa 64,196,40
ta 216,237,4096
mvaa 68,204,40
ta 218,237,4608
mvaa 68,200,8
dl 227,36,52,3072
mvaa 76,200,12
s_add_u32 s52,s52,s60
mvaa 76,204,44
mvaa 72,196,44
mvaa 72,192,12
mvaa 128,192,16
mvaa 128,196,48
buffer_load_dword a187,v241,s[48:51],s55 offen
mvaa 132,204,48
s_add_u32 s55,s55,s59
mvaa 132,200,16
mvaa 140,200,20
mvaa 140,204,52
mvaa 136,196,52
mvaa 136,192,20
buffer_load_dword a190,v241,s[48:51],s55 offen
mvaa 192,192,24
s_add_u32 s55,s55,s59
mvaa 192,196,56
mvaa 196,204,56
s_add_u32 s64,s64,1
mvaa 196,200,24
mvaa 204,200,28
mvaa 204,204,60
mvaa 200,196,60
s_waitcnt vmcnt(10) lgkmcnt(0)
s_barrier
mvaa 200,192,28
s_branch L_k_head
L_k_exit:
global_load_dwordx4 a[160:163],v244,s[30:31] offset:0
s_add_u32 m0,s62,32768
mvaa 216,208,92
s_cmp_lt_u32 s12,2
s_cselect_b32 s86,s22,s23
ta 32,234,0
s_and_b32 s87,s12,1
mvaa 216,212,124
s_lshl_b32 s86,s86,11
ta 34,234,512
s_lshl_b32 s87,s87,17
mvaa 220,220,124
s_add_u32 s86,s86,s87
ra 0,232,0
s_mov_b64 s[36:37],s[68:69]
mvaa 220,216,92
s_mov_b32 s38,4194304
ta 36,235,0
s_mov_b32 s52,s86
mvaa 212,216,88
s_mov_b32 s56,65536
ta 38,235,512
v_accvgpr_read_b32 v224,a242
mvaa 212,220,120
dl 228,40,53,0
mvaa 208,212,120
s_lshl_b32 s88,s18,1
ra 4,232,2048
v_accvgpr_read_b32 v225,a243
mvaa 208,208,88
s_add_u32 s88,s88,s12
ra 8,232,4096
v_accvgpr_read_b32 v226,a244
mvaa 152,208,84
s_mov_b32 s91,196608
ra 12,232,6144
v_accvgpr_read_b32 v227,a245
mvaa 152,212,116
s_lshl_b32 s88,s88,7
ra 16,232,8192
s_lshl_b32 s89,s18,6
mvaa 156,220,116
s_mov_b32 s90,6291456
ra 20,232,10240
s_mov_b32 s60,-65408
mvaa 156,216,84
dl 229,40,53,1024
mvaa 148,216,80
s_add_u32 s53,s53,s57
ra 24,232,12288
s_bfe_u32 s4,s21,65536
mvaa 148,220,112
s_lshl_b32 s4,s4,6
ra 28,232,14336
s_lshl_b32 s87,s20,2
mvaa 144,212,112
s_mov_b32 s26,32768
ta 40,236,0
s_mov_b32 s27,131072
mvaa 144,208,80
s_add_u32 s61,s98,32768
ta 42,236,512
s_mov_b32 s63,65536
mvaa 88,208,76
s_cmp_ge_u32 s63,65536
s_cselect_b32 s99,-2,1
ta 44,237,0
s_lshl_b32 s99,s99,15
mvaa 88,212,108
dl 230,44,54,2048
mvaa 92,220,108
s_mov_b32 s67,2048
ta 46,237,512
s_mov_b32 s66,8
mvaa 92,216,76
s_mov_b32 s64,1
ta 48,234,4096
mvaa 84,216,72
s_bitcmp1_b32 s12,0
s_cselect_b64 vcc,-1,0
ta 50,234,4608
mvaa 84,220,104
ta 52,235,4096
mvaa 80,212,104
ta 54,235,4608
mvaa 80,208,72
dl 231,44,54,3072
s_add_u32 m0,s62,49152
mvaa 24,208,68
s_add_u32 s54,s54,s57
ta 56,236,4096
mvaa 24,212,100
s_cmp_lt_u32 s12,2
s_cselect_b32 s57,262144,s91
ta 58,236,4608
mvaa 28,220,100
ta 60,237,4096
mvaa 28,216,68
ta 62,237,4608
mvaa 20,216,64
mvaa 20,220,96
dl 228,40,53,0
mvaa 16,212,96
v_accvgpr_read_b32 v228,a246
mvaa 16,208,64
s_waitcnt lgkmcnt(5)
maaa 128,32,0
maaa 132,36,0
maaa 140,36,4
dl 229,40,53,1024
maaa 136,32,4
s_waitcnt lgkmcnt(0)
s_barrier
mvaa 32,32,8
s_add_u32 s53,s53,s58
ta 96,234,16384
v_accvgpr_read_b32 v229,a247
mvaa 36,36,8
s_cmp_lt_u32 s12,2
s_cselect_b32 s42,8388608,s90
ta 98,234,16896
mvaa 44,36,12
s_cmp_lt_u32 s12,2
s_cselect_b32 s53,s88,s89
ra 64,233,0
s_and_b32 s89,s98,8192
mvaa 40,32,12
dl 230,44,54,2048
mvaa 96,32,16
s_bitcmp1_b32 s21,0
s_cselect_b32 s90,s57,0
ta 100,235,16384
s_add_u32 s53,s53,s90
mvaa 100,36,16
s_cmp_lt_u32 s12,2
s_cselect_b64 s[40:41],s[72:73],s[80:81]
ta 102,235,16896
v_accvgpr_read_b32 v230,a248
mvaa 108,36,20
s_lshl_b32 s90,s90,1
ra 68,233,2048
s_add_u32 s91,s89,65536
mvaa 104,32,20
s_bfe_u32 s88,s21,65539
ra 72,233,4096
s_lshl_b32 s88,s88,1
mvaa 160,32,24
dl 231,44,54,3072
mvaa 164,36,24
s_add_u32 m0,s98,65536
ra 76,233,6144
s_add_u32 s54,s54,s58
mvaa 172,36,28
s_cmp_eq_u32 s12,3
s_cselect_b64 s[44:45],s[68:69],s[40:41]
ra 80,233,8192
v_accvgpr_read_b32 v231,a249
mvaa 168,32,28
s_cmp_eq_u32 s12,3
s_cselect_b32 s46,0,s42
ra 84,233,10240
s_add_u32 s58,s57,s90
mvaa 184,40,28
s_cmp_eq_u32 s12,3
s_cselect_b32 s54,0,s53
ra 88,233,12288
s_sub_u32 s57,s57,s90
mvaa 188,44,28
global_load_dwordx4 a[168:171],v244,s[30:31] offset:2048
mvaa 180,44,24
L_vs2:
s_cbranch_execz L_vs10
s_add_u32 s90,s89,65536
ta 104,236,16384
s_add_u32 s88,s88,s87
mvaa 176,40,24
s_and_b32 s87,s12,1
ra 92,233,14336
s_add_u32 s88,s88,s87
mvaa 120,40,20
s_mul_i32 s88,s88,524288
ta 106,236,16896
s_and_b32 s87,s21,7
mvaa 124,44,20
s_add_u32 s86,s88,16777216
ta 108,237,16384
s_lshl_b32 s87,s87,16
mvaa 116,44,16
global_load_dwordx4 a[176:179],v244,s[84:85] offset:0
mvaa 112,40,16
s_add_u32 s86,s86,s87
ta 110,237,16896
s_lshr_b32 s87,s12,1
mvaa 56,40,12
s_lshl_b32 s87,s87,15
ta 112,234,20480
s_add_u32 s86,s86,s87
mvaa 60,44,12
s_add_u32 s24,s8,s86
s_addc_u32 s25,s9,0
ta 114,234,20992
s_lshl_b32 s86,%1,5
mvaa 52,44,8
s_lshl_b32 s87,s12,3
ta 116,235,20480
s_add_u32 s86,s86,s87
mvaa 48,40,8
global_load_dwordx4 a[164:167],v244,s[30:31] offset:1024
maaa 152,40,4
s_add_u32 s86,s86,83886080
ta 118,235,20992
s_mov_b32 s88,4096
maaa 156,44,4
s_cmp_lg_u32 s65,0
s_cbranch_scc1 L_k_ec1
L_k_ecr1:
ta 120,236,20480
maaa 148,44,0
s_cmp_eq_u32 s12,0
s_cselect_b32 s65,96,0
ta 122,236,20992
s_or_b32 s65,s65,128
maaa 144,40,0
ta 124,237,20480
mvaa 0,48,0
global_load_dwordx4 a[172:175],v244,s[30:31] offset:3072
mvaa 4,52,0
s_mov_b32 s30,1048577
ta 126,237,20992
s_mov_b32 s31,268435712
mvaa 12,52,4
mvaa 8,48,4
mvaa 64,48,8
mvaa 68,52,8
global_load_dwordx4 a[180:183],v244,s[84:85] offset:1024
mvaa 76,52,12
mvaa 72,48,12
mvaa 128,48,16
mvaa 132,52,16
mvaa 140,52,20
dl 224,36,52,0
mvaa 136,48,20
mvaa 192,48,24
mvaa 196,52,24
mvaa 204,52,28
mvaa 200,48,28
dl 225,36,52,1024
mvaa 216,56,28
mvaa 220,60,28
mvaa 212,60,24
mvaa 208,56,24
mvaa 152,56,20
dl 226,36,52,2048
mvaa 156,60,20
mvaa 148,60,16
mvaa 144,56,16
mvaa 88,56,12
s_waitcnt vmcnt(12) lgkmcnt(0)
s_barrier
mvaa 92,60,12
dl 227,36,52,3072
mvaa 84,60,8
s_add_u32 s52,s52,s56
s_add_u32 m0,s98,69632
ta 32,234,32768
mvaa 80,56,8
ta 34,234,33280
mvaa 24,56,4
ta 36,235,32768
mvaa 28,60,4
ta 38,235,33280
mvaa 20,60,0
dl 224,36,52,0
mvaa 16,56,0
ta 40,236,32768
maaa 128,96,64
ta 42,236,33280
maaa 132,100,64
ta 44,237,32768
maaa 140,100,68
ta 46,237,33280
maaa 136,96,68
dl 225,36,52,1024
mvaa 32,96,72
ta 48,234,36864
mvaa 36,100,72
ta 50,234,37376
mvaa 44,100,76
ta 52,235,36864
mvaa 40,96,76
ta 54,235,37376
mvaa 96,96,80
dl 226,36,52,2048
mvaa 100,100,80
ra 20,232,43008
mvaa 108,100,84
ra 24,232,45056
mvaa 104,96,84
ra 0,232,32768
mvaa 160,96,88
ra 28,232,47104
mvaa 164,100,88
dl 227,36,52,3072
mvaa 172,100,92
s_add_u32 m0,s62,0
ra 4,232,34816
s_add_u32 s52,s52,s60
mvaa 168,96,92
ra 16,232,40960
mvaa 184,104,92
ra 8,232,36864
mvaa 188,108,92
ra 12,232,38912
v_add_u32_e32 v232,s90,v232
mvaa 180,108,88
dl 228,40,53,0
mvaa 176,104,88
ta 60,237,36864
v_xor_b32_e32 v232,s4,v232
mvaa 120,104,84
ta 62,237,37376
mvaa 124,108,84
ta 56,236,36864
mvaa 116,108,80
ta 58,236,37376
mvaa 112,104,80
dl 229,40,53,1024
mvaa 56,104,76
s_add_u32 s53,s53,s57
mvaa 60,108,76
mvaa 52,108,72
mvaa 48,104,72
maaa 152,104,68
dl 230,44,54,2048
maaa 156,108,68
maaa 148,108,64
maaa 144,104,64
mvaa 0,112,64
mvaa 4,116,64
dl 231,44,54,3072
mvaa 12,116,68
s_add_u32 m0,s62,16384
s_add_u32 s54,s54,s57
mvaa 8,112,68
mvaa 64,112,72
mvaa 68,116,72
mvaa 76,116,76
dl 228,40,53,0
mvaa 72,112,76
mvaa 128,112,80
mvaa 132,116,80
mvaa 140,116,84
mvaa 136,112,84
dl 229,40,53,1024
mvaa 192,112,88
s_add_u32 s53,s53,s58
mvaa 196,116,88
mvaa 204,116,92
mvaa 200,112,92
mvaa 216,120,92
dl 230,44,54,2048
mvaa 220,124,92
mvaa 212,124,88
s_waitcnt vmcnt(20) lgkmcnt(0)
s_barrier
mvaa 208,120,88
ra 92,233,47104
mvaa 152,120,84
ta 96,234,49152
mvaa 156,124,84
dl 231,44,54,3072
mvaa 148,124,80
s_add_u32 m0,s98,0
ra 88,233,45056
s_add_u32 s54,s54,s58
mvaa 144,120,80
ta 98,234,49664
mvaa 88,120,76
ta 112,234,53248
mvaa 92,124,76
ta 116,235,53248
mvaa 84,124,72
dl 224,36,52,0
mvaa 80,120,72
ta 118,235,53760
mvaa 24,120,68
ta 114,234,53760
v_subrev_u32_e32 v234,s89,v234
mvaa 28,124,68
ta 100,235,49152
mvaa 20,124,64
ta 102,235,49664
v_subrev_u32_e32 v235,s89,v235
mvaa 16,120,64
dl 225,36,52,1024
maaa 128,32,0
ta 124,237,53248
maaa 132,36,0
ta 126,237,53760
maaa 140,36,4
ta 120,236,53248
maaa 136,32,4
ta 122,236,53760
mvaa 32,32,8
dl 226,36,52,2048
mvaa 36,36,8
ta 104,236,49152
mvaa 44,36,12
ta 106,236,49664
v_subrev_u32_e32 v236,s89,v236
mvaa 40,32,12
ta 108,237,49152
mvaa 56,40,12
ta 110,237,49664
v_subrev_u32_e32 v237,s89,v237
mvaa 60,44,12
dl 227,36,52,3072
mvaa 52,44,8
s_add_u32 s52,s52,s56
s_add_u32 m0,s98,4096
ra 84,233,43008
mvaa 48,40,8
ra 80,233,40960
maaa 152,40,4
ra 72,233,36864
maaa 156,44,4
ra 76,233,38912
maaa 148,44,0
dl 224,36,52,0
maaa 144,40,0
ra 68,233,34816
mvaa 0,48,0
ra 64,233,32768
v_add_u32_e32 v233,s91,v233
mvaa 4,52,0
v_xor_b32_e32 v233,s4,v233
mvaa 12,52,4
mvaa 8,48,4
dl 225,36,52,1024
mvaa 24,56,4
mvaa 28,60,4
mvaa 20,60,0
mvaa 16,56,0
mvaa 220,60,28
dl 226,36,52,2048
s_waitcnt lgkmcnt(2)
mvaa 220,124,92
mvaa 216,120,92
mvaa 216,56,28
mvaa 212,60,24
mvaa 212,124,88
dl 227,36,52,3072
mvaa 208,120,88
s_add_u32 m0,s62,32768
s_add_u32 s52,s52,s60
s_waitcnt vmcnt(25)
zvaa 246,160,176
mvaa 208,56,24
cv 223,222,223
cv 222,220,221
mvaa 204,52,28
cv 221,218,219
zvaa 250,168,176
mvaa 204,116,92
cv 220,216,217
cv 219,214,215
mvaa 200,112,92
dl 228,40,53,0
s_waitcnt vmcnt(25)
mvaa 246,164,180
mvaa 200,48,28
cv 218,212,213
mvaa 250,172,180
mvaa 196,52,24
cv 217,210,211
cv 216,208,209
mvaa 196,116,88
cv 215,206,207
v_cndmask_b32_e64 v246,0,v246,s[30:31]
mvaa 192,112,88
cv 214,204,205
v_cndmask_b32_e64 v250,0,v250,s[30:31]
mvaa 192,48,24
dl 229,40,53,1024
s_lshl_b64 s[30:31],s[30:31],1
v_cndmask_b32_e64 v246,v246,v247,s[30:31]
v_cndmask_b32_e64 v250,v250,v251,s[30:31]
mvaa 188,44,28
s_lshl_b64 s[30:31],s[30:31],1
s_add_u32 s53,s53,s57
v_cndmask_b32_e64 v246,v246,v248,s[30:31]
v_cndmask_b32_e64 v250,v250,v252,s[30:31]
mvaa 188,108,92
s_lshl_b64 s[30:31],s[30:31],1
cv 213,202,203
v_cndmask_b32_e64 v246,v246,v249,s[30:31]
mvaa 184,104,92
v_cndmask_b32_e64 v250,v250,v253,s[30:31]
v_add_f32_dpp v246,v246,v246 quad_perm:[1,0,3,2] row_mask:0xf bank_mask:0xf
mvaa 184,40,28
v_add_f32_dpp v250,v250,v250 quad_perm:[1,0,3,2] row_mask:0xf bank_mask:0xf
v_add_f32_dpp v246,v246,v246 quad_perm:[2,3,0,1] row_mask:0xf bank_mask:0xf
mvaa 180,44,24
dl 230,44,54,2048
v_add_f32_dpp v250,v250,v250 quad_perm:[2,3,0,1] row_mask:0xf bank_mask:0xf
mvaa 180,108,88
v_add_f32_dpp v246,v246,v246 row_half_mirror row_mask:0xf bank_mask:0xf
v_add_f32_dpp v250,v250,v250 row_half_mirror row_mask:0xf bank_mask:0xf
mvaa 176,104,88
v_add_f32_dpp v246,v246,v246 row_mirror row_mask:0xf bank_mask:0xf
v_add_f32_dpp v250,v250,v250 row_mirror row_mask:0xf bank_mask:0xf
mvaa 176,40,24
cv 212,200,201
cv 211,198,199
mvaa 172,36,28
v_permlane16_swap_b32_e32 v246,v250
mvaa 172,100,92
dl 231,44,54,3072
v_add_f32_e32 v246,v246,v250
v_mov_b32_e32 v250,v246
mvaa 168,96,92
s_add_u32 s54,s54,s57
v_permlane32_swap_b32_e32 v246,v250
mvaa 168,32,28
v_add_f32_e32 v246,v246,v250
v_lshrrev_b32_e32 v250,6,v244
mvaa 164,36,24
v_mul_f32_e32 v246,0xbfb8aa3b,v246
cv 210,196,197
mvaa 164,100,88
ex 246
mvaa 160,96,88
buffer_load_dword a242,v241,s[48:51],s55 offen
mvaa 160,32,24
s_add_u32 s55,s55,s59
v_add_f32_e32 v246,1.0,v246
cv 209,194,195
mvaa 156,60,20
v_rcp_f32_e32 v246,v246
mvaa 156,124,84
s_waitcnt vmcnt(17) lgkmcnt(0)
s_barrier
mvaa 152,120,84
s_add_u32 m0,s62,49152
cv 208,192,193
cv 207,190,191
mvaa 152,56,20
dl 228,40,53,0
mvaa 148,60,16
cv 206,188,189
cv 205,186,187
mvaa 148,124,80
cv 204,184,185
cv 203,182,183
mvaa 144,120,80
cv 202,180,181
cv 201,178,179
mvaa 144,56,16
cv 200,176,177
cv 199,174,175
mvaa 140,52,20
dl 229,40,53,1024
mvaa 140,116,84
s_add_u32 s53,s53,s58
cv 198,172,173
cv 197,170,171
mvaa 136,112,84
ra 4,232,0
cv 196,168,169
mvaa 136,48,20
ta 92,239,12288
cv 195,166,167
mvaa 132,52,16
ta 94,239,12800
cv 194,164,165
mvaa 132,116,80
dl 230,44,54,2048
mvaa 128,112,80
ta 88,238,12288
cv 193,162,163
mvaa 128,48,16
ta 90,238,12800
cv 192,160,161
mvaa 124,44,20
cv 191,158,159
cv 190,156,157
mvaa 124,108,84
cv 189,154,155
cv 188,152,153
mvaa 120,104,84
dl 231,44,54,3072
mvaa 120,40,20
s_add_u32 m0,s98,32768
cv 187,150,151
cv 186,148,149
mvaa 116,44,16
s_add_u32 s54,s54,s58
cv 185,146,147
cv 184,144,145
mvaa 116,108,80
cv 183,142,143
cv 182,140,141
mvaa 112,104,80
cv 181,138,139
cv 180,136,137
mvaa 112,40,16
buffer_load_dword a243,v241,s[48:51],s55 offen
mvaa 108,36,20
s_add_u32 s55,s55,s59
cv 179,134,135
cv 178,132,133
mvaa 108,100,84
cv 177,130,131
cv 176,128,129
mvaa 104,96,84
cv 175,126,127
cv 174,124,125
mvaa 104,32,20
cv 173,122,123
cv 172,120,121
mvaa 100,36,16
dl 224,36,52,0
mvaa 100,100,80
ta 44,235,0
cv 171,118,119
mvaa 96,96,80
ta 46,235,512
cv 170,116,117
mvaa 96,32,16
ta 40,234,0
cv 169,114,115
mvaa 92,60,12
ta 42,234,512
cv 168,112,113
mvaa 92,124,76
dl 225,36,52,1024
mvaa 88,120,76
cv 167,110,111
cv 166,108,109
mvaa 88,56,12
ta 84,237,8192
cv 165,106,107
mvaa 84,60,8
ta 86,237,8704
cv 164,104,105
mvaa 84,124,72
ta 80,236,8192
cv 163,102,103
mvaa 80,120,72
dl 226,36,52,2048
mvaa 80,56,8
ta 82,236,8704
cv 162,100,101
mvaa 76,52,12
ra 16,232,6144
cv 161,98,99
mvaa 76,116,76
ta 60,235,4096
cv 160,96,97
mvaa 72,112,76
ta 56,234,4096
cv 159,94,95
mvaa 72,48,12
dl 227,36,52,3072
mvaa 68,52,8
s_add_u32 m0,s62,0
ta 62,235,4608
s_add_u32 s52,s52,s56
mvaa 68,116,72
ta 58,234,4608
cv 151,78,79
mvaa 64,112,72
ra 12,232,4096
cv 150,76,77
mvaa 64,48,8
ta 52,237,0
cv 158,92,93
mvaa 60,108,76
buffer_load_dword a244,v241,s[48:51],s55 offen
mvaa 56,104,76
s_add_u32 s55,s55,s59
ta 54,237,512
cv 147,70,71
mvaa 52,108,72
ra 8,232,2048
cv 146,68,69
mvaa 48,104,72
ta 48,236,0
cv 145,66,67
mvaa 44,100,76
ta 50,236,512
cv 144,64,65
mvaa 40,96,76
buffer_load_dword a245,v241,s[48:51],s55 offen
mvaa 36,100,72
s_add_u32 s55,s55,s59
cv 143,62,63
cv 142,60,61
mvaa 32,96,72
cv 141,58,59
cv 140,56,57
mvaa 28,124,68
cv 139,54,55
cv 138,52,53
mvaa 24,120,68
cv 137,50,51
cv 136,48,49
mvaa 20,124,64
s_mov_b64 exec,65537
buffer_store_dword v246,v250,s[8:11],s86 offen sc0 sc1
s_mov_b64 exec,-1
mvaa 16,120,64
cv 135,46,47
cv 134,44,45
mvaa 12,116,68
ta 76,235,8192
cv 133,42,43
mvaa 8,112,68
L_vs3:
s_cbranch_execz L_vs11
ta 78,235,8704
cv 132,40,41
mvaa 4,116,64
ta 72,234,8192
cv 131,38,39
mvaa 0,112,64
ta 74,234,8704
cv 130,36,37
maaa 156,108,68
cv 129,34,35
cv 128,32,33
maaa 152,104,68
cv 127,30,31
cv 126,28,29
maaa 148,108,64
cv 125,26,27
cv 124,24,25
maaa 144,104,64
v_accvgpr_read_b32 v63,a159
v_accvgpr_read_b32 v62,a158
maaa 140,100,68
v_accvgpr_read_b32 v61,a157
v_accvgpr_read_b32 v60,a156
maaa 136,96,68
v_accvgpr_read_b32 v59,a155
v_accvgpr_read_b32 v58,a154
maaa 132,100,64
v_accvgpr_read_b32 v57,a153
v_accvgpr_read_b32 v56,a152
maaa 128,96,64
ta 68,237,4096
ta 70,237,4608
s_waitcnt lgkmcnt(4)
zaaa 236,40,4
cv 56,56,57
cv 57,58,59
zaaa 220,44,4
ta 64,236,4096
ta 66,236,4608
zaaa 216,44,8
cv 58,60,61
cv 59,62,63
zaaa 232,40,8
buffer_store_dwordx4 v[56:59],v244,s[24:27],0 offen offset:3072 sc0 sc1
zaaa 228,40,12
v_accvgpr_read_b32 v55,a151
v_accvgpr_read_b32 v54,a150
zaaa 212,44,12
v_accvgpr_read_b32 v53,a149
v_accvgpr_read_b32 v52,a148
zaaa 208,44,16
v_accvgpr_read_b32 v51,a147
v_accvgpr_read_b32 v50,a146
zaaa 224,40,16
v_accvgpr_read_b32 v49,a145
v_accvgpr_read_b32 v48,a144
zaaa 192,48,16
cv 48,48,49
cv 49,50,51
zaaa 176,52,16
cv 50,52,53
s_waitcnt vmcnt(26) lgkmcnt(0)
s_barrier
zaaa 180,52,12
dl 228,40,53,0
cv 51,54,55
zaaa 196,48,12
ra 24,233,0
v_accvgpr_read_b32 v47,a143
zaaa 200,48,8
ra 28,233,2048
v_accvgpr_read_b32 v46,a142
v_accvgpr_read_b32 v45,a141
zaaa 184,52,8
ra 32,233,4096
ra 36,233,6144
zaaa 188,52,4
v_accvgpr_read_b32 v43,a139
v_accvgpr_read_b32 v42,a138
zaaa 204,48,4
dl 229,40,53,1024
ta 96,236,24576
zaaa 172,56,4
s_add_u32 s53,s53,s57
ta 98,236,25088
v_accvgpr_read_b32 v41,a137
zaaa 156,60,4
ta 100,237,24576
v_accvgpr_read_b32 v39,a135
zaaa 152,60,8
ta 102,237,25088
ta 104,238,28672
zaaa 168,56,8
v_accvgpr_read_b32 v38,a134
v_accvgpr_read_b32 v35,a131
zaaa 164,56,12
dl 230,44,54,2048
ta 106,238,29184
zaaa 148,60,12
ta 108,239,28672
v_accvgpr_read_b32 v34,a130
zaaa 144,60,16
ta 110,239,29184
v_accvgpr_read_b32 v33,a129
zaaa 160,56,16
ta 44,235,16384
ta 46,235,16896
ta 40,234,16384
ta 42,234,16896
ta 52,237,16384
ta 54,237,16896
ta 48,236,16384
ta 50,236,16896
ta 60,235,20480
ta 62,235,20992
ta 56,234,20480
ta 58,234,20992
v_accvgpr_read_b32 v32,a128
zaaa 128,64,16
v_accvgpr_read_b32 v37,a133
v_accvgpr_read_b32 v36,a132
zaaa 112,68,16
dl 231,44,54,3072
zaaa 116,68,12
s_add_u32 m0,s61,4096
v_accvgpr_read_b32 v40,a136
v_accvgpr_read_b32 v44,a140
zaaa 132,64,12
s_add_u32 s54,s54,s57
cv 251,42,43
cv 250,40,41
zaaa 136,64,8
s_add_u32 s61,s63,s98
cv 253,46,47
cv 252,44,45
zaaa 120,68,8
s_add_u32 s63,s63,s99
cv 249,38,39
cv 247,34,35
)"
R"(zaaa 124,68,4
buffer_store_dwordx4 v[48:51],v244,s[24:27],0 offen offset:2048 sc0 sc1
zaaa 140,64,4
cv 246,32,33
cv 248,36,37
zvaa 40,72,4
ta 68,237,20480
cv 123,22,23
zvaa 44,76,4
ta 70,237,20992
cv 121,18,19
zvaa 52,76,8
ta 64,236,20480
cv 120,16,17
zvaa 48,72,8
dl 224,36,52,0
zvaa 56,72,12
ta 66,236,20992
cv 122,20,21
zvaa 60,76,12
cv 119,14,15
cv 118,12,13
zvaa 68,76,16
cv 117,10,11
cv 116,8,9
zvaa 64,72,16
cv 157,90,91
cv 156,88,89
zvaa 32,80,16
dl 225,36,52,1024
ta 76,235,24576
ta 78,235,25088
zvaa 36,84,16
ta 72,234,24576
ta 74,234,25088
zvaa 28,84,12
cv 153,82,83
cv 152,80,81
zvaa 24,80,12
cv 155,86,87
cv 154,84,85
zvaa 16,80,8
cv 149,74,75
cv 148,72,73
zvaa 20,84,8
dl 226,36,52,2048
zvaa 12,84,4
v_add_u32_e32 v232,s99,v232
cv 115,6,7
zvaa 8,80,4
cv 114,4,5
cv 113,2,3
zvaa 76,4,88
cv 112,0,1
zvaa 96,4,92
zvaa 100,8,92
dl 227,36,52,3072
zvaa 80,8,88
s_add_u32 s52,s52,s60
zvaa 84,12,88
zvaa 104,12,92
zvaa 108,16,92
s_waitcnt vmcnt(23) lgkmcnt(0)
s_barrier
zvaa 88,16,88
s_branch L_kvx
L_vtile:
L_vs4:
s_cbranch_execz L_vs8
s_waitcnt vmcnt(12)
s_barrier
ta 64,234,0
ta 66,234,512
ra 0,232,0
ta 68,235,0
ta 70,235,512
ra 4,232,2048
ra 16,232,8192
ra 20,232,10240
s_add_u32 m0,s62,32768
v_mov_b32_e32 v212,0
dl 228,40,53,0
v_mov_b32_e32 v213,0
v_mov_b32_e32 v214,0
v_mov_b32_e32 v215,0
v_mov_b32_e32 v220,0
v_mov_b32_e32 v221,0
v_mov_b32_e32 v222,0
v_mov_b32_e32 v223,0
v_mov_b32_e32 v216,0
v_mov_b32_e32 v217,0
v_mov_b32_e32 v218,0
v_mov_b32_e32 v219,0
v_mov_b32_e32 v208,0
v_mov_b32_e32 v209,0
v_mov_b32_e32 v210,0
v_mov_b32_e32 v211,0
v_mov_b32_e32 v148,0
v_mov_b32_e32 v149,0
v_mov_b32_e32 v150,0
v_mov_b32_e32 v151,0
v_mov_b32_e32 v156,0
s_waitcnt lgkmcnt(5)
zaaa 128,0,64
s_lshl_b32 s4,%1,1
ta 72,236,0
ta 74,236,512
ta 76,237,0
ta 78,237,512
ta 80,234,4096
s_waitcnt lgkmcnt(8)
zaaa 136,0,68
s_add_u32 s4,s4,s12
ta 82,234,4608
v_mov_b32_e32 v157,0
s_waitcnt lgkmcnt(6)
zaaa 140,4,68
s_mul_i32 s4,s4,1280
ta 84,235,4096
ta 86,235,4608
zaaa 132,4,64
dl 229,40,53,1024
zvaa 96,16,64
s_add_u32 s53,s53,s57
v_mov_b32_e32 v158,0
v_mov_b32_e32 v159,0
zvaa 104,16,68
s_mov_b32 s55,s4
v_mov_b32_e32 v152,0
v_mov_b32_e32 v153,0
zvaa 108,20,68
s_mul_i32 s4,%1,1792
v_mov_b32_e32 v154,0
v_mov_b32_e32 v155,0
zvaa 100,20,64
dl 230,44,54,2048
s_waitcnt lgkmcnt(6)
zvaa 116,20,72
s_lshl_b32 s88,%1,2
ra 32,233,0
v_mov_b32_e32 v144,0
s_waitcnt lgkmcnt(5)
zvaa 124,20,76
s_waitcnt vmcnt(11)
s_barrier
ra 8,232,4096
zvaa 120,16,76
s_cmp_eq_u32 s12,2
s_cselect_b32 s55,s4,s55
ra 12,232,6144
v_mov_b32_e32 v145,0
zvaa 112,16,72
dl 231,44,54,3072
zaaa 148,4,72
s_add_u32 m0,s98,36864
ra 24,232,12288
s_add_u32 s54,s54,s57
zaaa 156,4,76
s_lshl_b32 s4,%1,14
ra 28,232,14336
v_mov_b32_e32 v146,0
zaaa 152,0,76
s_add_u32 s88,s88,s12
ra 36,233,2048
v_mov_b32_e32 v147,0
zaaa 144,0,72
dl 224,36,52,0
s_waitcnt lgkmcnt(6)
zvaa 0,0,80
s_add_u32 s5,s4,16384
ra 40,233,4096
v_mov_b32_e32 v84,0
zvaa 8,0,84
s_cmp_eq_u32 s12,3
s_cselect_b32 s55,s4,s55
ra 44,233,6144
v_mov_b32_e32 v85,0
zvaa 12,4,84
s_mov_b32 s4,10
ra 48,233,8192
v_mov_b32_e32 v86,0
zvaa 4,4,80
dl 225,36,52,1024
zvaa 128,16,80
s_lshl_b32 s88,s88,12
ra 52,233,10240
v_mov_b32_e32 v87,0
zvaa 136,16,84
s_xor_b32 s87,s19,15
v_mov_b32_e32 v92,0
v_mov_b32_e32 v93,0
zvaa 140,20,84
s_cmp_eq_u32 s12,2
s_cselect_b32 s4,14,s4
ra 56,233,12288
v_mov_b32_e32 v94,0
zvaa 132,20,80
dl 226,36,52,2048
s_waitcnt lgkmcnt(5)
zvaa 32,8,64
s_add_u32 s30,s68,s88
s_addc_u32 s31,s69,0
v_mov_b32_e32 v95,0
v_mov_b32_e32 v88,0
zvaa 40,8,68
s_lshl_b32 s22,s87,7
ra 60,233,14336
v_mov_b32_e32 v89,0
zvaa 44,12,68
s_lshl_b32 s87,s87,2
v_mov_b32_e32 v90,0
v_mov_b32_e32 v91,0
zvaa 36,12,64
dl 227,36,52,3072
zvaa 160,24,64
s_add_u32 m0,s62,49152
s_add_u32 s52,s52,s60
v_mov_b32_e32 v80,0
zvaa 168,24,68
s_lshl_b32 s88,s19,2
v_mov_b32_e32 v81,0
v_mov_b32_e32 v82,0
zvaa 172,28,68
s_cmp_eq_u32 s12,3
s_cselect_b32 s4,10,s4
v_mov_b32_e32 v83,0
v_mov_b32_e32 v20,0
zvaa 164,28,64
dl 228,40,53,0
zvaa 180,28,72
s_add_u32 s87,s87,s12
v_mov_b32_e32 v21,0
v_mov_b32_e32 v22,0
zvaa 188,28,76
s_add_u32 s88,s88,s12
v_mov_b32_e32 v23,0
v_mov_b32_e32 v28,0
zvaa 184,24,76
s_waitcnt vmcnt(13)
s_barrier
ta 96,234,16384
ta 98,234,16896
ta 100,235,16384
ta 102,235,16896
zvaa 176,24,72
dl 229,40,53,1024
zvaa 52,12,72
s_lshl_b32 s90,s18,13
v_mov_b32_e32 v29,0
v_mov_b32_e32 v30,0
zvaa 60,12,76
s_add_u32 s90,s90,62914560
ta 104,236,16384
v_mov_b32_e32 v31,0
zvaa 56,8,76
s_mul_i32 s14,s87,327680
ta 106,236,16896
v_mov_b32_e32 v24,0
zvaa 48,8,72
dl 230,44,54,2048
zvaa 64,8,80
s_mul_i32 s13,s88,327680
ta 108,237,16384
v_mov_b32_e32 v25,0
zvaa 72,8,84
s_add_u32 s14,s14,s90
ta 110,237,16896
v_mov_b32_e32 v26,0
zvaa 76,12,84
s_add_u32 s13,s13,s90
v_mov_b32_e32 v27,0
v_mov_b32_e32 v16,0
zvaa 68,12,80
dl 231,44,54,3072
zvaa 192,24,80
s_add_u32 m0,s98,65536
v_mov_b32_e32 v17,0
v_mov_b32_e32 v18,0
zvaa 200,24,84
s_mul_i32 s90,s18,524288
ta 208,236,4096
v_mov_b32_e32 v19,0
zvaa 204,28,84
s_lshl_b32 s91,s12,12
ta 210,236,4608
v_lshlrev_b32_e32 v241,7,v247
zvaa 196,28,80
dl 224,36,52,0
s_waitcnt lgkmcnt(5)
maaa 128,32,96
s_add_u32 s90,s90,s91
ta 216,237,4096
v_mov_b32_e32 v242,134217728
maaa 136,32,100
s_add_u32 s90,s90,16777216
ta 218,237,4608
v_lshrrev_b32_e32 v248,3,v247
maaa 140,36,100
s_cmp_eq_u32 s12,3
s_cselect_b64 s[26:27],-1,0
ta 112,234,20480
v_and_b32_e32 v250,112,v244
maaa 132,36,96
dl 225,36,52,1024
mvaa 32,40,96
s_lshl_b32 s86,s19,1
ta 114,234,20992
v_and_b32_e32 v251,16,v247
mvaa 40,40,100
s_mov_b64 s[48:49],s[72:73]
ta 116,235,20480
v_lshlrev_b32_e32 v251,1,v251
mvaa 44,44,100
s_mov_b32 s50,8388608
ta 118,235,20992
v_mul_u32_u24_e32 v246,6144,v248
mvaa 36,44,96
dl 226,36,52,2048
mvaa 96,48,96
s_mov_b32 s59,655360
v_cmp_gt_u32_e64 s[24:25],s4,v247
v_add_u32_e32 v249,s90,v244
mvaa 104,48,100
s_add_u32 s86,s86,2
v_cndmask_b32_e64 v241,v242,v241,s[24:25]
v_add_u32_e32 v255,16777216,v249
mvaa 108,52,100
s_cmp_eq_u32 s12,2
s_cselect_b64 s[24:25],-1,0
v_accvgpr_write_b32 a240,v249
v_and_b32_e32 v249,48,v247
mvaa 100,52,96
dl 227,36,52,3072
mvaa 160,56,96
s_add_u32 s52,s52,s56
s_add_u32 m0,s98,69632
v_accvgpr_write_b32 a241,v255
mvaa 168,56,100
s_cmp_eq_u32 s12,2
s_cselect_b64 s[48:49],s[80:81],s[48:49]
ta 212,236,20480
v_xor_b32_e32 v249,v249,v250
mvaa 172,60,100
s_cmp_eq_u32 s12,2
s_cselect_b32 s50,6291456,s50
v_xor_b32_e32 v250,v250,v251
v_lshl_add_u32 v252,v248,11,v249
mvaa 164,60,96
dl 224,36,52,0
s_waitcnt lgkmcnt(1)
mvaa 180,60,104
s_cmp_eq_u32 s12,2
s_cselect_b32 s59,458752,s59
v_and_b32_e32 v251,48,v250
v_xor_b32_e32 v253,64,v252
mvaa 188,60,108
s_lshl_b32 s15,1,s86
v_accvgpr_write_b32 a242,v252
v_add_u32_e32 v246,v246,v251
mvaa 184,56,108
s_lshl_b32 s23,s19,7
v_add_u32_e32 v242,15360,v253
v_and_b32_e32 v251,64,v250
mvaa 176,56,104
dl 225,36,52,1024
mvaa 116,52,104
s_or_b32 s86,%1,s12
s_cselect_b32 s65,0,4
v_lshl_add_u32 v248,v251,5,v246
v_xor_b32_e32 v249,64,v251
mvaa 124,52,108
s_cmp_eq_u32 s12,3
s_cselect_b64 s[48:49],s[68:69],s[48:49]
v_mul_u32_u24_e32 v251,768,v251
v_accvgpr_write_b32 a243,v242
mvaa 120,48,108
s_cmp_eq_u32 s12,3
s_cselect_b32 s50,s5,s50
v_add_u32_e32 v242,30720,v252
v_lshl_add_u32 v249,v249,5,v246
mvaa 112,48,104
dl 226,36,52,2048
mvaa 52,44,104
s_cmp_eq_u32 s12,3
s_cselect_b32 s59,1280,s59
v_accvgpr_write_b32 a244,v242
v_add_u32_e32 v246,v246,v251
mvaa 60,44,108
s_add_u32 s53,s53,s58
v_add_u32_e32 v242,46080,v253
v_accvgpr_write_b32 a245,v242
mvaa 56,40,108
s_add_u32 s54,s54,s58
v_add_u32_e32 v243,4096,v246
v_add_u32_e32 v242,0,v248
mvaa 48,40,104
dl 227,36,52,3072
maaa 148,36,104
s_add_u32 s52,s52,s60
ta 214,236,20992
v_cndmask_b32_e64 v242,v228,v242,s[24:25]
maaa 156,36,108
s_mov_b32 s63,32768
ta 220,237,20480
v_cndmask_b32_e64 v242,v242,v243,s[26:27]
maaa 152,32,108
s_mov_b32 s61,s98
ta 222,237,20992
v_add_u32_e32 v243,101376,v246
maaa 144,32,104
s_mov_b32 s64,1
v_and_b32_e32 v246,15,v247
v_and_b32_e32 v246,12,v247
mvaa 0,32,112
v_accvgpr_write_b32 a246,v242
v_add_u32_e32 v242,48128,v249
mvaa 8,32,116
v_cndmask_b32_e64 v242,v229,v242,s[24:25]
v_cndmask_b32_e64 v242,v242,v243,s[26:27]
mvaa 12,36,116
v_lshlrev_b32_e32 v246,5,v246
v_accvgpr_write_b32 a247,v242
mvaa 4,36,112
v_add_u32_e32 v242,96256,v248
v_cndmask_b32_e64 v242,v230,v242,s[24:25]
mvaa 64,40,112
v_and_b32_e32 v248,3,v247
v_lshl_add_u32 v246,v248,3,v246
mvaa 72,40,116
v_accvgpr_write_b32 a248,v242
v_and_b32_e32 v248,24,v247
mvaa 76,44,116
v_add_u32_e32 v242,144384,v249
v_lshl_add_u32 v246,v248,2,v246
mvaa 68,44,112
v_and_b32_e32 v248,32,v247
v_cndmask_b32_e64 v242,v231,v242,s[24:25]
mvaa 128,48,112
v_add_u32_e32 v246,98304,v246
v_accvgpr_write_b32 a249,v242
mvaa 136,48,116
v_lshl_add_u32 v238,v248,5,v246
v_xor_b32_e32 v239,32,v238
mvaa 140,52,116
v_add_u32_e32 v232,32768,v232
v_add_u32_e32 v233,32768,v233
mvaa 132,52,112
mvaa 192,56,112
mvaa 200,56,116
mvaa 204,60,116
mvaa 196,60,112
s_add_u32 s4,s96,L_vrv-L_att_pc
s_addc_u32 s5,s97,0
s_mov_b64 exec,0
s_cmp_eq_u32 s12,0
s_cbranch_scc1 L_vs13
s_cmp_eq_u32 s12,1
s_cbranch_scc1 L_vs14
s_cmp_eq_u32 s12,2
s_cbranch_scc1 L_vs15
s_branch L_vs16
L_vrv:
s_mov_b64 exec,-1
s_sleep 6
s_waitcnt vmcnt(8)
s_waitcnt lgkmcnt(0)
s_barrier
L_v_head:
mvaa 212,28,208
L_vs5:
s_cbranch_execz L_vs9
ra 64,232,0
mvaa 212,60,212
s_add_u32 m0,s62,0
ta 192,234,32768
mvaa 220,60,220
dl 228,40,53,0
mvaa 220,28,216
ta 194,234,33280
s_nop 3
mvaa 216,24,216
ra 96,233,0
mvaa 216,56,220
ta 196,234,49152
mvaa 208,56,212
ta 198,234,49664
mvaa 208,24,208
ta 204,235,49152
mvaa 148,20,208
dl 229,40,53,1024
mvaa 148,52,212
s_add_u32 s53,s53,s57
ta 206,235,49664
mvaa 156,52,220
ta 200,235,32768
mvaa 156,20,216
ta 202,235,33280
mvaa 152,16,216
ra 68,232,2048
mvaa 152,48,220
ra 100,233,2048
mvaa 144,48,212
ra 72,232,4096
mvaa 144,16,208
dl 230,44,54,2048
mvaa 84,12,208
ra 104,233,4096
mvaa 84,44,212
ra 76,232,6144
mvaa 92,44,220
ra 108,233,6144
mvaa 92,12,216
ra 80,232,8192
mvaa 88,8,216
ra 112,233,8192
mvaa 88,40,220
dl 231,44,54,3072
mvaa 80,40,212
s_add_u32 s54,s54,s57
ra 84,232,10240
mvaa 80,8,208
ra 116,233,10240
mvaa 20,4,208
ra 88,232,12288
mvaa 20,36,212
ra 120,233,12288
mvaa 28,36,220
ra 92,232,14336
mvaa 28,4,216
s_add_u32 m0,s62,16384
ra 124,233,14336
mvaa 24,0,216
dl 228,40,53,0
mvaa 24,32,220
mvaa 16,32,212
mvaa 16,0,208
s_waitcnt lgkmcnt(8)
maaa 128,64,192
maaa 128,96,196
maaa 136,96,204
dl 229,40,53,1024
maaa 136,64,200
s_add_u32 s53,s53,s58
maaa 140,68,200
maaa 140,100,204
maaa 132,100,196
maaa 132,68,192
mvaa 32,72,192
mvaa 32,104,196
dl 230,44,54,2048
mvaa 40,104,204
mvaa 40,72,200
s_waitcnt lgkmcnt(0)
mvaa 44,76,200
ta 208,236,32768
mvaa 44,108,204
ta 210,236,33280
mvaa 36,108,196
ta 212,236,49152
mvaa 36,76,192
dl 231,44,54,3072
mvaa 96,80,192
s_add_u32 s54,s54,s58
ta 214,236,49664
mvaa 96,112,196
ta 220,237,49152
mvaa 104,112,204
ta 222,237,49664
mvaa 104,80,200
ta 216,237,32768
mvaa 108,84,200
ta 218,237,33280
mvaa 108,116,204
mvaa 100,116,196
s_add_u32 m0,s61,0
mvaa 100,84,192
dl 224,36,52,0
mvaa 160,88,192
mvaa 160,120,196
mvaa 168,120,204
mvaa 168,88,200
mvaa 172,92,200
mvaa 172,124,204
mvaa 164,124,196
dl 225,36,52,1024
mvaa 164,92,192
s_waitcnt lgkmcnt(0)
mvaa 180,92,208
mvaa 180,124,212
mvaa 188,124,220
ta 192,234,36864
mvaa 188,92,216
ta 194,234,37376
mvaa 184,88,216
ta 196,234,53248
mvaa 184,120,220
dl 226,36,52,2048
mvaa 176,120,212
ta 198,234,53760
mvaa 176,88,208
ta 204,235,53248
mvaa 116,84,208
ta 206,235,53760
mvaa 116,116,212
ta 200,235,36864
mvaa 124,116,220
ta 202,235,37376
mvaa 124,84,216
mvaa 120,80,216
dl 227,36,52,3072
mvaa 120,112,220
s_add_u32 s52,s52,s56
mvaa 112,112,212
mvaa 112,80,208
mvaa 52,76,208
mvaa 52,108,212
mvaa 60,108,220
mvaa 60,76,216
mvaa 56,72,216
s_add_u32 m0,s61,4096
mvaa 56,104,220
dl 224,36,52,0
mvaa 48,104,212
s_add_u32 s61,s63,s98
mvaa 48,72,208
s_cmp_ge_u32 s63,65536
s_cselect_b32 s99,-2,1
s_lshl_b32 s99,s99,15
maaa 148,68,208
s_add_u32 s63,s63,s99
v_add_u32_e32 v232,s99,v232
v_add_u32_e32 v233,s99,v233
maaa 148,100,212
maaa 156,100,220
maaa 156,68,216
maaa 152,64,216
dl 225,36,52,1024
maaa 152,96,220
maaa 144,96,212
s_waitcnt lgkmcnt(0)
maaa 144,64,208
mvaa 0,64,192
mvaa 0,96,196
mvaa 8,96,204
ta 208,236,36864
mvaa 8,64,200
ta 210,236,37376
mvaa 12,68,200
dl 226,36,52,2048
mvaa 12,100,204
ta 212,236,53248
mvaa 4,100,196
ta 214,236,53760
mvaa 4,68,192
ta 220,237,53248
mvaa 64,72,192
ta 222,237,53760
mvaa 64,104,196
ta 216,237,36864
mvaa 72,104,204
ta 218,237,37376
mvaa 72,72,200
dl 227,36,52,3072
mvaa 76,76,200
s_add_u32 s52,s52,s60
mvaa 76,108,204
mvaa 68,108,196
mvaa 68,76,192
mvaa 128,80,192
mvaa 128,112,196
buffer_load_dword a184,v241,s[48:51],s55 offen
mvaa 136,112,204
s_add_u32 s55,s55,s59
mvaa 136,80,200
mvaa 140,84,200
mvaa 140,116,204
mvaa 132,116,196
mvaa 132,84,192
buffer_load_dword a185,v241,s[48:51],s55 offen
mvaa 192,88,192
s_add_u32 s55,s55,s59
mvaa 192,120,196
mvaa 200,120,204
mvaa 200,88,200
mvaa 204,92,200
mvaa 204,124,204
s_cmp_eq_u32 s64,3
mvaa 196,124,196
s_waitcnt vmcnt(10) lgkmcnt(0)
s_barrier
mvaa 196,92,192
s_cbranch_scc1 L_v_exit
mvaa 212,92,208
ra 0,232,0
mvaa 212,124,212
s_add_u32 m0,s62,32768
ta 192,234,0
mvaa 220,124,220
dl 228,40,53,0
mvaa 220,92,216
ta 194,234,512
s_nop 3
mvaa 216,88,216
ra 32,233,0
mvaa 216,120,220
ta 196,234,16384
mvaa 208,120,212
ta 198,234,16896
mvaa 208,88,208
ta 204,235,16384
mvaa 148,84,208
dl 229,40,53,1024
mvaa 148,116,212
s_add_u32 s53,s53,s57
ta 206,235,16896
mvaa 156,116,220
ta 200,235,0
mvaa 156,84,216
ta 202,235,512
mvaa 152,80,216
ra 4,232,2048
mvaa 152,112,220
ra 36,233,2048
mvaa 144,112,212
ra 8,232,4096
mvaa 144,80,208
dl 230,44,54,2048
mvaa 84,76,208
ra 40,233,4096
mvaa 84,108,212
ra 12,232,6144
mvaa 92,108,220
ra 44,233,6144
mvaa 92,76,216
ra 16,232,8192
mvaa 88,72,216
ra 48,233,8192
mvaa 88,104,220
dl 231,44,54,3072
mvaa 80,104,212
s_add_u32 s54,s54,s57
ra 20,232,10240
mvaa 80,72,208
ra 52,233,10240
mvaa 20,68,208
ra 24,232,12288
mvaa 20,100,212
ra 56,233,12288
mvaa 28,100,220
ra 28,232,14336
mvaa 28,68,216
s_add_u32 m0,s62,49152
ra 60,233,14336
mvaa 24,64,216
dl 228,40,53,0
mvaa 24,96,220
mvaa 16,96,212
mvaa 16,64,208
s_waitcnt lgkmcnt(8)
maaa 128,0,192
maaa 128,32,196
maaa 136,32,204
dl 229,40,53,1024
maaa 136,0,200
s_add_u32 s53,s53,s58
maaa 140,4,200
maaa 140,36,204
maaa 132,36,196
maaa 132,4,192
mvaa 32,8,192
mvaa 32,40,196
dl 230,44,54,2048
mvaa 40,40,204
mvaa 40,8,200
s_waitcnt lgkmcnt(0)
mvaa 44,12,200
ta 208,236,0
mvaa 44,44,204
ta 210,236,512
mvaa 36,44,196
ta 212,236,16384
mvaa 36,12,192
dl 231,44,54,3072
mvaa 96,16,192
s_add_u32 s54,s54,s58
ta 214,236,16896
mvaa 96,48,196
ta 220,237,16384
mvaa 104,48,204
ta 222,237,16896
mvaa 104,16,200
ta 216,237,0
mvaa 108,20,200
ta 218,237,512
mvaa 108,52,204
mvaa 100,52,196
s_add_u32 m0,s61,0
mvaa 100,20,192
dl 224,36,52,0
mvaa 160,24,192
s_lshl_b32 s86,s21,2
s_add_u32 s86,s86,s12
s_mul_i32 s86,s86,384
mvaa 160,56,196
s_add_u32 s86,s86,4096
v_lshl_add_u32 v253,v244,3,s86
mvaa 168,56,204
s_mov_b64 exec,7
global_load_dword a188,v253,s[96:97]
s_mov_b64 exec,-1
mvaa 168,24,200
mvaa 172,28,200
mvaa 172,60,204
mvaa 164,60,196
dl 225,36,52,1024
mvaa 164,28,192
s_waitcnt lgkmcnt(0)
mvaa 180,28,208
mvaa 180,60,212
mvaa 188,60,220
ta 192,234,4096
mvaa 188,28,216
ta 194,234,4608
mvaa 184,24,216
ta 196,234,20480
mvaa 184,56,220
dl 226,36,52,2048
mvaa 176,56,212
ta 198,234,20992
mvaa 176,24,208
ta 204,235,20480
mvaa 116,20,208
ta 206,235,20992
mvaa 116,52,212
ta 200,235,4096
mvaa 124,52,220
ta 202,235,4608
mvaa 124,20,216
mvaa 120,16,216
dl 227,36,52,3072
mvaa 120,48,220
s_add_u32 s52,s52,s56
mvaa 112,48,212
mvaa 112,16,208
mvaa 52,12,208
mvaa 52,44,212
s_bitcmp1_b32 s65,s64
s_cbranch_scc1 L_v_ev
L_v_evr:
mvaa 60,44,220
mvaa 60,12,216
mvaa 56,8,216
s_add_u32 m0,s61,4096
mvaa 56,40,220
dl 224,36,52,0
mvaa 48,40,212
s_add_u32 s61,s63,s98
mvaa 48,8,208
s_cmp_ge_u32 s63,65536
s_cselect_b32 s99,-2,1
s_lshl_b32 s99,s99,15
maaa 148,4,208
s_add_u32 s63,s63,s99
v_add_u32_e32 v232,s99,v232
v_add_u32_e32 v233,s99,v233
maaa 148,36,212
maaa 156,36,220
maaa 156,4,216
maaa 152,0,216
dl 225,36,52,1024
maaa 152,32,220
maaa 144,32,212
s_waitcnt lgkmcnt(0)
maaa 144,0,208
mvaa 0,0,192
mvaa 0,32,196
mvaa 8,32,204
ta 208,236,4096
mvaa 8,0,200
ta 210,236,4608
mvaa 12,4,200
dl 226,36,52,2048
mvaa 12,36,204
ta 212,236,20480
mvaa 4,36,196
ta 214,236,20992
mvaa 4,4,192
ta 220,237,20480
mvaa 64,8,192
ta 222,237,20992
mvaa 64,40,196
ta 216,237,4096
mvaa 72,40,204
ta 218,237,4608
mvaa 72,8,200
dl 227,36,52,3072
mvaa 76,12,200
s_add_u32 s52,s52,s60
mvaa 76,44,204
mvaa 68,44,196
mvaa 68,12,192
mvaa 128,16,192
mvaa 128,48,196
buffer_load_dword a187,v241,s[48:51],s55 offen
mvaa 136,48,204
s_add_u32 s55,s55,s59
mvaa 136,16,200
mvaa 140,20,200
mvaa 140,52,204
mvaa 132,52,196
mvaa 132,20,192
buffer_load_dword a190,v241,s[48:51],s55 offen
mvaa 192,24,192
s_add_u32 s55,s55,s59
mvaa 192,56,196
mvaa 200,56,204
s_add_u32 s64,s64,1
mvaa 200,24,200
mvaa 204,28,200
mvaa 204,60,204
mvaa 196,60,196
s_waitcnt vmcnt(10) lgkmcnt(0)
s_barrier
mvaa 196,28,192
s_branch L_v_head
L_v_exit:
global_load_dwordx4 a[160:163],v244,s[30:31] offset:0
s_add_u32 m0,s62,32768
mvaa 212,92,208
s_cmp_lt_u32 s12,2
s_cselect_b32 s86,s22,s23
ta 32,234,0
s_and_b32 s87,s12,1
mvaa 212,124,212
s_lshl_b32 s86,s86,11
ta 34,234,512
s_lshl_b32 s87,s87,17
mvaa 220,124,220
s_add_u32 s86,s86,s87
ra 0,232,0
s_mov_b64 s[36:37],s[68:69]
mvaa 220,92,216
s_mov_b32 s38,4194304
ta 36,235,0
s_mov_b32 s52,s86
mvaa 216,88,216
s_mov_b32 s56,65536
ta 38,235,512
v_accvgpr_read_b32 v224,a242
mvaa 216,120,220
dl 228,40,53,0
mvaa 208,120,212
s_lshl_b32 s88,s18,1
ra 4,232,2048
v_accvgpr_read_b32 v225,a243
mvaa 208,88,208
s_add_u32 s88,s88,s12
ra 8,232,4096
v_accvgpr_read_b32 v226,a244
mvaa 148,84,208
s_mov_b32 s91,196608
ra 12,232,6144
v_accvgpr_read_b32 v227,a245
mvaa 148,116,212
s_lshl_b32 s88,s88,7
ra 16,232,8192
s_lshl_b32 s89,s18,6
mvaa 156,116,220
s_mov_b32 s90,6291456
ra 20,232,10240
s_mov_b32 s60,-65408
mvaa 156,84,216
dl 229,40,53,1024
mvaa 152,80,216
s_add_u32 s53,s53,s57
ra 24,232,12288
s_bfe_u32 s4,s21,65536
mvaa 152,112,220
s_lshl_b32 s4,s4,6
ra 28,232,14336
s_lshl_b32 s87,s20,2
mvaa 144,112,212
s_mov_b32 s26,32768
ta 40,236,0
s_mov_b32 s27,131072
mvaa 144,80,208
s_add_u32 s61,s98,32768
ta 42,236,512
s_mov_b32 s63,65536
mvaa 84,76,208
s_cmp_ge_u32 s63,65536
s_cselect_b32 s99,-2,1
ta 44,237,0
s_lshl_b32 s99,s99,15
mvaa 84,108,212
dl 230,44,54,2048
mvaa 92,108,220
s_cmp_eq_u32 s12,0
s_cselect_b32 s65,96,0
ta 46,237,512
)"
R"(s_mov_b32 s67,2048
mvaa 92,76,216
s_mov_b32 s66,8
ta 48,234,4096
s_mov_b32 s64,1
mvaa 88,72,216
s_or_b32 s65,s65,128
ta 50,234,4608
mvaa 88,104,220
s_bitcmp1_b32 s12,0
s_cselect_b64 vcc,-1,0
ta 52,235,4096
mvaa 80,104,212
ta 54,235,4608
mvaa 80,72,208
dl 231,44,54,3072
s_add_u32 m0,s62,49152
mvaa 20,68,208
s_add_u32 s54,s54,s57
ta 56,236,4096
mvaa 20,100,212
s_cmp_lt_u32 s12,2
s_cselect_b32 s57,262144,s91
ta 58,236,4608
mvaa 28,100,220
L_vs6:
s_cbranch_execz L_vs10
ta 60,237,4096
mvaa 28,68,216
ta 62,237,4608
mvaa 24,64,216
mvaa 24,96,220
dl 228,40,53,0
mvaa 16,96,212
v_accvgpr_read_b32 v228,a246
mvaa 16,64,208
s_waitcnt lgkmcnt(5)
maaa 128,0,32
maaa 136,0,36
maaa 140,4,36
dl 229,40,53,1024
maaa 132,4,32
s_waitcnt lgkmcnt(0)
s_barrier
mvaa 32,8,32
s_add_u32 s53,s53,s58
ta 96,234,16384
v_accvgpr_read_b32 v229,a247
mvaa 40,8,36
s_cmp_lt_u32 s12,2
s_cselect_b32 s42,8388608,s90
ta 98,234,16896
mvaa 44,12,36
s_cmp_lt_u32 s12,2
s_cselect_b32 s53,s88,s89
ra 64,233,0
s_and_b32 s89,s98,8192
mvaa 36,12,32
dl 230,44,54,2048
mvaa 96,16,32
s_bitcmp1_b32 s21,0
s_cselect_b32 s90,s57,0
ta 100,235,16384
s_add_u32 s53,s53,s90
mvaa 104,16,36
s_cmp_lt_u32 s12,2
s_cselect_b64 s[40:41],s[72:73],s[80:81]
ta 102,235,16896
v_accvgpr_read_b32 v230,a248
mvaa 108,20,36
s_lshl_b32 s90,s90,1
ra 68,233,2048
s_add_u32 s91,s89,65536
mvaa 100,20,32
s_bfe_u32 s88,s21,65539
ra 72,233,4096
s_lshl_b32 s88,s88,1
mvaa 160,24,32
dl 231,44,54,3072
mvaa 168,24,36
s_add_u32 m0,s98,65536
ra 76,233,6144
s_add_u32 s54,s54,s58
mvaa 172,28,36
s_cmp_eq_u32 s12,3
s_cselect_b64 s[44:45],s[68:69],s[40:41]
ra 80,233,8192
v_accvgpr_read_b32 v231,a249
mvaa 164,28,32
s_cmp_eq_u32 s12,3
s_cselect_b32 s46,0,s42
ra 84,233,10240
s_add_u32 s58,s57,s90
mvaa 180,28,40
s_cmp_eq_u32 s12,3
s_cselect_b32 s54,0,s53
ra 88,233,12288
s_sub_u32 s57,s57,s90
mvaa 188,28,44
global_load_dwordx4 a[168:171],v244,s[30:31] offset:2048
mvaa 184,24,44
s_add_u32 s90,s89,65536
ta 104,236,16384
s_add_u32 s88,s88,s87
mvaa 176,24,40
s_and_b32 s87,s12,1
ra 92,233,14336
s_add_u32 s88,s88,s87
mvaa 116,20,40
s_mul_i32 s88,s88,524288
ta 106,236,16896
s_and_b32 s87,s21,7
mvaa 124,20,44
s_add_u32 s86,s88,33554432
ta 108,237,16384
s_lshl_b32 s87,s87,16
mvaa 120,16,44
global_load_dwordx4 a[176:179],v244,s[84:85] offset:0
mvaa 112,16,40
s_add_u32 s86,s86,s87
ta 110,237,16896
s_lshr_b32 s87,s12,1
mvaa 52,12,40
s_lshl_b32 s87,s87,15
ta 112,234,20480
s_add_u32 s86,s86,s87
mvaa 60,12,44
s_add_u32 s24,s8,s86
s_addc_u32 s25,s9,0
ta 114,234,20992
s_lshl_b32 s86,%1,5
mvaa 56,8,44
s_lshl_b32 s87,s12,3
ta 116,235,20480
s_add_u32 s86,s86,s87
mvaa 48,8,40
global_load_dwordx4 a[164:167],v244,s[30:31] offset:1024
maaa 148,4,40
s_add_u32 s86,s86,83886080
ta 118,235,20992
s_mov_b32 s88,4096
maaa 156,4,44
ta 120,236,20480
maaa 152,0,44
ta 122,236,20992
maaa 144,0,40
ta 124,237,20480
mvaa 0,0,48
global_load_dwordx4 a[172:175],v244,s[30:31] offset:3072
mvaa 8,0,52
s_mov_b32 s30,1048577
ta 126,237,20992
s_mov_b32 s31,268435712
mvaa 12,4,52
mvaa 4,4,48
mvaa 64,8,48
mvaa 72,8,52
global_load_dwordx4 a[180:183],v244,s[84:85] offset:1024
mvaa 76,12,52
mvaa 68,12,48
mvaa 128,16,48
mvaa 136,16,52
mvaa 140,20,52
dl 224,36,52,0
mvaa 132,20,48
mvaa 192,24,48
mvaa 200,24,52
mvaa 204,28,52
mvaa 196,28,48
dl 225,36,52,1024
mvaa 212,28,56
mvaa 220,28,60
mvaa 216,24,60
mvaa 208,24,56
mvaa 148,20,56
dl 226,36,52,2048
mvaa 156,20,60
mvaa 152,16,60
mvaa 144,16,56
mvaa 84,12,56
s_waitcnt vmcnt(12) lgkmcnt(0)
s_barrier
mvaa 92,12,60
dl 227,36,52,3072
mvaa 88,8,60
s_add_u32 s52,s52,s56
s_add_u32 m0,s98,69632
ta 32,234,32768
mvaa 80,8,56
ta 34,234,33280
mvaa 20,4,56
ta 36,235,32768
mvaa 28,4,60
ta 38,235,33280
mvaa 24,0,60
dl 224,36,52,0
mvaa 16,0,56
ta 40,236,32768
maaa 128,64,96
ta 42,236,33280
maaa 136,64,100
ta 44,237,32768
maaa 140,68,100
ta 46,237,33280
maaa 132,68,96
dl 225,36,52,1024
mvaa 32,72,96
ta 48,234,36864
mvaa 40,72,100
ta 50,234,37376
mvaa 44,76,100
ta 52,235,36864
mvaa 36,76,96
ta 54,235,37376
mvaa 96,80,96
dl 226,36,52,2048
mvaa 104,80,100
ra 20,232,43008
mvaa 108,84,100
ra 0,232,32768
mvaa 100,84,96
ra 24,232,45056
mvaa 160,88,96
ra 4,232,34816
mvaa 168,88,100
dl 227,36,52,3072
mvaa 172,92,100
s_add_u32 m0,s62,0
ra 28,232,47104
s_add_u32 s52,s52,s60
mvaa 164,92,96
ra 8,232,36864
mvaa 180,92,104
ra 12,232,38912
mvaa 188,92,108
ra 16,232,40960
v_add_u32_e32 v232,s90,v232
mvaa 184,88,108
dl 228,40,53,0
mvaa 176,88,104
ta 60,237,36864
v_xor_b32_e32 v232,s4,v232
mvaa 116,84,104
ta 62,237,37376
mvaa 124,84,108
ta 56,236,36864
mvaa 120,80,108
ta 58,236,37376
mvaa 112,80,104
dl 229,40,53,1024
mvaa 52,76,104
s_add_u32 s53,s53,s57
mvaa 60,76,108
mvaa 56,72,108
mvaa 48,72,104
maaa 148,68,104
dl 230,44,54,2048
maaa 156,68,108
maaa 152,64,108
maaa 144,64,104
mvaa 0,64,112
mvaa 8,64,116
dl 231,44,54,3072
mvaa 12,68,116
s_add_u32 m0,s62,16384
s_add_u32 s54,s54,s57
mvaa 4,68,112
mvaa 64,72,112
mvaa 72,72,116
mvaa 76,76,116
dl 228,40,53,0
mvaa 68,76,112
mvaa 128,80,112
mvaa 136,80,116
mvaa 140,84,116
mvaa 132,84,112
dl 229,40,53,1024
mvaa 192,88,112
s_add_u32 s53,s53,s58
mvaa 200,88,116
mvaa 204,92,116
mvaa 196,92,112
mvaa 212,92,120
dl 230,44,54,2048
mvaa 220,92,124
mvaa 216,88,124
s_waitcnt vmcnt(20) lgkmcnt(0)
s_barrier
mvaa 208,88,120
ra 92,233,47104
mvaa 148,84,120
ta 96,234,49152
mvaa 156,84,124
dl 231,44,54,3072
mvaa 152,80,124
s_add_u32 m0,s98,0
ra 88,233,45056
s_add_u32 s54,s54,s58
mvaa 144,80,120
ta 98,234,49664
mvaa 84,76,120
ta 100,235,49152
mvaa 92,76,124
ta 116,235,53248
mvaa 88,72,124
dl 224,36,52,0
mvaa 80,72,120
ta 118,235,53760
mvaa 20,68,120
ta 102,235,49664
v_subrev_u32_e32 v235,s89,v235
mvaa 28,68,124
ta 104,236,49152
mvaa 24,64,124
ta 112,234,53248
mvaa 16,64,120
dl 225,36,52,1024
maaa 128,0,32
ta 124,237,53248
maaa 136,0,36
ta 126,237,53760
maaa 140,4,36
ta 120,236,53248
maaa 132,4,32
ta 122,236,53760
mvaa 32,8,32
dl 226,36,52,2048
mvaa 40,8,36
ta 114,234,53760
v_subrev_u32_e32 v234,s89,v234
mvaa 44,12,36
ta 106,236,49664
v_subrev_u32_e32 v236,s89,v236
mvaa 36,12,32
ta 108,237,49152
mvaa 52,12,40
ta 110,237,49664
v_subrev_u32_e32 v237,s89,v237
mvaa 60,12,44
dl 227,36,52,3072
mvaa 56,8,44
s_add_u32 s52,s52,s56
s_add_u32 m0,s98,4096
ra 84,233,43008
mvaa 48,8,40
ra 80,233,40960
maaa 148,4,40
ra 72,233,36864
maaa 156,4,44
ra 76,233,38912
maaa 152,0,44
dl 224,36,52,0
maaa 144,0,40
ra 68,233,34816
mvaa 0,0,48
ra 64,233,32768
v_add_u32_e32 v233,s91,v233
mvaa 8,0,52
v_xor_b32_e32 v233,s4,v233
mvaa 12,4,52
mvaa 4,4,48
dl 225,36,52,1024
mvaa 20,4,56
mvaa 28,4,60
mvaa 24,0,60
mvaa 16,0,56
mvaa 220,28,60
dl 226,36,52,2048
s_waitcnt lgkmcnt(2)
mvaa 220,92,124
mvaa 216,88,124
mvaa 216,24,60
mvaa 212,28,56
mvaa 212,92,120
dl 227,36,52,3072
mvaa 208,88,120
s_add_u32 m0,s62,32768
s_add_u32 s52,s52,s60
s_waitcnt vmcnt(25)
zvaa 246,160,176
mvaa 208,24,56
cv 223,222,223
cv 222,220,221
mvaa 204,28,52
cv 221,218,219
zvaa 250,168,176
mvaa 204,92,116
cv 220,216,217
cv 219,214,215
mvaa 200,88,116
dl 228,40,53,0
s_waitcnt vmcnt(25)
mvaa 246,164,180
mvaa 200,24,52
cv 218,212,213
mvaa 250,172,180
mvaa 196,28,48
cv 217,210,211
cv 216,208,209
mvaa 196,92,112
cv 215,206,207
v_cndmask_b32_e64 v246,0,v246,s[30:31]
mvaa 192,88,112
cv 214,204,205
v_cndmask_b32_e64 v250,0,v250,s[30:31]
mvaa 192,24,48
dl 229,40,53,1024
s_lshl_b64 s[30:31],s[30:31],1
v_cndmask_b32_e64 v246,v246,v247,s[30:31]
v_cndmask_b32_e64 v250,v250,v251,s[30:31]
mvaa 188,28,44
s_lshl_b64 s[30:31],s[30:31],1
s_add_u32 s53,s53,s57
v_cndmask_b32_e64 v246,v246,v248,s[30:31]
v_cndmask_b32_e64 v250,v250,v252,s[30:31]
mvaa 188,92,108
s_lshl_b64 s[30:31],s[30:31],1
cv 213,202,203
v_cndmask_b32_e64 v246,v246,v249,s[30:31]
mvaa 184,88,108
v_cndmask_b32_e64 v250,v250,v253,s[30:31]
v_add_f32_dpp v246,v246,v246 quad_perm:[1,0,3,2] row_mask:0xf bank_mask:0xf
mvaa 184,24,44
v_add_f32_dpp v250,v250,v250 quad_perm:[1,0,3,2] row_mask:0xf bank_mask:0xf
v_add_f32_dpp v246,v246,v246 quad_perm:[2,3,0,1] row_mask:0xf bank_mask:0xf
mvaa 180,28,40
dl 230,44,54,2048
v_add_f32_dpp v250,v250,v250 quad_perm:[2,3,0,1] row_mask:0xf bank_mask:0xf
mvaa 180,92,104
v_add_f32_dpp v246,v246,v246 row_half_mirror row_mask:0xf bank_mask:0xf
v_add_f32_dpp v250,v250,v250 row_half_mirror row_mask:0xf bank_mask:0xf
mvaa 176,88,104
v_add_f32_dpp v246,v246,v246 row_mirror row_mask:0xf bank_mask:0xf
v_add_f32_dpp v250,v250,v250 row_mirror row_mask:0xf bank_mask:0xf
mvaa 176,24,40
cv 212,200,201
cv 211,198,199
mvaa 172,28,36
v_permlane16_swap_b32_e32 v246,v250
mvaa 172,92,100
dl 231,44,54,3072
v_add_f32_e32 v246,v246,v250
v_mov_b32_e32 v250,v246
mvaa 168,88,100
s_add_u32 s54,s54,s57
v_permlane32_swap_b32_e32 v246,v250
mvaa 168,24,36
v_add_f32_e32 v246,v246,v250
v_lshrrev_b32_e32 v250,6,v244
mvaa 164,28,32
v_mul_f32_e32 v246,0xbfb8aa3b,v246
cv 210,196,197
mvaa 164,92,96
ex 246
mvaa 160,88,96
buffer_load_dword a242,v241,s[48:51],s55 offen
mvaa 160,24,32
s_add_u32 s55,s55,s59
v_add_f32_e32 v246,1.0,v246
cv 209,194,195
mvaa 156,20,60
v_rcp_f32_e32 v246,v246
mvaa 156,84,124
s_waitcnt vmcnt(17) lgkmcnt(0)
s_barrier
mvaa 152,80,124
s_add_u32 m0,s62,49152
cv 208,192,193
cv 207,190,191
mvaa 152,16,60
dl 228,40,53,0
mvaa 148,20,56
cv 206,188,189
cv 205,186,187
mvaa 148,84,120
cv 204,184,185
cv 203,182,183
mvaa 144,80,120
cv 202,180,181
cv 201,178,179
mvaa 144,16,56
cv 200,176,177
cv 199,174,175
mvaa 140,20,52
dl 229,40,53,1024
mvaa 140,84,116
s_add_u32 s53,s53,s58
cv 198,172,173
cv 197,170,171
mvaa 136,80,116
ra 4,232,0
cv 196,168,169
mvaa 136,16,52
ta 92,239,12288
cv 195,166,167
mvaa 132,20,48
ta 94,239,12800
cv 194,164,165
mvaa 132,84,112
dl 230,44,54,2048
mvaa 128,80,112
ta 88,238,12288
cv 193,162,163
mvaa 128,16,48
ta 90,238,12800
cv 192,160,161
mvaa 124,20,44
cv 191,158,159
cv 190,156,157
mvaa 124,84,108
cv 189,154,155
cv 188,152,153
mvaa 120,80,108
dl 231,44,54,3072
mvaa 120,16,44
s_add_u32 m0,s98,32768
cv 187,150,151
cv 186,148,149
mvaa 116,20,40
s_add_u32 s54,s54,s58
cv 185,146,147
cv 184,144,145
mvaa 116,84,104
cv 183,142,143
cv 182,140,141
mvaa 112,80,104
cv 181,138,139
cv 180,136,137
mvaa 112,16,40
buffer_load_dword a243,v241,s[48:51],s55 offen
mvaa 108,20,36
s_add_u32 s55,s55,s59
cv 179,134,135
cv 178,132,133
mvaa 108,84,100
cv 177,130,131
cv 176,128,129
mvaa 104,80,100
cv 175,126,127
cv 174,124,125
mvaa 104,16,36
ta 44,235,0
cv 173,122,123
mvaa 100,20,32
dl 224,36,52,0
mvaa 100,84,96
ta 46,235,512
cv 172,120,121
mvaa 96,80,96
cv 171,118,119
cv 170,116,117
mvaa 96,16,32
ta 40,234,0
cv 169,114,115
mvaa 92,12,60
ta 42,234,512
cv 168,112,113
mvaa 92,76,124
dl 225,36,52,1024
mvaa 88,72,124
cv 167,110,111
cv 166,108,109
mvaa 88,8,60
cv 165,106,107
cv 164,104,105
mvaa 84,12,56
ta 84,237,8192
cv 163,102,103
mvaa 84,76,120
ta 80,236,8192
cv 162,100,101
mvaa 80,72,120
dl 226,36,52,2048
mvaa 80,8,56
ta 86,237,8704
cv 161,98,99
mvaa 76,12,52
ta 82,236,8704
cv 160,96,97
mvaa 76,76,116
L_vs7:
s_cbranch_execz L_vs11
ra 16,232,6144
cv 159,94,95
mvaa 72,72,116
ta 60,235,4096
cv 158,92,93
mvaa 72,8,52
dl 227,36,52,3072
mvaa 68,12,48
s_add_u32 m0,s62,0
ta 56,234,4096
s_add_u32 s52,s52,s56
mvaa 68,76,112
ta 62,235,4608
cv 151,78,79
mvaa 64,72,112
ta 58,234,4608
cv 150,76,77
mvaa 64,8,48
ta 52,237,0
cv 157,90,91
mvaa 60,76,108
buffer_load_dword a244,v241,s[48:51],s55 offen
mvaa 56,72,108
s_add_u32 s55,s55,s59
ra 12,232,4096
cv 147,70,71
mvaa 52,76,104
ta 54,237,512
cv 146,68,69
mvaa 48,72,104
ra 8,232,2048
cv 145,66,67
mvaa 44,76,100
ta 48,236,0
cv 144,64,65
mvaa 40,72,100
buffer_load_dword a245,v241,s[48:51],s55 offen
mvaa 36,76,96
s_add_u32 s55,s55,s59
ta 50,236,512
cv 143,62,63
mvaa 32,72,96
cv 142,60,61
cv 141,58,59
mvaa 28,68,124
cv 140,56,57
cv 139,54,55
mvaa 24,64,124
cv 138,52,53
cv 137,50,51
mvaa 20,68,120
s_mov_b64 exec,65537
buffer_store_dword v246,v250,s[8:11],s86 offen sc0 sc1
s_mov_b64 exec,-1
mvaa 16,64,120
cv 136,48,49
cv 135,46,47
mvaa 12,68,116
ta 76,235,8192
cv 134,44,45
mvaa 8,64,116
ta 78,235,8704
cv 133,42,43
mvaa 4,68,112
ta 72,234,8192
cv 132,40,41
mvaa 0,64,112
ta 74,234,8704
cv 131,38,39
maaa 156,68,108
cv 130,36,37
cv 129,34,35
maaa 152,64,108
cv 128,32,33
cv 127,30,31
maaa 148,68,104
cv 126,28,29
cv 125,26,27
maaa 144,64,104
v_accvgpr_read_b32 v63,a159
v_accvgpr_read_b32 v62,a158
maaa 140,68,100
v_accvgpr_read_b32 v61,a157
v_accvgpr_read_b32 v60,a156
maaa 136,64,100
v_accvgpr_read_b32 v59,a155
v_accvgpr_read_b32 v58,a154
maaa 132,68,96
v_accvgpr_read_b32 v57,a153
v_accvgpr_read_b32 v56,a152
maaa 128,64,96
cv 56,56,57
cv 57,58,59
s_waitcnt lgkmcnt(2)
zaaa 236,40,4
ta 68,237,4096
ta 70,237,4608
zaaa 220,44,4
ta 64,236,4096
ta 66,236,4608
zaaa 216,44,8
cv 58,60,61
cv 59,62,63
zaaa 232,40,8
buffer_store_dwordx4 v[56:59],v244,s[24:27],0 offen offset:3072 sc0 sc1
zaaa 228,40,12
v_accvgpr_read_b32 v55,a151
v_accvgpr_read_b32 v54,a150
zaaa 212,44,12
v_accvgpr_read_b32 v53,a149
v_accvgpr_read_b32 v52,a148
zaaa 208,44,16
v_accvgpr_read_b32 v51,a147
v_accvgpr_read_b32 v50,a146
zaaa 224,40,16
v_accvgpr_read_b32 v49,a145
v_accvgpr_read_b32 v48,a144
zaaa 192,48,16
cv 48,48,49
cv 49,50,51
zaaa 176,52,16
cv 50,52,53
s_waitcnt vmcnt(26) lgkmcnt(0)
s_barrier
zaaa 180,52,12
dl 228,40,53,0
cv 51,54,55
zaaa 196,48,12
ra 24,233,0
v_accvgpr_read_b32 v47,a143
zaaa 200,48,8
ra 28,233,2048
v_accvgpr_read_b32 v46,a142
v_accvgpr_read_b32 v45,a141
zaaa 184,52,8
ra 32,233,4096
ra 36,233,6144
zaaa 188,52,4
v_accvgpr_read_b32 v43,a139
v_accvgpr_read_b32 v42,a138
zaaa 204,48,4
dl 229,40,53,1024
ta 96,236,24576
zaaa 172,56,4
s_add_u32 s53,s53,s57
ta 98,236,25088
v_accvgpr_read_b32 v41,a137
zaaa 156,60,4
ta 100,237,24576
v_accvgpr_read_b32 v39,a135
zaaa 152,60,8
ta 102,237,25088
ta 104,238,28672
zaaa 168,56,8
v_accvgpr_read_b32 v38,a134
v_accvgpr_read_b32 v35,a131
zaaa 164,56,12
dl 230,44,54,2048
ta 106,238,29184
zaaa 148,60,12
ta 108,239,28672
v_accvgpr_read_b32 v34,a130
zaaa 144,60,16
ta 110,239,29184
v_accvgpr_read_b32 v33,a129
zaaa 160,56,16
ta 44,235,16384
ta 46,235,16896
ta 40,234,16384
ta 42,234,16896
ta 52,237,16384
ta 54,237,16896
ta 48,236,16384
ta 50,236,16896
ta 60,235,20480
ta 62,235,20992
ta 56,234,20480
ta 58,234,20992
v_accvgpr_read_b32 v32,a128
zaaa 128,64,16
v_accvgpr_read_b32 v37,a133
v_accvgpr_read_b32 v36,a132
zaaa 112,68,16
dl 231,44,54,3072
zaaa 116,68,12
s_add_u32 m0,s61,4096
v_accvgpr_read_b32 v40,a136
v_accvgpr_read_b32 v44,a140
zaaa 132,64,12
s_add_u32 s54,s54,s57
cv 251,42,43
cv 250,40,41
zaaa 136,64,8
s_add_u32 s61,s63,s98
cv 253,46,47
cv 252,44,45
zaaa 120,68,8
s_add_u32 s63,s63,s99
cv 249,38,39
cv 247,34,35
zaaa 124,68,4
buffer_store_dwordx4 v[48:51],v244,s[24:27],0 offen offset:2048 sc0 sc1
zaaa 140,64,4
cv 246,32,33
cv 248,36,37
zvaa 40,72,4
ta 68,237,20480
cv 124,24,25
zvaa 44,76,4
ta 70,237,20992
cv 123,22,23
zvaa 52,76,8
ta 64,236,20480
cv 121,18,19
zvaa 48,72,8
dl 224,36,52,0
zvaa 56,72,12
ta 66,236,20992
cv 120,16,17
zvaa 60,76,12
cv 122,20,21
cv 119,14,15
zvaa 68,76,16
cv 118,12,13
cv 117,10,11
zvaa 64,72,16
cv 116,8,9
cv 156,88,89
zvaa 32,80,16
dl 225,36,52,1024
ta 76,235,24576
ta 78,235,25088
zvaa 36,84,16
ta 72,234,24576
ta 74,234,25088
zvaa 28,84,12
cv 153,82,83
cv 152,80,81
zvaa 24,80,12
cv 155,86,87
cv 154,84,85
zvaa 16,80,8
cv 149,74,75
cv 148,72,73
zvaa 20,84,8
dl 226,36,52,2048
zvaa 12,84,4
v_add_u32_e32 v232,s99,v232
cv 115,6,7
zvaa 8,80,4
cv 114,4,5
cv 113,2,3
zvaa 76,4,88
cv 112,0,1
zvaa 96,4,92
zvaa 100,8,92
dl 227,36,52,3072
zvaa 80,8,88
s_add_u32 s52,s52,s60
zvaa 84,12,88
zvaa 104,12,92
zvaa 108,16,92
s_waitcnt vmcnt(23) lgkmcnt(0)
s_barrier
zvaa 88,16,88
L_kvx:
L_vs8:
s_cbranch_execz L_vs12
L_p_exit:
s_add_u32 m0,s62,16384
ra 4,232,0
v_add_u32_e32 v233,0xffff0000,v233
maaa 236,40,24
dl 228,40,53,0
maaa 220,44,24
s_lshl_b32 s86,s21,2
ra 8,232,2048
s_add_u32 s86,s86,s12
maaa 216,44,28
s_lshl_b32 s86,s86,8
ra 12,232,4096
s_add_u32 s86,s86,L_p_exit-L_att_pc+2048
maaa 232,40,28
s_mov_b32 s67,4096
ra 16,232,6144
s_and_b32 s87,s12,1
maaa 228,40,32
s_cmp_lt_u32 s12,2
s_cselect_b32 s94,s22,s23
ra 80,233,0
s_lshr_b32 s94,s94,6
maaa 212,44,32
dl 229,40,53,1024
maaa 208,44,36
s_add_u32 s94,s94,s87
ra 84,233,2048
s_lshl_b32 s87,s18,5
maaa 224,40,36
s_add_u32 s94,s94,s87
ra 88,233,4096
s_lshl_b32 s94,s94,11
maaa 192,48,36
s_add_u32 s94,s94,12582912
ra 92,233,6144
s_add_u32 s53,s53,s58
maaa 176,52,36
s_bfe_u32 s88,s21,65539
ta 0,234,32768
s_lshl_b32 s87,s20,1
maaa 180,52,32
dl 230,44,54,2048
maaa 196,48,32
s_add_u32 s88,s88,s87
ta 2,234,33280
s_lshl_b32 s88,s88,7
maaa 200,48,28
s_add_u32 s85,s88,83894272
ta 40,235,32768
v_bfe_u32 v241,v244,6,4
maaa 184,52,28
s_lshl_b32 s91,s12,4
ta 42,235,33280
v_and_b32_e32 v241,12,v241
maaa 188,52,24
s_cmp_lt_u32 s12,2
s_cselect_b32 s90,s22,s23
s_mul_i32 s33,s90,10240
v_accvgpr_read_b32 v255,a241
maaa 204,48,24
dl 231,44,54,3072
maaa 172,56,24
s_add_u32 m0,s98,65536
ta 52,236,32768
s_add_u32 s54,s54,s58
maaa 156,60,24
s_mov_b32 s64,0x55505555
ta 54,236,33280
s_mov_b32 s65,0x50005500
maaa 152,60,28
s_lshl_b32 s95,s12,12
s_mov_b32 s66,0x50000
s_add_u32 s70,s95,32768
maaa 168,56,28
s_add_u32 s72,s95,65536
s_lshl_b32 s4,s21,2
s_add_u32 s4,s4,s12
maaa 164,56,32
buffer_store_dwordx4 v[250:253],v244,s[24:27],0 offen offset:1024 sc0 sc1
maaa 148,60,32
s_lshl_b32 s4,s4,8
v_lshl_add_u32 v253,v244,3,s86
v_mov_b32_e32 v250,-1
maaa 144,60,36
s_bitcmp1_b32 170,s20
s_cselect_b32 s5,L_odd-L_last,0
v_mov_b32_e32 v251,-1
v_bfe_u32 v252,v244,4,4
maaa 160,56,36
s_add_u32 s4,s4,s5
v_sub_u32_e32 v252,v252,v241
v_add_u32_e32 v252,s91,v252
maaa 128,64,36
s_cmp_lt_u32 s12,2
s_cselect_b32 s91,32768,0
s_add_u32 s91,s91,98304
ta 56,237,32768
maaa 112,68,36
s_mov_b64 exec,3
global_load_dword a250,v253,s[96:97]
s_mov_b64 exec,-1
maaa 116,68,32
s_add_u32 s4,s4,L_last-L_att_pc
ta 58,237,33280
v_add_u32_e32 v253,10240,v253
maaa 132,64,32
s_mov_b64 s[30:31],s[28:29]
v_mov_b32_e32 v241,0xf149f2ca
s_mov_b64 s[28:29],s[78:79]
maaa 136,64,28
s_add_u32 s68,s95,0
v_cvt_f32_i32_e32 v252,v252
v_mul_f32_e32 v252,0x7149f2ca,v252
maaa 120,68,28
s_add_u32 s69,s95,16384
s_add_u32 s74,s95,98304
s_add_u32 s71,s95,49152
maaa 124,68,24
dl 224,36,52,0
maaa 140,64,24
s_add_u32 s73,s95,81920
v_and_b32_e32 v245,48,v244
v_lshlrev_b32_e32 v245,3,v245
mvaa 40,72,24
s_waitcnt vmcnt(15) lgkmcnt(2)
s_barrier
mvaa 44,76,24
s_lshl_b32 s59,%1,2
ta 20,234,49152
s_lshl_b32 s84,%1,2
mvaa 52,76,28
s_add_u32 s59,s59,s12
ta 22,234,49664
s_add_u32 s75,s95,114688
mvaa 48,72,28
s_add_u32 s84,s84,s12
ta 44,235,49152
s_mul_i32 s84,s84,12288
mvaa 56,72,32
dl 225,36,52,1024
mvaa 60,76,32
s_sub_u32 s55,s14,s13
ta 46,235,49664
s_mov_b32 s48,0x38000000
mvaa 68,76,36
s_mov_b32 s81,1
ta 48,236,49152
s_mov_b32 s84,0
mvaa 64,72,36
)"
R"(ta 50,236,49664
mvaa 32,96,36
ta 60,237,49152
mvaa 36,100,36
dl 226,36,52,2048
mvaa 28,100,32
ta 62,237,49664
mvaa 24,96,32
ta 68,234,36864
mvaa 16,96,28
ta 70,234,37376
mvaa 20,100,28
ta 64,234,53248
mvaa 12,100,24
dl 227,36,52,3072
mvaa 8,96,24
s_add_u32 s52,s52,s56
s_add_u32 m0,s98,69632
ta 66,234,53760
mvaa 76,24,104
ta 76,235,53248
mvaa 96,24,108
ta 78,235,53760
mvaa 100,28,108
ta 72,235,36864
mvaa 80,28,104
buffer_store_dwordx4 v[246:249],v244,s[24:27],0 offen offset:0 sc0 sc1
mvaa 84,32,104
ta 74,235,37376
v_mov_b32_e32 v248,-1
mvaa 104,32,108
v_mov_b32_e32 v249,-1
v_mov_b32_e32 v247,1
mvaa 108,36,108
mvaa 88,36,104
maaa 236,0,4
s_waitcnt lgkmcnt(5)
maaa 236,20,80
dl 224,36,52,0
maaa 220,44,80
maaa 220,40,4
maaa 216,40,8
maaa 216,44,84
maaa 232,20,84
dl 225,36,52,1024
maaa 232,0,8
maaa 228,0,12
maaa 228,20,88
maaa 212,44,88
maaa 212,40,12
dl 226,36,52,2048
maaa 208,40,16
maaa 208,44,92
maaa 224,20,92
ta 40,236,36864
maaa 224,0,16
ta 42,236,37376
maaa 192,52,16
dl 227,36,52,3072
maaa 192,48,92
s_add_u32 s52,s52,s60
s_add_u32 m0,s98,0
ta 44,236,53248
maaa 176,60,92
ta 46,236,53760
maaa 176,56,16
ta 20,237,53248
maaa 180,56,12
ta 22,237,53760
maaa 180,60,88
dl 224,36,52,0
maaa 196,48,88
ta 0,237,36864
maaa 196,52,12
ta 2,237,37376
maaa 200,52,8
s_waitcnt vmcnt(16)
ra 24,232,32768
maaa 200,48,84
ra 28,232,34816
maaa 184,60,84
ra 32,232,36864
maaa 184,56,8
buffer_store_dwordx4 v[112:115],v244,s[24:27],s67 offen offset:0 sc0 sc1
maaa 188,56,4
ra 36,232,38912
v_add_u32_e32 v232,0x10000,v232
maaa 188,60,80
ra 96,233,32768
v_cndmask_b32_e64 v112,v241,0,s[64:65]
maaa 204,48,80
s_mov_b32 s64,0xaaa0aaaa
ra 100,233,34816
s_mov_b32 s65,0xa000aa00
maaa 204,52,4
ra 104,233,36864
v_cndmask_b32_e64 v113,v241,0,s[64:65]
maaa 172,68,4
dl 225,36,52,1024
s_waitcnt lgkmcnt(8)
maaa 172,64,80
s_mov_b32 s64,0x55405554
ra 108,233,38912
v_add_u32_e32 v233,0x10000,v233
maaa 156,76,80
s_mov_b32 s65,0x40005400
ta 56,234,40960
v_cndmask_b32_e64 v114,v241,0,s[64:65]
maaa 156,72,4
s_mov_b32 s64,0xaa80aaa8
ta 58,234,41472
s_mov_b32 s65,0x8000a800
maaa 152,72,8
ta 60,234,57344
v_cndmask_b32_e64 v115,v241,0,s[64:65]
maaa 152,76,84
dl 226,36,52,2048
maaa 168,64,84
ta 62,234,57856
maaa 168,68,8
ta 48,235,57344
maaa 164,68,12
ta 50,235,57856
maaa 164,64,88
ta 52,235,40960
maaa 148,76,88
buffer_store_dwordx4 v[248:251],v244,s[8:11],s94 offen offset:0 sc0 sc1
maaa 148,72,12
ta 54,235,41472
maaa 144,72,16
maaa 144,76,92
L_vs9:
s_cbranch_execz L_vr0
maaa 160,64,92
ta 72,236,40960
maaa 160,68,16
ta 74,236,41472
maaa 128,40,16
dl 227,36,52,3072
maaa 128,44,92
s_add_u32 s52,s52,s56
ta 76,236,57344
s_add_u32 m0,s98,4096
maaa 112,20,92
ta 78,236,57856
s_waitcnt lgkmcnt(6)
maaa 112,0,16
ta 64,237,57344
maaa 116,0,12
ta 66,237,57856
maaa 116,20,88
dl 224,36,52,0
maaa 132,44,88
ta 68,237,40960
maaa 132,40,12
ta 70,237,41472
maaa 136,40,8
maaa 136,44,84
maaa 120,20,84
dl 225,36,52,1024
maaa 120,0,8
maaa 124,0,4
maaa 124,20,80
maaa 140,44,80
ta 0,238,45056
ta 2,238,45568
maaa 140,40,4
buffer_store_dwordx4 v[248:251],v244,s[8:11],s94 offen offset:1024 sc0 sc1
ta 20,238,61440
ta 22,238,61952
mvaa 40,56,4
ta 44,239,61440
ta 46,239,61952
mvaa 40,60,80
ta 40,239,45056
ta 42,239,45568
mvaa 44,48,80
v_and_b32_e32 v250,240,v244
v_accvgpr_read_b32 v249,a240
s_waitcnt lgkmcnt(8)
mvaa 44,52,4
mvaa 52,52,8
mvaa 52,48,84
dl 226,36,52,2048
mvaa 48,60,84
mvaa 48,56,8
mvaa 56,56,12
mvaa 56,60,88
mvaa 60,48,88
dl 227,36,52,3072
mvaa 60,52,12
s_add_u32 s52,s52,s60
mvaa 68,52,16
mvaa 68,48,92
mvaa 64,60,92
mvaa 64,56,16
mvaa 32,72,16
s_waitcnt vmcnt(11) lgkmcnt(0)
s_barrier
s_add_u32 m0,s62,32768
mvaa 32,76,92
dl 228,40,53,0
mvaa 36,64,92
ta 52,234,0
mvaa 36,68,16
ta 54,234,512
mvaa 28,68,12
ta 48,234,16384
mvaa 28,64,88
ta 50,234,16896
mvaa 24,76,88
ta 60,235,16384
mvaa 24,72,12
dl 229,40,53,1024
mvaa 16,72,8
s_add_u32 s53,s53,s57
ta 62,235,16896
mvaa 16,76,84
ta 56,235,0
mvaa 20,64,84
ta 58,235,512
mvaa 20,68,8
mvaa 12,68,4
dl 230,44,54,2048
mvaa 12,64,80
mvaa 8,76,80
ta 68,236,0
mvaa 8,72,4
ta 70,236,512
mvaa 76,4,0
ta 64,236,16384
mvaa 76,80,20
dl 231,44,54,3072
mvaa 96,80,44
s_add_u32 m0,s62,49152
s_add_u32 s54,s54,s57
ta 66,236,16896
mvaa 96,4,40
ta 76,237,16384
mvaa 100,8,40
ta 78,237,16896
mvaa 100,84,44
ta 72,237,0
mvaa 80,84,20
ta 74,237,512
mvaa 80,8,0
dl 228,40,53,0
mvaa 84,12,0
ra 80,233,0
mvaa 84,88,20
ra 4,232,0
mvaa 104,88,44
ra 84,233,2048
mvaa 104,12,40
ra 8,232,2048
mvaa 108,16,40
dl 229,40,53,1024
mvaa 108,92,44
s_add_u32 s53,s53,s58
ra 88,233,4096
mvaa 88,92,20
ta 40,234,4096
mvaa 88,16,0
ta 42,234,4608
s_waitcnt lgkmcnt(8)
maaa 236,52,24
ta 44,234,20480
maaa 236,48,96
dl 230,44,54,2048
maaa 220,60,96
ta 46,234,20992
maaa 220,56,24
ta 20,235,20480
maaa 216,56,28
ta 22,235,20992
maaa 216,60,100
ta 0,235,4096
maaa 232,48,100
dl 231,44,54,3072
maaa 232,52,28
s_add_u32 m0,s98,32768
ta 2,235,4608
s_add_u32 s54,s54,s58
maaa 228,52,32
ra 12,232,4096
maaa 228,48,104
ra 16,232,6144
v_add_u32_e32 v232,0xffff0000,v232
maaa 212,60,104
ra 92,233,6144
v_add_u32_e32 v233,0xffff0000,v233
maaa 212,56,32
maaa 208,56,36
dl 224,36,52,0
maaa 208,60,108
maaa 224,48,108
ta 56,236,4096
maaa 224,52,36
ta 58,236,4608
maaa 192,68,36
ta 60,236,20480
maaa 192,64,108
dl 225,36,52,1024
maaa 176,76,108
ta 62,236,20992
s_waitcnt lgkmcnt(6)
maaa 176,72,36
ta 48,237,20480
maaa 180,72,32
ta 50,237,20992
maaa 180,76,104
ta 52,237,4096
maaa 196,64,104
buffer_store_dwordx4 v[116:119],v244,s[24:27],s67 offen offset:1024 sc0 sc1
maaa 196,68,32
ta 54,237,4608
maaa 200,68,28
maaa 200,64,100
maaa 184,76,100
maaa 184,72,28
dl 226,36,52,2048
maaa 188,72,24
maaa 188,76,96
maaa 204,64,96
ta 72,234,8192
maaa 204,68,24
ta 74,234,8704
maaa 172,40,24
ta 76,234,24576
maaa 172,44,96
dl 227,36,52,3072
maaa 156,20,96
s_add_u32 s52,s52,s56
s_add_u32 m0,s98,36864
ta 78,234,25088
maaa 156,0,24
ta 64,235,24576
maaa 152,0,28
ta 66,235,25088
maaa 152,20,100
ta 68,235,8192
maaa 168,44,100
dl 224,36,52,0
maaa 168,40,28
ta 70,235,8704
maaa 164,40,32
maaa 164,44,104
maaa 148,20,104
maaa 148,0,32
buffer_store_dwordx4 v[120:123],v244,s[24:27],s67 offen offset:2048 sc0 sc1
maaa 144,0,36
maaa 144,20,108
maaa 160,44,108
ta 0,236,8192
maaa 160,40,36
ta 2,236,8704
s_waitcnt lgkmcnt(3)
maaa 128,56,36
dl 225,36,52,1024
maaa 128,60,108
ta 20,236,24576
maaa 112,48,108
ta 22,236,25088
maaa 112,52,36
ta 44,237,24576
maaa 116,52,32
ta 46,237,25088
maaa 116,48,104
dl 226,36,52,2048
maaa 132,60,104
ta 40,237,8192
maaa 132,56,32
ta 42,237,8704
maaa 136,56,28
maaa 136,60,100
maaa 120,48,100
maaa 120,52,28
buffer_load_dword v246,off,s[8:11],s35 offset:1024 sc1
maaa 124,52,24
maaa 124,48,96
maaa 140,60,96
ta 52,238,12288
ta 54,238,12800
maaa 140,56,24
ta 48,238,28672
ta 50,238,29184
mvaa 40,72,24
buffer_store_dwordx4 v[124:127],v244,s[24:27],s67 offen offset:3072 sc0 sc1
ta 60,239,28672
mvaa 40,76,96
s_mov_b32 s67,8192
ta 62,239,29184
mvaa 44,64,96
ta 56,239,12288
ta 58,239,12800
s_waitcnt lgkmcnt(8)
mvaa 44,68,24
mvaa 52,68,28
mvaa 52,64,100
dl 227,36,52,3072
mvaa 48,76,100
s_add_u32 s52,s52,s60
mvaa 48,72,28
mvaa 56,72,32
mvaa 56,76,104
mvaa 60,64,104
mvaa 60,68,32
mvaa 68,68,36
mvaa 68,64,108
mvaa 64,76,108
mvaa 64,72,36
mvaa 32,0,36
s_waitcnt vmcnt(12) lgkmcnt(0)
s_barrier
s_add_u32 m0,s62,0
mvaa 32,20,108
dl 228,40,53,0
mvaa 36,44,108
ta 68,234,32768
mvaa 36,40,36
ta 70,234,33280
mvaa 28,40,32
ta 64,234,49152
mvaa 28,44,104
ta 66,234,49664
mvaa 24,20,104
ta 76,235,49152
mvaa 24,0,32
dl 229,40,53,1024
mvaa 16,0,28
s_add_u32 s53,s53,s57
ta 78,235,49664
mvaa 16,20,100
ta 72,235,32768
mvaa 20,44,100
ta 74,235,33280
mvaa 20,40,28
mvaa 12,40,24
mvaa 12,44,96
dl 230,44,54,2048
mvaa 8,20,96
ta 40,236,32768
mvaa 8,0,24
ta 42,236,33280
mvaa 76,24,52
ta 44,236,49152
mvaa 76,96,48
ta 46,236,49664
mvaa 96,96,60
dl 231,44,54,3072
mvaa 96,24,56
s_add_u32 m0,s62,16384
s_add_u32 s54,s54,s57
ta 20,237,49152
mvaa 100,28,56
ta 22,237,49664
mvaa 100,100,60
ta 0,237,32768
mvaa 80,100,48
ta 2,237,33280
mvaa 80,28,52
dl 228,40,53,0
mvaa 84,32,52
ra 96,233,0
mvaa 84,104,48
ra 24,232,0
mvaa 104,104,60
ra 100,233,2048
mvaa 104,32,56
ra 28,232,2048
mvaa 108,36,56
dl 229,40,53,1024
mvaa 108,108,60
s_add_u32 s53,s53,s58
ra 104,233,4096
mvaa 88,108,48
ta 56,234,36864
mvaa 88,36,52
ta 58,234,37376
s_waitcnt lgkmcnt(8)
maaa 236,68,4
ta 60,234,53248
maaa 236,64,80
ta 62,234,53760
maaa 220,76,80
dl 230,44,54,2048
maaa 220,72,4
ta 48,235,53248
maaa 216,72,8
ta 50,235,53760
maaa 216,76,84
ta 52,235,36864
maaa 232,64,84
ta 54,235,37376
maaa 232,68,8
dl 231,44,54,3072
maaa 228,68,12
s_add_u32 m0,s98,65536
ra 32,232,4096
s_add_u32 s54,s54,s58
maaa 228,64,88
ra 36,232,6144
maaa 212,76,88
ra 108,233,6144
maaa 212,72,12
s_waitcnt vmcnt(10)
v_readfirstlane_b32 s87,v246
maaa 208,72,16
dl 224,36,52,0
maaa 208,76,92
s_cmp_eq_u32 s12,0
s_cselect_b32 s89,s32,s87
s_cmp_lg_u32 s87,s89
s_cbranch_scc1 L_q_ack
L_q_acked:
maaa 224,64,92
s_and_b32 s87,s12,1
ta 72,236,36864
s_lshl_b32 s88,s87,6
maaa 224,68,16
s_add_u32 s90,s90,s88
ta 74,236,37376
s_lshl_b32 s88,s87,10
maaa 192,40,16
s_lshl_b32 s87,s87,14
ta 76,236,53248
s_add_u32 s91,s91,s87
maaa 192,44,92
dl 225,36,52,1024
maaa 176,20,92
s_lshr_b32 s87,s18,1
ta 78,236,53760
s_lshl_b32 s87,s87,4
s_waitcnt lgkmcnt(5)
maaa 176,0,16
s_add_u32 s33,s33,s88
ta 64,237,53248
v_cmp_eq_u32_e64 s[100:101],s87,v250
maaa 180,0,12
s_lshl_b32 s88,s18,11
ta 66,237,53760
v_lshlrev_b32_e32 v250,3,v250
maaa 180,20,88
s_add_u32 s88,s33,s88
ta 68,237,36864
v_add_u32_e32 v250,83894272,v250
maaa 196,44,88
s_add_u32 s33,s88,63176704
ta 70,237,37376
v_add_u32_e32 v251,s91,v244
maaa 196,40,12
buffer_store_dwordx4 v[128:131],v244,s[24:27],s67 offen offset:0 sc0 sc1
maaa 200,40,8
s_add_u32 s34,s88,63504384
v_cndmask_b32_e64 v242,31,-1,s[100:101]
v_and_b32_e32 v243,528,v242
maaa 200,44,84
s_add_u32 s50,s88,63832064
s_add_u32 s51,s88,64159744
s_lshr_b32 s91,s21,3
maaa 184,20,84
s_and_b32 s88,s20,4
s_and_b32 s89,s20,3
s_add_u32 s91,s91,s88
maaa 184,0,8
s_lshl_b32 s88,s89,6
s_lshl_b32 s86,s21,2
s_add_u32 s86,s86,s12
maaa 188,0,4
dl 226,36,52,2048
maaa 188,20,80
s_cmp_eq_u32 s90,0
s_cselect_b64 s[64:65],-1,0
s_and_b32 s89,s86,1
ta 0,234,40960
maaa 204,44,80
s_lshl_b32 s89,s89,9
ta 2,234,41472
s_lshr_b32 s49,s86,3
maaa 204,40,4
s_lshl_b32 s49,s49,11
ta 20,234,57344
v_add_f32_e32 v128,0x70c9f2ca,v252
maaa 172,56,4
ta 22,234,57856
v_add_f32_e32 v129,0xf0c9f2ca,v252
maaa 172,60,80
dl 227,36,52,3072
maaa 156,48,80
s_add_u32 s52,s52,s56
s_add_u32 m0,s98,69632
ta 44,235,57344
maaa 156,52,4
ta 46,235,57856
v_add_f32_e32 v130,0xf1977617,v252
maaa 152,52,8
ta 40,235,40960
v_add_f32_e32 v131,0xf1fc6f7c,v252
maaa 152,48,84
ta 42,235,41472
v_min_f32_e32 v128,0xc1a00000,v128
maaa 168,60,84
dl 224,36,52,0
maaa 168,56,8
v_min_f32_e32 v129,0xc1a00000,v129
v_min_f32_e32 v130,0xc1a00000,v130
maaa 164,56,12
v_min_f32_e32 v131,0xc1a00000,v131
v_lshrrev_b32_e32 v246,6,v244
maaa 164,60,88
v_lshl_add_u32 v245,v246,22,v245
v_lshlrev_b32_e32 v246,16,v244
maaa 148,48,88
maaa 148,52,12
maaa 144,52,16
buffer_store_dwordx4 v[132:135],v244,s[24:27],s67 offen offset:1024 sc0 sc1
maaa 144,48,92
maaa 160,60,92
ta 52,236,40960
v_add_f32_e32 v132,0xf343a333,v252
maaa 160,56,16
ta 54,236,41472
v_add_f32_e32 v133,0xf3504260,v252
s_waitcnt lgkmcnt(2)
maaa 128,72,16
ta 48,236,57344
v_add_f32_e32 v134,0xf35ce18d,v252
maaa 128,76,92
dl 225,36,52,1024
maaa 112,64,92
ta 50,236,57856
v_min_f32_e32 v132,0xc1a00000,v132
maaa 112,68,16
ta 60,237,57344
v_add_f32_e32 v135,0xf36980b9,v252
maaa 116,68,12
ta 62,237,57856
v_min_f32_e32 v133,0xc1a00000,v133
maaa 116,64,88
ta 56,237,40960
v_min_f32_e32 v134,0xc1a00000,v134
maaa 132,76,88
dl 226,36,52,2048
maaa 132,72,12
ta 58,237,41472
v_min_f32_e32 v135,0xc1a00000,v135
maaa 136,72,8
maaa 136,76,84
maaa 120,64,84
maaa 120,68,8
dl 227,36,52,3072
maaa 124,68,4
s_add_u32 s52,s52,s60
maaa 124,64,80
maaa 140,76,80
ta 68,238,45056
ta 70,238,45568
maaa 140,72,4
ta 64,238,61440
ta 66,238,61952
mvaa 40,0,4
ta 76,239,61440
mvaa 40,20,80
buffer_store_dwordx4 v[136:139],v244,s[24:27],s67 offen offset:2048 sc0 sc1
ta 78,239,61952
mvaa 44,44,80
ta 72,239,45056
ta 74,239,45568
mvaa 44,40,4
v_add_f32_e32 v136,0xf3c6caff,v252
v_add_f32_e32 v137,0xf3cd1a95,v252
mvaa 52,40,8
v_add_f32_e32 v138,0xf3d36a2b,v252
v_add_f32_e32 v139,0xf3d9b9c2,v252
mvaa 52,44,84
v_min_f32_e32 v136,0xc1a00000,v136
v_min_f32_e32 v137,0xc1a00000,v137
mvaa 48,20,84
L_vs10:
s_cbranch_execz L_vr0
v_min_f32_e32 v138,0xc1a00000,v138
v_min_f32_e32 v139,0xc1a00000,v139
mvaa 48,0,8
mvaa 56,0,12
mvaa 56,20,88
mvaa 60,44,88
mvaa 60,40,12
mvaa 68,40,16
mvaa 68,44,92
mvaa 64,20,92
mvaa 64,0,16
s_waitcnt lgkmcnt(0)
mvaa 32,52,16
s_waitcnt vmcnt(11)
s_barrier
mvaa 32,48,92
s_cmp_eq_u32 s12,0
s_cselect_b64 exec,1,0
buffer_atomic_add v247,off,s[8:11],s85 sc1
s_mov_b64 exec,-1
mvaa 36,60,92
s_add_u32 m0,s62,32768
ta 40,234,0
v_mov_b32_e32 v247,32
mvaa 36,56,16
ta 42,234,512
mvaa 28,56,12
ta 44,234,16384
mvaa 28,60,88
ta 46,234,16896
mvaa 24,48,88
dl 228,40,53,0
mvaa 24,52,12
ta 20,235,16384
mvaa 16,52,8
ta 22,235,16896
mvaa 16,48,84
ta 0,235,0
mvaa 20,60,84
ta 2,235,512
mvaa 20,56,8
dl 229,40,53,1024
mvaa 12,56,4
s_add_u32 s53,s53,s57
mvaa 12,60,80
mvaa 8,48,80
ta 56,236,0
mvaa 8,52,4
ta 58,236,512
mvaa 76,4,68
dl 230,44,54,2048
mvaa 76,80,64
ta 60,236,16384
mvaa 96,80,76
ta 62,236,16896
mvaa 96,4,72
ta 48,237,16384
mvaa 100,8,72
ta 50,237,16896
mvaa 100,84,76
dl 231,44,54,3072
mvaa 80,84,64
s_add_u32 m0,s62,49152
s_add_u32 s54,s54,s57
ta 52,237,0
mvaa 80,8,68
ta 54,237,512
mvaa 84,12,68
ra 80,233,32768
mvaa 84,88,64
ra 4,232,32768
mvaa 104,88,76
dl 228,40,53,0
mvaa 104,12,72
ra 84,233,34816
mvaa 108,16,72
ra 8,232,34816
mvaa 108,92,76
ra 12,232,36864
mvaa 88,92,64
ta 72,234,4096
mvaa 88,16,68
ta 74,234,4608
s_waitcnt lgkmcnt(9)
maaa 236,40,24
dl 229,40,53,1024
maaa 236,44,96
s_add_u32 s53,s53,s58
ta 76,234,20480
maaa 220,20,96
ta 78,234,20992
maaa 220,0,24
ta 64,235,20480
maaa 216,0,28
ta 66,235,20992
maaa 216,20,100
dl 230,44,54,2048
maaa 232,44,100
ta 68,235,4096
maaa 232,40,28
ta 70,235,4608
maaa 228,40,32
ra 88,233,36864
maaa 228,44,104
ra 92,233,38912
v_add_u32_e32 v233,0x10000,v233
maaa 212,20,104
dl 231,44,54,3072
maaa 212,0,32
s_add_u32 m0,s98,0
ra 16,232,38912
s_add_u32 s54,s54,s58
maaa 208,0,36
v_add_u32_e32 v232,0x10000,v232
maaa 208,20,108
maaa 224,44,108
ta 0,236,4096
maaa 224,40,36
dl 224,36,52,0
maaa 192,56,36
ta 2,236,4608
maaa 192,60,108
ta 20,236,20480
maaa 176,48,108
ta 22,236,20992
s_waitcnt lgkmcnt(6)
maaa 176,52,36
ta 44,237,20480
maaa 180,52,32
ta 46,237,20992
maaa 180,48,104
dl 225,36,52,1024
maaa 196,60,104
ta 40,237,4096
maaa 196,56,32
ta 42,237,4608
maaa 200,56,28
maaa 200,60,100
maaa 184,48,100
buffer_store_dwordx4 v[140:143],v244,s[24:27],s67 offen offset:3072 sc0 sc1
maaa 184,52,28
s_mov_b32 s67,12288
v_add_f32_e32 v140,0xf415e232,v252
v_add_f32_e32 v141,0xf41909fd,v252
maaa 188,52,24
v_add_f32_e32 v142,0xf41c31c8,v252
v_min_f32_e32 v140,0xc1a00000,v140
maaa 188,48,96
v_add_f32_e32 v143,0xf41f5993,v252
v_bfe_i32 v252,v244,4,1
maaa 204,60,96
ta 52,234,8192
v_min_f32_e32 v141,0xc1a00000,v141
maaa 204,56,24
dl 226,36,52,2048
maaa 172,72,24
ta 54,234,8704
v_min_f32_e32 v142,0xc1a00000,v142
maaa 172,76,96
ta 48,234,24576
v_min_f32_e32 v143,0xc1a00000,v143
maaa 156,64,96
ta 50,234,25088
v_xor_b32_e32 v252,65535,v252
maaa 156,68,24
ta 60,235,24576
maaa 152,68,28
dl 227,36,52,3072
maaa 152,64,100
s_add_u32 s52,s52,s56
s_add_u32 m0,s98,4096
ta 62,235,25088
maaa 168,76,100
ta 56,235,8192
maaa 168,72,28
ta 58,235,8704
maaa 164,72,32
maaa 164,76,104
maaa 148,64,104
dl 224,36,52,0
maaa 148,68,32
maaa 144,68,36
maaa 144,64,108
maaa 160,76,108
ta 68,236,8192
maaa 160,72,36
buffer_store_dwordx4 v[144:147],v244,s[24:27],s67 offen offset:0 sc0 sc1
s_waitcnt lgkmcnt(3)
maaa 128,0,36
ta 70,236,8704
maaa 128,20,108
ta 64,236,24576
maaa 112,44,108
ta 66,236,25088
maaa 112,40,36
ta 76,237,24576
maaa 116,40,32
dl 225,36,52,1024
maaa 116,44,104
ta 78,237,25088
maaa 132,20,104
ta 72,237,8192
maaa 132,0,32
ta 74,237,8704
maaa 136,0,28
maaa 136,20,100
dl 226,36,52,2048
maaa 120,44,100
maaa 120,40,28
maaa 124,40,24
maaa 124,44,96
maaa 140,20,96
buffer_store_dwordx4 v[148:151],v244,s[24:27],s67 offen offset:1024 sc0 sc1
ta 40,238,12288
ta 42,238,12800
maaa 140,0,24
ta 44,238,28672
ta 46,238,29184
mvaa 40,52,24
ta 20,239,28672
mvaa 40,48,96
ta 22,239,29184
mvaa 44,60,96
ta 0,239,12288
ta 2,239,12800
s_waitcnt lgkmcnt(8)
mvaa 44,56,24
mvaa 52,56,28
dl 227,36,52,3072
mvaa 52,60,100
s_add_u32 s52,s52,s60
mvaa 48,48,100
mvaa 48,52,28
mvaa 56,52,32
mvaa 56,48,104
mvaa 60,60,104
mvaa 60,56,32
mvaa 68,56,36
mvaa 68,60,108
mvaa 64,48,108
mvaa 64,52,36
mvaa 32,68,36
s_waitcnt vmcnt(11) lgkmcnt(0)
s_barrier
s_add_u32 m0,s62,0
mvaa 32,64,108
dl 228,40,53,0
mvaa 36,76,108
ta 56,234,32768
mvaa 36,72,36
ta 58,234,33280
mvaa 28,72,32
ta 60,234,49152
mvaa 28,76,104
ta 62,234,49664
mvaa 24,64,104
dl 229,40,53,1024
mvaa 24,68,32
s_add_u32 s53,s53,s57
ta 48,235,49152
mvaa 16,68,28
ta 50,235,49664
mvaa 16,64,100
ta 52,235,32768
mvaa 20,76,100
ta 54,235,33280
mvaa 20,72,28
dl 230,44,54,2048
mvaa 12,72,24
mvaa 12,76,96
mvaa 8,64,96
ta 72,236,32768
mvaa 8,68,24
ta 74,236,33280
mvaa 76,24,40
ta 76,236,49152
mvaa 76,96,44
dl 231,44,54,3072
mvaa 96,96,20
s_add_u32 m0,s62,16384
s_add_u32 s54,s54,s57
ta 78,236,49664
mvaa 96,24,0
ta 64,237,49152
mvaa 100,28,0
ta 66,237,49664
mvaa 100,100,20
ta 68,237,32768
mvaa 80,100,44
dl 228,40,53,0
mvaa 80,28,40
ta 70,237,33280
mvaa 84,32,40
ra 96,233,0
mvaa 84,104,44
ra 24,232,0
mvaa 104,104,20
ra 100,233,2048
mvaa 104,32,0
dl 229,40,53,1024
mvaa 108,36,0
s_add_u32 s53,s53,s58
ra 28,232,2048
mvaa 108,108,20
ra 104,233,4096
mvaa 88,108,44
ta 0,234,36864
mvaa 88,36,40
ta 2,234,37376
s_waitcnt lgkmcnt(8)
maaa 236,56,4
dl 230,44,54,2048
maaa 236,60,80
ta 20,234,53248
maaa 220,48,80
ta 22,234,53760
maaa 220,52,4
ta 44,235,53248
maaa 216,52,8
ta 46,235,53760
)"
R"(maaa 216,48,84
dl 231,44,54,3072
maaa 232,60,84
s_add_u32 m0,s98,32768
ta 40,235,36864
s_add_u32 s54,s54,s58
maaa 232,56,8
ta 42,235,37376
maaa 228,56,12
ra 32,232,4096
maaa 228,60,88
ra 108,233,6144
v_add_u32_e32 v233,0xffff0000,v233
maaa 212,48,88
ra 36,232,6144
v_add_u32_e32 v232,0xffff0000,v232
maaa 212,52,12
dl 224,36,52,0
maaa 208,52,16
maaa 208,48,92
maaa 224,60,92
ta 52,236,36864
maaa 224,56,16
ta 54,236,37376
maaa 192,72,16
dl 225,36,52,1024
maaa 192,76,92
ta 48,236,53248
maaa 176,64,92
ta 50,236,53760
s_waitcnt lgkmcnt(6)
maaa 176,68,16
ta 60,237,53248
maaa 180,68,12
ta 62,237,53760
maaa 180,64,88
dl 226,36,52,2048
maaa 196,76,88
ta 56,237,36864
maaa 196,72,12
ta 58,237,37376
maaa 200,72,8
maaa 200,76,84
maaa 184,64,84
buffer_store_dwordx4 v[152:155],v244,s[24:27],s67 offen offset:2048 sc0 sc1
maaa 184,68,8
maaa 188,68,4
maaa 188,64,80
maaa 204,76,80
ta 68,234,40960
maaa 204,72,4
ta 70,234,41472
maaa 172,0,4
dl 227,36,52,3072
maaa 172,20,80
s_add_u32 s52,s52,s56
s_add_u32 m0,s98,36864
ta 64,234,57344
maaa 156,44,80
ta 66,234,57856
maaa 156,40,4
ta 76,235,57344
maaa 152,40,8
ta 78,235,57856
maaa 152,44,84
dl 224,36,52,0
maaa 168,20,84
ta 72,235,40960
maaa 168,0,8
ta 74,235,41472
maaa 164,0,12
maaa 164,20,88
maaa 148,44,88
buffer_store_dwordx4 v[156:159],v244,s[24:27],s67 offen offset:3072 sc0 sc1
maaa 148,40,12
s_mov_b32 s67,16384
maaa 144,40,16
maaa 144,44,92
maaa 160,20,92
ta 40,236,40960
maaa 160,0,16
dl 225,36,52,1024
s_waitcnt lgkmcnt(3)
maaa 128,52,16
ta 42,236,41472
maaa 128,48,92
ta 44,236,57344
maaa 112,60,92
ta 46,236,57856
maaa 112,56,16
ta 20,237,57344
maaa 116,56,12
ta 22,237,57856
maaa 116,60,88
dl 226,36,52,2048
maaa 132,48,88
ta 0,237,40960
maaa 132,52,12
ta 2,237,41472
maaa 136,52,8
maaa 136,48,84
maaa 120,60,84
dl 227,36,52,3072
maaa 120,56,8
s_add_u32 s52,s52,s60
maaa 124,56,4
maaa 124,60,80
maaa 140,48,80
ta 56,238,45056
ta 58,238,45568
maaa 140,52,4
buffer_store_dwordx4 v[160:163],v244,s[24:27],s67 offen offset:0 sc0 sc1
ta 60,238,61440
ta 62,238,61952
mvaa 40,68,4
ta 48,239,61440
mvaa 40,64,80
ta 50,239,61952
mvaa 44,76,80
ta 52,239,45056
ta 54,239,45568
s_waitcnt lgkmcnt(8)
mvaa 44,72,4
mvaa 52,72,8
mvaa 52,76,84
mvaa 48,64,84
mvaa 48,68,8
mvaa 56,68,12
mvaa 56,64,88
mvaa 60,76,88
mvaa 60,72,12
mvaa 68,72,16
mvaa 68,76,92
mvaa 64,64,92
mvaa 64,68,16
mvaa 32,40,16
s_waitcnt vmcnt(11) lgkmcnt(0)
s_barrier
s_add_u32 m0,s62,32768
mvaa 32,44,92
dl 228,40,53,0
mvaa 36,20,92
ta 72,234,0
mvaa 36,0,16
ta 74,234,512
mvaa 28,0,12
ta 76,234,16384
mvaa 28,20,88
ta 78,234,16896
mvaa 24,44,88
ta 64,235,16384
mvaa 24,40,12
dl 229,40,53,1024
mvaa 16,40,8
s_add_u32 s53,s53,s57
ta 66,235,16896
mvaa 16,44,84
ta 68,235,0
mvaa 20,20,84
ta 70,235,512
mvaa 20,0,8
mvaa 12,0,4
dl 230,44,54,2048
mvaa 12,20,80
mvaa 8,44,80
ta 0,236,0
mvaa 8,40,4
ta 2,236,512
mvaa 76,4,56
ta 20,236,16384
mvaa 76,80,60
dl 231,44,54,3072
mvaa 96,80,48
s_add_u32 m0,s62,49152
s_add_u32 s54,s54,s57
ta 22,236,16896
mvaa 96,4,52
ta 44,237,16384
mvaa 100,8,52
ta 46,237,16896
mvaa 100,84,48
ta 40,237,0
mvaa 80,84,60
dl 228,40,53,0
mvaa 80,8,56
ta 42,237,512
mvaa 84,12,56
ra 80,233,0
mvaa 84,88,60
ra 4,232,0
mvaa 104,88,48
ra 84,233,2048
mvaa 104,12,52
ra 8,232,2048
mvaa 108,16,52
dl 229,40,53,1024
mvaa 108,92,48
s_add_u32 s53,s53,s58
ra 88,233,4096
mvaa 88,92,60
ta 52,234,4096
mvaa 88,16,56
ta 54,234,4608
s_waitcnt lgkmcnt(8)
maaa 236,72,24
ta 48,234,20480
maaa 236,76,96
dl 230,44,54,2048
maaa 220,64,96
L_vs11:
s_cbranch_execz L_vr0
ta 50,234,20992
maaa 220,68,24
ta 60,235,20480
maaa 216,68,28
ta 62,235,20992
maaa 216,64,100
ta 56,235,4096
v_accvgpr_read_b32 v0,a236
maaa 232,76,100
dl 231,44,54,3072
maaa 232,72,28
s_add_u32 m0,s98,65536
ta 58,235,4608
s_add_u32 s54,s54,s58
maaa 228,72,32
ra 12,232,4096
v_accvgpr_read_b32 v1,a237
maaa 228,76,104
ra 92,233,6144
v_accvgpr_read_b32 v2,a238
maaa 212,64,104
ra 16,232,6144
v_accvgpr_read_b32 v3,a239
maaa 212,68,32
dl 224,36,52,0
maaa 208,68,36
v_accvgpr_read_b32 v4,a220
v_accvgpr_read_b32 v5,a221
maaa 208,64,108
v_accvgpr_read_b32 v6,a222
v_accvgpr_read_b32 v7,a223
maaa 224,76,108
ta 68,236,4096
v_accvgpr_read_b32 v92,a216
maaa 224,72,36
ta 70,236,4608
v_accvgpr_read_b32 v93,a217
maaa 192,0,36
dl 225,36,52,1024
maaa 192,20,108
ta 64,236,20480
v_accvgpr_read_b32 v94,a218
maaa 176,44,108
ta 66,236,20992
v_accvgpr_read_b32 v95,a219
s_waitcnt lgkmcnt(6)
maaa 176,40,36
ta 76,237,20480
v_accvgpr_read_b32 v72,a232
maaa 180,40,32
ta 78,237,20992
v_accvgpr_read_b32 v73,a233
maaa 180,44,104
ta 72,237,4096
v_accvgpr_read_b32 v74,a234
maaa 196,20,104
s_mov_b64 exec,3
global_load_dword a250,v253,s[96:97]
s_mov_b64 exec,-1
maaa 196,0,32
ta 74,237,4608
v_accvgpr_read_b32 v75,a235
maaa 200,0,28
maaa 200,20,100
maaa 184,44,100
maaa 184,40,28
buffer_store_dwordx4 v[164:167],v244,s[24:27],s67 offen offset:1024 sc0 sc1
maaa 188,40,24
maaa 188,44,96
maaa 204,20,96
ta 40,234,8192
maaa 204,0,24
ta 42,234,8704
maaa 172,52,24
dl 226,36,52,2048
maaa 172,48,96
ta 44,234,24576
maaa 156,60,96
ta 46,234,25088
maaa 156,56,24
ta 20,235,24576
maaa 152,56,28
ta 22,235,25088
maaa 152,60,100
dl 227,36,52,3072
maaa 168,48,100
s_add_u32 s52,s52,s56
s_add_u32 m0,s98,69632
ta 0,235,8192
maaa 168,52,28
ta 2,235,8704
maaa 164,52,32
maaa 164,48,104
maaa 148,60,104
maaa 148,56,32
buffer_store_dwordx4 v[168:171],v244,s[24:27],s67 offen offset:2048 sc0 sc1
maaa 144,56,36
maaa 144,60,108
maaa 160,48,108
ta 56,236,8192
maaa 160,52,36
ta 58,236,8704
s_waitcnt lgkmcnt(4)
maaa 128,68,36
dl 224,36,52,0
maaa 128,64,108
ta 60,236,24576
maaa 112,76,108
ta 62,236,25088
maaa 112,72,36
ta 48,237,24576
maaa 116,72,32
ta 50,237,25088
maaa 116,76,104
dl 225,36,52,1024
maaa 132,64,104
ta 52,237,8192
maaa 132,68,32
ta 54,237,8704
maaa 136,68,28
maaa 136,64,100
maaa 120,76,100
dl 226,36,52,2048
maaa 120,72,28
maaa 124,72,24
maaa 124,76,96
maaa 140,64,96
ta 72,238,12288
ta 74,238,12800
maaa 140,68,24
ta 76,238,28672
ta 78,238,29184
mvaa 40,40,24
buffer_store_dwordx4 v[172:175],v244,s[24:27],s67 offen offset:3072 sc0 sc1
ta 64,239,28672
mvaa 40,44,96
s_mov_b32 s67,20480
ta 66,239,29184
mvaa 44,20,96
ta 68,239,12288
ta 70,239,12800
s_waitcnt lgkmcnt(8)
mvaa 44,0,24
mvaa 52,0,28
mvaa 52,20,100
dl 227,36,52,3072
mvaa 48,44,100
s_add_u32 s52,s52,s60
mvaa 48,40,28
mvaa 56,40,32
mvaa 56,44,104
mvaa 60,20,104
mvaa 60,0,32
mvaa 68,0,36
mvaa 68,20,108
mvaa 64,44,108
mvaa 64,40,36
mvaa 32,56,36
s_waitcnt vmcnt(12) lgkmcnt(0)
s_barrier
s_add_u32 m0,s62,0
mvaa 32,60,108
dl 228,40,53,0
mvaa 36,48,108
ta 0,234,32768
mvaa 36,52,36
ta 2,234,33280
mvaa 28,52,32
ta 20,234,49152
mvaa 28,48,104
ta 22,234,49664
mvaa 24,60,104
ta 44,235,49152
mvaa 24,56,32
dl 229,40,53,1024
mvaa 16,56,28
s_add_u32 s53,s53,s57
ta 46,235,49664
mvaa 16,60,100
ta 40,235,32768
mvaa 20,48,100
ta 42,235,33280
mvaa 20,52,28
ta 236,236,36864
mvaa 12,52,24
dl 230,44,54,2048
mvaa 12,48,96
ta 238,236,37376
mvaa 8,60,96
ta 52,236,32768
mvaa 8,56,24
ta 54,236,33280
mvaa 76,24,72
ta 48,236,49152
mvaa 76,96,76
ta 50,236,49664
mvaa 96,96,64
dl 231,44,54,3072
mvaa 96,24,68
s_add_u32 m0,s62,16384
s_add_u32 s54,s54,s57
ta 60,237,49152
mvaa 100,28,68
ta 62,237,49664
mvaa 100,100,64
ta 56,237,32768
mvaa 80,100,76
ta 58,237,33280
mvaa 80,28,72
dl 228,40,53,0
mvaa 84,32,72
ta 220,236,53248
mvaa 84,104,76
ta 222,236,53760
mvaa 104,104,64
ta 232,237,53248
mvaa 104,32,68
ta 234,237,53760
mvaa 108,36,68
dl 229,40,53,1024
mvaa 108,108,64
s_add_u32 s53,s53,s58
ta 216,237,36864
mvaa 88,108,76
ta 68,234,36864
mvaa 88,36,72
ta 70,234,37376
s_waitcnt lgkmcnt(8)
mvaa 0,0,4
ta 64,234,53248
mvaa 0,20,80
dl 230,44,54,2048
mvaa 4,44,80
ta 66,234,53760
mvaa 4,40,4
ta 76,235,53248
mvaa 92,40,8
ta 78,235,53760
mvaa 92,44,84
ta 72,235,36864
mvaa 72,20,84
ta 74,235,37376
mvaa 72,0,8
dl 231,44,54,3072
maaa 228,0,12
s_add_u32 m0,s98,0
ta 218,237,37376
s_add_u32 s54,s54,s58
maaa 228,20,88
ra 96,233,32768
maaa 212,44,88
ra 24,232,32768
maaa 212,40,12
ra 100,233,34816
maaa 208,40,16
dl 224,36,52,0
maaa 208,44,92
ra 28,232,34816
v_accvgpr_read_b32 v144,a228
maaa 224,20,92
ra 104,233,36864
v_accvgpr_read_b32 v145,a229
maaa 224,0,16
ra 32,232,36864
v_accvgpr_read_b32 v146,a230
maaa 192,52,16
ra 36,232,38912
v_accvgpr_read_b32 v147,a231
maaa 192,48,92
dl 225,36,52,1024
maaa 176,60,92
ra 108,233,38912
v_accvgpr_read_b32 v148,a212
s_waitcnt lgkmcnt(8)
maaa 176,56,16
ta 40,234,40960
v_accvgpr_read_b32 v149,a213
maaa 180,56,12
ta 42,234,41472
v_accvgpr_read_b32 v150,a214
maaa 180,60,88
ta 44,234,57344
v_accvgpr_read_b32 v151,a215
maaa 196,48,88
buffer_store_dwordx4 v[176:179],v244,s[24:27],s67 offen offset:0 sc0 sc1
maaa 196,52,12
ta 46,234,57856
v_accvgpr_read_b32 v156,a208
maaa 200,52,8
ta 20,235,57344
v_accvgpr_read_b32 v157,a209
maaa 200,48,84
ta 22,235,57856
v_accvgpr_read_b32 v158,a210
maaa 184,60,84
ta 0,235,40960
v_accvgpr_read_b32 v159,a211
maaa 184,56,8
ta 2,235,41472
v_accvgpr_read_b32 v152,a224
maaa 188,56,4
dl 226,36,52,2048
maaa 188,60,80
v_accvgpr_read_b32 v153,a225
v_accvgpr_read_b32 v154,a226
maaa 204,48,80
ta 56,236,40960
v_accvgpr_read_b32 v155,a227
maaa 204,52,4
ta 58,236,41472
v_add_u32_e32 v233,0x10000,v233
maaa 172,68,4
ta 60,236,57344
v_add_u32_e32 v232,0x10000,v232
maaa 172,64,80
dl 227,36,52,3072
maaa 156,76,80
s_add_u32 s52,s52,s56
s_add_u32 m0,s98,4096
ta 62,236,57856
maaa 156,72,4
ta 48,237,57344
maaa 152,72,8
ta 50,237,57856
maaa 152,76,84
ta 52,237,40960
maaa 168,64,84
dl 224,36,52,0
maaa 168,68,8
ta 54,237,41472
maaa 164,68,12
maaa 164,64,88
maaa 148,76,88
maaa 148,72,12
buffer_store_dwordx4 v[180:183],v244,s[24:27],s67 offen offset:1024 sc0 sc1
maaa 144,72,16
maaa 144,76,92
maaa 160,64,92
ta 72,238,45056
maaa 160,68,16
ta 74,238,45568
maaa 128,236,16
dl 225,36,52,1024
maaa 128,220,92
ta 76,238,61440
maaa 112,232,92
ta 78,238,61952
maaa 112,216,16
ta 64,239,61440
maaa 116,216,12
ta 66,239,61952
maaa 116,232,88
ta 68,239,45056
maaa 132,220,88
dl 226,36,52,2048
maaa 132,236,12
ta 70,239,45568
maaa 136,236,8
maaa 136,220,84
maaa 120,232,84
maaa 120,216,8
dl 227,36,52,3072
maaa 124,216,4
s_add_u32 s52,s52,s60
maaa 124,232,80
maaa 140,220,80
maaa 140,236,4
s_waitcnt lgkmcnt(1)
mvaa 40,40,4
buffer_store_dwordx4 v[184:187],v244,s[24:27],s67 offen offset:2048 sc0 sc1
mvaa 40,44,80
mvaa 44,20,80
mvaa 44,0,4
mvaa 52,0,8
mvaa 52,20,84
mvaa 48,44,84
mvaa 48,40,8
mvaa 56,40,12
mvaa 56,44,88
mvaa 60,20,88
mvaa 60,0,12
mvaa 68,0,16
mvaa 68,20,92
mvaa 64,44,92
mvaa 64,40,16
mvaa 32,56,16
s_waitcnt vmcnt(11) lgkmcnt(0)
s_barrier
s_add_u32 m0,s62,32768
mvaa 32,60,92
dl 228,40,53,0
mvaa 36,48,92
ta 216,234,0
mvaa 36,52,16
ta 218,234,512
mvaa 28,52,12
ta 232,234,16384
mvaa 28,48,88
ta 234,234,16896
mvaa 24,60,88
dl 229,40,53,1024
mvaa 24,56,12
s_add_u32 s53,s53,s57
ta 220,235,16384
mvaa 16,56,8
ta 222,235,16896
mvaa 16,60,84
ta 236,235,0
mvaa 20,48,84
ta 238,235,512
mvaa 20,52,8
dl 230,44,54,2048
mvaa 12,52,4
ta 0,236,0
mvaa 12,48,80
ta 2,236,512
mvaa 8,60,80
ta 20,236,16384
mvaa 8,56,4
ta 22,236,16896
mvaa 76,4,72
dl 231,44,54,3072
mvaa 76,80,76
s_add_u32 m0,s62,49152
s_add_u32 s54,s54,s57
ta 44,237,16384
mvaa 96,80,64
ta 46,237,16896
mvaa 96,4,68
ta 40,237,0
mvaa 100,8,68
ta 42,237,512
mvaa 100,84,64
dl 228,40,53,0
mvaa 80,84,76
ta 52,234,4096
mvaa 80,8,72
ta 54,234,4608
mvaa 84,12,72
ta 48,234,20480
mvaa 84,88,76
ta 50,234,20992
mvaa 104,88,64
dl 229,40,53,1024
mvaa 104,12,68
s_add_u32 s53,s53,s58
ta 60,235,20480
mvaa 108,16,68
ta 62,235,20992
mvaa 108,92,64
ta 56,235,4096
mvaa 88,92,76
ta 58,235,4608
mvaa 88,16,72
dl 230,44,54,2048
s_waitcnt lgkmcnt(8)
mvaa 0,216,24
ta 228,234,8192
mvaa 0,232,96
ta 68,236,4096
mvaa 4,220,96
ta 70,236,4608
mvaa 4,236,24
ta 64,236,20480
mvaa 92,236,28
ta 66,236,20992
mvaa 92,220,100
dl 231,44,54,3072
mvaa 72,232,100
s_add_u32 m0,s98,32768
ta 76,237,20480
s_add_u32 s54,s54,s58
mvaa 72,216,28
ta 78,237,20992
mvaa 144,216,32
ta 72,237,4096
mvaa 144,232,104
ta 74,237,4608
mvaa 148,220,104
dl 224,36,52,0
mvaa 148,236,32
ta 230,234,8704
mvaa 156,236,36
ta 212,234,24576
mvaa 156,220,108
ta 214,234,25088
mvaa 152,232,108
ta 224,235,24576
mvaa 152,216,36
dl 225,36,52,1024
maaa 192,0,36
ta 226,235,25088
maaa 192,20,108
ta 208,235,8192
maaa 176,44,108
ra 80,233,0
maaa 176,40,36
ta 210,235,8704
maaa 180,40,32
dl 226,36,52,2048
maaa 180,44,104
ra 4,232,0
v_accvgpr_read_b32 v160,a192
maaa 196,20,104
ra 84,233,2048
v_accvgpr_read_b32 v161,a193
maaa 196,0,32
ra 8,232,2048
v_accvgpr_read_b32 v162,a194
maaa 200,0,28
ra 12,232,4096
v_accvgpr_read_b32 v163,a195
maaa 200,20,100
buffer_store_dwordx4 v[188:191],v244,s[24:27],s67 offen offset:3072 sc0 sc1
maaa 184,44,100
s_mov_b32 s67,24576
ra 88,233,4096
v_accvgpr_read_b32 v164,a176
maaa 184,40,28
ra 92,233,6144
v_accvgpr_read_b32 v165,a177
maaa 188,40,24
ra 16,232,6144
v_accvgpr_read_b32 v166,a178
maaa 188,44,96
L_vs12:
s_cbranch_execz L_vr0
ta 236,236,8192
v_accvgpr_read_b32 v167,a179
maaa 204,20,96
ta 238,236,8704
v_accvgpr_read_b32 v172,a180
maaa 204,0,24
dl 227,36,52,3072
s_waitcnt lgkmcnt(8)
maaa 172,52,24
s_add_u32 s52,s52,s56
s_add_u32 m0,s98,36864
ta 220,236,24576
maaa 172,48,96
ta 222,236,25088
v_accvgpr_read_b32 v173,a181
maaa 156,60,96
ta 232,237,24576
v_accvgpr_read_b32 v174,a182
maaa 156,56,24
ta 234,237,25088
v_accvgpr_read_b32 v175,a183
maaa 152,56,28
dl 224,36,52,0
maaa 152,60,100
ta 216,237,8192
v_accvgpr_read_b32 v168,a196
maaa 168,48,100
ta 218,237,8704
v_accvgpr_read_b32 v169,a197
maaa 168,52,28
ta 40,238,12288
v_accvgpr_read_b32 v170,a198
maaa 164,52,32
ta 42,238,12800
v_accvgpr_read_b32 v171,a199
maaa 164,48,104
dl 225,36,52,1024
maaa 148,60,104
ta 44,238,28672
v_add_u32_e32 v233,0xffff0000,v233
maaa 148,56,32
ta 46,238,29184
v_add_u32_e32 v232,0xffff0000,v232
maaa 144,56,36
ta 20,239,28672
maaa 144,60,108
ta 22,239,29184
maaa 160,48,108
buffer_store_dwordx4 v[192:195],v244,s[24:27],s67 offen offset:0 sc0 sc1
maaa 160,52,36
ta 0,239,12288
maaa 128,68,36
ta 2,239,12800
maaa 128,64,108
maaa 112,76,108
maaa 112,72,36
maaa 116,72,32
dl 226,36,52,2048
maaa 116,76,104
maaa 132,64,104
maaa 132,68,32
maaa 136,68,28
maaa 136,64,100
dl 227,36,52,3072
maaa 120,76,100
s_add_u32 s52,s52,s60
maaa 120,72,28
maaa 124,72,24
maaa 124,76,96
maaa 140,64,96
buffer_store_dwordx4 v[196:199],v244,s[24:27],s67 offen offset:1024 sc0 sc1
maaa 140,68,24
mvaa 40,228,24
mvaa 40,212,96
mvaa 44,224,96
mvaa 44,208,24
mvaa 52,208,28
mvaa 52,224,100
mvaa 48,212,100
mvaa 48,228,28
mvaa 56,228,32
mvaa 56,212,104
mvaa 60,224,104
mvaa 60,208,32
mvaa 68,208,36
mvaa 68,224,108
mvaa 64,212,108
mvaa 64,228,36
s_waitcnt lgkmcnt(0)
mvaa 32,236,36
s_waitcnt vmcnt(11)
s_barrier
s_add_u32 m0,s62,0
mvaa 32,220,108
dl 228,40,53,0
mvaa 36,232,108
ta 56,234,32768
mvaa 36,216,36
ta 58,234,33280
mvaa 28,216,32
ta 60,234,49152
mvaa 28,232,104
ta 62,234,49664
mvaa 24,220,104
dl 229,40,53,1024
mvaa 24,236,32
s_add_u32 s53,s53,s57
ta 48,235,49152
mvaa 16,236,28
ta 50,235,49664
mvaa 16,220,100
ta 52,235,32768
mvaa 20,232,100
ta 54,235,33280
mvaa 20,216,28
dl 230,44,54,2048
mvaa 12,216,24
ta 72,236,32768
mvaa 12,232,96
ta 74,236,33280
mvaa 8,220,96
ta 76,236,49152
mvaa 8,236,24
ta 78,236,49664
mvaa 76,24,40
dl 231,44,54,3072
mvaa 76,96,44
s_add_u32 m0,s62,16384
s_add_u32 s54,s54,s57
ta 64,237,49152
mvaa 96,96,20
ta 66,237,49664
mvaa 96,24,0
ta 68,237,32768
mvaa 100,28,0
ta 70,237,33280
mvaa 100,100,20
dl 228,40,53,0
mvaa 80,100,44
ta 208,234,36864
mvaa 80,28,40
ta 210,234,37376
mvaa 84,32,40
ta 224,234,53248
mvaa 84,104,44
ta 226,234,53760
mvaa 104,104,20
ta 212,235,53248
mvaa 104,32,0
dl 229,40,53,1024
mvaa 108,36,0
s_add_u32 s53,s53,s58
ta 214,235,53760
mvaa 108,108,20
ta 228,235,36864
mvaa 88,108,44
ta 230,235,37376
mvaa 88,36,40
ta 192,236,40960
s_waitcnt lgkmcnt(9)
mvaa 0,56,4
dl 230,44,54,2048
mvaa 0,60,80
ta 216,236,36864
mvaa 4,48,80
ta 218,236,37376
mvaa 4,52,4
ta 232,236,53248
mvaa 92,52,8
ta 234,236,53760
mvaa 92,48,84
dl 231,44,54,3072
mvaa 72,60,84
s_add_u32 m0,s98,65536
ta 220,237,53248
s_add_u32 s54,s54,s58
mvaa 72,56,8
ta 222,237,53760
mvaa 144,56,12
ta 236,237,36864
mvaa 144,60,88
ta 238,237,37376
mvaa 148,48,88
dl 224,36,52,0
mvaa 148,52,12
ta 194,236,41472
mvaa 156,52,16
ta 176,236,57344
mvaa 156,48,92
ta 178,236,57856
mvaa 152,60,92
ta 196,237,57344
mvaa 152,56,16
ta 198,237,57856
mvaa 160,72,16
dl 225,36,52,1024
mvaa 160,76,92
ta 180,237,40960
mvaa 164,64,92
ta 182,237,41472
mvaa 164,68,16
ra 96,233,0
mvaa 172,68,12
ta 0,234,40960
mvaa 172,64,88
dl 226,36,52,2048
mvaa 168,76,88
ta 2,234,41472
mvaa 168,72,12
ta 20,234,57344
maaa 200,72,8
ta 22,234,57856
maaa 200,76,84
ta 44,235,57344
maaa 184,64,84
buffer_store_dwordx4 v[200:203],v244,s[24:27],s67 offen offset:2048 sc0 sc1
maaa 184,68,8
ta 46,235,57856
maaa 188,68,4
ta 40,235,40960
maaa 188,64,80
ta 42,235,41472
v_accvgpr_read_b32 v176,a200
maaa 204,76,80
ra 24,232,0
v_accvgpr_read_b32 v177,a201
maaa 204,72,4
dl 227,36,52,3072
s_waitcnt lgkmcnt(8)
maaa 172,208,4
s_add_u32 s52,s52,s56
s_add_u32 m0,s98,69632
ra 100,233,2048
maaa 172,224,80
ra 28,232,2048
v_accvgpr_read_b32 v178,a202
maaa 156,212,80
ra 32,232,4096
v_accvgpr_read_b32 v179,a203
maaa 156,228,4
ra 104,233,4096
v_accvgpr_read_b32 v180,a184
maaa 152,228,8
ra 108,233,6144
v_accvgpr_read_b32 v181,a185
maaa 152,212,84
dl 224,36,52,0
maaa 168,224,84
ra 36,232,6144
v_accvgpr_read_b32 v182,a186
maaa 168,208,8
ta 52,238,45056
v_accvgpr_read_b32 v183,a187
maaa 164,208,12
ta 54,238,45568
v_accvgpr_read_b32 v188,a188
maaa 164,224,88
ta 48,238,61440
v_accvgpr_read_b32 v189,a189
maaa 148,212,88
buffer_store_dwordx4 v[204:207],v244,s[24:27],s67 offen offset:3072 sc0 sc1
maaa 148,228,12
)"
R"(s_mov_b32 s67,28672
ta 50,238,61952
v_accvgpr_read_b32 v190,a190
maaa 144,228,16
ta 60,239,61440
v_accvgpr_read_b32 v184,a204
maaa 144,212,92
ta 62,239,61952
v_accvgpr_read_b32 v191,a191
maaa 160,224,92
ta 56,239,45056
v_accvgpr_read_b32 v185,a205
maaa 160,208,16
dl 225,36,52,1024
maaa 128,216,16
ta 58,239,45568
v_accvgpr_read_b32 v186,a206
maaa 128,232,92
v_accvgpr_read_b32 v187,a207
maaa 112,220,92
maaa 112,236,16
maaa 116,236,12
dl 226,36,52,2048
maaa 116,220,88
maaa 132,232,88
maaa 132,216,12
maaa 136,216,8
maaa 136,232,84
dl 227,36,52,3072
maaa 120,220,84
s_add_u32 s52,s52,s60
maaa 120,236,8
maaa 124,236,4
maaa 124,220,80
maaa 140,232,80
maaa 140,216,4
buffer_store_dwordx4 v[208:211],v244,s[24:27],s67 offen offset:0 sc0 sc1
s_waitcnt lgkmcnt(0)
mvaa 40,0,4
mvaa 40,20,80
mvaa 44,44,80
mvaa 44,40,4
mvaa 52,40,8
mvaa 52,44,84
mvaa 48,20,84
mvaa 48,0,8
mvaa 56,0,12
mvaa 56,20,88
mvaa 60,44,88
mvaa 60,40,12
mvaa 68,40,16
mvaa 68,44,92
mvaa 64,20,92
mvaa 64,0,16
mvaa 32,192,16
s_waitcnt vmcnt(11)
s_barrier
s_add_u32 m0,s62,32768
mvaa 32,176,92
dl 228,40,53,0
mvaa 36,196,92
ta 68,234,0
mvaa 36,180,16
ta 70,234,512
mvaa 28,180,12
ta 64,234,16384
mvaa 28,196,88
ta 66,234,16896
mvaa 24,176,88
dl 229,40,53,1024
mvaa 24,192,12
s_add_u32 s53,s53,s57
ta 76,235,16384
mvaa 16,192,8
ta 78,235,16896
mvaa 16,176,84
ta 72,235,0
mvaa 20,196,84
ta 74,235,512
mvaa 20,180,8
ta 228,236,0
mvaa 12,180,4
dl 230,44,54,2048
mvaa 12,196,80
ta 230,236,512
mvaa 8,176,80
ta 212,236,16384
mvaa 8,192,4
ta 214,236,16896
mvaa 76,4,52
ta 224,237,16384
mvaa 76,80,48
dl 231,44,54,3072
mvaa 96,80,60
s_add_u32 m0,s62,49152
s_add_u32 s54,s54,s57
ta 226,237,16896
mvaa 96,4,56
ta 208,237,0
mvaa 100,8,56
ta 210,237,512
mvaa 100,84,60
ta 236,234,4096
mvaa 80,84,48
dl 228,40,53,0
mvaa 80,8,52
ta 238,234,4608
mvaa 84,12,52
ta 220,234,20480
mvaa 84,88,48
ta 222,234,20992
mvaa 104,88,60
ta 232,235,20480
mvaa 104,12,56
dl 229,40,53,1024
mvaa 108,16,56
s_add_u32 s53,s53,s58
ta 234,235,20992
mvaa 108,92,60
ta 216,235,4096
mvaa 88,92,48
ta 218,235,4608
mvaa 88,16,52
ta 40,236,4096
s_waitcnt lgkmcnt(8)
mvaa 0,68,24
dl 230,44,54,2048
mvaa 0,64,96
ta 42,236,4608
mvaa 4,76,96
ta 44,236,20480
mvaa 4,72,24
ta 46,236,20992
mvaa 92,72,28
ta 20,237,20480
mvaa 92,76,100
ta 22,237,20992
mvaa 72,64,100
dl 231,44,54,3072
mvaa 72,68,28
s_add_u32 m0,s98,0
ta 200,238,12288
s_add_u32 s54,s54,s58
mvaa 144,68,32
ta 202,238,12800
mvaa 144,64,104
ta 0,237,4096
mvaa 148,76,104
ta 2,237,4608
mvaa 148,72,32
dl 224,36,52,0
mvaa 156,72,36
ta 184,238,28672
mvaa 156,76,108
ta 186,238,29184
mvaa 152,64,108
ta 204,239,28672
mvaa 152,68,36
ta 206,239,29184
mvaa 160,228,36
dl 225,36,52,1024
mvaa 160,212,108
ta 188,239,12288
mvaa 164,224,108
ta 190,239,12800
mvaa 164,208,36
ta 180,234,8192
mvaa 172,208,32
ta 182,234,8704
mvaa 172,224,104
dl 226,36,52,2048
mvaa 168,212,104
ta 196,234,24576
mvaa 168,228,32
ta 198,234,25088
mvaa 176,228,28
ta 176,235,24576
mvaa 176,212,100
ta 178,235,25088
mvaa 180,224,100
ta 192,235,8192
mvaa 180,208,28
buffer_store_dwordx4 v[212:215],v244,s[24:27],s67 offen offset:1024 sc0 sc1
mvaa 188,208,24
ta 194,235,8704
mvaa 188,224,96
ra 4,232,32768
mvaa 184,212,96
ra 80,233,32768
mvaa 184,228,24
ra 84,233,34816
s_waitcnt lgkmcnt(9)
maaa 172,236,24
dl 227,36,52,3072
maaa 172,220,96
s_add_u32 s52,s52,s56
s_add_u32 m0,s98,4096
ra 8,232,34816
maaa 156,232,96
ra 88,233,36864
maaa 156,216,24
ta 56,236,8192
maaa 152,216,28
ra 12,232,36864
v_accvgpr_read_b32 v192,a172
maaa 152,232,100
dl 224,36,52,0
maaa 168,220,100
ta 58,236,8704
v_accvgpr_read_b32 v193,a173
maaa 168,236,28
ta 60,236,24576
v_accvgpr_read_b32 v194,a174
maaa 164,236,32
ta 62,236,25088
v_accvgpr_read_b32 v195,a175
maaa 164,220,104
ta 48,237,24576
v_accvgpr_read_b32 v196,a156
maaa 148,232,104
buffer_store_dwordx4 v[216:219],v244,s[24:27],s67 offen offset:2048 sc0 sc1
maaa 148,216,32
ta 50,237,25088
v_accvgpr_read_b32 v197,a157
maaa 144,216,36
ra 92,233,38912
v_accvgpr_read_b32 v198,a158
maaa 144,232,108
ra 16,232,38912
v_accvgpr_read_b32 v199,a159
maaa 160,220,108
ta 52,237,8192
v_accvgpr_read_b32 v204,a152
maaa 160,236,36
ta 54,237,8704
v_accvgpr_read_b32 v205,a153
maaa 128,40,36
dl 225,36,52,1024
maaa 128,44,108
v_accvgpr_read_b32 v206,a154
v_accvgpr_read_b32 v207,a155
maaa 112,20,108
v_accvgpr_read_b32 v200,a168
v_accvgpr_read_b32 v201,a169
maaa 112,0,36
v_accvgpr_read_b32 v202,a170
v_accvgpr_read_b32 v203,a171
maaa 116,0,32
v_add_u32_e32 v233,0x10000,v233
v_add_u32_e32 v232,0x10000,v232
maaa 116,20,104
dl 226,36,52,2048
maaa 132,44,104
maaa 132,40,32
maaa 136,40,28
maaa 136,44,100
maaa 120,20,100
dl 227,36,52,3072
maaa 120,0,28
s_add_u32 s52,s52,s60
maaa 124,0,24
maaa 124,20,96
maaa 140,44,96
L_vs13:
s_cbranch_execz L_vr0s
maaa 140,40,24
buffer_store_dwordx4 v[220:223],v244,s[24:27],s67 offen offset:3072 sc0 sc1
mvaa 40,180,24
s_mov_b32 s67,0x5550055
s_mov_b32 s26,2097152
v_cndmask_b32_e64 v116,v241,0,s[66:67]
s_waitcnt lgkmcnt(0)
mvaa 40,196,96
s_mov_b32 s66,0xa0000
s_mov_b32 s67,0xaaa00aa
v_cndmask_b32_e64 v117,v241,0,s[66:67]
mvaa 44,176,96
s_mov_b32 s66,0x150001
s_mov_b32 s67,0x15550155
v_cndmask_b32_e64 v118,v241,0,s[66:67]
mvaa 44,192,24
s_mov_b32 s66,0x2a0002
s_mov_b32 s67,0x2aaa02aa
v_cndmask_b32_e64 v119,v241,0,s[66:67]
mvaa 52,192,28
s_lshr_b32 s66,s18,2
s_lshl_b32 s67,s18,13
s_lshl_b32 s66,s66,13
mvaa 52,176,100
s_mov_b32 s27,131072
v_cndmask_b32_e64 v120,v116,v241,s[64:65]
v_cndmask_b32_e64 v121,v117,v241,s[64:65]
mvaa 48,196,100
s_mov_b64 s[24:25],s[82:83]
v_cndmask_b32_e64 v122,v118,v241,s[64:65]
v_cndmask_b32_e64 v123,v119,v241,s[64:65]
mvaa 48,180,28
s_mov_b32 s83,0
s_or_b32 s82,s15,16384
mvaa 56,180,32
mvaa 56,196,104
mvaa 60,176,104
mvaa 60,192,32
mvaa 68,192,36
mvaa 68,176,108
mvaa 64,196,108
mvaa 64,180,36
mvaa 32,56,36
s_waitcnt vmcnt(11)
s_barrier
s_add_u32 m0,s62,0
mvaa 32,60,108
dl 228,40,53,0
mvaa 36,48,108
ta 72,234,32768
mvaa 36,52,36
ta 74,234,33280
mvaa 28,52,32
ta 76,234,49152
mvaa 28,48,104
ta 78,234,49664
mvaa 24,60,104
ta 64,235,49152
mvaa 24,56,32
dl 229,40,53,1024
mvaa 16,56,28
s_add_u32 s53,s53,s57
ta 66,235,49664
mvaa 16,60,100
ta 68,235,32768
mvaa 20,48,100
ta 70,235,33280
mvaa 20,52,28
ta 208,236,32768
mvaa 12,52,24
dl 230,44,54,2048
mvaa 12,48,96
ta 210,236,33280
mvaa 8,60,96
ta 224,236,49152
mvaa 8,56,24
ta 226,236,49664
mvaa 76,24,200
ta 212,237,49152
mvaa 76,96,184
dl 231,44,54,3072
mvaa 96,96,204
s_add_u32 m0,s62,16384
s_add_u32 s54,s54,s57
ta 214,237,49664
mvaa 96,24,188
ta 228,237,32768
mvaa 100,28,188
ta 230,237,33280
mvaa 100,100,204
ta 216,234,36864
mvaa 80,100,184
ta 218,234,37376
mvaa 80,28,200
dl 228,40,53,0
mvaa 84,32,200
ta 232,234,53248
mvaa 84,104,184
ta 234,234,53760
mvaa 104,104,204
ta 220,235,53248
mvaa 104,32,188
ta 222,235,53760
mvaa 108,36,188
dl 229,40,53,1024
mvaa 108,108,204
s_add_u32 s53,s53,s58
ta 236,235,36864
mvaa 88,108,184
ta 238,235,37376
mvaa 88,36,200
ta 0,236,36864
s_waitcnt lgkmcnt(8)
mvaa 0,72,4
ta 2,236,37376
mvaa 0,76,80
dl 230,44,54,2048
mvaa 4,64,80
ta 20,236,53248
mvaa 4,68,4
ta 22,236,53760
mvaa 92,68,8
ta 44,237,53248
mvaa 92,64,84
ta 46,237,53760
mvaa 72,76,84
dl 231,44,54,3072
mvaa 72,72,8
s_add_u32 m0,s98,32768
ta 40,237,36864
s_add_u32 s54,s54,s58
mvaa 144,72,12
ta 42,237,37376
mvaa 144,76,88
ta 192,234,40960
mvaa 148,64,88
ta 194,234,41472
mvaa 148,68,12
ta 176,234,57344
mvaa 156,68,16
dl 224,36,52,0
mvaa 156,64,92
ta 178,234,57856
mvaa 152,76,92
ta 196,235,57344
mvaa 152,72,16
ta 198,235,57856
mvaa 160,208,16
ta 180,235,40960
mvaa 160,224,92
dl 225,36,52,1024
mvaa 164,212,92
ta 182,235,41472
mvaa 164,228,16
ta 52,236,40960
mvaa 172,228,12
ta 54,236,41472
mvaa 172,212,88
ta 48,236,57344
mvaa 168,224,88
s_mov_b64 exec,3
global_load_dword a250,v253,s[96:97]
s_mov_b64 exec,-1
mvaa 168,208,12
ta 50,236,57856
v_lshrrev_b32_e32 v253,1,v244
mvaa 176,208,8
ta 60,237,57344
mvaa 176,224,84
ta 62,237,57856
mvaa 180,212,84
ta 56,237,40960
mvaa 180,228,8
dl 226,36,52,2048
mvaa 188,228,4
ta 58,237,41472
mvaa 188,212,80
ra 96,233,0
mvaa 184,224,80
ra 24,232,0
mvaa 184,208,4
ra 100,233,2048
s_waitcnt lgkmcnt(8)
mvaa 192,216,4
dl 227,36,52,3072
mvaa 192,232,80
s_add_u32 s52,s52,s56
s_add_u32 m0,s98,36864
ra 28,232,2048
mvaa 196,220,80
ra 104,233,4096
mvaa 196,236,4
ra 32,232,4096
mvaa 204,236,8
ta 188,238,45056
mvaa 204,220,84
ta 190,238,45568
mvaa 200,232,84
dl 224,36,52,0
mvaa 200,216,8
ta 204,238,61440
maaa 164,216,12
ta 206,238,61952
maaa 164,232,88
ta 184,239,61440
maaa 148,220,88
ta 186,239,61952
maaa 148,236,12
dl 225,36,52,1024
maaa 144,236,16
ra 108,233,6144
v_add_u32_e32 v233,0xffff0000,v233
maaa 144,220,92
ra 36,232,6144
v_add_u32_e32 v232,0xffff0000,v232
maaa 160,232,92
ta 200,239,45056
v_accvgpr_read_b32 v208,a164
maaa 160,216,16
ta 202,239,45568
v_accvgpr_read_b32 v209,a165
maaa 128,0,16
dl 226,36,52,2048
maaa 128,20,92
v_accvgpr_read_b32 v210,a166
v_accvgpr_read_b32 v211,a167
maaa 112,44,92
v_accvgpr_read_b32 v212,a148
v_accvgpr_read_b32 v213,a149
maaa 112,40,16
v_accvgpr_read_b32 v214,a150
v_accvgpr_read_b32 v215,a151
maaa 116,40,12
v_accvgpr_read_b32 v220,a144
v_accvgpr_read_b32 v221,a145
maaa 116,44,88
dl 227,36,52,3072
maaa 132,20,88
s_add_u32 s52,s52,s60
v_accvgpr_read_b32 v222,a146
v_accvgpr_read_b32 v223,a147
maaa 132,0,12
s_lshl_b32 s60,%1,7
v_accvgpr_read_b32 v216,a160
v_accvgpr_read_b32 v217,a161
maaa 136,0,8
s_and_b32 s87,s60,7552
v_accvgpr_read_b32 v218,a162
v_accvgpr_read_b32 v219,a163
maaa 136,20,84
s_add_u32 s61,s60,83914752
maaa 120,44,84
maaa 120,40,8
maaa 124,40,4
maaa 124,44,80
maaa 140,20,80
maaa 140,0,4
mvaa 40,192,4
mvaa 40,176,80
mvaa 44,196,80
mvaa 44,180,4
mvaa 52,180,8
mvaa 52,196,84
mvaa 48,176,84
mvaa 48,192,8
mvaa 56,192,12
s_waitcnt vmcnt(9) lgkmcnt(0)
s_barrier
mvaa 56,176,88
s_cmp_eq_u32 s12,0
s_cselect_b64 exec,1,0
buffer_atomic_add v247,off,s[8:11],s85 sc1
s_mov_b64 exec,-1
mvaa 60,196,88
s_add_u32 m0,s62,32768
ta 172,234,0
s_lshr_b32 s85,s86,1
mvaa 60,180,12
s_lshl_b32 s85,s85,11
ta 174,234,512
v_bfe_u32 v247,v244,2,6
mvaa 68,180,16
ta 156,234,16384
mvaa 68,196,92
ta 158,234,16896
mvaa 64,176,92
dl 228,40,53,0
mvaa 64,192,16
ta 168,235,16384
mvaa 32,52,16
ta 170,235,16896
mvaa 32,48,92
ta 152,235,0
mvaa 36,60,92
ta 154,235,512
mvaa 36,56,16
dl 229,40,53,1024
mvaa 28,56,12
s_add_u32 s53,s53,s57
ta 68,236,0
mvaa 28,60,88
ta 70,236,512
mvaa 24,48,88
ta 64,236,16384
mvaa 24,52,12
ta 66,236,16896
mvaa 16,52,8
dl 230,44,54,2048
mvaa 16,48,84
ta 76,237,16384
mvaa 20,60,84
ta 78,237,16896
mvaa 20,56,8
ta 72,237,0
mvaa 12,56,4
ta 74,237,512
mvaa 12,60,80
dl 231,44,54,3072
mvaa 8,48,80
s_add_u32 m0,s62,49152
s_add_u32 s54,s54,s57
ta 228,234,4096
mvaa 8,52,4
ta 230,234,4608
mvaa 76,4,188
ta 212,234,20480
mvaa 76,80,204
ta 214,234,20992
mvaa 96,80,184
dl 228,40,53,0
mvaa 96,4,200
ta 224,235,20480
mvaa 100,8,200
ta 226,235,20992
mvaa 100,84,184
ta 208,235,4096
mvaa 80,84,204
ta 210,235,4608
mvaa 80,8,188
dl 229,40,53,1024
mvaa 84,12,188
s_add_u32 s53,s53,s58
ta 236,236,4096
mvaa 84,88,204
ta 238,236,4608
mvaa 104,88,184
ta 220,236,20480
mvaa 104,12,200
ta 222,236,20992
mvaa 108,16,200
dl 230,44,54,2048
mvaa 108,92,184
ta 232,237,20480
mvaa 88,92,204
ta 234,237,20992
mvaa 88,16,188
ta 216,237,4096
s_waitcnt lgkmcnt(8)
mvaa 0,172,24
ta 218,237,4608
mvaa 0,156,96
dl 231,44,54,3072
mvaa 4,168,96
s_add_u32 m0,s98,65536
ta 40,234,8192
s_add_u32 s54,s54,s58
mvaa 4,152,24
ta 42,234,8704
mvaa 92,152,28
ta 44,234,24576
mvaa 92,168,100
ta 46,234,25088
mvaa 72,156,100
dl 224,36,52,0
mvaa 72,172,28
ta 20,235,24576
mvaa 144,172,32
ta 22,235,25088
mvaa 144,156,104
ta 0,235,8192
mvaa 148,168,104
ta 2,235,8704
mvaa 148,152,32
dl 225,36,52,1024
mvaa 156,152,36
ta 180,236,8192
mvaa 156,168,108
ta 182,236,8704
mvaa 152,156,108
ta 196,236,24576
mvaa 152,172,36
ta 198,236,25088
mvaa 160,68,36
dl 226,36,52,2048
mvaa 160,64,108
ta 176,237,24576
mvaa 164,76,108
ta 178,237,25088
mvaa 164,72,36
ta 192,237,8192
mvaa 172,72,32
ta 194,237,8704
mvaa 172,76,104
dl 227,36,52,3072
mvaa 168,64,104
s_add_u32 s52,s52,s56
s_add_u32 m0,s98,69632
ta 56,238,12288
mvaa 168,68,32
s_mov_b32 s98,s94
ta 58,238,12800
s_mov_b32 s99,s98
mvaa 176,68,28
s_and_b32 s94,s12,1
ta 60,238,28672
s_lshl_b32 s94,s94,14
mvaa 176,64,100
s_mul_i32 s56,s59,12288
ta 62,238,29184
mvaa 180,76,100
dl 224,36,52,0
mvaa 180,72,28
ta 48,239,28672
v_mov_b32_e32 v224,v244
mvaa 188,72,24
ta 50,239,29184
mvaa 188,76,96
ta 52,239,12288
mvaa 184,64,96
ta 54,239,12800
mvaa 184,68,24
dl 225,36,52,1024
mvaa 192,228,24
ra 80,233,0
v_add_u32_e32 v225,65536,v224
mvaa 192,212,96
ra 4,232,0
mvaa 196,224,96
ra 84,233,2048
s_waitcnt lgkmcnt(8)
mvaa 196,208,24
ra 8,232,2048
mvaa 204,208,28
dl 226,36,52,2048
mvaa 204,224,100
ra 88,233,4096
v_mov_b32_e32 v226,1065369472
mvaa 200,212,100
ra 12,232,4096
mvaa 200,228,28
ra 16,232,6144
v_add_u32_e32 v232,0x8000,v232
mvaa 208,228,32
ra 92,233,6144
v_add_u32_e32 v233,0x8000,v233
mvaa 208,212,104
dl 227,36,52,3072
mvaa 212,224,104
s_mov_b64 s[36:37],s[28:29]
s_mov_b32 s38,8388608
s_mov_b32 s39,131072
mvaa 212,208,32
s_lshl_b32 s52,s12,12
mvaa 220,208,36
mvaa 220,224,108
mvaa 216,212,108
mvaa 216,228,36
maaa 128,236,36
maaa 128,220,108
maaa 112,232,108
maaa 112,216,36
maaa 116,216,32
maaa 116,232,104
maaa 132,220,104
maaa 132,236,32
maaa 136,236,28
maaa 136,220,100
maaa 120,232,100
maaa 120,216,28
maaa 124,216,24
maaa 124,232,96
maaa 140,220,96
s_waitcnt vmcnt(8) lgkmcnt(0)
s_barrier
s_add_u32 m0,s62,0
maaa 140,236,24
dl 228,40,53,0
mvaa 40,40,24
ta 200,234,32768
mvaa 40,44,96
ta 202,234,33280
mvaa 44,20,96
ta 184,234,49152
mvaa 44,0,24
ta 186,234,49664
mvaa 52,0,28
dl 229,40,53,1024
mvaa 52,20,100
s_add_u32 s53,s53,s57
ta 204,235,49152
mvaa 48,44,100
ta 206,235,49664
mvaa 48,40,28
ta 188,235,32768
mvaa 56,40,32
ta 190,235,33280
mvaa 56,44,104
dl 230,44,54,2048
mvaa 60,20,104
ta 164,236,32768
mvaa 60,0,32
ta 166,236,33280
mvaa 68,0,36
ta 148,236,49152
mvaa 68,20,108
ta 150,236,49664
mvaa 64,44,108
dl 231,44,54,3072
mvaa 64,40,36
s_add_u32 m0,s62,16384
s_add_u32 s54,s54,s57
ta 160,237,49152
mvaa 32,180,36
ta 162,237,49664
mvaa 32,196,108
ta 144,237,32768
mvaa 36,176,108
ta 146,237,33280
mvaa 36,192,36
dl 228,40,53,0
mvaa 28,192,32
ta 152,234,36864
mvaa 28,176,104
ta 154,234,37376
mvaa 24,196,104
L_vs14:
s_cbranch_execz L_vr0s
ta 168,234,53248
mvaa 24,180,32
ta 170,234,53760
mvaa 16,180,28
dl 229,40,53,1024
mvaa 16,196,100
s_add_u32 s53,s53,s58
ta 156,235,53248
mvaa 20,176,100
ta 158,235,53760
mvaa 20,192,28
ta 172,235,36864
mvaa 12,192,24
ta 174,235,37376
mvaa 12,176,96
dl 230,44,54,2048
mvaa 8,196,96
ta 208,236,36864
mvaa 8,180,24
ta 210,236,37376
mvaa 76,24,56
ta 224,236,53248
mvaa 76,96,60
ta 226,236,53760
mvaa 96,96,48
dl 231,44,54,3072
mvaa 96,24,52
s_add_u32 s54,s54,s58
ta 212,237,53248
s_add_u32 s58,s56,8192
mvaa 100,28,52
ta 214,237,53760
mvaa 100,100,48
ta 228,237,36864
mvaa 80,100,60
ta 230,237,37376
mvaa 80,28,56
ta 216,234,40960
mvaa 84,32,56
ta 218,234,41472
mvaa 84,104,60
ta 232,234,57344
mvaa 104,104,48
ta 234,234,57856
mvaa 104,32,52
ta 220,235,57344
mvaa 108,36,52
ta 222,235,57856
mvaa 108,108,48
ta 236,235,40960
mvaa 88,108,60
ta 238,235,41472
mvaa 88,36,56
ta 0,236,40960
s_waitcnt lgkmcnt(10)
mvaa 0,200,4
ta 2,236,41472
mvaa 0,184,80
ta 20,236,57344
mvaa 4,204,80
ta 22,236,57856
mvaa 4,188,4
ta 192,237,57344
mvaa 92,188,8
ta 194,237,57856
mvaa 92,204,84
ta 176,237,40960
mvaa 72,184,84
ta 178,237,41472
mvaa 72,200,8
ta 196,238,45056
mvaa 144,200,12
ta 198,238,45568
mvaa 144,184,88
ta 180,238,61440
mvaa 148,204,88
ta 182,238,61952
mvaa 148,188,12
ra 24,232,0
mvaa 156,188,16
ra 28,232,2048
mvaa 156,204,92
ra 32,232,4096
mvaa 152,184,92
ra 36,232,6144
mvaa 152,200,16
ta 188,239,61440
mvaa 160,164,16
ta 190,239,61952
mvaa 160,148,92
ta 204,239,45056
mvaa 164,160,92
ta 206,239,45568
mvaa 164,144,16
mvaa 172,144,12
mvaa 172,160,88
mvaa 168,148,88
mvaa 168,164,12
mvaa 176,164,8
mvaa 176,148,84
mvaa 180,160,84
mvaa 180,144,8
mvaa 188,144,4
mvaa 188,160,80
mvaa 184,148,80
mvaa 184,164,4
mvaa 192,152,4
mvaa 192,168,80
mvaa 196,156,80
mvaa 196,172,4
mvaa 204,172,8
mvaa 204,156,84
mvaa 200,168,84
mvaa 200,152,8
mvaa 208,152,12
mvaa 208,168,88
mvaa 212,156,88
mvaa 212,172,12
mvaa 220,172,16
mvaa 220,156,92
mvaa 216,168,92
mvaa 216,152,16
maaa 128,208,16
maaa 128,224,92
maaa 112,212,92
s_waitcnt vmcnt(0) lgkmcnt(0)
s_barrier
s_add_u32 m0,s62,32768
maaa 112,228,16
dl 228,40,53,0
maaa 116,228,12
ta 40,234,0
maaa 116,212,88
ta 42,234,512
maaa 132,224,88
ta 44,235,0
maaa 132,208,12
ta 46,235,512
maaa 136,208,8
dl 229,40,53,1024
maaa 136,224,84
s_add_u32 s53,s53,s57
ta 48,236,0
maaa 120,212,84
ta 50,236,512
maaa 120,228,8
ta 52,237,0
maaa 124,228,4
ta 54,237,512
maaa 124,212,80
dl 230,44,54,2048
maaa 140,224,80
ta 56,234,4096
maaa 140,208,4
ta 58,234,4608
mvaa 40,216,4
ta 60,235,4096
mvaa 40,232,80
ta 62,235,4608
mvaa 44,220,80
dl 231,44,54,3072
mvaa 44,236,4
s_add_u32 m0,s62,49152
ta 64,236,4096
s_add_u32 s54,s54,s57
mvaa 52,236,8
s_add_u32 s57,s56,4096
ta 66,236,4608
mvaa 52,220,84
ta 68,237,4096
mvaa 48,232,84
ta 70,237,4608
mvaa 48,216,8
buffer_load_dword v248,v250,s[8:11],0 offen sc1
mvaa 56,216,12
ta 72,234,8192
mvaa 56,232,88
ta 74,234,8704
mvaa 60,220,88
ta 76,235,8192
mvaa 60,236,12
ta 78,235,8704
mvaa 68,236,16
dl 228,40,53,0
mvaa 68,220,92
ta 96,236,8192
mvaa 64,232,92
ta 98,236,8704
mvaa 64,216,16
ta 100,237,8192
mvaa 32,0,16
ta 102,237,8704
mvaa 32,20,92
dl 229,40,53,1024
mvaa 36,192,92
s_lshl_b32 s43,s19,15
ta 104,238,12288
s_add_u32 s42,s43,16384
)"
R"(mvaa 36,176,16
s_add_u32 s53,s87,83898368
ta 106,238,12800
s_lshl_b32 s87,s91,8
mvaa 28,176,12
s_lshl_b32 s91,s23,2
ta 108,239,12288
s_mov_b32 s40,858993459
mvaa 28,192,88
s_mov_b32 s41,858993459
ta 110,239,12800
v_cndmask_b32_e64 v228,0,v226,s[40:41]
mvaa 24,20,88
dl 230,44,54,2048
mvaa 24,0,12
ta 144,234,16384
v_cndmask_b32_e64 v229,0,v226,s[40:41]
mvaa 16,0,8
ta 146,234,16896
v_cndmask_b32_e64 v230,0,v226,s[40:41]
mvaa 16,20,84
ta 164,235,16384
mvaa 20,192,84
ta 166,235,16896
mvaa 20,176,8
dl 231,44,54,3072
mvaa 12,176,4
s_add_u32 s54,s87,s88
ta 168,236,16384
s_lshl_b32 s87,s12,16
mvaa 12,192,80
s_add_u32 s87,s87,s54
ta 170,236,16896
s_andn2_b32 s87,s87,64
mvaa 8,20,80
s_lshl_b32 s88,s66,5
ta 244,235,24576
s_add_u32 s63,s87,s88
mvaa 8,0,4
s_lshl_b32 s88,s67,5
ta 246,235,25088
s_add_u32 s65,s87,s88
mvaa 76,4,196
s_and_b32 s87,s20,4
ta 212,235,20480
s_lshl_b32 s87,s87,8
mvaa 76,80,180
s_and_b32 s88,s20,3
ta 214,235,20992
s_add_u32 s85,s85,s87
mvaa 96,80,188
s_add_u32 s85,s85,s89
ta 236,237,20480
s_lshl_b32 s89,s88,20
mvaa 96,4,204
s_add_u32 s85,s85,s89
ta 238,237,20992
s_and_b32 s89,s86,7
mvaa 100,8,204
s_add_u32 s49,s49,s87
ta 216,236,20480
s_lshl_b32 s89,s89,7
mvaa 100,84,188
s_add_u32 s49,s49,s89
ta 218,236,20992
s_lshl_b32 s89,s88,18
mvaa 80,84,180
s_add_u32 s49,s49,s89
ta 192,234,20480
s_lshl_b32 s89,s12,6
mvaa 80,8,196
s_add_u32 s89,s89,83886080
ta 194,234,20992
s_add_u32 s91,s91,s89
mvaa 84,12,196
s_mov_b32 s46,16384
ra 20,233,0
s_mov_b32 s86,268435456
mvaa 84,88,180
s_bfe_u32 s87,s98,327691
s_cselect_b32 s87,2048,0
ra 0,232,32768
v_cndmask_b32_e64 v231,0,v226,s[40:41]
mvaa 104,88,188
s_mov_b32 s40,0xf0000
ra 80,233,4096
s_mov_b32 s41,0xfff00ff
mvaa 104,12,204
s_sub_u32 s98,s98,s87
ra 4,233,32768
v_cndmask_b32_e64 v226,0,v252,s[40:41]
mvaa 108,16,204
s_mov_b32 s40,0x3f0003
ra 84,232,36864
s_mov_b32 s41,0x3fff03ff
mvaa 108,92,188
s_sub_u32 s47,32768,s43
ra 8,233,2048
v_cndmask_b32_e64 v227,0,v252,s[40:41]
mvaa 88,92,180
s_mov_b32 s40,0xfff0ffff
ra 88,233,36864
s_mov_b32 s41,0xf000ff00
mvaa 88,16,196
s_add_u32 s47,s47,32768
ra 12,232,34816
s_lshr_b32 s45,s15,1
s_waitcnt lgkmcnt(10)
mvaa 0,40,24
s_xor_b32 s62,s63,128
ta 188,237,16384
s_xor_b32 s64,s65,128
mvaa 4,44,24
s_ff1_i32_b32 s80,s45
ta 190,237,16896
s_sub_u32 s44,s56,2048
mvaa 92,44,28
ra 92,233,6144
mvaa 72,40,28
ra 16,233,34816
mvaa 144,40,32
mvaa 148,44,32
mvaa 156,44,36
mvaa 152,40,36
mvaa 160,48,36
ra 44,232,38912
v_mov_b32_e32 v232,0xc1a00000
mvaa 164,52,36
ra 40,233,38912
v_mov_b32_e32 v233,0xc1a00000
mvaa 172,52,32
mvaa 168,48,32
mvaa 176,48,28
mvaa 180,52,28
mvaa 188,52,24
mvaa 184,48,24
mvaa 192,56,24
ta 52,239,28672
mvaa 196,60,24
ta 54,239,29184
mvaa 204,60,28
mvaa 200,56,28
mvaa 208,56,32
mvaa 212,60,32
mvaa 220,60,36
mvaa 216,56,36
maaa 128,64,36
maaa 112,68,36
maaa 116,68,32
maaa 132,64,32
maaa 136,64,28
maaa 120,68,28
maaa 124,68,24
maaa 140,64,24
mvaa 40,72,24
mvaa 44,76,24
ta 64,238,28672
ta 66,238,29184
mvaa 52,76,28
s_waitcnt vmcnt(4)
v_and_b32_e32 v248,v242,v248
v_cmp_ne_u32_e64 s[100:101],v243,v248
mvaa 48,72,28
s_waitcnt vmcnt(0)
s_barrier
ta 148,234,32768
ta 150,234,33280
ta 152,234,49152
ta 154,234,49664
ta 156,235,49152
mvaa 56,72,32
s_cmp_lg_u64 s[100:101],0
s_cbranch_scc1 L_q_spin
L_q_ok:
ta 158,235,49664
ta 160,235,32768
ta 162,235,33280
ta 172,236,32768
ta 174,236,33280
ta 176,236,49152
ta 178,236,49664
mvaa 60,76,32
buffer_load_dword v242,v247,s[8:11],s91 offen offset:0 sc1
ta 180,237,49152
ta 182,237,49664
ta 184,237,32768
ta 186,237,33280
ta 196,234,36864
mvaa 68,76,36
s_mov_b32 m0,s95
ta 198,234,37376
ta 200,234,53248
ta 202,234,53760
ta 204,235,53248
ta 206,235,53760
ta 208,235,36864
ta 210,235,37376
mvaa 64,72,36
s_lshl_b32 s35,s90,2
ta 220,236,36864
ta 222,236,37376
ta 224,236,53248
mvaa 32,96,36
s_lshl_b32 s90,s22,2
ta 226,236,53760
ta 228,237,53248
mvaa 36,100,36
s_add_u32 s90,s90,s89
ta 230,237,53760
ta 232,237,36864
mvaa 28,100,32
ds 249,8,42,0
ta 234,237,37376
ta 240,235,40960
mvaa 24,96,32
s_add_u32 s35,s35,83886080
ta 242,235,41472
ta 48,239,45056
mvaa 16,96,28
ta 50,239,45568
ta 60,239,61440
mvaa 20,100,28
ta 62,239,61952
ta 56,238,61440
mvaa 12,100,24
ta 58,238,61952
ta 68,238,45056
mvaa 8,96,24
ds 249,8,42,1024
ta 70,238,45568
ta 76,237,24576
mvaa 76,24,104
ta 78,237,25088
ta 72,237,40960
mvaa 96,24,108
ta 74,237,41472
ta 100,237,57344
mvaa 100,28,108
ta 102,237,57856
ta 96,236,57344
mvaa 80,28,104
ta 98,236,57856
ta 24,236,40960
mvaa 84,32,104
ds 249,8,42,2048
ta 26,236,41472
mvaa 104,32,108
ta 28,236,24576
ta 30,236,25088
mvaa 108,36,108
v_lshl_add_u32 v238,v244,3,s4
v_min_u32_e32 v238,L_att_end-L_att_pc-4,v238
mvaa 88,36,104
ta 32,234,24576
ta 34,234,25088
s_waitcnt lgkmcnt(15)
mvaa 0,144,20
ta 108,234,40960
ta 110,234,41472
mvaa 0,148,0
ds 249,8,42,3072
ta 36,234,57344
ta 38,234,57856
ta 104,235,57344
ta 106,235,57856
mvaa 0,152,4
v_mov_b32_e32 v234,0xc1a00000
v_mov_b32_e32 v235,0xc1a00000
mvaa 4,156,4
v_cndmask_b32_e64 v250,0,v252,s[40:41]
s_mov_b32 s40,0xffc0fffc
mvaa 4,160,0
s_mov_b32 s41,0xc000fc00
v_bfe_i32 v248,v244,4,1
v_mul_f32_e32 v0,0x3e0293ee,v0
mvaa 4,164,20
v_mul_f32_e32 v1,0x3e0293ee,v1
v_mul_f32_e32 v2,0x3e0293ee,v2
mvaa 92,164,8
buffer_load_dword v240,v247,s[8:11],s90 offen offset:0 sc1
mvaa 92,160,12
v_mul_f32_e32 v3,0x3e0293ee,v3
cv 0,0,1
mvaa 92,156,16
v_mul_f32_e32 v4,0x3e0293ee,v4
cv 1,2,3
mvaa 72,152,16
v_mul_f32_e32 v5,0x3e0293ee,v5
s_waitcnt lgkmcnt(0)
s_barrier
mvaa 72,148,12
s_mov_b32 m0,s70
v_mul_f32_e32 v6,0x3e0293ee,v6
v_mul_f32_e32 v7,0x3e0293ee,v7
mvaa 72,144,8
buffer_load_dword v241,v247,s[8:11],s90 offen offset:256 sc1
mvaa 144,144,80
cv 2,4,5
cv 3,6,7
mvaa 144,148,84
v_mul_f32_e32 v92,0x3e0293ee,v92
v_mul_f32_e32 v93,0x3e0293ee,v93
mvaa 144,152,88
v_mul_f32_e32 v94,0x3e0293ee,v94
v_mul_f32_e32 v72,0x3e0293ee,v72
mvaa 148,156,88
v_mul_f32_e32 v95,0x3e0293ee,v95
v_mul_f32_e32 v73,0x3e0293ee,v73
mvaa 148,160,84
ds 249,8,43,0
mvaa 148,164,80
v_mul_f32_e32 v74,0x3e0293ee,v74
v_mul_f32_e32 v75,0x3e0293ee,v75
mvaa 156,164,92
cv 72,72,73
cv 73,74,75
mvaa 156,160,44
cv 74,92,93
cv 75,94,95
mvaa 156,156,40
v_mul_f32_e32 v144,0x3e0293ee,v144
v_mul_f32_e32 v145,0x3e0293ee,v145
mvaa 152,152,40
ds 249,8,43,1024
mvaa 152,148,44
v_mul_f32_e32 v146,0x3e0293ee,v146
v_mul_f32_e32 v147,0x3e0293ee,v147
mvaa 152,144,92
cv 144,144,145
v_mul_f32_e32 v148,0x3e0293ee,v148
mvaa 160,168,92
cv 145,146,147
v_mul_f32_e32 v149,0x3e0293ee,v149
mvaa 160,172,44
v_mul_f32_e32 v150,0x3e0293ee,v150
v_mul_f32_e32 v151,0x3e0293ee,v151
mvaa 160,176,40
ds 249,8,43,2048
mvaa 164,180,40
cv 146,148,149
cv 147,150,151
mvaa 164,184,44
L_vs15:
s_cbranch_execz L_vr0s
v_mul_f32_e32 v156,0x3e0293ee,v156
v_mul_f32_e32 v157,0x3e0293ee,v157
mvaa 164,188,92
v_mul_f32_e32 v152,0x3e0293ee,v152
v_mul_f32_e32 v158,0x3e0293ee,v158
mvaa 172,188,80
v_mul_f32_e32 v159,0x3e0293ee,v159
v_mul_f32_e32 v153,0x3e0293ee,v153
mvaa 172,184,84
ds 249,8,43,3072
s_mov_b32 m0,s72
mvaa 172,180,88
v_mul_f32_e32 v154,0x3e0293ee,v154
v_mul_f32_e32 v155,0x3e0293ee,v155
mvaa 168,176,88
cv 152,152,153
cv 153,154,155
mvaa 168,172,84
ds_write_b128 v251,v[0:3] offset:0
mvaa 168,168,80
cv 154,156,157
cv 155,158,159
mvaa 176,168,8
ds 255,8,42,0
mvaa 176,172,12
v_mul_f32_e32 v160,0x3e0293ee,v160
v_mul_f32_e32 v161,0x3e0293ee,v161
mvaa 176,176,16
v_mul_f32_e32 v162,0x3e0293ee,v162
v_mul_f32_e32 v163,0x3e0293ee,v163
mvaa 180,180,16
cv 160,160,161
v_mul_f32_e32 v164,0x3e0293ee,v164
mvaa 180,184,12
v_mul_f32_e32 v165,0x3e0293ee,v165
cv 161,162,163
mvaa 180,188,8
ds 255,8,42,1024
ds_write_b128 v251,v[72:75] offset:4096
mvaa 188,188,20
v_mul_f32_e32 v166,0x3e0293ee,v166
v_mul_f32_e32 v167,0x3e0293ee,v167
mvaa 188,184,0
cv 162,164,165
cv 163,166,167
mvaa 188,180,4
v_mul_f32_e32 v172,0x3e0293ee,v172
v_mul_f32_e32 v173,0x3e0293ee,v173
mvaa 184,176,4
v_mul_f32_e32 v174,0x3e0293ee,v174
v_mul_f32_e32 v175,0x3e0293ee,v175
mvaa 184,172,0
ds 255,8,42,2048
mvaa 184,168,20
v_mul_f32_e32 v168,0x3e0293ee,v168
v_mul_f32_e32 v169,0x3e0293ee,v169
mvaa 192,192,20
ds_write_b128 v251,v[144:147] offset:8192
mvaa 192,196,0
v_mul_f32_e32 v170,0x3e0293ee,v170
v_mul_f32_e32 v171,0x3e0293ee,v171
mvaa 192,200,4
cv 168,168,169
cv 169,170,171
mvaa 196,204,4
ds 255,8,42,3072
mvaa 196,208,0
cv 170,172,173
cv 171,174,175
mvaa 196,212,20
v_mul_f32_e32 v176,0x3e0293ee,v176
v_mul_f32_e32 v177,0x3e0293ee,v177
mvaa 204,212,8
v_mul_f32_e32 v178,0x3e0293ee,v178
v_mul_f32_e32 v179,0x3e0293ee,v179
mvaa 204,208,12
ds_write_b128 v251,v[152:155] offset:12288
mvaa 204,204,16
buffer_load_dword v243,v247,s[8:11],s91 offen offset:256 sc1
mvaa 200,200,16
cv 176,176,177
cv 177,178,179
mvaa 200,196,12
v_mul_f32_e32 v180,0x3e0293ee,v180
v_mul_f32_e32 v181,0x3e0293ee,v181
mvaa 200,192,8
v_mul_f32_e32 v182,0x3e0293ee,v182
v_mul_f32_e32 v183,0x3e0293ee,v183
mvaa 208,192,80
cv 178,180,181
cv 179,182,183
mvaa 208,196,84
v_mul_f32_e32 v188,0x3e0293ee,v188
v_mul_f32_e32 v189,0x3e0293ee,v189
mvaa 208,200,88
ds_write_b128 v251,v[160:163] offset:13312
mvaa 212,204,88
v_mul_f32_e32 v190,0x3e0293ee,v190
v_mul_f32_e32 v191,0x3e0293ee,v191
mvaa 212,208,84
v_mul_f32_e32 v184,0x3e0293ee,v184
v_mul_f32_e32 v185,0x3e0293ee,v185
mvaa 212,212,80
v_mul_f32_e32 v186,0x3e0293ee,v186
v_mul_f32_e32 v187,0x3e0293ee,v187
mvaa 220,212,92
cv 184,184,185
cv 185,186,187
mvaa 220,208,44
cv 186,188,189
cv 187,190,191
mvaa 220,204,40
v_mul_f32_e32 v192,0x3e0293ee,v192
v_mul_f32_e32 v193,0x3e0293ee,v193
mvaa 216,200,40
ds_write_b128 v251,v[168:171] offset:9216
mvaa 216,196,44
v_mul_f32_e32 v194,0x3e0293ee,v194
v_mul_f32_e32 v195,0x3e0293ee,v195
mvaa 216,192,92
cv 192,192,193
cv 193,194,195
maaa 128,216,92
v_mul_f32_e32 v196,0x3e0293ee,v196
v_mul_f32_e32 v197,0x3e0293ee,v197
maaa 128,220,44
v_mul_f32_e32 v198,0x3e0293ee,v198
v_mul_f32_e32 v199,0x3e0293ee,v199
maaa 128,224,40
cv 194,196,197
cv 195,198,199
maaa 112,228,40
v_mul_f32_e32 v204,0x3e0293ee,v204
v_mul_f32_e32 v205,0x3e0293ee,v205
maaa 112,232,44
ds_write_b128 v251,v[176:179] offset:5120
maaa 112,236,92
v_mul_f32_e32 v206,0x3e0293ee,v206
v_mul_f32_e32 v207,0x3e0293ee,v207
maaa 116,236,80
v_mul_f32_e32 v200,0x3e0293ee,v200
v_mul_f32_e32 v201,0x3e0293ee,v201
maaa 116,232,84
v_mul_f32_e32 v202,0x3e0293ee,v202
v_mul_f32_e32 v203,0x3e0293ee,v203
maaa 116,228,88
cv 200,200,201
cv 201,202,203
maaa 132,224,88
cv 202,204,205
cv 203,206,207
maaa 132,220,84
v_mul_f32_e32 v208,0x3e0293ee,v208
v_mul_f32_e32 v209,0x3e0293ee,v209
maaa 132,216,80
ds_write_b128 v251,v[184:187] offset:1024
maaa 136,216,8
v_mul_f32_e32 v210,0x3e0293ee,v210
v_mul_f32_e32 v211,0x3e0293ee,v211
maaa 136,220,12
cv 208,208,209
cv 209,210,211
maaa 136,224,16
v_mul_f32_e32 v212,0x3e0293ee,v212
v_mul_f32_e32 v213,0x3e0293ee,v213
maaa 120,228,16
s_waitcnt vmcnt(11)
s_barrier
maaa 120,232,12
v_mul_f32_e32 v214,0x3e0293ee,v214
v_mul_f32_e32 v215,0x3e0293ee,v215
maaa 120,236,8
cv 210,212,213
cv 211,214,215
maaa 124,236,20
ds_write_b128 v251,v[192:195] offset:2048
maaa 124,232,0
v_mul_f32_e32 v220,0x3e0293ee,v220
v_mul_f32_e32 v221,0x3e0293ee,v221
maaa 124,228,4
v_mul_f32_e32 v216,0x3e0293ee,v216
v_mul_f32_e32 v217,0x3e0293ee,v217
maaa 140,224,4
v_mul_f32_e32 v222,0x3e0293ee,v222
v_mul_f32_e32 v223,0x3e0293ee,v223
maaa 140,220,0
v_mul_f32_e32 v218,0x3e0293ee,v218
v_mul_f32_e32 v219,0x3e0293ee,v219
maaa 140,216,20
cv 216,216,217
cv 217,218,219
mvaa 88,92,64
cv 218,220,221
cv 219,222,223
mvaa 88,44,68
ds_write_b128 v251,v[200:203] offset:6144
mvaa 88,40,56
v_accvgpr_read_b32 v0,a128
v_accvgpr_read_b32 v1,a129
mvaa 108,40,60
v_accvgpr_read_b32 v2,a130
v_accvgpr_read_b32 v3,a131
mvaa 108,44,48
v_mul_f32_e32 v0,0x3e0293ee,v0
v_mul_f32_e32 v1,0x3e0293ee,v1
cv 88,88,89
cv 89,90,91
mvaa 108,92,52
buffer_store_dwordx2 v[88:89],v253,s[8:11],s99 offen offset:1024 sc0 sc1
mvaa 36,76,92
v_mul_f32_e32 v2,0x3e0293ee,v2
v_mul_f32_e32 v3,0x3e0293ee,v3
mvaa 36,72,44
cv 0,0,1
cv 1,2,3
mvaa 36,100,40
ds_write_b128 v251,v[208:211] offset:10240
mvaa 32,96,40
cv 108,108,109
cv 109,110,111
mvaa 32,24,44
buffer_store_dwordx2 v[108:109],v253,s[8:11],s99 offen offset:1536 sc0 sc1
mvaa 32,28,92
v_accvgpr_read_b32 v4,a112
v_accvgpr_read_b32 v5,a113
mvaa 64,32,92
v_accvgpr_read_b32 v6,a114
v_mul_f32_e32 v4,0x3e0293ee,v4
mvaa 64,108,44
v_mul_f32_e32 v5,0x3e0293ee,v5
v_mul_f32_e32 v6,0x3e0293ee,v6
mvaa 64,36,40
cv 32,32,33
cv 33,34,35
cv 34,36,37
cv 35,38,39
mvaa 68,104,40
buffer_store_dwordx4 v[32:35],v244,s[8:11],s99 offen sc0 sc1
ds_write_b128 v251,v[216:219] offset:14336
mvaa 68,240,44
v_accvgpr_read_b32 v7,a115
v_mul_f32_e32 v7,0x3e0293ee,v7
mvaa 68,244,92
cv 2,4,5
cv 3,6,7
mvaa 60,244,80
ra 192,224,0
v_accvgpr_read_b32 v92,a116
mvaa 60,240,84
ra 196,224,1024
v_accvgpr_read_b32 v93,a117
mvaa 60,104,88
ra 200,224,2048
v_mul_f32_e32 v92,0x3e0293ee,v92
mvaa 56,36,88
ra 204,224,3072
v_mul_f32_e32 v93,0x3e0293ee,v93
mvaa 56,108,84
ds_write_b128 v251,v[0:3] offset:15360
mvaa 56,32,80
v_accvgpr_read_b32 v94,a118
v_accvgpr_read_b32 v95,a119
mvaa 24,28,80
v_accvgpr_read_b32 v72,a132
v_accvgpr_read_b32 v73,a133
mvaa 24,24,84
v_accvgpr_read_b32 v74,a134
v_mul_f32_e32 v94,0x3e0293ee,v94
mvaa 24,96,88
v_mul_f32_e32 v95,0x3e0293ee,v95
v_mul_f32_e32 v72,0x3e0293ee,v72
mvaa 28,100,88
v_mul_f32_e32 v73,0x3e0293ee,v73
v_mul_f32_e32 v74,0x3e0293ee,v74
v_accvgpr_read_b32 v75,a135
mvaa 28,72,84
v_mul_f32_e32 v75,0x3e0293ee,v75
cv 72,72,73
cv 73,74,75
cv 74,92,93
mvaa 28,76,80
ra 208,224,4096
cv 75,94,95
mvaa 20,76,8
ds_write_b128 v251,v[72:75] offset:11264
mvaa 20,72,12
v_accvgpr_read_b32 v144,a136
v_accvgpr_read_b32 v145,a137
mvaa 20,100,16
s_mov_b64 exec,3
global_load_dword v239,v238,s[96:97]
s_mov_b64 exec,-1
ra 212,224,5120
ra 248,224,14336
ra 252,224,15360
mvaa 16,96,16
v_accvgpr_read_b32 v146,a138
v_mul_f32_e32 v144,0x3e0293ee,v144
v_mul_f32_e32 v145,0x3e0293ee,v145
v_mul_f32_e32 v146,0x3e0293ee,v146
v_accvgpr_read_b32 v147,a139
v_mul_f32_e32 v147,0x3e0293ee,v147
cv 144,144,145
mvaa 16,24,12
ra 236,224,11264
v_accvgpr_read_b32 v148,a120
v_accvgpr_read_b32 v149,a121
v_accvgpr_read_b32 v150,a122
cv 145,146,147
mvaa 16,28,8
v_mul_f32_e32 v148,0x3e0293ee,v148
v_mul_f32_e32 v149,0x3e0293ee,v149
v_mul_f32_e32 v150,0x3e0293ee,v150
v_accvgpr_read_b32 v151,a123
v_mul_f32_e32 v151,0x3e0293ee,v151
cv 146,148,149
mvaa 48,32,8
ra 228,224,9216
cv 147,150,151
mvaa 48,108,12
ds_write_b128 v251,v[144:147] offset:7168
mvaa 48,36,16
v_accvgpr_read_b32 v156,a124
v_accvgpr_read_b32 v157,a125
mvaa 52,104,16
ra 232,224,10240
ra 216,224,6144
ra 220,224,7168
ra 224,224,8192
v_accvgpr_read_b32 v158,a126
v_accvgpr_read_b32 v159,a127
mvaa 52,240,12
v_accvgpr_read_b32 v152,a140
v_mul_f32_e32 v156,0x3e0293ee,v156
v_mul_f32_e32 v157,0x3e0293ee,v157
v_mul_f32_e32 v158,0x3e0293ee,v158
v_mul_f32_e32 v159,0x3e0293ee,v159
mvaa 52,244,8
v_mul_f32_e32 v152,0x3e0293ee,v152
v_accvgpr_read_b32 v153,a141
v_mul_f32_e32 v153,0x3e0293ee,v153
v_accvgpr_read_b32 v154,a142
v_accvgpr_read_b32 v155,a143
mvaa 44,244,20
v_mul_f32_e32 v154,0x3e0293ee,v154
v_mul_f32_e32 v155,0x3e0293ee,v155
cv 152,152,153
cv 153,154,155
cv 154,156,157
mvaa 44,240,0
ra 244,224,13312
cv 155,158,159
mvaa 44,104,4
ds_write_b128 v251,v[152:155] offset:3072
mvaa 40,36,4
cv 64,64,65
cv 56,56,57
mvaa 40,108,0
cv 57,58,59
cv 58,60,61
mvaa 40,32,20
ra 240,224,12288
cv 59,62,63
mvaa 8,28,20
cv 16,16,17
cv 17,18,19
mvaa 8,24,0
cv 48,48,49
cv 194,44,45
mvaa 8,96,4
cv 195,46,47
cv 192,40,41
mvaa 12,100,4
cv 193,42,43
cv 49,50,51
mvaa 12,72,0
s_cmp_eq_u32 s19,0
s_cselect_b32 s76,32768,0
s_cselect_b32 s77,49152,s86
s_cselect_b32 s78,65536,s46
s_cselect_b32 s79,81920,s86
mvaa 12,76,20
s_add_u32 s86,s95,98304
cv 8,8,9
v_add_u32_e32 v236,s86,v244
mvaa 76,20,64
s_add_u32 s46,s46,16384
v_xor_b32_e32 v237,4096,v236
s_waitcnt lgkmcnt(0)
s_barrier
s_mov_b32 m0,s68
mvaa 76,0,68
ds 249,8,76,0
ra 128,237,32768
ra 132,237,33792
mvaa 76,4,56
s_add_u32 s95,s47,32768
ra 136,237,34816
cv 9,10,11
mvaa 96,4,60
ra 140,237,35840
ra 144,237,49152
mvaa 96,0,48
cv 10,12,13
cv 11,14,15
mvaa 96,20,52
ra 148,237,50176
cv 50,52,53
mvaa 100,8,52
ds 249,8,76,1024
ra 152,237,51200
ra 156,237,52224
mvaa 100,12,48
ra 176,236,16384
cv 51,54,55
mvaa 100,16,60
ra 180,236,17408
cv 18,20,21
mvaa 80,16,56
ra 184,236,18432
cv 19,22,23
mvaa 80,12,68
ra 188,236,19456
cv 24,24,25
mvaa 80,8,64
ds 249,8,76,2048
ra 160,236,0
mvaa 84,80,64
ra 164,236,1024
cv 25,26,27
mvaa 84,84,68
ra 168,236,2048
ra 172,236,3072
mvaa 84,88,56
cv 26,28,29
cv 27,30,31
mvaa 104,88,60
cv 65,66,67
cv 66,68,69
mvaa 104,84,48
ds 249,8,76,3072
mvaa 104,80,52
s_mov_b32 m0,s69
cv 67,70,71
v_cndmask_b32_e64 v252,0,v252,s[40:41]
cvvv 164,8,192,112
cv 70,76,77
cv 71,78,79
cvvv 168,8,48,116
ds 249,8,77,0
cvvv 172,16,48,112
cv 94,96,97
v_mov_b32_e32 v76,v88
cvvv 176,16,56,116
cv 95,98,99
s_waitcnt vmcnt(9) lgkmcnt(0)
s_barrier
cvvv 180,24,56,112
ds 249,8,77,1024
cvvv 184,24,64,116
ra 0,224,32768
cv 96,100,101
cvvv 188,32,64,112
ra 4,224,33792
cv 97,102,103
cvaa 0,192,128,232
buffer_load_dword v17,v247,s[8:11],s35 offen offset:64 sc1
mvaa 0,200,132
ra 8,224,34816
cv 72,80,81
mvaa 0,208,136
ra 12,224,35840
cv 73,82,83
mvaa 0,216,140
ds 249,8,77,2048
cvaa 4,220,140,232
ra 16,224,36864
cv 74,84,85
mvaa 4,212,136
ra 20,224,37888
v_max3_f32 v172,v168,v169,v172
mvaa 4,204,132
ds 249,8,77,3072
mvaa 4,196,128
s_mov_b32 m0,s74
ra 24,224,38912
v_max_f32_e32 v172,v172,v173
cvaa 32,224,128,232
ra 28,224,39936
v_max3_f32 v174,v170,v171,v174
mvaa 32,232,132
buffer_load_dword v19,v247,s[8:11],s35 offen offset:192 sc1
mvaa 32,240,136
ra 32,224,40960
v_max_f32_e32 v174,v174,v175
mvaa 32,248,140
ra 36,224,41984
ra 40,224,43008
cvaa 36,252,140,232
buffer_load_dword v18,v247,s[8:11],s35 offen offset:128 sc1
ra 44,224,44032
mvaa 36,244,136
ra 48,224,45056
v_max_f32_e32 v148,v172,v174
mvaa 36,236,132
ra 56,224,47104
ra 60,224,48128
mvaa 36,228,128
buffer_load_dword v16,v247,s[8:11],s35 offen offset:0 sc1
ra 52,224,46080
cvaa 8,192,144,232
v_mov_b32_e32 v149,v148
cv 75,86,87
mvaa 8,200,148
v_permlane16_swap_b32_e32 v148,v149
mvaa 8,208,152
ds 255,8,43,0
mvaa 8,216,156
v_max_f32_e32 v148,v148,v149
v_mov_b32_e32 v149,v148
cvaa 12,220,156,232
L_vs16:
s_cbranch_execz L_vr0s
cv 98,104,105
v_max3_f32 v180,v176,v177,v180
mvaa 12,212,152
ds 255,8,43,1024
mvaa 12,204,148
v_permlane32_swap_b32_e32 v148,v149
mvaa 12,196,144
v_max_f32_e32 v148,v148,v149
s_waitcnt lgkmcnt(0)
s_barrier
cvaa 40,224,144,232
ds 255,8,43,2048
mvaa 40,232,148
ra 64,225,0
v_mul_f32_e32 v148,0xbe8293ee,v148
mvaa 40,240,152
ra 68,225,1024
v_fmamk_f32 v172,v172,0x3e8293ee,v148
mvaa 40,248,156
ds 255,8,43,3072
cvaa 44,252,156,232
s_mov_b32 m0,s70
ra 72,225,2048
v_fmamk_f32 v174,v174,0x3e8293ee,v148
mvaa 44,244,152
ra 76,225,3072
v_max3_f32 v182,v178,v179,v182
mvaa 44,236,148
ds 249,8,78,0
mvaa 44,228,144
ra 80,225,4096
v_max_f32_e32 v180,v180,v181
cvaa 24,192,176,128
ra 84,225,5120
v_max_f32_e32 v182,v182,v183
mvaa 24,200,180
ds 249,8,78,1024
mvaa 24,208,184
ra 88,225,6144
v_max_f32_e32 v152,v180,v182
mvaa 24,216,188
ra 92,225,7168
v_mov_b32_e32 v153,v152
cvaa 28,220,188,132
ds 249,8,78,2048
mvaa 28,212,184
ra 96,225,8192
v_max3_f32 v188,v184,v185,v188
mvaa 28,204,180
ra 100,225,9216
ra 104,225,10240
mvaa 28,196,176
ds 249,8,78,3072
ra 108,225,11264
cvaa 56,224,176,136
s_mov_b32 m0,s71
ra 112,225,12288
v_max3_f32 v190,v186,v187,v190
mvaa 56,232,180
ra 116,225,13312
ra 120,225,14336
mvaa 56,240,184
ds 249,8,79,0
ra 124,225,15360
mvaa 56,248,188
ex 172
cvaa 60,252,188,140
ex 174
mvaa 60,244,184
ds 249,8,79,1024
mvaa 60,236,180
v_permlane16_swap_b32_e32 v152,v153
mvaa 60,228,176
cv 172,172,172
cv 174,174,174
cvaa 80,0,160,128
ds 249,8,79,2048
mvaa 80,8,164
v_and_b32_e32 v168,v226,v172
v_max_f32_e32 v152,v152,v153
mvaa 80,16,168
v_and_b32_e32 v169,v227,v174
v_and_b32_e32 v170,v250,v172
v_and_b32_e32 v171,v252,v174
mvaa 80,24,172
ds 249,8,79,3072
v_mov_b32_e32 v153,v152
cvaa 84,28,172,132
v_max_f32_e32 v188,v188,v189
v_permlane32_swap_b32_e32 v152,v153
v_max_f32_e32 v152,v152,v153
mvaa 84,20,168
v_mul_f32_e32 v152,0xbe8293ee,v152
v_fmamk_f32 v180,v180,0x3e8293ee,v152
mvaa 84,12,164
v_fmamk_f32 v182,v182,0x3e8293ee,v152
v_max_f32_e32 v190,v190,v191
mvaa 84,4,160
ex 180
cvaa 112,32,160,136
ex 182
v_max_f32_e32 v156,v188,v190
mvaa 112,40,164
v_mov_b32_e32 v157,v156
cv 180,180,180
mvaa 112,48,168
v_permlane16_swap_b32_e32 v156,v157
mvaa 112,56,172
cv 182,182,182
v_and_b32_e32 v176,v226,v180
v_max_f32_e32 v156,v156,v157
s_waitcnt vmcnt(15) lgkmcnt(0)
s_barrier
s_mov_b32 m0,s72
cvaa 116,60,172,140
ds 255,8,76,0
v_and_b32_e32 v177,v227,v182
mvaa 116,52,168
v_and_b32_e32 v178,v250,v180
v_and_b32_e32 v179,v252,v182
v_mov_b32_e32 v157,v156
mvaa 116,44,164
cv 99,106,107
v_permlane32_swap_b32_e32 v156,v157
v_max_f32_e32 v156,v156,v157
mvaa 116,36,160
ds 255,8,76,1024
v_mul_f32_e32 v156,0xbe8293ee,v156
zvvv 148,228,168
v_fmamk_f32 v188,v188,0x3e8293ee,v156
ex 188
zvvv 200,70,168
v_fmamk_f32 v190,v190,0x3e8293ee,v156
)"
R"(ex 190
zvvv 204,94,168
ds 255,8,76,2048
zvvv 152,228,176
cv 188,188,188
cv 190,190,190
v_and_b32_e32 v184,v226,v188
zvvv 208,72,176
v_and_b32_e32 v185,v227,v190
v_and_b32_e32 v186,v250,v188
v_and_b32_e32 v187,v252,v190
zvvv 212,96,176
ds 255,8,76,3072
s_mov_b32 m0,s73
v_mov_b32_e32 v77,v89
zvvv 156,228,184
v_mov_b32_e32 v100,v108
v_mov_b32_e32 v101,v109
zvvv 216,74,184
ex 0
zvvv 220,98,184
ds 255,8,77,0
cvaa 64,0,128,232
ex 1
mvaa 64,8,132
ex 2
mvaa 64,16,136
ds 255,8,77,1024
mvaa 64,24,140
ds 255,8,77,2048
v_mov_b32_e32 v170,v70
v_mov_b32_e32 v171,v71
cvaa 68,28,140,232
ex 3
mvaa 68,20,136
ex 4
mvaa 68,12,132
ds 255,8,77,3072
mvaa 68,4,128
ex 5
v_and_b32_e32 v248,2863311530,v248
cvaa 96,32,128,232
ex 6
mvaa 96,40,132
v_sub_f32_e32 v17,1.0,v17
v_rcp_f32_e32 v148,v148
mvaa 96,48,136
v_mul_f32_e32 v148,v148,v17
s_waitcnt vmcnt(21)
v_sub_f32_e32 v18,1.0,v18
v_sub_f32_e32 v19,1.0,v19
mvaa 96,56,140
v_mul_f32_e32 v200,v148,v200
v_mul_f32_e32 v201,v148,v201
v_mul_f32_e32 v202,v148,v202
cvaa 100,60,140,232
v_mul_f32_e32 v203,v148,v203
cv 200,200,201
mvaa 100,52,136
v_mul_f32_e32 v204,v148,v204
v_mul_f32_e32 v205,v148,v205
cv 201,202,203
mvaa 100,44,132
v_mul_f32_e32 v206,v148,v206
v_mul_f32_e32 v207,v148,v207
cv 202,204,205
mvaa 100,36,128
buffer_load_dwordx4 v[176:179],v244,s[8:11],s98 offen sc1
cv 203,206,207
cvaa 72,0,144,232
v_rcp_f32_e32 v152,v152
mvaa 72,8,148
v_mul_f32_e32 v152,v152,v18
v_mul_f32_e32 v208,v152,v208
v_mul_f32_e32 v209,v152,v209
mvaa 72,16,152
buffer_load_dwordx2 v[168:169],v253,s[8:11],s98 offen offset:1024 sc1
v_mul_f32_e32 v210,v152,v210
mvaa 72,24,156
v_mul_f32_e32 v211,v152,v211
cv 208,208,209
cv 209,210,211
cvaa 76,28,156,232
v_mul_f32_e32 v212,v152,v212
v_mul_f32_e32 v213,v152,v213
mvaa 76,20,152
buffer_load_dwordx2 v[172:173],v253,s[8:11],s98 offen offset:1536 sc1
v_mul_f32_e32 v214,v152,v214
mvaa 76,12,148
v_mul_f32_e32 v215,v152,v215
v_rcp_f32_e32 v156,v156
mvaa 76,4,144
cv 210,212,213
cv 211,214,215
v_mul_f32_e32 v156,v156,v19
cvaa 104,32,144,232
buffer_store_dwordx4 v[200:203],v244,s[8:11],s34 offen sc0 sc1
mvaa 104,40,148
v_mul_f32_e32 v216,v156,v216
v_mul_f32_e32 v217,v156,v217
v_mul_f32_e32 v218,v156,v218
mvaa 104,48,152
v_mul_f32_e32 v219,v156,v219
cv 216,216,217
cv 217,218,219
mvaa 104,56,156
buffer_store_dwordx4 v[208:211],v244,s[8:11],s50 offen sc0 sc1
v_mul_f32_e32 v220,v156,v220
cvaa 108,60,156,232
v_mul_f32_e32 v221,v156,v221
v_mul_f32_e32 v222,v156,v222
mvaa 108,52,152
v_mul_f32_e32 v223,v156,v223
cv 218,220,221
cv 219,222,223
mvaa 108,44,148
buffer_store_dwordx4 v[216:219],v244,s[8:11],s51 offen sc0 sc1
v_mov_b32_e32 v160,v120
mvaa 108,36,144
v_mov_b32_e32 v161,v121
ex 7
cvaa 88,0,176,232
ex 32
mvaa 88,8,180
v_mov_b32_e32 v162,v122
ex 33
mvaa 88,16,184
v_mov_b32_e32 v175,v95
ex 34
mvaa 88,24,188
s_cmp_ge_i32 s47,s46
s_cselect_b32 s76,s95,s46
s_cselect_b32 s77,s95,268435456
v_mov_b32_e32 v174,v94
ex 35
cvaa 92,28,188,232
s_add_u32 s77,s77,16384
ex 36
mvaa 92,20,184
v_mov_b32_e32 v163,v123
ex 37
mvaa 92,12,180
cv 0,0,1
ex 38
mvaa 92,4,176
cv 1,2,3
ex 39
cvaa 120,32,176,232
buffer_load_dword v247,v246,s[24:27],s49 offen
mvaa 120,40,180
s_add_u32 s49,s49,32768
cv 2,4,5
ex 8
mvaa 120,48,184
cv 3,6,7
ex 9
mvaa 120,56,188
buffer_load_dword v247,v245,s[36:39],s85 offen
cv 4,32,33
cvaa 124,60,188,232
s_add_u32 s85,s85,131072
ex 10
mvaa 124,52,184
cv 5,34,35
ex 11
mvaa 124,44,180
buffer_load_dword v247,v246,s[24:27],s49 offen
cv 6,36,37
mvaa 124,36,176
s_cmp_lg_u32 s19,0
s_cbranch_scc1 L_j0
s_nop 1
s_lshl_b32 s86,s12,12
s_add_u32 s86,s86,131072
v_add_u32_e32 v236,s86,v244
ra 160,236,0
ra 164,236,1024
ra 168,236,2048
ra 172,236,3072
ra 176,236,16384
ra 180,236,17408
ra 184,236,18432
ra 188,236,19456
v_add_u32_e32 v224,s94,v224
s_waitcnt lgkmcnt(0)
L_j0:
cv 7,38,39
ex 12
zaav 0,64,0
s_add_u32 s49,s49,32768
ex 13
maav 0,96,4
ex 14
v_or_b32_e32 v248,1145324612,v248
zaav 4,68,0
ex 15
v_mov_b64_e32 v[144:145],0
maav 4,100,4
cv 8,8,9
ex 40
zaav 8,72,0
ex 41
maav 8,104,4
cv 9,10,11
ex 42
zaav 12,76,0
cv 10,12,13
ex 43
maav 12,108,4
cv 11,14,15
ex 44
v_smfmac_f32_16x16x64_bf16 v[144:147],v[228:231],v[0:7],v248
ex 45
zaav 16,80,0
cv 12,40,41
ex 46
maav 16,112,4
s_waitcnt vmcnt(8)
v_max3_u32 v180,v176,v177,v178
cv 13,42,43
zaav 20,84,0
s_waitcnt vmcnt(7)
v_max3_u32 v180,v180,v179,v168
s_waitcnt vmcnt(6)
v_max3_u32 v180,v180,v169,v172
v_max_u32_e32 v180,v180,v173
v_cmp_eq_u32_e64 s[40:41],-1,v180
maav 20,116,4
s_cmp_lg_u64 s[40:41],0
s_cbranch_scc1 L_bx
L_bxr:
cv 14,44,45
ex 47
mvvv 160,176,192
zaav 24,88,0
cv 15,46,47
v_sub_f32_e32 v16,1.0,v16
maav 24,120,4
ex 24
zaav 28,92,0
ex 25
maav 28,124,4
v_max3_f32 v164,v160,v161,v164
v_max_f32_e32 v164,v164,v165
v_max3_f32 v166,v162,v163,v166
v_max_f32_e32 v166,v166,v167
v_max_f32_e32 v180,v164,v166
v_mov_b32_e32 v181,v180
zaav 32,64,8
ex 26
v_permlane16_swap_b32_e32 v180,v181
v_max_f32_e32 v180,v180,v181
v_mov_b32_e32 v181,v180
maav 32,96,12
ex 27
v_permlane32_swap_b32_e32 v180,v181
v_max_f32_e32 v180,v180,v181
v_mul_f32_e32 v180,0xbe8293ee,v180
v_fmamk_f32 v164,v164,0x3e8293ee,v180
zaav 36,68,8
ex 164
v_fmamk_f32 v166,v166,0x3e8293ee,v180
ex 166
cv 164,164,164
cv 166,166,166
maav 36,100,12
v_and_b32_e32 v160,v226,v164
v_and_b32_e32 v161,v227,v166
v_and_b32_e32 v162,v250,v164
v_and_b32_e32 v163,v252,v166
zaav 40,72,8
v_mov_b64_e32 v[148:149],0
zvvv 180,228,160
maav 40,104,12
ra 192,224,0
zvvv 184,168,160
zaav 44,76,8
ra 196,224,1024
zvvv 188,172,160
maav 44,108,12
ra 200,224,2048
v_rcp_f32_e32 v180,v180
cv 24,24,25
v_mul_f32_e32 v180,v180,v16
v_mul_f32_e32 v184,v180,v184
v_mul_f32_e32 v185,v180,v185
v_smfmac_f32_16x16x64_bf16 v[148:151],v[228:231],v[8:15],v248
v_mul_f32_e32 v186,v180,v186
v_mul_f32_e32 v187,v180,v187
v_mul_f32_e32 v188,v180,v188
v_mul_f32_e32 v189,v180,v189
v_mul_f32_e32 v190,v180,v190
v_mul_f32_e32 v191,v180,v191
zaav 48,80,8
cv 184,184,185
cv 185,186,187
cv 186,188,189
cv 187,190,191
cv 25,26,27
ex 28
maav 48,112,12
buffer_store_dwordx4 v[184:187],v244,s[8:11],s33 offen sc0 sc1
ex 29
ex 30
cv 26,28,29
zaav 52,84,8
ex 31
ex 56
cv 27,30,31
ex 57
maav 52,116,12
ex 58
ex 59
ex 60
zaav 56,88,8
ex 61
maav 56,120,12
cv 28,56,57
ex 62
zaav 60,92,8
ra 204,224,3072
ra 208,224,4096
ex 63
maav 60,124,12
ra 212,224,5120
cv 29,58,59
cv 30,60,61
cv 31,62,63
zvav 192,64,24
ra 216,224,6144
ra 220,224,7168
ex 64
mvav 192,96,28
ra 224,224,8192
v_mov_b32_e32 v251,-1
ex 65
zvav 196,68,24
ra 228,224,9216
ra 232,224,10240
ex 66
mvav 196,100,28
ra 236,224,11264
v_mov_b64_e32 v[152:153],0
ex 67
zvav 200,72,24
ra 240,224,12288
ra 244,224,13312
v_mov_b64_e32 v[156:157],0
cv 64,64,65
mvav 200,104,28
ra 248,224,14336
ra 252,224,15360
ex 68
zvav 204,76,24
ex 69
ex 70
mvav 204,108,28
cv 65,66,67
ex 71
cv 66,68,69
v_smfmac_f32_16x16x64_bf16 v[156:159],v[228:231],v[24:31],v248
ex 96
ex 97
zvav 208,80,24
ex 98
ex 99
mvav 208,112,28
cv 67,70,71
cv 68,96,97
ex 100
zvav 212,84,24
ex 101
ex 102
mvav 212,116,28
cv 69,98,99
ex 103
cv 70,100,101
zvav 216,88,24
ex 72
ex 73
mvav 216,120,28
ex 74
ex 75
zvav 220,92,24
ex 76
ex 77
mvav 220,124,28
v_mov_b32_e32 v250,-1
v_mov_b32_e32 v252,-1
v_mov_b32_e32 v253,-1
cv 71,102,103
s_mov_b64 exec,0
s_bitcmp1_b32 170,s20
s_cbranch_scc1 L_vbO
s_cmp_eq_u32 s12,0
s_cbranch_scc1 L_vs18
s_cmp_eq_u32 s12,1
s_cbranch_scc1 L_vs19
s_cmp_eq_u32 s12,2
s_cbranch_scc1 L_vs20
s_branch L_vs21
L_vbO:
s_cmp_eq_u32 s12,0
s_cbranch_scc1 L_vs25
s_cmp_eq_u32 s12,1
s_cbranch_scc1 L_vs26
s_cmp_eq_u32 s12,2
s_cbranch_scc1 L_vs27
s_branch L_vs28
L_vr1:
s_mov_b64 exec,-1
s_waitcnt lgkmcnt(0)
s_barrier
cvaa 0,192,128,232
mvaa 0,200,132
s_add_u32 s81,s81,1
ex 78
mvaa 0,208,136
s_add_u32 s44,s44,1024
ex 79
mvaa 0,216,140
cv 72,72,73
cv 73,74,75
cvaa 4,220,140,232
s_mov_b32 m0,s68
ex 104
mvaa 4,212,136
ds 249,8,76,0
mvaa 4,204,132
ex 105
mvaa 4,196,128
ex 106
cvaa 32,224,128,232
ra 64,225,32768
cv 74,76,77
mvaa 32,232,132
ex 107
mvaa 32,240,136
ra 68,225,33792
cv 75,78,79
mvaa 32,248,140
ex 108
cvaa 36,252,140,232
ds 249,8,76,1024
mvaa 36,244,136
ex 109
mvaa 36,236,132
ra 72,225,34816
cv 76,104,105
mvaa 36,228,128
ex 110
cvaa 8,192,144,232
ra 76,225,35840
cv 77,106,107
mvaa 8,200,148
ex 111
mvaa 8,208,152
ex 80
mvaa 8,216,156
ds 249,8,76,2048
cvaa 12,220,156,232
ra 80,225,36864
cv 78,108,109
mvaa 12,212,152
ex 81
mvaa 12,204,148
ra 84,225,37888
cv 79,110,111
mvaa 12,196,144
ex 82
cvaa 40,224,144,232
ex 83
mvaa 40,232,148
ra 88,225,38912
cv 80,80,81
mvaa 40,240,152
ds 249,8,76,3072
mvaa 40,248,156
ex 84
cvaa 44,252,156,232
ra 92,225,39936
cv 81,82,83
mvaa 44,244,152
ex 85
mvaa 44,236,148
ex 86
mvaa 44,228,144
ra 96,225,40960
cv 82,84,85
cvaa 16,192,160,232
s_mov_b32 m0,s69
ex 87
mvaa 16,200,164
ra 100,225,41984
cv 83,86,87
mvaa 16,208,168
ds 249,8,77,0
mvaa 16,216,172
ex 112
cvaa 20,220,172,232
ex 113
mvaa 20,212,168
ra 104,225,43008
cv 84,112,113
mvaa 20,204,164
ex 114
mvaa 20,196,160
ra 108,225,44032
cvaa 48,224,160,232
L_vs17:
s_cbranch_execz L_vr1
ex 115
mvaa 48,232,164
ds 249,8,77,1024
mvaa 48,240,168
ex 116
mvaa 48,248,172
ra 112,225,45056
cv 85,114,115
cvaa 52,252,172,232
ex 117
mvaa 52,244,168
ra 116,225,46080
cv 86,116,117
mvaa 52,236,164
ex 118
mvaa 52,228,160
ex 119
cvaa 24,192,176,232
ds 249,8,77,2048
mvaa 24,200,180
ra 120,225,47104
cv 87,118,119
mvaa 24,208,184
ex 88
mvaa 24,216,188
ra 124,225,48128
cvaa 28,220,188,232
mvaa 28,212,184
ex 89
mvaa 28,204,180
ex 90
mvaa 28,196,176
ds 249,8,77,3072
cvaa 56,224,176,232
ex 91
mvaa 56,232,180
ex 92
mvaa 56,240,184
ex 93
mvaa 56,248,188
ex 94
cvaa 60,252,188,232
mvaa 60,244,184
ex 95
mvaa 60,236,180
cv 88,88,89
cv 89,90,91
mvaa 60,228,176
s_waitcnt vmcnt(8) lgkmcnt(0)
s_barrier
maav 0,64,64
ex 120
maav 0,96,68
s_mov_b32 m0,s74
ex 121
maav 4,68,64
ds 255,8,78,0
maav 4,100,68
ex 122
maav 8,72,64
ex 123
maav 8,104,68
cv 90,92,93
cv 91,94,95
maav 12,76,64
ex 124
maav 12,108,68
ex 125
v_smfmac_f32_16x16x64_bf16 v[144:147],v[228:231],v[64:71],v248
cv 92,120,121
maav 16,80,64
ex 126
maav 16,112,68
buffer_load_dword v247,v245,s[36:39],s85 offen
maav 20,84,64
ex 127
maav 20,116,68
cv 93,122,123
cv 94,124,125
maav 24,88,64
cv 95,126,127
maav 24,120,68
ra 192,224,32768
maav 28,92,64
ds 255,8,78,1024
maav 28,124,68
s_lshl_b32 s86,s19,3
s_lshr_b32 s87,s18,2
s_add_u32 s86,s86,s87
maav 32,64,72
ra 196,224,33792
maav 32,96,76
s_add_u32 s85,s85,131072
maav 36,68,72
ra 200,224,34816
maav 36,100,76
s_lshl_b32 s86,s86,7
s_add_u32 s86,s86,83898368
v_mov_b32_e32 v238,1
maav 40,72,72
ds 255,8,78,2048
maav 40,104,76
s_cmp_eq_u32 s12,0
s_cselect_b64 exec,1,0
buffer_atomic_add v238,off,s[8:11],s86 sc1
s_mov_b64 exec,-1
maav 44,76,72
ra 204,224,35840
maav 44,108,76
v_smfmac_f32_16x16x64_bf16 v[148:151],v[228:231],v[72:79],v248
ra 208,224,36864
maav 48,80,72
maav 48,112,76
maav 52,84,72
ds 255,8,78,3072
maav 52,116,76
ra 212,224,37888
maav 56,88,72
ex 0
maav 56,120,76
ra 216,224,38912
maav 60,92,72
ex 1
maav 60,124,76
s_mov_b32 m0,s75
ex 2
zvav 160,64,80
ra 220,224,39936
cv 0,0,1
mvav 160,96,84
ds 255,8,79,0
zvav 164,68,80
ex 3
mvav 164,100,84
ra 224,224,40960
cv 1,2,3
zvav 168,72,80
ex 4
mvav 168,104,84
ra 228,224,41984
zvav 172,76,80
ds 255,8,79,1024
mvav 172,108,84
s_add_u32 s46,s46,16384
ex 5
v_smfmac_f32_16x16x64_bf16 v[152:155],v[228:231],v[80:87],v248
ra 232,224,43008
zvav 176,80,80
s_add_u32 s47,s47,32768
ex 6
mvav 176,112,84
s_add_u32 s95,s47,32768
ex 7
zvav 180,84,80
ra 236,224,44032
cv 2,4,5
mvav 180,116,84
ex 32
zvav 184,88,80
ds 255,8,79,2048
mvav 184,120,84
ex 33
zvav 188,92,80
ra 240,224,45056
cv 3,6,7
mvav 188,124,84
ex 34
mvav 192,64,88
ra 244,224,46080
cv 4,32,33
mvav 192,96,92
ex 35
mvav 196,68,88
ex 36
mvav 196,100,92
ds 255,8,79,3072
mvav 200,72,88
ra 248,224,47104
cv 5,34,35
mvav 200,104,92
s_cmp_ge_i32 s47,s46
ex 37
mvav 204,76,88
s_cselect_b32 s78,s95,s46
ex 38
mvav 204,108,92
s_cselect_b32 s79,s95,268435456
ex 39
v_smfmac_f32_16x16x64_bf16 v[156:159],v[228:231],v[88:95],v248
ra 252,224,48128
mvav 208,80,88
s_add_u32 s79,s79,16384
ex 8
mvav 208,112,92
ex 9
mvav 212,84,88
ex 10
mvav 212,116,92
ex 11
mvav 216,88,88
cv 6,36,37
cv 7,38,39
mvav 216,120,92
ex 12
mvav 220,92,88
ex 13
mvav 220,124,92
s_waitcnt vmcnt(16) lgkmcnt(0)
s_barrier
cvaa 64,192,128,232
s_bitcmp0_b32 s82,s81
s_cbranch_scc1 L_loop
s_bitcmp1_b32 s15,s81
s_cbranch_scc1 L_short
.p2align 6
L_loop:
mvaa 64,200,132
s_add_u32 s81,s81,1
ex 14
mvaa 64,208,136
s_add_u32 s44,s44,1024
ex 15
mvaa 64,216,140
cv 8,8,9
cv 9,10,11
cvaa 68,220,140,232
s_mov_b32 m0,s70
ex 40
mvaa 68,212,136
ds 249,8,78,0
mvaa 68,204,132
ex 41
mvaa 68,196,128
ex 42
cvaa 96,224,128,232
ra 64,225,0
cv 10,12,13
mvaa 96,232,132
ex 43
mvaa 96,240,136
ra 68,225,1024
cv 11,14,15
mvaa 96,248,140
ex 44
cvaa 100,252,140,232
ds 249,8,78,1024
mvaa 100,244,136
ex 45
mvaa 100,236,132
ra 72,225,2048
cv 12,40,41
mvaa 100,228,128
ex 46
cvaa 72,192,144,232
ra 76,225,3072
cv 13,42,43
mvaa 72,200,148
ex 47
mvaa 72,208,152
ex 16
mvaa 72,216,156
ds 249,8,78,2048
cvaa 76,220,156,232
ra 80,225,4096
cv 14,44,45
mvaa 76,212,152
ex 17
mvaa 76,204,148
ra 84,225,5120
cv 15,46,47
mvaa 76,196,144
ex 18
cvaa 104,224,144,232
ex 19
mvaa 104,232,148
ra 88,225,6144
cv 16,16,17
mvaa 104,240,152
ds 249,8,78,3072
mvaa 104,248,156
ex 20
cvaa 108,252,156,232
ra 92,225,7168
cv 17,18,19
mvaa 108,244,152
ex 21
mvaa 108,236,148
ex 22
mvaa 108,228,144
ra 96,225,8192
cv 18,20,21
cvaa 80,192,160,232
s_mov_b32 m0,s71
ex 23
mvaa 80,200,164
ra 100,225,9216
cv 19,22,23
mvaa 80,208,168
ds 249,8,79,0
mvaa 80,216,172
ex 48
cvaa 84,220,172,232
ex 49
mvaa 84,212,168
ra 104,225,10240
cv 20,48,49
mvaa 84,204,164
ex 50
mvaa 84,196,160
ra 108,225,11264
cvaa 112,224,160,232
ex 51
mvaa 112,232,164
ds 249,8,79,1024
mvaa 112,240,168
ex 52
mvaa 112,248,172
ra 112,225,12288
cv 21,50,51
cvaa 116,252,172,232
ex 53
mvaa 116,244,168
ra 116,225,13312
cv 22,52,53
mvaa 116,236,164
ex 54
mvaa 116,228,160
ex 55
cvaa 88,192,176,232
ds 249,8,79,2048
mvaa 88,200,180
ra 120,225,14336
cv 23,54,55
mvaa 88,208,184
ex 24
mvaa 88,216,188
ra 124,225,15360
cvaa 92,220,188,232
buffer_store_dwordx4 v[250:253],v244,s[8:11],s44 offen offset:1024 sc0 sc1
mvaa 92,212,184
ex 25
mvaa 92,204,180
ex 26
mvaa 92,196,176
ds 249,8,79,3072
cvaa 120,224,176,232
ex 27
mvaa 120,232,180
ex 28
mvaa 120,240,184
ex 29
mvaa 120,248,188
ex 30
cvaa 124,252,188,232
buffer_store_dwordx4 v[250:253],v244,s[8:11],s44 offen sc0 sc1
mvaa 124,244,184
ex 31
mvaa 124,236,180
cv 24,24,25
cv 25,26,27
mvaa 124,228,176
s_waitcnt vmcnt(19) lgkmcnt(0)
s_barrier
maav 0,64,0
ex 56
maav 0,96,4
s_mov_b32 m0,s72
ex 57
maav 4,68,0
ds 255,8,76,0
maav 4,100,4
ex 58
maav 8,72,0
ex 59
maav 8,104,4
cv 26,28,29
cv 27,30,31
maav 12,76,0
s_cmp_lg_u32 s80,s81
ex 60
maav 12,108,4
ex 61
v_smfmac_f32_16x16x64_bf16 v[144:147],v[228:231],v[0:7],v248
cv 28,56,57
maav 16,80,0
ex 62
maav 16,112,4
buffer_load_dword v247,v246,s[24:27],s49 offen
maav 20,84,0
ex 63
maav 20,116,4
cv 29,58,59
cv 30,60,61
maav 24,88,0
s_cbranch_scc1 L_evb_r
v_add_u32_e32 v224,s94,v224
s_lshl_b32 s86,s12,12
s_add_u32 s86,s86,131072
v_add_u32_e32 v236,s86,v244
ra 160,236,0
ra 164,236,1024
ra 168,236,2048
ra 172,236,3072
ra 176,236,16384
ra 180,236,17408
ra 184,236,18432
ra 188,236,19456
L_evb_r:
cv 31,62,63
maav 24,120,4
ra 192,224,0
maav 28,92,0
ds 255,8,76,1024
maav 28,124,4
maav 32,64,8
ra 196,224,1024
maav 32,96,12
s_add_u32 s49,s49,32768
maav 36,68,8
ra 200,224,2048
maav 36,100,12
maav 40,72,8
ds 255,8,76,2048
maav 40,104,12
maav 44,76,8
ra 204,224,3072
maav 44,108,12
v_smfmac_f32_16x16x64_bf16 v[148:151],v[228:231],v[8:15],v248
ra 208,224,4096
maav 48,80,8
maav 48,112,12
maav 52,84,8
ds 255,8,76,3072
maav 52,116,12
ra 212,224,5120
maav 56,88,8
ex 64
maav 56,120,12
ra 216,224,6144
maav 60,92,8
ex 65
maav 60,124,12
s_mov_b32 m0,s73
ex 66
mvav 160,64,16
ra 220,224,7168
cv 64,64,65
mvav 160,96,20
ds 255,8,77,0
mvav 164,68,16
ex 67
mvav 164,100,20
ra 224,224,8192
cv 65,66,67
mvav 168,72,16
ex 68
mvav 168,104,20
ra 228,224,9216
mvav 172,76,16
ds 255,8,77,1024
mvav 172,108,20
s_add_u32 s46,s46,16384
ex 69
v_smfmac_f32_16x16x64_bf16 v[152:155],v[228:231],v[16:23],v248
ra 232,224,10240
mvav 176,80,16
s_add_u32 s47,s47,32768
ex 70
mvav 176,112,20
s_add_u32 s95,s47,32768
ex 71
mvav 180,84,16
ra 236,224,11264
cv 66,68,69
mvav 180,116,20
ex 96
mvav 184,88,16
ds 255,8,77,2048
mvav 184,120,20
ex 97
mvav 188,92,16
ra 240,224,12288
cv 67,70,71
mvav 188,124,20
ex 98
mvav 192,64,24
ra 244,224,13312
cv 68,96,97
mvav 192,96,28
)"
R"(ex 99
mvav 196,68,24
ex 100
mvav 196,100,28
ds 255,8,77,3072
mvav 200,72,24
ra 248,224,14336
cv 69,98,99
mvav 200,104,28
s_cmp_ge_i32 s47,s46
ex 101
mvav 204,76,24
s_cselect_b32 s76,s95,s46
ex 102
mvav 204,108,28
s_cselect_b32 s77,s95,268435456
ex 103
v_smfmac_f32_16x16x64_bf16 v[156:159],v[228:231],v[24:31],v248
ra 252,224,15360
mvav 208,80,24
s_add_u32 s77,s77,16384
ex 72
mvav 208,112,28
ex 73
mvav 212,84,24
ex 74
mvav 212,116,28
ex 75
mvav 216,88,24
cv 70,100,101
cv 71,102,103
mvav 216,120,28
ex 76
mvav 220,92,24
ex 77
mvav 220,124,28
s_waitcnt vmcnt(19) lgkmcnt(0)
s_barrier
cvaa 0,192,128,232
mvaa 0,200,132
s_add_u32 s81,s81,1
ex 78
mvaa 0,208,136
s_add_u32 s44,s44,1024
ex 79
mvaa 0,216,140
cv 72,72,73
cv 73,74,75
cvaa 4,220,140,232
s_mov_b32 m0,s68
ex 104
mvaa 4,212,136
ds 249,8,76,0
mvaa 4,204,132
ex 105
mvaa 4,196,128
ex 106
cvaa 32,224,128,232
ra 64,225,32768
cv 74,76,77
mvaa 32,232,132
ex 107
mvaa 32,240,136
ra 68,225,33792
cv 75,78,79
mvaa 32,248,140
ex 108
cvaa 36,252,140,232
ds 249,8,76,1024
mvaa 36,244,136
ex 109
mvaa 36,236,132
ra 72,225,34816
cv 76,104,105
mvaa 36,228,128
ex 110
cvaa 8,192,144,232
ra 76,225,35840
cv 77,106,107
mvaa 8,200,148
ex 111
mvaa 8,208,152
ex 80
mvaa 8,216,156
ds 249,8,76,2048
cvaa 12,220,156,232
ra 80,225,36864
cv 78,108,109
mvaa 12,212,152
ex 81
mvaa 12,204,148
ra 84,225,37888
cv 79,110,111
mvaa 12,196,144
ex 82
cvaa 40,224,144,232
ex 83
mvaa 40,232,148
ra 88,225,38912
cv 80,80,81
mvaa 40,240,152
ds 249,8,76,3072
mvaa 40,248,156
ex 84
cvaa 44,252,156,232
ra 92,225,39936
cv 81,82,83
mvaa 44,244,152
ex 85
mvaa 44,236,148
ex 86
mvaa 44,228,144
ra 96,225,40960
cv 82,84,85
cvaa 16,192,160,232
s_mov_b32 m0,s69
ex 87
mvaa 16,200,164
ra 100,225,41984
cv 83,86,87
mvaa 16,208,168
ds 249,8,77,0
mvaa 16,216,172
ex 112
cvaa 20,220,172,232
ex 113
mvaa 20,212,168
ra 104,225,43008
cv 84,112,113
mvaa 20,204,164
ex 114
mvaa 20,196,160
ra 108,225,44032
cvaa 48,224,160,232
ex 115
mvaa 48,232,164
ds 249,8,77,1024
mvaa 48,240,168
ex 116
mvaa 48,248,172
ra 112,225,45056
cv 85,114,115
cvaa 52,252,172,232
ex 117
mvaa 52,244,168
ra 116,225,46080
cv 86,116,117
mvaa 52,236,164
ex 118
mvaa 52,228,160
ex 119
cvaa 24,192,176,232
ds 249,8,77,2048
mvaa 24,200,180
ra 120,225,47104
cv 87,118,119
mvaa 24,208,184
ex 88
mvaa 24,216,188
ra 124,225,48128
cvaa 28,220,188,232
mvaa 28,212,184
ex 89
mvaa 28,204,180
ex 90
mvaa 28,196,176
ds 249,8,77,3072
cvaa 56,224,176,232
ex 91
mvaa 56,232,180
ex 92
mvaa 56,240,184
ex 93
mvaa 56,248,188
ex 94
cvaa 60,252,188,232
mvaa 60,244,184
ex 95
mvaa 60,236,180
cv 88,88,89
cv 89,90,91
mvaa 60,228,176
s_waitcnt vmcnt(18) lgkmcnt(0)
s_barrier
maav 0,64,64
ex 120
maav 0,96,68
s_mov_b32 m0,s74
ex 121
maav 4,68,64
ds 255,8,78,0
maav 4,100,68
ex 122
maav 8,72,64
ex 123
maav 8,104,68
cv 90,92,93
cv 91,94,95
maav 12,76,64
ex 124
maav 12,108,68
ex 125
v_smfmac_f32_16x16x64_bf16 v[144:147],v[228:231],v[64:71],v248
cv 92,120,121
maav 16,80,64
ex 126
maav 16,112,68
buffer_load_dword v247,v245,s[36:39],s85 offen
maav 20,84,64
ex 127
maav 20,116,68
cv 93,122,123
cv 94,124,125
maav 24,88,64
cv 95,126,127
maav 24,120,68
ra 192,224,32768
maav 28,92,64
ds 255,8,78,1024
maav 28,124,68
maav 32,64,72
ra 196,224,33792
maav 32,96,76
s_add_u32 s85,s85,131072
maav 36,68,72
ra 200,224,34816
maav 36,100,76
maav 40,72,72
ds 255,8,78,2048
maav 40,104,76
maav 44,76,72
ra 204,224,35840
maav 44,108,76
v_smfmac_f32_16x16x64_bf16 v[148:151],v[228:231],v[72:79],v248
ra 208,224,36864
maav 48,80,72
maav 48,112,76
maav 52,84,72
ds 255,8,78,3072
maav 52,116,76
ra 212,224,37888
maav 56,88,72
ex 0
maav 56,120,76
ra 216,224,38912
maav 60,92,72
ex 1
maav 60,124,76
s_mov_b32 m0,s75
ex 2
mvav 160,64,80
ra 220,224,39936
cv 0,0,1
mvav 160,96,84
ds 255,8,79,0
mvav 164,68,80
ex 3
mvav 164,100,84
ra 224,224,40960
cv 1,2,3
mvav 168,72,80
ex 4
mvav 168,104,84
ra 228,224,41984
mvav 172,76,80
ds 255,8,79,1024
mvav 172,108,84
s_add_u32 s46,s46,16384
ex 5
v_smfmac_f32_16x16x64_bf16 v[152:155],v[228:231],v[80:87],v248
ra 232,224,43008
mvav 176,80,80
s_add_u32 s47,s47,32768
ex 6
mvav 176,112,84
s_add_u32 s95,s47,32768
ex 7
mvav 180,84,80
ra 236,224,44032
cv 2,4,5
mvav 180,116,84
ex 32
mvav 184,88,80
ds 255,8,79,2048
mvav 184,120,84
ex 33
mvav 188,92,80
ra 240,224,45056
cv 3,6,7
mvav 188,124,84
ex 34
mvav 192,64,88
ra 244,224,46080
cv 4,32,33
mvav 192,96,92
ex 35
mvav 196,68,88
ex 36
mvav 196,100,92
ds 255,8,79,3072
mvav 200,72,88
ra 248,224,47104
cv 5,34,35
mvav 200,104,92
s_cmp_ge_i32 s47,s46
ex 37
mvav 204,76,88
s_cselect_b32 s78,s95,s46
ex 38
mvav 204,108,92
s_cselect_b32 s79,s95,268435456
ex 39
v_smfmac_f32_16x16x64_bf16 v[156:159],v[228:231],v[88:95],v248
ra 252,224,48128
mvav 208,80,88
s_add_u32 s79,s79,16384
ex 8
mvav 208,112,92
ex 9
mvav 212,84,88
ex 10
mvav 212,116,92
ex 11
mvav 216,88,88
cv 6,36,37
cv 7,38,39
mvav 216,120,92
ex 12
mvav 220,92,88
ex 13
mvav 220,124,92
s_waitcnt vmcnt(17) lgkmcnt(0)
s_barrier
cvaa 64,192,128,232
s_bitcmp0_b32 s82,s81
s_cbranch_scc1 L_loop
s_bitcmp0_b32 s15,s81
s_cbranch_scc1 L_eva_r
L_short:
v_add_f32_e32 v226,v152,v153
v_add_f32_e32 v227,v156,v157
v_subrev_u32_e32 v236,0x23800000,v226
v_subrev_u32_e32 v237,0x23800000,v227
v_max_u32_e32 v236,v236,v237
v_cmp_lt_u32_e64 s[40:41],s48,v236
v_rcp_f32_e32 v226,v226
v_rcp_f32_e32 v227,v227
s_add_u32 s86,s13,4096
v_mov_b32_e32 v236,0
v_mov_b32_e32 v237,0
v_mov_b32_e32 v238,0
v_mov_b32_e32 v239,0
v_mov_b64_e32 v[152:153],0
v_mov_b64_e32 v[156:157],0
v_mul_f32_e32 v226,v242,v226
v_mul_f32_e32 v227,v243,v227
v_add_u32_e32 v225,s94,v225
v_pk_mul_f32 v[160:161],v[160:161],v[226:227] op_sel:[0,0] op_sel_hi:[1,0]
v_pk_mul_f32 v[162:163],v[162:163],v[226:227] op_sel:[0,0] op_sel_hi:[1,0]
v_pk_mul_f32 v[164:165],v[164:165],v[226:227] op_sel:[0,0] op_sel_hi:[1,0]
v_pk_mul_f32 v[166:167],v[166:167],v[226:227] op_sel:[0,0] op_sel_hi:[1,0]
cv 160,160,161
cv 161,162,163
cv 162,164,165
cv 163,166,167
buffer_store_dwordx4 v[160:163],v244,s[8:11],s13 offen offset:0 sc0 sc1
v_pk_mul_f32 v[168:169],v[168:169],v[226:227] op_sel:[0,0] op_sel_hi:[1,0]
v_pk_mul_f32 v[170:171],v[170:171],v[226:227] op_sel:[0,0] op_sel_hi:[1,0]
v_pk_mul_f32 v[172:173],v[172:173],v[226:227] op_sel:[0,0] op_sel_hi:[1,0]
v_pk_mul_f32 v[174:175],v[174:175],v[226:227] op_sel:[0,0] op_sel_hi:[1,0]
cv 168,168,169
cv 169,170,171
cv 170,172,173
cv 171,174,175
buffer_store_dwordx4 v[168:171],v244,s[8:11],s13 offen offset:2048 sc0 sc1
v_pk_mul_f32 v[176:177],v[176:177],v[226:227] op_sel:[0,0] op_sel_hi:[1,0]
v_pk_mul_f32 v[178:179],v[178:179],v[226:227] op_sel:[0,0] op_sel_hi:[1,0]
v_pk_mul_f32 v[180:181],v[180:181],v[226:227] op_sel:[0,0] op_sel_hi:[1,0]
v_pk_mul_f32 v[182:183],v[182:183],v[226:227] op_sel:[0,0] op_sel_hi:[1,0]
cv 176,176,177
cv 177,178,179
cv 178,180,181
v_mfma_f32_32x32x16_bf16 v[160:175],v[236:239],v[236:239],0
cv 179,182,183
buffer_store_dwordx4 v[176:179],v244,s[8:11],s86 offen offset:0 sc0 sc1
v_pk_mul_f32 v[184:185],v[184:185],v[226:227] op_sel:[0,0] op_sel_hi:[1,0]
v_pk_mul_f32 v[186:187],v[186:187],v[226:227] op_sel:[0,0] op_sel_hi:[1,0]
v_pk_mul_f32 v[188:189],v[188:189],v[226:227] op_sel:[0,0] op_sel_hi:[1,0]
v_pk_mul_f32 v[190:191],v[190:191],v[226:227] op_sel:[0,0] op_sel_hi:[1,0]
cv 184,184,185
cv 185,186,187
cv 186,188,189
cv 187,190,191
buffer_store_dwordx4 v[184:187],v244,s[8:11],s86 offen offset:2048 sc0 sc1
v_pk_mul_f32 v[192:193],v[192:193],v[226:227] op_sel:[0,1] op_sel_hi:[1,1]
v_pk_mul_f32 v[194:195],v[194:195],v[226:227] op_sel:[0,1] op_sel_hi:[1,1]
v_pk_mul_f32 v[196:197],v[196:197],v[226:227] op_sel:[0,1] op_sel_hi:[1,1]
v_pk_mul_f32 v[198:199],v[198:199],v[226:227] op_sel:[0,1] op_sel_hi:[1,1]
cv 192,192,193
cv 193,194,195
cv 194,196,197
v_mfma_f32_32x32x16_bf16 v[176:191],v[236:239],v[236:239],0
cv 195,198,199
buffer_store_dwordx4 v[192:195],v244,s[8:11],s13 offen offset:1024 sc0 sc1
v_pk_mul_f32 v[200:201],v[200:201],v[226:227] op_sel:[0,1] op_sel_hi:[1,1]
v_pk_mul_f32 v[202:203],v[202:203],v[226:227] op_sel:[0,1] op_sel_hi:[1,1]
v_pk_mul_f32 v[204:205],v[204:205],v[226:227] op_sel:[0,1] op_sel_hi:[1,1]
v_pk_mul_f32 v[206:207],v[206:207],v[226:227] op_sel:[0,1] op_sel_hi:[1,1]
cv 200,200,201
cv 201,202,203
cv 202,204,205
cv 203,206,207
buffer_store_dwordx4 v[200:203],v244,s[8:11],s13 offen offset:3072 sc0 sc1
v_pk_mul_f32 v[208:209],v[208:209],v[226:227] op_sel:[0,1] op_sel_hi:[1,1]
v_pk_mul_f32 v[210:211],v[210:211],v[226:227] op_sel:[0,1] op_sel_hi:[1,1]
v_pk_mul_f32 v[212:213],v[212:213],v[226:227] op_sel:[0,1] op_sel_hi:[1,1]
v_pk_mul_f32 v[214:215],v[214:215],v[226:227] op_sel:[0,1] op_sel_hi:[1,1]
cv 208,208,209
cv 209,210,211
cv 210,212,213
v_mfma_f32_32x32x16_bf16 v[192:207],v[236:239],v[236:239],0
cv 211,214,215
buffer_store_dwordx4 v[208:211],v244,s[8:11],s86 offen offset:1024 sc0 sc1
v_pk_mul_f32 v[216:217],v[216:217],v[226:227] op_sel:[0,1] op_sel_hi:[1,1]
v_pk_mul_f32 v[218:219],v[218:219],v[226:227] op_sel:[0,1] op_sel_hi:[1,1]
v_pk_mul_f32 v[220:221],v[220:221],v[226:227] op_sel:[0,1] op_sel_hi:[1,1]
v_pk_mul_f32 v[222:223],v[222:223],v[226:227] op_sel:[0,1] op_sel_hi:[1,1]
cv 216,216,217
cv 217,218,219
cv 218,220,221
cv 219,222,223
buffer_store_dwordx4 v[216:219],v244,s[8:11],s86 offen offset:3072 sc0 sc1
s_nop 1
v_mfma_f32_32x32x16_bf16 v[208:223],v[236:239],v[236:239],0
L_eva_r:
s_cmp_lg_u32 s81,14
s_cbranch_scc1 L_loop
s_add_u32 s81,s81,1
s_bitcmp1_b32 170,s20
s_cbranch_scc1 L_odd
L_last:
s_lshl_b32 s86,s21,2
s_sub_u32 s87,s13,s67
s_add_u32 s86,s86,s12
mvaa 64,200,132
s_lshl_b32 s86,s86,8
v_add_u32_e32 v252,s87,v244
s_add_u32 s86,s86,L_last-L_att_pc+1024
v_lshl_add_u32 v226,v244,3,s86
s_lshl_b32 s86,s12,1
s_bfe_u32 s87,s21,65539
mvaa 64,208,136
s_add_u32 s86,s86,s87
v_min_u32_e32 v226,L_att_end-L_att_pc-4,v226
s_lshl_b32 s86,s86,7
s_add_u32 s86,s86,L_pend2-L_att_pc
v_writelane_b32 v226,s86,2
mvaa 64,216,140
s_mov_b64 exec,7
global_load_dword v253,v226,s[96:97]
s_mov_b64 exec,-1
cvaa 68,220,140,232
s_add_u32 m0,s52,32768
v_and_b32_e32 v224,16,v244
v_lshlrev_b32_e32 v224,5,v224
mvaa 68,212,136
s_xor_b32 s51,s62,0
v_lshrrev_b32_e32 v239,4,v244
v_lshrrev_b32_e32 v236,3,v239
mvaa 68,204,132
s_lshl_b32 s87,s12,13
v_and_b32_e32 v237,112,v244
v_and_b32_e32 v238,48,v239
mvaa 68,196,128
s_xor_b32 s87,s87,8192
ra 64,225,0
v_lshlrev_b32_e32 v238,1,v238
cvaa 96,224,128,232
buffer_load_dword v253,v224,s[8:11],s53 offen sc1
mvaa 96,232,132
s_mov_b32 s84,6
ra 68,225,1024
v_xor_b32_e32 v237,v237,v238
mvaa 96,240,136
s_add_u32 s81,s81,1
ra 72,225,2048
v_lshl_add_u32 v245,v236,11,v237
mvaa 96,248,140
s_mov_b32 s84,4
v_add_u32_e32 v246,15360,v245
v_add_u32_e32 v249,30720,v245
cvaa 100,252,140,232
s_andn2_b32 s69,64,s54
ra 76,225,3072
s_waitcnt vmcnt(9)
v_add_u32_e32 v247,46080,v245
mvaa 100,244,136
dl 245,24,51,0
mvaa 100,236,132
s_add_u32 s88,s14,4096
ra 80,225,4096
cv 8,8,9
mvaa 100,228,128
s_lshl_b32 s35,s19,1
ra 84,225,5120
cv 9,10,11
cvaa 72,192,144,232
s_lshr_b32 s33,40569,s35
ra 88,225,6144
cv 10,12,13
mvaa 72,200,148
s_and_b32 s33,s33,3
ra 92,225,7168
v_add_u32_e32 v250,4096,v252
mvaa 72,208,152
dl 246,24,51,1024
mvaa 72,216,156
s_lshl_b32 s33,s33,13
ra 96,225,8192
v_mov_b32_e32 v251,4
cvaa 76,220,156,232
s_lshr_b32 s34,59294,s35
ra 100,225,9216
s_and_b32 s34,s34,3
mvaa 76,212,152
s_lshl_b32 s34,s34,13
ra 104,225,10240
s_xor_b32 s35,s33,s34
mvaa 76,204,148
s_and_b32 s98,s19,3
ra 108,225,11264
s_lshl_b32 s98,s98,13
mvaa 76,196,144
dl 249,24,51,2048
cvaa 104,224,144,232
s_mov_b32 s74,0x300003
ra 112,225,12288
s_mov_b32 s75,0x30000300
mvaa 104,232,148
s_lshl_b32 s71,s12,15
ra 116,225,13312
s_add_u32 s71,s71,s54
mvaa 104,240,152
s_mov_b64 s[44:45],s[30:31]
ra 120,225,14336
s_mov_b32 s46,4194304
mvaa 104,248,156
s_mov_b32 s47,131072
ex 14
cvaa 108,252,156,232
dl 247,24,51,3072
mvaa 108,244,152
s_add_u32 m0,s52,49152
s_xor_b32 s51,s63,0
ra 124,225,15360
mvaa 108,236,148
s_lshl_b32 s70,s23,11
ex 15
mvaa 108,228,144
s_add_u32 s70,s70,s71
ex 40
cvaa 80,192,160,232
ex 41
mvaa 80,200,164
dl 245,24,51,0
mvaa 80,208,168
ex 42
mvaa 80,216,172
ex 43
cvaa 84,220,172,232
ex 44
mvaa 84,212,168
ex 45
mvaa 84,204,164
dl 246,24,51,1024
mvaa 84,196,160
ex 46
cvaa 112,224,160,232
ex 47
mvaa 112,232,164
ex 16
mvaa 112,240,168
ex 17
mvaa 112,248,172
dl 249,24,51,2048
cvaa 116,252,172,232
ex 18
mvaa 116,244,168
ex 19
mvaa 116,236,164
ex 20
mvaa 116,228,160
ex 21
cvaa 88,192,176,232
dl 247,24,51,3072
s_waitcnt vmcnt(8)
v_cmp_ne_u32_e32 vcc,4,v253
mvaa 88,200,180
s_cbranch_vccnz L_spbe
L_sprbe:
ex 22
mvaa 88,208,184
s_xor_b32 s50,s66,262144
ex 23
mvaa 88,216,188
ex 48
cvaa 92,220,188,232
ex 49
mvaa 92,212,184
la 128,252,50,0
mvaa 92,204,180
s_waitcnt lgkmcnt(0)
s_barrier
mvaa 92,196,176
s_mov_b32 m0,s72
ex 50
cvaa 120,224,176,232
s_lshl_b32 s72,s22,11
ex 51
mvaa 120,232,180
s_add_u32 s72,s72,s71
ex 52
mvaa 120,240,184
ds 255,8,76,0
mvaa 120,248,188
s_add_u32 s71,s70,131072
ex 53
cvaa 124,252,188,232
ex 54
mvaa 124,244,184
ex 55
mvaa 124,236,180
ex 24
mvaa 124,228,176
ds 255,8,76,1024
maav 0,64,0
s_cmp_lg_u32 s19,7
s_cbranch_scc1 L_j7e
s_nop 1
s_lshl_b32 s86,s12,12
s_add_u32 s86,s86,131072
v_add_u32_e32 v236,s86,v244
ra 160,236,0
ra 164,236,1024
ra 168,236,2048
ra 172,236,3072
ra 176,236,16384
ra 180,236,17408
ra 184,236,18432
ra 188,236,19456
v_add_u32_e32 v224,s94,v224
s_waitcnt lgkmcnt(0)
L_j7e:
ex 25
maav 0,96,4
ex 26
maav 4,68,0
ex 27
maav 4,100,4
cv 11,14,15
cv 12,40,41
maav 8,72,0
ds 255,8,76,2048
maav 8,104,4
cv 13,42,43
cv 14,44,45
maav 12,76,0
cv 15,46,47
cv 16,16,17
maav 12,108,4
ex 28
v_smfmac_f32_16x16x64_bf16 v[144:147],v[228:231],v[0:7],v248
cv 17,18,19
maav 16,80,0
ds 255,8,76,3072
maav 16,112,4
s_mov_b32 m0,s73
ex 29
maav 20,84,0
s_add_u32 s73,s72,131072
ex 30
maav 20,116,4
ex 31
maav 24,88,0
ex 56
maav 24,120,4
ds 255,8,77,0
maav 28,92,0
ex 57
maav 28,124,4
ex 58
maav 32,64,8
ex 59
maav 32,96,12
ex 60
maav 36,68,8
ds 255,8,77,1024
maav 36,100,12
cv 18,20,21
cv 19,22,23
maav 40,72,8
cv 20,48,49
cv 21,50,51
maav 40,104,12
cv 22,52,53
cv 23,54,55
maav 44,76,8
ex 61
maav 44,108,12
ds 255,8,77,2048
v_smfmac_f32_16x16x64_bf16 v[148:151],v[228:231],v[8:15],v248
cv 24,24,25
maav 48,80,8
L_vs18:
s_cbranch_execz L_vs22
ex 62
maav 48,112,12
ex 63
maav 52,84,8
ra 192,244,0
cv 25,26,27
maav 52,116,12
ds 255,8,77,3072
maav 56,88,8
cv 26,28,29
cv 27,30,31
maav 56,120,12
cv 28,56,57
cv 29,58,59
maav 60,92,8
cv 30,60,61
cv 31,62,63
maav 60,124,12
ex 64
mvav 160,64,16
la 132,252,50,2048
mvav 160,96,20
ex 65
mvav 164,68,16
ex 66
mvav 164,100,20
ex 67
mvav 168,72,16
ex 68
mvav 168,104,20
la 136,250,50,0
mvav 172,76,16
ex 69
mvav 172,108,20
ex 70
v_smfmac_f32_16x16x64_bf16 v[152:155],v[228:231],v[16:23],v248
ra 200,244,2048
mvav 176,80,16
ra 208,244,4096
cv 64,64,65
mvav 176,112,20
la 140,250,50,2048
mvav 180,84,16
ra 216,244,6144
cv 65,66,67
mvav 180,116,20
ex 71
mvav 184,88,16
ex 96
mvav 184,120,20
s_barrier
ex 97
mvav 188,92,16
la 156,250,50,3072
mvav 188,124,20
s_add_u32 m0,s52,131072
ra 196,244,1024
s_xor_b32 s51,s62,1048576
mvav 192,64,24
ra 204,244,3072
cv 66,68,69
mvav 192,96,28
ra 212,244,5120
cv 67,70,71
mvav 196,68,24
ra 220,244,7168
cv 68,96,97
mvav 196,100,28
la 152,250,50,1024
mvav 200,72,24
ex 98
mvav 200,104,28
ex 99
mvav 204,76,24
ra 224,244,8192
cv 69,98,99
mvav 204,108,28
ra 232,244,10240
v_add_u32_e32 v6,s87,v244
v_smfmac_f32_16x16x64_bf16 v[156:159],v[228:231],v[24:31],v248
ra 240,244,12288
mvav 208,80,24
la 148,252,50,3072
mvav 208,112,28
s_xor_b32 s87,s52,4096
ra 248,244,14336
v_add_u32_e32 v7,98304,v6
mvav 212,84,24
s_add_u32 s87,s87,150528
ra 252,244,15360
v_add_u32_e32 v5,65536,v244
mvav 212,116,28
ra 244,244,13312
v_add_u32_e32 v32,s87,v244
mvav 216,88,24
ra 228,244,9216
v_and_b32_e32 v224,960,v244
mvav 216,120,28
la 144,252,50,1024
mvav 220,92,24
s_add_u32 s50,s50,s55
ra 236,244,11264
mvav 220,124,28
ra 64,225,32768
s_waitcnt lgkmcnt(8)
cvaa 24,192,176,232
dl 245,24,51,0
mvaa 24,200,180
ra 96,225,40960
mvaa 24,208,184
ra 68,225,33792
mvaa 24,216,188
dl 246,24,51,1024
cvaa 16,192,160,128
ra 100,225,41984
mvaa 16,200,164
ra 72,225,34816
mvaa 16,208,168
dl 249,24,51,2048
mvaa 16,216,172
ra 104,225,43008
cvaa 28,196,176,232
ra 76,225,35840
mvaa 28,204,180
dl 247,24,51,3072
mvaa 28,212,184
s_add_u32 m0,s52,147456
s_xor_b32 s51,s63,1048576
ra 108,225,44032
mvaa 28,220,188
ra 80,225,36864
cvaa 20,196,160,132
dl 245,24,51,0
mvaa 20,204,164
ra 112,225,45056
mvaa 20,212,168
ra 192,244,16384
mvaa 20,220,172
dl 246,24,51,1024
cvaa 56,224,176,232
ra 200,244,18432
s_waitcnt lgkmcnt(7)
mvaa 56,232,180
ra 208,244,20480
mvaa 56,240,184
dl 249,24,51,2048
mvaa 56,248,188
ra 216,244,22528
cvaa 48,224,160,136
ra 196,244,17408
mvaa 48,232,164
ra 204,244,19456
mvaa 48,240,168
ra 212,244,21504
mvaa 48,248,172
ra 220,244,23552
cvaa 60,228,176,232
ra 224,244,24576
mvaa 60,236,180
ra 232,244,26624
mvaa 60,244,184
ra 240,244,28672
mvaa 60,252,188
ra 248,244,30720
cvaa 52,228,160,140
ra 84,225,37888
mvaa 52,236,164
ra 116,225,46080
mvaa 52,244,168
la 160,252,50,0
ra 228,244,25600
mvaa 52,252,172
ra 236,244,27648
s_waitcnt lgkmcnt(10)
cvaa 8,192,176,128
ra 244,244,29696
mvaa 8,200,180
la 164,252,50,2048
ra 252,244,31744
mvaa 8,208,184
ra 88,225,38912
mvaa 8,216,188
ra 120,225,47104
cvaa 12,196,176,132
la 168,250,50,0
mvaa 12,204,180
ra 92,225,39936
ra 124,225,48128
s_waitcnt lgkmcnt(9)
mvaa 12,212,184
ex 100
mvaa 12,220,188
la 172,250,50,2048
cvaa 40,224,176,136
ex 101
mvaa 40,232,180
ex 102
mvaa 40,240,184
ex 103
mvaa 40,248,188
cv 70,100,101
cv 71,102,103
s_waitcnt lgkmcnt(4)
cvaa 44,228,176,140
ex 72
mvaa 44,236,180
s_waitcnt vmcnt(18) lgkmcnt(0)
s_barrier
ex 73
mvaa 44,244,184
la 176,252,50,1024
mvaa 44,252,188
ex 74
maav 0,64,64
ex 75
maav 0,96,68
la 188,250,50,3072
maav 4,68,64
ra 192,5,0
cv 72,72,73
maav 4,100,68
ra 196,5,1024
cv 73,74,75
maav 8,72,64
la 184,250,50,1024
ds_write_b128 v6,a[0:3] offset:0
maav 8,104,68
ex 76
maav 12,76,64
ex 77
maav 12,108,68
la 180,252,50,3072
ds_write_b128 v6,a[4:7] offset:1024
v_smfmac_f32_16x16x64_bf16 v[144:147],v[228:231],v[64:71],v248
cv 74,76,77
maav 16,80,64
ex 78
maav 16,112,68
ds_write_b128 v6,a[8:11] offset:2048
ex 79
maav 20,84,64
ex 104
maav 20,116,68
ds_write_b128 v6,a[12:15] offset:3072
ex 105
maav 24,88,64
ex 106
ex 107
ex 108
maav 24,120,68
ds_write_b128 v6,a[16:19] offset:4096
cv 75,78,79
maav 28,92,64
ex 109
ex 110
ex 111
maav 28,124,68
cv 76,104,105
cv 77,106,107
cv 78,108,109
cv 79,110,111
maav 32,64,72
ds_write_b128 v6,a[20:23] offset:5120
maav 32,96,76
ex 80
maav 36,68,72
ex 81
maav 36,100,76
ds_write_b128 v6,a[24:27] offset:6144
maav 40,72,72
ex 82
maav 40,104,76
ex 83
maav 44,76,72
ds_write_b128 v6,a[28:31] offset:7168
maav 44,108,76
cv 80,80,81
cv 81,82,83
v_smfmac_f32_16x16x64_bf16 v[148:151],v[228:231],v[72:79],v248
ex 84
maav 48,80,72
ds_write_b128 v7,a[32:35] offset:0
maav 48,112,76
ex 85
ex 86
)"
R"(ex 87
maav 52,84,72
ex 112
ex 113
maav 52,116,76
ds_write_b128 v7,a[36:39] offset:1024
cv 82,84,85
maav 56,88,72
ex 114
ex 115
ex 116
maav 56,120,76
ex 117
ex 118
ex 119
maav 60,92,72
ds_write_b128 v7,a[40:43] offset:2048
cv 83,86,87
maav 60,124,76
cv 84,112,113
cv 85,114,115
cv 86,116,117
cv 87,118,119
mvav 160,64,80
ex 88
mvav 160,96,84
ds_write_b128 v7,a[44:47] offset:3072
mvav 164,68,80
v_add_f32_e32 v130,v144,v145
v_add_f32_e32 v131,v148,v149
mvav 164,100,84
ex 89
mvav 168,72,80
ds_write_b128 v7,a[48:51] offset:4096
mvav 168,104,84
ex 90
mvav 172,76,80
ex 91
mvav 172,108,84
ds_write_b128 v32,v[128:131]
v_smfmac_f32_16x16x64_bf16 v[152:155],v[228:231],v[80:87],v248
ex 92
ex 93
mvav 176,80,80
ds_write_b128 v7,a[52:55] offset:5120
ex 94
mvav 176,112,84
ra 200,5,2048
ra 204,5,3072
ex 95
cv 88,88,89
mvav 180,84,80
ra 208,5,4096
ra 212,5,5120
cv 89,90,91
cv 90,92,93
ex 120
mvav 180,116,84
ds_write_b128 v7,a[56:59] offset:6144
ex 121
mvav 184,88,80
ex 122
ex 123
cv 91,94,95
mvav 184,120,84
ex 124
ex 125
mvav 188,92,80
ds_write_b128 v7,a[60:63] offset:7168
ex 126
mvav 188,124,84
ra 216,5,6144
ra 220,5,7168
ra 224,5,8192
ra 232,5,10240
ex 127
cv 92,120,121
mvav 192,64,88
ra 240,5,12288
ra 248,5,14336
ra 228,5,9216
ra 236,5,11264
ra 244,5,13312
ra 252,5,15360
cv 93,122,123
cv 94,124,125
cv 95,126,127
v_xor_b32_e32 v6,8192,v6
s_nop 0
mvav 192,96,92
ex 16
mvav 196,68,88
s_waitcnt lgkmcnt(10)
s_xor_b32 s87,s87,0
mvav 196,100,92
s_xor_b32 s50,s66,294912
s_barrier
v_xor_b32_e32 v7,8192,v7
mvav 200,72,88
la 4,252,50,0
ra 64,5,16384
ra 96,5,24576
rv 64,6,0
rv 68,6,1024
rv 72,6,2048
mvav 200,104,92
rv 76,6,3072
rv 80,6,4096
rv 84,6,5120
ex 17
ex 18
mvav 204,76,88
rv 96,7,0
rv 100,7,1024
ex 19
mvav 204,108,92
la 12,252,50,2048
rv 104,7,2048
rv 108,7,3072
cv 16,16,17
v_smfmac_f32_16x16x64_bf16 v[156:159],v[228:231],v[88:95],v248
rv 112,7,4096
rv 116,7,5120
ex 20
ex 21
mvav 208,80,88
rv 120,7,6144
rv 124,7,7168
ex 22
ex 23
mvav 208,112,92
la 20,250,50,0
ra 68,5,17408
ra 100,5,25600
cv 17,18,19
cv 18,20,21
mvav 212,84,88
ra 72,5,18432
ra 104,5,26624
ex 48
ex 49
mvav 212,116,92
ra 76,5,19456
ra 108,5,27648
ex 50
ex 51
mvav 216,88,88
la 28,250,50,2048
ra 80,5,20480
ra 112,5,28672
ra 84,5,21504
mvav 216,120,92
ra 116,5,29696
cv 19,22,23
ex 52
ex 53
mvav 220,92,88
ra 88,5,22528
cv 20,48,49
ex 54
ex 55
mvav 220,124,92
la 60,250,50,3072
ra 120,5,30720
cv 21,50,51
cv 22,52,53
cv 23,54,55
s_bitcmp1_b32 s82,s81
s_cbranch_scc1 L_pend2
L_ret_eb4:
mvav 160,192,16
ra 92,5,23552
rv 88,6,6144
ra 124,5,31744
rv 92,6,7168
ex 24
s_waitcnt lgkmcnt(10)
mvav 160,224,20
ex 25
ex 26
v_smfmac_f32_16x16x64_bf16 v[152:155],v[228:231],v[16:23],v248
ex 27
v_xor_b32_e32 v32,4096,v32
mvav 164,196,16
la 52,250,50,1024
ds_read_b128 v[236:239],v32
cv 24,24,25
ex 28
mvav 164,228,20
ex 29
cv 25,26,27
ex 30
ex 31
mvav 168,200,16
ex 56
ex 57
mvav 168,232,20
la 44,252,50,3072
cv 26,28,29
ex 58
ex 59
mvav 172,204,16
ex 60
ex 61
ex 62
mvav 172,236,20
s_waitcnt lgkmcnt(0)
s_barrier
cv 27,30,31
ex 63
mvav 176,208,16
la 36,252,50,1024
cv 28,56,57
cv 29,58,59
cv 30,60,61
cv 31,62,63
mvav 176,240,20
s_add_u32 m0,s52,147456
ex 8
s_xor_b32 s51,s63,1048576
ex 9
ex 10
mvav 180,212,16
s_add_u32 s50,s50,s55
ex 11
cv 8,8,9
cv 9,10,11
ex 12
mvav 180,244,20
dl 247,24,51,3072
ex 13
ex 14
mvav 184,216,16
s_add_u32 m0,s52,0
ex 15
ex 40
ex 41
mvav 184,248,20
s_xor_b32 s51,s64,0
ex 42
ex 43
ex 44
mvav 188,220,16
la 0,252,50,0
ex 45
ex 46
mvav 188,252,20
ex 47
cv 10,12,13
cv 11,14,15
cv 12,40,41
cv 13,42,43
cv 14,44,45
v_smfmac_f32_16x16x64_bf16 v[156:159],v[228:231],v[24:31],v248
cv 15,46,47
v_bfe_u32 v225,v244,1,5
s_nop 0
v_smfmac_f32_16x16x64_bf16 v[156:159],v[228:231],v[8:15],v248
v_lshl_add_u32 v224,v224,1,v225
v_bfe_u32 v225,v244,7,2
v_lshl_add_u32 v224,v225,5,v224
mvav 192,192,24
la 8,252,50,2048
v_xor_b32_e32 v224,s69,v224
v_add_f32_e32 v152,v152,v153
v_add_f32_e32 v137,v152,v238
v_subrev_u32_e32 v236,0x23800000,v137
mvav 192,224,28
v_rcp_f32_e32 v137,v137
mvav 192,64,8
tv 128,224,32768
v_mul_f32_e32 v232,v240,v137
v_mul_f32_e32 v64,v232,v64
v_mul_f32_e32 v65,v232,v65
v_mul_f32_e32 v66,v232,v66
mvav 192,96,12
la 16,250,50,0
v_mul_f32_e32 v67,v232,v67
v_mul_f32_e32 v68,v232,v68
v_mul_f32_e32 v69,v232,v69
v_mul_f32_e32 v70,v232,v70
mvav 196,196,24
v_fmac_f32_e32 v64,v232,v160
v_fmac_f32_e32 v65,v232,v161
v_mul_f32_e32 v71,v232,v71
mvav 196,228,28
v_fmac_f32_e32 v66,v232,v162
v_fmac_f32_e32 v67,v232,v163
v_fmac_f32_e32 v68,v232,v164
v_fmac_f32_e32 v69,v232,v165
v_fmac_f32_e32 v70,v232,v166
mvav 196,68,8
la 24,250,50,2048
v_fmac_f32_e32 v71,v232,v167
cv 160,64,65
cv 161,66,67
cv 162,68,69
cv 163,70,71
mvav 196,100,12
tv 130,224,34816
tv 132,224,36864
mvav 200,200,24
tv 134,224,38912
tv 144,224,40960
v_mul_f32_e32 v72,v232,v72
v_mul_f32_e32 v73,v232,v73
v_mul_f32_e32 v74,v232,v74
mvav 200,232,28
L_vs19:
s_cbranch_execz L_vs23
buffer_store_dwordx4 v[160:163],v244,s[8:11],s14 offen offset:0 sc0 sc1
v_mul_f32_e32 v75,v232,v75
v_mul_f32_e32 v76,v232,v76
v_mul_f32_e32 v77,v232,v77
v_mul_f32_e32 v78,v232,v78
mvav 200,72,8
v_fmac_f32_e32 v72,v232,v168
v_fmac_f32_e32 v73,v232,v169
v_mul_f32_e32 v79,v232,v79
mvav 200,104,12
v_fmac_f32_e32 v74,v232,v170
v_fmac_f32_e32 v75,v232,v171
v_fmac_f32_e32 v76,v232,v172
v_fmac_f32_e32 v77,v232,v173
v_fmac_f32_e32 v78,v232,v174
mvav 204,204,24
la 56,250,50,3072
v_fmac_f32_e32 v79,v232,v175
cv 168,72,73
cv 169,74,75
cv 170,76,77
cv 171,78,79
mvav 204,236,28
tv 146,224,43008
tv 148,224,45056
mvav 204,76,8
tv 150,224,47104
v_mul_f32_e32 v80,v232,v80
v_mul_f32_e32 v81,v232,v81
v_mul_f32_e32 v82,v232,v82
mvav 204,108,12
buffer_store_dwordx4 v[168:171],v244,s[8:11],s14 offen offset:2048 sc0 sc1
v_mul_f32_e32 v83,v232,v83
v_mul_f32_e32 v84,v232,v84
v_mul_f32_e32 v85,v232,v85
v_mul_f32_e32 v86,v232,v86
v_mul_f32_e32 v87,v232,v87
mvav 208,208,24
v_fmac_f32_e32 v80,v232,v176
v_fmac_f32_e32 v81,v232,v177
v_fmac_f32_e32 v82,v232,v178
mvav 208,240,28
v_fmac_f32_e32 v83,v232,v179
v_fmac_f32_e32 v84,v232,v180
v_fmac_f32_e32 v85,v232,v181
cv 176,80,81
cv 177,82,83
mvav 208,80,8
la 48,250,50,1024
v_fmac_f32_e32 v86,v232,v182
v_fmac_f32_e32 v87,v232,v183
cv 178,84,85
cv 179,86,87
mvav 208,112,12
v_mul_f32_e32 v88,v232,v88
v_mul_f32_e32 v89,v232,v89
v_mul_f32_e32 v90,v232,v90
mvav 212,212,24
v_mul_f32_e32 v91,v232,v91
v_mul_f32_e32 v92,v232,v92
v_mul_f32_e32 v93,v232,v93
v_mul_f32_e32 v94,v232,v94
v_mul_f32_e32 v95,v232,v95
mvav 212,244,28
buffer_store_dwordx4 v[176:179],v244,s[8:11],s88 offen offset:0 sc0 sc1
v_fmac_f32_e32 v88,v232,v184
v_fmac_f32_e32 v89,v232,v185
v_fmac_f32_e32 v90,v232,v186
v_fmac_f32_e32 v91,v232,v187
cv 184,88,89
mvav 212,84,8
v_fmac_f32_e32 v92,v232,v188
cv 185,90,91
mvav 212,116,12
v_fmac_f32_e32 v93,v232,v189
v_fmac_f32_e32 v94,v232,v190
v_fmac_f32_e32 v95,v232,v191
cv 186,92,93
cv 187,94,95
mvav 216,216,24
la 40,252,50,3072
v_add_f32_e32 v156,v156,v157
v_add_f32_e32 v141,v156,v239
v_subrev_u32_e32 v237,0x23800000,v141
v_rcp_f32_e32 v141,v141
mvav 216,248,28
v_mul_f32_e32 v233,v241,v141
v_mul_f32_e32 v96,v233,v96
mvav 216,88,8
v_mul_f32_e32 v97,v233,v97
v_mul_f32_e32 v98,v233,v98
v_mul_f32_e32 v99,v233,v99
v_mul_f32_e32 v100,v233,v100
mvav 216,120,12
buffer_store_dwordx4 v[184:187],v244,s[8:11],s88 offen offset:2048 sc0 sc1
v_fmac_f32_e32 v96,v233,v192
v_fmac_f32_e32 v97,v233,v193
v_mul_f32_e32 v101,v233,v101
v_mul_f32_e32 v102,v233,v102
v_mul_f32_e32 v103,v233,v103
mvav 220,220,24
v_fmac_f32_e32 v98,v233,v194
v_fmac_f32_e32 v99,v233,v195
mvav 220,252,28
v_fmac_f32_e32 v100,v233,v196
v_fmac_f32_e32 v101,v233,v197
cv 192,96,97
cv 193,98,99
mvav 220,92,8
la 32,252,50,1024
v_fmac_f32_e32 v102,v233,v198
v_fmac_f32_e32 v103,v233,v199
cv 194,100,101
cv 195,102,103
mvav 220,124,12
v_xor_b32_e32 v225,32,v224
v_xor_b32_e32 v226,64,v224
s_waitcnt lgkmcnt(0)
zvva 0,128,128
tv 138,225,34816
tv 142,225,38912
tv 152,225,40960
s_waitcnt vmcnt(36)
mvva 0,132,132
tv 136,225,32768
tv 154,225,43008
mvva 0,144,136
tv 140,225,36864
tv 158,225,47104
mvva 0,148,140
tv 156,225,45056
v_mul_f32_e32 v104,v233,v104
v_mul_f32_e32 v105,v233,v105
v_mul_f32_e32 v106,v233,v106
zvva 4,148,156
buffer_store_dwordx4 v[192:195],v244,s[8:11],s14 offen offset:1024 sc0 sc1
v_mul_f32_e32 v107,v233,v107
v_mul_f32_e32 v108,v233,v108
v_mul_f32_e32 v109,v233,v109
v_mul_f32_e32 v110,v233,v110
mvva 4,144,152
v_fmac_f32_e32 v104,v233,v200
v_fmac_f32_e32 v105,v233,v201
v_fmac_f32_e32 v106,v233,v202
v_mul_f32_e32 v111,v233,v111
mvva 4,132,148
v_fmac_f32_e32 v107,v233,v203
v_fmac_f32_e32 v108,v233,v204
v_fmac_f32_e32 v109,v233,v205
cv 200,104,105
cv 201,106,107
mvva 4,128,144
v_fmac_f32_e32 v110,v233,v206
v_fmac_f32_e32 v111,v233,v207
cv 202,108,109
cv 203,110,111
s_waitcnt vmcnt(16)
zvva 8,128,160
v_cmp_lt_u32_e64 s[42:43],s48,v236
v_mul_f32_e32 v112,v233,v112
s_or_b64 s[40:41],s[40:41],s[42:43]
v_cmp_lt_u32_e64 s[42:43],s48,v237
mvva 8,132,164
s_or_b64 s[40:41],s[40:41],s[42:43]
v_mul_f32_e32 v113,v233,v113
v_mul_f32_e32 v114,v233,v114
v_mul_f32_e32 v115,v233,v115
mvva 8,144,168
dl 245,36,51,0
v_mul_f32_e32 v116,v233,v116
v_mul_f32_e32 v117,v233,v117
v_mul_f32_e32 v118,v233,v118
v_mul_f32_e32 v119,v233,v119
v_mul_f32_e32 v120,v233,v120
mvva 8,148,172
v_fmac_f32_e32 v112,v233,v208
v_fmac_f32_e32 v113,v233,v209
v_fmac_f32_e32 v114,v233,v210
v_fmac_f32_e32 v115,v233,v211
zvva 12,148,188
v_fmac_f32_e32 v116,v233,v212
v_fmac_f32_e32 v117,v233,v213
cv 208,112,113
cv 209,114,115
mvva 12,144,184
v_fmac_f32_e32 v118,v233,v214
v_fmac_f32_e32 v119,v233,v215
cv 210,116,117
cv 211,118,119
mvva 12,132,180
v_mul_f32_e32 v121,v233,v121
v_mul_f32_e32 v122,v233,v122
v_mul_f32_e32 v123,v233,v123
mvva 12,128,176
v_mul_f32_e32 v124,v233,v124
v_mul_f32_e32 v125,v233,v125
v_mul_f32_e32 v126,v233,v126
v_mul_f32_e32 v127,v233,v127
s_waitcnt lgkmcnt(0)
zvva 28,136,176
buffer_store_dwordx4 v[200:203],v244,s[8:11],s14 offen offset:3072 sc0 sc1
tv 144,226,40960
v_fmac_f32_e32 v120,v233,v216
v_fmac_f32_e32 v121,v233,v217
v_fmac_f32_e32 v122,v233,v218
v_fmac_f32_e32 v123,v233,v219
mvva 28,140,180
tv 128,226,32768
tv 130,226,34816
cv 216,120,121
cv 217,122,123
mvva 28,152,184
tv 132,226,36864
tv 134,226,38912
v_fmac_f32_e32 v124,v233,v220
v_fmac_f32_e32 v125,v233,v221
v_fmac_f32_e32 v126,v233,v222
mvva 28,156,188
tv 146,226,43008
v_fmac_f32_e32 v127,v233,v223
cv 218,124,125
cv 219,126,127
zvva 24,156,172
tv 148,226,45056
tv 150,226,47104
mvva 24,152,168
v_xor_b32_e32 v227,96,v224
v_add_u32_e32 v232,131072,v224
mvva 24,140,164
buffer_store_dwordx4 v[208:211],v244,s[8:11],s88 offen offset:1024 sc0 sc1
mvva 24,136,160
v_add_u32_e32 v233,131072,v225
v_add_u32_e32 v234,131072,v226
zvva 20,136,144
v_add_u32_e32 v235,131072,v227
v_mov_b32_e32 v242,83914752
mvva 20,140,148
v_add_u32_e32 v228,65536,v224
v_add_u32_e32 v229,65536,v225
mvva 20,152,152
v_add_u32_e32 v230,65536,v226
v_add_u32_e32 v231,65536,v227
mvva 20,156,156
v_and_b32_e32 v243,240,v244
v_lshlrev_b32_e32 v243,7,v243
zvva 16,156,140
buffer_store_dwordx4 v[216:219],v244,s[8:11],s88 offen offset:3072 sc0 sc1
s_cmp_lg_u64 s[40:41],0
s_cbranch_scc1 L_redo
L_redo_r4:
mvva 16,152,136
tv 156,227,45056
v_mov_b32_e32 v236,16256
mvva 16,140,132
tv 158,227,47104
v_and_b32_e32 v248,256,v244
mvva 16,136,128
tv 152,227,40960
v_lshrrev_b32_e32 v248,3,v248
s_waitcnt lgkmcnt(3)
zvva 32,128,128
tv 140,227,36864
v_add_u32_e32 v243,v243,v248
mvva 32,132,132
tv 136,227,32768
tv 138,227,34816
mvva 32,144,136
dl 246,36,51,1024
tv 142,227,38912
mvva 32,148,140
tv 154,227,43008
v_and_b32_e32 v248,512,v244
zvva 36,148,156
v_lshrrev_b32_e32 v248,5,v248
v_add_u32_e32 v243,v243,v248
mvva 36,144,152
v_and_b32_e32 v248,16,v244
v_lshlrev_b32_e32 v248,v248,v236
mvva 36,132,148
v_cndmask_b32_e64 v238,0,v248,s[74:75]
s_mov_b32 s74,0xc0000c
mvva 36,128,144
s_mov_b32 s75,0xc0000c00
v_mov_b32_e32 v240,0
v_cndmask_b32_e64 v239,0,v248,s[74:75]
zvva 40,128,160
dl 249,36,51,2048
mvva 40,132,164
v_mov_b32_e32 v241,0
v_mov_b32_e32 v236,0
mvva 40,144,168
v_mov_b32_e32 v237,0
mvva 40,148,172
zvva 44,148,188
mvva 44,144,184
mvva 44,132,180
dl 247,36,51,3072
s_add_u32 m0,s52,16384
s_xor_b32 s51,s65,0
mvva 44,128,176
tv 144,224,57344
s_waitcnt lgkmcnt(1)
zvva 60,136,176
tv 132,224,53248
tv 134,224,55296
mvva 60,140,180
tv 128,224,49152
tv 130,224,51200
mvva 60,152,184
mvva 60,156,188
tv 146,224,59392
zvva 56,156,172
dl 245,36,51,0
tv 148,224,61440
tv 150,224,63488
mvva 56,152,168
mvva 56,140,164
mvva 56,136,160
zvva 52,136,144
mvva 52,140,148
mvva 52,152,152
dl 246,36,51,1024
mvva 52,156,156
zvva 48,156,140
mvva 48,152,136
mvva 48,140,132
tv 156,225,61440
mvva 48,136,128
tv 152,225,57344
s_waitcnt lgkmcnt(2)
zvva 64,128,128
dl 249,36,51,2048
tv 140,225,53248
mvva 64,132,132
tv 136,225,49152
tv 138,225,51200
mvva 64,144,136
tv 142,225,55296
mvva 64,148,140
tv 154,225,59392
zvva 68,148,156
tv 158,225,63488
mvva 68,144,152
mvva 68,132,148
dl 247,36,51,3072
s_lshl_b32 s51,s33,5
s_add_u32 m0,s52,98304
s_xor_b32 s51,s64,s51
mvva 68,128,144
zvva 72,128,160
mvva 72,132,164
s_waitcnt vmcnt(18)
s_mov_b32 s90,0
mvva 72,144,168
s_xor_b32 s50,s67,0
mvva 72,148,172
zvva 76,148,188
dl 245,36,51,0
mvva 76,144,184
mvva 76,132,180
tv 148,226,61440
mvva 76,128,176
tv 144,226,57344
s_waitcnt lgkmcnt(2)
zvva 92,136,176
tv 132,226,53248
mvva 92,140,180
tv 128,226,49152
tv 130,226,51200
mvva 92,152,184
dl 246,36,51,1024
tv 134,226,55296
mvva 92,156,188
tv 146,226,59392
zvva 88,156,172
tv 150,226,63488
mvva 88,152,168
mvva 88,140,164
mvva 88,136,160
zvva 84,136,144
dl 249,36,51,2048
mvva 84,140,148
mvva 84,152,152
mvva 84,156,156
zvva 80,156,140
mvva 80,152,136
mvva 80,140,132
dl 247,36,51,3072
mvva 80,136,128
s_lshl_b32 s51,s33,5
tv 152,227,57344
s_add_u32 m0,s52,114688
s_waitcnt lgkmcnt(1)
zvva 96,128,128
s_xor_b32 s51,s65,s51
tv 140,227,53248
tv 142,227,55296
mvva 96,132,132
tv 136,227,49152
tv 138,227,51200
mvva 96,144,136
mvva 96,148,140
tv 154,227,59392
zvva 100,148,156
lv 164,252,50,0
tv 156,227,61440
tv 158,227,63488
mvva 100,144,152
mvva 100,132,148
mvva 100,128,144
zvva 104,128,160
mvva 104,132,164
mvva 104,144,168
lv 172,252,50,2048
mvva 104,148,172
zvva 108,148,188
mvva 108,144,184
mvva 108,132,180
mvva 108,128,176
s_barrier
s_waitcnt lgkmcnt(0)
zvva 124,136,176
lv 180,250,50,0
tv 132,232,4096
tv 144,232,8192
tv 146,232,10240
mvva 124,140,180
tv 128,232,0
tv 130,232,2048
mvva 124,152,184
tv 134,232,6144
mvva 124,156,188
tv 148,232,12288
tv 150,232,14336
zvva 120,156,172
mvva 120,152,168
mvva 120,140,164
lv 188,250,50,2048
mvva 120,136,160
zvva 116,136,144
mvva 116,140,148
mvva 116,152,152
mvva 116,156,156
zvva 112,156,140
lv 220,250,50,3072
mvva 112,152,136
mvva 112,140,132
tv 156,233,12288
mvva 112,136,128
tv 152,233,8192
s_waitcnt lgkmcnt(2)
mvva 0,128,4
tv 140,233,4096
mvva 0,132,12
tv 136,233,0
tv 138,233,2048
mvva 0,144,20
lv 212,250,50,1024
tv 142,233,6144
mvva 0,148,28
tv 154,233,10240
mvva 4,148,60
tv 158,233,14336
mvva 4,144,52
mvva 4,132,44
mvva 4,128,36
mvva 8,128,0
lv 204,252,50,3072
mvva 8,132,8
mvva 8,144,16
mvva 8,148,24
mvva 12,148,56
s_waitcnt vmcnt(27)
mvva 12,144,48
s_waitcnt vmcnt(25)
mvva 12,132,40
lv 196,252,50,1024
s_waitcnt vmcnt(24)
mvva 12,128,32
tv 144,234,8192
s_waitcnt lgkmcnt(1)
mvva 28,136,32
tv 132,234,4096
tv 134,234,6144
mvva 28,140,40
tv 128,234,0
tv 130,234,2048
mvva 28,152,48
mvva 28,156,56
tv 146,234,10240
mvva 24,156,24
dl 245,36,51,0
s_waitcnt vmcnt(20)
s_barrier
tv 148,234,12288
tv 150,234,14336
mvva 24,152,16
mvva 24,140,8
mvva 24,136,0
mvva 20,136,36
mvva 20,140,44
mvva 20,152,52
s_cmp_eq_u32 s12,0
s_cselect_b64 exec,1,0
buffer_store_dword v251,off,s[8:11],s61 sc0 sc1
s_mov_b64 exec,-1
mvva 20,156,60
mvva 16,156,28
mvva 16,152,20
mvva 16,140,12
tv 156,235,12288
mvva 16,136,4
tv 152,235,8192
s_waitcnt lgkmcnt(2)
mvva 32,128,4
dl 246,36,51,1024
tv 140,235,4096
mvva 32,132,12
tv 136,235,0
tv 138,235,2048
mvva 32,144,20
tv 142,235,6144
mvva 32,148,28
tv 154,235,10240
mvva 36,148,60
tv 158,235,14336
mvva 36,144,52
mvva 36,132,44
dl 249,36,51,2048
mvva 36,128,36
mvva 40,128,0
mvva 40,132,8
mvva 40,144,16
mvva 40,148,24
mvva 44,148,56
dl 247,36,51,3072
s_lshl_b32 s51,s34,5
s_add_u32 m0,s52,65536
s_xor_b32 s51,s64,s51
mvva 44,144,48
tv 148,232,28672
mvva 44,132,40
s_xor_b32 s53,s60,s33
tv 150,232,30720
mvva 44,128,32
tv 144,232,24576
s_waitcnt lgkmcnt(3)
mvva 60,136,32
tv 132,232,20480
mvva 60,140,40
tv 128,232,16384
tv 130,232,18432
mvva 60,152,48
dl 245,36,51,0
tv 134,232,22528
mvva 60,156,56
tv 146,232,26624
mvva 56,156,24
mvva 56,152,16
mvva 56,140,8
mvva 56,136,0
mvva 52,136,36
buffer_load_dword v253,v242,s[8:11],s53 offen sc1
mvva 52,140,44
mvva 52,152,52
mvva 52,156,60
mvva 48,156,28
mvva 48,152,20
mvva 48,140,12
dl 246,36,51,1024
mvva 48,136,4
tv 152,233,24576
s_waitcnt lgkmcnt(1)
mvva 64,128,4
tv 140,233,20480
tv 142,233,22528
mvva 64,132,12
tv 136,233,16384
tv 138,233,18432
mvva 64,144,20
mvva 64,148,28
tv 154,233,26624
mvva 68,148,60
dl 249,36,51,2048
tv 156,233,28672
tv 158,233,30720
mvva 68,144,52
mvva 68,132,44
mvva 68,128,36
mvva 72,128,0
mvva 72,132,8
mvva 72,144,16
dl 247,36,51,3072
s_lshl_b32 s51,s34,5
s_add_u32 m0,s52,81920
s_xor_b32 s51,s65,s51
mvva 72,148,24
L_vs20:
s_cbranch_execz L_vs24
mvva 76,148,56
mvva 76,144,48
mvva 76,132,40
tv 148,234,28672
mvva 76,128,32
tv 144,234,24576
s_waitcnt lgkmcnt(2)
mvva 92,136,32
dl 245,36,51,0
tv 132,234,20480
mvva 92,140,40
tv 128,234,16384
tv 130,234,18432
mvva 92,152,48
tv 134,234,22528
mvva 92,156,56
tv 146,234,26624
mvva 88,156,24
tv 150,234,30720
mvva 88,152,16
mvva 88,140,8
dl 246,36,51,1024
mvva 88,136,0
mvva 84,136,36
mvva 84,140,44
mvva 84,152,52
mvva 84,156,60
mvva 80,156,28
dl 249,36,51,2048
mvva 80,152,20
mvva 80,140,12
tv 156,235,28672
mvva 80,136,4
tv 152,235,24576
s_waitcnt lgkmcnt(2)
mvva 96,128,4
tv 140,235,20480
mvva 96,132,12
tv 136,235,16384
tv 138,235,18432
mvva 96,144,20
dl 247,36,51,3072
s_lshl_b32 s51,s35,5
s_add_u32 m0,s52,32768
s_xor_b32 s51,s64,s51
tv 142,235,22528
mvva 96,148,28
tv 154,235,26624
mvva 100,148,60
tv 158,235,30720
mvva 100,144,52
mvva 100,132,44
mvva 100,128,36
mvva 104,128,0
dl 245,36,51,0
mvva 104,132,8
mvva 104,144,16
mvva 104,148,24
mvva 108,148,56
mvva 108,144,48
mvva 108,132,40
dl 246,36,51,1024
mvva 108,128,32
s_waitcnt vmcnt(27)
s_barrier
s_waitcnt lgkmcnt(0)
mvva 124,136,32
tv 132,224,4096
tv 134,224,6144
tv 144,224,8192
mvva 124,140,40
tv 128,224,0
tv 130,224,2048
mvva 124,152,48
mvva 124,156,56
tv 146,224,10240
mvva 120,156,24
dl 249,36,51,2048
tv 148,224,12288
tv 150,224,14336
mvva 120,152,16
mvva 120,140,8
mvva 120,136,0
mvva 116,136,36
mvva 116,140,44
mvva 116,152,52
dl 247,36,51,3072
s_lshl_b32 s51,s35,5
s_add_u32 m0,s52,49152
s_xor_b32 s51,s65,s51
mvva 116,156,60
)"
R"(mvva 112,156,28
mvva 112,152,20
s_waitcnt vmcnt(11)
v_cmp_ne_u32_e32 vcc,4,v253
mvva 112,140,12
dl 245,36,51,0
s_cbranch_vccnz L_spa3e
L_spra3e:
mvva 112,136,4
s_xor_b32 s50,s67,s33
tv 152,225,8192
s_waitcnt lgkmcnt(1)
mvvv 0,128,164
tv 140,225,4096
tv 142,225,6144
mvvv 0,132,172
tv 136,225,0
tv 138,225,2048
mvvv 0,144,180
mvvv 0,148,188
dl 246,36,51,1024
tv 154,225,10240
mvvv 4,148,220
tv 156,225,12288
tv 158,225,14336
mvvv 4,144,212
mvvv 4,132,204
mvvv 4,128,196
mvvv 8,128,160
la 196,252,50,0
s_xor_b32 s53,s60,s34
mvvv 8,132,168
mvvv 8,144,176
mvvv 8,148,184
mvvv 12,148,216
mvvv 12,144,208
la 204,252,50,2048
mvvv 12,132,200
tv 148,226,12288
mvvv 12,128,192
tv 144,226,8192
s_waitcnt lgkmcnt(2)
mvvv 28,136,192
tv 132,226,4096
mvvv 28,140,200
tv 128,226,0
tv 130,226,2048
mvvv 28,152,208
buffer_load_dword v253,v242,s[8:11],s53 offen sc1
tv 134,226,6144
mvvv 28,156,216
tv 146,226,10240
mvvv 24,156,184
tv 150,226,14336
mvvv 24,152,176
mvvv 24,140,168
mvvv 24,136,160
la 212,250,50,0
mvvv 20,136,196
mvvv 20,140,204
mvvv 20,152,212
mvvv 20,156,220
la 220,250,50,2048
mvvv 16,156,188
mvvv 16,152,180
mvvv 16,140,172
tv 156,227,12288
mvvv 16,136,164
la 252,250,50,3072
tv 152,227,8192
s_waitcnt lgkmcnt(2)
mvvv 32,128,164
tv 140,227,4096
mvvv 32,132,172
tv 136,227,0
tv 138,227,2048
mvvv 32,144,180
tv 142,227,6144
mvvv 32,148,188
la 244,250,50,1024
tv 154,227,10240
mvvv 36,148,220
tv 158,227,14336
mvvv 36,144,212
mvvv 36,132,204
mvvv 36,128,196
la 236,252,50,3072
mvvv 40,128,160
mvvv 40,132,168
mvvv 40,144,176
mvvv 40,148,184
mvvv 44,148,216
la 228,252,50,1024
s_add_u32 s50,s50,s55
mvvv 44,144,208
mvvv 44,132,200
tv 148,224,28672
tv 150,224,30720
mvvv 44,128,192
s_waitcnt lgkmcnt(2)
mvvv 60,136,192
la 192,252,50,0
tv 132,224,20480
mvvv 60,140,200
tv 128,224,16384
tv 130,224,18432
mvvv 60,152,208
tv 134,224,22528
mvvv 60,156,216
tv 144,224,24576
tv 146,224,26624
mvvv 56,156,184
la 200,252,50,2048
mvvv 56,152,176
mvvv 56,140,168
mvvv 56,136,160
mvvv 52,136,196
la 208,250,50,0
mvvv 52,140,204
mvvv 52,152,212
mvvv 52,156,220
mvvv 48,156,188
la 216,250,50,2048
mvvv 48,152,180
mvvv 48,140,172
tv 156,225,28672
mvvv 48,136,164
tv 152,225,24576
s_waitcnt lgkmcnt(2)
mvvv 64,128,164
tv 140,225,20480
mvvv 64,132,172
la 248,250,50,3072
tv 136,225,16384
tv 138,225,18432
mvvv 64,144,180
tv 142,225,22528
mvvv 64,148,188
tv 154,225,26624
mvvv 68,148,220
tv 158,225,30720
mvvv 68,144,212
la 240,250,50,1024
mvvv 68,132,204
mvvv 68,128,196
mvvv 72,128,160
mvvv 72,132,168
la 232,252,50,3072
mvvv 72,144,176
mvvv 72,148,184
mvvv 76,148,216
mvvv 76,144,208
mvvv 76,132,200
la 224,252,50,1024
mvvv 76,128,192
tv 144,226,24576
s_waitcnt lgkmcnt(1)
mvvv 92,136,192
tv 132,226,20480
tv 134,226,22528
mvvv 92,140,200
tv 128,226,16384
tv 130,226,18432
mvvv 92,152,208
dl 249,36,51,2048
mvvv 92,156,216
tv 146,226,26624
mvvv 88,156,184
tv 148,226,28672
tv 150,226,30720
mvvv 88,152,176
mvvv 88,140,168
dl 247,36,51,3072
s_lshl_b32 s51,s98,5
s_xor_b32 s51,s64,s51
s_add_u32 m0,s52,131072
s_xor_b32 s51,s51,4194304
mvvv 88,136,160
s_waitcnt vmcnt(16)
v_cmp_ne_u32_e32 vcc,4,v253
mvvv 84,136,196
s_cbranch_vccnz L_spa4e
L_spra4e:
mvvv 84,140,204
s_xor_b32 s50,s67,s34
mvvv 84,152,212
dl 245,36,51,0
mvvv 84,156,220
mvvv 80,156,188
mvvv 80,152,180
mvvv 80,140,172
la 68,252,50,0
mvvv 80,136,164
tv 152,227,24576
s_waitcnt lgkmcnt(1)
mvvv 96,128,164
tv 140,227,20480
tv 142,227,22528
mvvv 96,132,172
tv 136,227,16384
tv 138,227,18432
mvvv 96,144,180
s_xor_b32 s53,s60,s35
mvvv 96,148,188
la 76,252,50,2048
tv 154,227,26624
mvvv 100,148,220
tv 156,227,28672
tv 158,227,30720
mvvv 100,144,212
mvvv 100,132,204
mvvv 100,128,196
la 84,250,50,0
mvvv 104,128,160
mvvv 104,132,168
mvvv 104,144,176
mvvv 104,148,184
buffer_load_dword v253,v242,s[8:11],s53 offen sc1
mvvv 108,148,216
mvvv 108,144,208
mvvv 108,132,200
mvvv 108,128,192
s_barrier
s_waitcnt lgkmcnt(0)
mvvv 124,136,192
la 92,250,50,2048
tv 132,228,36864
tv 144,228,40960
tv 146,228,43008
mvvv 124,140,200
tv 128,228,32768
tv 130,228,34816
mvvv 124,152,208
tv 134,228,38912
mvvv 124,156,216
tv 148,228,45056
tv 150,228,47104
mvvv 120,156,184
la 124,250,50,3072
mvvv 120,152,176
mvvv 120,140,168
mvvv 120,136,160
mvvv 116,136,196
la 116,250,50,1024
mvvv 116,140,204
mvvv 116,152,212
mvvv 116,156,220
mvvv 112,156,188
la 108,252,50,3072
mvvv 112,152,180
mvvv 112,140,172
tv 156,229,45056
tv 158,229,47104
mvvv 112,136,164
s_waitcnt lgkmcnt(2)
mvva 0,128,196
la 100,252,50,1024
s_add_u32 s50,s50,s55
tv 140,229,36864
mvva 0,132,204
tv 136,229,32768
tv 138,229,34816
s_waitcnt vmcnt(21)
mvva 0,144,212
tv 142,229,38912
mvva 0,148,220
tv 152,229,40960
tv 154,229,43008
mvva 4,148,252
mvva 4,144,244
la 64,252,50,0
mvva 4,132,236
s_waitcnt vmcnt(21)
mvva 4,128,228
s_waitcnt vmcnt(20)
mvva 8,128,192
s_waitcnt vmcnt(19)
mvva 8,132,200
la 72,252,50,2048
s_waitcnt vmcnt(19)
mvva 8,144,208
s_waitcnt vmcnt(18)
mvva 8,148,216
s_waitcnt vmcnt(17)
mvva 12,148,248
s_waitcnt vmcnt(16)
mvva 12,144,240
la 80,250,50,0
s_waitcnt vmcnt(16)
mvva 12,132,232
tv 148,230,45056
s_waitcnt vmcnt(15)
mvva 12,128,224
tv 144,230,40960
s_waitcnt lgkmcnt(2)
mvva 28,136,224
tv 132,230,36864
mvva 28,140,232
la 88,250,50,2048
tv 128,230,32768
tv 130,230,34816
mvva 28,152,240
tv 134,230,38912
mvva 28,156,248
tv 146,230,43008
mvva 24,156,216
tv 150,230,47104
mvva 24,152,208
la 120,250,50,3072
mvva 24,140,200
mvva 24,136,192
mvva 20,136,228
mvva 20,140,236
mvva 20,152,244
la 112,250,50,1024
mvva 20,156,252
mvva 16,156,220
mvva 16,152,212
mvva 16,140,204
la 104,252,50,3072
mvva 16,136,196
tv 152,231,40960
s_waitcnt lgkmcnt(1)
mvva 32,128,196
tv 140,231,36864
tv 142,231,38912
mvva 32,132,204
tv 136,231,32768
tv 138,231,34816
mvva 32,144,212
la 96,252,50,1024
mvva 32,148,220
tv 154,231,43008
mvva 36,148,252
tv 156,231,45056
tv 158,231,47104
mvva 36,144,244
mvva 36,132,236
mvva 36,128,228
dl 246,36,51,1024
mvva 40,128,192
mvva 40,132,200
mvva 40,144,208
mvva 40,148,216
dl 249,36,51,2048
mvva 44,148,248
mvva 44,144,240
mvva 44,132,232
tv 148,228,61440
mvva 44,128,224
dl 247,36,51,3072
s_lshl_b32 s51,s98,5
s_xor_b32 s51,s65,s51
s_add_u32 m0,s52,147456
s_xor_b32 s51,s51,4194304
tv 144,228,57344
s_waitcnt lgkmcnt(2)
mvva 60,136,224
tv 132,228,53248
mvva 60,140,232
tv 128,228,49152
tv 130,228,51200
s_waitcnt vmcnt(16)
v_cmp_ne_u32_e32 vcc,4,v253
mvva 60,152,240
s_cbranch_vccnz L_spa5e
L_spra5e:
tv 134,228,55296
s_xor_b32 s50,s67,s35
mvva 60,156,248
dl 245,36,51,0
tv 146,228,59392
mvva 56,156,216
tv 150,228,63488
mvva 56,152,208
mvva 56,140,200
mvva 56,136,192
la 132,252,50,0
mvva 52,136,228
mvva 52,140,236
mvva 52,152,244
mvva 52,156,252
mvva 48,156,220
la 140,252,50,2048
s_xor_b32 s53,s60,s98
s_xor_b32 s53,s53,512
mvva 48,152,212
tv 156,229,61440
mvva 48,140,204
tv 158,229,63488
mvva 48,136,196
tv 152,229,57344
s_waitcnt lgkmcnt(3)
mvva 64,128,196
la 148,250,50,0
tv 140,229,53248
mvva 64,132,204
tv 136,229,49152
tv 138,229,51200
mvva 64,144,212
tv 142,229,55296
mvva 64,148,220
tv 154,229,59392
mvva 68,148,252
buffer_load_dword v253,v242,s[8:11],s53 offen sc1
mvva 68,144,244
mvva 68,132,236
mvva 68,128,228
mvva 72,128,192
mvva 72,132,200
la 156,250,50,2048
mvva 72,144,208
mvva 72,148,216
mvva 76,148,248
mvva 76,144,240
la 188,250,50,3072
mvva 76,132,232
tv 148,230,61440
mvva 76,128,224
tv 144,230,57344
s_waitcnt lgkmcnt(2)
mvva 92,136,224
tv 132,230,53248
mvva 92,140,232
la 180,250,50,1024
tv 128,230,49152
tv 130,230,51200
mvva 92,152,240
tv 134,230,55296
mvva 92,156,248
tv 146,230,59392
mvva 88,156,216
tv 150,230,63488
mvva 88,152,208
la 172,252,50,3072
mvva 88,140,200
mvva 88,136,192
mvva 84,136,228
mvva 84,140,236
la 164,252,50,1024
s_add_u32 s50,s50,s55
mvva 84,152,244
mvva 84,156,252
mvva 80,156,220
mvva 80,152,212
mvva 80,140,204
la 128,252,50,0
mvva 80,136,196
tv 152,231,57344
s_waitcnt lgkmcnt(1)
mvva 96,128,196
L_vs21:
s_cbranch_execz L_vs17
tv 140,231,53248
tv 142,231,55296
mvva 96,132,204
tv 136,231,49152
tv 138,231,51200
mvva 96,144,212
la 136,252,50,2048
mvva 96,148,220
tv 154,231,59392
mvva 100,148,252
tv 156,231,61440
tv 158,231,63488
mvva 100,144,244
mvva 100,132,236
la 144,250,50,0
mvva 100,128,228
mvva 104,128,192
mvva 104,132,200
mvva 104,144,208
la 152,250,50,2048
mvva 104,148,216
mvva 108,148,248
mvva 108,144,240
mvva 108,132,232
la 184,250,50,3072
mvva 108,128,224
s_barrier
s_waitcnt lgkmcnt(0)
mvva 124,136,224
tv 132,228,4096
tv 134,228,6144
tv 144,228,8192
mvva 124,140,232
tv 128,228,0
tv 130,228,2048
mvva 124,152,240
mvva 124,156,248
la 176,250,50,1024
tv 146,228,10240
mvva 120,156,216
tv 148,228,12288
tv 150,228,14336
mvva 120,152,208
mvva 120,140,200
mvva 120,136,192
la 168,252,50,3072
mvva 116,136,228
mvva 116,140,236
mvva 116,152,244
mvva 116,156,252
la 160,252,50,1024
mvva 112,156,220
mvva 112,152,212
mvva 112,140,204
tv 156,229,12288
tv 158,229,14336
mvva 112,136,196
s_waitcnt lgkmcnt(2)
mvva 0,128,68
dl 246,36,51,1024
tv 140,229,4096
mvva 0,132,76
tv 136,229,0
tv 138,229,2048
mvva 0,144,84
tv 142,229,6144
s_waitcnt vmcnt(22)
mvva 0,148,92
tv 152,229,8192
tv 154,229,10240
mvva 4,148,124
dl 249,36,51,2048
mvva 4,144,116
mvva 4,132,108
mvva 4,128,100
mvva 8,128,64
dl 247,36,51,3072
s_lshl_b32 s51,s98,5
s_xor_b32 s51,s64,s51
s_add_u32 m0,s52,0
s_xor_b32 s51,s51,4456448
mvva 8,132,72
mvva 8,144,80
mvva 8,148,88
s_waitcnt vmcnt(16)
v_cmp_ne_u32_e32 vcc,4,v253
mvva 12,148,120
dl 245,36,51,0
s_cbranch_vccnz L_spa6e
L_spra6e:
mvva 12,144,112
s_xor_b32 s50,s67,s98
mvva 12,132,104
s_xor_b32 s50,s50,131072
tv 148,230,12288
tv 150,230,14336
mvva 12,128,96
s_waitcnt lgkmcnt(2)
mvva 28,136,96
dl 246,36,51,1024
tv 132,230,4096
mvva 28,140,104
tv 128,230,0
tv 130,230,2048
mvva 28,152,112
tv 134,230,6144
mvva 28,156,120
tv 144,230,8192
tv 146,230,10240
mvva 24,156,88
mvva 24,152,80
dl 249,36,51,2048
mvva 24,140,72
s_xor_b32 s53,s60,s98
mvva 24,136,64
s_xor_b32 s53,s53,8704
mvva 20,136,100
mvva 20,140,108
dl 247,36,51,3072
mvva 20,152,116
s_lshl_b32 s51,s98,5
s_add_u32 m0,s52,16384
s_xor_b32 s51,s65,s51
mvva 20,156,124
s_xor_b32 s51,s51,4456448
mvva 16,156,92
mvva 16,152,84
buffer_load_dword v253,v242,s[8:11],s53 offen sc1
mvva 16,140,76
tv 156,231,12288
mvva 16,136,68
tv 152,231,8192
s_waitcnt lgkmcnt(2)
mvva 32,128,68
tv 140,231,4096
mvva 32,132,76
la 4,252,50,0
tv 136,231,0
tv 138,231,2048
mvva 32,144,84
tv 142,231,6144
mvva 32,148,92
tv 154,231,10240
mvva 36,148,124
tv 158,231,14336
mvva 36,144,116
la 12,252,50,2048
mvva 36,132,108
mvva 36,128,100
mvva 40,128,64
mvva 40,132,72
mvva 40,144,80
la 20,250,50,0
mvva 40,148,88
mvva 44,148,120
mvva 44,144,112
mvva 44,132,104
la 28,250,50,2048
mvva 44,128,96
tv 144,228,24576
s_waitcnt lgkmcnt(1)
mvva 60,136,96
tv 132,228,20480
tv 134,228,22528
mvva 60,140,104
tv 128,228,16384
tv 130,228,18432
mvva 60,152,112
la 60,250,50,3072
mvva 60,156,120
tv 146,228,26624
mvva 56,156,88
tv 148,228,28672
tv 150,228,30720
mvva 56,152,80
mvva 56,140,72
mvva 56,136,64
la 52,250,50,1024
mvva 52,136,100
mvva 52,140,108
mvva 52,152,116
mvva 52,156,124
la 44,252,50,3072
mvva 48,156,92
mvva 48,152,84
mvva 48,140,76
tv 156,229,28672
mvva 48,136,68
la 36,252,50,1024
s_add_u32 s50,s50,s55
tv 152,229,24576
s_waitcnt lgkmcnt(2)
mvva 64,128,68
tv 140,229,20480
mvva 64,132,76
tv 136,229,16384
tv 138,229,18432
mvva 64,144,84
tv 142,229,22528
mvva 64,148,92
la 0,252,50,0
tv 154,229,26624
mvva 68,148,124
tv 158,229,30720
mvva 68,144,116
mvva 68,132,108
mvva 68,128,100
la 8,252,50,2048
mvva 72,128,64
mvva 72,132,72
mvva 72,144,80
mvva 72,148,88
mvva 76,148,120
la 16,250,50,0
mvva 76,144,112
mvva 76,132,104
tv 148,230,28672
tv 150,230,30720
mvva 76,128,96
s_waitcnt lgkmcnt(2)
mvva 92,136,96
la 24,250,50,2048
tv 132,230,20480
mvva 92,140,104
tv 128,230,16384
tv 130,230,18432
mvva 92,152,112
tv 134,230,22528
mvva 92,156,120
tv 144,230,24576
tv 146,230,26624
mvva 88,156,88
la 56,250,50,3072
mvva 88,152,80
mvva 88,140,72
mvva 88,136,64
mvva 84,136,100
mvva 84,140,108
la 48,250,50,1024
mvva 84,152,116
mvva 84,156,124
mvva 80,156,92
mvva 80,152,84
la 40,252,50,3072
mvva 80,140,76
tv 156,231,28672
mvva 80,136,68
tv 152,231,24576
s_waitcnt lgkmcnt(2)
mvva 96,128,68
tv 140,231,20480
mvva 96,132,76
la 32,252,50,1024
tv 136,231,16384
tv 138,231,18432
mvva 96,144,84
tv 142,231,22528
mvva 96,148,92
tv 154,231,26624
mvva 100,148,124
tv 158,231,30720
mvva 100,144,116
dl 245,36,51,0
s_waitcnt vmcnt(17)
v_cmp_ne_u32_e32 vcc,4,v253
mvva 100,132,108
s_cbranch_vccnz L_spa7e
L_spra7e:
mvva 100,128,100
s_xor_b32 s50,s67,s98
mvva 104,128,64
s_xor_b32 s50,s50,139264
mvva 104,132,72
dl 246,36,51,1024
mvva 104,144,80
mvva 104,148,88
mvva 108,148,120
mvva 108,144,112
mvva 108,132,104
lv 164,252,50,0
mvva 108,128,96
s_barrier
s_waitcnt lgkmcnt(0)
mvva 124,136,96
s_xor_b32 s53,s60,s98
tv 132,224,36864
tv 134,224,38912
tv 144,224,40960
s_xor_b32 s53,s53,16896
mvva 124,140,104
tv 128,224,32768
tv 130,224,34816
mvva 124,152,112
lv 172,252,50,2048
mvva 124,156,120
tv 146,224,43008
mvva 120,156,88
tv 148,224,45056
tv 150,224,47104
mvva 120,152,80
mvva 120,140,72
buffer_load_dword v253,v242,s[8:11],s53 offen sc1
mvva 120,136,64
mvva 116,136,100
mvva 116,140,108
mvva 116,152,116
lv 180,250,50,0
mvva 116,156,124
mvva 112,156,92
mvva 112,152,84
mvva 112,140,76
lv 188,250,50,2048
mvva 112,136,68
tv 152,225,40960
s_waitcnt lgkmcnt(1)
mvva 0,128,132
tv 140,225,36864
tv 142,225,38912
mvva 0,132,140
tv 136,225,32768
tv 138,225,34816
mvva 0,144,148
mvva 0,148,156
lv 220,250,50,3072
tv 154,225,43008
mvva 4,148,188
tv 156,225,45056
tv 158,225,47104
mvva 4,144,180
mvva 4,132,172
mvva 4,128,164
lv 212,250,50,1024
mvva 8,128,128
mvva 8,132,136
mvva 8,144,144
mvva 8,148,152
lv 204,252,50,3072
mvva 12,148,184
mvva 12,144,176
mvva 12,132,168
tv 148,226,45056
tv 150,226,47104
mvva 12,128,160
s_waitcnt lgkmcnt(2)
mvva 28,136,160
lv 196,252,50,1024
s_add_u32 s50,s50,s55
tv 132,226,36864
mvva 28,140,168
tv 128,226,32768
tv 130,226,34816
mvva 28,152,176
tv 134,226,38912
mvva 28,156,184
tv 144,226,40960
tv 146,226,43008
mvva 24,156,152
lv 160,252,50,0
mvva 24,152,144
mvva 24,140,136
mvva 24,136,128
mvva 20,136,164
lv 168,252,50,2048
mvva 20,140,172
mvva 20,152,180
mvva 20,156,188
mvva 16,156,156
lv 176,250,50,0
mvva 16,152,148
mvva 16,140,140
tv 156,227,45056
tv 158,227,47104
mvva 16,136,132
s_waitcnt lgkmcnt(2)
mvva 32,128,132
lv 184,250,50,2048
tv 140,227,36864
mvva 32,132,140
tv 136,227,32768
tv 138,227,34816
mvva 32,144,148
tv 142,227,38912
mvva 32,148,156
tv 152,227,40960
tv 154,227,43008
mvva 36,148,188
mvva 36,144,180
lv 216,250,50,3072
mvva 36,132,172
mvva 36,128,164
mvva 40,128,128
mvva 40,132,136
lv 208,250,50,1024
mvva 40,144,144
mvva 40,148,152
mvva 44,148,184
mvva 44,144,176
lv 200,252,50,3072
mvva 44,132,168
tv 148,224,61440
mvva 44,128,160
tv 144,224,57344
s_waitcnt lgkmcnt(2)
mvva 60,136,160
tv 132,224,53248
mvva 60,140,168
tv 128,224,49152
tv 130,224,51200
mvva 60,152,176
lv 192,252,50,1024
tv 134,224,55296
mvva 60,156,184
tv 146,224,59392
mvva 56,156,152
tv 150,224,63488
mvva 56,152,144
mvva 56,140,136
dl 249,36,51,2048
mvva 56,136,128
mvva 52,136,164
mvva 52,140,172
mvva 52,152,180
dl 247,36,51,3072
s_lshl_b32 s51,s98,5
s_xor_b32 s51,s64,s51
s_add_u32 m0,s52,98304
s_xor_b32 s51,s51,4718592
mvva 52,156,188
mvva 48,156,156
mvva 48,152,148
mvva 48,140,140
dl 245,36,51,0
mvva 48,136,132
tv 152,225,57344
s_waitcnt vmcnt(17)
v_cmp_ne_u32_e32 vcc,4,v253
s_waitcnt lgkmcnt(1)
mvva 64,128,132
s_cbranch_vccnz L_spa8e
L_spra8e:
tv 140,225,53248
tv 142,225,55296
mvva 64,132,140
s_xor_b32 s50,s67,s98
tv 136,225,49152
tv 138,225,51200
mvva 64,144,148
dl 246,36,51,1024
mvva 64,148,156
s_xor_b32 s50,s50,147456
tv 154,225,59392
mvva 68,148,188
tv 156,225,61440
tv 158,225,63488
mvva 68,144,180
mvva 68,132,172
mvva 68,128,164
dl 249,36,51,2048
mvva 72,128,128
mvva 72,132,136
mvva 72,144,144
s_xor_b32 s53,s60,s98
mvva 72,148,152
dl 247,36,51,3072
s_xor_b32 s53,s53,25088
mvva 76,148,184
s_lshl_b32 s51,s98,5
s_add_u32 m0,s52,114688
s_xor_b32 s51,s65,s51
mvva 76,144,176
s_xor_b32 s51,s51,4718592
mvva 76,132,168
tv 148,226,61440
mvva 76,128,160
buffer_load_dword v253,v242,s[8:11],s53 offen sc1
tv 144,226,57344
s_waitcnt lgkmcnt(2)
mvva 92,136,160
tv 132,226,53248
mvva 92,140,168
tv 128,226,49152
tv 130,226,51200
mvva 92,152,176
tv 134,226,55296
mvva 92,156,184
la 196,252,50,0
tv 146,226,59392
mvva 88,156,152
tv 150,226,63488
mvva 88,152,144
mvva 88,140,136
mvva 88,136,128
la 204,252,50,2048
mvva 84,136,164
mvva 84,140,172
mvva 84,152,180
mvva 84,156,188
mvva 80,156,156
la 212,250,50,0
mvva 80,152,148
mvva 80,140,140
tv 156,227,61440
tv 158,227,63488
mvva 80,136,132
s_waitcnt lgkmcnt(2)
mvva 96,128,132
la 220,250,50,2048
tv 140,227,53248
mvva 96,132,140
tv 136,227,49152
tv 138,227,51200
mvva 96,144,148
tv 142,227,55296
mvva 96,148,156
tv 152,227,57344
tv 154,227,59392
mvva 100,148,188
la 252,250,50,3072
mvva 100,144,180
mvva 100,132,172
mvva 100,128,164
mvva 104,128,128
mvva 104,132,136
la 244,250,50,1024
mvva 104,144,144
mvva 104,148,152
mvva 108,148,184
mvva 108,144,176
la 236,252,50,3072
mvva 108,132,168
mvva 108,128,160
s_barrier
s_waitcnt lgkmcnt(0)
mvva 124,136,160
tv 132,232,4096
tv 144,232,8192
mvva 124,140,168
L_vs22:
s_cbranch_execz L_vr1
la 228,252,50,1024
s_add_u32 s50,s50,s55
tv 128,232,0
tv 130,232,2048
mvva 124,152,176
tv 134,232,6144
mvva 124,156,184
tv 146,232,10240
)"
R"(mvva 120,156,152
tv 148,232,12288
tv 150,232,14336
mvva 120,152,144
la 192,252,50,0
mvva 120,140,136
mvva 120,136,128
mvva 116,136,164
mvva 116,140,172
la 200,252,50,2048
mvva 116,152,180
mvva 116,156,188
mvva 112,156,156
mvva 112,152,148
mvva 112,140,140
la 208,250,50,0
mvva 112,136,132
tv 152,233,8192
s_waitcnt lgkmcnt(1)
mvva 0,128,4
tv 140,233,4096
tv 142,233,6144
mvva 0,132,12
tv 136,233,0
tv 138,233,2048
mvva 0,144,20
la 216,250,50,2048
mvva 0,148,28
tv 154,233,10240
mvva 4,148,60
tv 156,233,12288
tv 158,233,14336
mvva 4,144,52
mvva 4,132,44
la 248,250,50,3072
mvva 4,128,36
mvva 8,128,0
mvva 8,132,8
mvva 8,144,16
la 240,250,50,1024
mvva 8,148,24
mvva 12,148,56
mvva 12,144,48
mvva 12,132,40
la 232,252,50,3072
mvva 12,128,32
tv 144,234,8192
s_waitcnt lgkmcnt(1)
mvva 28,136,32
tv 132,234,4096
tv 134,234,6144
mvva 28,140,40
tv 128,234,0
tv 130,234,2048
mvva 28,152,48
mvva 28,156,56
la 224,252,50,1024
tv 146,234,10240
mvva 24,156,24
tv 148,234,12288
tv 150,234,14336
mvva 24,152,16
mvva 24,140,8
mvva 24,136,0
dl 245,36,51,0
mvva 20,136,36
mvva 20,140,44
s_waitcnt vmcnt(17)
v_cmp_ne_u32_e32 vcc,4,v253
mvva 20,152,52
s_cbranch_vccnz L_spa9e
L_spra9e:
mvva 20,156,60
dl 246,36,51,1024
mvva 16,156,28
s_xor_b32 s50,s67,s98
mvva 16,152,20
s_xor_b32 s50,s50,155648
mvva 16,140,12
tv 156,235,12288
tv 158,235,14336
mvva 16,136,4
s_waitcnt lgkmcnt(2)
mvva 32,128,4
dl 249,36,51,2048
tv 140,235,4096
mvva 32,132,12
tv 136,235,0
tv 138,235,2048
mvva 32,144,20
tv 142,235,6144
mvva 32,148,28
tv 152,235,8192
tv 154,235,10240
mvva 36,148,60
dl 247,36,51,3072
s_lshl_b32 s51,s98,5
s_xor_b32 s51,s64,s51
s_add_u32 m0,s52,65536
s_xor_b32 s51,s51,4980736
s_xor_b32 s53,s60,s98
mvva 36,144,52
s_xor_b32 s53,s53,25216
mvva 36,132,44
mvva 36,128,36
mvva 40,128,0
dl 245,36,51,0
mvva 40,132,8
mvva 40,144,16
mvva 40,148,24
mvva 44,148,56
buffer_load_dword v253,v242,s[8:11],s53 offen sc1
mvva 44,144,48
mvva 44,132,40
tv 148,232,28672
tv 150,232,30720
mvva 44,128,32
s_waitcnt lgkmcnt(2)
mvva 60,136,32
dl 246,36,51,1024
tv 132,232,20480
mvva 60,140,40
tv 128,232,16384
tv 130,232,18432
mvva 60,152,48
tv 134,232,22528
mvva 60,156,56
tv 144,232,24576
tv 146,232,26624
mvva 56,156,24
mvva 56,152,16
dl 249,36,51,2048
mvva 56,140,8
mvva 56,136,0
mvva 52,136,36
mvva 52,140,44
dl 247,36,51,3072
s_lshl_b32 s51,s98,5
s_xor_b32 s51,s64,s51
s_add_u32 m0,s52,81920
s_xor_b32 s51,s51,6029312
mvva 52,152,52
mvva 52,156,60
mvva 48,156,28
mvva 48,152,20
dl 245,36,51,0
mvva 48,140,12
tv 156,233,28672
mvva 48,136,4
tv 152,233,24576
s_waitcnt lgkmcnt(2)
mvva 64,128,4
tv 140,233,20480
mvva 64,132,12
tv 136,233,16384
tv 138,233,18432
mvva 64,144,20
dl 246,36,51,1024
tv 142,233,22528
mvva 64,148,28
tv 154,233,26624
mvva 68,148,60
tv 158,233,30720
mvva 68,144,52
mvva 68,132,44
dl 249,36,51,2048
mvva 68,128,36
mvva 72,128,0
mvva 72,132,8
mvva 72,144,16
dl 247,36,51,3072
mvva 72,148,24
s_lshl_b32 s51,s98,5
s_add_u32 m0,s52,32768
s_xor_b32 s51,s65,s51
mvva 76,148,56
s_xor_b32 s51,s51,4980736
mvva 76,144,48
mvva 76,132,40
la 68,252,50,0
mvva 76,128,32
tv 144,224,8192
s_waitcnt lgkmcnt(1)
mvva 92,136,32
tv 132,224,4096
tv 134,224,6144
mvva 92,140,40
tv 128,224,0
tv 130,224,2048
mvva 92,152,48
la 76,252,50,2048
mvva 92,156,56
tv 146,224,10240
mvva 88,156,24
tv 148,224,12288
tv 150,224,14336
mvva 88,152,16
mvva 88,140,8
mvva 88,136,0
la 84,250,50,0
mvva 84,136,36
mvva 84,140,44
mvva 84,152,52
mvva 84,156,60
la 92,250,50,2048
mvva 80,156,28
mvva 80,152,20
mvva 80,140,12
tv 156,225,12288
mvva 80,136,4
la 124,250,50,3072
tv 152,225,8192
s_waitcnt lgkmcnt(2)
mvvv 0,128,164
tv 140,225,4096
mvvv 0,132,172
tv 136,225,0
tv 138,225,2048
mvvv 0,144,180
tv 142,225,6144
mvvv 0,148,188
la 116,250,50,1024
tv 154,225,10240
mvvv 4,148,220
tv 158,225,14336
mvvv 4,144,212
mvvv 4,132,204
mvvv 4,128,196
la 108,252,50,3072
mvvv 8,128,160
mvvv 8,132,168
mvvv 8,144,176
mvvv 8,148,184
mvvv 12,148,216
la 100,252,50,1024
s_add_u32 s50,s50,s55
mvvv 12,144,208
mvvv 12,132,200
tv 148,226,12288
tv 150,226,14336
mvvv 12,128,192
s_barrier
tv 144,226,8192
s_waitcnt lgkmcnt(3)
mvvv 28,136,192
la 64,252,50,0
mvvv 28,140,200
tv 128,226,0
tv 130,226,2048
mvvv 28,152,208
tv 132,226,4096
tv 134,226,6144
mvvv 28,156,216
tv 146,226,10240
s_waitcnt vmcnt(16)
v_cmp_ne_u32_e32 vcc,4,v253
mvvv 24,156,184
la 72,252,50,2048
s_cbranch_vccnz L_spa10e
L_spra10e:
mvvv 24,152,176
v_and_b32_e32 v242,48,v244
v_lshlrev_b32_e32 v242,3,v242
mvvv 24,140,168
mvvv 24,136,160
mvvv 20,136,196
mvvv 20,140,204
la 80,250,50,0
mvvv 20,152,212
mvvv 20,156,220
mvvv 16,156,188
s_andn2_b32 s53,s61,384
mvvv 16,152,180
la 88,250,50,2048
mvvv 16,140,172
tv 156,227,12288
mvvv 16,136,164
tv 152,227,8192
s_waitcnt lgkmcnt(2)
mvvv 32,128,164
tv 140,227,4096
mvvv 32,132,172
buffer_load_dword v253,v242,s[8:11],s53 offen sc1
tv 136,227,0
tv 138,227,2048
mvvv 32,144,180
tv 142,227,6144
mvvv 32,148,188
tv 154,227,10240
mvvv 36,148,220
tv 158,227,14336
mvvv 36,144,212
la 120,250,50,3072
mvvv 36,132,204
mvvv 36,128,196
mvvv 40,128,160
mvvv 40,132,168
la 112,250,50,1024
mvvv 40,144,176
mvvv 40,148,184
mvvv 44,148,216
mvvv 44,144,208
mvvv 44,132,200
la 104,252,50,3072
mvvv 44,128,192
tv 144,224,24576
s_waitcnt lgkmcnt(1)
mvvv 60,136,192
tv 132,224,20480
tv 134,224,22528
mvvv 60,140,200
tv 128,224,16384
tv 130,224,18432
mvvv 60,152,208
la 96,252,50,1024
s_xor_b32 s50,s67,s98
s_xor_b32 s50,s50,188416
mvvv 60,156,216
tv 146,224,26624
mvvv 56,156,184
tv 148,224,28672
tv 150,224,30720
mvvv 56,152,176
mvvv 56,140,168
la 132,252,50,0
mvvv 56,136,160
mvvv 52,136,196
mvvv 52,140,204
mvvv 52,152,212
la 140,252,50,2048
mvvv 52,156,220
mvvv 48,156,188
mvvv 48,152,180
mvvv 48,140,172
la 148,250,50,0
mvvv 48,136,164
tv 152,225,24576
s_waitcnt lgkmcnt(1)
mvvv 64,128,164
tv 140,225,20480
tv 142,225,22528
mvvv 64,132,172
tv 136,225,16384
tv 138,225,18432
mvvv 64,144,180
mvvv 64,148,188
la 156,250,50,2048
tv 154,225,26624
mvvv 68,148,220
tv 156,225,28672
tv 158,225,30720
mvvv 68,144,212
mvvv 68,132,204
mvvv 68,128,196
la 188,250,50,3072
mvvv 72,128,160
mvvv 72,132,168
mvvv 72,144,176
mvvv 72,148,184
la 180,250,50,1024
mvvv 76,148,216
mvvv 76,144,208
mvvv 76,132,200
tv 148,228,45056
tv 150,228,47104
mvvv 76,128,192
s_waitcnt lgkmcnt(2)
mvvv 92,136,192
la 172,252,50,3072
tv 132,228,36864
mvvv 92,140,200
tv 128,228,32768
tv 130,228,34816
mvvv 92,152,208
tv 134,228,38912
mvvv 92,156,216
tv 144,228,40960
tv 146,228,43008
mvvv 88,156,184
la 164,252,50,1024
s_add_u32 s50,s50,s55
mvvv 88,152,176
mvvv 88,140,168
mvvv 88,136,160
mvvv 84,136,196
la 128,252,50,0
mvvv 84,140,204
mvvv 84,152,212
mvvv 84,156,220
mvvv 80,156,188
la 136,252,50,2048
mvvv 80,152,180
mvvv 80,140,172
tv 156,229,45056
tv 158,229,47104
mvvv 80,136,164
s_waitcnt lgkmcnt(2)
mvva 0,128,196
la 144,250,50,0
tv 140,229,36864
mvva 0,132,204
tv 136,229,32768
tv 138,229,34816
mvva 0,144,212
tv 142,229,38912
mvva 0,148,220
tv 152,229,40960
tv 154,229,43008
mvva 4,148,252
mvva 4,144,244
la 152,250,50,2048
mvva 4,132,236
mvva 4,128,228
mvva 8,128,192
mvva 8,132,200
la 184,250,50,3072
mvva 8,144,208
mvva 8,148,216
s_waitcnt vmcnt(17)
v_cmp_ne_u32_e32 vcc,4,v253
mvva 12,148,248
s_cbranch_vccnz L_sppe
L_sprpe:
mvva 12,144,240
la 176,250,50,1024
mvva 12,132,232
tv 148,230,45056
mvva 12,128,224
tv 144,230,40960
s_waitcnt lgkmcnt(2)
mvva 28,136,224
tv 132,230,36864
mvva 28,140,232
tv 128,230,32768
tv 130,230,34816
mvva 28,152,240
la 168,252,50,3072
tv 134,230,38912
mvva 28,156,248
tv 146,230,43008
mvva 24,156,216
tv 150,230,47104
mvva 24,152,208
mvva 24,140,200
la 160,252,50,1024
mvva 24,136,192
mvva 20,136,228
mvva 20,140,236
mvva 20,152,244
dl 245,36,51,0
mvva 20,156,252
mvva 16,156,220
mvva 16,152,212
mvva 16,140,204
dl 246,36,51,1024
mvva 16,136,196
tv 152,231,40960
s_waitcnt lgkmcnt(1)
mvva 32,128,196
tv 140,231,36864
tv 142,231,38912
mvva 32,132,204
tv 136,231,32768
tv 138,231,34816
mvva 32,144,212
dl 249,36,51,2048
mvva 32,148,220
tv 154,231,43008
mvva 36,148,252
tv 156,231,45056
tv 158,231,47104
mvva 36,144,244
mvva 36,132,236
mvva 36,128,228
dl 247,36,51,3072
s_lshl_b32 s51,s98,5
s_xor_b32 s51,s65,s51
s_add_u32 m0,s52,49152
s_xor_b32 s51,s51,6029312
mvva 40,128,192
mvva 40,132,200
mvva 40,144,208
mvva 40,148,216
dl 245,36,51,0
mvva 44,148,248
mvva 44,144,240
mvva 44,132,232
mvva 44,128,224
dl 246,36,51,1024
s_waitcnt lgkmcnt(0)
mvva 60,136,224
s_barrier
mvva 60,140,232
tv 144,228,57344
tv 146,228,59392
tv 128,228,49152
tv 130,228,51200
mvva 60,152,240
tv 132,228,53248
tv 134,228,55296
mvva 60,156,248
dl 249,36,51,2048
mvva 56,156,216
tv 148,228,61440
tv 150,228,63488
mvva 56,152,208
mvva 56,140,200
mvva 56,136,192
dl 247,36,51,3072
mvva 52,136,228
mvva 52,140,236
mvva 52,152,244
mvva 52,156,252
mvva 48,156,220
mvva 48,152,212
mvva 48,140,204
tv 156,229,61440
mvva 48,136,196
tv 152,229,57344
s_waitcnt lgkmcnt(2)
mvva 64,128,196
tv 140,229,53248
mvva 64,132,204
tv 136,229,49152
tv 138,229,51200
mvva 64,144,212
tv 142,229,55296
mvva 64,148,220
L_vs23:
s_cbranch_execz L_vr1
tv 154,229,59392
mvva 68,148,252
tv 158,229,63488
mvva 68,144,244
mvva 68,132,236
mvva 68,128,228
mvva 72,128,192
mvva 72,132,200
mvva 72,144,208
mvva 72,148,216
mvva 76,148,248
mvva 76,144,240
mvva 76,132,232
tv 148,228,12288
mvva 76,128,224
tv 144,228,8192
s_waitcnt lgkmcnt(2)
mvva 92,136,224
tv 132,228,4096
mvva 92,140,232
tv 128,228,0
tv 130,228,2048
mvva 92,152,240
tv 134,228,6144
mvva 92,156,248
tv 146,228,10240
mvva 88,156,216
tv 150,228,14336
mvva 88,152,208
mvva 88,140,200
mvva 88,136,192
mvva 84,136,228
mvva 84,140,236
mvva 84,152,244
mvva 84,156,252
mvva 80,156,220
mvva 80,152,212
mvva 80,140,204
tv 156,229,12288
mvva 80,136,196
tv 152,229,8192
s_waitcnt lgkmcnt(2)
mvva 0,128,68
tv 140,229,4096
mvva 0,132,76
tv 136,229,0
tv 138,229,2048
mvva 0,144,84
tv 142,229,6144
mvva 0,148,92
tv 154,229,10240
mvva 4,148,124
tv 158,229,14336
mvva 4,144,116
mvva 4,132,108
mvva 4,128,100
mvva 8,128,64
mvva 8,132,72
mvva 8,144,80
mvva 8,148,88
s_waitcnt vmcnt(10)
mvva 12,148,120
mvva 12,144,112
mvva 12,132,104
tv 148,228,28672
mvva 12,128,96
tv 144,228,24576
s_waitcnt lgkmcnt(2)
mvva 28,136,96
tv 132,228,20480
mvva 28,140,104
tv 128,228,16384
tv 130,228,18432
mvva 28,152,112
tv 134,228,22528
mvva 28,156,120
tv 146,228,26624
mvva 24,156,88
tv 150,228,30720
mvva 24,152,80
mvva 24,140,72
mvva 24,136,64
mvva 20,136,100
mvva 20,140,108
mvva 20,152,116
mvva 20,156,124
mvva 16,156,92
mvva 16,152,84
mvva 16,140,76
tv 156,229,28672
mvva 16,136,68
tv 152,229,24576
s_waitcnt lgkmcnt(2)
mvva 0,128,132
tv 140,229,20480
mvva 0,132,140
tv 136,229,16384
tv 138,229,18432
mvva 0,144,148
tv 142,229,22528
mvva 0,148,156
tv 154,229,26624
mvva 4,148,188
tv 158,229,30720
mvva 4,144,180
mvva 4,132,172
mvva 4,128,164
mvva 8,128,128
mvva 8,132,136
cv 0,0,1
cv 1,2,3
mvva 8,144,144
mvva 8,148,152
mvva 12,148,184
mvva 12,144,176
cv 4,4,5
cv 5,6,7
s_waitcnt vmcnt(8)
mvva 12,132,168
tv 148,230,12288
mvva 12,128,160
tv 144,230,8192
cv 8,8,9
s_waitcnt lgkmcnt(2)
mvva 28,136,160
tv 132,230,4096
cv 9,10,11
mvva 28,140,168
tv 128,230,0
tv 130,230,2048
mvva 28,152,176
tv 134,230,6144
cv 12,12,13
mvva 28,156,184
tv 146,230,10240
cv 13,14,15
mvva 24,156,152
tv 150,230,14336
mvva 24,152,144
mvva 24,140,136
mvva 24,136,128
mvva 20,136,164
cv 14,28,29
cv 15,30,31
mvva 20,140,172
buffer_store_dwordx4 v[12:15],v244,s[8:11],s56 offen offset:3072 sc0 sc1
mvva 20,152,180
mvva 20,156,188
mvva 16,156,156
cv 10,24,25
cv 11,26,27
mvva 16,152,148
buffer_store_dwordx4 v[8:11],v244,s[8:11],s56 offen offset:2048 sc0 sc1
mvva 16,140,140
tv 156,231,12288
mvva 16,136,132
cv 6,20,21
cv 7,22,23
s_waitcnt lgkmcnt(1)
mvva 32,128,68
tv 140,231,4096
mvva 32,132,76
tv 136,231,0
tv 138,231,2048
mvva 32,144,84
buffer_store_dwordx4 v[4:7],v244,s[8:11],s56 offen offset:1024 sc0 sc1
tv 142,231,6144
mvva 32,148,92
tv 152,231,8192
tv 154,231,10240
mvva 36,148,124
tv 158,231,14336
cv 2,16,17
cv 3,18,19
mvva 36,144,116
mvva 36,132,108
buffer_store_dwordx4 v[0:3],v244,s[8:11],s56 offen offset:0 sc0 sc1
mvva 36,128,100
mvva 40,128,64
mvva 40,132,72
mvva 40,144,80
mvva 40,148,88
mvva 44,148,120
mvva 44,144,112
mvva 44,132,104
tv 148,230,28672
mvva 44,128,96
s_waitcnt vmcnt(4)
s_barrier
tv 144,230,24576
s_waitcnt lgkmcnt(2)
mvva 60,136,96
tv 132,230,20480
mvva 60,140,104
tv 128,230,16384
tv 130,230,18432
mvva 60,152,112
tv 134,230,22528
mvva 60,156,120
tv 146,230,26624
mvva 56,156,88
tv 150,230,30720
mvva 56,152,80
mvva 56,140,72
mvva 56,136,64
mvva 52,136,100
mvva 52,140,108
mvva 52,152,116
mvva 52,156,124
mvva 48,156,92
mvva 48,152,84
mvva 48,140,76
tv 156,231,28672
mvva 48,136,68
tv 152,231,24576
s_waitcnt lgkmcnt(2)
mvva 32,128,132
tv 140,231,20480
mvva 32,132,140
tv 136,231,16384
tv 138,231,18432
mvva 32,144,148
tv 142,231,22528
mvva 32,148,156
tv 154,231,26624
mvva 36,148,188
tv 158,231,30720
mvva 36,144,180
mvva 36,132,172
mvva 36,128,164
mvva 40,128,128
mvva 40,132,136
cv 32,32,33
cv 33,34,35
mvva 40,144,144
mvva 40,148,152
mvva 44,148,184
mvva 44,144,176
cv 36,36,37
cv 37,38,39
mvva 44,132,168
tv 148,224,45056
mvva 44,128,160
tv 144,224,40960
cv 40,40,41
s_waitcnt lgkmcnt(2)
mvva 60,136,160
tv 132,224,36864
cv 41,42,43
mvva 60,140,168
tv 128,224,32768
tv 130,224,34816
mvva 60,152,176
tv 134,224,38912
cv 44,44,45
mvva 60,156,184
tv 146,224,43008
cv 45,46,47
mvva 56,156,152
tv 150,224,47104
mvva 56,152,144
mvva 56,140,136
mvva 56,136,128
mvva 52,136,164
cv 46,60,61
cv 47,62,63
mvva 52,140,172
buffer_store_dwordx4 v[44:47],v244,s[8:11],s57 offen offset:3072 sc0 sc1
mvva 52,152,180
mvva 52,156,188
mvva 48,156,156
cv 42,56,57
cv 43,58,59
mvva 48,152,148
buffer_store_dwordx4 v[40:43],v244,s[8:11],s57 offen offset:2048 sc0 sc1
mvva 48,140,140
tv 156,225,45056
mvva 48,136,132
cv 38,52,53
cv 39,54,55
s_waitcnt lgkmcnt(1)
mvva 64,128,68
tv 140,225,36864
mvva 64,132,76
buffer_store_dwordx4 v[36:39],v244,s[8:11],s57 offen offset:1024 sc0 sc1
tv 136,225,32768
tv 138,225,34816
mvva 64,144,84
tv 142,225,38912
cv 34,48,49
mvva 64,148,92
tv 152,225,40960
tv 154,225,43008
mvva 68,148,124
tv 158,225,47104
cv 35,50,51
mvva 68,144,116
buffer_store_dwordx4 v[32:35],v244,s[8:11],s57 offen offset:0 sc0 sc1
mvva 68,132,108
mvva 68,128,100
mvva 72,128,64
mvva 72,132,72
mvva 72,144,80
mvva 72,148,88
mvva 76,148,120
mvva 76,144,112
mvva 76,132,104
tv 148,224,61440
mvva 76,128,96
tv 144,224,57344
s_waitcnt lgkmcnt(2)
mvva 92,136,96
tv 132,224,53248
mvva 92,140,104
tv 128,224,49152
tv 130,224,51200
mvva 92,152,112
tv 134,224,55296
mvva 92,156,120
tv 146,224,59392
mvva 88,156,88
tv 150,224,63488
mvva 88,152,80
mvva 88,140,72
mvva 88,136,64
mvva 84,136,100
mvva 84,140,108
mvva 84,152,116
mvva 84,156,124
mvva 80,156,92
mvva 80,152,84
mvva 80,140,76
tv 156,225,61440
mvva 80,136,68
tv 152,225,57344
s_waitcnt lgkmcnt(2)
mvva 64,128,132
tv 140,225,53248
mvva 64,132,140
tv 136,225,49152
tv 138,225,51200
mvva 64,144,148
tv 142,225,55296
mvva 64,148,156
tv 154,225,59392
mvva 68,148,188
tv 158,225,63488
mvva 68,144,180
mvva 68,132,172
mvva 68,128,164
s_xor_b32 s82,s59,12
mvva 72,128,128
s_mul_i32 s82,s82,12288
cv 64,64,65
s_add_u32 s82,s82,0
mvva 72,132,136
lv 16,244,82,0
mvva 72,144,144
cv 65,66,67
cv 68,68,69
mvva 72,148,152
cv 69,70,71
mvva 76,148,184
mvva 76,144,176
mvva 76,132,168
lv 20,244,82,1024
mvva 76,128,160
tv 144,234,24576
s_waitcnt lgkmcnt(1)
mvva 92,136,160
tv 132,234,20480
tv 134,234,22528
mvva 92,140,168
tv 128,234,16384
tv 130,234,18432
mvva 92,152,176
cv 72,72,73
cv 73,74,75
mvva 92,156,184
lv 24,244,82,2048
tv 146,234,26624
mvva 88,156,152
tv 148,234,28672
tv 150,234,30720
mvva 88,152,144
cv 76,76,77
cv 77,78,79
mvva 88,140,136
cv 78,92,93
cv 79,94,95
mvva 88,136,128
mvva 84,136,164
lv 28,244,82,3072
mvva 84,140,172
mvva 84,152,180
mvva 84,156,188
mvva 80,156,156
buffer_store_dwordx4 v[76:79],v244,s[8:11],s58 offen offset:3072 sc0 sc1
mvva 80,152,148
cv 74,88,89
cv 75,90,91
mvva 80,140,140
tv 156,235,28672
mvva 80,136,132
cv 70,84,85
cv 71,86,87
s_waitcnt lgkmcnt(1)
mvva 96,128,4
buffer_store_dwordx4 v[72:75],v244,s[8:11],s58 offen offset:2048 sc0 sc1
tv 140,235,20480
mvva 96,132,12
tv 136,235,16384
tv 138,235,18432
mvva 96,144,20
tv 142,235,22528
cv 66,80,81
mvva 96,148,28
tv 152,235,24576
tv 154,235,26624
mvva 100,148,60
tv 158,235,30720
cv 67,82,83
mvva 100,144,52
buffer_store_dwordx4 v[68:71],v244,s[8:11],s58 offen offset:1024 sc0 sc1
mvva 100,132,44
mvva 100,128,36
mvva 104,128,0
mvva 104,132,8
buffer_store_dwordx4 v[64:67],v244,s[8:11],s58 offen offset:0 sc0 sc1
mvva 104,144,16
mvva 104,148,24
mvva 108,148,56
mvva 108,144,48
mvva 108,132,40
tv 148,226,28672
mvva 108,128,32
tv 144,226,24576
s_waitcnt lgkmcnt(2)
mvva 124,136,32
tv 132,226,20480
mvva 124,140,40
tv 128,226,16384
tv 130,226,18432
mvva 124,152,48
tv 134,226,22528
mvva 124,156,56
tv 146,226,26624
mvva 120,156,24
tv 150,226,30720
mvva 120,152,16
mvva 120,140,8
mvva 120,136,0
mvva 116,136,36
mvva 116,140,44
s_waitcnt vmcnt(7)
v_max_u32_e32 v248,v16,v17
mvva 116,152,52
v_max3_u32 v248,v248,v18,v19
s_waitcnt vmcnt(6)
v_max3_u32 v248,v248,v20,v21
v_max3_u32 v248,v248,v22,v23
mvva 116,156,60
s_waitcnt vmcnt(5)
v_max3_u32 v248,v248,v24,v25
v_max3_u32 v248,v248,v26,v27
s_waitcnt vmcnt(4)
v_max3_u32 v248,v248,v28,v29
mvva 112,156,28
v_max3_u32 v248,v248,v30,v31
v_cmp_eq_u32_e32 vcc,-1,v248
)"
R"(mvva 112,152,20
s_cbranch_vccnz L_spx0e
L_sprx0e:
mvva 112,140,12
tv 156,227,28672
tv 158,227,30720
v_lshlrev_b32_e32 v251,16,v18
v_and_b32_e32 v255,4294901760,v18
mvva 112,136,4
tv 152,227,24576
tv 154,227,26624
s_waitcnt lgkmcnt(4)
mvvv 96,128,164
v_lshlrev_b32_e32 v253,16,v19
v_and_b32_e32 v242,4294901760,v19
mvvv 96,132,172
tv 136,227,16384
tv 138,227,18432
mvvv 96,144,180
tv 140,227,20480
tv 142,227,22528
v_add_f32_e32 v112,v112,v251
mvvv 96,148,188
v_add_f32_e32 v113,v113,v255
v_add_f32_e32 v114,v114,v253
mvvv 100,148,220
v_add_f32_e32 v115,v115,v242
v_lshlrev_b32_e32 v251,16,v22
mvvv 100,144,212
v_and_b32_e32 v255,4294901760,v22
v_lshlrev_b32_e32 v253,16,v23
mvvv 100,132,204
v_and_b32_e32 v242,4294901760,v23
v_add_f32_e32 v116,v116,v251
v_add_f32_e32 v117,v117,v255
mvvv 100,128,196
v_add_f32_e32 v118,v118,v253
v_add_f32_e32 v119,v119,v242
mvvv 104,128,160
v_lshlrev_b32_e32 v251,16,v26
v_and_b32_e32 v255,4294901760,v26
mvvv 104,132,168
v_lshlrev_b32_e32 v253,16,v27
v_and_b32_e32 v242,4294901760,v27
mvvv 104,144,176
v_add_f32_e32 v120,v120,v251
v_add_f32_e32 v121,v121,v255
v_add_f32_e32 v122,v122,v253
mvvv 104,148,184
v_add_f32_e32 v123,v123,v242
v_lshlrev_b32_e32 v251,16,v30
mvvv 108,148,216
v_and_b32_e32 v255,4294901760,v30
v_lshlrev_b32_e32 v253,16,v31
mvvv 108,144,208
v_and_b32_e32 v242,4294901760,v31
v_add_f32_e32 v124,v124,v251
mvvv 108,132,200
v_add_f32_e32 v125,v125,v255
v_add_f32_e32 v126,v126,v253
v_add_f32_e32 v127,v127,v242
mvvv 108,128,192
tv 144,230,57344
v_lshlrev_b32_e32 v251,16,v16
s_waitcnt lgkmcnt(1)
mvvv 124,136,192
tv 132,230,53248
v_and_b32_e32 v255,4294901760,v16
mvvv 124,140,200
tv 128,230,49152
tv 130,230,51200
mvvv 124,152,208
L_vs24:
s_cbranch_execz L_vr1
tv 134,230,55296
v_lshlrev_b32_e32 v253,16,v17
mvvv 124,156,216
tv 146,230,59392
v_and_b32_e32 v242,4294901760,v17
mvvv 120,156,184
tv 148,230,61440
tv 150,230,63488
mvvv 120,152,176
v_add_f32_e32 v96,v96,v251
v_add_f32_e32 v97,v97,v255
mvvv 120,140,168
v_add_f32_e32 v98,v98,v253
v_add_f32_e32 v99,v99,v242
mvvv 120,136,160
v_lshlrev_b32_e32 v251,16,v20
v_and_b32_e32 v255,4294901760,v20
mvvv 116,136,196
v_lshlrev_b32_e32 v253,16,v21
v_and_b32_e32 v242,4294901760,v21
mvvv 116,140,204
v_add_f32_e32 v100,v100,v251
v_add_f32_e32 v101,v101,v255
mvvv 116,152,212
v_add_f32_e32 v102,v102,v253
v_add_f32_e32 v103,v103,v242
mvvv 116,156,220
v_lshlrev_b32_e32 v251,16,v24
v_and_b32_e32 v255,4294901760,v24
mvvv 112,156,188
v_lshlrev_b32_e32 v253,16,v25
v_and_b32_e32 v242,4294901760,v25
mvvv 112,152,180
v_add_f32_e32 v104,v104,v251
v_add_f32_e32 v105,v105,v255
mvvv 112,140,172
tv 156,231,61440
v_add_f32_e32 v106,v106,v253
mvvv 112,136,164
tv 152,231,57344
v_add_f32_e32 v107,v107,v242
s_waitcnt lgkmcnt(2)
mvva 96,128,196
tv 140,231,53248
v_lshlrev_b32_e32 v251,16,v28
mvva 96,132,204
tv 136,231,49152
tv 138,231,51200
mvva 96,144,212
tv 142,231,55296
v_and_b32_e32 v255,4294901760,v28
mvva 96,148,220
tv 154,231,59392
v_lshlrev_b32_e32 v253,16,v29
mvva 100,148,252
tv 158,231,63488
v_and_b32_e32 v242,4294901760,v29
mvva 100,144,244
v_add_f32_e32 v108,v108,v251
v_add_f32_e32 v109,v109,v255
mvva 100,132,236
v_add_f32_e32 v110,v110,v253
v_add_f32_e32 v111,v111,v242
mvva 100,128,228
mvva 104,128,192
mvva 104,132,200
mvva 104,144,208
mvva 104,148,216
mvva 108,148,248
mvva 108,144,240
mvva 108,132,232
tv 148,226,45056
mvva 108,128,224
tv 144,226,40960
s_waitcnt lgkmcnt(2)
mvva 124,136,224
s_xor_b32 s83,s59,8
tv 132,226,36864
s_mul_i32 s83,s83,12288
mvva 124,140,232
s_add_u32 s83,s83,4096
tv 128,226,32768
tv 130,226,34816
mvva 124,152,240
lv 48,244,83,0
tv 134,226,38912
mvva 124,156,248
tv 146,226,43008
mvva 120,156,216
lv 52,244,83,1024
tv 150,226,47104
mvva 120,152,208
mvva 120,140,200
lv 56,244,83,2048
mvva 120,136,192
mvva 116,136,228
lv 60,244,83,3072
mvva 116,140,236
mvva 116,152,244
s_xor_b32 s84,s59,4
mvva 116,156,252
s_mul_i32 s84,s84,12288
mvva 112,156,220
s_add_u32 s84,s84,8192
mvva 112,152,212
lv 80,244,84,0
mvva 112,140,204
tv 156,227,45056
mvva 112,136,196
lv 84,244,84,1024
tv 152,227,40960
s_waitcnt lgkmcnt(2)
mvva 96,128,68
tv 140,227,36864
mvva 96,132,76
lv 88,244,84,2048
tv 136,227,32768
tv 138,227,34816
mvva 96,144,84
tv 142,227,38912
mvva 96,148,92
lv 92,244,84,3072
tv 154,227,43008
mvva 100,148,124
tv 158,227,47104
mvva 100,144,116
mvva 100,132,108
mvva 100,128,100
mvva 104,128,64
mvva 104,132,72
mvva 104,144,80
mvva 104,148,88
mvva 108,148,120
mvva 108,144,112
mvva 108,132,104
tv 148,226,61440
mvva 108,128,96
tv 144,226,57344
s_waitcnt lgkmcnt(2)
mvva 124,136,96
tv 132,226,53248
mvva 124,140,104
tv 128,226,49152
tv 130,226,51200
mvva 124,152,112
tv 134,226,55296
mvva 124,156,120
tv 146,226,59392
mvva 120,156,88
tv 150,226,63488
mvva 120,152,80
mvva 120,140,72
mvva 120,136,64
mvva 116,136,100
mvva 116,140,108
mvva 116,152,116
mvva 116,156,124
mvva 112,156,92
mvva 112,152,84
mvva 112,140,76
tv 156,227,61440
mvva 112,136,68
tv 152,227,57344
s_waitcnt lgkmcnt(2)
mvva 96,128,132
tv 140,227,53248
mvva 96,132,140
tv 136,227,49152
tv 138,227,51200
mvva 96,144,148
tv 142,227,55296
mvva 96,148,156
tv 154,227,59392
mvva 100,148,188
tv 158,227,63488
mvva 100,144,180
mvva 100,132,172
mvva 100,128,164
mvva 104,128,128
mvva 104,132,136
mvva 104,144,144
mvva 104,148,152
s_waitcnt vmcnt(7)
v_max_u32_e32 v248,v48,v49
mvva 108,148,184
v_max3_u32 v248,v248,v50,v51
s_waitcnt vmcnt(6)
v_max3_u32 v248,v248,v52,v53
mvva 108,144,176
v_max3_u32 v248,v248,v54,v55
s_waitcnt vmcnt(5)
v_max3_u32 v248,v248,v56,v57
mvva 108,132,168
v_max3_u32 v248,v248,v58,v59
s_waitcnt vmcnt(4)
v_max3_u32 v248,v248,v60,v61
mvva 108,128,160
v_max3_u32 v248,v248,v62,v63
v_cmp_eq_u32_e32 vcc,-1,v248
s_waitcnt lgkmcnt(0)
mvva 124,136,160
s_cbranch_vccnz L_spx1e
L_sprx1e:
mvva 124,140,168
mvva 124,152,176
mvva 124,156,184
s_waitcnt vmcnt(3)
v_max_u32_e32 v248,v80,v81
mvva 120,156,152
v_max3_u32 v248,v248,v82,v83
s_waitcnt vmcnt(2)
v_max3_u32 v248,v248,v84,v85
mvva 120,152,144
v_max3_u32 v248,v248,v86,v87
s_waitcnt vmcnt(1)
v_max3_u32 v248,v248,v88,v89
mvva 120,140,136
v_max3_u32 v248,v248,v90,v91
s_waitcnt vmcnt(0)
v_max3_u32 v248,v248,v92,v93
mvva 120,136,128
v_max3_u32 v248,v248,v94,v95
v_cmp_eq_u32_e32 vcc,-1,v248
mvva 116,136,164
s_cbranch_vccnz L_spx2e
L_sprx2e:
mvva 116,140,172
mvva 116,152,180
mvva 116,156,188
mvva 112,156,156
mvva 112,152,148
mvva 112,140,140
mvva 112,136,132
mvvv 108,238,60
mvvv 108,238,92
mvvv 124,236,60
mvvv 124,236,92
mvvv 104,238,56
mvvv 104,238,88
mvvv 120,236,56
mvvv 120,236,88
mvvv 100,238,52
mvvv 100,238,84
cv 108,108,109
cv 109,110,111
cv 110,124,125
cv 111,126,127
cv 104,104,105
v_permlane16_swap_b32_e32 v108,v110
mvvv 116,236,52
v_permlane16_swap_b32_e32 v109,v111
cv 105,106,107
cv 106,120,121
cv 107,122,123
mvvv 116,236,84
buffer_store_dwordx4 v[108:111],v243,s[44:47],s73 offen sc0 sc1
v_permlane16_swap_b32_e32 v104,v106
v_permlane16_swap_b32_e32 v105,v107
mvvv 96,238,48
cv 100,100,101
cv 101,102,103
mvvv 96,238,80
buffer_store_dwordx4 v[104:107],v243,s[44:47],s72 offen sc0 sc1
cv 102,116,117
cv 103,118,119
mvvv 112,236,48
v_permlane16_swap_b32_e32 v100,v102
v_permlane16_swap_b32_e32 v101,v103
mvvv 112,236,80
buffer_store_dwordx4 v[100:103],v243,s[44:47],s71 offen sc0 sc1
cv 96,96,97
cv 97,98,99
s_nop 4
cv 98,112,113
cv 99,114,115
s_nop 0
v_permlane16_swap_b32_e32 v96,v98
v_permlane16_swap_b32_e32 v97,v99
buffer_store_dwordx4 v[96:99],v243,s[44:47],s70 offen sc0 sc1
s_endpgm
L_odd:
L_vs25:
s_cbranch_execz L_vs29
s_lshl_b32 s86,s21,2
s_sub_u32 s87,s13,s67
s_add_u32 s86,s86,s12
mvaa 64,200,132
s_lshl_b32 s86,s86,8
v_add_u32_e32 v252,s87,v244
s_add_u32 s86,s86,L_odd-L_att_pc+1024
v_lshl_add_u32 v226,v244,3,s86
s_lshl_b32 s86,s12,1
s_bfe_u32 s87,s21,65539
mvaa 64,208,136
s_add_u32 s86,s86,s87
v_min_u32_e32 v226,L_att_end-L_att_pc-4,v226
s_lshl_b32 s86,s86,7
s_add_u32 s86,s86,L_pend2-L_att_pc
v_writelane_b32 v226,s86,2
mvaa 64,216,140
s_mov_b64 exec,7
global_load_dword v253,v226,s[96:97]
s_mov_b64 exec,-1
cvaa 68,220,140,232
s_add_u32 m0,s52,32768
v_and_b32_e32 v224,16,v244
v_lshlrev_b32_e32 v224,5,v224
mvaa 68,212,136
s_xor_b32 s51,s62,0
v_lshrrev_b32_e32 v239,4,v244
v_lshrrev_b32_e32 v236,3,v239
mvaa 68,204,132
s_lshl_b32 s87,s12,13
v_and_b32_e32 v237,112,v244
v_and_b32_e32 v238,48,v239
mvaa 68,196,128
s_xor_b32 s87,s87,8192
ra 64,225,0
v_lshlrev_b32_e32 v238,1,v238
cvaa 96,224,128,232
buffer_load_dword v253,v224,s[8:11],s53 offen sc1
mvaa 96,232,132
s_mov_b32 s84,7
ra 68,225,1024
v_xor_b32_e32 v237,v237,v238
mvaa 96,240,136
s_add_u32 s81,s81,1
ra 72,225,2048
v_lshl_add_u32 v245,v236,11,v237
mvaa 96,248,140
s_mov_b32 s84,5
v_add_u32_e32 v246,15360,v245
v_add_u32_e32 v249,30720,v245
cvaa 100,252,140,232
s_andn2_b32 s69,64,s54
ra 76,225,3072
s_waitcnt vmcnt(9)
v_add_u32_e32 v247,46080,v245
mvaa 100,244,136
dl 245,24,51,0
mvaa 100,236,132
s_add_u32 s88,s14,4096
ra 80,225,4096
cv 8,8,9
mvaa 100,228,128
s_lshl_b32 s35,s19,1
ra 84,225,5120
cv 9,10,11
cvaa 72,192,144,232
s_lshr_b32 s33,40569,s35
ra 88,225,6144
cv 10,12,13
mvaa 72,200,148
s_and_b32 s33,s33,3
ra 92,225,7168
v_add_u32_e32 v250,4096,v252
mvaa 72,208,152
dl 246,24,51,1024
mvaa 72,216,156
s_lshl_b32 s33,s33,13
ra 96,225,8192
v_mov_b32_e32 v251,4
cvaa 76,220,156,232
s_lshr_b32 s34,59294,s35
ra 100,225,9216
s_and_b32 s34,s34,3
mvaa 76,212,152
s_lshl_b32 s34,s34,13
ra 104,225,10240
s_xor_b32 s35,s33,s34
mvaa 76,204,148
s_and_b32 s98,s19,3
ra 108,225,11264
s_lshl_b32 s98,s98,13
mvaa 76,196,144
dl 249,24,51,2048
cvaa 104,224,144,232
s_mov_b32 s74,0x300003
ra 112,225,12288
s_mov_b32 s75,0x30000300
mvaa 104,232,148
s_lshl_b32 s71,s12,15
ra 116,225,13312
s_add_u32 s71,s71,s54
mvaa 104,240,152
s_mov_b64 s[44:45],s[30:31]
ra 120,225,14336
s_mov_b32 s46,4194304
mvaa 104,248,156
s_mov_b32 s47,131072
ex 14
cvaa 108,252,156,232
dl 247,24,51,3072
mvaa 108,244,152
s_add_u32 m0,s52,49152
s_xor_b32 s51,s63,0
ra 124,225,15360
mvaa 108,236,148
s_lshl_b32 s70,s23,11
ex 15
mvaa 108,228,144
s_add_u32 s70,s70,s71
ex 40
cvaa 80,192,160,232
ex 41
mvaa 80,200,164
dl 245,24,51,0
mvaa 80,208,168
ex 42
mvaa 80,216,172
ex 43
cvaa 84,220,172,232
ex 44
mvaa 84,212,168
ex 45
mvaa 84,204,164
dl 246,24,51,1024
mvaa 84,196,160
ex 46
cvaa 112,224,160,232
ex 47
mvaa 112,232,164
ex 16
mvaa 112,240,168
ex 17
mvaa 112,248,172
dl 249,24,51,2048
cvaa 116,252,172,232
ex 18
mvaa 116,244,168
ex 19
mvaa 116,236,164
ex 20
mvaa 116,228,160
ex 21
cvaa 88,192,176,232
dl 247,24,51,3072
s_waitcnt vmcnt(8)
v_cmp_ne_u32_e32 vcc,4,v253
mvaa 88,200,180
s_cbranch_vccnz L_spbo
L_sprbo:
ex 22
mvaa 88,208,184
s_xor_b32 s50,s66,262144
ex 23
mvaa 88,216,188
ex 48
cvaa 92,220,188,232
ex 49
mvaa 92,212,184
la 128,252,50,0
mvaa 92,204,180
s_waitcnt lgkmcnt(0)
s_barrier
mvaa 92,196,176
s_mov_b32 m0,s72
ex 50
cvaa 120,224,176,232
s_lshl_b32 s72,s22,11
ex 51
mvaa 120,232,180
s_add_u32 s72,s72,s71
ex 52
mvaa 120,240,184
ds 255,8,76,0
mvaa 120,248,188
s_add_u32 s71,s70,131072
ex 53
cvaa 124,252,188,232
ex 54
mvaa 124,244,184
ex 55
mvaa 124,236,180
ex 24
mvaa 124,228,176
ds 255,8,76,1024
maav 0,64,0
s_cmp_lg_u32 s19,7
s_cbranch_scc1 L_j7o
s_nop 1
s_lshl_b32 s86,s12,12
s_add_u32 s86,s86,131072
v_add_u32_e32 v236,s86,v244
ra 160,236,0
ra 164,236,1024
ra 168,236,2048
ra 172,236,3072
ra 176,236,16384
ra 180,236,17408
ra 184,236,18432
ra 188,236,19456
v_add_u32_e32 v224,s94,v224
s_waitcnt lgkmcnt(0)
L_j7o:
ex 25
maav 0,96,4
ex 26
maav 4,68,0
ex 27
maav 4,100,4
cv 11,14,15
cv 12,40,41
maav 8,72,0
ds 255,8,76,2048
maav 8,104,4
cv 13,42,43
cv 14,44,45
maav 12,76,0
cv 15,46,47
cv 16,16,17
maav 12,108,4
ex 28
v_smfmac_f32_16x16x64_bf16 v[144:147],v[228:231],v[0:7],v248
cv 17,18,19
maav 16,80,0
ds 255,8,76,3072
maav 16,112,4
s_mov_b32 m0,s73
ex 29
maav 20,84,0
s_add_u32 s73,s72,131072
ex 30
maav 20,116,4
ex 31
maav 24,88,0
ex 56
maav 24,120,4
ds 255,8,77,0
maav 28,92,0
ex 57
maav 28,124,4
ex 58
maav 32,64,8
ex 59
maav 32,96,12
ex 60
maav 36,68,8
ds 255,8,77,1024
maav 36,100,12
cv 18,20,21
cv 19,22,23
maav 40,72,8
cv 20,48,49
cv 21,50,51
maav 40,104,12
cv 22,52,53
cv 23,54,55
maav 44,76,8
ex 61
maav 44,108,12
ds 255,8,77,2048
v_smfmac_f32_16x16x64_bf16 v[148:151],v[228:231],v[8:15],v248
cv 24,24,25
maav 48,80,8
ex 62
maav 48,112,12
ex 63
maav 52,84,8
ra 192,244,0
cv 25,26,27
maav 52,116,12
ds 255,8,77,3072
maav 56,88,8
cv 26,28,29
cv 27,30,31
maav 56,120,12
cv 28,56,57
cv 29,58,59
maav 60,92,8
cv 30,60,61
cv 31,62,63
maav 60,124,12
ex 64
mvav 160,64,16
la 132,252,50,2048
mvav 160,96,20
ex 65
mvav 164,68,16
ex 66
mvav 164,100,20
ex 67
mvav 168,72,16
ex 68
mvav 168,104,20
la 136,250,50,0
mvav 172,76,16
ex 69
mvav 172,108,20
ex 70
v_smfmac_f32_16x16x64_bf16 v[152:155],v[228:231],v[16:23],v248
ra 200,244,2048
mvav 176,80,16
ra 208,244,4096
cv 64,64,65
mvav 176,112,20
L_vs26:
s_cbranch_execz L_vs30
la 140,250,50,2048
mvav 180,84,16
ra 216,244,6144
cv 65,66,67
mvav 180,116,20
ex 71
mvav 184,88,16
ex 96
mvav 184,120,20
s_barrier
ex 97
mvav 188,92,16
la 156,250,50,3072
mvav 188,124,20
s_add_u32 m0,s52,131072
ra 196,244,1024
s_xor_b32 s51,s62,1048576
mvav 192,64,24
ra 204,244,3072
cv 66,68,69
mvav 192,96,28
ra 212,244,5120
cv 67,70,71
mvav 196,68,24
ra 220,244,7168
cv 68,96,97
mvav 196,100,28
la 152,250,50,1024
mvav 200,72,24
ex 98
mvav 200,104,28
ex 99
mvav 204,76,24
ra 224,244,8192
cv 69,98,99
mvav 204,108,28
ra 232,244,10240
v_add_u32_e32 v6,s87,v244
v_smfmac_f32_16x16x64_bf16 v[156:159],v[228:231],v[24:31],v248
ra 240,244,12288
mvav 208,80,24
la 148,252,50,3072
mvav 208,112,28
s_xor_b32 s87,s52,4096
ra 248,244,14336
v_add_u32_e32 v7,98304,v6
mvav 212,84,24
s_add_u32 s87,s87,150528
ra 252,244,15360
v_add_u32_e32 v5,65536,v244
mvav 212,116,28
ra 244,244,13312
v_add_u32_e32 v32,s87,v244
mvav 216,88,24
ra 228,244,9216
v_and_b32_e32 v224,960,v244
mvav 216,120,28
la 144,252,50,1024
mvav 220,92,24
s_add_u32 s50,s50,s55
ra 236,244,11264
mvav 220,124,28
ra 64,225,32768
s_waitcnt lgkmcnt(8)
cvaa 24,192,176,232
dl 245,24,51,0
mvaa 24,200,180
ra 96,225,40960
mvaa 24,208,184
ra 68,225,33792
mvaa 24,216,188
dl 246,24,51,1024
cvaa 16,192,160,128
ra 100,225,41984
mvaa 16,200,164
ra 72,225,34816
mvaa 16,208,168
dl 249,24,51,2048
mvaa 16,216,172
ra 104,225,43008
cvaa 28,196,176,232
ra 76,225,35840
mvaa 28,204,180
dl 247,24,51,3072
mvaa 28,212,184
s_add_u32 m0,s52,147456
s_xor_b32 s51,s63,1048576
ra 108,225,44032
mvaa 28,220,188
ra 80,225,36864
cvaa 20,196,160,132
dl 245,24,51,0
mvaa 20,204,164
ra 112,225,45056
mvaa 20,212,168
ra 192,244,16384
mvaa 20,220,172
dl 246,24,51,1024
cvaa 56,224,176,232
ra 200,244,18432
s_waitcnt lgkmcnt(7)
mvaa 56,232,180
ra 208,244,20480
mvaa 56,240,184
dl 249,24,51,2048
mvaa 56,248,188
ra 216,244,22528
cvaa 48,224,160,136
ra 196,244,17408
mvaa 48,232,164
ra 204,244,19456
mvaa 48,240,168
ra 212,244,21504
mvaa 48,248,172
ra 220,244,23552
cvaa 60,228,176,232
ra 224,244,24576
mvaa 60,236,180
ra 232,244,26624
mvaa 60,244,184
ra 240,244,28672
mvaa 60,252,188
ra 248,244,30720
cvaa 52,228,160,140
ra 84,225,37888
mvaa 52,236,164
ra 116,225,46080
mvaa 52,244,168
la 160,252,50,0
ra 228,244,25600
mvaa 52,252,172
ra 236,244,27648
s_waitcnt lgkmcnt(10)
cvaa 8,192,176,128
ra 244,244,29696
mvaa 8,200,180
la 164,252,50,2048
ra 252,244,31744
mvaa 8,208,184
ra 88,225,38912
mvaa 8,216,188
ra 120,225,47104
cvaa 12,196,176,132
la 168,250,50,0
mvaa 12,204,180
ra 92,225,39936
ra 124,225,48128
s_waitcnt lgkmcnt(9)
mvaa 12,212,184
ex 100
mvaa 12,220,188
la 172,250,50,2048
cvaa 40,224,176,136
ex 101
mvaa 40,232,180
ex 102
mvaa 40,240,184
ex 103
mvaa 40,248,188
cv 70,100,101
cv 71,102,103
s_waitcnt lgkmcnt(4)
cvaa 44,228,176,140
ex 72
mvaa 44,236,180
s_waitcnt vmcnt(18) lgkmcnt(0)
s_barrier
ex 73
mvaa 44,244,184
la 176,252,50,1024
mvaa 44,252,188
ex 74
maav 0,64,64
ex 75
maav 0,96,68
la 188,250,50,3072
maav 4,68,64
ra 192,5,0
cv 72,72,73
maav 4,100,68
ra 196,5,1024
cv 73,74,75
maav 8,72,64
la 184,250,50,1024
ds_write_b128 v6,a[0:3] offset:0
maav 8,104,68
ex 76
maav 12,76,64
ex 77
maav 12,108,68
la 180,252,50,3072
ds_write_b128 v6,a[4:7] offset:1024
v_smfmac_f32_16x16x64_bf16 v[144:147],v[228:231],v[64:71],v248
cv 74,76,77
maav 16,80,64
ex 78
maav 16,112,68
ds_write_b128 v6,a[8:11] offset:2048
ex 79
maav 20,84,64
ex 104
maav 20,116,68
ds_write_b128 v6,a[12:15] offset:3072
ex 105
maav 24,88,64
ex 106
ex 107
ex 108
maav 24,120,68
ds_write_b128 v6,a[16:19] offset:4096
cv 75,78,79
maav 28,92,64
ex 109
ex 110
ex 111
maav 28,124,68
cv 76,104,105
cv 77,106,107
cv 78,108,109
cv 79,110,111
maav 32,64,72
ds_write_b128 v6,a[20:23] offset:5120
maav 32,96,76
ex 80
maav 36,68,72
ex 81
maav 36,100,76
ds_write_b128 v6,a[24:27] offset:6144
maav 40,72,72
ex 82
maav 40,104,76
ex 83
maav 44,76,72
ds_write_b128 v6,a[28:31] offset:7168
maav 44,108,76
cv 80,80,81
cv 81,82,83
v_smfmac_f32_16x16x64_bf16 v[148:151],v[228:231],v[72:79],v248
ex 84
maav 48,80,72
ds_write_b128 v7,a[32:35] offset:0
maav 48,112,76
ex 85
ex 86
ex 87
maav 52,84,72
ex 112
ex 113
maav 52,116,76
ds_write_b128 v7,a[36:39] offset:1024
cv 82,84,85
maav 56,88,72
ex 114
ex 115
ex 116
maav 56,120,76
ex 117
ex 118
ex 119
maav 60,92,72
ds_write_b128 v7,a[40:43] offset:2048
cv 83,86,87
maav 60,124,76
cv 84,112,113
cv 85,114,115
cv 86,116,117
cv 87,118,119
mvav 160,64,80
ex 88
mvav 160,96,84
ds_write_b128 v7,a[44:47] offset:3072
mvav 164,68,80
v_add_f32_e32 v130,v144,v145
v_add_f32_e32 v131,v148,v149
mvav 164,100,84
ex 89
mvav 168,72,80
ds_write_b128 v7,a[48:51] offset:4096
mvav 168,104,84
ex 90
mvav 172,76,80
ex 91
mvav 172,108,84
ds_write_b128 v32,v[128:131]
v_smfmac_f32_16x16x64_bf16 v[152:155],v[228:231],v[80:87],v248
ex 92
ex 93
mvav 176,80,80
ds_write_b128 v7,a[52:55] offset:5120
ex 94
mvav 176,112,84
ra 200,5,2048
ra 204,5,3072
ex 95
cv 88,88,89
mvav 180,84,80
ra 208,5,4096
ra 212,5,5120
cv 89,90,91
cv 90,92,93
ex 120
mvav 180,116,84
ds_write_b128 v7,a[56:59] offset:6144
ex 121
mvav 184,88,80
ex 122
ex 123
cv 91,94,95
mvav 184,120,84
ex 124
ex 125
mvav 188,92,80
ds_write_b128 v7,a[60:63] offset:7168
ex 126
mvav 188,124,84
ra 216,5,6144
ra 220,5,7168
ra 224,5,8192
ra 232,5,10240
ex 127
cv 92,120,121
mvav 192,64,88
ra 240,5,12288
ra 248,5,14336
ra 228,5,9216
ra 236,5,11264
ra 244,5,13312
ra 252,5,15360
)"
R"(cv 93,122,123
cv 94,124,125
cv 95,126,127
v_xor_b32_e32 v6,8192,v6
s_nop 0
mvav 192,96,92
ex 16
mvav 196,68,88
s_waitcnt lgkmcnt(10)
s_xor_b32 s87,s87,0
mvav 196,100,92
s_xor_b32 s50,s66,294912
s_barrier
v_xor_b32_e32 v7,8192,v7
mvav 200,72,88
la 4,252,50,0
ra 64,5,16384
ra 96,5,24576
rv 64,6,0
rv 68,6,1024
rv 72,6,2048
mvav 200,104,92
rv 76,6,3072
rv 80,6,4096
rv 84,6,5120
ex 17
ex 18
mvav 204,76,88
rv 96,7,0
rv 100,7,1024
ex 19
mvav 204,108,92
la 12,252,50,2048
rv 104,7,2048
rv 108,7,3072
cv 16,16,17
v_smfmac_f32_16x16x64_bf16 v[156:159],v[228:231],v[88:95],v248
rv 112,7,4096
rv 116,7,5120
ex 20
ex 21
mvav 208,80,88
rv 120,7,6144
rv 124,7,7168
ex 22
ex 23
mvav 208,112,92
la 20,250,50,0
ra 68,5,17408
ra 100,5,25600
cv 17,18,19
cv 18,20,21
mvav 212,84,88
ra 72,5,18432
ra 104,5,26624
ex 48
ex 49
mvav 212,116,92
ra 76,5,19456
ra 108,5,27648
ex 50
ex 51
mvav 216,88,88
la 28,250,50,2048
ra 80,5,20480
ra 112,5,28672
ra 84,5,21504
mvav 216,120,92
ra 116,5,29696
cv 19,22,23
ex 52
ex 53
mvav 220,92,88
ra 88,5,22528
cv 20,48,49
ex 54
ex 55
mvav 220,124,92
la 60,250,50,3072
ra 120,5,30720
cv 21,50,51
cv 22,52,53
cv 23,54,55
s_bitcmp1_b32 s82,s81
s_cbranch_scc1 L_pend2
L_ret_eb5:
mvav 160,192,16
ra 92,5,23552
rv 88,6,6144
ra 124,5,31744
rv 92,6,7168
ex 24
s_waitcnt lgkmcnt(10)
mvav 160,224,20
ex 25
ex 26
v_smfmac_f32_16x16x64_bf16 v[152:155],v[228:231],v[16:23],v248
ex 27
v_xor_b32_e32 v32,4096,v32
mvav 164,196,16
la 52,250,50,1024
ds_read_b128 v[236:239],v32
cv 24,24,25
ex 28
mvav 164,228,20
ex 29
cv 25,26,27
ex 30
ex 31
mvav 168,200,16
ex 56
ex 57
mvav 168,232,20
la 44,252,50,3072
cv 26,28,29
ex 58
ex 59
mvav 172,204,16
ex 60
ex 61
ex 62
mvav 172,236,20
s_waitcnt lgkmcnt(0)
s_barrier
cv 27,30,31
ex 63
mvav 176,208,16
la 36,252,50,1024
cv 28,56,57
cv 29,58,59
cv 30,60,61
cv 31,62,63
mvav 176,240,20
s_add_u32 m0,s52,147456
ex 8
s_xor_b32 s51,s63,1048576
ex 9
ex 10
mvav 180,212,16
s_add_u32 s50,s50,s55
ex 11
cv 8,8,9
cv 9,10,11
ex 12
mvav 180,244,20
dl 247,24,51,3072
ex 13
ex 14
mvav 184,216,16
s_add_u32 m0,s52,0
ex 15
ex 40
ex 41
mvav 184,248,20
s_xor_b32 s51,s64,0
ex 42
ex 43
ex 44
mvav 188,220,16
la 0,252,50,0
ex 45
ex 46
mvav 188,252,20
ex 47
cv 10,12,13
cv 11,14,15
cv 12,40,41
cv 13,42,43
cv 14,44,45
v_smfmac_f32_16x16x64_bf16 v[156:159],v[228:231],v[24:31],v248
cv 15,46,47
v_bfe_u32 v225,v244,1,5
s_nop 0
v_smfmac_f32_16x16x64_bf16 v[156:159],v[228:231],v[8:15],v248
v_lshl_add_u32 v224,v224,1,v225
v_bfe_u32 v225,v244,7,2
v_lshl_add_u32 v224,v225,5,v224
mvav 192,192,24
la 8,252,50,2048
v_xor_b32_e32 v224,s69,v224
v_add_f32_e32 v152,v152,v153
v_add_f32_e32 v137,v152,v238
v_subrev_u32_e32 v236,0x23800000,v137
mvav 192,224,28
v_rcp_f32_e32 v137,v137
mvav 192,64,8
tv 128,224,32768
v_mul_f32_e32 v232,v240,v137
v_mul_f32_e32 v64,v232,v64
v_mul_f32_e32 v65,v232,v65
v_mul_f32_e32 v66,v232,v66
mvav 192,96,12
la 16,250,50,0
v_mul_f32_e32 v67,v232,v67
v_mul_f32_e32 v68,v232,v68
v_mul_f32_e32 v69,v232,v69
v_mul_f32_e32 v70,v232,v70
mvav 196,196,24
v_fmac_f32_e32 v64,v232,v160
v_fmac_f32_e32 v65,v232,v161
v_mul_f32_e32 v71,v232,v71
mvav 196,228,28
v_fmac_f32_e32 v66,v232,v162
v_fmac_f32_e32 v67,v232,v163
v_fmac_f32_e32 v68,v232,v164
v_fmac_f32_e32 v69,v232,v165
v_fmac_f32_e32 v70,v232,v166
mvav 196,68,8
la 24,250,50,2048
v_fmac_f32_e32 v71,v232,v167
cv 160,64,65
cv 161,66,67
cv 162,68,69
cv 163,70,71
mvav 196,100,12
tv 130,224,34816
tv 132,224,36864
mvav 200,200,24
tv 134,224,38912
tv 144,224,40960
v_mul_f32_e32 v72,v232,v72
v_mul_f32_e32 v73,v232,v73
v_mul_f32_e32 v74,v232,v74
mvav 200,232,28
buffer_store_dwordx4 v[160:163],v244,s[8:11],s14 offen offset:0 sc0 sc1
v_mul_f32_e32 v75,v232,v75
v_mul_f32_e32 v76,v232,v76
v_mul_f32_e32 v77,v232,v77
v_mul_f32_e32 v78,v232,v78
mvav 200,72,8
v_fmac_f32_e32 v72,v232,v168
v_fmac_f32_e32 v73,v232,v169
v_mul_f32_e32 v79,v232,v79
mvav 200,104,12
v_fmac_f32_e32 v74,v232,v170
v_fmac_f32_e32 v75,v232,v171
v_fmac_f32_e32 v76,v232,v172
v_fmac_f32_e32 v77,v232,v173
v_fmac_f32_e32 v78,v232,v174
mvav 204,204,24
la 56,250,50,3072
v_fmac_f32_e32 v79,v232,v175
cv 168,72,73
cv 169,74,75
cv 170,76,77
cv 171,78,79
mvav 204,236,28
tv 146,224,43008
tv 148,224,45056
mvav 204,76,8
tv 150,224,47104
v_mul_f32_e32 v80,v232,v80
v_mul_f32_e32 v81,v232,v81
v_mul_f32_e32 v82,v232,v82
mvav 204,108,12
buffer_store_dwordx4 v[168:171],v244,s[8:11],s14 offen offset:2048 sc0 sc1
v_mul_f32_e32 v83,v232,v83
v_mul_f32_e32 v84,v232,v84
v_mul_f32_e32 v85,v232,v85
v_mul_f32_e32 v86,v232,v86
v_mul_f32_e32 v87,v232,v87
mvav 208,208,24
v_fmac_f32_e32 v80,v232,v176
v_fmac_f32_e32 v81,v232,v177
v_fmac_f32_e32 v82,v232,v178
mvav 208,240,28
v_fmac_f32_e32 v83,v232,v179
v_fmac_f32_e32 v84,v232,v180
v_fmac_f32_e32 v85,v232,v181
cv 176,80,81
cv 177,82,83
mvav 208,80,8
la 48,250,50,1024
v_fmac_f32_e32 v86,v232,v182
v_fmac_f32_e32 v87,v232,v183
cv 178,84,85
cv 179,86,87
mvav 208,112,12
L_vs27:
s_cbranch_execz L_vs31
v_mul_f32_e32 v88,v232,v88
v_mul_f32_e32 v89,v232,v89
v_mul_f32_e32 v90,v232,v90
mvav 212,212,24
v_mul_f32_e32 v91,v232,v91
v_mul_f32_e32 v92,v232,v92
v_mul_f32_e32 v93,v232,v93
v_mul_f32_e32 v94,v232,v94
v_mul_f32_e32 v95,v232,v95
mvav 212,244,28
buffer_store_dwordx4 v[176:179],v244,s[8:11],s88 offen offset:0 sc0 sc1
v_fmac_f32_e32 v88,v232,v184
v_fmac_f32_e32 v89,v232,v185
v_fmac_f32_e32 v90,v232,v186
v_fmac_f32_e32 v91,v232,v187
cv 184,88,89
mvav 212,84,8
v_fmac_f32_e32 v92,v232,v188
cv 185,90,91
mvav 212,116,12
v_fmac_f32_e32 v93,v232,v189
v_fmac_f32_e32 v94,v232,v190
v_fmac_f32_e32 v95,v232,v191
cv 186,92,93
cv 187,94,95
mvav 216,216,24
la 40,252,50,3072
v_add_f32_e32 v156,v156,v157
v_add_f32_e32 v141,v156,v239
v_subrev_u32_e32 v237,0x23800000,v141
v_rcp_f32_e32 v141,v141
mvav 216,248,28
v_mul_f32_e32 v233,v241,v141
v_mul_f32_e32 v96,v233,v96
mvav 216,88,8
v_mul_f32_e32 v97,v233,v97
v_mul_f32_e32 v98,v233,v98
v_mul_f32_e32 v99,v233,v99
v_mul_f32_e32 v100,v233,v100
mvav 216,120,12
buffer_store_dwordx4 v[184:187],v244,s[8:11],s88 offen offset:2048 sc0 sc1
v_fmac_f32_e32 v96,v233,v192
v_fmac_f32_e32 v97,v233,v193
v_mul_f32_e32 v101,v233,v101
v_mul_f32_e32 v102,v233,v102
v_mul_f32_e32 v103,v233,v103
mvav 220,220,24
v_fmac_f32_e32 v98,v233,v194
v_fmac_f32_e32 v99,v233,v195
mvav 220,252,28
v_fmac_f32_e32 v100,v233,v196
v_fmac_f32_e32 v101,v233,v197
cv 192,96,97
cv 193,98,99
mvav 220,92,8
la 32,252,50,1024
v_fmac_f32_e32 v102,v233,v198
v_fmac_f32_e32 v103,v233,v199
cv 194,100,101
cv 195,102,103
mvav 220,124,12
v_xor_b32_e32 v225,32,v224
v_xor_b32_e32 v226,64,v224
s_waitcnt lgkmcnt(0)
zvva 0,128,128
tv 138,225,34816
tv 142,225,38912
tv 152,225,40960
s_waitcnt vmcnt(36)
mvva 0,132,132
tv 136,225,32768
tv 154,225,43008
mvva 0,144,136
tv 140,225,36864
tv 158,225,47104
mvva 0,148,140
tv 156,225,45056
v_mul_f32_e32 v104,v233,v104
v_mul_f32_e32 v105,v233,v105
v_mul_f32_e32 v106,v233,v106
zvva 4,148,156
buffer_store_dwordx4 v[192:195],v244,s[8:11],s14 offen offset:1024 sc0 sc1
v_mul_f32_e32 v107,v233,v107
v_mul_f32_e32 v108,v233,v108
v_mul_f32_e32 v109,v233,v109
v_mul_f32_e32 v110,v233,v110
mvva 4,144,152
v_fmac_f32_e32 v104,v233,v200
v_fmac_f32_e32 v105,v233,v201
v_fmac_f32_e32 v106,v233,v202
v_mul_f32_e32 v111,v233,v111
mvva 4,132,148
v_fmac_f32_e32 v107,v233,v203
v_fmac_f32_e32 v108,v233,v204
v_fmac_f32_e32 v109,v233,v205
cv 200,104,105
cv 201,106,107
mvva 4,128,144
v_fmac_f32_e32 v110,v233,v206
v_fmac_f32_e32 v111,v233,v207
cv 202,108,109
cv 203,110,111
s_waitcnt vmcnt(16)
zvva 8,128,160
v_cmp_lt_u32_e64 s[42:43],s48,v236
v_mul_f32_e32 v112,v233,v112
s_or_b64 s[40:41],s[40:41],s[42:43]
v_cmp_lt_u32_e64 s[42:43],s48,v237
mvva 8,132,164
s_or_b64 s[40:41],s[40:41],s[42:43]
v_mul_f32_e32 v113,v233,v113
v_mul_f32_e32 v114,v233,v114
v_mul_f32_e32 v115,v233,v115
mvva 8,144,168
dl 245,36,51,0
v_mul_f32_e32 v116,v233,v116
v_mul_f32_e32 v117,v233,v117
v_mul_f32_e32 v118,v233,v118
v_mul_f32_e32 v119,v233,v119
v_mul_f32_e32 v120,v233,v120
mvva 8,148,172
v_fmac_f32_e32 v112,v233,v208
v_fmac_f32_e32 v113,v233,v209
v_fmac_f32_e32 v114,v233,v210
v_fmac_f32_e32 v115,v233,v211
zvva 12,148,188
v_fmac_f32_e32 v116,v233,v212
v_fmac_f32_e32 v117,v233,v213
cv 208,112,113
cv 209,114,115
mvva 12,144,184
v_fmac_f32_e32 v118,v233,v214
v_fmac_f32_e32 v119,v233,v215
cv 210,116,117
cv 211,118,119
mvva 12,132,180
v_mul_f32_e32 v121,v233,v121
v_mul_f32_e32 v122,v233,v122
v_mul_f32_e32 v123,v233,v123
mvva 12,128,176
v_mul_f32_e32 v124,v233,v124
v_mul_f32_e32 v125,v233,v125
v_mul_f32_e32 v126,v233,v126
v_mul_f32_e32 v127,v233,v127
s_waitcnt lgkmcnt(0)
zvva 28,136,176
buffer_store_dwordx4 v[200:203],v244,s[8:11],s14 offen offset:3072 sc0 sc1
tv 144,226,40960
v_fmac_f32_e32 v120,v233,v216
v_fmac_f32_e32 v121,v233,v217
v_fmac_f32_e32 v122,v233,v218
v_fmac_f32_e32 v123,v233,v219
mvva 28,140,180
tv 128,226,32768
tv 130,226,34816
cv 216,120,121
cv 217,122,123
mvva 28,152,184
tv 132,226,36864
tv 134,226,38912
v_fmac_f32_e32 v124,v233,v220
v_fmac_f32_e32 v125,v233,v221
v_fmac_f32_e32 v126,v233,v222
mvva 28,156,188
tv 146,226,43008
v_fmac_f32_e32 v127,v233,v223
cv 218,124,125
cv 219,126,127
zvva 24,156,172
tv 148,226,45056
tv 150,226,47104
mvva 24,152,168
v_xor_b32_e32 v227,96,v224
v_add_u32_e32 v232,131072,v224
mvva 24,140,164
buffer_store_dwordx4 v[208:211],v244,s[8:11],s88 offen offset:1024 sc0 sc1
mvva 24,136,160
v_add_u32_e32 v233,131072,v225
v_add_u32_e32 v234,131072,v226
zvva 20,136,144
v_add_u32_e32 v235,131072,v227
v_mov_b32_e32 v242,83914752
mvva 20,140,148
v_add_u32_e32 v228,65536,v224
v_add_u32_e32 v229,65536,v225
mvva 20,152,152
v_add_u32_e32 v230,65536,v226
v_add_u32_e32 v231,65536,v227
mvva 20,156,156
v_and_b32_e32 v243,240,v244
v_lshlrev_b32_e32 v243,7,v243
zvva 16,156,140
buffer_store_dwordx4 v[216:219],v244,s[8:11],s88 offen offset:3072 sc0 sc1
s_cmp_lg_u64 s[40:41],0
s_cbranch_scc1 L_redo
L_redo_r5:
mvva 16,152,136
tv 156,227,45056
v_mov_b32_e32 v236,16256
mvva 16,140,132
tv 158,227,47104
v_and_b32_e32 v248,256,v244
mvva 16,136,128
tv 152,227,40960
v_lshrrev_b32_e32 v248,3,v248
s_waitcnt lgkmcnt(3)
zvva 32,128,128
tv 140,227,36864
v_add_u32_e32 v243,v243,v248
mvva 32,132,132
tv 136,227,32768
tv 138,227,34816
mvva 32,144,136
dl 246,36,51,1024
tv 142,227,38912
mvva 32,148,140
tv 154,227,43008
v_and_b32_e32 v248,512,v244
zvva 36,148,156
v_lshrrev_b32_e32 v248,5,v248
v_add_u32_e32 v243,v243,v248
mvva 36,144,152
v_and_b32_e32 v248,16,v244
v_lshlrev_b32_e32 v248,v248,v236
mvva 36,132,148
v_cndmask_b32_e64 v238,0,v248,s[74:75]
s_mov_b32 s74,0xc0000c
mvva 36,128,144
s_mov_b32 s75,0xc0000c00
v_mov_b32_e32 v240,0
v_cndmask_b32_e64 v239,0,v248,s[74:75]
zvva 40,128,160
dl 249,36,51,2048
mvva 40,132,164
v_mov_b32_e32 v241,0
v_mov_b32_e32 v236,0
mvva 40,144,168
v_mov_b32_e32 v237,0
mvva 40,148,172
zvva 44,148,188
mvva 44,144,184
mvva 44,132,180
dl 247,36,51,3072
s_add_u32 m0,s52,16384
s_xor_b32 s51,s65,0
mvva 44,128,176
tv 144,224,57344
s_waitcnt lgkmcnt(1)
zvva 60,136,176
tv 132,224,53248
tv 134,224,55296
mvva 60,140,180
tv 128,224,49152
tv 130,224,51200
mvva 60,152,184
mvva 60,156,188
tv 146,224,59392
zvva 56,156,172
dl 245,36,51,0
tv 148,224,61440
tv 150,224,63488
mvva 56,152,168
mvva 56,140,164
mvva 56,136,160
zvva 52,136,144
mvva 52,140,148
mvva 52,152,152
dl 246,36,51,1024
mvva 52,156,156
zvva 48,156,140
mvva 48,152,136
mvva 48,140,132
tv 156,225,61440
mvva 48,136,128
tv 152,225,57344
s_waitcnt lgkmcnt(2)
zvva 64,128,128
dl 249,36,51,2048
tv 140,225,53248
mvva 64,132,132
tv 136,225,49152
tv 138,225,51200
mvva 64,144,136
tv 142,225,55296
mvva 64,148,140
tv 154,225,59392
zvva 68,148,156
tv 158,225,63488
mvva 68,144,152
mvva 68,132,148
dl 247,36,51,3072
s_lshl_b32 s51,s33,5
s_add_u32 m0,s52,98304
s_xor_b32 s51,s64,s51
mvva 68,128,144
zvva 72,128,160
mvva 72,132,164
s_waitcnt vmcnt(18)
s_mov_b32 s90,0
mvva 72,144,168
s_xor_b32 s50,s67,0
mvva 72,148,172
zvva 76,148,188
dl 245,36,51,0
mvva 76,144,184
mvva 76,132,180
tv 148,226,61440
mvva 76,128,176
tv 144,226,57344
s_waitcnt lgkmcnt(2)
zvva 92,136,176
tv 132,226,53248
mvva 92,140,180
tv 128,226,49152
tv 130,226,51200
mvva 92,152,184
dl 246,36,51,1024
tv 134,226,55296
mvva 92,156,188
tv 146,226,59392
zvva 88,156,172
tv 150,226,63488
mvva 88,152,168
mvva 88,140,164
mvva 88,136,160
zvva 84,136,144
dl 249,36,51,2048
mvva 84,140,148
mvva 84,152,152
mvva 84,156,156
zvva 80,156,140
mvva 80,152,136
mvva 80,140,132
dl 247,36,51,3072
mvva 80,136,128
s_lshl_b32 s51,s33,5
tv 152,227,57344
s_add_u32 m0,s52,114688
s_waitcnt lgkmcnt(1)
zvva 96,128,128
s_xor_b32 s51,s65,s51
tv 140,227,53248
tv 142,227,55296
mvva 96,132,132
tv 136,227,49152
tv 138,227,51200
mvva 96,144,136
mvva 96,148,140
tv 154,227,59392
zvva 100,148,156
lv 164,252,50,0
tv 156,227,61440
tv 158,227,63488
mvva 100,144,152
mvva 100,132,148
mvva 100,128,144
zvva 104,128,160
mvva 104,132,164
mvva 104,144,168
lv 172,252,50,2048
mvva 104,148,172
zvva 108,148,188
mvva 108,144,184
mvva 108,132,180
mvva 108,128,176
s_barrier
s_waitcnt lgkmcnt(0)
zvva 124,136,176
lv 180,250,50,0
tv 132,232,4096
tv 144,232,8192
tv 146,232,10240
mvva 124,140,180
tv 128,232,0
tv 130,232,2048
mvva 124,152,184
tv 134,232,6144
mvva 124,156,188
tv 148,232,12288
tv 150,232,14336
zvva 120,156,172
mvva 120,152,168
mvva 120,140,164
lv 188,250,50,2048
mvva 120,136,160
zvva 116,136,144
mvva 116,140,148
mvva 116,152,152
mvva 116,156,156
zvva 112,156,140
lv 220,250,50,3072
mvva 112,152,136
mvva 112,140,132
tv 156,233,12288
mvva 112,136,128
tv 152,233,8192
s_waitcnt lgkmcnt(2)
mvva 0,128,4
tv 140,233,4096
mvva 0,132,12
tv 136,233,0
tv 138,233,2048
mvva 0,144,20
lv 212,250,50,1024
tv 142,233,6144
mvva 0,148,28
tv 154,233,10240
mvva 4,148,60
tv 158,233,14336
mvva 4,144,52
mvva 4,132,44
mvva 4,128,36
mvva 8,128,0
lv 204,252,50,3072
mvva 8,132,8
mvva 8,144,16
mvva 8,148,24
mvva 12,148,56
s_waitcnt vmcnt(27)
mvva 12,144,48
s_waitcnt vmcnt(25)
mvva 12,132,40
lv 196,252,50,1024
s_waitcnt vmcnt(24)
mvva 12,128,32
tv 144,234,8192
s_waitcnt lgkmcnt(1)
mvva 28,136,32
tv 132,234,4096
tv 134,234,6144
mvva 28,140,40
tv 128,234,0
tv 130,234,2048
mvva 28,152,48
mvva 28,156,56
tv 146,234,10240
mvva 24,156,24
dl 245,36,51,0
s_waitcnt vmcnt(20)
s_barrier
tv 148,234,12288
tv 150,234,14336
mvva 24,152,16
mvva 24,140,8
mvva 24,136,0
mvva 20,136,36
mvva 20,140,44
mvva 20,152,52
s_cmp_eq_u32 s12,0
s_cselect_b64 exec,1,0
buffer_store_dword v251,off,s[8:11],s61 sc0 sc1
s_mov_b64 exec,-1
mvva 20,156,60
mvva 16,156,28
mvva 16,152,20
mvva 16,140,12
tv 156,235,12288
mvva 16,136,4
tv 152,235,8192
s_waitcnt lgkmcnt(2)
mvva 32,128,4
dl 246,36,51,1024
tv 140,235,4096
mvva 32,132,12
tv 136,235,0
tv 138,235,2048
mvva 32,144,20
tv 142,235,6144
mvva 32,148,28
tv 154,235,10240
mvva 36,148,60
tv 158,235,14336
mvva 36,144,52
mvva 36,132,44
dl 249,36,51,2048
mvva 36,128,36
mvva 40,128,0
mvva 40,132,8
mvva 40,144,16
mvva 40,148,24
mvva 44,148,56
dl 247,36,51,3072
s_lshl_b32 s51,s34,5
s_add_u32 m0,s52,65536
s_xor_b32 s51,s64,s51
mvva 44,144,48
tv 148,232,28672
mvva 44,132,40
s_xor_b32 s53,s60,s33
tv 150,232,30720
mvva 44,128,32
tv 144,232,24576
s_waitcnt lgkmcnt(3)
mvva 60,136,32
tv 132,232,20480
mvva 60,140,40
tv 128,232,16384
tv 130,232,18432
mvva 60,152,48
dl 245,36,51,0
tv 134,232,22528
mvva 60,156,56
tv 146,232,26624
mvva 56,156,24
mvva 56,152,16
mvva 56,140,8
mvva 56,136,0
mvva 52,136,36
buffer_load_dword v253,v242,s[8:11],s53 offen sc1
mvva 52,140,44
mvva 52,152,52
mvva 52,156,60
mvva 48,156,28
mvva 48,152,20
mvva 48,140,12
dl 246,36,51,1024
mvva 48,136,4
tv 152,233,24576
s_waitcnt lgkmcnt(1)
mvva 64,128,4
tv 140,233,20480
tv 142,233,22528
mvva 64,132,12
tv 136,233,16384
tv 138,233,18432
mvva 64,144,20
mvva 64,148,28
tv 154,233,26624
mvva 68,148,60
dl 249,36,51,2048
tv 156,233,28672
tv 158,233,30720
mvva 68,144,52
mvva 68,132,44
mvva 68,128,36
mvva 72,128,0
mvva 72,132,8
mvva 72,144,16
dl 247,36,51,3072
s_lshl_b32 s51,s34,5
s_add_u32 m0,s52,81920
s_xor_b32 s51,s65,s51
mvva 72,148,24
mvva 76,148,56
mvva 76,144,48
mvva 76,132,40
tv 148,234,28672
mvva 76,128,32
tv 144,234,24576
s_waitcnt lgkmcnt(2)
mvva 92,136,32
dl 245,36,51,0
tv 132,234,20480
mvva 92,140,40
tv 128,234,16384
tv 130,234,18432
mvva 92,152,48
tv 134,234,22528
mvva 92,156,56
tv 146,234,26624
mvva 88,156,24
tv 150,234,30720
mvva 88,152,16
mvva 88,140,8
dl 246,36,51,1024
mvva 88,136,0
mvva 84,136,36
mvva 84,140,44
mvva 84,152,52
mvva 84,156,60
mvva 80,156,28
dl 249,36,51,2048
mvva 80,152,20
mvva 80,140,12
tv 156,235,28672
mvva 80,136,4
tv 152,235,24576
s_waitcnt lgkmcnt(2)
mvva 96,128,4
tv 140,235,20480
mvva 96,132,12
L_vs28:
s_cbranch_execz L_vs17
tv 136,235,16384
tv 138,235,18432
mvva 96,144,20
dl 247,36,51,3072
s_lshl_b32 s51,s35,5
s_add_u32 m0,s52,32768
s_xor_b32 s51,s64,s51
tv 142,235,22528
mvva 96,148,28
tv 154,235,26624
mvva 100,148,60
tv 158,235,30720
mvva 100,144,52
mvva 100,132,44
mvva 100,128,36
mvva 104,128,0
dl 245,36,51,0
mvva 104,132,8
mvva 104,144,16
mvva 104,148,24
mvva 108,148,56
mvva 108,144,48
mvva 108,132,40
dl 246,36,51,1024
mvva 108,128,32
s_waitcnt vmcnt(27)
s_barrier
s_waitcnt lgkmcnt(0)
mvva 124,136,32
tv 132,224,4096
tv 134,224,6144
tv 144,224,8192
mvva 124,140,40
tv 128,224,0
tv 130,224,2048
mvva 124,152,48
mvva 124,156,56
tv 146,224,10240
mvva 120,156,24
dl 249,36,51,2048
tv 148,224,12288
tv 150,224,14336
mvva 120,152,16
mvva 120,140,8
mvva 120,136,0
mvva 116,136,36
mvva 116,140,44
mvva 116,152,52
dl 247,36,51,3072
s_lshl_b32 s51,s35,5
s_add_u32 m0,s52,49152
s_xor_b32 s51,s65,s51
mvva 116,156,60
mvva 112,156,28
mvva 112,152,20
s_waitcnt vmcnt(11)
v_cmp_ne_u32_e32 vcc,4,v253
mvva 112,140,12
dl 245,36,51,0
s_cbranch_vccnz L_spa3o
L_spra3o:
mvva 112,136,4
s_xor_b32 s50,s67,s33
tv 152,225,8192
s_waitcnt lgkmcnt(1)
mvvv 0,128,164
tv 140,225,4096
tv 142,225,6144
mvvv 0,132,172
tv 136,225,0
tv 138,225,2048
mvvv 0,144,180
dl 246,36,51,1024
mvvv 0,148,188
tv 154,225,10240
mvvv 4,148,220
tv 156,225,12288
tv 158,225,14336
mvvv 4,144,212
mvvv 4,132,204
la 196,252,50,0
mvvv 4,128,196
mvvv 8,128,160
s_xor_b32 s53,s60,s34
mvvv 8,132,168
mvvv 8,144,176
la 204,252,50,2048
mvvv 8,148,184
mvvv 12,148,216
mvvv 12,144,208
mvvv 12,132,200
buffer_load_dword v253,v242,s[8:11],s53 offen sc1
mvvv 12,128,192
tv 144,226,8192
s_waitcnt lgkmcnt(1)
mvvv 28,136,192
tv 132,226,4096
tv 134,226,6144
mvvv 28,140,200
tv 128,226,0
tv 130,226,2048
mvvv 28,152,208
la 212,250,50,0
mvvv 28,156,216
tv 146,226,10240
mvvv 24,156,184
tv 148,226,12288
tv 150,226,14336
mvvv 24,152,176
mvvv 24,140,168
mvvv 24,136,160
la 220,250,50,2048
mvvv 20,136,196
mvvv 20,140,204
mvvv 20,152,212
mvvv 20,156,220
la 252,250,50,3072
mvvv 16,156,188
mvvv 16,152,180
mvvv 16,140,172
tv 156,227,12288
mvvv 16,136,164
la 244,250,50,1024
tv 152,227,8192
s_waitcnt lgkmcnt(2)
mvvv 32,128,164
tv 140,227,4096
mvvv 32,132,172
tv 136,227,0
tv 138,227,2048
mvvv 32,144,180
tv 142,227,6144
mvvv 32,148,188
la 236,252,50,3072
tv 154,227,10240
mvvv 36,148,220
tv 158,227,14336
)"
R"(mvvv 36,144,212
mvvv 36,132,204
mvvv 36,128,196
la 228,252,50,1024
s_add_u32 s50,s50,s55
mvvv 40,128,160
mvvv 40,132,168
mvvv 40,144,176
mvvv 40,148,184
mvvv 44,148,216
la 192,252,50,0
mvvv 44,144,208
mvvv 44,132,200
tv 148,224,28672
tv 150,224,30720
mvvv 44,128,192
s_waitcnt lgkmcnt(2)
mvvv 60,136,192
la 200,252,50,2048
tv 132,224,20480
mvvv 60,140,200
tv 128,224,16384
tv 130,224,18432
mvvv 60,152,208
tv 134,224,22528
mvvv 60,156,216
tv 144,224,24576
tv 146,224,26624
mvvv 56,156,184
la 208,250,50,0
mvvv 56,152,176
mvvv 56,140,168
mvvv 56,136,160
mvvv 52,136,196
la 216,250,50,2048
mvvv 52,140,204
mvvv 52,152,212
mvvv 52,156,220
mvvv 48,156,188
la 248,250,50,3072
mvvv 48,152,180
mvvv 48,140,172
tv 156,225,28672
mvvv 48,136,164
tv 152,225,24576
s_waitcnt lgkmcnt(2)
mvvv 64,128,164
tv 140,225,20480
mvvv 64,132,172
la 240,250,50,1024
tv 136,225,16384
tv 138,225,18432
mvvv 64,144,180
tv 142,225,22528
mvvv 64,148,188
tv 154,225,26624
mvvv 68,148,220
tv 158,225,30720
mvvv 68,144,212
la 232,252,50,3072
mvvv 68,132,204
mvvv 68,128,196
mvvv 72,128,160
mvvv 72,132,168
la 224,252,50,1024
mvvv 72,144,176
mvvv 72,148,184
mvvv 76,148,216
mvvv 76,144,208
mvvv 76,132,200
dl 249,36,51,2048
mvvv 76,128,192
tv 144,226,24576
s_waitcnt lgkmcnt(1)
mvvv 92,136,192
tv 132,226,20480
tv 134,226,22528
mvvv 92,140,200
tv 128,226,16384
tv 130,226,18432
mvvv 92,152,208
dl 247,36,51,3072
s_lshl_b32 s51,s98,5
s_xor_b32 s51,s64,s51
s_add_u32 m0,s52,131072
s_xor_b32 s51,s51,4194304
mvvv 92,156,216
tv 146,226,26624
mvvv 88,156,184
tv 148,226,28672
tv 150,226,30720
mvvv 88,152,176
mvvv 88,140,168
dl 245,36,51,0
mvvv 88,136,160
s_waitcnt vmcnt(17)
v_cmp_ne_u32_e32 vcc,4,v253
mvvv 84,136,196
s_cbranch_vccnz L_spa4o
L_spra4o:
mvvv 84,140,204
s_xor_b32 s50,s67,s34
mvvv 84,152,212
dl 246,36,51,1024
mvvv 84,156,220
mvvv 80,156,188
mvvv 80,152,180
mvvv 80,140,172
la 68,252,50,0
mvvv 80,136,164
tv 152,227,24576
s_waitcnt lgkmcnt(1)
mvvv 96,128,164
tv 140,227,20480
tv 142,227,22528
mvvv 96,132,172
tv 136,227,16384
tv 138,227,18432
mvvv 96,144,180
s_xor_b32 s53,s60,s35
mvvv 96,148,188
la 76,252,50,2048
tv 154,227,26624
mvvv 100,148,220
tv 156,227,28672
tv 158,227,30720
mvvv 100,144,212
mvvv 100,132,204
mvvv 100,128,196
la 84,250,50,0
mvvv 104,128,160
mvvv 104,132,168
mvvv 104,144,176
mvvv 104,148,184
buffer_load_dword v253,v242,s[8:11],s53 offen sc1
mvvv 108,148,216
mvvv 108,144,208
mvvv 108,132,200
mvvv 108,128,192
s_barrier
s_waitcnt lgkmcnt(0)
mvvv 124,136,192
la 92,250,50,2048
tv 132,228,36864
tv 144,228,40960
tv 146,228,43008
mvvv 124,140,200
tv 128,228,32768
tv 130,228,34816
mvvv 124,152,208
tv 134,228,38912
mvvv 124,156,216
tv 148,228,45056
tv 150,228,47104
mvvv 120,156,184
la 124,250,50,3072
mvvv 120,152,176
mvvv 120,140,168
mvvv 120,136,160
mvvv 116,136,196
la 116,250,50,1024
mvvv 116,140,204
mvvv 116,152,212
mvvv 116,156,220
mvvv 112,156,188
la 108,252,50,3072
mvvv 112,152,180
mvvv 112,140,172
tv 156,229,45056
tv 158,229,47104
mvvv 112,136,164
s_waitcnt lgkmcnt(2)
mvva 0,128,196
la 100,252,50,1024
s_add_u32 s50,s50,s55
tv 140,229,36864
mvva 0,132,204
tv 136,229,32768
tv 138,229,34816
s_waitcnt vmcnt(21)
mvva 0,144,212
tv 142,229,38912
mvva 0,148,220
tv 152,229,40960
tv 154,229,43008
mvva 4,148,252
mvva 4,144,244
la 64,252,50,0
mvva 4,132,236
mvva 4,128,228
s_waitcnt vmcnt(21)
mvva 8,128,192
s_waitcnt vmcnt(20)
mvva 8,132,200
la 72,252,50,2048
s_waitcnt vmcnt(20)
mvva 8,144,208
s_waitcnt vmcnt(19)
mvva 8,148,216
s_waitcnt vmcnt(18)
mvva 12,148,248
s_waitcnt vmcnt(17)
mvva 12,144,240
la 80,250,50,0
s_waitcnt vmcnt(17)
mvva 12,132,232
tv 148,230,45056
s_waitcnt vmcnt(16)
mvva 12,128,224
tv 144,230,40960
s_waitcnt lgkmcnt(2)
mvva 28,136,224
tv 132,230,36864
mvva 28,140,232
la 88,250,50,2048
tv 128,230,32768
tv 130,230,34816
mvva 28,152,240
tv 134,230,38912
mvva 28,156,248
tv 146,230,43008
mvva 24,156,216
tv 150,230,47104
mvva 24,152,208
la 120,250,50,3072
mvva 24,140,200
mvva 24,136,192
mvva 20,136,228
mvva 20,140,236
mvva 20,152,244
la 112,250,50,1024
mvva 20,156,252
mvva 16,156,220
mvva 16,152,212
mvva 16,140,204
la 104,252,50,3072
mvva 16,136,196
tv 152,231,40960
s_waitcnt lgkmcnt(1)
mvva 32,128,196
tv 140,231,36864
tv 142,231,38912
mvva 32,132,204
tv 136,231,32768
tv 138,231,34816
mvva 32,144,212
la 96,252,50,1024
mvva 32,148,220
tv 154,231,43008
mvva 36,148,252
tv 156,231,45056
tv 158,231,47104
mvva 36,144,244
mvva 36,132,236
mvva 36,128,228
dl 249,36,51,2048
mvva 40,128,192
mvva 40,132,200
mvva 40,144,208
mvva 40,148,216
dl 247,36,51,3072
s_lshl_b32 s51,s98,5
s_xor_b32 s51,s65,s51
s_add_u32 m0,s52,147456
s_xor_b32 s51,s51,4194304
mvva 44,148,248
mvva 44,144,240
mvva 44,132,232
tv 148,228,61440
mvva 44,128,224
dl 245,36,51,0
tv 144,228,57344
s_waitcnt lgkmcnt(2)
mvva 60,136,224
tv 132,228,53248
mvva 60,140,232
tv 128,228,49152
tv 130,228,51200
s_waitcnt vmcnt(16)
v_cmp_ne_u32_e32 vcc,4,v253
mvva 60,152,240
s_cbranch_vccnz L_spa5o
L_spra5o:
tv 134,228,55296
s_xor_b32 s50,s67,s35
mvva 60,156,248
dl 246,36,51,1024
tv 146,228,59392
mvva 56,156,216
tv 150,228,63488
mvva 56,152,208
mvva 56,140,200
mvva 56,136,192
la 132,252,50,0
mvva 52,136,228
mvva 52,140,236
mvva 52,152,244
mvva 52,156,252
mvva 48,156,220
la 140,252,50,2048
s_xor_b32 s53,s60,s98
s_xor_b32 s53,s53,512
mvva 48,152,212
tv 156,229,61440
mvva 48,140,204
tv 158,229,63488
mvva 48,136,196
tv 152,229,57344
s_waitcnt lgkmcnt(3)
mvva 64,128,196
la 148,250,50,0
tv 140,229,53248
mvva 64,132,204
tv 136,229,49152
tv 138,229,51200
mvva 64,144,212
tv 142,229,55296
mvva 64,148,220
tv 154,229,59392
mvva 68,148,252
buffer_load_dword v253,v242,s[8:11],s53 offen sc1
mvva 68,144,244
mvva 68,132,236
mvva 68,128,228
mvva 72,128,192
mvva 72,132,200
la 156,250,50,2048
mvva 72,144,208
mvva 72,148,216
mvva 76,148,248
mvva 76,144,240
la 188,250,50,3072
mvva 76,132,232
tv 148,230,61440
mvva 76,128,224
tv 144,230,57344
s_waitcnt lgkmcnt(2)
mvva 92,136,224
tv 132,230,53248
mvva 92,140,232
la 180,250,50,1024
tv 128,230,49152
tv 130,230,51200
mvva 92,152,240
tv 134,230,55296
mvva 92,156,248
tv 146,230,59392
mvva 88,156,216
tv 150,230,63488
mvva 88,152,208
la 172,252,50,3072
mvva 88,140,200
mvva 88,136,192
mvva 84,136,228
mvva 84,140,236
la 164,252,50,1024
s_add_u32 s50,s50,s55
mvva 84,152,244
mvva 84,156,252
mvva 80,156,220
mvva 80,152,212
mvva 80,140,204
la 128,252,50,0
mvva 80,136,196
tv 152,231,57344
s_waitcnt lgkmcnt(1)
mvva 96,128,196
tv 140,231,53248
tv 142,231,55296
mvva 96,132,204
tv 136,231,49152
tv 138,231,51200
mvva 96,144,212
la 136,252,50,2048
mvva 96,148,220
tv 154,231,59392
mvva 100,148,252
tv 156,231,61440
tv 158,231,63488
mvva 100,144,244
mvva 100,132,236
la 144,250,50,0
mvva 100,128,228
mvva 104,128,192
mvva 104,132,200
mvva 104,144,208
la 152,250,50,2048
mvva 104,148,216
mvva 108,148,248
mvva 108,144,240
mvva 108,132,232
la 184,250,50,3072
mvva 108,128,224
s_barrier
s_waitcnt lgkmcnt(0)
mvva 124,136,224
tv 132,228,4096
tv 134,228,6144
tv 144,228,8192
mvva 124,140,232
tv 128,228,0
tv 130,228,2048
mvva 124,152,240
mvva 124,156,248
la 176,250,50,1024
tv 146,228,10240
mvva 120,156,216
L_vs29:
s_cbranch_execz L_vr1
tv 148,228,12288
tv 150,228,14336
mvva 120,152,208
mvva 120,140,200
mvva 120,136,192
la 168,252,50,3072
mvva 116,136,228
mvva 116,140,236
mvva 116,152,244
mvva 116,156,252
la 160,252,50,1024
mvva 112,156,220
mvva 112,152,212
mvva 112,140,204
tv 156,229,12288
tv 158,229,14336
mvva 112,136,196
s_waitcnt lgkmcnt(2)
mvva 0,128,68
dl 249,36,51,2048
tv 140,229,4096
mvva 0,132,76
tv 136,229,0
tv 138,229,2048
mvva 0,144,84
tv 142,229,6144
s_waitcnt vmcnt(22)
mvva 0,148,92
tv 152,229,8192
tv 154,229,10240
mvva 4,148,124
dl 247,36,51,3072
s_lshl_b32 s51,s98,5
s_xor_b32 s51,s64,s51
s_add_u32 m0,s52,0
s_xor_b32 s51,s51,4456448
mvva 4,144,116
mvva 4,132,108
mvva 4,128,100
mvva 8,128,64
dl 245,36,51,0
mvva 8,132,72
mvva 8,144,80
mvva 8,148,88
s_waitcnt vmcnt(16)
v_cmp_ne_u32_e32 vcc,4,v253
mvva 12,148,120
dl 246,36,51,1024
s_cbranch_vccnz L_spa6o
L_spra6o:
mvva 12,144,112
s_xor_b32 s50,s67,s98
mvva 12,132,104
s_xor_b32 s50,s50,131072
tv 148,230,12288
tv 150,230,14336
mvva 12,128,96
s_waitcnt lgkmcnt(2)
mvva 28,136,96
dl 249,36,51,2048
tv 132,230,4096
mvva 28,140,104
tv 128,230,0
tv 130,230,2048
mvva 28,152,112
tv 134,230,6144
mvva 28,156,120
tv 144,230,8192
tv 146,230,10240
mvva 24,156,88
mvva 24,152,80
la 4,252,50,0
mvva 24,140,72
s_xor_b32 s53,s60,s98
mvva 24,136,64
s_xor_b32 s53,s53,8704
mvva 20,136,100
mvva 20,140,108
la 12,252,50,2048
mvva 20,152,116
mvva 20,156,124
mvva 16,156,92
mvva 16,152,84
buffer_load_dword v253,v242,s[8:11],s53 offen sc1
mvva 16,140,76
tv 156,231,12288
mvva 16,136,68
tv 152,231,8192
s_waitcnt lgkmcnt(2)
mvva 32,128,68
tv 140,231,4096
mvva 32,132,76
la 20,250,50,0
tv 136,231,0
tv 138,231,2048
mvva 32,144,84
tv 142,231,6144
mvva 32,148,92
tv 154,231,10240
mvva 36,148,124
tv 158,231,14336
mvva 36,144,116
la 28,250,50,2048
mvva 36,132,108
mvva 36,128,100
mvva 40,128,64
mvva 40,132,72
mvva 40,144,80
la 60,250,50,3072
mvva 40,148,88
mvva 44,148,120
mvva 44,144,112
mvva 44,132,104
la 52,250,50,1024
mvva 44,128,96
tv 144,228,24576
s_waitcnt lgkmcnt(1)
mvva 60,136,96
tv 132,228,20480
tv 134,228,22528
mvva 60,140,104
tv 128,228,16384
tv 130,228,18432
mvva 60,152,112
la 44,252,50,3072
mvva 60,156,120
tv 146,228,26624
mvva 56,156,88
tv 148,228,28672
tv 150,228,30720
mvva 56,152,80
mvva 56,140,72
mvva 56,136,64
la 36,252,50,1024
s_add_u32 s50,s50,s55
mvva 52,136,100
mvva 52,140,108
mvva 52,152,116
mvva 52,156,124
la 0,252,50,0
mvva 48,156,92
mvva 48,152,84
mvva 48,140,76
tv 156,229,28672
mvva 48,136,68
la 8,252,50,2048
tv 152,229,24576
s_waitcnt lgkmcnt(2)
mvva 64,128,68
tv 140,229,20480
mvva 64,132,76
tv 136,229,16384
tv 138,229,18432
mvva 64,144,84
tv 142,229,22528
mvva 64,148,92
la 16,250,50,0
tv 154,229,26624
mvva 68,148,124
tv 158,229,30720
mvva 68,144,116
mvva 68,132,108
mvva 68,128,100
la 24,250,50,2048
mvva 72,128,64
mvva 72,132,72
mvva 72,144,80
mvva 72,148,88
mvva 76,148,120
la 56,250,50,3072
mvva 76,144,112
mvva 76,132,104
tv 148,224,45056
tv 150,224,47104
mvva 76,128,96
s_waitcnt lgkmcnt(2)
mvva 92,136,96
la 48,250,50,1024
tv 132,224,36864
mvva 92,140,104
tv 128,224,32768
tv 130,224,34816
mvva 92,152,112
tv 134,224,38912
mvva 92,156,120
tv 144,224,40960
tv 146,224,43008
mvva 88,156,88
la 40,252,50,3072
mvva 88,152,80
mvva 88,140,72
mvva 88,136,64
mvva 84,136,100
mvva 84,140,108
la 32,252,50,1024
mvva 84,152,116
mvva 84,156,124
mvva 80,156,92
mvva 80,152,84
dl 247,36,51,3072
s_lshl_b32 s51,s98,5
s_xor_b32 s51,s64,s51
s_add_u32 m0,s52,16384
s_xor_b32 s51,s51,4718592
mvva 80,140,76
tv 152,225,40960
mvva 80,136,68
tv 154,225,43008
s_waitcnt lgkmcnt(2)
mvva 0,128,132
tv 140,225,36864
mvva 0,132,140
dl 245,36,51,0
tv 136,225,32768
tv 138,225,34816
mvva 0,144,148
tv 142,225,38912
s_waitcnt vmcnt(24)
mvva 0,148,156
mvva 4,148,188
tv 156,225,45056
tv 158,225,47104
mvva 4,144,180
dl 246,36,51,1024
s_waitcnt vmcnt(17)
v_cmp_ne_u32_e32 vcc,4,v253
mvva 4,132,172
s_cbranch_vccnz L_spa7o
L_spra7o:
mvva 4,128,164
s_xor_b32 s50,s67,s98
mvva 8,128,128
s_xor_b32 s50,s50,139264
mvva 8,132,136
dl 249,36,51,2048
mvva 8,144,144
mvva 8,148,152
mvva 12,148,184
mvva 12,144,176
mvva 12,132,168
dl 247,36,51,3072
mvva 12,128,160
s_barrier
tv 144,226,40960
s_lshl_b32 s51,s98,5
s_waitcnt lgkmcnt(1)
mvva 28,136,160
s_xor_b32 s53,s60,s98
tv 132,226,36864
tv 134,226,38912
s_xor_b32 s53,s53,16896
mvva 28,140,168
s_xor_b32 s51,s65,s51
tv 128,226,32768
tv 130,226,34816
mvva 28,152,176
lv 164,252,50,0
mvva 28,156,184
s_add_u32 m0,s52,98304
tv 146,226,43008
s_xor_b32 s51,s51,4456448
mvva 24,156,152
tv 148,226,45056
tv 150,226,47104
mvva 24,152,144
mvva 24,140,136
buffer_load_dword v253,v242,s[8:11],s53 offen sc1
mvva 24,136,128
mvva 20,136,164
mvva 20,140,172
mvva 20,152,180
lv 172,252,50,2048
mvva 20,156,188
mvva 16,156,156
mvva 16,152,148
mvva 16,140,140
lv 180,250,50,0
mvva 16,136,132
tv 152,227,40960
s_waitcnt lgkmcnt(1)
mvva 32,128,132
tv 140,227,36864
tv 142,227,38912
mvva 32,132,140
tv 136,227,32768
tv 138,227,34816
mvva 32,144,148
mvva 32,148,156
lv 188,250,50,2048
tv 154,227,43008
mvva 36,148,188
tv 156,227,45056
tv 158,227,47104
mvva 36,144,180
mvva 36,132,172
mvva 36,128,164
lv 220,250,50,3072
mvva 40,128,128
mvva 40,132,136
mvva 40,144,144
mvva 40,148,152
lv 212,250,50,1024
mvva 44,148,184
mvva 44,144,176
mvva 44,132,168
tv 148,224,61440
tv 150,224,63488
mvva 44,128,160
s_waitcnt lgkmcnt(2)
mvva 60,136,160
lv 204,252,50,3072
tv 132,224,53248
mvva 60,140,168
tv 128,224,49152
tv 130,224,51200
mvva 60,152,176
tv 134,224,55296
mvva 60,156,184
tv 144,224,57344
tv 146,224,59392
mvva 56,156,152
lv 196,252,50,1024
s_add_u32 s50,s50,s55
mvva 56,152,144
mvva 56,140,136
mvva 56,136,128
mvva 52,136,164
lv 160,252,50,0
mvva 52,140,172
mvva 52,152,180
mvva 52,156,188
mvva 48,156,156
lv 168,252,50,2048
mvva 48,152,148
mvva 48,140,140
tv 156,225,61440
tv 158,225,63488
mvva 48,136,132
s_waitcnt lgkmcnt(2)
mvva 64,128,132
lv 176,250,50,0
tv 140,225,53248
mvva 64,132,140
tv 136,225,49152
tv 138,225,51200
mvva 64,144,148
tv 142,225,55296
mvva 64,148,156
tv 152,225,57344
tv 154,225,59392
mvva 68,148,188
mvva 68,144,180
lv 184,250,50,2048
mvva 68,132,172
mvva 68,128,164
mvva 72,128,128
mvva 72,132,136
lv 216,250,50,3072
mvva 72,144,144
mvva 72,148,152
mvva 76,148,184
mvva 76,144,176
lv 208,250,50,1024
mvva 76,132,168
tv 148,232,12288
mvva 76,128,160
tv 144,232,8192
s_waitcnt lgkmcnt(2)
mvva 92,136,160
tv 132,232,4096
mvva 92,140,168
tv 128,232,0
tv 130,232,2048
mvva 92,152,176
lv 200,252,50,3072
tv 134,232,6144
mvva 92,156,184
tv 146,232,10240
mvva 88,156,152
tv 150,232,14336
mvva 88,152,144
mvva 88,140,136
lv 192,252,50,1024
mvva 88,136,128
mvva 84,136,164
mvva 84,140,172
mvva 84,152,180
dl 245,36,51,0
mvva 84,156,188
mvva 80,156,156
mvva 80,152,148
mvva 80,140,140
dl 246,36,51,1024
mvva 80,136,132
tv 152,233,8192
s_waitcnt vmcnt(17)
v_cmp_ne_u32_e32 vcc,4,v253
s_waitcnt lgkmcnt(1)
mvva 0,128,4
s_cbranch_vccnz L_spa8o
L_spra8o:
tv 140,233,4096
tv 142,233,6144
mvva 0,132,12
s_xor_b32 s50,s67,s98
tv 136,233,0
s_xor_b32 s50,s50,147456
tv 138,233,2048
mvva 0,144,20
dl 249,36,51,2048
mvva 0,148,28
tv 154,233,10240
v_and_b32_e32 v242,48,v244
mvva 4,148,60
tv 156,233,12288
tv 158,233,14336
mvva 4,144,52
v_lshlrev_b32_e32 v242,3,v242
mvva 4,132,44
mvva 4,128,36
la 196,252,50,0
mvva 8,128,0
s_andn2_b32 s53,s61,384
mvva 8,132,8
mvva 8,144,16
mvva 8,148,24
la 204,252,50,2048
mvva 12,148,56
mvva 12,144,48
mvva 12,132,40
tv 148,234,12288
mvva 12,128,32
buffer_load_dword v253,v242,s[8:11],s53 offen sc1
tv 144,234,8192
s_waitcnt lgkmcnt(2)
mvva 28,136,32
tv 132,234,4096
mvva 28,140,40
tv 128,234,0
tv 130,234,2048
mvva 28,152,48
tv 134,234,6144
mvva 28,156,56
la 212,250,50,0
tv 146,234,10240
mvva 24,156,24
tv 150,234,14336
mvva 24,152,16
mvva 24,140,8
mvva 24,136,0
la 220,250,50,2048
mvva 20,136,36
mvva 20,140,44
mvva 20,152,52
mvva 20,156,60
mvva 16,156,28
la 252,250,50,3072
mvva 16,152,20
mvva 16,140,12
tv 156,235,12288
tv 158,235,14336
mvva 16,136,4
s_waitcnt lgkmcnt(2)
mvva 32,128,4
la 244,250,50,1024
tv 140,235,4096
mvva 32,132,12
tv 136,235,0
tv 138,235,2048
mvva 32,144,20
tv 142,235,6144
mvva 32,148,28
tv 152,235,8192
tv 154,235,10240
mvva 36,148,60
la 236,252,50,3072
mvva 36,144,52
mvva 36,132,44
mvva 36,128,36
mvva 40,128,0
mvva 40,132,8
la 228,252,50,1024
s_add_u32 s50,s50,s55
mvva 40,144,16
mvva 40,148,24
mvva 44,148,56
mvva 44,144,48
la 192,252,50,0
mvva 44,132,40
mvva 44,128,32
s_barrier
s_waitcnt lgkmcnt(0)
mvva 60,136,32
tv 132,232,20480
tv 144,232,24576
mvva 60,140,40
la 200,252,50,2048
tv 128,232,16384
tv 130,232,18432
mvva 60,152,48
tv 134,232,22528
mvva 60,156,56
tv 146,232,26624
mvva 56,156,24
tv 148,232,28672
tv 150,232,30720
mvva 56,152,16
la 208,250,50,0
mvva 56,140,8
mvva 56,136,0
mvva 52,136,36
mvva 52,140,44
la 216,250,50,2048
mvva 52,152,52
mvva 52,156,60
mvva 48,156,28
mvva 48,152,20
mvva 48,140,12
la 248,250,50,3072
mvva 48,136,4
tv 152,233,24576
s_waitcnt lgkmcnt(1)
mvva 64,128,4
tv 140,233,20480
tv 142,233,22528
mvva 64,132,12
tv 136,233,16384
tv 138,233,18432
mvva 64,144,20
la 240,250,50,1024
mvva 64,148,28
tv 154,233,26624
mvva 68,148,60
tv 156,233,28672
tv 158,233,30720
mvva 68,144,52
L_vs30:
s_cbranch_execz L_vr1
mvva 68,132,44
la 232,252,50,3072
mvva 68,128,36
mvva 72,128,0
mvva 72,132,8
mvva 72,144,16
la 224,252,50,1024
mvva 72,148,24
mvva 76,148,56
mvva 76,144,48
mvva 76,132,40
dl 247,36,51,3072
s_lshl_b32 s51,s98,5
s_xor_b32 s51,s65,s51
s_add_u32 m0,s52,114688
s_xor_b32 s51,s51,4718592
mvva 76,128,32
tv 144,224,8192
s_waitcnt lgkmcnt(1)
mvva 92,136,32
tv 132,224,4096
tv 134,224,6144
mvva 92,140,40
tv 128,224,0
tv 130,224,2048
mvva 92,152,48
mvva 92,156,56
dl 245,36,51,0
tv 146,224,10240
mvva 88,156,24
tv 148,224,12288
tv 150,224,14336
mvva 88,152,16
mvva 88,140,8
mvva 88,136,0
dl 246,36,51,1024
s_waitcnt vmcnt(17)
v_cmp_ne_u32_e32 vcc,4,v253
mvva 84,136,36
s_cbranch_vccnz L_sppo
L_sprpo:
mvva 84,140,44
mvva 84,152,52
mvva 84,156,60
dl 249,36,51,2048
mvva 80,156,28
mvva 80,152,20
mvva 80,140,12
tv 156,225,12288
mvva 80,136,4
tv 152,225,8192
s_waitcnt lgkmcnt(2)
mvvv 0,128,164
)"
R"(dl 247,36,51,3072
tv 140,225,4096
mvvv 0,132,172
tv 136,225,0
tv 138,225,2048
mvvv 0,144,180
tv 142,225,6144
mvvv 0,148,188
tv 154,225,10240
mvvv 4,148,220
tv 158,225,14336
mvvv 4,144,212
mvvv 4,132,204
mvvv 4,128,196
mvvv 8,128,160
mvvv 8,132,168
mvvv 8,144,176
mvvv 8,148,184
mvvv 12,148,216
mvvv 12,144,208
mvvv 12,132,200
tv 148,224,28672
mvvv 12,128,192
tv 144,224,24576
s_waitcnt lgkmcnt(2)
mvvv 28,136,192
tv 132,224,20480
mvvv 28,140,200
tv 128,224,16384
tv 130,224,18432
mvvv 28,152,208
tv 134,224,22528
mvvv 28,156,216
tv 146,224,26624
mvvv 24,156,184
tv 150,224,30720
mvvv 24,152,176
mvvv 24,140,168
mvvv 24,136,160
mvvv 20,136,196
mvvv 20,140,204
mvvv 20,152,212
mvvv 20,156,220
mvvv 16,156,188
mvvv 16,152,180
mvvv 16,140,172
tv 156,225,28672
mvvv 16,136,164
tv 152,225,24576
s_waitcnt lgkmcnt(2)
mvva 0,128,196
tv 140,225,20480
mvva 0,132,204
tv 136,225,16384
tv 138,225,18432
s_waitcnt vmcnt(14)
mvva 0,144,212
tv 142,225,22528
mvva 0,148,220
tv 154,225,26624
mvva 4,148,252
tv 158,225,30720
mvva 4,144,244
mvva 4,132,236
s_waitcnt vmcnt(13)
mvva 4,128,228
s_waitcnt vmcnt(12)
mvva 8,128,192
cv 0,0,1
cv 1,2,3
s_waitcnt vmcnt(11)
mvva 8,132,200
s_waitcnt vmcnt(10)
mvva 8,144,208
cv 4,4,5
cv 5,6,7
s_waitcnt vmcnt(9)
mvva 8,148,216
s_waitcnt vmcnt(8)
mvva 12,148,248
s_waitcnt vmcnt(7)
mvva 12,144,240
s_waitcnt vmcnt(6)
mvva 12,132,232
tv 148,226,12288
s_waitcnt vmcnt(5)
mvva 12,128,224
tv 144,226,8192
cv 8,8,9
s_waitcnt lgkmcnt(2)
mvva 28,136,224
tv 132,226,4096
cv 9,10,11
mvva 28,140,232
tv 128,226,0
tv 130,226,2048
mvva 28,152,240
tv 134,226,6144
cv 12,12,13
mvva 28,156,248
tv 146,226,10240
cv 13,14,15
mvva 24,156,216
tv 150,226,14336
mvva 24,152,208
mvva 24,140,200
mvva 24,136,192
mvva 20,136,228
cv 14,28,29
cv 15,30,31
mvva 20,140,236
buffer_store_dwordx4 v[12:15],v244,s[8:11],s56 offen offset:3072 sc0 sc1
mvva 20,152,244
mvva 20,156,252
mvva 16,156,220
cv 10,24,25
cv 11,26,27
mvva 16,152,212
buffer_store_dwordx4 v[8:11],v244,s[8:11],s56 offen offset:2048 sc0 sc1
mvva 16,140,204
tv 156,227,12288
mvva 16,136,196
cv 6,20,21
cv 7,22,23
s_waitcnt lgkmcnt(1)
mvvv 32,128,164
tv 140,227,4096
mvvv 32,132,172
buffer_store_dwordx4 v[4:7],v244,s[8:11],s56 offen offset:1024 sc0 sc1
tv 136,227,0
tv 138,227,2048
mvvv 32,144,180
tv 142,227,6144
cv 2,16,17
mvvv 32,148,188
tv 152,227,8192
tv 154,227,10240
mvvv 36,148,220
tv 158,227,14336
cv 3,18,19
mvvv 36,144,212
buffer_store_dwordx4 v[0:3],v244,s[8:11],s56 offen offset:0 sc0 sc1
mvvv 36,132,204
mvvv 36,128,196
mvvv 40,128,160
mvvv 40,132,168
mvvv 40,144,176
mvvv 40,148,184
mvvv 44,148,216
mvvv 44,144,208
mvvv 44,132,200
tv 148,226,28672
mvvv 44,128,192
s_waitcnt vmcnt(4)
s_barrier
tv 144,226,24576
s_waitcnt lgkmcnt(2)
mvvv 60,136,192
tv 132,226,20480
mvvv 60,140,200
tv 128,226,16384
tv 130,226,18432
mvvv 60,152,208
tv 134,226,22528
mvvv 60,156,216
tv 146,226,26624
mvvv 56,156,184
tv 150,226,30720
mvvv 56,152,176
mvvv 56,140,168
mvvv 56,136,160
mvvv 52,136,196
mvvv 52,140,204
mvvv 52,152,212
mvvv 52,156,220
mvvv 48,156,188
mvvv 48,152,180
mvvv 48,140,172
tv 156,227,28672
mvvv 48,136,164
tv 152,227,24576
s_waitcnt lgkmcnt(2)
mvva 32,128,196
tv 140,227,20480
mvva 32,132,204
tv 136,227,16384
tv 138,227,18432
mvva 32,144,212
tv 142,227,22528
mvva 32,148,220
tv 154,227,26624
mvva 36,148,252
tv 158,227,30720
mvva 36,144,244
mvva 36,132,236
mvva 36,128,228
mvva 40,128,192
mvva 40,132,200
cv 32,32,33
cv 33,34,35
mvva 40,144,208
mvva 40,148,216
mvva 44,148,248
mvva 44,144,240
cv 36,36,37
cv 37,38,39
mvva 44,132,232
tv 148,228,45056
mvva 44,128,224
tv 144,228,40960
cv 40,40,41
s_waitcnt lgkmcnt(2)
mvva 60,136,224
tv 132,228,36864
cv 41,42,43
mvva 60,140,232
tv 128,228,32768
tv 130,228,34816
mvva 60,152,240
tv 134,228,38912
cv 44,44,45
mvva 60,156,248
tv 146,228,43008
cv 45,46,47
mvva 56,156,216
tv 150,228,47104
mvva 56,152,208
mvva 56,140,200
mvva 56,136,192
mvva 52,136,228
cv 46,60,61
cv 47,62,63
mvva 52,140,236
buffer_store_dwordx4 v[44:47],v244,s[8:11],s57 offen offset:3072 sc0 sc1
mvva 52,152,244
mvva 52,156,252
mvva 48,156,220
cv 42,56,57
cv 43,58,59
mvva 48,152,212
buffer_store_dwordx4 v[40:43],v244,s[8:11],s57 offen offset:2048 sc0 sc1
mvva 48,140,204
tv 156,229,45056
mvva 48,136,196
cv 38,52,53
cv 39,54,55
s_waitcnt lgkmcnt(1)
mvvv 64,128,164
tv 140,229,36864
mvvv 64,132,172
buffer_store_dwordx4 v[36:39],v244,s[8:11],s57 offen offset:1024 sc0 sc1
tv 136,229,32768
tv 138,229,34816
mvvv 64,144,180
tv 142,229,38912
cv 34,48,49
mvvv 64,148,188
tv 152,229,40960
tv 154,229,43008
mvvv 68,148,220
tv 158,229,47104
cv 35,50,51
mvvv 68,144,212
buffer_store_dwordx4 v[32:35],v244,s[8:11],s57 offen offset:0 sc0 sc1
mvvv 68,132,204
mvvv 68,128,196
mvvv 72,128,160
mvvv 72,132,168
mvvv 72,144,176
mvvv 72,148,184
mvvv 76,148,216
mvvv 76,144,208
mvvv 76,132,200
tv 148,228,61440
mvvv 76,128,192
tv 144,228,57344
s_waitcnt lgkmcnt(2)
mvvv 92,136,192
tv 132,228,53248
mvvv 92,140,200
tv 128,228,49152
tv 130,228,51200
mvvv 92,152,208
tv 134,228,55296
mvvv 92,156,216
tv 146,228,59392
mvvv 88,156,184
tv 150,228,63488
mvvv 88,152,176
mvvv 88,140,168
mvvv 88,136,160
mvvv 84,136,196
mvvv 84,140,204
mvvv 84,152,212
mvvv 84,156,220
mvvv 80,156,188
mvvv 80,152,180
mvvv 80,140,172
tv 156,229,61440
mvvv 80,136,164
tv 152,229,57344
s_waitcnt lgkmcnt(2)
mvva 64,128,196
tv 140,229,53248
mvva 64,132,204
tv 136,229,49152
tv 138,229,51200
mvva 64,144,212
tv 142,229,55296
mvva 64,148,220
tv 154,229,59392
mvva 68,148,252
tv 158,229,63488
mvva 68,144,244
mvva 68,132,236
mvva 68,128,228
s_xor_b32 s82,s59,12
mvva 72,128,192
s_mul_i32 s82,s82,12288
cv 64,64,65
s_add_u32 s82,s82,0
mvva 72,132,200
lv 16,244,82,0
mvva 72,144,208
cv 65,66,67
cv 68,68,69
mvva 72,148,216
cv 69,70,71
mvva 76,148,248
mvva 76,144,240
lv 20,244,82,1024
mvva 76,132,232
tv 148,230,28672
mvva 76,128,224
tv 144,230,24576
cv 72,72,73
s_waitcnt lgkmcnt(2)
mvva 92,136,224
tv 132,230,20480
cv 73,74,75
mvva 92,140,232
tv 128,230,16384
tv 130,230,18432
mvva 92,152,240
lv 24,244,82,2048
tv 134,230,22528
mvva 92,156,248
tv 146,230,26624
cv 76,76,77
mvva 88,156,216
tv 150,230,30720
cv 77,78,79
mvva 88,152,208
mvva 88,140,200
lv 28,244,82,3072
mvva 88,136,192
cv 78,92,93
cv 79,94,95
mvva 84,136,228
mvva 84,140,236
mvva 84,152,244
buffer_store_dwordx4 v[76:79],v244,s[8:11],s58 offen offset:3072 sc0 sc1
mvva 84,156,252
mvva 80,156,220
cv 74,88,89
cv 75,90,91
mvva 80,152,212
mvva 80,140,204
buffer_store_dwordx4 v[72:75],v244,s[8:11],s58 offen offset:2048 sc0 sc1
tv 156,231,28672
mvva 80,136,196
cv 70,84,85
cv 71,86,87
s_waitcnt lgkmcnt(1)
mvva 96,128,68
tv 140,231,20480
mvva 96,132,76
tv 136,231,16384
tv 138,231,18432
mvva 96,144,84
buffer_store_dwordx4 v[68:71],v244,s[8:11],s58 offen offset:1024 sc0 sc1
tv 142,231,22528
mvva 96,148,92
tv 152,231,24576
tv 154,231,26624
mvva 100,148,124
tv 158,231,30720
cv 66,80,81
cv 67,82,83
mvva 100,144,116
mvva 100,132,108
mvva 100,128,100
buffer_store_dwordx4 v[64:67],v244,s[8:11],s58 offen offset:0 sc0 sc1
mvva 104,128,64
mvva 104,132,72
mvva 104,144,80
mvva 104,148,88
mvva 108,148,120
mvva 108,144,112
mvva 108,132,104
tv 148,226,61440
mvva 108,128,96
tv 144,226,57344
s_waitcnt lgkmcnt(2)
mvva 124,136,96
tv 132,226,53248
mvva 124,140,104
tv 128,226,49152
tv 130,226,51200
mvva 124,152,112
tv 134,226,55296
mvva 124,156,120
tv 146,226,59392
mvva 120,156,88
tv 150,226,63488
mvva 120,152,80
mvva 120,140,72
mvva 120,136,64
mvva 116,136,100
mvva 116,140,108
s_waitcnt vmcnt(7)
v_max_u32_e32 v248,v16,v17
mvva 116,152,116
v_max3_u32 v248,v248,v18,v19
s_waitcnt vmcnt(6)
v_max3_u32 v248,v248,v20,v21
v_max3_u32 v248,v248,v22,v23
mvva 116,156,124
s_waitcnt vmcnt(5)
v_max3_u32 v248,v248,v24,v25
v_max3_u32 v248,v248,v26,v27
s_waitcnt vmcnt(4)
v_max3_u32 v248,v248,v28,v29
mvva 112,156,92
v_max3_u32 v248,v248,v30,v31
v_cmp_eq_u32_e32 vcc,-1,v248
mvva 112,152,84
s_cbranch_vccnz L_spx0o
L_sprx0o:
mvva 112,140,76
tv 156,227,61440
tv 158,227,63488
v_lshlrev_b32_e32 v251,16,v18
v_and_b32_e32 v255,4294901760,v18
mvva 112,136,68
tv 152,227,57344
tv 154,227,59392
s_waitcnt lgkmcnt(4)
mvva 96,128,132
v_lshlrev_b32_e32 v253,16,v19
v_and_b32_e32 v242,4294901760,v19
mvva 96,132,140
tv 136,227,49152
tv 138,227,51200
mvva 96,144,148
tv 140,227,53248
tv 142,227,55296
v_add_f32_e32 v112,v112,v251
mvva 96,148,156
v_add_f32_e32 v113,v113,v255
v_add_f32_e32 v114,v114,v253
mvva 100,148,188
v_add_f32_e32 v115,v115,v242
v_lshlrev_b32_e32 v251,16,v22
mvva 100,144,180
v_and_b32_e32 v255,4294901760,v22
v_lshlrev_b32_e32 v253,16,v23
mvva 100,132,172
v_and_b32_e32 v242,4294901760,v23
v_add_f32_e32 v116,v116,v251
v_add_f32_e32 v117,v117,v255
mvva 100,128,164
v_add_f32_e32 v118,v118,v253
v_add_f32_e32 v119,v119,v242
mvva 104,128,128
v_lshlrev_b32_e32 v251,16,v26
v_and_b32_e32 v255,4294901760,v26
mvva 104,132,136
v_lshlrev_b32_e32 v253,16,v27
v_and_b32_e32 v242,4294901760,v27
mvva 104,144,144
v_add_f32_e32 v120,v120,v251
v_add_f32_e32 v121,v121,v255
v_add_f32_e32 v122,v122,v253
mvva 104,148,152
v_add_f32_e32 v123,v123,v242
v_lshlrev_b32_e32 v251,16,v30
mvva 108,148,184
v_and_b32_e32 v255,4294901760,v30
v_lshlrev_b32_e32 v253,16,v31
mvva 108,144,176
v_and_b32_e32 v242,4294901760,v31
v_add_f32_e32 v124,v124,v251
mvva 108,132,168
L_vs31:
s_cbranch_execz L_vr1
v_add_f32_e32 v125,v125,v255
v_add_f32_e32 v126,v126,v253
v_add_f32_e32 v127,v127,v242
mvva 108,128,160
tv 144,234,24576
v_lshlrev_b32_e32 v251,16,v16
s_waitcnt lgkmcnt(1)
mvva 124,136,160
tv 132,234,20480
v_and_b32_e32 v255,4294901760,v16
mvva 124,140,168
tv 128,234,16384
tv 130,234,18432
mvva 124,152,176
tv 134,234,22528
v_lshlrev_b32_e32 v253,16,v17
mvva 124,156,184
tv 146,234,26624
v_and_b32_e32 v242,4294901760,v17
mvva 120,156,152
tv 148,234,28672
tv 150,234,30720
mvva 120,152,144
v_add_f32_e32 v96,v96,v251
v_add_f32_e32 v97,v97,v255
mvva 120,140,136
v_add_f32_e32 v98,v98,v253
v_add_f32_e32 v99,v99,v242
mvva 120,136,128
v_lshlrev_b32_e32 v251,16,v20
v_and_b32_e32 v255,4294901760,v20
mvva 116,136,164
v_lshlrev_b32_e32 v253,16,v21
v_and_b32_e32 v242,4294901760,v21
mvva 116,140,172
v_add_f32_e32 v100,v100,v251
v_add_f32_e32 v101,v101,v255
mvva 116,152,180
v_add_f32_e32 v102,v102,v253
v_add_f32_e32 v103,v103,v242
mvva 116,156,188
v_lshlrev_b32_e32 v251,16,v24
v_and_b32_e32 v255,4294901760,v24
mvva 112,156,156
v_lshlrev_b32_e32 v253,16,v25
v_and_b32_e32 v242,4294901760,v25
mvva 112,152,148
v_add_f32_e32 v104,v104,v251
v_add_f32_e32 v105,v105,v255
mvva 112,140,140
tv 156,235,28672
v_add_f32_e32 v106,v106,v253
mvva 112,136,132
tv 152,235,24576
v_add_f32_e32 v107,v107,v242
s_waitcnt lgkmcnt(2)
mvva 96,128,4
tv 140,235,20480
v_lshlrev_b32_e32 v251,16,v28
mvva 96,132,12
tv 136,235,16384
tv 138,235,18432
mvva 96,144,20
tv 142,235,22528
v_and_b32_e32 v255,4294901760,v28
mvva 96,148,28
tv 154,235,26624
v_lshlrev_b32_e32 v253,16,v29
mvva 100,148,60
tv 158,235,30720
v_and_b32_e32 v242,4294901760,v29
mvva 100,144,52
v_add_f32_e32 v108,v108,v251
v_add_f32_e32 v109,v109,v255
mvva 100,132,44
v_add_f32_e32 v110,v110,v253
v_add_f32_e32 v111,v111,v242
mvva 100,128,36
mvva 104,128,0
mvva 104,132,8
mvva 104,144,16
mvva 104,148,24
mvva 108,148,56
mvva 108,144,48
mvva 108,132,40
tv 148,230,45056
mvva 108,128,32
tv 144,230,40960
s_waitcnt lgkmcnt(2)
mvva 124,136,32
s_xor_b32 s83,s59,8
tv 132,230,36864
s_mul_i32 s83,s83,12288
mvva 124,140,40
s_add_u32 s83,s83,4096
tv 128,230,32768
tv 130,230,34816
mvva 124,152,48
lv 48,244,83,0
tv 134,230,38912
mvva 124,156,56
tv 146,230,43008
mvva 120,156,24
lv 52,244,83,1024
tv 150,230,47104
mvva 120,152,16
mvva 120,140,8
lv 56,244,83,2048
mvva 120,136,0
mvva 116,136,36
lv 60,244,83,3072
mvva 116,140,44
mvva 116,152,52
s_xor_b32 s84,s59,4
mvva 116,156,60
s_mul_i32 s84,s84,12288
mvva 112,156,28
s_add_u32 s84,s84,8192
mvva 112,152,20
lv 80,244,84,0
mvva 112,140,12
tv 156,231,45056
mvva 112,136,4
lv 84,244,84,1024
tv 152,231,40960
s_waitcnt lgkmcnt(2)
mvvv 96,128,164
tv 140,231,36864
mvvv 96,132,172
lv 88,244,84,2048
tv 136,231,32768
tv 138,231,34816
mvvv 96,144,180
tv 142,231,38912
mvvv 96,148,188
lv 92,244,84,3072
tv 154,231,43008
mvvv 100,148,220
tv 158,231,47104
mvvv 100,144,212
mvvv 100,132,204
mvvv 100,128,196
mvvv 104,128,160
mvvv 104,132,168
mvvv 104,144,176
mvvv 104,148,184
mvvv 108,148,216
mvvv 108,144,208
mvvv 108,132,200
tv 148,230,61440
mvvv 108,128,192
tv 144,230,57344
s_waitcnt lgkmcnt(2)
mvvv 124,136,192
tv 132,230,53248
mvvv 124,140,200
tv 128,230,49152
tv 130,230,51200
mvvv 124,152,208
tv 134,230,55296
mvvv 124,156,216
tv 146,230,59392
mvvv 120,156,184
tv 150,230,63488
mvvv 120,152,176
mvvv 120,140,168
mvvv 120,136,160
mvvv 116,136,196
mvvv 116,140,204
mvvv 116,152,212
mvvv 116,156,220
mvvv 112,156,188
mvvv 112,152,180
mvvv 112,140,172
tv 156,231,61440
mvvv 112,136,164
tv 152,231,57344
s_waitcnt lgkmcnt(2)
mvva 96,128,196
tv 140,231,53248
mvva 96,132,204
tv 136,231,49152
tv 138,231,51200
mvva 96,144,212
tv 142,231,55296
mvva 96,148,220
tv 154,231,59392
mvva 100,148,252
tv 158,231,63488
mvva 100,144,244
mvva 100,132,236
mvva 100,128,228
mvva 104,128,192
mvva 104,132,200
mvva 104,144,208
mvva 104,148,216
s_waitcnt vmcnt(7)
v_max_u32_e32 v248,v48,v49
mvva 108,148,248
v_max3_u32 v248,v248,v50,v51
s_waitcnt vmcnt(6)
v_max3_u32 v248,v248,v52,v53
mvva 108,144,240
v_max3_u32 v248,v248,v54,v55
s_waitcnt vmcnt(5)
v_max3_u32 v248,v248,v56,v57
mvva 108,132,232
v_max3_u32 v248,v248,v58,v59
s_waitcnt vmcnt(4)
v_max3_u32 v248,v248,v60,v61
mvva 108,128,224
v_max3_u32 v248,v248,v62,v63
v_cmp_eq_u32_e32 vcc,-1,v248
s_waitcnt lgkmcnt(0)
mvva 124,136,224
s_cbranch_vccnz L_spx1o
L_sprx1o:
mvva 124,140,232
mvva 124,152,240
mvva 124,156,248
s_waitcnt vmcnt(3)
v_max_u32_e32 v248,v80,v81
mvva 120,156,216
v_max3_u32 v248,v248,v82,v83
s_waitcnt vmcnt(2)
v_max3_u32 v248,v248,v84,v85
mvva 120,152,208
v_max3_u32 v248,v248,v86,v87
s_waitcnt vmcnt(1)
v_max3_u32 v248,v248,v88,v89
mvva 120,140,200
v_max3_u32 v248,v248,v90,v91
s_waitcnt vmcnt(0)
v_max3_u32 v248,v248,v92,v93
mvva 120,136,192
v_max3_u32 v248,v248,v94,v95
v_cmp_eq_u32_e32 vcc,-1,v248
mvva 116,136,228
s_cbranch_vccnz L_spx2o
L_sprx2o:
mvva 116,140,236
mvva 116,152,244
mvva 116,156,252
mvva 112,156,220
mvva 112,152,212
mvva 112,140,204
mvva 112,136,196
mvvv 108,238,60
mvvv 108,238,92
mvvv 124,236,60
mvvv 124,236,92
mvvv 104,238,56
mvvv 104,238,88
mvvv 120,236,56
mvvv 120,236,88
mvvv 100,238,52
mvvv 100,238,84
cv 108,108,109
cv 109,110,111
cv 110,124,125
cv 111,126,127
cv 104,104,105
v_permlane16_swap_b32_e32 v108,v110
mvvv 116,236,52
v_permlane16_swap_b32_e32 v109,v111
cv 105,106,107
cv 106,120,121
cv 107,122,123
mvvv 116,236,84
buffer_store_dwordx4 v[108:111],v243,s[44:47],s73 offen sc0 sc1
v_permlane16_swap_b32_e32 v104,v106
v_permlane16_swap_b32_e32 v105,v107
mvvv 96,238,48
cv 100,100,101
cv 101,102,103
mvvv 96,238,80
buffer_store_dwordx4 v[104:107],v243,s[44:47],s72 offen sc0 sc1
cv 102,116,117
cv 103,118,119
mvvv 112,236,48
v_permlane16_swap_b32_e32 v100,v102
v_permlane16_swap_b32_e32 v101,v103
mvvv 112,236,80
buffer_store_dwordx4 v[100:103],v243,s[44:47],s71 offen sc0 sc1
cv 96,96,97
cv 97,98,99
s_nop 4
cv 98,112,113
cv 99,114,115
s_nop 0
v_permlane16_swap_b32_e32 v96,v98
v_permlane16_swap_b32_e32 v97,v99
buffer_store_dwordx4 v[96:99],v243,s[44:47],s70 offen sc0 sc1
L_oddx:
s_endpgm
L_k_ec1:
buffer_store_dwordx4 a[252:255],v244,s[8:11],s35 offen offset:1024 sc0 sc1
s_branch L_k_ecr1
L_q_ack:
s_mov_b32 s86,65536
L_q_ack2:
s_sleep 8
buffer_load_dword v246,off,s[8:11],s35 offset:1024 sc1
s_waitcnt vmcnt(0)
v_readfirstlane_b32 s87,v246
s_nop 0
s_cmp_eq_u32 s87,s32
s_cbranch_scc1 L_q_acked
s_sub_u32 s86,s86,1
s_cbranch_scc0 L_q_ack2
s_branch L_q_acked
L_q_spin:
s_mov_b32 s6,65536
L_q_spin1:
s_sleep 8
buffer_load_dword v248,v250,s[8:11],0 offen sc1
s_or_b32 s7,%1,s12
s_cbranch_scc1 L_q_spin2
buffer_load_dwordx4 a[252:255],v244,s[8:11],s35 offen sc1
s_waitcnt vmcnt(0)
buffer_store_dwordx4 a[252:255],v244,s[8:11],s35 offen offset:1024 sc0 sc1
L_q_spin2:
s_waitcnt vmcnt(0)
v_and_b32_e32 v248,v242,v248
v_cmp_ne_u32_e64 s[100:101],v243,v248
s_nop 1
s_cmp_eq_u64 s[100:101],0
s_cbranch_scc1 L_q_ok
s_sub_u32 s6,s6,1
s_cbranch_scc0 L_q_spin1
s_branch L_q_ok
L_bx:
s_mov_b32 s87,65536
L_bx1:
s_sleep 2
buffer_load_dwordx4 v[176:179],v244,s[8:11],s98 offen sc1
buffer_load_dwordx2 v[168:169],v253,s[8:11],s98 offen offset:1024 sc1
buffer_load_dwordx2 v[172:173],v253,s[8:11],s98 offen offset:1536 sc1
s_waitcnt vmcnt(0)
v_max3_u32 v180,v176,v177,v178
v_max3_u32 v180,v180,v179,v168
v_max3_u32 v180,v180,v169,v172
v_max_u32_e32 v180,v180,v173
v_cmp_eq_u32_e64 s[40:41],-1,v180
s_nop 1
s_cmp_eq_u64 s[40:41],0
s_cbranch_scc1 L_bxr
s_sub_u32 s87,s87,1
s_cbranch_scc0 L_bx1
s_branch L_bxr
L_pend2:
s_nop 7
v_add_f32_e32 v226,v152,v153
v_add_f32_e32 v227,v156,v157
v_subrev_u32_e32 v236,0x23800000,v226
v_subrev_u32_e32 v237,0x23800000,v227
v_max_u32_e32 v236,v236,v237
v_cmp_lt_u32_e64 s[40:41],s48,v236
v_rcp_f32_e32 v226,v226
v_rcp_f32_e32 v227,v227
s_add_u32 s86,s13,4096
v_mov_b32_e32 v236,0
v_mov_b32_e32 v237,0
v_mov_b32_e32 v238,0
v_mov_b32_e32 v239,0
v_mov_b64_e32 v[152:153],0
v_mov_b64_e32 v[156:157],0
v_mul_f32_e32 v226,v242,v226
v_mul_f32_e32 v227,v243,v227
v_add_u32_e32 v225,s94,v225
v_pk_mul_f32 v[160:161],v[160:161],v[226:227] op_sel:[0,0] op_sel_hi:[1,0]
v_pk_mul_f32 v[162:163],v[162:163],v[226:227] op_sel:[0,0] op_sel_hi:[1,0]
v_pk_mul_f32 v[164:165],v[164:165],v[226:227] op_sel:[0,0] op_sel_hi:[1,0]
v_pk_mul_f32 v[166:167],v[166:167],v[226:227] op_sel:[0,0] op_sel_hi:[1,0]
cv 160,160,161
cv 161,162,163
cv 162,164,165
cv 163,166,167
buffer_store_dwordx4 v[160:163],v244,s[8:11],s13 offen offset:0 sc0 sc1
v_pk_mul_f32 v[168:169],v[168:169],v[226:227] op_sel:[0,0] op_sel_hi:[1,0]
v_pk_mul_f32 v[170:171],v[170:171],v[226:227] op_sel:[0,0] op_sel_hi:[1,0]
v_pk_mul_f32 v[172:173],v[172:173],v[226:227] op_sel:[0,0] op_sel_hi:[1,0]
v_pk_mul_f32 v[174:175],v[174:175],v[226:227] op_sel:[0,0] op_sel_hi:[1,0]
cv 168,168,169
cv 169,170,171
cv 170,172,173
cv 171,174,175
buffer_store_dwordx4 v[168:171],v244,s[8:11],s13 offen offset:2048 sc0 sc1
v_pk_mul_f32 v[176:177],v[176:177],v[226:227] op_sel:[0,0] op_sel_hi:[1,0]
v_pk_mul_f32 v[178:179],v[178:179],v[226:227] op_sel:[0,0] op_sel_hi:[1,0]
v_pk_mul_f32 v[180:181],v[180:181],v[226:227] op_sel:[0,0] op_sel_hi:[1,0]
v_pk_mul_f32 v[182:183],v[182:183],v[226:227] op_sel:[0,0] op_sel_hi:[1,0]
cv 176,176,177
cv 177,178,179
cv 178,180,181
v_mfma_f32_32x32x16_bf16 v[160:175],v[236:239],v[236:239],0
cv 179,182,183
buffer_store_dwordx4 v[176:179],v244,s[8:11],s86 offen offset:0 sc0 sc1
v_pk_mul_f32 v[184:185],v[184:185],v[226:227] op_sel:[0,0] op_sel_hi:[1,0]
v_pk_mul_f32 v[186:187],v[186:187],v[226:227] op_sel:[0,0] op_sel_hi:[1,0]
v_pk_mul_f32 v[188:189],v[188:189],v[226:227] op_sel:[0,0] op_sel_hi:[1,0]
v_pk_mul_f32 v[190:191],v[190:191],v[226:227] op_sel:[0,0] op_sel_hi:[1,0]
cv 184,184,185
cv 185,186,187
cv 186,188,189
cv 187,190,191
buffer_store_dwordx4 v[184:187],v244,s[8:11],s86 offen offset:2048 sc0 sc1
v_pk_mul_f32 v[192:193],v[192:193],v[226:227] op_sel:[0,1] op_sel_hi:[1,1]
v_pk_mul_f32 v[194:195],v[194:195],v[226:227] op_sel:[0,1] op_sel_hi:[1,1]
v_pk_mul_f32 v[196:197],v[196:197],v[226:227] op_sel:[0,1] op_sel_hi:[1,1]
v_pk_mul_f32 v[198:199],v[198:199],v[226:227] op_sel:[0,1] op_sel_hi:[1,1]
cv 192,192,193
cv 193,194,195
cv 194,196,197
v_mfma_f32_32x32x16_bf16 v[176:191],v[236:239],v[236:239],0
cv 195,198,199
buffer_store_dwordx4 v[192:195],v244,s[8:11],s13 offen offset:1024 sc0 sc1
v_pk_mul_f32 v[200:201],v[200:201],v[226:227] op_sel:[0,1] op_sel_hi:[1,1]
v_pk_mul_f32 v[202:203],v[202:203],v[226:227] op_sel:[0,1] op_sel_hi:[1,1]
v_pk_mul_f32 v[204:205],v[204:205],v[226:227] op_sel:[0,1] op_sel_hi:[1,1]
v_pk_mul_f32 v[206:207],v[206:207],v[226:227] op_sel:[0,1] op_sel_hi:[1,1]
cv 200,200,201
cv 201,202,203
cv 202,204,205
cv 203,206,207
buffer_store_dwordx4 v[200:203],v244,s[8:11],s13 offen offset:3072 sc0 sc1
v_pk_mul_f32 v[208:209],v[208:209],v[226:227] op_sel:[0,1] op_sel_hi:[1,1]
v_pk_mul_f32 v[210:211],v[210:211],v[226:227] op_sel:[0,1] op_sel_hi:[1,1]
v_pk_mul_f32 v[212:213],v[212:213],v[226:227] op_sel:[0,1] op_sel_hi:[1,1]
v_pk_mul_f32 v[214:215],v[214:215],v[226:227] op_sel:[0,1] op_sel_hi:[1,1]
cv 208,208,209
cv 209,210,211
cv 210,212,213
v_mfma_f32_32x32x16_bf16 v[192:207],v[236:239],v[236:239],0
cv 211,214,215
buffer_store_dwordx4 v[208:211],v244,s[8:11],s86 offen offset:1024 sc0 sc1
v_pk_mul_f32 v[216:217],v[216:217],v[226:227] op_sel:[0,1] op_sel_hi:[1,1]
v_pk_mul_f32 v[218:219],v[218:219],v[226:227] op_sel:[0,1] op_sel_hi:[1,1]
v_pk_mul_f32 v[220:221],v[220:221],v[226:227] op_sel:[0,1] op_sel_hi:[1,1]
v_pk_mul_f32 v[222:223],v[222:223],v[226:227] op_sel:[0,1] op_sel_hi:[1,1]
cv 216,216,217
cv 217,218,219
cv 218,220,221
cv 219,222,223
buffer_store_dwordx4 v[216:219],v244,s[8:11],s86 offen offset:3072 sc0 sc1
s_nop 1
v_mfma_f32_32x32x16_bf16 v[208:223],v[236:239],v[236:239],0
s_nop 7
s_nop 1
s_cmp_eq_u32 s84,4
s_cbranch_scc1 L_ret_eb4
s_cmp_lg_u32 s80,s81
s_branch L_ret_eb5
L_redo:
s_waitcnt vmcnt(0) lgkmcnt(0)
s_nop 15
s_nop 3
s_load_dwordx2 s[76:77],%0,0
s_load_dwordx2 s[80:81],%0,16
s_mov_b32 s78,4194304
s_mov_b32 s79,131072
s_mov_b32 s82,8388608
s_mov_b32 s83,131072
s_mov_b32 s86,0
s_waitcnt lgkmcnt(0)
L_rd_t:
s_nop 15
s_nop 3
s_cmp_lt_u32 s86,2
s_cselect_b32 s88,s23,s22
s_cselect_b32 s95,s13,s14
s_and_b32 s89,s86,1
s_lshl_b32 s90,s89,10
s_add_u32 s95,s95,s90
s_lshl_b32 s89,s89,6
s_add_u32 s88,s88,s89
s_lshl_b32 s89,s12,4
s_add_u32 s88,s88,s89
v_bfe_u32 v213,v244,4,4
v_lshrrev_b32_e32 v214,8,v244
v_add_u32_e32 v210,s88,v213
v_lshlrev_b32_e32 v208,11,v210
v_lshl_add_u32 v208,v214,4,v208
v_lshlrev_b32_e32 v209,16,v214
v_lshl_add_u32 v209,v213,1,v209
s_lshl_b32 s89,s18,8
v_add_u32_e32 v209,s89,v209
v_lshlrev_b32_e32 v214,2,v214
v_sub_u32_e32 v210,v210,v214
v_accvgpr_write_b32 a192,0
v_accvgpr_write_b32 a193,0
v_accvgpr_write_b32 a194,0
v_accvgpr_write_b32 a195,0
v_accvgpr_write_b32 a196,0
v_accvgpr_write_b32 a197,0
v_accvgpr_write_b32 a198,0
v_accvgpr_write_b32 a199,0
v_accvgpr_write_b32 a200,0
v_accvgpr_write_b32 a201,0
v_accvgpr_write_b32 a202,0
v_accvgpr_write_b32 a203,0
v_accvgpr_write_b32 a204,0
v_accvgpr_write_b32 a205,0
v_accvgpr_write_b32 a206,0
)"
R"(v_accvgpr_write_b32 a207,0
v_accvgpr_write_b32 a208,0
v_accvgpr_write_b32 a209,0
v_accvgpr_write_b32 a210,0
v_accvgpr_write_b32 a211,0
v_accvgpr_write_b32 a212,0
v_accvgpr_write_b32 a213,0
v_accvgpr_write_b32 a214,0
v_accvgpr_write_b32 a215,0
v_accvgpr_write_b32 a216,0
v_accvgpr_write_b32 a217,0
v_accvgpr_write_b32 a218,0
v_accvgpr_write_b32 a219,0
v_accvgpr_write_b32 a220,0
v_accvgpr_write_b32 a221,0
v_accvgpr_write_b32 a222,0
v_accvgpr_write_b32 a223,0
s_mov_b32 s87,0
L_rd_q:
s_nop 15
s_nop 3
s_lshl_b32 s89,s87,6
s_lshl_b32 s90,s87,18
buffer_load_dwordx4 v[200:203],v208,s[76:79],s89 offen
buffer_load_ushort v160,v209,s[80:83],s90 offen offset:0
buffer_load_ushort v164,v209,s[80:83],s90 offen offset:32
buffer_load_ushort v168,v209,s[80:83],s90 offen offset:64
buffer_load_ushort v172,v209,s[80:83],s90 offen offset:96
buffer_load_ushort v176,v209,s[80:83],s90 offen offset:128
buffer_load_ushort v180,v209,s[80:83],s90 offen offset:160
buffer_load_ushort v184,v209,s[80:83],s90 offen offset:192
buffer_load_ushort v188,v209,s[80:83],s90 offen offset:224
s_add_u32 s90,s90,8192
buffer_load_ushort v192,v209,s[80:83],s90 offen offset:0
buffer_load_ushort v193,v209,s[80:83],s90 offen offset:32
buffer_load_ushort v194,v209,s[80:83],s90 offen offset:64
buffer_load_ushort v195,v209,s[80:83],s90 offen offset:96
buffer_load_ushort v196,v209,s[80:83],s90 offen offset:128
buffer_load_ushort v197,v209,s[80:83],s90 offen offset:160
buffer_load_ushort v198,v209,s[80:83],s90 offen offset:192
buffer_load_ushort v199,v209,s[80:83],s90 offen offset:224
s_add_u32 s90,s90,8192
s_waitcnt vmcnt(0)
v_lshl_or_b32 v160,v192,16,v160
v_lshl_or_b32 v164,v193,16,v164
v_lshl_or_b32 v168,v194,16,v168
v_lshl_or_b32 v172,v195,16,v172
v_lshl_or_b32 v176,v196,16,v176
v_lshl_or_b32 v180,v197,16,v180
v_lshl_or_b32 v184,v198,16,v184
v_lshl_or_b32 v188,v199,16,v188
buffer_load_ushort v161,v209,s[80:83],s90 offen offset:0
buffer_load_ushort v165,v209,s[80:83],s90 offen offset:32
buffer_load_ushort v169,v209,s[80:83],s90 offen offset:64
buffer_load_ushort v173,v209,s[80:83],s90 offen offset:96
buffer_load_ushort v177,v209,s[80:83],s90 offen offset:128
buffer_load_ushort v181,v209,s[80:83],s90 offen offset:160
buffer_load_ushort v185,v209,s[80:83],s90 offen offset:192
buffer_load_ushort v189,v209,s[80:83],s90 offen offset:224
s_add_u32 s90,s90,8192
buffer_load_ushort v192,v209,s[80:83],s90 offen offset:0
buffer_load_ushort v193,v209,s[80:83],s90 offen offset:32
buffer_load_ushort v194,v209,s[80:83],s90 offen offset:64
buffer_load_ushort v195,v209,s[80:83],s90 offen offset:96
buffer_load_ushort v196,v209,s[80:83],s90 offen offset:128
buffer_load_ushort v197,v209,s[80:83],s90 offen offset:160
buffer_load_ushort v198,v209,s[80:83],s90 offen offset:192
buffer_load_ushort v199,v209,s[80:83],s90 offen offset:224
s_add_u32 s90,s90,8192
s_waitcnt vmcnt(0)
v_lshl_or_b32 v161,v192,16,v161
v_lshl_or_b32 v165,v193,16,v165
v_lshl_or_b32 v169,v194,16,v169
v_lshl_or_b32 v173,v195,16,v173
v_lshl_or_b32 v177,v196,16,v177
v_lshl_or_b32 v181,v197,16,v181
v_lshl_or_b32 v185,v198,16,v185
v_lshl_or_b32 v189,v199,16,v189
buffer_load_ushort v162,v209,s[80:83],s90 offen offset:0
buffer_load_ushort v166,v209,s[80:83],s90 offen offset:32
buffer_load_ushort v170,v209,s[80:83],s90 offen offset:64
buffer_load_ushort v174,v209,s[80:83],s90 offen offset:96
buffer_load_ushort v178,v209,s[80:83],s90 offen offset:128
buffer_load_ushort v182,v209,s[80:83],s90 offen offset:160
buffer_load_ushort v186,v209,s[80:83],s90 offen offset:192
buffer_load_ushort v190,v209,s[80:83],s90 offen offset:224
s_add_u32 s90,s90,8192
buffer_load_ushort v192,v209,s[80:83],s90 offen offset:0
buffer_load_ushort v193,v209,s[80:83],s90 offen offset:32
buffer_load_ushort v194,v209,s[80:83],s90 offen offset:64
buffer_load_ushort v195,v209,s[80:83],s90 offen offset:96
buffer_load_ushort v196,v209,s[80:83],s90 offen offset:128
buffer_load_ushort v197,v209,s[80:83],s90 offen offset:160
buffer_load_ushort v198,v209,s[80:83],s90 offen offset:192
buffer_load_ushort v199,v209,s[80:83],s90 offen offset:224
s_add_u32 s90,s90,8192
s_waitcnt vmcnt(0)
v_lshl_or_b32 v162,v192,16,v162
v_lshl_or_b32 v166,v193,16,v166
v_lshl_or_b32 v170,v194,16,v170
v_lshl_or_b32 v174,v195,16,v174
v_lshl_or_b32 v178,v196,16,v178
v_lshl_or_b32 v182,v197,16,v182
v_lshl_or_b32 v186,v198,16,v186
v_lshl_or_b32 v190,v199,16,v190
buffer_load_ushort v163,v209,s[80:83],s90 offen offset:0
buffer_load_ushort v167,v209,s[80:83],s90 offen offset:32
buffer_load_ushort v171,v209,s[80:83],s90 offen offset:64
buffer_load_ushort v175,v209,s[80:83],s90 offen offset:96
buffer_load_ushort v179,v209,s[80:83],s90 offen offset:128
buffer_load_ushort v183,v209,s[80:83],s90 offen offset:160
buffer_load_ushort v187,v209,s[80:83],s90 offen offset:192
buffer_load_ushort v191,v209,s[80:83],s90 offen offset:224
s_add_u32 s90,s90,8192
buffer_load_ushort v192,v209,s[80:83],s90 offen offset:0
buffer_load_ushort v193,v209,s[80:83],s90 offen offset:32
buffer_load_ushort v194,v209,s[80:83],s90 offen offset:64
buffer_load_ushort v195,v209,s[80:83],s90 offen offset:96
buffer_load_ushort v196,v209,s[80:83],s90 offen offset:128
buffer_load_ushort v197,v209,s[80:83],s90 offen offset:160
buffer_load_ushort v198,v209,s[80:83],s90 offen offset:192
buffer_load_ushort v199,v209,s[80:83],s90 offen offset:224
s_add_u32 s90,s90,8192
s_waitcnt vmcnt(0)
v_lshl_or_b32 v163,v192,16,v163
v_lshl_or_b32 v167,v193,16,v167
v_lshl_or_b32 v171,v194,16,v171
v_lshl_or_b32 v175,v195,16,v175
v_lshl_or_b32 v179,v196,16,v179
v_lshl_or_b32 v183,v197,16,v183
v_lshl_or_b32 v187,v198,16,v187
v_lshl_or_b32 v191,v199,16,v191
mavv 192,160,200
mavv 196,164,200
mavv 200,168,200
mavv 204,172,200
mavv 208,176,200
mavv 212,180,200
mavv 216,184,200
mavv 220,188,200
s_add_u32 s87,s87,1
s_cmp_lt_u32 s87,32
s_cbranch_scc1 L_rd_q
s_nop 15
s_nop 3
v_accvgpr_read_b32 v192,a192
v_accvgpr_read_b32 v193,a193
v_accvgpr_read_b32 v194,a194
v_accvgpr_read_b32 v195,a195
v_accvgpr_read_b32 v196,a196
v_accvgpr_read_b32 v197,a197
v_accvgpr_read_b32 v198,a198
v_accvgpr_read_b32 v199,a199
v_mul_f32_e32 v192,0x3e0293ee,v192
v_mul_f32_e32 v193,0x3e0293ee,v193
v_mul_f32_e32 v194,0x3e0293ee,v194
v_mul_f32_e32 v195,0x3e0293ee,v195
v_mul_f32_e32 v196,0x3e0293ee,v196
v_mul_f32_e32 v197,0x3e0293ee,v197
v_mul_f32_e32 v198,0x3e0293ee,v198
v_mul_f32_e32 v199,0x3e0293ee,v199
cv 192,192,193
cv 193,194,195
cv 194,196,197
cv 195,198,199
v_accvgpr_write_b32 a224,v192
v_accvgpr_write_b32 a225,v193
v_accvgpr_write_b32 a226,v194
v_accvgpr_write_b32 a227,v195
v_accvgpr_read_b32 v192,a200
v_accvgpr_read_b32 v193,a201
v_accvgpr_read_b32 v194,a202
v_accvgpr_read_b32 v195,a203
v_accvgpr_read_b32 v196,a204
v_accvgpr_read_b32 v197,a205
v_accvgpr_read_b32 v198,a206
v_accvgpr_read_b32 v199,a207
v_mul_f32_e32 v192,0x3e0293ee,v192
v_mul_f32_e32 v193,0x3e0293ee,v193
v_mul_f32_e32 v194,0x3e0293ee,v194
v_mul_f32_e32 v195,0x3e0293ee,v195
v_mul_f32_e32 v196,0x3e0293ee,v196
v_mul_f32_e32 v197,0x3e0293ee,v197
v_mul_f32_e32 v198,0x3e0293ee,v198
v_mul_f32_e32 v199,0x3e0293ee,v199
cv 192,192,193
cv 193,194,195
cv 194,196,197
cv 195,198,199
v_accvgpr_write_b32 a228,v192
v_accvgpr_write_b32 a229,v193
v_accvgpr_write_b32 a230,v194
v_accvgpr_write_b32 a231,v195
v_accvgpr_read_b32 v192,a208
v_accvgpr_read_b32 v193,a209
v_accvgpr_read_b32 v194,a210
v_accvgpr_read_b32 v195,a211
v_accvgpr_read_b32 v196,a212
v_accvgpr_read_b32 v197,a213
v_accvgpr_read_b32 v198,a214
v_accvgpr_read_b32 v199,a215
v_mul_f32_e32 v192,0x3e0293ee,v192
v_mul_f32_e32 v193,0x3e0293ee,v193
v_mul_f32_e32 v194,0x3e0293ee,v194
v_mul_f32_e32 v195,0x3e0293ee,v195
v_mul_f32_e32 v196,0x3e0293ee,v196
v_mul_f32_e32 v197,0x3e0293ee,v197
v_mul_f32_e32 v198,0x3e0293ee,v198
v_mul_f32_e32 v199,0x3e0293ee,v199
cv 192,192,193
cv 193,194,195
cv 194,196,197
cv 195,198,199
v_accvgpr_write_b32 a232,v192
v_accvgpr_write_b32 a233,v193
v_accvgpr_write_b32 a234,v194
v_accvgpr_write_b32 a235,v195
v_accvgpr_read_b32 v192,a216
v_accvgpr_read_b32 v193,a217
v_accvgpr_read_b32 v194,a218
v_accvgpr_read_b32 v195,a219
v_accvgpr_read_b32 v196,a220
v_accvgpr_read_b32 v197,a221
v_accvgpr_read_b32 v198,a222
v_accvgpr_read_b32 v199,a223
v_mul_f32_e32 v192,0x3e0293ee,v192
v_mul_f32_e32 v193,0x3e0293ee,v193
v_mul_f32_e32 v194,0x3e0293ee,v194
v_mul_f32_e32 v195,0x3e0293ee,v195
v_mul_f32_e32 v196,0x3e0293ee,v196
v_mul_f32_e32 v197,0x3e0293ee,v197
v_mul_f32_e32 v198,0x3e0293ee,v198
v_mul_f32_e32 v199,0x3e0293ee,v199
cv 192,192,193
cv 193,194,195
cv 194,196,197
cv 195,198,199
v_accvgpr_write_b32 a236,v192
v_accvgpr_write_b32 a237,v193
v_accvgpr_write_b32 a238,v194
v_accvgpr_write_b32 a239,v195
v_mov_b32_e32 v160,0
v_mov_b32_e32 v161,0
v_mov_b32_e32 v162,0
v_mov_b32_e32 v163,0
v_mov_b32_e32 v164,0
v_mov_b32_e32 v165,0
v_mov_b32_e32 v166,0
v_mov_b32_e32 v167,0
v_mov_b32_e32 v168,0
v_mov_b32_e32 v169,0
v_mov_b32_e32 v170,0
v_mov_b32_e32 v171,0
v_mov_b32_e32 v172,0
v_mov_b32_e32 v173,0
v_mov_b32_e32 v174,0
v_mov_b32_e32 v175,0
v_mov_b32_e32 v176,0
v_mov_b32_e32 v177,0
v_mov_b32_e32 v178,0
v_mov_b32_e32 v179,0
v_mov_b32_e32 v180,0
v_mov_b32_e32 v181,0
v_mov_b32_e32 v182,0
v_mov_b32_e32 v183,0
v_mov_b32_e32 v184,0
v_mov_b32_e32 v185,0
v_mov_b32_e32 v186,0
v_mov_b32_e32 v187,0
v_mov_b32_e32 v188,0
v_mov_b32_e32 v189,0
v_mov_b32_e32 v190,0
v_mov_b32_e32 v191,0
v_mov_b32_e32 v216,0xf149f2ca
v_mov_b32_e32 v211,v216
v_mov_b32_e32 v212,0
s_lshr_b32 s91,s88,5
s_mov_b32 s87,0
L_rd_a:
s_nop 15
s_nop 3
s_mul_i32 s89,s18,524288
s_lshl_b32 s90,s87,13
s_add_u32 s89,s89,s90
s_add_u32 s90,s89,16777216
la 64,244,90,0
la 68,244,90,1024
la 72,244,90,2048
la 76,244,90,3072
s_add_u32 s90,s89,16781312
la 80,244,90,0
la 84,244,90,1024
la 88,244,90,2048
la 92,244,90,3072
s_add_u32 s90,s89,33554432
la 96,244,90,0
la 100,244,90,1024
la 104,244,90,2048
la 108,244,90,3072
s_add_u32 s90,s89,33558528
la 112,244,90,0
la 116,244,90,1024
la 120,244,90,2048
la 124,244,90,3072
s_waitcnt vmcnt(0)
zvaa 192,64,224
mvaa 192,72,228
mvaa 192,80,232
mvaa 192,88,236
zvaa 196,68,224
mvaa 196,76,228
mvaa 196,84,232
mvaa 196,92,236
v_cmp_gt_i32_e32 vcc,0,v210
s_nop 2
v_cndmask_b32_e32 v192,v192,v216,vcc
v_cmp_gt_i32_e32 vcc,1,v210
s_nop 1
v_cndmask_b32_e32 v193,v193,v216,vcc
v_cmp_gt_i32_e32 vcc,2,v210
s_nop 1
v_cndmask_b32_e32 v194,v194,v216,vcc
v_cmp_gt_i32_e32 vcc,3,v210
s_nop 1
v_cndmask_b32_e32 v195,v195,v216,vcc
v_cmp_gt_i32_e32 vcc,16,v210
s_nop 1
v_cndmask_b32_e32 v196,v196,v216,vcc
v_cmp_gt_i32_e32 vcc,17,v210
s_nop 1
v_cndmask_b32_e32 v197,v197,v216,vcc
v_cmp_gt_i32_e32 vcc,18,v210
s_nop 1
v_cndmask_b32_e32 v198,v198,v216,vcc
v_cmp_gt_i32_e32 vcc,19,v210
s_nop 1
v_cndmask_b32_e32 v199,v199,v216,vcc
v_max3_f32 v213,v192,v193,v194
v_max3_f32 v214,v195,v196,v197
v_max3_f32 v213,v213,v198,v199
v_max_f32_e32 v213,v213,v214
v_mov_b32_e32 v214,v213
s_nop 1
v_permlane16_swap_b32_e32 v213,v214
v_max_f32_e32 v213,v213,v214
v_mov_b32_e32 v214,v213
s_nop 1
v_permlane32_swap_b32_e32 v213,v214
v_max_f32_e32 v213,v213,v214
v_max_f32_e32 v213,v213,v211
v_sub_f32_e32 v215,v211,v213
ex 215
v_mov_b32_e32 v211,v213
v_sub_f32_e32 v192,v192,v211
v_sub_f32_e32 v193,v193,v211
v_sub_f32_e32 v194,v194,v211
v_sub_f32_e32 v195,v195,v211
v_sub_f32_e32 v196,v196,v211
v_sub_f32_e32 v197,v197,v211
v_sub_f32_e32 v198,v198,v211
v_sub_f32_e32 v199,v199,v211
ex 192
ex 193
ex 194
ex 195
ex 196
ex 197
ex 198
ex 199
v_mul_f32_e32 v212,v215,v212
v_add_f32_e32 v212,v212,v192
v_add_f32_e32 v212,v212,v193
v_add_f32_e32 v212,v212,v194
v_add_f32_e32 v212,v212,v195
v_add_f32_e32 v212,v212,v196
v_add_f32_e32 v212,v212,v197
v_add_f32_e32 v212,v212,v198
v_add_f32_e32 v212,v212,v199
cv 192,192,193
cv 193,194,195
cv 194,196,197
cv 195,198,199
v_mul_f32_e32 v160,v215,v160
v_mul_f32_e32 v161,v215,v161
v_mul_f32_e32 v162,v215,v162
v_mul_f32_e32 v163,v215,v163
v_mul_f32_e32 v164,v215,v164
v_mul_f32_e32 v165,v215,v165
v_mul_f32_e32 v166,v215,v166
v_mul_f32_e32 v167,v215,v167
v_mul_f32_e32 v168,v215,v168
v_mul_f32_e32 v169,v215,v169
v_mul_f32_e32 v170,v215,v170
v_mul_f32_e32 v171,v215,v171
v_mul_f32_e32 v172,v215,v172
v_mul_f32_e32 v173,v215,v173
v_mul_f32_e32 v174,v215,v174
v_mul_f32_e32 v175,v215,v175
v_mul_f32_e32 v176,v215,v176
v_mul_f32_e32 v177,v215,v177
v_mul_f32_e32 v178,v215,v178
v_mul_f32_e32 v179,v215,v179
v_mul_f32_e32 v180,v215,v180
v_mul_f32_e32 v181,v215,v181
v_mul_f32_e32 v182,v215,v182
v_mul_f32_e32 v183,v215,v183
v_mul_f32_e32 v184,v215,v184
v_mul_f32_e32 v185,v215,v185
v_mul_f32_e32 v186,v215,v186
v_mul_f32_e32 v187,v215,v187
v_mul_f32_e32 v188,v215,v188
v_mul_f32_e32 v189,v215,v189
v_mul_f32_e32 v190,v215,v190
v_mul_f32_e32 v191,v215,v191
mvav 160,96,192
mvav 164,100,192
mvav 168,104,192
mvav 172,108,192
mvav 176,112,192
mvav 180,116,192
mvav 184,120,192
mvav 188,124,192
v_subrev_u32_e32 v210,32,v210
s_add_u32 s87,s87,1
s_cmp_le_u32 s87,s91
s_cbranch_scc1 L_rd_a
s_nop 15
s_nop 3
s_lshl_b32 s89,s88,2
s_add_u32 s89,s89,83886080
v_bfe_u32 v213,v244,2,6
buffer_load_dword v215,v213,s[8:11],s89 offen sc1
v_mov_b32_e32 v214,v212
s_nop 1
v_permlane16_swap_b32_e32 v212,v214
v_add_f32_e32 v212,v212,v214
v_mov_b32_e32 v214,v212
s_nop 1
v_permlane32_swap_b32_e32 v212,v214
v_add_f32_e32 v212,v212,v214
v_rcp_f32_e32 v212,v212
s_waitcnt vmcnt(0)
v_mul_f32_e32 v215,v215,v212
v_mul_f32_e32 v160,v215,v160
v_mul_f32_e32 v161,v215,v161
v_mul_f32_e32 v162,v215,v162
v_mul_f32_e32 v163,v215,v163
v_mul_f32_e32 v164,v215,v164
v_mul_f32_e32 v165,v215,v165
v_mul_f32_e32 v166,v215,v166
v_mul_f32_e32 v167,v215,v167
v_mul_f32_e32 v168,v215,v168
v_mul_f32_e32 v169,v215,v169
v_mul_f32_e32 v170,v215,v170
v_mul_f32_e32 v171,v215,v171
v_mul_f32_e32 v172,v215,v172
v_mul_f32_e32 v173,v215,v173
v_mul_f32_e32 v174,v215,v174
v_mul_f32_e32 v175,v215,v175
v_mul_f32_e32 v176,v215,v176
v_mul_f32_e32 v177,v215,v177
v_mul_f32_e32 v178,v215,v178
v_mul_f32_e32 v179,v215,v179
v_mul_f32_e32 v180,v215,v180
v_mul_f32_e32 v181,v215,v181
v_mul_f32_e32 v182,v215,v182
v_mul_f32_e32 v183,v215,v183
v_mul_f32_e32 v184,v215,v184
v_mul_f32_e32 v185,v215,v185
v_mul_f32_e32 v186,v215,v186
v_mul_f32_e32 v187,v215,v187
v_mul_f32_e32 v188,v215,v188
v_mul_f32_e32 v189,v215,v189
v_mul_f32_e32 v190,v215,v190
v_mul_f32_e32 v191,v215,v191
s_add_u32 s89,s95,4096
cv 160,160,161
cv 161,162,163
cv 162,164,165
cv 163,166,167
buffer_store_dwordx4 v[160:163],v244,s[8:11],s95 offen offset:0 sc0 sc1
cv 168,168,169
cv 169,170,171
cv 170,172,173
cv 171,174,175
buffer_store_dwordx4 v[168:171],v244,s[8:11],s95 offen offset:2048 sc0 sc1
cv 176,176,177
cv 177,178,179
cv 178,180,181
cv 179,182,183
buffer_store_dwordx4 v[176:179],v244,s[8:11],s89 offen offset:0 sc0 sc1
cv 184,184,185
cv 185,186,187
cv 186,188,189
cv 187,190,191
buffer_store_dwordx4 v[184:187],v244,s[8:11],s89 offen offset:2048 sc0 sc1
s_waitcnt vmcnt(0)
v_mov_b32_e32 v192,v160
v_accvgpr_read_b32 v160,a240
v_accvgpr_write_b32 a240,v192
v_mov_b32_e32 v193,v161
v_accvgpr_read_b32 v161,a241
v_accvgpr_write_b32 a241,v193
v_mov_b32_e32 v194,v162
v_accvgpr_read_b32 v162,a242
v_accvgpr_write_b32 a242,v194
v_mov_b32_e32 v195,v163
v_accvgpr_read_b32 v163,a243
v_accvgpr_write_b32 a243,v195
v_mov_b32_e32 v200,v168
v_accvgpr_read_b32 v168,a244
v_accvgpr_write_b32 a244,v200
v_mov_b32_e32 v201,v169
v_accvgpr_read_b32 v169,a245
v_accvgpr_write_b32 a245,v201
v_mov_b32_e32 v202,v170
v_accvgpr_read_b32 v170,a246
v_accvgpr_write_b32 a246,v202
v_mov_b32_e32 v203,v171
v_accvgpr_read_b32 v171,a247
v_accvgpr_write_b32 a247,v203
v_mov_b32_e32 v208,v176
v_accvgpr_read_b32 v176,a248
v_accvgpr_write_b32 a248,v208
v_mov_b32_e32 v209,v177
v_accvgpr_read_b32 v177,a249
v_accvgpr_write_b32 a249,v209
v_mov_b32_e32 v210,v178
v_accvgpr_read_b32 v178,a250
v_accvgpr_write_b32 a250,v210
v_mov_b32_e32 v211,v179
v_accvgpr_read_b32 v179,a251
v_accvgpr_write_b32 a251,v211
v_mov_b32_e32 v216,v184
v_accvgpr_read_b32 v184,a252
v_accvgpr_write_b32 a252,v216
v_mov_b32_e32 v217,v185
v_accvgpr_read_b32 v185,a253
v_accvgpr_write_b32 a253,v217
v_mov_b32_e32 v218,v186
v_accvgpr_read_b32 v186,a254
v_accvgpr_write_b32 a254,v218
v_mov_b32_e32 v219,v187
v_accvgpr_read_b32 v187,a255
v_accvgpr_write_b32 a255,v219
s_add_u32 s86,s86,1
s_cmp_lt_u32 s86,4
s_cbranch_scc1 L_rd_t
s_nop 15
s_nop 3
s_nop 1
s_cmp_eq_u32 s84,4
s_cbranch_scc1 L_redo_r4
s_cmp_lg_u32 s80,s81
s_branch L_redo_r5
L_k_ev:
L_v_ev:
buffer_load_dwordx4 a[252:255],v244,s[8:11],s35 offen sc1
s_branch L_k_evr
L_spbe:
s_mov_b32 s91,65536
L_splbe:
s_sleep 2
buffer_load_dword v253,v224,s[8:11],s53 offen sc1
s_waitcnt vmcnt(0)
v_cmp_ne_u32_e32 vcc,4,v253
s_nop 1
s_cbranch_vccz L_sprbe
s_sub_u32 s91,s91,1
s_cbranch_scc0 L_splbe
s_branch L_sprbe
L_spa3e:
s_mov_b32 s91,65536
L_spla3e:
s_sleep 2
buffer_load_dword v253,v242,s[8:11],s53 offen sc1
s_waitcnt vmcnt(0)
v_cmp_ne_u32_e32 vcc,4,v253
s_nop 1
s_cbranch_vccz L_spra3e
s_sub_u32 s91,s91,1
s_cbranch_scc0 L_spla3e
s_branch L_spra3e
L_spa4e:
s_mov_b32 s91,65536
L_spla4e:
s_sleep 2
buffer_load_dword v253,v242,s[8:11],s53 offen sc1
s_waitcnt vmcnt(0)
v_cmp_ne_u32_e32 vcc,4,v253
s_nop 1
s_cbranch_vccz L_spra4e
s_sub_u32 s91,s91,1
s_cbranch_scc0 L_spla4e
s_branch L_spra4e
L_spa5e:
s_mov_b32 s91,65536
L_spla5e:
s_sleep 2
buffer_load_dword v253,v242,s[8:11],s53 offen sc1
s_waitcnt vmcnt(0)
v_cmp_ne_u32_e32 vcc,4,v253
s_nop 1
s_cbranch_vccz L_spra5e
s_sub_u32 s91,s91,1
s_cbranch_scc0 L_spla5e
s_branch L_spra5e
L_spa6e:
s_mov_b32 s91,65536
L_spla6e:
s_sleep 2
buffer_load_dword v253,v242,s[8:11],s53 offen sc1
s_waitcnt vmcnt(0)
v_cmp_ne_u32_e32 vcc,4,v253
s_nop 1
s_cbranch_vccz L_spra6e
s_sub_u32 s91,s91,1
s_cbranch_scc0 L_spla6e
s_branch L_spra6e
L_spa7e:
s_mov_b32 s91,65536
L_spla7e:
s_sleep 2
buffer_load_dword v253,v242,s[8:11],s53 offen sc1
s_waitcnt vmcnt(0)
v_cmp_ne_u32_e32 vcc,4,v253
s_nop 1
s_cbranch_vccz L_spra7e
s_sub_u32 s91,s91,1
s_cbranch_scc0 L_spla7e
s_branch L_spra7e
L_spa8e:
s_mov_b32 s91,65536
L_spla8e:
s_sleep 2
buffer_load_dword v253,v242,s[8:11],s53 offen sc1
s_waitcnt vmcnt(0)
v_cmp_ne_u32_e32 vcc,4,v253
s_nop 1
s_cbranch_vccz L_spra8e
s_sub_u32 s91,s91,1
s_cbranch_scc0 L_spla8e
s_branch L_spra8e
L_spa9e:
s_mov_b32 s91,65536
L_spla9e:
s_sleep 2
buffer_load_dword v253,v242,s[8:11],s53 offen sc1
s_waitcnt vmcnt(0)
v_cmp_ne_u32_e32 vcc,4,v253
s_nop 1
s_cbranch_vccz L_spra9e
s_sub_u32 s91,s91,1
s_cbranch_scc0 L_spla9e
s_branch L_spra9e
L_spa10e:
s_mov_b32 s91,65536
L_spla10e:
s_sleep 2
buffer_load_dword v253,v242,s[8:11],s53 offen sc1
s_waitcnt vmcnt(0)
v_cmp_ne_u32_e32 vcc,4,v253
s_nop 1
s_cbranch_vccz L_spra10e
s_sub_u32 s91,s91,1
s_cbranch_scc0 L_spla10e
s_branch L_spra10e
L_sppe:
s_mov_b32 s91,65536
L_splpe:
s_sleep 2
buffer_load_dword v253,v242,s[8:11],s53 offen sc1
s_waitcnt vmcnt(0)
v_cmp_ne_u32_e32 vcc,4,v253
s_nop 1
s_cbranch_vccz L_sprpe
s_sub_u32 s91,s91,1
s_cbranch_scc0 L_splpe
s_branch L_sprpe
L_spx0e:
s_mov_b32 s91,65536
L_splx0e:
lv 16,244,82,0
lv 20,244,82,1024
lv 24,244,82,2048
lv 28,244,82,3072
s_waitcnt vmcnt(0)
v_max_u32_e32 v248,v16,v17
v_max3_u32 v248,v248,v18,v19
v_max3_u32 v248,v248,v20,v21
v_max3_u32 v248,v248,v22,v23
v_max3_u32 v248,v248,v24,v25
v_max3_u32 v248,v248,v26,v27
v_max3_u32 v248,v248,v28,v29
v_max3_u32 v248,v248,v30,v31
v_cmp_eq_u32_e32 vcc,-1,v248
s_nop 1
s_cbranch_vccz L_sprx0e
s_sub_u32 s91,s91,1
s_cbranch_scc0 L_splx0e
s_branch L_sprx0e
L_spx1e:
s_mov_b32 s91,65536
L_splx1e:
lv 48,244,83,0
lv 52,244,83,1024
lv 56,244,83,2048
lv 60,244,83,3072
s_waitcnt vmcnt(0)
v_max_u32_e32 v248,v48,v49
v_max3_u32 v248,v248,v50,v51
v_max3_u32 v248,v248,v52,v53
v_max3_u32 v248,v248,v54,v55
v_max3_u32 v248,v248,v56,v57
v_max3_u32 v248,v248,v58,v59
v_max3_u32 v248,v248,v60,v61
v_max3_u32 v248,v248,v62,v63
v_cmp_eq_u32_e32 vcc,-1,v248
s_nop 1
s_cbranch_vccz L_sprx1e
s_sub_u32 s91,s91,1
s_cbranch_scc0 L_splx1e
s_branch L_sprx1e
L_spx2e:
s_mov_b32 s91,65536
L_splx2e:
lv 80,244,84,0
lv 84,244,84,1024
lv 88,244,84,2048
lv 92,244,84,3072
s_waitcnt vmcnt(0)
v_max_u32_e32 v248,v80,v81
v_max3_u32 v248,v248,v82,v83
v_max3_u32 v248,v248,v84,v85
v_max3_u32 v248,v248,v86,v87
v_max3_u32 v248,v248,v88,v89
v_max3_u32 v248,v248,v90,v91
v_max3_u32 v248,v248,v92,v93
v_max3_u32 v248,v248,v94,v95
v_cmp_eq_u32_e32 vcc,-1,v248
s_nop 1
s_cbranch_vccz L_sprx2e
s_sub_u32 s91,s91,1
s_cbranch_scc0 L_splx2e
s_branch L_sprx2e
L_spbo:
s_mov_b32 s91,65536
L_splbo:
s_sleep 2
buffer_load_dword v253,v224,s[8:11],s53 offen sc1
s_waitcnt vmcnt(0)
v_cmp_ne_u32_e32 vcc,4,v253
s_nop 1
s_cbranch_vccz L_sprbo
s_sub_u32 s91,s91,1
s_cbranch_scc0 L_splbo
s_branch L_sprbo
L_spa3o:
s_mov_b32 s91,65536
L_spla3o:
s_sleep 2
buffer_load_dword v253,v242,s[8:11],s53 offen sc1
s_waitcnt vmcnt(0)
v_cmp_ne_u32_e32 vcc,4,v253
s_nop 1
s_cbranch_vccz L_spra3o
s_sub_u32 s91,s91,1
s_cbranch_scc0 L_spla3o
s_branch L_spra3o
L_spa4o:
s_mov_b32 s91,65536
L_spla4o:
s_sleep 2
buffer_load_dword v253,v242,s[8:11],s53 offen sc1
s_waitcnt vmcnt(0)
v_cmp_ne_u32_e32 vcc,4,v253
s_nop 1
s_cbranch_vccz L_spra4o
s_sub_u32 s91,s91,1
s_cbranch_scc0 L_spla4o
s_branch L_spra4o
L_spa5o:
s_mov_b32 s91,65536
L_spla5o:
s_sleep 2
buffer_load_dword v253,v242,s[8:11],s53 offen sc1
s_waitcnt vmcnt(0)
v_cmp_ne_u32_e32 vcc,4,v253
s_nop 1
s_cbranch_vccz L_spra5o
s_sub_u32 s91,s91,1
s_cbranch_scc0 L_spla5o
s_branch L_spra5o
L_spa6o:
s_mov_b32 s91,65536
L_spla6o:
s_sleep 2
buffer_load_dword v253,v242,s[8:11],s53 offen sc1
s_waitcnt vmcnt(0)
v_cmp_ne_u32_e32 vcc,4,v253
s_nop 1
s_cbranch_vccz L_spra6o
s_sub_u32 s91,s91,1
s_cbranch_scc0 L_spla6o
s_branch L_spra6o
L_spa7o:
s_mov_b32 s91,65536
L_spla7o:
s_sleep 2
buffer_load_dword v253,v242,s[8:11],s53 offen sc1
s_waitcnt vmcnt(0)
v_cmp_ne_u32_e32 vcc,4,v253
s_nop 1
s_cbranch_vccz L_spra7o
s_sub_u32 s91,s91,1
s_cbranch_scc0 L_spla7o
s_branch L_spra7o
L_spa8o:
s_mov_b32 s91,65536
L_spla8o:
s_sleep 2
buffer_load_dword v253,v242,s[8:11],s53 offen sc1
s_waitcnt vmcnt(0)
v_cmp_ne_u32_e32 vcc,4,v253
s_nop 1
s_cbranch_vccz L_spra8o
s_sub_u32 s91,s91,1
s_cbranch_scc0 L_spla8o
s_branch L_spra8o
L_sppo:
s_mov_b32 s91,65536
L_splpo:
s_sleep 2
buffer_load_dword v253,v242,s[8:11],s53 offen sc1
s_waitcnt vmcnt(0)
v_cmp_ne_u32_e32 vcc,4,v253
s_nop 1
s_cbranch_vccz L_sprpo
s_sub_u32 s91,s91,1
s_cbranch_scc0 L_splpo
s_branch L_sprpo
L_spx0o:
s_mov_b32 s91,65536
L_splx0o:
lv 16,244,82,0
lv 20,244,82,1024
lv 24,244,82,2048
lv 28,244,82,3072
s_waitcnt vmcnt(0)
v_max_u32_e32 v248,v16,v17
v_max3_u32 v248,v248,v18,v19
v_max3_u32 v248,v248,v20,v21
v_max3_u32 v248,v248,v22,v23
v_max3_u32 v248,v248,v24,v25
v_max3_u32 v248,v248,v26,v27
v_max3_u32 v248,v248,v28,v29
v_max3_u32 v248,v248,v30,v31
v_cmp_eq_u32_e32 vcc,-1,v248
s_nop 1
s_cbranch_vccz L_sprx0o
s_sub_u32 s91,s91,1
s_cbranch_scc0 L_splx0o
s_branch L_sprx0o
L_spx1o:
s_mov_b32 s91,65536
L_splx1o:
lv 48,244,83,0
lv 52,244,83,1024
lv 56,244,83,2048
lv 60,244,83,3072
s_waitcnt vmcnt(0)
v_max_u32_e32 v248,v48,v49
v_max3_u32 v248,v248,v50,v51
v_max3_u32 v248,v248,v52,v53
v_max3_u32 v248,v248,v54,v55
v_max3_u32 v248,v248,v56,v57
v_max3_u32 v248,v248,v58,v59
v_max3_u32 v248,v248,v60,v61
v_max3_u32 v248,v248,v62,v63
v_cmp_eq_u32_e32 vcc,-1,v248
s_nop 1
s_cbranch_vccz L_sprx1o
s_sub_u32 s91,s91,1
s_cbranch_scc0 L_splx1o
s_branch L_sprx1o
L_spx2o:
s_mov_b32 s91,65536
L_splx2o:
lv 80,244,84,0
lv 84,244,84,1024
lv 88,244,84,2048
lv 92,244,84,3072
s_waitcnt vmcnt(0)
v_max_u32_e32 v248,v80,v81
v_max3_u32 v248,v248,v82,v83
v_max3_u32 v248,v248,v84,v85
v_max3_u32 v248,v248,v86,v87
v_max3_u32 v248,v248,v88,v89
v_max3_u32 v248,v248,v90,v91
v_max3_u32 v248,v248,v92,v93
v_max3_u32 v248,v248,v94,v95
v_cmp_eq_u32_e32 vcc,-1,v248
s_nop 1
s_cbranch_vccz L_sprx2o
s_sub_u32 s91,s91,1
s_cbranch_scc0 L_splx2o
s_branch L_sprx2o
L_att_end:
s_endpgm
.p2align 12
.purgem cv
.purgem cvaa
.purgem cvvv
.purgem dl
.purgem ds
.purgem ex
.purgem la
.purgem lv
.purgem maaa
.purgem maav
.purgem mavv
.purgem mvaa
.purgem mvav
.purgem mvva
.purgem mvvv
.purgem ra
.purgem rv
.purgem ta
.purgem tv
.purgem zaaa
.purgem zaav
.purgem zvaa
.purgem zvav
.purgem zvva
.purgem zvvv
)"
        :: "s"(__builtin_amdgcn_kernarg_segment_ptr()), "s"(blockIdx.x), "v"(tid)
        : "v0", "v1", "v2", "v3", "v4", "v5", "v6", "v7", "v8", "v9", "v10", "v11", "v12", "v13", "v14", "v15", "v16", "v17", "v18", "v19", "v20", "v21", "v22", "v23", "v24", "v25", "v26", "v27", "v28", "v29", "v30", "v31", "v32", "v33", "v34", "v35", "v36", "v37", "v38", "v39", "v40", "v41", "v42", "v43", "v44", "v45", "v46", "v47", "v48", "v49", "v50", "v51", "v52", "v53", "v54", "v55", "v56", "v57", "v58", "v59", "v60", "v61", "v62", "v63", "v64", "v65", "v66", "v67", "v68", "v69", "v70", "v71", "v72", "v73", "v74", "v75", "v76", "v77", "v78", "v79", "v80", "v81", "v82", "v83", "v84", "v85", "v86", "v87", "v88", "v89", "v90", "v91", "v92", "v93", "v94", "v95", "v96", "v97", "v98", "v99", "v100", "v101", "v102", "v103", "v104", "v105", "v106", "v107", "v108", "v109", "v110", "v111", "v112", "v113", "v114", "v115", "v116", "v117", "v118", "v119", "v120", "v121", "v122", "v123", "v124", "v125", "v126", "v127", "v128", "v129", "v130", "v131", "v132", "v133", "v134", "v135", "v136", "v137", "v138", "v139", "v140", "v141", "v142", "v143", "v144", "v145", "v146", "v147", "v148", "v149", "v150", "v151", "v152", "v153", "v154", "v155", "v156", "v157", "v158", "v159", "v160", "v161", "v162", "v163", "v164", "v165", "v166", "v167", "v168", "v169", "v170", "v171", "v172", "v173", "v174", "v175", "v176", "v177", "v178", "v179", "v180", "v181", "v182", "v183", "v184", "v185", "v186", "v187", "v188", "v189", "v190", "v191", "v192", "v193", "v194", "v195", "v196", "v197", "v198", "v199", "v200", "v201", "v202", "v203", "v204", "v205", "v206", "v207", "v208", "v209", "v210", "v211", "v212", "v213", "v214", "v215", "v216", "v217", "v218", "v219", "v220", "v221", "v222", "v223", "v224", "v225", "v226", "v227", "v228", "v229", "v230", "v231", "v232", "v233", "v234", "v235", "v236", "v237", "v238", "v239", "v240", "v241", "v242", "v243", "v244", "v245", "v246", "v247", "v248", "v249", "v250", "v251", "v252", "v253", "v254", "a0", "a1", "a2", "a3", "a4", "a5", "a6", "a7", "a8", "a9", "a10", "a11", "a12", "a13", "a14", "a15", "a16", "a17", "a18", "a19", "a20", "a21", "a22", "a23", "a24", "a25", "a26", "a27", "a28", "a29", "a30", "a31", "a32", "a33", "a34", "a35", "a36", "a37", "a38", "a39", "a40", "a41", "a42", "a43", "a44", "a45", "a46", "a47", "a48", "a49", "a50", "a51", "a52", "a53", "a54", "a55", "a56", "a57", "a58", "a59", "a60", "a61", "a62", "a63", "a64", "a65", "a66", "a67", "a68", "a69", "a70", "a71", "a72", "a73", "a74", "a75", "a76", "a77", "a78", "a79", "a80", "a81", "a82", "a83", "a84", "a85", "a86", "a87", "a88", "a89", "a90", "a91", "a92", "a93", "a94", "a95", "a96", "a97", "a98", "a99", "a100", "a101", "a102", "a103", "a104", "a105", "a106", "a107", "a108", "a109", "a110", "a111", "a112", "a113", "a114", "a115", "a116", "a117", "a118", "a119", "a120", "a121", "a122", "a123", "a124", "a125", "a126", "a127", "a128", "a129", "a130", "a131", "a132", "a133", "a134", "a135", "a136", "a137", "a138", "a139", "a140", "a141", "a142", "a143", "a144", "a145", "a146", "a147", "a148", "a149", "a150", "a151", "a152", "a153", "a154", "a155", "a156", "a157", "a158", "a159", "a160", "a161", "a162", "a163", "a164", "a165", "a166", "a167", "a168", "a169", "a170", "a171", "a172", "a173", "a174", "a175", "a176", "a177", "a178", "a179", "a180", "a181", "a182", "a183", "a184", "a185", "a186", "a187", "a188", "a189", "a190", "a191", "a192", "a193", "a194", "a195", "a196", "a197", "a198", "a199", "a200", "a201", "a202", "a203", "a204", "a205", "a206", "a207", "a208", "a209", "a210", "a211", "a212", "a213", "a214", "a215", "a216", "a217", "a218", "a219", "a220", "a221", "a222", "a223", "a224", "a225", "a226", "a227", "a228", "a229", "a230", "a231", "a232", "a233", "a234", "a235", "a236", "a237", "a238", "a239", "a240", "a241", "a242", "a243", "a244", "a245", "a246", "a247", "a248", "a249", "a250", "a251", "a252", "a253", "a254", "a255", "s4", "s5", "s6", "s7", "s8", "s9", "s10", "s11", "s12", "s13", "s14", "s15", "s16", "s17", "s18", "s19", "s20", "s21", "s22", "s23", "s24", "s25", "s26", "s27", "s28", "s29", "s30", "s31", "s32", "s33", "s34", "s35", "s36", "s37", "s38", "s39", "s40", "s41", "s42", "s43", "s44", "s45", "s46", "s47", "s48", "s49", "s50", "s51", "s52", "s53", "s54", "s55", "s56", "s57", "s58", "s59", "s60", "s61", "s62", "s63", "s64", "s65", "s66", "s67", "s68", "s69", "s70", "s71", "s72", "s73", "s74", "s75", "s76", "s77", "s78", "s79", "s80", "s81", "s82", "s83", "s84", "s85", "s86", "s87", "s88", "s89", "s90", "s91", "s92", "s93", "s94", "s95", "s96", "s97", "s98", "s99", "s100", "s101", "vcc", "scc", "m0", "memory");
}

extern "C" void kernel_launch(void* const* d_in, const int* in_sizes, int n_in, void* d_out, int out_size, void* d_ws, size_t ws_size, hipStream_t stream) {
    (void)in_sizes; (void)n_in; (void)out_size; (void)ws_size;
    char* ws = (char*)d_ws + 0ull;
    Args a;
    a.p[0] = d_in[0]; a.p[1] = d_in[1]; a.p[2] = d_in[2]; a.p[3] = d_in[3]; a.p[4] = d_in[4]; a.p[5] = d_in[5]; a.p[6] = d_in[7]; a.p[7] = d_in[8]; a.p[8] = d_in[9]; a.p[9] = d_out; a.p[10] = ws;
    mega<<<256, 256, 0, stream>>>(a);
}
```

```cpp
#include <hip/hip_runtime.h>
#include <hip/hip_bf16.h>
#include <stdint.h>
#include <type_traits>

#define LAS __attribute__((address_space(3)))
struct Args { const void* p[11]; };
__global__ __launch_bounds__(256, 1)
void mega(Args args) {
    __shared__ __attribute__((aligned(1024))) char smem[163840];
    const unsigned tid = threadIdx.x + (unsigned)(uintptr_t)(LAS char*)smem;
    asm volatile(
R"(.macro cv a,b,c
v_cvt_pk_bf16_f32 v\a,v\b,v\c
.endm
.macro cvaa a,b,c,d
v_mfma_f32_16x16x32_bf16 v[\a:\a+3],a[\b:\b+3],a[\c:\c+3],v[\d:\d+3]
.endm
.macro cvvv a,b,c,d
v_mfma_f32_16x16x32_bf16 v[\a:\a+3],v[\b:\b+3],v[\c:\c+3],v[\d:\d+3]
.endm
.macro dl a,b,c,d
buffer_load_dwordx4 v\a,s[\b:\b+3],s\c offen offset:\d lds
.endm
.macro ds a,b,c,d
buffer_load_dwordx4 v\a,s[\b:\b+3],s\c offen offset:\d sc1 lds
.endm
.macro ex a
v_exp_f32_e32 v\a,v\a
.endm
.macro la a,b,c,d
buffer_load_dwordx4 a[\a:\a+3],v\b,s[8:11],s\c offen offset:\d sc1
.endm
.macro lv a,b,c,d
buffer_load_dwordx4 v[\a:\a+3],v\b,s[8:11],s\c offen offset:\d sc1
.endm
.macro maaa a,b,c
v_mfma_f32_16x16x32_bf16 a[\a:\a+3],a[\b:\b+3],a[\c:\c+3],a[\a:\a+3]
.endm
.macro maav a,b,c
v_mfma_f32_16x16x32_bf16 a[\a:\a+3],a[\b:\b+3],v[\c:\c+3],a[\a:\a+3]
.endm
.macro mavv a,b,c
v_mfma_f32_16x16x32_bf16 a[\a:\a+3],v[\b:\b+3],v[\c:\c+3],a[\a:\a+3]
.endm
.macro mvaa a,b,c
v_mfma_f32_16x16x32_bf16 v[\a:\a+3],a[\b:\b+3],a[\c:\c+3],v[\a:\a+3]
.endm
.macro mvav a,b,c
v_mfma_f32_16x16x32_bf16 v[\a:\a+3],a[\b:\b+3],v[\c:\c+3],v[\a:\a+3]
.endm
.macro mvva a,b,c
v_mfma_f32_16x16x32_bf16 v[\a:\a+3],v[\b:\b+3],a[\c:\c+3],v[\a:\a+3]
.endm
.macro mvvv a,b,c
v_mfma_f32_16x16x32_bf16 v[\a:\a+3],v[\b:\b+3],v[\c:\c+3],v[\a:\a+3]
.endm
.macro ra a,b,c
ds_read_b128 a[\a:\a+3],v\b offset:\c
.endm
.macro rv a,b,c
ds_read_b128 v[\a:\a+3],v\b offset:\c
.endm
.macro ta a,b,c
ds_read_b64_tr_b16 a[\a:\a+1],v\b offset:\c
.endm
.macro tv a,b,c
ds_read_b64_tr_b16 v[\a:\a+1],v\b offset:\c
.endm
.macro zaaa a,b,c
v_mfma_f32_16x16x32_bf16 a[\a:\a+3],a[\b:\b+3],a[\c:\c+3],0
.endm
.macro zaav a,b,c
v_mfma_f32_16x16x32_bf16 a[\a:\a+3],a[\b:\b+3],v[\c:\c+3],0
.endm
.macro zvaa a,b,c
v_mfma_f32_16x16x32_bf16 v[\a:\a+3],a[\b:\b+3],a[\c:\c+3],0
.endm
.macro zvav a,b,c
v_mfma_f32_16x16x32_bf16 v[\a:\a+3],a[\b:\b+3],v[\c:\c+3],0
.endm
.macro zvva a,b,c
v_mfma_f32_16x16x32_bf16 v[\a:\a+3],v[\b:\b+3],a[\c:\c+3],0
.endm
.macro zvvv a,b,c
v_mfma_f32_16x16x32_bf16 v[\a:\a+3],v[\b:\b+3],v[\c:\c+3],0
.endm
L_begin:
s_load_dwordx2 s[28:29],%0,72
s_load_dwordx16 s[68:83],%0,0
s_load_dwordx2 s[84:85],%0,64
s_load_dwordx2 s[8:9],%0,80
v_lshrrev_b32_e32 v246,6,%2
v_and_b32_e32 v247,63,%2
v_lshlrev_b32_e32 v244,4,v247
v_readfirstlane_b32 s12,v246
s_and_b32 s20,%1,7
s_lshr_b32 s21,%1,3
s_getpc_b64 s[96:97]
L_att_pc:
s_lshl_b32 s86,s21,2
s_add_u32 s86,s86,s12
s_mul_i32 s86,s86,512
v_lshl_add_u32 v246,v247,7,s86
v_min_u32_e32 v246,L_att_end-L_att_pc-4,v246
s_mov_b64 exec,15
global_load_dword a188,v246,s[96:97]
s_mov_b64 exec,-1
s_and_b32 s19,s21,7
s_lshr_b32 s86,s21,3
s_lshl_b32 s18,s20,2
s_add_u32 s18,s18,s86
v_lshrrev_b32_e32 v248,3,v247
v_and_b32_e32 v250,112,v244
v_and_b32_e32 v249,48,v247
v_xor_b32_e32 v249,v249,v250
v_and_b32_e32 v251,16,v247
v_lshlrev_b32_e32 v251,1,v251
v_xor_b32_e32 v250,v250,v251
v_lshl_add_u32 v224,v248,10,v249
v_xor_b32_e32 v225,64,v224
v_add_u32_e32 v227,21504,v225
v_add_u32_e32 v226,14336,v224
v_add_u32_e32 v225,7168,v225
v_lshl_add_u32 v228,v248,13,v250
v_xor_b32_e32 v229,64,v228
v_add_u32_e32 v231,193536,v229
v_add_u32_e32 v230,129024,v228
v_add_u32_e32 v229,64512,v229
s_lshl_b32 s86,s19,2
s_add_u32 s86,s86,s12
s_lshl_b32 s52,s86,16
s_lshr_b32 s52,s86,1
s_lshl_b32 s52,s52,16
s_and_b32 s87,s12,1
s_lshl_b32 s87,s87,20
s_add_u32 s52,s52,s87
s_bfe_u32 s87,s21,65539
s_lshl_b32 s88,s20,1
s_add_u32 s87,s87,s88
s_lshl_b32 s87,s87,2
s_add_u32 s87,s87,s12
s_lshl_b32 s53,s87,7
s_mov_b32 s54,s53
s_mov_b32 s39,131072
s_mov_b32 s43,131072
s_mov_b32 s47,131072
s_mov_b32 s51,131072
s_mov_b32 s38,2097152
s_mov_b32 s42,4194304
s_mov_b32 s46,4194304
s_mov_b32 s56,32768
s_mov_b32 s60,-32640
s_mov_b32 s57,262144
s_mov_b32 s58,262144
s_lshl_b32 s98,s12,13
s_lshl_b32 s62,s12,12
s_add_u32 s62,s62,98304
s_lshl_b32 s35,%1,2
s_add_u32 s35,s35,83896320
v_lshrrev_b32_e32 v48,2,v247
v_and_b32_e32 v49,3,v247
v_lshlrev_b32_e32 v49,7,v49
v_lshl_add_u32 v48,v48,13,v49
s_lshl_b32 s89,s86,14
s_and_b32 s90,s53,7680
s_add_u32 s90,s90,s89
s_lshr_b32 s89,s21,3
s_lshl_b32 s89,s89,2
s_add_u32 s89,s89,s12
s_lshl_b32 s89,s89,14
s_lshl_b32 s91,s19,18
s_add_u32 s89,s89,s91
s_cmp_ge_u32 s21,16
s_waitcnt lgkmcnt(0)
s_mov_b64 s[36:37],s[70:71]
s_cselect_b64 s[40:41],s[76:77],s[74:75]
s_mov_b64 s[44:45],s[40:41]
s_bfm_b64 exec,8,0
buffer_load_dword a189,v48,s[40:43],s90 offen
s_mov_b64 exec,-1
s_and_b32 s9,s9,65535
s_mov_b32 s10,83951616
s_mov_b32 s11,131072
buffer_load_dword v44,off,s[8:11],s35 sc1
buffer_load_dword v45,off,s[8:11],s35 offset:1024 sc1
s_add_u32 m0,s62,0
s_nop 0
dl 228,40,53,0
dl 229,40,53,1024
s_add_u32 s53,s53,s57
dl 230,44,54,2048
dl 231,44,54,3072
s_add_u32 s54,s54,s57
s_add_u32 m0,s98,0
s_nop 0
dl 224,36,52,0
dl 225,36,52,1024
dl 226,36,52,2048
dl 227,36,52,3072
s_add_u32 s52,s52,s56
s_add_u32 m0,s98,4096
s_nop 0
dl 224,36,52,0
dl 225,36,52,1024
dl 226,36,52,2048
dl 227,36,52,3072
s_add_u32 s52,s52,s60
v_and_b32_e32 v246,15,v247
v_lshlrev_b32_e32 v248,3,v247
v_xor_b32_e32 v248,v248,v247
v_and_b32_e32 v248,112,v248
v_lshl_add_u32 v232,v246,7,v248
s_and_b32 s86,s98,16384
v_add_u32_e32 v232,s86,v232
v_xor_b32_e32 v233,64,v232
v_and_b32_e32 v246,12,v247
v_lshlrev_b32_e32 v246,5,v246
v_and_b32_e32 v248,3,v247
v_lshl_add_u32 v246,v248,3,v246
v_and_b32_e32 v248,24,v247
v_lshl_add_u32 v246,v248,2,v246
s_and_b32 s87,s98,8192
s_add_u32 s87,s87,98304
v_add_u32_e32 v246,s87,v246
v_and_b32_e32 v248,48,v247
v_lshl_add_u32 v234,v248,6,v246
v_xor_b32_e32 v235,32,v234
v_xor_b32_e32 v236,64,v234
v_xor_b32_e32 v237,96,v234
s_mov_b64 exec,0
s_cmp_ge_u32 s21,16
s_cbranch_scc1 L_vbV
s_cmp_eq_u32 s12,0
s_cbranch_scc1 L_vs1
s_cmp_eq_u32 s12,1
s_cbranch_scc1 L_vs2
s_cmp_eq_u32 s12,2
s_cbranch_scc1 L_vs3
s_branch L_vs8
L_vbV:
s_cmp_eq_u32 s12,0
s_cbranch_scc1 L_vs4
s_cmp_eq_u32 s12,1
s_cbranch_scc1 L_vs5
s_cmp_eq_u32 s12,2
s_cbranch_scc1 L_vs6
s_branch L_vs7
L_vr0:
s_mov_b64 exec,-1
s_add_u32 m0,s62,16384
s_nop 0
dl 228,40,53,0
dl 229,40,53,1024
s_add_u32 s53,s53,s58
dl 230,44,54,2048
dl 231,44,54,3072
s_add_u32 s54,s54,s58
s_add_u32 m0,s98,32768
s_nop 0
dl 224,36,52,0
dl 225,36,52,1024
dl 226,36,52,2048
dl 227,36,52,3072
s_add_u32 s52,s52,s56
s_waitcnt vmcnt(20)
s_cmp_eq_u32 s12,0
s_cbranch_scc1 L_rq0
s_branch L_rqx
L_vr0s:
s_setpc_b64 s[4:5]
L_rq0:
v_readfirstlane_b32 s32,v44
v_readfirstlane_b32 s86,v45
s_add_u32 s32,s32,1
s_cmp_eq_u32 s32,s86
s_addc_u32 s32,s32,0
v_mov_b32_e32 v44,s32
v_mov_b32_e32 v45,0
v_lshlrev_b32_e32 v46,3,v244
v_mov_b32_e32 v47,0
s_lshl_b32 s86,%1,7
s_add_u32 s87,s86,83914752
s_and_b32 s86,s86,16383
s_add_u32 s86,s86,83898368
s_mov_b64 exec,1
buffer_store_dword v44,off,s[8:11],s35 sc0 sc1
v_mov_b32_e32 v48,0
v_mov_b32_e32 v49,0
buffer_store_dwordx4 v[46:49],off,s[8:11],s86 sc0 sc1
buffer_store_dwordx4 v[46:49],off,s[8:11],s87 sc0 sc1
s_cmp_lg_u32 %1,0
s_cbranch_scc1 L_rqy
s_mov_b64 exec,65535
s_mov_b32 s86,83894272
buffer_store_dword v45,v46,s[8:11],s86 offen sc0 sc1
L_rqy:
s_mov_b64 exec,-1
L_rqx:
s_cmp_ge_u32 s21,16
s_cbranch_scc1 L_vtile
s_waitcnt vmcnt(12)
s_barrier
ta 64,234,0
ta 66,234,512
ra 0,232,0
ta 68,235,0
ta 70,235,512
ra 4,232,2048
ra 16,232,8192
ra 20,232,10240
s_add_u32 m0,s62,32768
v_mov_b32_e32 v216,0
dl 228,40,53,0
v_mov_b32_e32 v217,0
v_mov_b32_e32 v218,0
v_mov_b32_e32 v219,0
v_mov_b32_e32 v220,0
v_mov_b32_e32 v221,0
v_mov_b32_e32 v222,0
v_mov_b32_e32 v223,0
v_mov_b32_e32 v212,0
v_mov_b32_e32 v213,0
v_mov_b32_e32 v214,0
v_mov_b32_e32 v215,0
v_mov_b32_e32 v208,0
v_mov_b32_e32 v209,0
v_mov_b32_e32 v210,0
v_mov_b32_e32 v211,0
v_mov_b32_e32 v152,0
v_mov_b32_e32 v153,0
v_mov_b32_e32 v154,0
v_mov_b32_e32 v155,0
v_mov_b32_e32 v156,0
s_waitcnt lgkmcnt(5)
zaaa 128,64,0
s_lshl_b32 s4,%1,1
ta 72,236,0
ta 74,236,512
ta 76,237,0
ta 78,237,512
ta 80,234,4096
s_waitcnt lgkmcnt(8)
zaaa 132,68,0
s_add_u32 s4,s4,s12
ta 82,234,4608
v_mov_b32_e32 v157,0
s_waitcnt lgkmcnt(6)
zaaa 140,68,4
s_mul_i32 s4,s4,1280
ta 84,235,4096
ta 86,235,4608
zaaa 136,64,4
dl 229,40,53,1024
zvaa 96,64,16
s_add_u32 s53,s53,s57
v_mov_b32_e32 v158,0
v_mov_b32_e32 v159,0
zvaa 100,68,16
s_mov_b32 s55,s4
v_mov_b32_e32 v148,0
v_mov_b32_e32 v149,0
zvaa 108,68,20
s_mul_i32 s4,%1,1792
v_mov_b32_e32 v150,0
v_mov_b32_e32 v151,0
zvaa 104,64,20
dl 230,44,54,2048
s_waitcnt lgkmcnt(6)
zvaa 120,72,20
s_lshl_b32 s88,%1,2
ra 32,233,0
v_mov_b32_e32 v144,0
s_waitcnt lgkmcnt(5)
zvaa 124,76,20
s_waitcnt vmcnt(11)
s_barrier
ra 8,232,4096
zvaa 116,76,16
s_cmp_eq_u32 s12,2
s_cselect_b32 s55,s4,s55
ra 12,232,6144
v_mov_b32_e32 v145,0
zvaa 112,72,16
dl 231,44,54,3072
zaaa 152,72,4
s_add_u32 m0,s98,36864
ra 24,232,12288
s_add_u32 s54,s54,s57
zaaa 156,76,4
s_lshl_b32 s4,%1,14
ra 28,232,14336
v_mov_b32_e32 v146,0
zaaa 148,76,0
s_add_u32 s88,s88,s12
ra 36,233,2048
v_mov_b32_e32 v147,0
zaaa 144,72,0
dl 224,36,52,0
s_waitcnt lgkmcnt(6)
zvaa 0,80,0
s_add_u32 s5,s4,16384
ra 40,233,4096
v_mov_b32_e32 v88,0
zvaa 4,84,0
s_cmp_eq_u32 s12,3
s_cselect_b32 s55,s4,s55
ra 44,233,6144
v_mov_b32_e32 v89,0
zvaa 12,84,4
s_mov_b32 s4,10
ra 48,233,8192
v_mov_b32_e32 v90,0
zvaa 8,80,4
dl 225,36,52,1024
zvaa 128,80,16
s_lshl_b32 s88,s88,12
ra 52,233,10240
v_mov_b32_e32 v91,0
zvaa 132,84,16
s_xor_b32 s87,s19,15
v_mov_b32_e32 v92,0
v_mov_b32_e32 v93,0
zvaa 140,84,20
s_cmp_eq_u32 s12,2
s_cselect_b32 s4,14,s4
ra 56,233,12288
v_mov_b32_e32 v94,0
zvaa 136,80,20
dl 226,36,52,2048
s_waitcnt lgkmcnt(5)
zvaa 32,64,8
s_add_u32 s30,s68,s88
s_addc_u32 s31,s69,0
v_mov_b32_e32 v95,0
v_mov_b32_e32 v84,0
zvaa 36,68,8
s_lshl_b32 s22,s87,7
ra 60,233,14336
v_mov_b32_e32 v85,0
zvaa 44,68,12
s_lshl_b32 s87,s87,2
v_mov_b32_e32 v86,0
v_mov_b32_e32 v87,0
zvaa 40,64,12
dl 227,36,52,3072
zvaa 160,64,24
s_add_u32 m0,s62,49152
s_add_u32 s52,s52,s60
v_mov_b32_e32 v80,0
zvaa 164,68,24
s_lshl_b32 s88,s19,2
v_mov_b32_e32 v81,0
v_mov_b32_e32 v82,0
zvaa 172,68,28
s_cmp_eq_u32 s12,3
s_cselect_b32 s4,10,s4
v_mov_b32_e32 v83,0
v_mov_b32_e32 v24,0
zvaa 168,64,28
dl 228,40,53,0
zvaa 184,72,28
s_add_u32 s87,s87,s12
v_mov_b32_e32 v25,0
v_mov_b32_e32 v26,0
zvaa 188,76,28
s_add_u32 s88,s88,s12
v_mov_b32_e32 v27,0
v_mov_b32_e32 v28,0
zvaa 180,76,24
s_waitcnt vmcnt(13)
s_barrier
ta 96,234,16384
ta 98,234,16896
ta 100,235,16384
ta 102,235,16896
zvaa 176,72,24
dl 229,40,53,1024
zvaa 56,72,12
s_lshl_b32 s90,s18,13
v_mov_b32_e32 v29,0
v_mov_b32_e32 v30,0
zvaa 60,76,12
s_add_u32 s90,s90,62914560
ta 104,236,16384
v_mov_b32_e32 v31,0
zvaa 52,76,8
s_mul_i32 s14,s87,327680
ta 106,236,16896
v_mov_b32_e32 v20,0
zvaa 48,72,8
dl 230,44,54,2048
zvaa 64,80,8
s_mul_i32 s13,s88,327680
ta 108,237,16384
v_mov_b32_e32 v21,0
zvaa 68,84,8
s_add_u32 s14,s14,s90
ta 110,237,16896
v_mov_b32_e32 v22,0
zvaa 76,84,12
s_add_u32 s13,s13,s90
v_mov_b32_e32 v23,0
v_mov_b32_e32 v16,0
zvaa 72,80,12
dl 231,44,54,3072
zvaa 192,80,24
s_add_u32 m0,s98,65536
v_mov_b32_e32 v17,0
v_mov_b32_e32 v18,0
zvaa 196,84,24
s_mul_i32 s90,s18,531456
ta 208,236,4096
v_mov_b32_e32 v19,0
zvaa 204,84,28
s_lshl_b32 s91,s12,12
ta 210,236,4608
v_lshlrev_b32_e32 v241,7,v247
zvaa 200,80,28
dl 224,36,52,0
s_waitcnt lgkmcnt(5)
maaa 128,96,32
s_add_u32 s90,s90,s91
ta 216,237,4096
v_mov_b32_e32 v242,134217728
maaa 132,100,32
s_add_u32 s90,s90,16777216
ta 218,237,4608
v_lshrrev_b32_e32 v248,3,v247
maaa 140,100,36
s_cmp_eq_u32 s12,3
s_cselect_b64 s[26:27],-1,0
ta 112,234,20480
v_and_b32_e32 v250,112,v244
maaa 136,96,36
dl 225,36,52,1024
mvaa 32,96,40
s_lshl_b32 s86,s19,1
ta 114,234,20992
v_and_b32_e32 v251,16,v247
mvaa 36,100,40
s_mov_b64 s[48:49],s[72:73]
ta 116,235,20480
v_lshlrev_b32_e32 v251,1,v251
mvaa 44,100,44
s_mov_b32 s50,8388608
ta 118,235,20992
v_mul_u32_u24_e32 v246,6144,v248
mvaa 40,96,44
dl 226,36,52,2048
mvaa 96,96,48
s_mov_b32 s59,655360
v_cmp_gt_u32_e64 s[24:25],s4,v247
v_add_u32_e32 v249,s90,v244
mvaa 100,100,48
s_add_u32 s86,s86,2
v_cndmask_b32_e64 v241,v242,v241,s[24:25]
v_add_u32_e32 v255,17006592,v249
mvaa 108,100,52
s_cmp_eq_u32 s12,2
s_cselect_b64 s[24:25],-1,0
v_accvgpr_write_b32 a240,v249
v_and_b32_e32 v249,48,v247
mvaa 104,96,52
dl 227,36,52,3072
mvaa 160,96,56
s_add_u32 s52,s52,s56
s_add_u32 m0,s98,69632
v_accvgpr_write_b32 a241,v255
mvaa 164,100,56
s_cmp_eq_u32 s12,2
s_cselect_b64 s[48:49],s[80:81],s[48:49]
ta 212,236,20480
v_xor_b32_e32 v249,v249,v250
mvaa 172,100,60
s_cmp_eq_u32 s12,2
s_cselect_b32 s50,6291456,s50
v_xor_b32_e32 v250,v250,v251
v_lshl_add_u32 v252,v248,11,v249
mvaa 168,96,60
dl 224,36,52,0
s_waitcnt lgkmcnt(1)
mvaa 184,104,60
s_cmp_eq_u32 s12,2
s_cselect_b32 s59,458752,s59
v_and_b32_e32 v251,48,v250
v_xor_b32_e32 v253,64,v252
mvaa 188,108,60
s_lshl_b32 s15,1,s86
v_accvgpr_write_b32 a242,v252
v_add_u32_e32 v246,v246,v251
mvaa 180,108,56
s_lshl_b32 s23,s19,7
v_add_u32_e32 v242,15360,v253
v_and_b32_e32 v251,64,v250
mvaa 176,104,56
dl 225,36,52,1024
mvaa 120,104,52
s_or_b32 s86,%1,s12
s_cselect_b32 s65,0,4
v_lshl_add_u32 v248,v251,5,v246
v_xor_b32_e32 v249,64,v251
mvaa 124,108,52
s_cmp_eq_u32 s12,3
s_cselect_b64 s[48:49],s[68:69],s[48:49]
v_mul_u32_u24_e32 v251,768,v251
v_accvgpr_write_b32 a243,v242
mvaa 116,108,48
s_cmp_eq_u32 s12,3
s_cselect_b32 s50,s5,s50
v_add_u32_e32 v242,30720,v252
v_lshl_add_u32 v249,v249,5,v246
mvaa 112,104,48
dl 226,36,52,2048
mvaa 56,104,44
s_cmp_eq_u32 s12,3
s_cselect_b32 s59,1280,s59
v_accvgpr_write_b32 a244,v242
v_add_u32_e32 v246,v246,v251
mvaa 60,108,44
s_add_u32 s53,s53,s58
v_add_u32_e32 v242,46080,v253
v_accvgpr_write_b32 a245,v242
mvaa 52,108,40
s_add_u32 s54,s54,s58
v_add_u32_e32 v243,4096,v246
v_add_u32_e32 v242,0,v248
mvaa 48,104,40
dl 227,36,52,3072
maaa 152,104,36
s_add_u32 s52,s52,s60
ta 214,236,20992
v_cndmask_b32_e64 v242,v228,v242,s[24:25]
maaa 156,108,36
s_mov_b32 s63,32768
ta 220,237,20480
v_cndmask_b32_e64 v242,v242,v243,s[26:27]
maaa 148,108,32
s_mov_b32 s61,s98
ta 222,237,20992
v_add_u32_e32 v243,101376,v246
maaa 144,104,32
s_mov_b32 s64,1
v_and_b32_e32 v246,15,v247
v_and_b32_e32 v246,12,v247
mvaa 0,112,32
v_accvgpr_write_b32 a246,v242
v_add_u32_e32 v242,48128,v249
mvaa 4,116,32
v_cndmask_b32_e64 v242,v229,v242,s[24:25]
v_cndmask_b32_e64 v242,v242,v243,s[26:27]
mvaa 12,116,36
v_lshlrev_b32_e32 v246,5,v246
v_accvgpr_write_b32 a247,v242
mvaa 8,112,36
v_add_u32_e32 v242,96256,v248
v_cndmask_b32_e64 v242,v230,v242,s[24:25]
mvaa 64,112,40
v_and_b32_e32 v248,3,v247
v_lshl_add_u32 v246,v248,3,v246
mvaa 68,116,40
v_accvgpr_write_b32 a248,v242
v_and_b32_e32 v248,24,v247
mvaa 76,116,44
v_add_u32_e32 v242,144384,v249
v_lshl_add_u32 v246,v248,2,v246
mvaa 72,112,44
v_and_b32_e32 v248,32,v247
v_cndmask_b32_e64 v242,v231,v242,s[24:25]
mvaa 128,112,48
v_add_u32_e32 v246,98304,v246
v_accvgpr_write_b32 a249,v242
mvaa 132,116,48
v_lshl_add_u32 v238,v248,5,v246
v_xor_b32_e32 v239,32,v238
mvaa 140,116,52
v_add_u32_e32 v232,32768,v232
v_add_u32_e32 v233,32768,v233
mvaa 136,112,52
mvaa 192,112,56
mvaa 196,116,56
mvaa 204,116,60
mvaa 200,112,60
s_add_u32 s4,s96,L_vrk-L_att_pc
s_addc_u32 s5,s97,0
s_mov_b64 exec,0
s_cmp_eq_u32 s12,0
s_cbranch_scc1 L_vs13
s_cmp_eq_u32 s12,1
s_cbranch_scc1 L_vs14
s_cmp_eq_u32 s12,2
s_cbranch_scc1 L_vs15
s_branch L_vs16
L_vrk:
s_mov_b64 exec,-1
s_sleep 6
s_waitcnt vmcnt(8)
s_waitcnt lgkmcnt(0)
s_barrier
L_k_head:
mvaa 216,208,28
ra 64,232,0
mvaa 216,212,60
s_add_u32 m0,s62,0
ta 192,234,32768
mvaa 220,220,60
dl 228,40,53,0
mvaa 220,216,28
ta 194,234,33280
s_nop 3
mvaa 212,216,24
ra 96,233,0
mvaa 212,220,56
ta 196,234,49152
mvaa 208,212,56
ta 198,234,49664
mvaa 208,208,24
ta 204,235,49152
mvaa 152,208,20
dl 229,40,53,1024
mvaa 152,212,52
s_add_u32 s53,s53,s57
ta 206,235,49664
mvaa 156,220,52
ta 200,235,32768
mvaa 156,216,20
ta 202,235,33280
mvaa 148,216,16
ra 68,232,2048
mvaa 148,220,48
ra 100,233,2048
mvaa 144,212,48
ra 72,232,4096
mvaa 144,208,16
dl 230,44,54,2048
mvaa 88,208,12
ra 104,233,4096
mvaa 88,212,44
ra 76,232,6144
mvaa 92,220,44
ra 108,233,6144
mvaa 92,216,12
ra 80,232,8192
mvaa 84,216,8
ra 112,233,8192
mvaa 84,220,40
dl 231,44,54,3072
mvaa 80,212,40
s_add_u32 s54,s54,s57
ra 84,232,10240
mvaa 80,208,8
ra 116,233,10240
mvaa 24,208,4
ra 88,232,12288
mvaa 24,212,36
ra 120,233,12288
mvaa 28,220,36
ra 92,232,14336
mvaa 28,216,4
s_add_u32 m0,s62,16384
ra 124,233,14336
mvaa 20,216,0
L_vs1:
s_cbranch_execz L_vs9
dl 228,40,53,0
mvaa 20,220,32
mvaa 16,212,32
mvaa 16,208,0
s_waitcnt lgkmcnt(8)
maaa 128,192,64
maaa 128,196,96
maaa 132,204,96
dl 229,40,53,1024
maaa 132,200,64
s_add_u32 s53,s53,s58
maaa 140,200,68
maaa 140,204,100
maaa 136,196,100
maaa 136,192,68
mvaa 32,192,72
mvaa 32,196,104
dl 230,44,54,2048
mvaa 36,204,104
mvaa 36,200,72
s_waitcnt lgkmcnt(0)
mvaa 44,200,76
ta 208,236,32768
mvaa 44,204,108
ta 210,236,33280
mvaa 40,196,108
ta 212,236,49152
mvaa 40,192,76
dl 231,44,54,3072
mvaa 96,192,80
s_add_u32 s54,s54,s58
ta 214,236,49664
mvaa 96,196,112
ta 220,237,49152
mvaa 100,204,112
ta 222,237,49664
mvaa 100,200,80
ta 216,237,32768
mvaa 108,200,84
ta 218,237,33280
mvaa 108,204,116
mvaa 104,196,116
s_add_u32 m0,s61,0
mvaa 104,192,84
dl 224,36,52,0
mvaa 160,192,88
mvaa 160,196,120
mvaa 164,204,120
mvaa 164,200,88
mvaa 172,200,92
mvaa 172,204,124
mvaa 168,196,124
dl 225,36,52,1024
mvaa 168,192,92
s_waitcnt lgkmcnt(0)
mvaa 184,208,92
mvaa 184,212,124
mvaa 188,220,124
ta 192,234,36864
mvaa 188,216,92
ta 194,234,37376
mvaa 180,216,88
ta 196,234,53248
mvaa 180,220,120
dl 226,36,52,2048
mvaa 176,212,120
ta 198,234,53760
mvaa 176,208,88
ta 204,235,53248
mvaa 120,208,84
ta 206,235,53760
mvaa 120,212,116
ta 200,235,36864
mvaa 124,220,116
ta 202,235,37376
mvaa 124,216,84
mvaa 116,216,80
dl 227,36,52,3072
mvaa 116,220,112
s_add_u32 s52,s52,s56
mvaa 112,212,112
mvaa 112,208,80
mvaa 56,208,76
mvaa 56,212,108
mvaa 60,220,108
mvaa 60,216,76
mvaa 52,216,72
s_add_u32 m0,s61,4096
mvaa 52,220,104
dl 224,36,52,0
mvaa 48,212,104
s_add_u32 s61,s63,s98
mvaa 48,208,72
s_cmp_ge_u32 s63,65536
s_cselect_b32 s99,-2,1
s_lshl_b32 s99,s99,15
maaa 152,208,68
s_add_u32 s63,s63,s99
v_add_u32_e32 v232,s99,v232
v_add_u32_e32 v233,s99,v233
maaa 152,212,100
maaa 156,220,100
maaa 156,216,68
maaa 148,216,64
dl 225,36,52,1024
maaa 148,220,96
maaa 144,212,96
s_waitcnt lgkmcnt(0)
maaa 144,208,64
mvaa 0,192,64
mvaa 0,196,96
mvaa 4,204,96
ta 208,236,36864
mvaa 4,200,64
ta 210,236,37376
mvaa 12,200,68
dl 226,36,52,2048
mvaa 12,204,100
ta 212,236,53248
mvaa 8,196,100
ta 214,236,53760
mvaa 8,192,68
ta 220,237,53248
mvaa 64,192,72
ta 222,237,53760
mvaa 64,196,104
ta 216,237,36864
mvaa 68,204,104
ta 218,237,37376
mvaa 68,200,72
dl 227,36,52,3072
mvaa 76,200,76
s_add_u32 s52,s52,s60
mvaa 76,204,108
mvaa 72,196,108
mvaa 72,192,76
mvaa 128,192,80
mvaa 128,196,112
buffer_load_dword a184,v241,s[48:51],s55 offen
mvaa 132,204,112
s_add_u32 s55,s55,s59
mvaa 132,200,80
mvaa 140,200,84
mvaa 140,204,116
mvaa 136,196,116
mvaa 136,192,84
buffer_load_dword a185,v241,s[48:51],s55 offen
mvaa 192,192,88
s_add_u32 s55,s55,s59
mvaa 192,196,120
mvaa 196,204,120
mvaa 196,200,88
mvaa 204,200,92
mvaa 204,204,124
s_cmp_eq_u32 s64,3
mvaa 200,196,124
s_waitcnt vmcnt(10) lgkmcnt(0)
s_barrier
mvaa 200,192,92
s_cbranch_scc1 L_k_exit
mvaa 216,208,92
ra 0,232,0
mvaa 216,212,124
s_add_u32 m0,s62,32768
ta 192,234,0
mvaa 220,220,124
dl 228,40,53,0
mvaa 220,216,92
ta 194,234,512
s_nop 3
mvaa 212,216,88
ra 32,233,0
mvaa 212,220,120
ta 196,234,16384
mvaa 208,212,120
ta 198,234,16896
mvaa 208,208,88
ta 204,235,16384
mvaa 152,208,84
dl 229,40,53,1024
mvaa 152,212,116
s_add_u32 s53,s53,s57
ta 206,235,16896
mvaa 156,220,116
ta 200,235,0
mvaa 156,216,84
ta 202,235,512
mvaa 148,216,80
ra 4,232,2048
mvaa 148,220,112
ra 36,233,2048
mvaa 144,212,112
ra 8,232,4096
mvaa 144,208,80
dl 230,44,54,2048
mvaa 88,208,76
ra 40,233,4096
mvaa 88,212,108
ra 12,232,6144
mvaa 92,220,108
ra 44,233,6144
mvaa 92,216,76
ra 16,232,8192
mvaa 84,216,72
ra 48,233,8192
mvaa 84,220,104
dl 231,44,54,3072
mvaa 80,212,104
s_add_u32 s54,s54,s57
ra 20,232,10240
mvaa 80,208,72
ra 52,233,10240
mvaa 24,208,68
ra 24,232,12288
mvaa 24,212,100
ra 56,233,12288
mvaa 28,220,100
ra 28,232,14336
mvaa 28,216,68
s_add_u32 m0,s62,49152
ra 60,233,14336
mvaa 20,216,64
dl 228,40,53,0
mvaa 20,220,96
mvaa 16,212,96
mvaa 16,208,64
s_waitcnt lgkmcnt(8)
maaa 128,192,0
maaa 128,196,32
maaa 132,204,32
dl 229,40,53,1024
maaa 132,200,0
s_add_u32 s53,s53,s58
maaa 140,200,4
maaa 140,204,36
maaa 136,196,36
maaa 136,192,4
mvaa 32,192,8
mvaa 32,196,40
dl 230,44,54,2048
mvaa 36,204,40
mvaa 36,200,8
s_waitcnt lgkmcnt(0)
mvaa 44,200,12
ta 208,236,0
mvaa 44,204,44
ta 210,236,512
mvaa 40,196,44
)"
R"(ta 212,236,16384
mvaa 40,192,12
dl 231,44,54,3072
mvaa 96,192,16
s_add_u32 s54,s54,s58
ta 214,236,16896
mvaa 96,196,48
ta 220,237,16384
mvaa 100,204,48
ta 222,237,16896
mvaa 100,200,16
ta 216,237,0
mvaa 108,200,20
ta 218,237,512
mvaa 108,204,52
mvaa 104,196,52
s_add_u32 m0,s61,0
mvaa 104,192,20
dl 224,36,52,0
mvaa 160,192,24
s_lshl_b32 s86,s21,2
s_add_u32 s86,s86,s12
s_mul_i32 s86,s86,384
mvaa 160,196,56
s_add_u32 s86,s86,4096
v_lshl_add_u32 v253,v244,3,s86
mvaa 164,204,56
s_mov_b64 exec,7
global_load_dword a188,v253,s[96:97]
s_mov_b64 exec,-1
mvaa 164,200,24
mvaa 172,200,28
mvaa 172,204,60
mvaa 168,196,60
dl 225,36,52,1024
mvaa 168,192,28
s_waitcnt lgkmcnt(0)
mvaa 184,208,28
mvaa 184,212,60
mvaa 188,220,60
ta 192,234,4096
mvaa 188,216,28
ta 194,234,4608
mvaa 180,216,24
ta 196,234,20480
mvaa 180,220,56
dl 226,36,52,2048
mvaa 176,212,56
ta 198,234,20992
mvaa 176,208,24
ta 204,235,20480
mvaa 120,208,20
ta 206,235,20992
mvaa 120,212,52
ta 200,235,4096
mvaa 124,220,52
ta 202,235,4608
mvaa 124,216,20
mvaa 116,216,16
dl 227,36,52,3072
mvaa 116,220,48
s_add_u32 s52,s52,s56
mvaa 112,212,48
mvaa 112,208,16
mvaa 56,208,12
mvaa 56,212,44
s_bitcmp1_b32 s65,s64
s_cbranch_scc1 L_k_ev
L_k_evr:
mvaa 60,220,44
mvaa 60,216,12
mvaa 52,216,8
s_add_u32 m0,s61,4096
mvaa 52,220,40
dl 224,36,52,0
mvaa 48,212,40
s_add_u32 s61,s63,s98
mvaa 48,208,8
s_cmp_ge_u32 s63,65536
s_cselect_b32 s99,-2,1
s_lshl_b32 s99,s99,15
maaa 152,208,4
s_add_u32 s63,s63,s99
v_add_u32_e32 v232,s99,v232
v_add_u32_e32 v233,s99,v233
maaa 152,212,36
maaa 156,220,36
maaa 156,216,4
maaa 148,216,0
dl 225,36,52,1024
maaa 148,220,32
maaa 144,212,32
s_waitcnt lgkmcnt(0)
maaa 144,208,0
mvaa 0,192,0
mvaa 0,196,32
mvaa 4,204,32
ta 208,236,4096
mvaa 4,200,0
ta 210,236,4608
mvaa 12,200,4
dl 226,36,52,2048
mvaa 12,204,36
ta 212,236,20480
mvaa 8,196,36
ta 214,236,20992
mvaa 8,192,4
ta 220,237,20480
mvaa 64,192,8
ta 222,237,20992
mvaa 64,196,40
ta 216,237,4096
mvaa 68,204,40
ta 218,237,4608
mvaa 68,200,8
dl 227,36,52,3072
mvaa 76,200,12
s_add_u32 s52,s52,s60
mvaa 76,204,44
mvaa 72,196,44
mvaa 72,192,12
mvaa 128,192,16
mvaa 128,196,48
buffer_load_dword a187,v241,s[48:51],s55 offen
mvaa 132,204,48
s_add_u32 s55,s55,s59
mvaa 132,200,16
mvaa 140,200,20
mvaa 140,204,52
mvaa 136,196,52
mvaa 136,192,20
buffer_load_dword a190,v241,s[48:51],s55 offen
mvaa 192,192,24
s_add_u32 s55,s55,s59
mvaa 192,196,56
mvaa 196,204,56
s_add_u32 s64,s64,1
mvaa 196,200,24
mvaa 204,200,28
mvaa 204,204,60
mvaa 200,196,60
s_waitcnt vmcnt(10) lgkmcnt(0)
s_barrier
mvaa 200,192,28
s_branch L_k_head
L_k_exit:
global_load_dwordx4 a[160:163],v244,s[30:31] offset:0
s_add_u32 m0,s62,32768
mvaa 216,208,92
s_cmp_lt_u32 s12,2
s_cselect_b32 s86,s22,s23
ta 32,234,0
s_and_b32 s87,s12,1
mvaa 216,212,124
s_lshl_b32 s86,s86,11
ta 34,234,512
s_lshl_b32 s87,s87,17
mvaa 220,220,124
s_add_u32 s86,s86,s87
ra 0,232,0
s_mov_b64 s[36:37],s[68:69]
mvaa 220,216,92
s_mov_b32 s38,4194304
ta 36,235,0
s_mov_b32 s52,s86
mvaa 212,216,88
s_mov_b32 s56,65536
ta 38,235,512
v_accvgpr_read_b32 v224,a242
mvaa 212,220,120
dl 228,40,53,0
mvaa 208,212,120
s_lshl_b32 s88,s18,1
ra 4,232,2048
v_accvgpr_read_b32 v225,a243
mvaa 208,208,88
s_add_u32 s88,s88,s12
ra 8,232,4096
v_accvgpr_read_b32 v226,a244
mvaa 152,208,84
s_mov_b32 s91,196608
ra 12,232,6144
v_accvgpr_read_b32 v227,a245
mvaa 152,212,116
s_lshl_b32 s88,s88,7
ra 16,232,8192
s_lshl_b32 s89,s18,6
mvaa 156,220,116
s_mov_b32 s90,6291456
ra 20,232,10240
s_mov_b32 s60,-65408
mvaa 156,216,84
dl 229,40,53,1024
mvaa 148,216,80
s_add_u32 s53,s53,s57
ra 24,232,12288
s_bfe_u32 s4,s21,65536
mvaa 148,220,112
s_lshl_b32 s4,s4,6
ra 28,232,14336
s_lshl_b32 s87,s20,2
mvaa 144,212,112
s_mov_b32 s26,278528
ta 40,236,0
s_mov_b32 s27,131072
mvaa 144,208,80
s_add_u32 s61,s98,32768
ta 42,236,512
s_mov_b32 s63,65536
mvaa 88,208,76
s_cmp_ge_u32 s63,65536
s_cselect_b32 s99,-2,1
ta 44,237,0
s_lshl_b32 s99,s99,15
mvaa 88,212,108
dl 230,44,54,2048
mvaa 92,220,108
s_mov_b32 s67,2048
ta 46,237,512
s_mov_b32 s66,8
mvaa 92,216,76
s_mov_b32 s64,1
ta 48,234,4096
mvaa 84,216,72
s_bitcmp1_b32 s12,0
s_cselect_b64 vcc,-1,0
ta 50,234,4608
mvaa 84,220,104
ta 52,235,4096
mvaa 80,212,104
ta 54,235,4608
mvaa 80,208,72
dl 231,44,54,3072
s_add_u32 m0,s62,49152
mvaa 24,208,68
s_add_u32 s54,s54,s57
ta 56,236,4096
mvaa 24,212,100
s_cmp_lt_u32 s12,2
s_cselect_b32 s57,262144,s91
ta 58,236,4608
mvaa 28,220,100
ta 60,237,4096
mvaa 28,216,68
ta 62,237,4608
mvaa 20,216,64
mvaa 20,220,96
dl 228,40,53,0
mvaa 16,212,96
v_accvgpr_read_b32 v228,a246
mvaa 16,208,64
s_waitcnt lgkmcnt(5)
maaa 128,32,0
maaa 132,36,0
maaa 140,36,4
dl 229,40,53,1024
maaa 136,32,4
s_waitcnt lgkmcnt(0)
s_barrier
mvaa 32,32,8
s_add_u32 s53,s53,s58
ta 96,234,16384
v_accvgpr_read_b32 v229,a247
mvaa 36,36,8
s_cmp_lt_u32 s12,2
s_cselect_b32 s42,8388608,s90
ta 98,234,16896
mvaa 44,36,12
s_cmp_lt_u32 s12,2
s_cselect_b32 s53,s88,s89
ra 64,233,0
s_and_b32 s89,s98,8192
mvaa 40,32,12
dl 230,44,54,2048
mvaa 96,32,16
s_bitcmp1_b32 s21,0
s_cselect_b32 s90,s57,0
ta 100,235,16384
s_add_u32 s53,s53,s90
mvaa 100,36,16
s_cmp_lt_u32 s12,2
s_cselect_b64 s[40:41],s[72:73],s[80:81]
ta 102,235,16896
v_accvgpr_read_b32 v230,a248
mvaa 108,36,20
s_lshl_b32 s90,s90,1
ra 68,233,2048
s_add_u32 s91,s89,65536
mvaa 104,32,20
s_bfe_u32 s88,s21,65539
ra 72,233,4096
s_lshl_b32 s88,s88,1
mvaa 160,32,24
dl 231,44,54,3072
mvaa 164,36,24
s_add_u32 m0,s98,65536
ra 76,233,6144
s_add_u32 s54,s54,s58
mvaa 172,36,28
s_cmp_eq_u32 s12,3
s_cselect_b64 s[44:45],s[68:69],s[40:41]
ra 80,233,8192
v_accvgpr_read_b32 v231,a249
mvaa 168,32,28
s_cmp_eq_u32 s12,3
s_cselect_b32 s46,0,s42
ra 84,233,10240
s_add_u32 s58,s57,s90
mvaa 184,40,28
s_cmp_eq_u32 s12,3
s_cselect_b32 s54,0,s53
ra 88,233,12288
s_sub_u32 s57,s57,s90
mvaa 188,44,28
L_vs2:
s_cbranch_execz L_vs10
global_load_dwordx4 a[168:171],v244,s[30:31] offset:2048
mvaa 180,44,24
s_add_u32 s90,s89,65536
ta 104,236,16384
s_add_u32 s88,s88,s87
mvaa 176,40,24
s_and_b32 s87,s12,1
ra 92,233,14336
s_add_u32 s88,s88,s87
mvaa 120,40,20
s_mul_i32 s88,s88,531456
ta 106,236,16896
s_and_b32 s87,s21,7
mvaa 124,44,20
s_add_u32 s86,s88,16777216
ta 108,237,16384
s_lshl_b32 s87,s87,15
mvaa 116,44,16
global_load_dwordx4 a[176:179],v244,s[84:85] offset:0
mvaa 112,40,16
s_add_u32 s86,s86,s87
ta 110,237,16896
s_lshr_b32 s87,s12,1
mvaa 56,40,12
s_lshl_b32 s87,s87,14
ta 112,234,20480
s_add_u32 s86,s86,s87
mvaa 60,44,12
s_add_u32 s24,s8,s86
s_addc_u32 s25,s9,0
ta 114,234,20992
s_lshl_b32 s86,%1,5
mvaa 52,44,8
s_lshl_b32 s87,s12,3
ta 116,235,20480
s_add_u32 s86,s86,s87
mvaa 48,40,8
global_load_dwordx4 a[164:167],v244,s[30:31] offset:1024
maaa 152,40,4
s_add_u32 s86,s86,83886080
ta 118,235,20992
s_mov_b32 s88,4096
maaa 156,44,4
s_cmp_lg_u32 s65,0
s_cbranch_scc1 L_k_ec1
L_k_ecr1:
ta 120,236,20480
maaa 148,44,0
s_cmp_eq_u32 s12,0
s_cselect_b32 s65,96,0
ta 122,236,20992
s_or_b32 s65,s65,128
maaa 144,40,0
ta 124,237,20480
mvaa 0,48,0
global_load_dwordx4 a[172:175],v244,s[30:31] offset:3072
mvaa 4,52,0
s_mov_b32 s30,1048577
ta 126,237,20992
s_mov_b32 s31,268435712
mvaa 12,52,4
mvaa 8,48,4
mvaa 64,48,8
mvaa 68,52,8
global_load_dwordx4 a[180:183],v244,s[84:85] offset:1024
mvaa 76,52,12
mvaa 72,48,12
mvaa 128,48,16
mvaa 132,52,16
mvaa 140,52,20
dl 224,36,52,0
mvaa 136,48,20
mvaa 192,48,24
mvaa 196,52,24
mvaa 204,52,28
mvaa 200,48,28
dl 225,36,52,1024
mvaa 216,56,28
mvaa 220,60,28
mvaa 212,60,24
mvaa 208,56,24
mvaa 152,56,20
dl 226,36,52,2048
mvaa 156,60,20
mvaa 148,60,16
mvaa 144,56,16
mvaa 88,56,12
s_waitcnt vmcnt(12) lgkmcnt(0)
s_barrier
mvaa 92,60,12
dl 227,36,52,3072
mvaa 84,60,8
s_add_u32 s52,s52,s56
s_add_u32 m0,s98,69632
ta 32,234,32768
mvaa 80,56,8
ta 34,234,33280
mvaa 24,56,4
ta 36,235,32768
mvaa 28,60,4
ta 38,235,33280
mvaa 20,60,0
dl 224,36,52,0
mvaa 16,56,0
ta 40,236,32768
maaa 128,96,64
ta 42,236,33280
maaa 132,100,64
ta 44,237,32768
maaa 140,100,68
ta 46,237,33280
maaa 136,96,68
dl 225,36,52,1024
mvaa 32,96,72
ta 48,234,36864
mvaa 36,100,72
ta 50,234,37376
mvaa 44,100,76
ta 52,235,36864
mvaa 40,96,76
ta 54,235,37376
mvaa 96,96,80
dl 226,36,52,2048
mvaa 100,100,80
ra 20,232,43008
mvaa 108,100,84
ra 24,232,45056
mvaa 104,96,84
ra 0,232,32768
mvaa 160,96,88
ra 28,232,47104
mvaa 164,100,88
dl 227,36,52,3072
mvaa 172,100,92
s_add_u32 m0,s62,0
ra 4,232,34816
s_add_u32 s52,s52,s60
mvaa 168,96,92
ra 16,232,40960
mvaa 184,104,92
ra 8,232,36864
mvaa 188,108,92
ra 12,232,38912
v_add_u32_e32 v232,s90,v232
mvaa 180,108,88
dl 228,40,53,0
mvaa 176,104,88
ta 60,237,36864
v_xor_b32_e32 v232,s4,v232
mvaa 120,104,84
ta 62,237,37376
mvaa 124,108,84
ta 56,236,36864
mvaa 116,108,80
ta 58,236,37376
mvaa 112,104,80
dl 229,40,53,1024
mvaa 56,104,76
s_add_u32 s53,s53,s57
mvaa 60,108,76
mvaa 52,108,72
mvaa 48,104,72
maaa 152,104,68
dl 230,44,54,2048
maaa 156,108,68
maaa 148,108,64
maaa 144,104,64
mvaa 0,112,64
mvaa 4,116,64
dl 231,44,54,3072
mvaa 12,116,68
s_add_u32 m0,s62,16384
s_add_u32 s54,s54,s57
mvaa 8,112,68
mvaa 64,112,72
mvaa 68,116,72
mvaa 76,116,76
dl 228,40,53,0
mvaa 72,112,76
mvaa 128,112,80
mvaa 132,116,80
mvaa 140,116,84
mvaa 136,112,84
dl 229,40,53,1024
mvaa 192,112,88
s_add_u32 s53,s53,s58
mvaa 196,116,88
mvaa 204,116,92
mvaa 200,112,92
mvaa 216,120,92
dl 230,44,54,2048
mvaa 220,124,92
mvaa 212,124,88
s_waitcnt vmcnt(20) lgkmcnt(0)
s_barrier
mvaa 208,120,88
ra 92,233,47104
mvaa 152,120,84
ta 96,234,49152
mvaa 156,124,84
dl 231,44,54,3072
mvaa 148,124,80
s_add_u32 m0,s98,0
ra 88,233,45056
s_add_u32 s54,s54,s58
mvaa 144,120,80
ta 98,234,49664
mvaa 88,120,76
ta 112,234,53248
mvaa 92,124,76
ta 116,235,53248
mvaa 84,124,72
dl 224,36,52,0
mvaa 80,120,72
ta 118,235,53760
mvaa 24,120,68
ta 114,234,53760
v_subrev_u32_e32 v234,s89,v234
mvaa 28,124,68
ta 100,235,49152
mvaa 20,124,64
ta 102,235,49664
v_subrev_u32_e32 v235,s89,v235
mvaa 16,120,64
dl 225,36,52,1024
maaa 128,32,0
ta 124,237,53248
maaa 132,36,0
ta 126,237,53760
maaa 140,36,4
ta 120,236,53248
maaa 136,32,4
ta 122,236,53760
mvaa 32,32,8
dl 226,36,52,2048
mvaa 36,36,8
ta 104,236,49152
mvaa 44,36,12
ta 106,236,49664
v_subrev_u32_e32 v236,s89,v236
mvaa 40,32,12
ta 108,237,49152
mvaa 56,40,12
ta 110,237,49664
v_subrev_u32_e32 v237,s89,v237
mvaa 60,44,12
dl 227,36,52,3072
mvaa 52,44,8
s_add_u32 s52,s52,s56
s_add_u32 m0,s98,4096
ra 84,233,43008
mvaa 48,40,8
ra 80,233,40960
maaa 152,40,4
ra 72,233,36864
maaa 156,44,4
ra 76,233,38912
maaa 148,44,0
dl 224,36,52,0
maaa 144,40,0
ra 68,233,34816
mvaa 0,48,0
ra 64,233,32768
v_add_u32_e32 v233,s91,v233
mvaa 4,52,0
v_xor_b32_e32 v233,s4,v233
mvaa 12,52,4
mvaa 8,48,4
dl 225,36,52,1024
mvaa 24,56,4
mvaa 28,60,4
mvaa 20,60,0
mvaa 16,56,0
mvaa 220,60,28
dl 226,36,52,2048
s_waitcnt lgkmcnt(2)
mvaa 220,124,92
mvaa 216,120,92
mvaa 216,56,28
mvaa 212,60,24
mvaa 212,124,88
dl 227,36,52,3072
mvaa 208,120,88
s_add_u32 m0,s62,32768
s_add_u32 s52,s52,s60
s_waitcnt vmcnt(25)
zvaa 246,160,176
mvaa 208,56,24
cv 223,222,223
cv 222,220,221
mvaa 204,52,28
cv 221,218,219
zvaa 250,168,176
mvaa 204,116,92
cv 220,216,217
cv 219,214,215
mvaa 200,112,92
dl 228,40,53,0
s_waitcnt vmcnt(25)
mvaa 246,164,180
mvaa 200,48,28
cv 218,212,213
mvaa 250,172,180
mvaa 196,52,24
cv 217,210,211
cv 216,208,209
mvaa 196,116,88
cv 215,206,207
v_cndmask_b32_e64 v246,0,v246,s[30:31]
mvaa 192,112,88
cv 214,204,205
v_cndmask_b32_e64 v250,0,v250,s[30:31]
mvaa 192,48,24
dl 229,40,53,1024
s_lshl_b64 s[30:31],s[30:31],1
v_cndmask_b32_e64 v246,v246,v247,s[30:31]
v_cndmask_b32_e64 v250,v250,v251,s[30:31]
mvaa 188,44,28
s_lshl_b64 s[30:31],s[30:31],1
s_add_u32 s53,s53,s57
v_cndmask_b32_e64 v246,v246,v248,s[30:31]
v_cndmask_b32_e64 v250,v250,v252,s[30:31]
mvaa 188,108,92
s_lshl_b64 s[30:31],s[30:31],1
cv 213,202,203
v_cndmask_b32_e64 v246,v246,v249,s[30:31]
mvaa 184,104,92
v_cndmask_b32_e64 v250,v250,v253,s[30:31]
v_add_f32_dpp v246,v246,v246 quad_perm:[1,0,3,2] row_mask:0xf bank_mask:0xf
mvaa 184,40,28
v_add_f32_dpp v250,v250,v250 quad_perm:[1,0,3,2] row_mask:0xf bank_mask:0xf
v_add_f32_dpp v246,v246,v246 quad_perm:[2,3,0,1] row_mask:0xf bank_mask:0xf
mvaa 180,44,24
dl 230,44,54,2048
v_add_f32_dpp v250,v250,v250 quad_perm:[2,3,0,1] row_mask:0xf bank_mask:0xf
mvaa 180,108,88
v_add_f32_dpp v246,v246,v246 row_half_mirror row_mask:0xf bank_mask:0xf
v_add_f32_dpp v250,v250,v250 row_half_mirror row_mask:0xf bank_mask:0xf
mvaa 176,104,88
v_add_f32_dpp v246,v246,v246 row_mirror row_mask:0xf bank_mask:0xf
v_add_f32_dpp v250,v250,v250 row_mirror row_mask:0xf bank_mask:0xf
mvaa 176,40,24
cv 212,200,201
cv 211,198,199
mvaa 172,36,28
v_permlane16_swap_b32_e32 v246,v250
mvaa 172,100,92
dl 231,44,54,3072
v_add_f32_e32 v246,v246,v250
v_mov_b32_e32 v250,v246
mvaa 168,96,92
s_add_u32 s54,s54,s57
v_permlane32_swap_b32_e32 v246,v250
mvaa 168,32,28
v_add_f32_e32 v246,v246,v250
v_lshrrev_b32_e32 v250,6,v244
mvaa 164,36,24
v_mul_f32_e32 v246,0xbfb8aa3b,v246
cv 210,196,197
mvaa 164,100,88
ex 246
mvaa 160,96,88
buffer_load_dword a242,v241,s[48:51],s55 offen
mvaa 160,32,24
s_add_u32 s55,s55,s59
v_add_f32_e32 v246,1.0,v246
cv 209,194,195
mvaa 156,60,20
v_rcp_f32_e32 v246,v246
mvaa 156,124,84
s_waitcnt vmcnt(17) lgkmcnt(0)
s_barrier
mvaa 152,120,84
s_add_u32 m0,s62,49152
cv 208,192,193
cv 207,190,191
mvaa 152,56,20
dl 228,40,53,0
mvaa 148,60,16
cv 206,188,189
cv 205,186,187
mvaa 148,124,80
cv 204,184,185
cv 203,182,183
mvaa 144,120,80
cv 202,180,181
cv 201,178,179
mvaa 144,56,16
cv 200,176,177
cv 199,174,175
mvaa 140,52,20
dl 229,40,53,1024
mvaa 140,116,84
s_add_u32 s53,s53,s58
cv 198,172,173
cv 197,170,171
mvaa 136,112,84
ra 4,232,0
cv 196,168,169
mvaa 136,48,20
ta 92,239,12288
cv 195,166,167
mvaa 132,52,16
ta 94,239,12800
cv 194,164,165
mvaa 132,116,80
dl 230,44,54,2048
mvaa 128,112,80
ta 88,238,12288
cv 193,162,163
mvaa 128,48,16
ta 90,238,12800
cv 192,160,161
mvaa 124,44,20
cv 191,158,159
cv 190,156,157
mvaa 124,108,84
cv 189,154,155
cv 188,152,153
mvaa 120,104,84
dl 231,44,54,3072
mvaa 120,40,20
s_add_u32 m0,s98,32768
cv 187,150,151
cv 186,148,149
mvaa 116,44,16
s_add_u32 s54,s54,s58
cv 185,146,147
cv 184,144,145
mvaa 116,108,80
cv 183,142,143
cv 182,140,141
mvaa 112,104,80
cv 181,138,139
cv 180,136,137
mvaa 112,40,16
buffer_load_dword a243,v241,s[48:51],s55 offen
mvaa 108,36,20
s_add_u32 s55,s55,s59
cv 179,134,135
cv 178,132,133
mvaa 108,100,84
cv 177,130,131
cv 176,128,129
mvaa 104,96,84
cv 175,126,127
cv 174,124,125
mvaa 104,32,20
cv 173,122,123
cv 172,120,121
mvaa 100,36,16
dl 224,36,52,0
mvaa 100,100,80
ta 44,235,0
cv 171,118,119
mvaa 96,96,80
ta 46,235,512
cv 170,116,117
mvaa 96,32,16
ta 40,234,0
cv 169,114,115
mvaa 92,60,12
ta 42,234,512
cv 168,112,113
mvaa 92,124,76
dl 225,36,52,1024
mvaa 88,120,76
cv 167,110,111
cv 166,108,109
mvaa 88,56,12
ta 84,237,8192
cv 165,106,107
mvaa 84,60,8
ta 86,237,8704
cv 164,104,105
mvaa 84,124,72
ta 80,236,8192
cv 163,102,103
mvaa 80,120,72
dl 226,36,52,2048
mvaa 80,56,8
ta 82,236,8704
cv 162,100,101
mvaa 76,52,12
ra 16,232,6144
cv 161,98,99
mvaa 76,116,76
ta 60,235,4096
cv 160,96,97
mvaa 72,112,76
ta 56,234,4096
cv 159,94,95
mvaa 72,48,12
dl 227,36,52,3072
mvaa 68,52,8
s_add_u32 m0,s62,0
ta 62,235,4608
s_add_u32 s52,s52,s56
mvaa 68,116,72
ta 58,234,4608
cv 151,78,79
mvaa 64,112,72
ra 12,232,4096
cv 150,76,77
mvaa 64,48,8
ta 52,237,0
cv 158,92,93
mvaa 60,108,76
buffer_load_dword a244,v241,s[48:51],s55 offen
mvaa 56,104,76
s_add_u32 s55,s55,s59
ta 54,237,512
cv 147,70,71
mvaa 52,108,72
ra 8,232,2048
cv 146,68,69
mvaa 48,104,72
ta 48,236,0
cv 145,66,67
mvaa 44,100,76
ta 50,236,512
cv 144,64,65
mvaa 40,96,76
buffer_load_dword a245,v241,s[48:51],s55 offen
mvaa 36,100,72
s_add_u32 s55,s55,s59
cv 143,62,63
cv 142,60,61
mvaa 32,96,72
cv 141,58,59
cv 140,56,57
mvaa 28,124,68
cv 139,54,55
cv 138,52,53
mvaa 24,120,68
cv 137,50,51
cv 136,48,49
mvaa 20,124,64
s_mov_b64 exec,65537
buffer_store_dword v246,v250,s[8:11],s86 offen sc0 sc1
s_mov_b64 exec,-1
mvaa 16,120,64
cv 135,46,47
cv 134,44,45
mvaa 12,116,68
L_vs3:
s_cbranch_execz L_vs11
ta 76,235,8192
cv 133,42,43
mvaa 8,112,68
ta 78,235,8704
cv 132,40,41
mvaa 4,116,64
ta 72,234,8192
cv 131,38,39
mvaa 0,112,64
ta 74,234,8704
cv 130,36,37
maaa 156,108,68
cv 129,34,35
cv 128,32,33
maaa 152,104,68
cv 127,30,31
cv 126,28,29
maaa 148,108,64
cv 125,26,27
cv 124,24,25
maaa 144,104,64
v_accvgpr_read_b32 v63,a159
v_accvgpr_read_b32 v62,a158
maaa 140,100,68
v_accvgpr_read_b32 v61,a157
v_accvgpr_read_b32 v60,a156
maaa 136,96,68
v_accvgpr_read_b32 v59,a155
v_accvgpr_read_b32 v58,a154
maaa 132,100,64
v_accvgpr_read_b32 v57,a153
v_accvgpr_read_b32 v56,a152
maaa 128,96,64
ta 68,237,4096
ta 70,237,4608
s_waitcnt lgkmcnt(4)
zaaa 236,40,4
cv 56,56,57
cv 57,58,59
zaaa 220,44,4
ta 64,236,4096
ta 66,236,4608
zaaa 216,44,8
cv 58,60,61
cv 59,62,63
zaaa 232,40,8
buffer_store_dwordx4 v[56:59],v244,s[24:27],0 offen offset:3072 sc0 sc1
zaaa 228,40,12
v_accvgpr_read_b32 v55,a151
v_accvgpr_read_b32 v54,a150
zaaa 212,44,12
v_accvgpr_read_b32 v53,a149
v_accvgpr_read_b32 v52,a148
zaaa 208,44,16
v_accvgpr_read_b32 v51,a147
v_accvgpr_read_b32 v50,a146
zaaa 224,40,16
v_accvgpr_read_b32 v49,a145
v_accvgpr_read_b32 v48,a144
zaaa 192,48,16
cv 48,48,49
cv 49,50,51
zaaa 176,52,16
cv 50,52,53
s_waitcnt vmcnt(26) lgkmcnt(0)
s_barrier
zaaa 180,52,12
dl 228,40,53,0
cv 51,54,55
zaaa 196,48,12
ra 24,233,0
v_accvgpr_read_b32 v47,a143
zaaa 200,48,8
ra 28,233,2048
v_accvgpr_read_b32 v46,a142
v_accvgpr_read_b32 v45,a141
zaaa 184,52,8
ra 32,233,4096
ra 36,233,6144
zaaa 188,52,4
v_accvgpr_read_b32 v43,a139
v_accvgpr_read_b32 v42,a138
zaaa 204,48,4
dl 229,40,53,1024
ta 96,236,24576
zaaa 172,56,4
s_add_u32 s53,s53,s57
ta 98,236,25088
v_accvgpr_read_b32 v41,a137
zaaa 156,60,4
ta 100,237,24576
v_accvgpr_read_b32 v39,a135
zaaa 152,60,8
ta 102,237,25088
ta 104,238,28672
zaaa 168,56,8
v_accvgpr_read_b32 v38,a134
v_accvgpr_read_b32 v35,a131
zaaa 164,56,12
dl 230,44,54,2048
ta 106,238,29184
zaaa 148,60,12
ta 108,239,28672
v_accvgpr_read_b32 v34,a130
zaaa 144,60,16
ta 110,239,29184
v_accvgpr_read_b32 v33,a129
zaaa 160,56,16
ta 44,235,16384
ta 46,235,16896
ta 40,234,16384
ta 42,234,16896
ta 52,237,16384
ta 54,237,16896
ta 48,236,16384
ta 50,236,16896
ta 60,235,20480
ta 62,235,20992
ta 56,234,20480
ta 58,234,20992
v_accvgpr_read_b32 v32,a128
zaaa 128,64,16
v_accvgpr_read_b32 v37,a133
v_accvgpr_read_b32 v36,a132
zaaa 112,68,16
dl 231,44,54,3072
zaaa 116,68,12
s_add_u32 m0,s61,4096
v_accvgpr_read_b32 v40,a136
v_accvgpr_read_b32 v44,a140
zaaa 132,64,12
s_add_u32 s54,s54,s57
cv 251,42,43
cv 250,40,41
zaaa 136,64,8
s_add_u32 s61,s63,s98
cv 253,46,47
)"
R"(cv 252,44,45
zaaa 120,68,8
s_add_u32 s63,s63,s99
cv 249,38,39
cv 247,34,35
zaaa 124,68,4
buffer_store_dwordx4 v[48:51],v244,s[24:27],0 offen offset:2048 sc0 sc1
zaaa 140,64,4
cv 246,32,33
cv 248,36,37
zvaa 40,72,4
ta 68,237,20480
cv 123,22,23
zvaa 44,76,4
ta 70,237,20992
cv 121,18,19
zvaa 52,76,8
ta 64,236,20480
cv 120,16,17
zvaa 48,72,8
dl 224,36,52,0
zvaa 56,72,12
ta 66,236,20992
cv 122,20,21
zvaa 60,76,12
cv 119,14,15
cv 118,12,13
zvaa 68,76,16
cv 117,10,11
cv 116,8,9
zvaa 64,72,16
cv 157,90,91
cv 156,88,89
zvaa 32,80,16
dl 225,36,52,1024
ta 76,235,24576
ta 78,235,25088
zvaa 36,84,16
ta 72,234,24576
ta 74,234,25088
zvaa 28,84,12
cv 153,82,83
cv 152,80,81
zvaa 24,80,12
cv 155,86,87
cv 154,84,85
zvaa 16,80,8
cv 149,74,75
cv 148,72,73
zvaa 20,84,8
dl 226,36,52,2048
zvaa 12,84,4
v_add_u32_e32 v232,s99,v232
cv 115,6,7
zvaa 8,80,4
cv 114,4,5
cv 113,2,3
zvaa 76,4,88
cv 112,0,1
zvaa 96,4,92
zvaa 100,8,92
dl 227,36,52,3072
zvaa 80,8,88
s_add_u32 s52,s52,s60
zvaa 84,12,88
zvaa 104,12,92
zvaa 108,16,92
s_waitcnt vmcnt(23) lgkmcnt(0)
s_barrier
zvaa 88,16,88
s_branch L_kvx
L_vtile:
L_vs4:
s_cbranch_execz L_vs8
s_waitcnt vmcnt(12)
s_barrier
ta 64,234,0
ta 66,234,512
ra 0,232,0
ta 68,235,0
ta 70,235,512
ra 4,232,2048
ra 16,232,8192
ra 20,232,10240
s_add_u32 m0,s62,32768
v_mov_b32_e32 v212,0
dl 228,40,53,0
v_mov_b32_e32 v213,0
v_mov_b32_e32 v214,0
v_mov_b32_e32 v215,0
v_mov_b32_e32 v220,0
v_mov_b32_e32 v221,0
v_mov_b32_e32 v222,0
v_mov_b32_e32 v223,0
v_mov_b32_e32 v216,0
v_mov_b32_e32 v217,0
v_mov_b32_e32 v218,0
v_mov_b32_e32 v219,0
v_mov_b32_e32 v208,0
v_mov_b32_e32 v209,0
v_mov_b32_e32 v210,0
v_mov_b32_e32 v211,0
v_mov_b32_e32 v148,0
v_mov_b32_e32 v149,0
v_mov_b32_e32 v150,0
v_mov_b32_e32 v151,0
v_mov_b32_e32 v156,0
s_waitcnt lgkmcnt(5)
zaaa 128,0,64
s_lshl_b32 s4,%1,1
ta 72,236,0
ta 74,236,512
ta 76,237,0
ta 78,237,512
ta 80,234,4096
s_waitcnt lgkmcnt(8)
zaaa 136,0,68
s_add_u32 s4,s4,s12
ta 82,234,4608
v_mov_b32_e32 v157,0
s_waitcnt lgkmcnt(6)
zaaa 140,4,68
s_mul_i32 s4,s4,1280
ta 84,235,4096
ta 86,235,4608
zaaa 132,4,64
dl 229,40,53,1024
zvaa 96,16,64
s_add_u32 s53,s53,s57
v_mov_b32_e32 v158,0
v_mov_b32_e32 v159,0
zvaa 104,16,68
s_mov_b32 s55,s4
v_mov_b32_e32 v152,0
v_mov_b32_e32 v153,0
zvaa 108,20,68
s_mul_i32 s4,%1,1792
v_mov_b32_e32 v154,0
v_mov_b32_e32 v155,0
zvaa 100,20,64
dl 230,44,54,2048
s_waitcnt lgkmcnt(6)
zvaa 116,20,72
s_lshl_b32 s88,%1,2
ra 32,233,0
v_mov_b32_e32 v144,0
s_waitcnt lgkmcnt(5)
zvaa 124,20,76
s_waitcnt vmcnt(11)
s_barrier
ra 8,232,4096
zvaa 120,16,76
s_cmp_eq_u32 s12,2
s_cselect_b32 s55,s4,s55
ra 12,232,6144
v_mov_b32_e32 v145,0
zvaa 112,16,72
dl 231,44,54,3072
zaaa 148,4,72
s_add_u32 m0,s98,36864
ra 24,232,12288
s_add_u32 s54,s54,s57
zaaa 156,4,76
s_lshl_b32 s4,%1,14
ra 28,232,14336
v_mov_b32_e32 v146,0
zaaa 152,0,76
s_add_u32 s88,s88,s12
ra 36,233,2048
v_mov_b32_e32 v147,0
zaaa 144,0,72
dl 224,36,52,0
s_waitcnt lgkmcnt(6)
zvaa 0,0,80
s_add_u32 s5,s4,16384
ra 40,233,4096
v_mov_b32_e32 v84,0
zvaa 8,0,84
s_cmp_eq_u32 s12,3
s_cselect_b32 s55,s4,s55
ra 44,233,6144
v_mov_b32_e32 v85,0
zvaa 12,4,84
s_mov_b32 s4,10
ra 48,233,8192
v_mov_b32_e32 v86,0
zvaa 4,4,80
dl 225,36,52,1024
zvaa 128,16,80
s_lshl_b32 s88,s88,12
ra 52,233,10240
v_mov_b32_e32 v87,0
zvaa 136,16,84
s_xor_b32 s87,s19,15
v_mov_b32_e32 v92,0
v_mov_b32_e32 v93,0
zvaa 140,20,84
s_cmp_eq_u32 s12,2
s_cselect_b32 s4,14,s4
ra 56,233,12288
v_mov_b32_e32 v94,0
zvaa 132,20,80
dl 226,36,52,2048
s_waitcnt lgkmcnt(5)
zvaa 32,8,64
s_add_u32 s30,s68,s88
s_addc_u32 s31,s69,0
v_mov_b32_e32 v95,0
v_mov_b32_e32 v88,0
zvaa 40,8,68
s_lshl_b32 s22,s87,7
ra 60,233,14336
v_mov_b32_e32 v89,0
zvaa 44,12,68
s_lshl_b32 s87,s87,2
v_mov_b32_e32 v90,0
v_mov_b32_e32 v91,0
zvaa 36,12,64
dl 227,36,52,3072
zvaa 160,24,64
s_add_u32 m0,s62,49152
s_add_u32 s52,s52,s60
v_mov_b32_e32 v80,0
zvaa 168,24,68
s_lshl_b32 s88,s19,2
v_mov_b32_e32 v81,0
v_mov_b32_e32 v82,0
zvaa 172,28,68
s_cmp_eq_u32 s12,3
s_cselect_b32 s4,10,s4
v_mov_b32_e32 v83,0
v_mov_b32_e32 v20,0
zvaa 164,28,64
dl 228,40,53,0
zvaa 180,28,72
s_add_u32 s87,s87,s12
v_mov_b32_e32 v21,0
v_mov_b32_e32 v22,0
zvaa 188,28,76
s_add_u32 s88,s88,s12
v_mov_b32_e32 v23,0
v_mov_b32_e32 v28,0
zvaa 184,24,76
s_waitcnt vmcnt(13)
s_barrier
ta 96,234,16384
ta 98,234,16896
ta 100,235,16384
ta 102,235,16896
zvaa 176,24,72
dl 229,40,53,1024
zvaa 52,12,72
s_lshl_b32 s90,s18,13
v_mov_b32_e32 v29,0
v_mov_b32_e32 v30,0
zvaa 60,12,76
s_add_u32 s90,s90,62914560
ta 104,236,16384
v_mov_b32_e32 v31,0
zvaa 56,8,76
s_mul_i32 s14,s87,327680
ta 106,236,16896
v_mov_b32_e32 v24,0
zvaa 48,8,72
dl 230,44,54,2048
zvaa 64,8,80
s_mul_i32 s13,s88,327680
ta 108,237,16384
v_mov_b32_e32 v25,0
zvaa 72,8,84
s_add_u32 s14,s14,s90
ta 110,237,16896
v_mov_b32_e32 v26,0
zvaa 76,12,84
s_add_u32 s13,s13,s90
v_mov_b32_e32 v27,0
v_mov_b32_e32 v16,0
zvaa 68,12,80
dl 231,44,54,3072
zvaa 192,24,80
s_add_u32 m0,s98,65536
v_mov_b32_e32 v17,0
v_mov_b32_e32 v18,0
zvaa 200,24,84
s_mul_i32 s90,s18,531456
ta 208,236,4096
v_mov_b32_e32 v19,0
zvaa 204,28,84
s_lshl_b32 s91,s12,12
ta 210,236,4608
v_lshlrev_b32_e32 v241,7,v247
zvaa 196,28,80
dl 224,36,52,0
s_waitcnt lgkmcnt(5)
maaa 128,32,96
s_add_u32 s90,s90,s91
ta 216,237,4096
v_mov_b32_e32 v242,134217728
maaa 136,32,100
s_add_u32 s90,s90,16777216
ta 218,237,4608
v_lshrrev_b32_e32 v248,3,v247
maaa 140,36,100
s_cmp_eq_u32 s12,3
s_cselect_b64 s[26:27],-1,0
ta 112,234,20480
v_and_b32_e32 v250,112,v244
maaa 132,36,96
dl 225,36,52,1024
mvaa 32,40,96
s_lshl_b32 s86,s19,1
ta 114,234,20992
v_and_b32_e32 v251,16,v247
mvaa 40,40,100
s_mov_b64 s[48:49],s[72:73]
ta 116,235,20480
v_lshlrev_b32_e32 v251,1,v251
mvaa 44,44,100
s_mov_b32 s50,8388608
ta 118,235,20992
v_mul_u32_u24_e32 v246,6144,v248
mvaa 36,44,96
dl 226,36,52,2048
mvaa 96,48,96
s_mov_b32 s59,655360
v_cmp_gt_u32_e64 s[24:25],s4,v247
v_add_u32_e32 v249,s90,v244
mvaa 104,48,100
s_add_u32 s86,s86,2
v_cndmask_b32_e64 v241,v242,v241,s[24:25]
v_add_u32_e32 v255,17006592,v249
mvaa 108,52,100
s_cmp_eq_u32 s12,2
s_cselect_b64 s[24:25],-1,0
v_accvgpr_write_b32 a240,v249
v_and_b32_e32 v249,48,v247
mvaa 100,52,96
dl 227,36,52,3072
mvaa 160,56,96
s_add_u32 s52,s52,s56
s_add_u32 m0,s98,69632
v_accvgpr_write_b32 a241,v255
mvaa 168,56,100
s_cmp_eq_u32 s12,2
s_cselect_b64 s[48:49],s[80:81],s[48:49]
ta 212,236,20480
v_xor_b32_e32 v249,v249,v250
mvaa 172,60,100
s_cmp_eq_u32 s12,2
s_cselect_b32 s50,6291456,s50
v_xor_b32_e32 v250,v250,v251
v_lshl_add_u32 v252,v248,11,v249
mvaa 164,60,96
dl 224,36,52,0
s_waitcnt lgkmcnt(1)
mvaa 180,60,104
s_cmp_eq_u32 s12,2
s_cselect_b32 s59,458752,s59
v_and_b32_e32 v251,48,v250
v_xor_b32_e32 v253,64,v252
mvaa 188,60,108
s_lshl_b32 s15,1,s86
v_accvgpr_write_b32 a242,v252
v_add_u32_e32 v246,v246,v251
mvaa 184,56,108
s_lshl_b32 s23,s19,7
v_add_u32_e32 v242,15360,v253
v_and_b32_e32 v251,64,v250
mvaa 176,56,104
dl 225,36,52,1024
mvaa 116,52,104
s_or_b32 s86,%1,s12
s_cselect_b32 s65,0,4
v_lshl_add_u32 v248,v251,5,v246
v_xor_b32_e32 v249,64,v251
mvaa 124,52,108
s_cmp_eq_u32 s12,3
s_cselect_b64 s[48:49],s[68:69],s[48:49]
v_mul_u32_u24_e32 v251,768,v251
v_accvgpr_write_b32 a243,v242
mvaa 120,48,108
s_cmp_eq_u32 s12,3
s_cselect_b32 s50,s5,s50
v_add_u32_e32 v242,30720,v252
v_lshl_add_u32 v249,v249,5,v246
mvaa 112,48,104
dl 226,36,52,2048
mvaa 52,44,104
s_cmp_eq_u32 s12,3
s_cselect_b32 s59,1280,s59
v_accvgpr_write_b32 a244,v242
v_add_u32_e32 v246,v246,v251
mvaa 60,44,108
s_add_u32 s53,s53,s58
v_add_u32_e32 v242,46080,v253
v_accvgpr_write_b32 a245,v242
mvaa 56,40,108
s_add_u32 s54,s54,s58
v_add_u32_e32 v243,4096,v246
v_add_u32_e32 v242,0,v248
mvaa 48,40,104
dl 227,36,52,3072
maaa 148,36,104
s_add_u32 s52,s52,s60
ta 214,236,20992
v_cndmask_b32_e64 v242,v228,v242,s[24:25]
maaa 156,36,108
s_mov_b32 s63,32768
ta 220,237,20480
v_cndmask_b32_e64 v242,v242,v243,s[26:27]
maaa 152,32,108
s_mov_b32 s61,s98
ta 222,237,20992
v_add_u32_e32 v243,101376,v246
maaa 144,32,104
s_mov_b32 s64,1
v_and_b32_e32 v246,15,v247
v_and_b32_e32 v246,12,v247
mvaa 0,32,112
v_accvgpr_write_b32 a246,v242
v_add_u32_e32 v242,48128,v249
mvaa 8,32,116
v_cndmask_b32_e64 v242,v229,v242,s[24:25]
v_cndmask_b32_e64 v242,v242,v243,s[26:27]
mvaa 12,36,116
v_lshlrev_b32_e32 v246,5,v246
v_accvgpr_write_b32 a247,v242
mvaa 4,36,112
v_add_u32_e32 v242,96256,v248
v_cndmask_b32_e64 v242,v230,v242,s[24:25]
mvaa 64,40,112
v_and_b32_e32 v248,3,v247
v_lshl_add_u32 v246,v248,3,v246
mvaa 72,40,116
v_accvgpr_write_b32 a248,v242
v_and_b32_e32 v248,24,v247
mvaa 76,44,116
v_add_u32_e32 v242,144384,v249
v_lshl_add_u32 v246,v248,2,v246
mvaa 68,44,112
v_and_b32_e32 v248,32,v247
v_cndmask_b32_e64 v242,v231,v242,s[24:25]
mvaa 128,48,112
v_add_u32_e32 v246,98304,v246
v_accvgpr_write_b32 a249,v242
mvaa 136,48,116
v_lshl_add_u32 v238,v248,5,v246
v_xor_b32_e32 v239,32,v238
mvaa 140,52,116
v_add_u32_e32 v232,32768,v232
v_add_u32_e32 v233,32768,v233
mvaa 132,52,112
mvaa 192,56,112
mvaa 200,56,116
mvaa 204,60,116
mvaa 196,60,112
s_add_u32 s4,s96,L_vrv-L_att_pc
s_addc_u32 s5,s97,0
s_mov_b64 exec,0
s_cmp_eq_u32 s12,0
s_cbranch_scc1 L_vs13
s_cmp_eq_u32 s12,1
s_cbranch_scc1 L_vs14
s_cmp_eq_u32 s12,2
s_cbranch_scc1 L_vs15
s_branch L_vs16
L_vrv:
s_mov_b64 exec,-1
s_sleep 6
s_waitcnt vmcnt(8)
s_waitcnt lgkmcnt(0)
s_barrier
L_v_head:
mvaa 212,28,208
L_vs5:
s_cbranch_execz L_vs9
ra 64,232,0
mvaa 212,60,212
s_add_u32 m0,s62,0
ta 192,234,32768
mvaa 220,60,220
dl 228,40,53,0
mvaa 220,28,216
ta 194,234,33280
s_nop 3
mvaa 216,24,216
ra 96,233,0
mvaa 216,56,220
ta 196,234,49152
mvaa 208,56,212
ta 198,234,49664
mvaa 208,24,208
ta 204,235,49152
mvaa 148,20,208
dl 229,40,53,1024
mvaa 148,52,212
s_add_u32 s53,s53,s57
ta 206,235,49664
mvaa 156,52,220
ta 200,235,32768
mvaa 156,20,216
ta 202,235,33280
mvaa 152,16,216
ra 68,232,2048
mvaa 152,48,220
ra 100,233,2048
mvaa 144,48,212
ra 72,232,4096
mvaa 144,16,208
dl 230,44,54,2048
mvaa 84,12,208
ra 104,233,4096
mvaa 84,44,212
ra 76,232,6144
mvaa 92,44,220
ra 108,233,6144
mvaa 92,12,216
ra 80,232,8192
mvaa 88,8,216
ra 112,233,8192
mvaa 88,40,220
dl 231,44,54,3072
mvaa 80,40,212
s_add_u32 s54,s54,s57
ra 84,232,10240
mvaa 80,8,208
ra 116,233,10240
mvaa 20,4,208
ra 88,232,12288
mvaa 20,36,212
ra 120,233,12288
mvaa 28,36,220
ra 92,232,14336
mvaa 28,4,216
s_add_u32 m0,s62,16384
ra 124,233,14336
mvaa 24,0,216
dl 228,40,53,0
mvaa 24,32,220
mvaa 16,32,212
mvaa 16,0,208
s_waitcnt lgkmcnt(8)
maaa 128,64,192
maaa 128,96,196
maaa 136,96,204
dl 229,40,53,1024
maaa 136,64,200
s_add_u32 s53,s53,s58
maaa 140,68,200
maaa 140,100,204
maaa 132,100,196
maaa 132,68,192
mvaa 32,72,192
mvaa 32,104,196
dl 230,44,54,2048
mvaa 40,104,204
mvaa 40,72,200
s_waitcnt lgkmcnt(0)
mvaa 44,76,200
ta 208,236,32768
mvaa 44,108,204
ta 210,236,33280
mvaa 36,108,196
ta 212,236,49152
mvaa 36,76,192
dl 231,44,54,3072
mvaa 96,80,192
s_add_u32 s54,s54,s58
ta 214,236,49664
mvaa 96,112,196
ta 220,237,49152
mvaa 104,112,204
ta 222,237,49664
mvaa 104,80,200
ta 216,237,32768
mvaa 108,84,200
ta 218,237,33280
mvaa 108,116,204
mvaa 100,116,196
s_add_u32 m0,s61,0
mvaa 100,84,192
dl 224,36,52,0
mvaa 160,88,192
mvaa 160,120,196
mvaa 168,120,204
mvaa 168,88,200
mvaa 172,92,200
mvaa 172,124,204
mvaa 164,124,196
dl 225,36,52,1024
mvaa 164,92,192
s_waitcnt lgkmcnt(0)
mvaa 180,92,208
mvaa 180,124,212
mvaa 188,124,220
ta 192,234,36864
mvaa 188,92,216
ta 194,234,37376
mvaa 184,88,216
ta 196,234,53248
mvaa 184,120,220
dl 226,36,52,2048
mvaa 176,120,212
ta 198,234,53760
mvaa 176,88,208
ta 204,235,53248
mvaa 116,84,208
ta 206,235,53760
mvaa 116,116,212
ta 200,235,36864
mvaa 124,116,220
ta 202,235,37376
mvaa 124,84,216
mvaa 120,80,216
dl 227,36,52,3072
mvaa 120,112,220
s_add_u32 s52,s52,s56
mvaa 112,112,212
mvaa 112,80,208
mvaa 52,76,208
mvaa 52,108,212
mvaa 60,108,220
mvaa 60,76,216
mvaa 56,72,216
s_add_u32 m0,s61,4096
mvaa 56,104,220
dl 224,36,52,0
mvaa 48,104,212
s_add_u32 s61,s63,s98
mvaa 48,72,208
s_cmp_ge_u32 s63,65536
s_cselect_b32 s99,-2,1
s_lshl_b32 s99,s99,15
maaa 148,68,208
s_add_u32 s63,s63,s99
v_add_u32_e32 v232,s99,v232
v_add_u32_e32 v233,s99,v233
maaa 148,100,212
maaa 156,100,220
maaa 156,68,216
maaa 152,64,216
dl 225,36,52,1024
maaa 152,96,220
maaa 144,96,212
s_waitcnt lgkmcnt(0)
maaa 144,64,208
mvaa 0,64,192
mvaa 0,96,196
mvaa 8,96,204
ta 208,236,36864
mvaa 8,64,200
ta 210,236,37376
mvaa 12,68,200
dl 226,36,52,2048
mvaa 12,100,204
ta 212,236,53248
mvaa 4,100,196
ta 214,236,53760
mvaa 4,68,192
ta 220,237,53248
mvaa 64,72,192
ta 222,237,53760
mvaa 64,104,196
ta 216,237,36864
mvaa 72,104,204
ta 218,237,37376
mvaa 72,72,200
dl 227,36,52,3072
mvaa 76,76,200
s_add_u32 s52,s52,s60
mvaa 76,108,204
mvaa 68,108,196
mvaa 68,76,192
mvaa 128,80,192
mvaa 128,112,196
buffer_load_dword a184,v241,s[48:51],s55 offen
mvaa 136,112,204
s_add_u32 s55,s55,s59
mvaa 136,80,200
mvaa 140,84,200
mvaa 140,116,204
mvaa 132,116,196
mvaa 132,84,192
buffer_load_dword a185,v241,s[48:51],s55 offen
mvaa 192,88,192
s_add_u32 s55,s55,s59
mvaa 192,120,196
mvaa 200,120,204
mvaa 200,88,200
mvaa 204,92,200
mvaa 204,124,204
s_cmp_eq_u32 s64,3
mvaa 196,124,196
s_waitcnt vmcnt(10) lgkmcnt(0)
s_barrier
mvaa 196,92,192
s_cbranch_scc1 L_v_exit
mvaa 212,92,208
ra 0,232,0
mvaa 212,124,212
s_add_u32 m0,s62,32768
ta 192,234,0
mvaa 220,124,220
dl 228,40,53,0
mvaa 220,92,216
ta 194,234,512
s_nop 3
mvaa 216,88,216
ra 32,233,0
mvaa 216,120,220
ta 196,234,16384
mvaa 208,120,212
ta 198,234,16896
mvaa 208,88,208
ta 204,235,16384
mvaa 148,84,208
dl 229,40,53,1024
mvaa 148,116,212
s_add_u32 s53,s53,s57
ta 206,235,16896
mvaa 156,116,220
ta 200,235,0
mvaa 156,84,216
ta 202,235,512
mvaa 152,80,216
ra 4,232,2048
mvaa 152,112,220
ra 36,233,2048
mvaa 144,112,212
ra 8,232,4096
mvaa 144,80,208
dl 230,44,54,2048
mvaa 84,76,208
ra 40,233,4096
mvaa 84,108,212
ra 12,232,6144
mvaa 92,108,220
ra 44,233,6144
mvaa 92,76,216
ra 16,232,8192
mvaa 88,72,216
ra 48,233,8192
mvaa 88,104,220
dl 231,44,54,3072
mvaa 80,104,212
s_add_u32 s54,s54,s57
ra 20,232,10240
mvaa 80,72,208
ra 52,233,10240
mvaa 20,68,208
ra 24,232,12288
mvaa 20,100,212
ra 56,233,12288
mvaa 28,100,220
ra 28,232,14336
mvaa 28,68,216
s_add_u32 m0,s62,49152
ra 60,233,14336
mvaa 24,64,216
dl 228,40,53,0
mvaa 24,96,220
mvaa 16,96,212
mvaa 16,64,208
s_waitcnt lgkmcnt(8)
maaa 128,0,192
maaa 128,32,196
maaa 136,32,204
dl 229,40,53,1024
maaa 136,0,200
s_add_u32 s53,s53,s58
maaa 140,4,200
maaa 140,36,204
maaa 132,36,196
maaa 132,4,192
mvaa 32,8,192
mvaa 32,40,196
dl 230,44,54,2048
mvaa 40,40,204
mvaa 40,8,200
s_waitcnt lgkmcnt(0)
mvaa 44,12,200
ta 208,236,0
mvaa 44,44,204
ta 210,236,512
mvaa 36,44,196
ta 212,236,16384
mvaa 36,12,192
dl 231,44,54,3072
mvaa 96,16,192
s_add_u32 s54,s54,s58
ta 214,236,16896
mvaa 96,48,196
ta 220,237,16384
mvaa 104,48,204
ta 222,237,16896
mvaa 104,16,200
ta 216,237,0
mvaa 108,20,200
ta 218,237,512
mvaa 108,52,204
mvaa 100,52,196
s_add_u32 m0,s61,0
mvaa 100,20,192
dl 224,36,52,0
mvaa 160,24,192
s_lshl_b32 s86,s21,2
s_add_u32 s86,s86,s12
s_mul_i32 s86,s86,384
mvaa 160,56,196
s_add_u32 s86,s86,4096
v_lshl_add_u32 v253,v244,3,s86
mvaa 168,56,204
s_mov_b64 exec,7
global_load_dword a188,v253,s[96:97]
s_mov_b64 exec,-1
mvaa 168,24,200
mvaa 172,28,200
mvaa 172,60,204
mvaa 164,60,196
dl 225,36,52,1024
mvaa 164,28,192
s_waitcnt lgkmcnt(0)
mvaa 180,28,208
mvaa 180,60,212
mvaa 188,60,220
ta 192,234,4096
mvaa 188,28,216
ta 194,234,4608
mvaa 184,24,216
ta 196,234,20480
mvaa 184,56,220
dl 226,36,52,2048
mvaa 176,56,212
ta 198,234,20992
mvaa 176,24,208
ta 204,235,20480
mvaa 116,20,208
ta 206,235,20992
mvaa 116,52,212
ta 200,235,4096
mvaa 124,52,220
ta 202,235,4608
mvaa 124,20,216
mvaa 120,16,216
dl 227,36,52,3072
mvaa 120,48,220
s_add_u32 s52,s52,s56
mvaa 112,48,212
mvaa 112,16,208
mvaa 52,12,208
mvaa 52,44,212
s_bitcmp1_b32 s65,s64
s_cbranch_scc1 L_v_ev
L_v_evr:
mvaa 60,44,220
mvaa 60,12,216
mvaa 56,8,216
s_add_u32 m0,s61,4096
mvaa 56,40,220
dl 224,36,52,0
mvaa 48,40,212
s_add_u32 s61,s63,s98
mvaa 48,8,208
s_cmp_ge_u32 s63,65536
s_cselect_b32 s99,-2,1
s_lshl_b32 s99,s99,15
maaa 148,4,208
s_add_u32 s63,s63,s99
v_add_u32_e32 v232,s99,v232
v_add_u32_e32 v233,s99,v233
maaa 148,36,212
maaa 156,36,220
maaa 156,4,216
maaa 152,0,216
dl 225,36,52,1024
maaa 152,32,220
maaa 144,32,212
s_waitcnt lgkmcnt(0)
maaa 144,0,208
mvaa 0,0,192
mvaa 0,32,196
mvaa 8,32,204
ta 208,236,4096
mvaa 8,0,200
ta 210,236,4608
mvaa 12,4,200
dl 226,36,52,2048
mvaa 12,36,204
ta 212,236,20480
mvaa 4,36,196
ta 214,236,20992
mvaa 4,4,192
ta 220,237,20480
mvaa 64,8,192
ta 222,237,20992
mvaa 64,40,196
ta 216,237,4096
mvaa 72,40,204
ta 218,237,4608
mvaa 72,8,200
dl 227,36,52,3072
mvaa 76,12,200
s_add_u32 s52,s52,s60
mvaa 76,44,204
mvaa 68,44,196
mvaa 68,12,192
mvaa 128,16,192
mvaa 128,48,196
buffer_load_dword a187,v241,s[48:51],s55 offen
mvaa 136,48,204
s_add_u32 s55,s55,s59
mvaa 136,16,200
mvaa 140,20,200
mvaa 140,52,204
mvaa 132,52,196
mvaa 132,20,192
buffer_load_dword a190,v241,s[48:51],s55 offen
mvaa 192,24,192
s_add_u32 s55,s55,s59
mvaa 192,56,196
mvaa 200,56,204
s_add_u32 s64,s64,1
mvaa 200,24,200
mvaa 204,28,200
mvaa 204,60,204
mvaa 196,60,196
s_waitcnt vmcnt(10) lgkmcnt(0)
s_barrier
mvaa 196,28,192
s_branch L_v_head
L_v_exit:
global_load_dwordx4 a[160:163],v244,s[30:31] offset:0
s_add_u32 m0,s62,32768
mvaa 212,92,208
s_cmp_lt_u32 s12,2
s_cselect_b32 s86,s22,s23
ta 32,234,0
s_and_b32 s87,s12,1
mvaa 212,124,212
s_lshl_b32 s86,s86,11
ta 34,234,512
s_lshl_b32 s87,s87,17
mvaa 220,124,220
s_add_u32 s86,s86,s87
ra 0,232,0
s_mov_b64 s[36:37],s[68:69]
mvaa 220,92,216
s_mov_b32 s38,4194304
ta 36,235,0
s_mov_b32 s52,s86
mvaa 216,88,216
s_mov_b32 s56,65536
ta 38,235,512
v_accvgpr_read_b32 v224,a242
mvaa 216,120,220
dl 228,40,53,0
mvaa 208,120,212
s_lshl_b32 s88,s18,1
ra 4,232,2048
v_accvgpr_read_b32 v225,a243
mvaa 208,88,208
s_add_u32 s88,s88,s12
ra 8,232,4096
v_accvgpr_read_b32 v226,a244
mvaa 148,84,208
s_mov_b32 s91,196608
ra 12,232,6144
v_accvgpr_read_b32 v227,a245
mvaa 148,116,212
s_lshl_b32 s88,s88,7
ra 16,232,8192
s_lshl_b32 s89,s18,6
mvaa 156,116,220
s_mov_b32 s90,6291456
ra 20,232,10240
s_mov_b32 s60,-65408
mvaa 156,84,216
dl 229,40,53,1024
mvaa 152,80,216
s_add_u32 s53,s53,s57
ra 24,232,12288
s_bfe_u32 s4,s21,65536
mvaa 152,112,220
s_lshl_b32 s4,s4,6
ra 28,232,14336
s_lshl_b32 s87,s20,2
mvaa 144,112,212
s_mov_b32 s26,278528
ta 40,236,0
s_mov_b32 s27,131072
mvaa 144,80,208
s_add_u32 s61,s98,32768
ta 42,236,512
s_mov_b32 s63,65536
mvaa 84,76,208
s_cmp_ge_u32 s63,65536
s_cselect_b32 s99,-2,1
ta 44,237,0
s_lshl_b32 s99,s99,15
mvaa 84,108,212
)"
R"(dl 230,44,54,2048
mvaa 92,108,220
s_cmp_eq_u32 s12,0
s_cselect_b32 s65,96,0
ta 46,237,512
s_mov_b32 s67,2048
mvaa 92,76,216
s_mov_b32 s66,8
ta 48,234,4096
s_mov_b32 s64,1
mvaa 88,72,216
s_or_b32 s65,s65,128
ta 50,234,4608
mvaa 88,104,220
s_bitcmp1_b32 s12,0
s_cselect_b64 vcc,-1,0
ta 52,235,4096
mvaa 80,104,212
ta 54,235,4608
mvaa 80,72,208
dl 231,44,54,3072
s_add_u32 m0,s62,49152
mvaa 20,68,208
s_add_u32 s54,s54,s57
ta 56,236,4096
mvaa 20,100,212
L_vs6:
s_cbranch_execz L_vs10
s_cmp_lt_u32 s12,2
s_cselect_b32 s57,262144,s91
ta 58,236,4608
mvaa 28,100,220
ta 60,237,4096
mvaa 28,68,216
ta 62,237,4608
mvaa 24,64,216
mvaa 24,96,220
dl 228,40,53,0
mvaa 16,96,212
v_accvgpr_read_b32 v228,a246
mvaa 16,64,208
s_waitcnt lgkmcnt(5)
maaa 128,0,32
maaa 136,0,36
maaa 140,4,36
dl 229,40,53,1024
maaa 132,4,32
s_waitcnt lgkmcnt(0)
s_barrier
mvaa 32,8,32
s_add_u32 s53,s53,s58
ta 96,234,16384
v_accvgpr_read_b32 v229,a247
mvaa 40,8,36
s_cmp_lt_u32 s12,2
s_cselect_b32 s42,8388608,s90
ta 98,234,16896
mvaa 44,12,36
s_cmp_lt_u32 s12,2
s_cselect_b32 s53,s88,s89
ra 64,233,0
s_and_b32 s89,s98,8192
mvaa 36,12,32
dl 230,44,54,2048
mvaa 96,16,32
s_bitcmp1_b32 s21,0
s_cselect_b32 s90,s57,0
ta 100,235,16384
s_add_u32 s53,s53,s90
mvaa 104,16,36
s_cmp_lt_u32 s12,2
s_cselect_b64 s[40:41],s[72:73],s[80:81]
ta 102,235,16896
v_accvgpr_read_b32 v230,a248
mvaa 108,20,36
s_lshl_b32 s90,s90,1
ra 68,233,2048
s_add_u32 s91,s89,65536
mvaa 100,20,32
s_bfe_u32 s88,s21,65539
ra 72,233,4096
s_lshl_b32 s88,s88,1
mvaa 160,24,32
dl 231,44,54,3072
mvaa 168,24,36
s_add_u32 m0,s98,65536
ra 76,233,6144
s_add_u32 s54,s54,s58
mvaa 172,28,36
s_cmp_eq_u32 s12,3
s_cselect_b64 s[44:45],s[68:69],s[40:41]
ra 80,233,8192
v_accvgpr_read_b32 v231,a249
mvaa 164,28,32
s_cmp_eq_u32 s12,3
s_cselect_b32 s46,0,s42
ra 84,233,10240
s_add_u32 s58,s57,s90
mvaa 180,28,40
s_cmp_eq_u32 s12,3
s_cselect_b32 s54,0,s53
ra 88,233,12288
s_sub_u32 s57,s57,s90
mvaa 188,28,44
global_load_dwordx4 a[168:171],v244,s[30:31] offset:2048
mvaa 184,24,44
s_add_u32 s90,s89,65536
ta 104,236,16384
s_add_u32 s88,s88,s87
mvaa 176,24,40
s_and_b32 s87,s12,1
ra 92,233,14336
s_add_u32 s88,s88,s87
mvaa 116,20,40
s_mul_i32 s88,s88,531456
ta 106,236,16896
s_and_b32 s87,s21,7
mvaa 124,20,44
s_add_u32 s86,s88,33783808
ta 108,237,16384
s_lshl_b32 s87,s87,15
mvaa 120,16,44
global_load_dwordx4 a[176:179],v244,s[84:85] offset:0
mvaa 112,16,40
s_add_u32 s86,s86,s87
ta 110,237,16896
s_lshr_b32 s87,s12,1
mvaa 52,12,40
s_lshl_b32 s87,s87,14
ta 112,234,20480
s_add_u32 s86,s86,s87
mvaa 60,12,44
s_add_u32 s24,s8,s86
s_addc_u32 s25,s9,0
ta 114,234,20992
s_lshl_b32 s86,%1,5
mvaa 56,8,44
s_lshl_b32 s87,s12,3
ta 116,235,20480
s_add_u32 s86,s86,s87
mvaa 48,8,40
global_load_dwordx4 a[164:167],v244,s[30:31] offset:1024
maaa 148,4,40
s_add_u32 s86,s86,83886080
ta 118,235,20992
s_mov_b32 s88,4096
maaa 156,4,44
ta 120,236,20480
maaa 152,0,44
ta 122,236,20992
maaa 144,0,40
ta 124,237,20480
mvaa 0,0,48
global_load_dwordx4 a[172:175],v244,s[30:31] offset:3072
mvaa 8,0,52
s_mov_b32 s30,1048577
ta 126,237,20992
s_mov_b32 s31,268435712
mvaa 12,4,52
mvaa 4,4,48
mvaa 64,8,48
mvaa 72,8,52
global_load_dwordx4 a[180:183],v244,s[84:85] offset:1024
mvaa 76,12,52
mvaa 68,12,48
mvaa 128,16,48
mvaa 136,16,52
mvaa 140,20,52
dl 224,36,52,0
mvaa 132,20,48
mvaa 192,24,48
mvaa 200,24,52
mvaa 204,28,52
mvaa 196,28,48
dl 225,36,52,1024
mvaa 212,28,56
mvaa 220,28,60
mvaa 216,24,60
mvaa 208,24,56
mvaa 148,20,56
dl 226,36,52,2048
mvaa 156,20,60
mvaa 152,16,60
mvaa 144,16,56
mvaa 84,12,56
s_waitcnt vmcnt(12) lgkmcnt(0)
s_barrier
mvaa 92,12,60
dl 227,36,52,3072
mvaa 88,8,60
s_add_u32 s52,s52,s56
s_add_u32 m0,s98,69632
ta 32,234,32768
mvaa 80,8,56
ta 34,234,33280
mvaa 20,4,56
ta 36,235,32768
mvaa 28,4,60
ta 38,235,33280
mvaa 24,0,60
dl 224,36,52,0
mvaa 16,0,56
ta 40,236,32768
maaa 128,64,96
ta 42,236,33280
maaa 136,64,100
ta 44,237,32768
maaa 140,68,100
ta 46,237,33280
maaa 132,68,96
dl 225,36,52,1024
mvaa 32,72,96
ta 48,234,36864
mvaa 40,72,100
ta 50,234,37376
mvaa 44,76,100
ta 52,235,36864
mvaa 36,76,96
ta 54,235,37376
mvaa 96,80,96
dl 226,36,52,2048
mvaa 104,80,100
ra 20,232,43008
mvaa 108,84,100
ra 0,232,32768
mvaa 100,84,96
ra 24,232,45056
mvaa 160,88,96
ra 4,232,34816
mvaa 168,88,100
dl 227,36,52,3072
mvaa 172,92,100
s_add_u32 m0,s62,0
ra 28,232,47104
s_add_u32 s52,s52,s60
mvaa 164,92,96
ra 8,232,36864
mvaa 180,92,104
ra 12,232,38912
mvaa 188,92,108
ra 16,232,40960
v_add_u32_e32 v232,s90,v232
mvaa 184,88,108
dl 228,40,53,0
mvaa 176,88,104
ta 60,237,36864
v_xor_b32_e32 v232,s4,v232
mvaa 116,84,104
ta 62,237,37376
mvaa 124,84,108
ta 56,236,36864
mvaa 120,80,108
ta 58,236,37376
mvaa 112,80,104
dl 229,40,53,1024
mvaa 52,76,104
s_add_u32 s53,s53,s57
mvaa 60,76,108
mvaa 56,72,108
mvaa 48,72,104
maaa 148,68,104
dl 230,44,54,2048
maaa 156,68,108
maaa 152,64,108
maaa 144,64,104
mvaa 0,64,112
mvaa 8,64,116
dl 231,44,54,3072
mvaa 12,68,116
s_add_u32 m0,s62,16384
s_add_u32 s54,s54,s57
mvaa 4,68,112
mvaa 64,72,112
mvaa 72,72,116
mvaa 76,76,116
dl 228,40,53,0
mvaa 68,76,112
mvaa 128,80,112
mvaa 136,80,116
mvaa 140,84,116
mvaa 132,84,112
dl 229,40,53,1024
mvaa 192,88,112
s_add_u32 s53,s53,s58
mvaa 200,88,116
mvaa 204,92,116
mvaa 196,92,112
mvaa 212,92,120
dl 230,44,54,2048
mvaa 220,92,124
mvaa 216,88,124
s_waitcnt vmcnt(20) lgkmcnt(0)
s_barrier
mvaa 208,88,120
ra 92,233,47104
mvaa 148,84,120
ta 96,234,49152
mvaa 156,84,124
dl 231,44,54,3072
mvaa 152,80,124
s_add_u32 m0,s98,0
ra 88,233,45056
s_add_u32 s54,s54,s58
mvaa 144,80,120
ta 98,234,49664
mvaa 84,76,120
ta 100,235,49152
mvaa 92,76,124
ta 116,235,53248
mvaa 88,72,124
dl 224,36,52,0
mvaa 80,72,120
ta 118,235,53760
mvaa 20,68,120
ta 102,235,49664
v_subrev_u32_e32 v235,s89,v235
mvaa 28,68,124
ta 104,236,49152
mvaa 24,64,124
ta 112,234,53248
mvaa 16,64,120
dl 225,36,52,1024
maaa 128,0,32
ta 124,237,53248
maaa 136,0,36
ta 126,237,53760
maaa 140,4,36
ta 120,236,53248
maaa 132,4,32
ta 122,236,53760
mvaa 32,8,32
dl 226,36,52,2048
mvaa 40,8,36
ta 114,234,53760
v_subrev_u32_e32 v234,s89,v234
mvaa 44,12,36
ta 106,236,49664
v_subrev_u32_e32 v236,s89,v236
mvaa 36,12,32
ta 108,237,49152
mvaa 52,12,40
ta 110,237,49664
v_subrev_u32_e32 v237,s89,v237
mvaa 60,12,44
dl 227,36,52,3072
mvaa 56,8,44
s_add_u32 s52,s52,s56
s_add_u32 m0,s98,4096
ra 84,233,43008
mvaa 48,8,40
ra 80,233,40960
maaa 148,4,40
ra 72,233,36864
maaa 156,4,44
ra 76,233,38912
maaa 152,0,44
dl 224,36,52,0
maaa 144,0,40
ra 68,233,34816
mvaa 0,0,48
ra 64,233,32768
v_add_u32_e32 v233,s91,v233
mvaa 8,0,52
v_xor_b32_e32 v233,s4,v233
mvaa 12,4,52
mvaa 4,4,48
dl 225,36,52,1024
mvaa 20,4,56
mvaa 28,4,60
mvaa 24,0,60
mvaa 16,0,56
mvaa 220,28,60
dl 226,36,52,2048
s_waitcnt lgkmcnt(2)
mvaa 220,92,124
mvaa 216,88,124
mvaa 216,24,60
mvaa 212,28,56
mvaa 212,92,120
dl 227,36,52,3072
mvaa 208,88,120
s_add_u32 m0,s62,32768
s_add_u32 s52,s52,s60
s_waitcnt vmcnt(25)
zvaa 246,160,176
mvaa 208,24,56
cv 223,222,223
cv 222,220,221
mvaa 204,28,52
cv 221,218,219
zvaa 250,168,176
mvaa 204,92,116
cv 220,216,217
cv 219,214,215
mvaa 200,88,116
dl 228,40,53,0
s_waitcnt vmcnt(25)
mvaa 246,164,180
mvaa 200,24,52
cv 218,212,213
mvaa 250,172,180
mvaa 196,28,48
cv 217,210,211
cv 216,208,209
mvaa 196,92,112
cv 215,206,207
v_cndmask_b32_e64 v246,0,v246,s[30:31]
mvaa 192,88,112
cv 214,204,205
v_cndmask_b32_e64 v250,0,v250,s[30:31]
mvaa 192,24,48
dl 229,40,53,1024
s_lshl_b64 s[30:31],s[30:31],1
v_cndmask_b32_e64 v246,v246,v247,s[30:31]
v_cndmask_b32_e64 v250,v250,v251,s[30:31]
mvaa 188,28,44
s_lshl_b64 s[30:31],s[30:31],1
s_add_u32 s53,s53,s57
v_cndmask_b32_e64 v246,v246,v248,s[30:31]
v_cndmask_b32_e64 v250,v250,v252,s[30:31]
mvaa 188,92,108
s_lshl_b64 s[30:31],s[30:31],1
cv 213,202,203
v_cndmask_b32_e64 v246,v246,v249,s[30:31]
mvaa 184,88,108
v_cndmask_b32_e64 v250,v250,v253,s[30:31]
v_add_f32_dpp v246,v246,v246 quad_perm:[1,0,3,2] row_mask:0xf bank_mask:0xf
mvaa 184,24,44
v_add_f32_dpp v250,v250,v250 quad_perm:[1,0,3,2] row_mask:0xf bank_mask:0xf
v_add_f32_dpp v246,v246,v246 quad_perm:[2,3,0,1] row_mask:0xf bank_mask:0xf
mvaa 180,28,40
dl 230,44,54,2048
v_add_f32_dpp v250,v250,v250 quad_perm:[2,3,0,1] row_mask:0xf bank_mask:0xf
mvaa 180,92,104
v_add_f32_dpp v246,v246,v246 row_half_mirror row_mask:0xf bank_mask:0xf
v_add_f32_dpp v250,v250,v250 row_half_mirror row_mask:0xf bank_mask:0xf
mvaa 176,88,104
v_add_f32_dpp v246,v246,v246 row_mirror row_mask:0xf bank_mask:0xf
v_add_f32_dpp v250,v250,v250 row_mirror row_mask:0xf bank_mask:0xf
mvaa 176,24,40
cv 212,200,201
cv 211,198,199
mvaa 172,28,36
v_permlane16_swap_b32_e32 v246,v250
mvaa 172,92,100
dl 231,44,54,3072
v_add_f32_e32 v246,v246,v250
v_mov_b32_e32 v250,v246
mvaa 168,88,100
s_add_u32 s54,s54,s57
v_permlane32_swap_b32_e32 v246,v250
mvaa 168,24,36
v_add_f32_e32 v246,v246,v250
v_lshrrev_b32_e32 v250,6,v244
mvaa 164,28,32
v_mul_f32_e32 v246,0xbfb8aa3b,v246
cv 210,196,197
mvaa 164,92,96
ex 246
mvaa 160,88,96
buffer_load_dword a242,v241,s[48:51],s55 offen
mvaa 160,24,32
s_add_u32 s55,s55,s59
v_add_f32_e32 v246,1.0,v246
cv 209,194,195
mvaa 156,20,60
v_rcp_f32_e32 v246,v246
mvaa 156,84,124
s_waitcnt vmcnt(17) lgkmcnt(0)
s_barrier
mvaa 152,80,124
s_add_u32 m0,s62,49152
cv 208,192,193
cv 207,190,191
mvaa 152,16,60
dl 228,40,53,0
mvaa 148,20,56
cv 206,188,189
cv 205,186,187
mvaa 148,84,120
cv 204,184,185
cv 203,182,183
mvaa 144,80,120
cv 202,180,181
cv 201,178,179
mvaa 144,16,56
cv 200,176,177
cv 199,174,175
mvaa 140,20,52
dl 229,40,53,1024
mvaa 140,84,116
s_add_u32 s53,s53,s58
cv 198,172,173
cv 197,170,171
mvaa 136,80,116
ra 4,232,0
cv 196,168,169
mvaa 136,16,52
ta 92,239,12288
cv 195,166,167
mvaa 132,20,48
ta 94,239,12800
cv 194,164,165
mvaa 132,84,112
dl 230,44,54,2048
mvaa 128,80,112
ta 88,238,12288
cv 193,162,163
mvaa 128,16,48
ta 90,238,12800
cv 192,160,161
mvaa 124,20,44
cv 191,158,159
cv 190,156,157
mvaa 124,84,108
cv 189,154,155
cv 188,152,153
mvaa 120,80,108
dl 231,44,54,3072
mvaa 120,16,44
s_add_u32 m0,s98,32768
cv 187,150,151
cv 186,148,149
mvaa 116,20,40
s_add_u32 s54,s54,s58
cv 185,146,147
cv 184,144,145
mvaa 116,84,104
cv 183,142,143
cv 182,140,141
mvaa 112,80,104
cv 181,138,139
cv 180,136,137
mvaa 112,16,40
buffer_load_dword a243,v241,s[48:51],s55 offen
mvaa 108,20,36
s_add_u32 s55,s55,s59
cv 179,134,135
cv 178,132,133
mvaa 108,84,100
cv 177,130,131
cv 176,128,129
mvaa 104,80,100
cv 175,126,127
cv 174,124,125
mvaa 104,16,36
ta 44,235,0
cv 173,122,123
mvaa 100,20,32
dl 224,36,52,0
mvaa 100,84,96
ta 46,235,512
cv 172,120,121
mvaa 96,80,96
cv 171,118,119
cv 170,116,117
mvaa 96,16,32
ta 40,234,0
cv 169,114,115
mvaa 92,12,60
ta 42,234,512
cv 168,112,113
mvaa 92,76,124
dl 225,36,52,1024
mvaa 88,72,124
cv 167,110,111
cv 166,108,109
mvaa 88,8,60
cv 165,106,107
cv 164,104,105
mvaa 84,12,56
ta 84,237,8192
cv 163,102,103
mvaa 84,76,120
ta 80,236,8192
cv 162,100,101
mvaa 80,72,120
dl 226,36,52,2048
mvaa 80,8,56
ta 86,237,8704
cv 161,98,99
mvaa 76,12,52
L_vs7:
s_cbranch_execz L_vs11
ta 82,236,8704
cv 160,96,97
mvaa 76,76,116
ra 16,232,6144
cv 159,94,95
mvaa 72,72,116
ta 60,235,4096
cv 158,92,93
mvaa 72,8,52
dl 227,36,52,3072
mvaa 68,12,48
s_add_u32 m0,s62,0
ta 56,234,4096
s_add_u32 s52,s52,s56
mvaa 68,76,112
ta 62,235,4608
cv 151,78,79
mvaa 64,72,112
ta 58,234,4608
cv 150,76,77
mvaa 64,8,48
ta 52,237,0
cv 157,90,91
mvaa 60,76,108
buffer_load_dword a244,v241,s[48:51],s55 offen
mvaa 56,72,108
s_add_u32 s55,s55,s59
ra 12,232,4096
cv 147,70,71
mvaa 52,76,104
ta 54,237,512
cv 146,68,69
mvaa 48,72,104
ra 8,232,2048
cv 145,66,67
mvaa 44,76,100
ta 48,236,0
cv 144,64,65
mvaa 40,72,100
buffer_load_dword a245,v241,s[48:51],s55 offen
mvaa 36,76,96
s_add_u32 s55,s55,s59
ta 50,236,512
cv 143,62,63
mvaa 32,72,96
cv 142,60,61
cv 141,58,59
mvaa 28,68,124
cv 140,56,57
cv 139,54,55
mvaa 24,64,124
cv 138,52,53
cv 137,50,51
mvaa 20,68,120
s_mov_b64 exec,65537
buffer_store_dword v246,v250,s[8:11],s86 offen sc0 sc1
s_mov_b64 exec,-1
mvaa 16,64,120
cv 136,48,49
cv 135,46,47
mvaa 12,68,116
ta 76,235,8192
cv 134,44,45
mvaa 8,64,116
ta 78,235,8704
cv 133,42,43
mvaa 4,68,112
ta 72,234,8192
cv 132,40,41
mvaa 0,64,112
ta 74,234,8704
cv 131,38,39
maaa 156,68,108
cv 130,36,37
cv 129,34,35
maaa 152,64,108
cv 128,32,33
cv 127,30,31
maaa 148,68,104
cv 126,28,29
cv 125,26,27
maaa 144,64,104
v_accvgpr_read_b32 v63,a159
v_accvgpr_read_b32 v62,a158
maaa 140,68,100
v_accvgpr_read_b32 v61,a157
v_accvgpr_read_b32 v60,a156
maaa 136,64,100
v_accvgpr_read_b32 v59,a155
v_accvgpr_read_b32 v58,a154
maaa 132,68,96
v_accvgpr_read_b32 v57,a153
v_accvgpr_read_b32 v56,a152
maaa 128,64,96
cv 56,56,57
cv 57,58,59
s_waitcnt lgkmcnt(2)
zaaa 236,40,4
ta 68,237,4096
ta 70,237,4608
zaaa 220,44,4
ta 64,236,4096
ta 66,236,4608
zaaa 216,44,8
cv 58,60,61
cv 59,62,63
zaaa 232,40,8
buffer_store_dwordx4 v[56:59],v244,s[24:27],0 offen offset:3072 sc0 sc1
zaaa 228,40,12
v_accvgpr_read_b32 v55,a151
v_accvgpr_read_b32 v54,a150
zaaa 212,44,12
v_accvgpr_read_b32 v53,a149
v_accvgpr_read_b32 v52,a148
zaaa 208,44,16
v_accvgpr_read_b32 v51,a147
v_accvgpr_read_b32 v50,a146
zaaa 224,40,16
v_accvgpr_read_b32 v49,a145
v_accvgpr_read_b32 v48,a144
zaaa 192,48,16
cv 48,48,49
cv 49,50,51
zaaa 176,52,16
cv 50,52,53
s_waitcnt vmcnt(26) lgkmcnt(0)
s_barrier
zaaa 180,52,12
dl 228,40,53,0
cv 51,54,55
zaaa 196,48,12
ra 24,233,0
v_accvgpr_read_b32 v47,a143
zaaa 200,48,8
ra 28,233,2048
v_accvgpr_read_b32 v46,a142
v_accvgpr_read_b32 v45,a141
zaaa 184,52,8
ra 32,233,4096
ra 36,233,6144
zaaa 188,52,4
v_accvgpr_read_b32 v43,a139
v_accvgpr_read_b32 v42,a138
zaaa 204,48,4
dl 229,40,53,1024
ta 96,236,24576
zaaa 172,56,4
s_add_u32 s53,s53,s57
ta 98,236,25088
v_accvgpr_read_b32 v41,a137
zaaa 156,60,4
ta 100,237,24576
v_accvgpr_read_b32 v39,a135
zaaa 152,60,8
ta 102,237,25088
ta 104,238,28672
zaaa 168,56,8
v_accvgpr_read_b32 v38,a134
v_accvgpr_read_b32 v35,a131
zaaa 164,56,12
dl 230,44,54,2048
ta 106,238,29184
zaaa 148,60,12
ta 108,239,28672
v_accvgpr_read_b32 v34,a130
zaaa 144,60,16
ta 110,239,29184
v_accvgpr_read_b32 v33,a129
zaaa 160,56,16
ta 44,235,16384
ta 46,235,16896
ta 40,234,16384
ta 42,234,16896
ta 52,237,16384
ta 54,237,16896
ta 48,236,16384
ta 50,236,16896
ta 60,235,20480
ta 62,235,20992
ta 56,234,20480
ta 58,234,20992
v_accvgpr_read_b32 v32,a128
zaaa 128,64,16
v_accvgpr_read_b32 v37,a133
v_accvgpr_read_b32 v36,a132
zaaa 112,68,16
dl 231,44,54,3072
zaaa 116,68,12
s_add_u32 m0,s61,4096
v_accvgpr_read_b32 v40,a136
v_accvgpr_read_b32 v44,a140
zaaa 132,64,12
s_add_u32 s54,s54,s57
cv 251,42,43
cv 250,40,41
zaaa 136,64,8
s_add_u32 s61,s63,s98
cv 253,46,47
cv 252,44,45
zaaa 120,68,8
s_add_u32 s63,s63,s99
cv 249,38,39
cv 247,34,35
zaaa 124,68,4
buffer_store_dwordx4 v[48:51],v244,s[24:27],0 offen offset:2048 sc0 sc1
zaaa 140,64,4
cv 246,32,33
cv 248,36,37
zvaa 40,72,4
ta 68,237,20480
cv 124,24,25
zvaa 44,76,4
ta 70,237,20992
cv 123,22,23
zvaa 52,76,8
ta 64,236,20480
cv 121,18,19
zvaa 48,72,8
dl 224,36,52,0
zvaa 56,72,12
ta 66,236,20992
cv 120,16,17
zvaa 60,76,12
cv 122,20,21
cv 119,14,15
zvaa 68,76,16
cv 118,12,13
cv 117,10,11
zvaa 64,72,16
cv 116,8,9
cv 156,88,89
zvaa 32,80,16
dl 225,36,52,1024
ta 76,235,24576
ta 78,235,25088
zvaa 36,84,16
ta 72,234,24576
ta 74,234,25088
zvaa 28,84,12
cv 153,82,83
cv 152,80,81
zvaa 24,80,12
cv 155,86,87
cv 154,84,85
zvaa 16,80,8
cv 149,74,75
cv 148,72,73
zvaa 20,84,8
dl 226,36,52,2048
zvaa 12,84,4
v_add_u32_e32 v232,s99,v232
cv 115,6,7
zvaa 8,80,4
cv 114,4,5
cv 113,2,3
zvaa 76,4,88
cv 112,0,1
zvaa 96,4,92
zvaa 100,8,92
dl 227,36,52,3072
zvaa 80,8,88
s_add_u32 s52,s52,s60
zvaa 84,12,88
zvaa 104,12,92
zvaa 108,16,92
s_waitcnt vmcnt(23) lgkmcnt(0)
s_barrier
zvaa 88,16,88
L_kvx:
L_vs8:
s_cbranch_execz L_vs12
L_p_exit:
s_add_u32 m0,s62,16384
ra 4,232,0
v_add_u32_e32 v233,0xffff0000,v233
maaa 236,40,24
dl 228,40,53,0
maaa 220,44,24
s_lshl_b32 s86,s21,2
ra 8,232,2048
s_add_u32 s86,s86,s12
maaa 216,44,28
s_lshl_b32 s86,s86,8
ra 12,232,4096
s_add_u32 s86,s86,L_p_exit-L_att_pc+2048
maaa 232,40,28
s_mov_b32 s67,4096
ra 16,232,6144
s_and_b32 s87,s12,1
maaa 228,40,32
s_cmp_lt_u32 s12,2
s_cselect_b32 s94,s22,s23
ra 80,233,0
s_lshr_b32 s94,s94,6
maaa 212,44,32
dl 229,40,53,1024
maaa 208,44,36
s_add_u32 s94,s94,s87
ra 84,233,2048
s_lshl_b32 s87,s18,5
maaa 224,40,36
s_add_u32 s94,s94,s87
ra 88,233,4096
s_lshl_b32 s94,s94,11
maaa 192,48,36
s_add_u32 s94,s94,12582912
ra 92,233,6144
s_add_u32 s53,s53,s58
maaa 176,52,36
s_bfe_u32 s88,s21,65539
ta 0,234,32768
s_lshl_b32 s87,s20,1
maaa 180,52,32
dl 230,44,54,2048
maaa 196,48,32
s_add_u32 s88,s88,s87
ta 2,234,33280
s_lshl_b32 s88,s88,7
maaa 200,48,28
s_add_u32 s85,s88,83894272
ta 40,235,32768
v_bfe_u32 v241,v244,6,4
maaa 184,52,28
s_lshl_b32 s91,s12,4
ta 42,235,33280
v_and_b32_e32 v241,12,v241
maaa 188,52,24
s_cmp_lt_u32 s12,2
s_cselect_b32 s90,s22,s23
s_mul_i32 s33,s90,10240
v_accvgpr_read_b32 v255,a241
maaa 204,48,24
s_mov_b32 s64,0x55505555
s_mov_b32 s65,0x50005500
ta 52,236,32768
maaa 172,56,24
dl 231,44,54,3072
maaa 156,60,24
s_add_u32 m0,s98,65536
ta 54,236,33280
s_add_u32 s54,s54,s58
maaa 152,60,28
s_lshl_b32 s95,s12,12
s_mov_b32 s66,0x50000
s_add_u32 s70,s95,32768
maaa 168,56,28
s_add_u32 s72,s95,65536
s_lshl_b32 s4,s21,2
s_add_u32 s4,s4,s12
maaa 164,56,32
s_lshl_b32 s4,s4,8
s_bitcmp1_b32 170,s20
s_cselect_b32 s5,L_odd-L_last,0
maaa 148,60,32
s_add_u32 s4,s4,s5
s_mov_b64 s[30:31],s[28:29]
s_add_u32 s4,s4,L_last-L_att_pc
maaa 144,60,36
buffer_store_dwordx4 v[250:253],v244,s[24:27],0 offen offset:1024 sc0 sc1
maaa 160,56,36
s_mov_b64 s[28:29],s[78:79]
v_lshl_add_u32 v253,v244,3,s86
v_mov_b32_e32 v250,-1
maaa 128,64,36
s_add_u32 s68,s95,0
v_mov_b32_e32 v251,-1
v_bfe_u32 v252,v244,4,4
maaa 112,68,36
s_add_u32 s69,s95,16384
ta 56,237,32768
v_sub_u32_e32 v252,v252,v241
maaa 116,68,32
s_add_u32 s74,s95,98304
ta 58,237,33280
v_add_u32_e32 v252,s91,v252
maaa 132,64,32
s_cmp_lt_u32 s12,2
s_cselect_b32 s91,32768,0
s_add_u32 s91,s91,98304
v_mov_b32_e32 v241,0xf149f2ca
maaa 136,64,28
s_mov_b64 exec,3
global_load_dword a250,v253,s[96:97]
s_mov_b64 exec,-1
maaa 120,68,28
s_add_u32 s71,s95,49152
v_add_u32_e32 v253,10240,v253
v_cvt_f32_i32_e32 v252,v252
maaa 124,68,24
s_add_u32 s73,s95,81920
v_mul_f32_e32 v252,0x7149f2ca,v252
v_and_b32_e32 v245,48,v244
maaa 140,64,24
s_lshl_b32 s59,%1,2
v_lshlrev_b32_e32 v245,3,v245
s_lshl_b32 s84,%1,2
mvaa 40,72,24
s_waitcnt vmcnt(14) lgkmcnt(2)
s_barrier
mvaa 44,76,24
s_add_u32 s59,s59,s12
ta 20,234,49152
s_add_u32 s75,s95,114688
mvaa 52,76,28
dl 224,36,52,0
mvaa 48,72,28
s_add_u32 s84,s84,s12
ta 22,234,49664
s_mul_i32 s84,s84,12288
mvaa 56,72,32
s_sub_u32 s55,s14,s13
ta 44,235,49152
s_mov_b32 s48,0x38000000
mvaa 60,76,32
s_mov_b32 s81,1
ta 46,235,49664
)"
R"(s_mov_b32 s84,0
mvaa 68,76,36
ta 48,236,49152
mvaa 64,72,36
dl 225,36,52,1024
mvaa 32,96,36
ta 50,236,49664
mvaa 36,100,36
ta 60,237,49152
mvaa 28,100,32
ta 62,237,49664
mvaa 24,96,32
ta 68,234,36864
mvaa 16,96,28
dl 226,36,52,2048
mvaa 20,100,28
ta 70,234,37376
mvaa 12,100,24
ta 64,234,53248
mvaa 8,96,24
ta 66,234,53760
mvaa 76,24,104
ta 76,235,53248
mvaa 96,24,108
ta 78,235,53760
mvaa 100,28,108
buffer_store_dwordx4 v[246:249],v244,s[24:27],0 offen offset:0 sc0 sc1
mvaa 80,28,104
ta 72,235,36864
v_mov_b32_e32 v248,-1
mvaa 84,32,104
ta 74,235,37376
v_mov_b32_e32 v249,-1
mvaa 104,32,108
v_mov_b32_e32 v247,1
mvaa 108,36,108
mvaa 88,36,104
maaa 236,0,4
dl 227,36,52,3072
s_waitcnt lgkmcnt(5)
maaa 236,20,80
s_add_u32 s52,s52,s56
s_add_u32 m0,s98,69632
maaa 220,44,80
maaa 220,40,4
maaa 216,40,8
maaa 216,44,84
maaa 232,20,84
dl 224,36,52,0
maaa 232,0,8
maaa 228,0,12
maaa 228,20,88
maaa 212,44,88
maaa 212,40,12
maaa 208,40,16
dl 225,36,52,1024
maaa 208,44,92
maaa 224,20,92
ta 40,236,36864
maaa 224,0,16
ta 42,236,37376
maaa 192,52,16
ta 44,236,53248
maaa 192,48,92
dl 226,36,52,2048
maaa 176,60,92
ta 46,236,53760
maaa 176,56,16
ta 20,237,53248
maaa 180,56,12
ta 22,237,53760
maaa 180,60,88
ta 0,237,36864
maaa 196,48,88
ta 2,237,37376
maaa 196,52,12
dl 227,36,52,3072
maaa 200,52,8
s_add_u32 s52,s52,s60
s_add_u32 m0,s98,0
s_waitcnt vmcnt(15)
ra 24,232,32768
maaa 200,48,84
ra 28,232,34816
maaa 184,60,84
ra 32,232,36864
maaa 184,56,8
ra 36,232,38912
v_add_u32_e32 v232,0x10000,v232
maaa 188,56,4
ra 96,233,32768
maaa 188,60,80
dl 224,36,52,0
maaa 204,48,80
ta 56,234,40960
maaa 204,52,4
ta 58,234,41472
maaa 172,68,4
ta 60,234,57344
s_waitcnt lgkmcnt(8)
maaa 172,64,80
ta 62,234,57856
maaa 156,76,80
ta 48,235,57344
maaa 156,72,4
buffer_store_dwordx4 v[112:115],v244,s[24:27],s67 offen offset:0 sc0 sc1
maaa 152,72,8
ta 50,235,57856
v_cndmask_b32_e64 v112,v241,0,s[64:65]
maaa 152,76,84
s_mov_b32 s64,0xaaa0aaaa
ta 52,235,40960
s_mov_b32 s65,0xa000aa00
maaa 168,64,84
ta 54,235,41472
v_cndmask_b32_e64 v113,v241,0,s[64:65]
maaa 168,68,8
s_mov_b32 s64,0x55405554
ra 100,233,34816
s_mov_b32 s65,0x40005400
maaa 164,68,12
ra 104,233,36864
v_cndmask_b32_e64 v114,v241,0,s[64:65]
maaa 164,64,88
dl 225,36,52,1024
maaa 148,76,88
s_mov_b32 s64,0xaa80aaa8
ra 108,233,38912
v_add_u32_e32 v233,0x10000,v233
maaa 148,72,12
s_mov_b32 s65,0x8000a800
maaa 144,72,16
v_cndmask_b32_e64 v115,v241,0,s[64:65]
maaa 144,76,92
L_vs9:
s_cbranch_execz L_vr0
maaa 160,64,92
dl 226,36,52,2048
maaa 160,68,16
ta 72,236,40960
maaa 128,40,16
ta 74,236,41472
maaa 128,44,92
ta 76,236,57344
maaa 112,20,92
ta 78,236,57856
maaa 112,0,16
dl 227,36,52,3072
maaa 116,0,12
s_add_u32 s52,s52,s56
s_add_u32 m0,s98,4096
ta 64,237,57344
maaa 116,20,88
ta 66,237,57856
maaa 132,44,88
ta 68,237,40960
maaa 132,40,12
ta 70,237,41472
maaa 136,40,8
maaa 136,44,84
dl 224,36,52,0
maaa 120,20,84
maaa 120,0,8
maaa 124,0,4
maaa 124,20,80
maaa 140,44,80
ta 0,238,45056
maaa 140,40,4
buffer_store_dwordx4 v[248:251],v244,s[8:11],s94 offen offset:0 sc0 sc1
s_waitcnt lgkmcnt(3)
mvaa 40,56,4
ta 2,238,45568
mvaa 40,60,80
ta 20,238,61440
mvaa 44,48,80
ta 22,238,61952
mvaa 44,52,4
ta 44,239,61440
mvaa 52,52,8
ta 46,239,61952
mvaa 52,48,84
dl 225,36,52,1024
mvaa 48,60,84
ta 40,239,45056
mvaa 48,56,8
ta 42,239,45568
mvaa 56,56,12
mvaa 56,60,88
mvaa 60,48,88
mvaa 60,52,12
dl 226,36,52,2048
mvaa 68,52,16
mvaa 68,48,92
mvaa 64,60,92
mvaa 64,56,16
mvaa 32,72,16
dl 227,36,52,3072
mvaa 32,76,92
s_waitcnt vmcnt(10) lgkmcnt(0)
s_barrier
mvaa 36,64,92
s_add_u32 m0,s62,32768
ta 52,234,0
s_add_u32 s52,s52,s60
mvaa 36,68,16
ta 54,234,512
mvaa 28,68,12
ta 48,234,16384
mvaa 28,64,88
dl 228,40,53,0
mvaa 24,76,88
ta 50,234,16896
mvaa 24,72,12
ta 60,235,16384
mvaa 16,72,8
ta 62,235,16896
mvaa 16,76,84
ta 56,235,0
mvaa 20,64,84
ta 58,235,512
mvaa 20,68,8
dl 229,40,53,1024
mvaa 12,68,4
s_add_u32 s53,s53,s57
mvaa 12,64,80
mvaa 8,76,80
ta 68,236,0
mvaa 8,72,4
ta 70,236,512
mvaa 76,4,0
ta 64,236,16384
mvaa 76,80,20
dl 230,44,54,2048
mvaa 96,80,44
ta 66,236,16896
mvaa 96,4,40
ta 76,237,16384
mvaa 100,8,40
ta 78,237,16896
mvaa 100,84,44
ta 72,237,0
mvaa 80,84,20
ta 74,237,512
mvaa 80,8,0
dl 231,44,54,3072
mvaa 84,12,0
s_add_u32 m0,s62,49152
s_add_u32 s54,s54,s57
ra 80,233,0
mvaa 84,88,20
ra 4,232,0
mvaa 104,88,44
ra 84,233,2048
mvaa 104,12,40
ra 8,232,2048
mvaa 108,16,40
ra 88,233,4096
mvaa 108,92,44
dl 228,40,53,0
mvaa 88,92,20
ta 40,234,4096
mvaa 88,16,0
ta 42,234,4608
s_waitcnt lgkmcnt(8)
maaa 236,52,24
ta 44,234,20480
maaa 236,48,96
ta 46,234,20992
maaa 220,60,96
dl 229,40,53,1024
maaa 220,56,24
s_add_u32 s53,s53,s58
ta 20,235,20480
maaa 216,56,28
ta 22,235,20992
maaa 216,60,100
ta 0,235,4096
maaa 232,48,100
ta 2,235,4608
maaa 232,52,28
dl 230,44,54,2048
maaa 228,52,32
ra 12,232,4096
maaa 228,48,104
ra 92,233,6144
v_add_u32_e32 v233,0xffff0000,v233
maaa 212,60,104
ra 16,232,6144
v_add_u32_e32 v232,0xffff0000,v232
maaa 212,56,32
maaa 208,56,36
maaa 208,60,108
dl 231,44,54,3072
maaa 224,48,108
s_add_u32 m0,s98,32768
ta 56,236,4096
s_add_u32 s54,s54,s58
maaa 224,52,36
ta 58,236,4608
maaa 192,68,36
ta 60,236,20480
maaa 192,64,108
ta 62,236,20992
maaa 176,76,108
ta 48,237,20480
s_waitcnt lgkmcnt(7)
maaa 176,72,36
dl 224,36,52,0
maaa 180,72,32
ta 50,237,20992
maaa 180,76,104
ta 52,237,4096
maaa 196,64,104
ta 54,237,4608
maaa 196,68,32
maaa 200,68,28
maaa 200,64,100
dl 225,36,52,1024
maaa 184,76,100
maaa 184,72,28
maaa 188,72,24
maaa 188,76,96
maaa 204,64,96
ta 72,234,8192
maaa 204,68,24
dl 226,36,52,2048
maaa 172,40,24
ta 74,234,8704
maaa 172,44,96
ta 76,234,24576
maaa 156,20,96
ta 78,234,25088
maaa 156,0,24
ta 64,235,24576
maaa 152,0,28
buffer_store_dwordx4 v[248:251],v244,s[8:11],s94 offen offset:1024 sc0 sc1
maaa 152,20,100
ta 66,235,25088
v_and_b32_e32 v250,240,v244
maaa 168,44,100
ta 68,235,8192
v_accvgpr_read_b32 v249,a240
maaa 168,40,28
ta 70,235,8704
maaa 164,40,32
maaa 164,44,104
dl 227,36,52,3072
maaa 148,20,104
s_add_u32 s52,s52,s56
s_add_u32 m0,s98,36864
maaa 148,0,32
maaa 144,0,36
maaa 144,20,108
maaa 160,44,108
ta 0,236,8192
maaa 160,40,36
dl 224,36,52,0
s_waitcnt lgkmcnt(3)
maaa 128,56,36
ta 2,236,8704
maaa 128,60,108
ta 20,236,24576
maaa 112,48,108
ta 22,236,25088
maaa 112,52,36
ta 44,237,24576
maaa 116,52,32
ta 46,237,25088
maaa 116,48,104
dl 225,36,52,1024
maaa 132,60,104
ta 40,237,8192
maaa 132,56,32
ta 42,237,8704
maaa 136,56,28
maaa 136,60,100
maaa 120,48,100
maaa 120,52,28
buffer_load_dword v246,off,s[8:11],s35 offset:1024 sc1
maaa 124,52,24
maaa 124,48,96
maaa 140,60,96
ta 52,238,12288
maaa 140,56,24
ta 54,238,12800
mvaa 40,72,24
ta 48,238,28672
mvaa 40,76,96
buffer_store_dwordx4 v[116:119],v244,s[24:27],s67 offen offset:1024 sc0 sc1
ta 50,238,29184
ta 60,239,28672
mvaa 44,64,96
s_waitcnt lgkmcnt(5)
mvaa 44,68,24
ta 62,239,29184
mvaa 52,68,28
ta 56,239,12288
ta 58,239,12800
mvaa 52,64,100
mvaa 48,76,100
dl 226,36,52,2048
mvaa 48,72,28
mvaa 56,72,32
mvaa 56,76,104
mvaa 60,64,104
mvaa 60,68,32
dl 227,36,52,3072
mvaa 68,68,36
s_add_u32 s52,s52,s60
mvaa 68,64,108
mvaa 64,76,108
mvaa 64,72,36
mvaa 32,0,36
s_waitcnt vmcnt(11) lgkmcnt(0)
s_barrier
mvaa 32,20,108
s_add_u32 m0,s62,0
ta 68,234,32768
mvaa 36,44,108
dl 228,40,53,0
mvaa 36,40,36
ta 70,234,33280
mvaa 28,40,32
ta 64,234,49152
mvaa 28,44,104
ta 66,234,49664
mvaa 24,20,104
ta 76,235,49152
mvaa 24,0,32
ta 78,235,49664
mvaa 16,0,28
dl 229,40,53,1024
mvaa 16,20,100
s_add_u32 s53,s53,s57
ta 72,235,32768
mvaa 20,44,100
ta 74,235,33280
mvaa 20,40,28
mvaa 12,40,24
mvaa 12,44,96
mvaa 8,20,96
dl 230,44,54,2048
mvaa 8,0,24
ta 40,236,32768
mvaa 76,24,52
ta 42,236,33280
mvaa 76,96,48
ta 44,236,49152
mvaa 96,96,60
ta 46,236,49664
mvaa 96,24,56
dl 231,44,54,3072
mvaa 100,28,56
s_add_u32 m0,s62,16384
ta 20,237,49152
s_add_u32 s54,s54,s57
mvaa 100,100,60
ta 22,237,49664
mvaa 80,100,48
ta 0,237,32768
mvaa 80,28,52
ta 2,237,33280
mvaa 84,32,52
dl 228,40,53,0
mvaa 84,104,48
ra 96,233,0
mvaa 104,104,60
ra 24,232,0
mvaa 104,32,56
ra 28,232,2048
mvaa 108,36,56
ra 100,233,2048
mvaa 108,108,60
dl 229,40,53,1024
mvaa 88,108,48
s_add_u32 s53,s53,s58
ta 56,234,36864
mvaa 88,36,52
ta 58,234,37376
s_waitcnt lgkmcnt(8)
maaa 236,68,4
ta 60,234,53248
maaa 236,64,80
ta 62,234,53760
maaa 220,76,80
ta 48,235,53248
maaa 220,72,4
dl 230,44,54,2048
maaa 216,72,8
ta 50,235,53760
maaa 216,76,84
ta 52,235,36864
maaa 232,64,84
ta 54,235,37376
maaa 232,68,8
ra 104,233,4096
maaa 228,68,12
ra 32,232,4096
maaa 228,64,88
dl 231,44,54,3072
maaa 212,76,88
s_add_u32 m0,s98,65536
ra 108,233,6144
s_add_u32 s54,s54,s58
maaa 212,72,12
ra 36,232,6144
maaa 208,72,16
s_waitcnt vmcnt(11)
v_readfirstlane_b32 s87,v246
maaa 208,76,92
s_cmp_eq_u32 s12,0
s_cselect_b32 s89,s32,s87
ta 72,236,36864
maaa 224,64,92
s_cmp_lg_u32 s87,s89
s_cbranch_scc1 L_q_ack
L_q_acked:
ta 74,236,37376
s_and_b32 s87,s12,1
maaa 224,68,16
dl 224,36,52,0
maaa 192,40,16
s_lshl_b32 s88,s87,6
ta 76,236,53248
s_add_u32 s90,s90,s88
maaa 192,44,92
s_lshl_b32 s88,s87,10
ta 78,236,53760
s_lshl_b32 s87,s87,14
maaa 176,20,92
s_add_u32 s91,s91,s87
ta 64,237,53248
s_lshr_b32 s87,s18,1
s_waitcnt lgkmcnt(7)
maaa 176,0,16
s_lshl_b32 s87,s87,4
ta 66,237,53760
v_cmp_eq_u32_e64 s[100:101],s87,v250
maaa 180,0,12
s_add_u32 s33,s33,s88
ta 68,237,36864
v_lshlrev_b32_e32 v250,3,v250
maaa 180,20,88
dl 225,36,52,1024
maaa 196,44,88
s_lshl_b32 s88,s18,11
ta 70,237,37376
v_add_u32_e32 v250,83894272,v250
maaa 196,40,12
s_add_u32 s88,s33,s88
v_add_u32_e32 v251,s91,v244
v_cndmask_b32_e64 v242,31,-1,s[100:101]
maaa 200,40,8
s_add_u32 s33,s88,63176704
v_and_b32_e32 v243,528,v242
v_and_b32_e32 v242,1023,v242
maaa 200,44,84
s_add_u32 s34,s88,63504384
s_add_u32 s50,s88,63832064
s_add_u32 s51,s88,64159744
maaa 184,20,84
dl 226,36,52,2048
maaa 184,0,8
s_lshr_b32 s91,s21,3
s_and_b32 s88,s20,4
s_and_b32 s89,s20,3
maaa 188,0,4
s_add_u32 s91,s91,s88
s_lshl_b32 s88,s89,6
s_lshl_b32 s86,s21,2
maaa 188,20,80
s_add_u32 s86,s86,s12
s_cmp_eq_u32 s90,0
s_cselect_b64 s[64:65],-1,0
maaa 204,44,80
s_mov_b32 s32,s85
ta 0,234,40960
s_and_b32 s89,s86,1
maaa 204,40,4
s_lshl_b32 s89,s89,9
ta 2,234,41472
s_lshr_b32 s49,s86,3
maaa 172,56,4
buffer_store_dwordx4 v[120:123],v244,s[24:27],s67 offen offset:2048 sc0 sc1
maaa 172,60,80
s_lshl_b32 s49,s49,11
ta 20,234,57344
v_lshrrev_b32_e32 v246,6,v244
maaa 156,48,80
ta 22,234,57856
v_lshl_add_u32 v245,v246,22,v245
maaa 156,52,4
ta 44,235,57344
v_lshlrev_b32_e32 v246,16,v244
maaa 152,52,8
ta 46,235,57856
maaa 152,48,84
ta 40,235,40960
maaa 168,60,84
dl 227,36,52,3072
maaa 168,56,8
s_add_u32 s52,s52,s56
s_add_u32 m0,s98,69632
ta 42,235,41472
maaa 164,56,12
maaa 164,60,88
maaa 148,48,88
maaa 148,52,12
maaa 144,52,16
dl 224,36,52,0
maaa 144,48,92
maaa 160,60,92
ta 52,236,40960
maaa 160,56,16
ta 54,236,41472
s_waitcnt lgkmcnt(3)
maaa 128,72,16
ta 48,236,57344
maaa 128,76,92
ta 50,236,57856
maaa 112,64,92
buffer_store_dwordx4 v[124:127],v244,s[24:27],s67 offen offset:3072 sc0 sc1
maaa 112,68,16
s_mov_b32 s67,8192
ta 60,237,57344
maaa 116,68,12
ta 62,237,57856
maaa 116,64,88
ta 56,237,40960
maaa 132,76,88
ta 58,237,41472
maaa 132,72,12
dl 225,36,52,1024
maaa 136,72,8
maaa 136,76,84
maaa 120,64,84
maaa 120,68,8
maaa 124,68,4
dl 226,36,52,2048
maaa 124,64,80
maaa 140,76,80
ta 68,238,45056
ta 70,238,45568
maaa 140,72,4
ta 64,238,61440
ta 66,238,61952
mvaa 40,0,4
ta 76,239,61440
mvaa 40,20,80
ta 78,239,61952
mvaa 44,44,80
buffer_store_dwordx4 v[128:131],v244,s[24:27],s67 offen offset:0 sc0 sc1
ta 72,239,45056
ta 74,239,45568
s_waitcnt lgkmcnt(8)
mvaa 44,40,4
v_add_f32_e32 v128,0x70c9f2ca,v252
v_add_f32_e32 v129,0xf0c9f2ca,v252
mvaa 52,40,8
v_add_f32_e32 v130,0xf1977617,v252
v_add_f32_e32 v131,0xf1fc6f7c,v252
mvaa 52,44,84
v_min_f32_e32 v128,0xc1a00000,v128
v_min_f32_e32 v129,0xc1a00000,v129
mvaa 48,20,84
v_min_f32_e32 v130,0xc1a00000,v130
v_min_f32_e32 v131,0xc1a00000,v131
mvaa 48,0,8
mvaa 56,0,12
dl 227,36,52,3072
mvaa 56,20,88
s_add_u32 s52,s52,s60
mvaa 60,44,88
mvaa 60,40,12
mvaa 68,40,16
mvaa 68,44,92
mvaa 64,20,92
mvaa 64,0,16
mvaa 32,52,16
L_vs10:
s_cbranch_execz L_vr0
s_waitcnt vmcnt(11) lgkmcnt(0)
s_barrier
mvaa 32,48,92
s_cmp_eq_u32 s12,0
s_cselect_b64 exec,1,0
buffer_atomic_add v247,off,s[8:11],s85 sc1
s_mov_b64 exec,-1
mvaa 36,60,92
s_add_u32 m0,s62,32768
ta 40,234,0
v_mov_b32_e32 v247,32
mvaa 36,56,16
ta 42,234,512
mvaa 28,56,12
ta 44,234,16384
mvaa 28,60,88
ta 46,234,16896
mvaa 24,48,88
ta 20,235,16384
mvaa 24,52,12
dl 228,40,53,0
mvaa 16,52,8
ta 22,235,16896
mvaa 16,48,84
ta 0,235,0
mvaa 20,60,84
ta 2,235,512
mvaa 20,56,8
mvaa 12,56,4
mvaa 12,60,80
dl 229,40,53,1024
mvaa 8,48,80
s_add_u32 s53,s53,s57
ta 56,236,0
mvaa 8,52,4
ta 58,236,512
mvaa 76,4,68
ta 60,236,16384
mvaa 76,80,64
ta 62,236,16896
mvaa 96,80,76
ta 48,237,16384
mvaa 96,4,72
dl 230,44,54,2048
mvaa 100,8,72
ta 50,237,16896
mvaa 100,84,76
ta 52,237,0
mvaa 80,84,64
ta 54,237,512
mvaa 80,8,68
ra 80,233,32768
mvaa 84,12,68
ra 4,232,32768
mvaa 84,88,64
dl 231,44,54,3072
mvaa 104,88,76
s_add_u32 m0,s62,49152
s_add_u32 s54,s54,s57
ra 84,233,34816
mvaa 104,12,72
ra 8,232,34816
mvaa 108,16,72
ra 88,233,36864
mvaa 108,92,76
ra 12,232,36864
mvaa 88,92,64
dl 228,40,53,0
mvaa 88,16,68
ta 72,234,4096
s_waitcnt lgkmcnt(8)
maaa 236,40,24
ta 74,234,4608
maaa 236,44,96
ta 76,234,20480
maaa 220,20,96
ta 78,234,20992
maaa 220,0,24
dl 229,40,53,1024
maaa 216,0,28
s_add_u32 s53,s53,s58
ta 64,235,20480
maaa 216,20,100
ta 66,235,20992
maaa 232,44,100
ta 68,235,4096
maaa 232,40,28
ta 70,235,4608
maaa 228,40,32
ra 16,232,38912
v_add_u32_e32 v232,0x10000,v232
maaa 228,44,104
dl 230,44,54,2048
maaa 212,20,104
ra 92,233,38912
v_add_u32_e32 v233,0x10000,v233
maaa 212,0,32
maaa 208,0,36
maaa 208,20,108
maaa 224,44,108
ta 0,236,4096
maaa 224,40,36
dl 231,44,54,3072
maaa 192,56,36
s_add_u32 m0,s98,0
ta 2,236,4608
s_add_u32 s54,s54,s58
maaa 192,60,108
ta 20,236,20480
maaa 176,48,108
ta 22,236,20992
s_waitcnt lgkmcnt(5)
maaa 176,52,36
ta 44,237,20480
maaa 180,52,32
ta 46,237,20992
maaa 180,48,104
dl 224,36,52,0
maaa 196,60,104
ta 40,237,4096
maaa 196,56,32
ta 42,237,4608
maaa 200,56,28
maaa 200,60,100
maaa 184,48,100
maaa 184,52,28
dl 225,36,52,1024
maaa 188,52,24
maaa 188,48,96
maaa 204,60,96
ta 52,234,8192
maaa 204,56,24
ta 54,234,8704
maaa 172,72,24
buffer_store_dwordx4 v[132:135],v244,s[24:27],s67 offen offset:1024 sc0 sc1
maaa 172,76,96
ta 48,234,24576
v_add_f32_e32 v132,0xf343a333,v252
maaa 156,64,96
ta 50,234,25088
v_add_f32_e32 v133,0xf3504260,v252
maaa 156,68,24
ta 60,235,24576
v_add_f32_e32 v134,0xf35ce18d,v252
maaa 152,68,28
ta 62,235,25088
v_min_f32_e32 v132,0xc1a00000,v132
maaa 152,64,100
dl 226,36,52,2048
maaa 168,76,100
ta 56,235,8192
v_add_f32_e32 v135,0xf36980b9,v252
maaa 168,72,28
ta 58,235,8704
v_min_f32_e32 v133,0xc1a00000,v133
maaa 164,72,32
v_min_f32_e32 v134,0xc1a00000,v134
v_min_f32_e32 v135,0xc1a00000,v135
maaa 164,76,104
maaa 148,64,104
maaa 148,68,32
dl 227,36,52,3072
maaa 144,68,36
s_add_u32 s52,s52,s56
s_add_u32 m0,s98,4096
maaa 144,64,108
maaa 160,76,108
ta 68,236,8192
maaa 160,72,36
ta 70,236,8704
s_waitcnt lgkmcnt(4)
maaa 128,0,36
ta 64,236,24576
maaa 128,20,108
dl 224,36,52,0
maaa 112,44,108
ta 66,236,25088
maaa 112,40,36
ta 76,237,24576
maaa 116,40,32
ta 78,237,25088
maaa 116,44,104
ta 72,237,8192
maaa 132,20,104
ta 74,237,8704
maaa 132,0,32
dl 225,36,52,1024
maaa 136,0,28
maaa 136,20,100
maaa 120,44,100
maaa 120,40,28
maaa 124,40,24
maaa 124,44,96
buffer_store_dwordx4 v[136:139],v244,s[24:27],s67 offen offset:2048 sc0 sc1
maaa 140,20,96
ta 40,238,12288
ta 42,238,12800
maaa 140,0,24
ta 44,238,28672
ta 46,238,29184
mvaa 40,52,24
ta 20,239,28672
ta 22,239,29184
mvaa 40,48,96
ta 0,239,12288
ta 2,239,12800
mvaa 44,60,96
dl 226,36,52,2048
s_waitcnt lgkmcnt(8)
mvaa 44,56,24
v_add_f32_e32 v136,0xf3c6caff,v252
v_add_f32_e32 v137,0xf3cd1a95,v252
mvaa 52,56,28
v_add_f32_e32 v138,0xf3d36a2b,v252
v_add_f32_e32 v139,0xf3d9b9c2,v252
mvaa 52,60,100
v_min_f32_e32 v136,0xc1a00000,v136
v_min_f32_e32 v137,0xc1a00000,v137
mvaa 48,48,100
v_min_f32_e32 v138,0xc1a00000,v138
v_min_f32_e32 v139,0xc1a00000,v139
mvaa 48,52,28
dl 227,36,52,3072
mvaa 56,52,32
s_add_u32 s52,s52,s60
mvaa 56,48,104
mvaa 60,60,104
mvaa 60,56,32
mvaa 68,56,36
mvaa 68,60,108
mvaa 64,48,108
mvaa 64,52,36
mvaa 32,68,36
s_waitcnt vmcnt(10) lgkmcnt(0)
s_barrier
s_add_u32 m0,s62,0
mvaa 32,64,108
dl 228,40,53,0
mvaa 36,76,108
ta 56,234,32768
mvaa 36,72,36
ta 58,234,33280
mvaa 28,72,32
ta 60,234,49152
mvaa 28,76,104
ta 62,234,49664
mvaa 24,64,104
ta 48,235,49152
mvaa 24,68,32
dl 229,40,53,1024
mvaa 16,68,28
s_add_u32 s53,s53,s57
ta 50,235,49664
mvaa 16,64,100
ta 52,235,32768
mvaa 20,76,100
ta 54,235,33280
mvaa 20,72,28
mvaa 12,72,24
dl 230,44,54,2048
mvaa 12,76,96
mvaa 8,64,96
ta 72,236,32768
mvaa 8,68,24
ta 74,236,33280
mvaa 76,24,40
ta 76,236,49152
mvaa 76,96,44
ta 78,236,49664
mvaa 96,96,20
dl 231,44,54,3072
mvaa 96,24,0
s_add_u32 m0,s62,16384
s_add_u32 s54,s54,s57
ta 64,237,49152
mvaa 100,28,0
ta 66,237,49664
mvaa 100,100,20
ta 68,237,32768
mvaa 80,100,44
ta 70,237,33280
mvaa 80,28,40
ra 96,233,0
mvaa 84,32,40
dl 228,40,53,0
mvaa 84,104,44
ra 24,232,0
mvaa 104,104,20
ra 100,233,2048
mvaa 104,32,0
ra 28,232,2048
mvaa 108,36,0
ra 104,233,4096
mvaa 108,108,20
ra 32,232,4096
mvaa 88,108,44
dl 229,40,53,1024
mvaa 88,36,40
s_add_u32 s53,s53,s58
ta 0,234,36864
s_waitcnt lgkmcnt(8)
maaa 236,56,4
ta 2,234,37376
maaa 236,60,80
ta 20,234,53248
maaa 220,48,80
ta 22,234,53760
maaa 220,52,4
ta 44,235,53248
maaa 216,52,8
dl 230,44,54,2048
maaa 216,48,84
ta 46,235,53760
maaa 232,60,84
ta 40,235,36864
maaa 232,56,8
ta 42,235,37376
maaa 228,56,12
ra 36,232,6144
)"
R"(v_add_u32_e32 v232,0xffff0000,v232
maaa 228,60,88
dl 231,44,54,3072
maaa 212,48,88
s_add_u32 m0,s98,32768
ra 108,233,6144
s_add_u32 s54,s54,s58
maaa 212,52,12
v_add_u32_e32 v233,0xffff0000,v233
maaa 208,52,16
maaa 208,48,92
maaa 224,60,92
dl 224,36,52,0
maaa 224,56,16
ta 52,236,36864
maaa 192,72,16
ta 54,236,37376
maaa 192,76,92
ta 48,236,53248
maaa 176,64,92
ta 50,236,53760
s_waitcnt lgkmcnt(5)
maaa 176,68,16
ta 60,237,53248
maaa 180,68,12
dl 225,36,52,1024
maaa 180,64,88
ta 62,237,53760
maaa 196,76,88
ta 56,237,36864
maaa 196,72,12
ta 58,237,37376
maaa 200,72,8
maaa 200,76,84
maaa 184,64,84
dl 226,36,52,2048
maaa 184,68,8
maaa 188,68,4
maaa 188,64,80
maaa 204,76,80
ta 68,234,40960
maaa 204,72,4
ta 70,234,41472
maaa 172,0,4
buffer_store_dwordx4 v[140:143],v244,s[24:27],s67 offen offset:3072 sc0 sc1
maaa 172,20,80
s_mov_b32 s67,12288
ta 64,234,57344
v_add_f32_e32 v140,0xf415e232,v252
maaa 156,44,80
ta 66,234,57856
v_add_f32_e32 v141,0xf41909fd,v252
maaa 156,40,4
ta 76,235,57344
v_add_f32_e32 v142,0xf41c31c8,v252
maaa 152,40,8
ta 78,235,57856
v_min_f32_e32 v140,0xc1a00000,v140
maaa 152,44,84
ta 72,235,40960
v_add_f32_e32 v143,0xf41f5993,v252
maaa 168,20,84
dl 227,36,52,3072
maaa 168,0,8
s_add_u32 s52,s52,s56
s_add_u32 m0,s98,36864
ta 74,235,41472
maaa 164,0,12
v_bfe_i32 v252,v244,4,1
v_min_f32_e32 v141,0xc1a00000,v141
maaa 164,20,88
v_min_f32_e32 v142,0xc1a00000,v142
v_min_f32_e32 v143,0xc1a00000,v143
maaa 148,44,88
v_xor_b32_e32 v252,65535,v252
maaa 148,40,12
dl 224,36,52,0
maaa 144,40,16
maaa 144,44,92
maaa 160,20,92
ta 40,236,40960
maaa 160,0,16
ta 42,236,41472
s_waitcnt lgkmcnt(3)
maaa 128,52,16
buffer_store_dwordx4 v[144:147],v244,s[24:27],s67 offen offset:0 sc0 sc1
maaa 128,48,92
ta 44,236,57344
maaa 112,60,92
ta 46,236,57856
maaa 112,56,16
ta 20,237,57344
maaa 116,56,12
ta 22,237,57856
maaa 116,60,88
ta 0,237,40960
maaa 132,48,88
dl 225,36,52,1024
maaa 132,52,12
ta 2,237,41472
maaa 136,52,8
maaa 136,48,84
maaa 120,60,84
maaa 120,56,8
maaa 124,56,4
dl 226,36,52,2048
maaa 124,60,80
maaa 140,48,80
ta 56,238,45056
ta 58,238,45568
maaa 140,52,4
ta 60,238,61440
ta 62,238,61952
mvaa 40,68,4
ta 48,239,61440
ta 50,239,61952
mvaa 40,64,80
mvaa 44,76,80
buffer_store_dwordx4 v[148:151],v244,s[24:27],s67 offen offset:1024 sc0 sc1
ta 52,239,45056
ta 54,239,45568
s_waitcnt lgkmcnt(8)
mvaa 44,72,4
mvaa 52,72,8
mvaa 52,76,84
mvaa 48,64,84
mvaa 48,68,8
mvaa 56,68,12
dl 227,36,52,3072
mvaa 56,64,88
s_add_u32 s52,s52,s60
mvaa 60,76,88
mvaa 60,72,12
mvaa 68,72,16
mvaa 68,76,92
mvaa 64,64,92
mvaa 64,68,16
mvaa 32,40,16
s_waitcnt vmcnt(11) lgkmcnt(0)
s_barrier
s_add_u32 m0,s62,32768
mvaa 32,44,92
dl 228,40,53,0
mvaa 36,20,92
ta 72,234,0
mvaa 36,0,16
ta 74,234,512
mvaa 28,0,12
ta 76,234,16384
mvaa 28,20,88
ta 78,234,16896
mvaa 24,44,88
ta 64,235,16384
mvaa 24,40,12
dl 229,40,53,1024
mvaa 16,40,8
s_add_u32 s53,s53,s57
ta 66,235,16896
mvaa 16,44,84
ta 68,235,0
mvaa 20,20,84
ta 70,235,512
mvaa 20,0,8
mvaa 12,0,4
mvaa 12,20,80
dl 230,44,54,2048
mvaa 8,44,80
ta 0,236,0
mvaa 8,40,4
ta 2,236,512
mvaa 76,4,56
ta 20,236,16384
mvaa 76,80,60
ta 22,236,16896
mvaa 96,80,48
dl 231,44,54,3072
mvaa 96,4,52
s_add_u32 m0,s62,49152
ta 44,237,16384
s_add_u32 s54,s54,s57
mvaa 100,8,52
ta 46,237,16896
mvaa 100,84,48
ta 40,237,0
mvaa 80,84,60
ta 42,237,512
mvaa 80,8,56
dl 228,40,53,0
mvaa 84,12,56
ra 80,233,0
mvaa 84,88,60
ra 4,232,0
mvaa 104,88,48
ra 84,233,2048
mvaa 104,12,52
ra 8,232,2048
mvaa 108,16,52
ra 88,233,4096
mvaa 108,92,48
dl 229,40,53,1024
mvaa 88,92,60
s_add_u32 s53,s53,s58
ta 52,234,4096
mvaa 88,16,56
ta 54,234,4608
s_waitcnt lgkmcnt(8)
maaa 236,72,24
ta 48,234,20480
maaa 236,76,96
ta 50,234,20992
maaa 220,64,96
L_vs11:
s_cbranch_execz L_vr0
ta 60,235,20480
maaa 220,68,24
dl 230,44,54,2048
maaa 216,68,28
ta 62,235,20992
maaa 216,64,100
ta 56,235,4096
maaa 232,76,100
ta 58,235,4608
maaa 232,72,28
ra 12,232,4096
maaa 228,72,32
ra 16,232,6144
maaa 228,76,104
dl 231,44,54,3072
maaa 212,64,104
s_add_u32 m0,s98,65536
ra 92,233,6144
s_add_u32 s54,s54,s58
maaa 212,68,32
maaa 208,68,36
maaa 208,64,108
maaa 224,76,108
ta 68,236,4096
maaa 224,72,36
dl 224,36,52,0
maaa 192,0,36
ta 70,236,4608
maaa 192,20,108
ta 64,236,20480
maaa 176,44,108
ta 66,236,20992
s_waitcnt lgkmcnt(5)
maaa 176,40,36
ta 76,237,20480
maaa 180,40,32
s_mov_b64 exec,3
global_load_dword a250,v253,s[96:97]
s_mov_b64 exec,-1
maaa 180,44,104
ta 78,237,20992
maaa 196,20,104
ta 72,237,4096
maaa 196,0,32
ta 74,237,4608
maaa 200,0,28
maaa 200,20,100
dl 225,36,52,1024
maaa 184,44,100
maaa 184,40,28
maaa 188,40,24
maaa 188,44,96
maaa 204,20,96
ta 40,234,8192
maaa 204,0,24
dl 226,36,52,2048
maaa 172,52,24
ta 42,234,8704
maaa 172,48,96
ta 44,234,24576
maaa 156,60,96
ta 46,234,25088
maaa 156,56,24
ta 20,235,24576
maaa 152,56,28
ta 22,235,25088
maaa 152,60,100
buffer_store_dwordx4 v[152:155],v244,s[24:27],s67 offen offset:2048 sc0 sc1
maaa 168,48,100
ta 0,235,8192
maaa 168,52,28
ta 2,235,8704
maaa 164,52,32
maaa 164,48,104
maaa 148,60,104
maaa 148,56,32
dl 227,36,52,3072
maaa 144,56,36
s_add_u32 s52,s52,s56
s_add_u32 m0,s98,69632
maaa 144,60,108
maaa 160,48,108
ta 56,236,8192
maaa 160,52,36
ta 58,236,8704
s_waitcnt lgkmcnt(4)
maaa 128,68,36
ta 60,236,24576
maaa 128,64,108
dl 224,36,52,0
maaa 112,76,108
ta 62,236,25088
maaa 112,72,36
ta 48,237,24576
maaa 116,72,32
ta 50,237,25088
maaa 116,76,104
ta 52,237,8192
maaa 132,64,104
dl 225,36,52,1024
maaa 132,68,32
ta 54,237,8704
maaa 136,68,28
maaa 136,64,100
maaa 120,76,100
maaa 120,72,28
dl 226,36,52,2048
maaa 124,72,24
maaa 124,76,96
maaa 140,64,96
ta 72,238,12288
ta 74,238,12800
maaa 140,68,24
ta 76,238,28672
ta 78,238,29184
mvaa 40,40,24
ta 64,239,28672
mvaa 40,44,96
buffer_store_dwordx4 v[156:159],v244,s[24:27],s67 offen offset:3072 sc0 sc1
ta 66,239,29184
mvaa 44,20,96
s_mov_b32 s67,262144
ta 68,239,12288
ta 70,239,12800
s_waitcnt lgkmcnt(8)
mvaa 44,0,24
mvaa 52,0,28
mvaa 52,20,100
mvaa 48,44,100
mvaa 48,40,28
dl 227,36,52,3072
mvaa 56,40,32
s_add_u32 s52,s52,s60
mvaa 56,44,104
mvaa 60,20,104
mvaa 60,0,32
mvaa 68,0,36
mvaa 68,20,108
mvaa 64,44,108
mvaa 64,40,36
mvaa 32,56,36
s_waitcnt vmcnt(11) lgkmcnt(0)
s_barrier
s_add_u32 m0,s62,0
mvaa 32,60,108
dl 228,40,53,0
mvaa 36,48,108
ta 0,234,32768
mvaa 36,52,36
ta 2,234,33280
mvaa 28,52,32
ta 20,234,49152
mvaa 28,48,104
ta 22,234,49664
mvaa 24,60,104
ta 44,235,49152
mvaa 24,56,32
dl 229,40,53,1024
mvaa 16,56,28
s_add_u32 s53,s53,s57
ta 46,235,49664
mvaa 16,60,100
ta 40,235,32768
mvaa 20,48,100
ta 42,235,33280
mvaa 20,52,28
mvaa 12,52,24
mvaa 12,48,96
dl 230,44,54,2048
mvaa 8,60,96
ta 52,236,32768
mvaa 8,56,24
ta 54,236,33280
mvaa 76,24,72
ta 48,236,49152
mvaa 76,96,76
ta 50,236,49664
mvaa 96,96,64
ta 60,237,49152
mvaa 96,24,68
dl 231,44,54,3072
mvaa 100,28,68
s_add_u32 m0,s62,16384
ta 62,237,49664
s_add_u32 s54,s54,s57
mvaa 100,100,64
ta 56,237,32768
mvaa 80,100,76
ta 58,237,33280
mvaa 80,28,72
ra 96,233,32768
mvaa 84,32,72
ra 24,232,32768
mvaa 84,104,76
dl 228,40,53,0
mvaa 104,104,64
ra 100,233,34816
mvaa 104,32,68
ra 28,232,34816
mvaa 108,36,68
ra 104,233,36864
mvaa 108,108,64
ra 32,232,36864
mvaa 88,108,76
ta 68,234,36864
mvaa 88,36,72
dl 229,40,53,1024
s_waitcnt lgkmcnt(8)
maaa 236,0,4
s_add_u32 s53,s53,s58
ta 70,234,37376
maaa 236,20,80
ta 64,234,53248
maaa 220,44,80
ta 66,234,53760
maaa 220,40,4
ta 76,235,53248
maaa 216,40,8
ta 78,235,53760
maaa 216,44,84
dl 230,44,54,2048
maaa 232,20,84
ta 72,235,36864
v_accvgpr_read_b32 v0,a236
maaa 232,0,8
ta 74,235,37376
v_accvgpr_read_b32 v1,a237
maaa 228,0,12
ra 108,233,38912
v_accvgpr_read_b32 v2,a238
maaa 228,20,88
ra 36,232,38912
v_accvgpr_read_b32 v3,a239
maaa 212,44,88
dl 231,44,54,3072
maaa 212,40,12
s_add_u32 m0,s98,0
s_add_u32 s54,s54,s58
v_accvgpr_read_b32 v4,a220
maaa 208,40,16
v_accvgpr_read_b32 v5,a221
v_accvgpr_read_b32 v6,a222
maaa 208,44,92
v_accvgpr_read_b32 v7,a223
v_accvgpr_read_b32 v92,a216
maaa 224,20,92
ta 40,236,36864
v_accvgpr_read_b32 v93,a217
maaa 224,0,16
dl 224,36,52,0
maaa 192,52,16
ta 42,236,37376
v_accvgpr_read_b32 v94,a218
maaa 192,48,92
ta 44,236,53248
v_accvgpr_read_b32 v95,a219
maaa 176,60,92
ta 46,236,53760
v_accvgpr_read_b32 v72,a232
s_waitcnt lgkmcnt(5)
maaa 176,56,16
ta 20,237,53248
v_accvgpr_read_b32 v73,a233
maaa 180,56,12
ta 22,237,53760
v_accvgpr_read_b32 v74,a234
maaa 180,60,88
dl 225,36,52,1024
maaa 196,48,88
ta 0,237,36864
v_accvgpr_read_b32 v75,a235
maaa 196,52,12
ta 2,237,37376
v_add_u32_e32 v233,0x10000,v233
maaa 200,52,8
v_add_u32_e32 v232,0x10000,v232
maaa 200,48,84
maaa 184,60,84
maaa 184,56,8
dl 226,36,52,2048
maaa 188,56,4
maaa 188,60,80
maaa 204,48,80
ta 56,234,40960
maaa 204,52,4
ta 58,234,41472
maaa 172,68,4
ta 60,234,57344
maaa 172,64,80
buffer_store_dwordx4 v[160:163],v244,s[24:27],s67 offen offset:0 sc0 sc1
maaa 156,76,80
ta 62,234,57856
maaa 156,72,4
ta 48,235,57344
maaa 152,72,8
ta 50,235,57856
maaa 152,76,84
ta 52,235,40960
maaa 168,64,84
ta 54,235,41472
maaa 168,68,8
dl 227,36,52,3072
maaa 164,68,12
s_add_u32 s52,s52,s56
s_add_u32 m0,s98,4096
maaa 164,64,88
maaa 148,76,88
maaa 148,72,12
maaa 144,72,16
dl 224,36,52,0
maaa 144,76,92
maaa 160,64,92
ta 72,236,40960
maaa 160,68,16
ta 74,236,41472
s_waitcnt lgkmcnt(3)
maaa 128,40,16
ta 76,236,57344
maaa 128,44,92
ta 78,236,57856
maaa 112,20,92
buffer_store_dwordx4 v[164:167],v244,s[24:27],s67 offen offset:1024 sc0 sc1
maaa 112,0,16
ta 64,237,57344
maaa 116,0,12
ta 66,237,57856
maaa 116,20,88
ta 68,237,40960
maaa 132,44,88
ta 70,237,41472
maaa 132,40,12
maaa 136,40,8
dl 225,36,52,1024
maaa 136,44,84
maaa 120,20,84
maaa 120,0,8
maaa 124,0,4
maaa 124,20,80
maaa 140,44,80
dl 226,36,52,2048
ta 0,238,45056
ta 2,238,45568
maaa 140,40,4
ta 20,238,61440
ta 22,238,61952
mvaa 40,56,4
ta 44,239,61440
mvaa 40,60,80
ta 46,239,61952
mvaa 44,48,80
ta 40,239,45056
ta 42,239,45568
s_waitcnt lgkmcnt(8)
mvaa 44,52,4
mvaa 52,52,8
buffer_store_dwordx4 v[168:171],v244,s[24:27],s67 offen offset:2048 sc0 sc1
mvaa 52,48,84
mvaa 48,60,84
mvaa 48,56,8
mvaa 56,56,12
mvaa 56,60,88
dl 227,36,52,3072
mvaa 60,48,88
s_add_u32 s52,s52,s60
mvaa 60,52,12
mvaa 68,52,16
mvaa 68,48,92
mvaa 64,60,92
mvaa 64,56,16
mvaa 32,72,16
s_waitcnt vmcnt(11) lgkmcnt(0)
s_barrier
s_add_u32 m0,s62,32768
mvaa 32,76,92
dl 228,40,53,0
mvaa 36,64,92
ta 52,234,0
mvaa 36,68,16
ta 54,234,512
mvaa 28,68,12
ta 48,234,16384
mvaa 28,64,88
ta 50,234,16896
mvaa 24,76,88
dl 229,40,53,1024
mvaa 24,72,12
s_add_u32 s53,s53,s57
ta 60,235,16384
mvaa 16,72,8
ta 62,235,16896
mvaa 16,76,84
ta 56,235,0
mvaa 20,64,84
ta 58,235,512
mvaa 20,68,8
dl 230,44,54,2048
mvaa 12,68,4
ta 236,236,4096
mvaa 12,64,80
ta 238,236,4608
mvaa 8,76,80
ta 68,236,0
mvaa 8,72,4
ta 70,236,512
mvaa 76,4,0
ta 64,236,16384
mvaa 76,80,20
dl 231,44,54,3072
mvaa 96,80,44
s_add_u32 m0,s62,49152
ta 66,236,16896
s_add_u32 s54,s54,s57
mvaa 96,4,40
ta 76,237,16384
mvaa 100,8,40
ta 78,237,16896
mvaa 100,84,44
ta 72,237,0
mvaa 80,84,20
ta 74,237,512
mvaa 80,8,0
dl 228,40,53,0
mvaa 84,12,0
ta 220,236,20480
mvaa 84,88,20
ta 222,236,20992
mvaa 104,88,44
ta 232,237,20480
mvaa 104,12,40
ta 234,237,20992
mvaa 108,16,40
ta 216,237,4096
mvaa 108,92,44
dl 229,40,53,1024
mvaa 88,92,20
s_add_u32 s53,s53,s58
ta 40,234,4096
mvaa 88,16,0
ta 42,234,4608
s_waitcnt lgkmcnt(8)
mvaa 0,52,24
ta 44,234,20480
mvaa 0,48,96
ta 46,234,20992
mvaa 4,60,96
ta 20,235,20480
mvaa 4,56,24
dl 230,44,54,2048
mvaa 92,56,28
ta 22,235,20992
mvaa 92,60,100
ta 0,235,4096
mvaa 72,48,100
ta 2,235,4608
mvaa 72,52,28
ta 218,237,4608
maaa 228,52,32
dl 231,44,54,3072
maaa 228,48,104
s_add_u32 m0,s98,32768
ra 80,233,0
s_add_u32 s54,s54,s58
maaa 212,60,104
ra 4,232,0
maaa 212,56,32
ra 8,232,2048
maaa 208,56,36
ra 84,233,2048
v_accvgpr_read_b32 v144,a228
maaa 208,60,108
dl 224,36,52,0
maaa 224,48,108
ra 88,233,4096
v_accvgpr_read_b32 v145,a229
maaa 224,52,36
ra 12,232,4096
v_accvgpr_read_b32 v146,a230
maaa 192,68,36
ra 92,233,6144
v_accvgpr_read_b32 v147,a231
maaa 192,64,108
ra 16,232,6144
v_accvgpr_read_b32 v148,a212
maaa 176,76,108
ta 56,234,8192
v_accvgpr_read_b32 v149,a213
s_waitcnt lgkmcnt(9)
maaa 176,72,36
dl 225,36,52,1024
maaa 180,72,32
ta 58,234,8704
v_accvgpr_read_b32 v150,a214
maaa 180,76,104
ta 60,234,24576
v_accvgpr_read_b32 v151,a215
maaa 196,64,104
ta 62,234,25088
v_accvgpr_read_b32 v156,a208
maaa 196,68,32
ta 48,235,24576
v_accvgpr_read_b32 v157,a209
maaa 200,68,28
ta 50,235,25088
v_accvgpr_read_b32 v158,a210
maaa 200,64,100
dl 226,36,52,2048
maaa 184,76,100
ta 52,235,8192
v_accvgpr_read_b32 v159,a211
maaa 184,72,28
ta 54,235,8704
v_accvgpr_read_b32 v152,a224
maaa 188,72,24
v_accvgpr_read_b32 v153,a225
v_accvgpr_read_b32 v154,a226
maaa 188,76,96
v_accvgpr_read_b32 v155,a227
v_add_u32_e32 v233,0xffff0000,v233
maaa 204,64,96
ta 72,236,8192
v_add_u32_e32 v232,0xffff0000,v232
maaa 204,68,24
buffer_store_dwordx4 v[172:175],v244,s[24:27],s67 offen offset:3072 sc0 sc1
maaa 172,40,24
s_mov_b32 s67,266240
ta 74,236,8704
maaa 172,44,96
L_vs12:
s_cbranch_execz L_vr0
ta 76,236,24576
maaa 156,20,96
ta 78,236,25088
maaa 156,0,24
ta 64,237,24576
maaa 152,0,28
ta 66,237,25088
maaa 152,20,100
dl 227,36,52,3072
maaa 168,44,100
s_add_u32 s52,s52,s56
s_add_u32 m0,s98,36864
ta 68,237,8192
maaa 168,40,28
ta 70,237,8704
maaa 164,40,32
maaa 164,44,104
maaa 148,20,104
dl 224,36,52,0
maaa 148,0,32
maaa 144,0,36
maaa 144,20,108
maaa 160,44,108
ta 0,238,12288
maaa 160,40,36
buffer_store_dwordx4 v[176:179],v244,s[24:27],s67 offen offset:0 sc0 sc1
maaa 128,236,36
ta 2,238,12800
maaa 128,220,108
ta 20,238,28672
maaa 112,232,108
ta 22,238,29184
maaa 112,216,36
ta 44,239,28672
maaa 116,216,32
ta 46,239,29184
maaa 116,232,104
dl 225,36,52,1024
maaa 132,220,104
ta 40,239,12288
maaa 132,236,32
ta 42,239,12800
maaa 136,236,28
maaa 136,220,100
maaa 120,232,100
maaa 120,216,28
dl 226,36,52,2048
maaa 124,216,24
maaa 124,232,96
maaa 140,220,96
maaa 140,236,24
s_waitcnt lgkmcnt(2)
mvaa 40,56,24
mvaa 40,60,96
buffer_store_dwordx4 v[180:183],v244,s[24:27],s67 offen offset:1024 sc0 sc1
mvaa 44,48,96
mvaa 44,52,24
mvaa 52,52,28
mvaa 52,48,100
mvaa 48,60,100
mvaa 48,56,28
dl 227,36,52,3072
mvaa 56,56,32
s_add_u32 s52,s52,s60
mvaa 56,60,104
mvaa 60,48,104
mvaa 60,52,32
mvaa 68,52,36
mvaa 68,48,108
mvaa 64,60,108
mvaa 64,56,36
mvaa 32,72,36
s_waitcnt vmcnt(11) lgkmcnt(0)
s_barrier
mvaa 32,76,108
s_cmp_eq_u32 s12,0
s_cselect_b64 exec,1,0
buffer_atomic_add v247,off,s[8:11],s85 sc1
s_mov_b64 exec,-1
mvaa 36,64,108
s_add_u32 m0,s62,0
ta 216,234,32768
s_lshr_b32 s85,s86,1
mvaa 36,68,36
s_lshl_b32 s85,s85,11
ta 218,234,33280
v_bfe_u32 v247,v244,2,6
mvaa 28,68,32
ta 232,234,49152
mvaa 28,64,104
ta 234,234,49664
mvaa 24,76,104
ta 220,235,49152
mvaa 24,72,32
dl 228,40,53,0
mvaa 16,72,28
ta 222,235,49664
mvaa 16,76,100
ta 236,235,32768
mvaa 20,64,100
ta 238,235,33280
mvaa 20,68,28
ta 52,236,32768
mvaa 12,68,24
dl 229,40,53,1024
mvaa 12,64,96
s_add_u32 s53,s53,s57
ta 54,236,33280
mvaa 8,76,96
ta 48,236,49152
mvaa 8,72,24
ta 50,236,49664
mvaa 76,24,0
ta 60,237,49152
mvaa 76,96,20
dl 230,44,54,2048
mvaa 96,96,44
ta 62,237,49664
mvaa 96,24,40
ta 56,237,32768
mvaa 100,28,40
ta 58,237,33280
mvaa 100,100,44
ta 68,234,36864
mvaa 80,100,20
dl 231,44,54,3072
mvaa 80,28,0
s_add_u32 m0,s62,16384
s_add_u32 s54,s54,s57
ta 70,234,37376
mvaa 84,32,0
ta 64,234,53248
mvaa 84,104,20
ta 66,234,53760
mvaa 104,104,44
ta 76,235,53248
mvaa 104,32,40
ta 78,235,53760
mvaa 108,36,40
dl 228,40,53,0
mvaa 108,108,44
ta 72,235,36864
mvaa 88,108,20
ta 74,235,37376
mvaa 88,36,0
ta 228,234,40960
s_waitcnt lgkmcnt(8)
mvaa 0,216,4
ta 230,234,41472
mvaa 0,232,80
ta 212,234,57344
mvaa 4,220,80
dl 229,40,53,1024
mvaa 4,236,4
s_add_u32 s53,s53,s58
ta 40,236,36864
mvaa 92,236,8
ta 42,236,37376
mvaa 92,220,84
ta 44,236,53248
mvaa 72,232,84
ta 46,236,53760
mvaa 72,216,8
ta 20,237,53248
mvaa 144,216,12
dl 230,44,54,2048
mvaa 144,232,88
ta 22,237,53760
mvaa 148,220,88
ta 0,237,36864
mvaa 148,236,12
ta 2,237,37376
mvaa 156,236,16
ta 214,234,57856
mvaa 156,220,92
ta 224,235,57344
mvaa 152,232,92
dl 231,44,54,3072
mvaa 152,216,16
s_add_u32 m0,s98,65536
ta 226,235,57856
s_add_u32 s54,s54,s58
maaa 192,52,16
ta 208,235,40960
maaa 192,48,92
ra 96,233,0
maaa 176,60,92
ta 210,235,41472
maaa 176,56,16
dl 224,36,52,0
maaa 180,56,12
ra 24,232,0
maaa 180,60,88
ra 100,233,2048
v_accvgpr_read_b32 v160,a192
maaa 196,48,88
ra 28,232,2048
v_accvgpr_read_b32 v161,a193
maaa 196,52,12
ra 104,233,4096
v_accvgpr_read_b32 v162,a194
maaa 200,52,8
ra 32,232,4096
v_accvgpr_read_b32 v163,a195
maaa 200,48,84
dl 225,36,52,1024
maaa 184,60,84
ra 108,233,6144
v_accvgpr_read_b32 v164,a176
maaa 184,56,8
ra 36,232,6144
v_accvgpr_read_b32 v165,a177
maaa 188,56,4
ta 236,236,40960
v_accvgpr_read_b32 v166,a178
maaa 188,60,80
ta 238,236,41472
v_accvgpr_read_b32 v167,a179
maaa 204,48,80
ta 220,236,57344
v_accvgpr_read_b32 v172,a180
maaa 204,52,4
dl 226,36,52,2048
s_waitcnt lgkmcnt(8)
maaa 172,68,4
ta 222,236,57856
v_accvgpr_read_b32 v173,a181
maaa 172,64,80
ta 232,237,57344
v_accvgpr_read_b32 v174,a182
maaa 156,76,80
ta 234,237,57856
v_accvgpr_read_b32 v175,a183
maaa 156,72,4
ta 216,237,40960
v_accvgpr_read_b32 v168,a196
maaa 152,72,8
ta 218,237,41472
v_accvgpr_read_b32 v169,a197
maaa 152,76,84
buffer_store_dwordx4 v[184:187],v244,s[24:27],s67 offen offset:2048 sc0 sc1
maaa 168,64,84
ta 56,238,45056
v_accvgpr_read_b32 v170,a198
maaa 168,68,8
ta 58,238,45568
v_accvgpr_read_b32 v171,a199
maaa 164,68,12
ta 60,238,61440
maaa 164,64,88
ta 62,238,61952
maaa 148,76,88
ta 48,239,61440
maaa 148,72,12
dl 227,36,52,3072
maaa 144,72,16
s_add_u32 s52,s52,s56
s_add_u32 m0,s98,69632
)"
R"(ta 50,239,61952
maaa 144,76,92
ta 52,239,45056
maaa 160,64,92
ta 54,239,45568
maaa 160,68,16
maaa 128,40,16
dl 224,36,52,0
maaa 128,44,92
maaa 112,20,92
maaa 112,0,16
maaa 116,0,12
maaa 116,20,88
dl 225,36,52,1024
maaa 132,44,88
maaa 132,40,12
maaa 136,40,8
maaa 136,44,84
maaa 120,20,84
maaa 120,0,8
dl 226,36,52,2048
maaa 124,0,4
maaa 124,20,80
maaa 140,44,80
maaa 140,40,4
mvaa 40,228,4
mvaa 40,212,80
buffer_store_dwordx4 v[188:191],v244,s[24:27],s67 offen offset:3072 sc0 sc1
mvaa 44,224,80
s_mov_b32 s67,270336
mvaa 44,208,4
mvaa 52,208,8
mvaa 52,224,84
mvaa 48,212,84
mvaa 48,228,8
dl 227,36,52,3072
mvaa 56,228,12
s_add_u32 s52,s52,s60
mvaa 56,212,88
mvaa 60,224,88
mvaa 60,208,12
mvaa 68,208,16
mvaa 68,224,92
mvaa 64,212,92
mvaa 64,228,16
s_waitcnt lgkmcnt(0)
mvaa 32,236,16
s_waitcnt vmcnt(10)
s_barrier
s_add_u32 m0,s62,32768
mvaa 32,220,92
dl 228,40,53,0
mvaa 36,232,92
ta 72,234,0
mvaa 36,216,16
ta 74,234,512
mvaa 28,216,12
ta 76,234,16384
mvaa 28,232,88
ta 78,234,16896
mvaa 24,220,88
ta 64,235,16384
mvaa 24,236,12
dl 229,40,53,1024
mvaa 16,236,8
s_add_u32 s53,s53,s57
ta 66,235,16896
mvaa 16,220,84
ta 68,235,0
mvaa 20,232,84
ta 70,235,512
mvaa 20,216,8
ta 0,236,0
mvaa 12,216,4
ta 2,236,512
mvaa 12,232,80
dl 230,44,54,2048
mvaa 8,220,80
ta 20,236,16384
mvaa 8,236,4
ta 22,236,16896
mvaa 76,4,56
ta 44,237,16384
mvaa 76,80,60
ta 46,237,16896
mvaa 96,80,48
ta 40,237,0
mvaa 96,4,52
dl 231,44,54,3072
mvaa 100,8,52
s_add_u32 m0,s62,49152
s_add_u32 s54,s54,s57
ta 42,237,512
mvaa 100,84,48
ta 208,234,4096
mvaa 80,84,60
ta 210,234,4608
mvaa 80,8,56
ta 224,234,20480
mvaa 84,12,56
ta 226,234,20992
mvaa 84,88,60
dl 228,40,53,0
mvaa 104,88,48
ta 212,235,20480
mvaa 104,12,52
ta 214,235,20992
mvaa 108,16,52
ta 228,235,4096
mvaa 108,92,48
ta 230,235,4608
mvaa 88,92,60
ta 192,236,8192
mvaa 88,16,56
dl 229,40,53,1024
s_waitcnt lgkmcnt(8)
mvaa 0,72,24
s_add_u32 s53,s53,s58
ta 194,236,8704
mvaa 0,76,96
ta 216,236,4096
mvaa 4,64,96
ta 218,236,4608
mvaa 4,68,24
ta 232,236,20480
mvaa 92,68,28
dl 230,44,54,2048
mvaa 92,64,100
ta 234,236,20992
mvaa 72,76,100
ta 220,237,20480
mvaa 72,72,28
ta 222,237,20992
mvaa 144,72,32
ta 236,237,4096
mvaa 144,76,104
dl 231,44,54,3072
mvaa 148,64,104
s_add_u32 m0,s98,0
ta 238,237,4608
s_add_u32 s54,s54,s58
mvaa 148,68,32
ta 176,236,24576
mvaa 156,68,36
ta 178,236,25088
mvaa 156,64,108
ta 196,237,24576
mvaa 152,76,108
ta 198,237,25088
mvaa 152,72,36
dl 224,36,52,0
mvaa 160,0,36
ta 180,237,8192
mvaa 160,20,108
ra 80,233,32768
mvaa 164,44,108
ta 182,237,8704
mvaa 164,40,36
ra 4,232,32768
mvaa 172,40,32
ta 52,234,8192
mvaa 172,44,104
dl 225,36,52,1024
mvaa 168,20,104
ta 54,234,8704
mvaa 168,0,32
ta 48,234,24576
maaa 200,0,28
ta 50,234,25088
maaa 200,20,100
ta 60,235,24576
maaa 184,44,100
ta 62,235,25088
maaa 184,40,28
dl 226,36,52,2048
maaa 188,40,24
ta 56,235,8192
maaa 188,44,96
ta 58,235,8704
v_accvgpr_read_b32 v176,a200
maaa 204,20,96
ra 8,232,34816
v_accvgpr_read_b32 v177,a201
maaa 204,0,24
ra 84,233,34816
v_accvgpr_read_b32 v178,a202
s_waitcnt lgkmcnt(9)
maaa 172,208,24
ra 88,233,36864
v_accvgpr_read_b32 v179,a203
maaa 172,224,96
buffer_store_dwordx4 v[192:195],v244,s[24:27],s67 offen offset:0 sc0 sc1
maaa 156,212,96
ra 12,232,36864
v_accvgpr_read_b32 v180,a184
maaa 156,228,24
ra 92,233,38912
v_accvgpr_read_b32 v181,a185
maaa 152,228,28
ra 16,232,38912
v_accvgpr_read_b32 v182,a186
maaa 152,212,100
ta 68,238,12288
v_accvgpr_read_b32 v183,a187
maaa 168,224,100
dl 227,36,52,3072
maaa 168,208,28
s_add_u32 s52,s52,s56
s_add_u32 m0,s98,4096
ta 70,238,12800
maaa 164,208,32
ta 64,238,28672
v_accvgpr_read_b32 v188,a188
maaa 164,224,104
ta 66,238,29184
v_accvgpr_read_b32 v189,a189
maaa 148,212,104
ta 76,239,28672
v_accvgpr_read_b32 v190,a190
maaa 148,228,32
dl 224,36,52,0
maaa 144,228,36
ta 78,239,29184
v_accvgpr_read_b32 v191,a191
maaa 144,212,108
ta 72,239,12288
v_accvgpr_read_b32 v184,a204
maaa 160,224,108
ta 74,239,12800
v_accvgpr_read_b32 v185,a205
maaa 160,208,36
v_accvgpr_read_b32 v186,a206
v_accvgpr_read_b32 v187,a207
maaa 128,216,36
v_add_u32_e32 v233,0x10000,v233
v_add_u32_e32 v232,0x10000,v232
maaa 128,232,108
buffer_store_dwordx4 v[196:199],v244,s[24:27],s67 offen offset:1024 sc0 sc1
maaa 112,220,108
maaa 112,236,36
maaa 116,236,32
maaa 116,220,104
maaa 132,232,104
maaa 132,216,32
dl 225,36,52,1024
maaa 136,216,28
maaa 136,232,100
maaa 120,220,100
maaa 120,236,28
maaa 124,236,24
maaa 124,220,96
dl 226,36,52,2048
maaa 140,232,96
maaa 140,216,24
s_waitcnt lgkmcnt(0)
mvaa 40,52,24
mvaa 40,48,96
mvaa 44,60,96
mvaa 44,56,24
buffer_store_dwordx4 v[200:203],v244,s[24:27],s67 offen offset:2048 sc0 sc1
mvaa 52,56,28
mvaa 52,60,100
mvaa 48,48,100
mvaa 48,52,28
mvaa 56,52,32
dl 227,36,52,3072
mvaa 56,48,104
s_add_u32 s52,s52,s60
mvaa 60,60,104
L_vs13:
s_cbranch_execz L_vr0s
mvaa 60,56,32
mvaa 68,56,36
mvaa 68,60,108
mvaa 64,48,108
mvaa 64,52,36
mvaa 32,192,36
s_waitcnt vmcnt(11)
s_barrier
s_add_u32 m0,s62,0
mvaa 32,176,108
dl 228,40,53,0
mvaa 36,196,108
ta 40,234,32768
mvaa 36,180,36
ta 42,234,33280
mvaa 28,180,32
ta 44,234,49152
mvaa 28,196,104
ta 46,234,49664
mvaa 24,176,104
ta 20,235,49152
mvaa 24,192,32
dl 229,40,53,1024
mvaa 16,192,28
s_add_u32 s53,s53,s57
ta 22,235,49664
mvaa 16,176,100
ta 0,235,32768
mvaa 20,196,100
ta 2,235,33280
mvaa 20,180,28
ta 228,236,32768
mvaa 12,180,24
ta 230,236,33280
mvaa 12,196,96
dl 230,44,54,2048
mvaa 8,176,96
ta 212,236,49152
mvaa 8,192,24
ta 214,236,49664
mvaa 76,24,68
ta 224,237,49152
mvaa 76,96,64
ta 226,237,49664
mvaa 96,96,76
dl 231,44,54,3072
mvaa 96,24,72
s_add_u32 m0,s62,16384
s_add_u32 s54,s54,s57
ta 208,237,32768
mvaa 100,28,72
ta 210,237,33280
mvaa 100,100,76
ta 236,234,36864
mvaa 80,100,64
ta 238,234,37376
mvaa 80,28,68
ta 220,234,53248
mvaa 84,32,68
dl 228,40,53,0
mvaa 84,104,64
ta 222,234,53760
mvaa 104,104,76
ta 232,235,53248
mvaa 104,32,72
ta 234,235,53760
mvaa 108,36,72
ta 216,235,36864
mvaa 108,108,76
ta 218,235,37376
mvaa 88,108,64
dl 229,40,53,1024
mvaa 88,36,68
s_add_u32 s53,s53,s58
ta 56,236,36864
s_waitcnt lgkmcnt(8)
mvaa 0,40,4
ta 58,236,37376
mvaa 0,44,80
ta 60,236,53248
mvaa 4,20,80
ta 62,236,53760
mvaa 4,0,4
ta 48,237,53248
mvaa 92,0,8
dl 230,44,54,2048
mvaa 92,20,84
ta 50,237,53760
mvaa 72,44,84
ta 200,238,45056
mvaa 72,40,8
ta 202,238,45568
mvaa 144,40,12
ta 184,238,61440
mvaa 144,44,88
ta 186,238,61952
mvaa 148,20,88
dl 231,44,54,3072
mvaa 148,0,12
s_add_u32 m0,s98,32768
ta 204,239,61440
s_add_u32 s54,s54,s58
mvaa 156,0,16
ta 52,237,36864
mvaa 156,20,92
ta 54,237,37376
mvaa 152,44,92
ta 206,239,61952
mvaa 152,40,16
dl 224,36,52,0
mvaa 160,228,16
ta 188,239,45056
mvaa 160,212,92
ta 190,239,45568
mvaa 164,224,92
ta 180,234,40960
mvaa 164,208,16
ta 182,234,41472
mvaa 172,208,12
s_mov_b64 exec,3
global_load_dword a250,v253,s[96:97]
s_mov_b64 exec,-1
mvaa 172,224,88
ta 196,234,57344
v_lshrrev_b32_e32 v253,1,v244
mvaa 168,212,88
ta 198,234,57856
mvaa 168,228,12
ta 176,235,57344
mvaa 176,228,8
ta 178,235,57856
mvaa 176,212,84
ta 192,235,40960
mvaa 180,224,84
dl 225,36,52,1024
mvaa 180,208,8
ta 194,235,41472
mvaa 188,208,4
ra 96,233,0
mvaa 188,224,80
ra 24,232,0
mvaa 184,212,80
ra 100,233,2048
mvaa 184,228,4
ra 28,232,2048
s_waitcnt lgkmcnt(9)
maaa 172,236,4
buffer_store_dwordx4 v[204:207],v244,s[24:27],s67 offen offset:3072 sc0 sc1
maaa 172,220,80
s_mov_b32 s67,274432
ra 32,232,4096
maaa 156,232,80
ra 104,233,4096
maaa 156,216,4
ta 72,236,40960
maaa 152,216,8
ta 74,236,41472
v_accvgpr_read_b32 v192,a172
maaa 152,232,84
ta 76,236,57344
v_accvgpr_read_b32 v193,a173
maaa 168,220,84
dl 226,36,52,2048
maaa 168,236,8
ta 78,236,57856
v_accvgpr_read_b32 v194,a174
maaa 164,236,12
ta 64,237,57344
v_accvgpr_read_b32 v195,a175
maaa 164,220,88
ta 66,237,57856
v_accvgpr_read_b32 v196,a156
maaa 148,232,88
ra 108,233,6144
v_add_u32_e32 v233,0xffff0000,v233
maaa 148,216,12
ra 36,232,6144
v_add_u32_e32 v232,0xffff0000,v232
maaa 144,216,16
dl 227,36,52,3072
maaa 144,232,92
s_add_u32 s52,s52,s56
s_add_u32 m0,s98,36864
ta 68,237,40960
maaa 160,220,92
ta 70,237,41472
v_accvgpr_read_b32 v197,a157
maaa 160,236,16
v_accvgpr_read_b32 v198,a158
v_accvgpr_read_b32 v199,a159
maaa 128,56,16
v_accvgpr_read_b32 v204,a152
v_accvgpr_read_b32 v205,a153
maaa 128,60,92
buffer_store_dwordx4 v[208:211],v244,s[24:27],s67 offen offset:0 sc0 sc1
maaa 112,48,92
v_accvgpr_read_b32 v206,a154
v_accvgpr_read_b32 v207,a155
maaa 112,52,16
v_accvgpr_read_b32 v200,a168
v_accvgpr_read_b32 v201,a169
maaa 116,52,12
v_accvgpr_read_b32 v202,a170
v_accvgpr_read_b32 v203,a171
maaa 116,48,88
maaa 132,60,88
dl 224,36,52,0
maaa 132,56,12
maaa 136,56,8
maaa 136,60,84
maaa 120,48,84
maaa 120,52,8
dl 225,36,52,1024
maaa 124,52,4
maaa 124,48,80
maaa 140,60,80
maaa 140,56,4
mvaa 40,180,4
buffer_store_dwordx4 v[212:215],v244,s[24:27],s67 offen offset:1024 sc0 sc1
s_waitcnt lgkmcnt(0)
mvaa 40,196,80
mvaa 44,176,80
mvaa 44,192,4
mvaa 52,192,8
mvaa 52,176,84
dl 226,36,52,2048
mvaa 48,196,84
mvaa 48,180,8
mvaa 56,180,12
s_waitcnt vmcnt(11)
s_barrier
mvaa 56,196,88
ta 0,234,0
mvaa 60,176,88
dl 227,36,52,3072
mvaa 60,192,12
s_add_u32 m0,s62,32768
ta 2,234,512
s_add_u32 s52,s52,s60
mvaa 68,192,16
s_lshl_b32 s60,%1,7
ta 20,234,16384
s_and_b32 s87,s60,7552
mvaa 68,176,92
s_add_u32 s61,s60,83914752
ta 22,234,16896
mvaa 64,196,92
ta 44,235,16384
mvaa 64,180,16
dl 228,40,53,0
mvaa 32,72,16
ta 46,235,16896
mvaa 32,76,92
ta 40,235,0
mvaa 36,64,92
ta 42,235,512
mvaa 36,68,16
ta 208,236,0
mvaa 28,68,12
dl 229,40,53,1024
mvaa 28,64,88
s_add_u32 s53,s53,s57
ta 210,236,512
mvaa 24,76,88
ta 224,236,16384
mvaa 24,72,12
ta 226,236,16896
mvaa 16,72,8
ta 212,237,16384
mvaa 16,76,84
dl 230,44,54,2048
mvaa 20,64,84
ta 214,237,16896
mvaa 20,68,8
ta 228,237,0
mvaa 12,68,4
ta 230,237,512
mvaa 12,64,80
ta 216,234,4096
mvaa 8,76,80
dl 231,44,54,3072
mvaa 8,72,4
s_add_u32 m0,s62,49152
s_add_u32 s54,s54,s57
ta 218,234,4608
mvaa 76,4,200
ta 232,234,20480
mvaa 76,80,184
ta 234,234,20992
mvaa 96,80,204
ta 220,235,20480
mvaa 96,4,188
dl 228,40,53,0
mvaa 100,8,188
ta 222,235,20992
mvaa 100,84,204
ta 236,235,4096
mvaa 80,84,184
ta 238,235,4608
mvaa 80,8,200
ta 52,236,4096
mvaa 84,12,200
dl 229,40,53,1024
mvaa 84,88,184
s_add_u32 s53,s53,s58
ta 54,236,4608
mvaa 104,88,204
ta 48,236,20480
mvaa 104,12,188
ta 50,236,20992
mvaa 108,16,188
ta 60,237,20480
mvaa 108,92,204
dl 230,44,54,2048
mvaa 88,92,184
ta 62,237,20992
mvaa 88,16,200
ta 56,237,4096
s_waitcnt lgkmcnt(8)
mvaa 0,0,24
ta 58,237,4608
mvaa 0,20,96
ta 192,234,8192
mvaa 4,44,96
dl 231,44,54,3072
mvaa 4,40,24
s_add_u32 m0,s98,65536
ta 194,234,8704
s_add_u32 s54,s54,s58
mvaa 92,40,28
ta 176,234,24576
mvaa 92,44,100
ta 178,234,25088
mvaa 72,20,100
ta 196,235,24576
mvaa 72,0,28
dl 224,36,52,0
mvaa 144,0,32
ta 198,235,25088
mvaa 144,20,104
ta 180,235,8192
mvaa 148,44,104
ta 182,235,8704
mvaa 148,40,32
ta 68,236,8192
mvaa 156,40,36
dl 225,36,52,1024
mvaa 156,44,108
ta 70,236,8704
mvaa 152,20,108
ta 64,236,24576
mvaa 152,0,36
ta 66,236,25088
mvaa 160,208,36
ta 76,237,24576
mvaa 160,224,108
dl 226,36,52,2048
mvaa 164,212,108
ta 78,237,25088
mvaa 164,228,36
ta 72,237,8192
mvaa 172,228,32
ta 74,237,8704
mvaa 172,212,104
ra 80,233,0
mvaa 168,224,104
buffer_store_dwordx4 v[216:219],v244,s[24:27],s67 offen offset:2048 sc0 sc1
mvaa 168,208,32
ra 4,232,0
mvaa 176,208,28
ra 8,232,2048
mvaa 176,224,100
ra 84,233,2048
mvaa 180,212,100
ra 88,233,4096
mvaa 180,228,28
dl 227,36,52,3072
mvaa 188,228,24
s_add_u32 s52,s52,s56
s_add_u32 m0,s98,69632
ra 12,232,4096
mvaa 188,212,96
s_mov_b32 s98,s94
ta 188,238,12288
s_mov_b32 s99,s98
mvaa 184,224,96
s_and_b32 s94,s12,1
ta 190,238,12800
s_lshl_b32 s94,s94,14
mvaa 184,208,24
s_mul_i32 s56,s59,12288
ta 204,238,28672
mvaa 192,216,24
dl 224,36,52,0
mvaa 192,232,96
ta 206,238,29184
v_mov_b32_e32 v224,v244
mvaa 196,220,96
ta 184,239,28672
s_waitcnt lgkmcnt(8)
mvaa 196,236,24
ta 186,239,29184
mvaa 204,236,28
ra 16,232,6144
v_add_u32_e32 v232,0x8000,v232
mvaa 204,220,100
dl 225,36,52,1024
mvaa 200,232,100
ra 92,233,6144
v_add_u32_e32 v233,0x8000,v233
mvaa 200,216,28
ta 200,239,12288
v_add_u32_e32 v225,65536,v224
maaa 164,216,32
ta 202,239,12800
maaa 164,232,104
maaa 148,220,104
dl 226,36,52,2048
maaa 148,236,32
v_mov_b32_e32 v226,1065369472
maaa 144,236,36
maaa 144,220,108
maaa 160,232,108
maaa 160,216,36
dl 227,36,52,3072
maaa 128,52,36
s_mov_b64 s[36:37],s[28:29]
v_accvgpr_read_b32 v208,a164
v_accvgpr_read_b32 v209,a165
maaa 128,48,108
s_mov_b32 s38,8388608
v_accvgpr_read_b32 v210,a166
v_accvgpr_read_b32 v211,a167
maaa 112,60,108
s_mov_b32 s39,131072
v_accvgpr_read_b32 v212,a148
v_accvgpr_read_b32 v213,a149
maaa 112,56,36
s_lshl_b32 s52,s12,12
v_accvgpr_read_b32 v214,a150
v_accvgpr_read_b32 v215,a151
maaa 116,56,32
buffer_store_dwordx4 v[220:223],v244,s[24:27],s67 offen offset:3072 sc0 sc1
maaa 116,60,104
s_mov_b32 s67,0x5550055
v_accvgpr_read_b32 v216,a160
v_accvgpr_read_b32 v217,a161
maaa 132,48,104
s_mov_b32 s26,2097152
v_accvgpr_read_b32 v218,a162
v_accvgpr_read_b32 v219,a163
maaa 132,52,32
s_mov_b32 s27,131072
v_accvgpr_read_b32 v220,a144
v_accvgpr_read_b32 v221,a145
maaa 136,52,28
s_mov_b64 s[24:25],s[82:83]
v_accvgpr_read_b32 v222,a146
v_accvgpr_read_b32 v223,a147
maaa 136,48,100
s_mov_b32 s83,0
v_cndmask_b32_e64 v116,v241,0,s[66:67]
s_mov_b32 s66,0xa0000
maaa 120,60,100
s_mov_b32 s67,0xaaa00aa
v_cndmask_b32_e64 v120,v116,v241,s[64:65]
v_cndmask_b32_e64 v117,v241,0,s[66:67]
maaa 120,56,28
s_mov_b32 s66,0x150001
s_mov_b32 s67,0x15550155
v_cndmask_b32_e64 v118,v241,0,s[66:67]
maaa 124,56,24
s_mov_b32 s66,0x2a0002
s_mov_b32 s67,0x2aaa02aa
v_cndmask_b32_e64 v119,v241,0,s[66:67]
maaa 124,60,96
s_lshr_b32 s66,s18,2
s_lshl_b32 s67,s18,13
s_lshl_b32 s66,s66,13
maaa 140,48,96
s_waitcnt vmcnt(10) lgkmcnt(0)
s_barrier
s_add_u32 m0,s62,0
maaa 140,52,24
dl 228,40,53,0
mvaa 40,192,24
s_or_b32 s82,s15,16384
ta 172,234,32768
v_cndmask_b32_e64 v121,v117,v241,s[64:65]
mvaa 40,176,96
ta 174,234,33280
v_cndmask_b32_e64 v122,v118,v241,s[64:65]
mvaa 44,196,96
ta 156,234,49152
v_cndmask_b32_e64 v123,v119,v241,s[64:65]
mvaa 44,180,24
ta 158,234,49664
mvaa 52,180,28
dl 229,40,53,1024
mvaa 52,196,100
s_add_u32 s53,s53,s57
ta 168,235,49152
mvaa 48,176,100
ta 170,235,49664
mvaa 48,192,28
ta 152,235,32768
mvaa 56,192,32
ta 154,235,33280
mvaa 56,176,104
dl 230,44,54,2048
mvaa 60,196,104
ta 20,236,32768
mvaa 60,180,32
ta 22,236,33280
mvaa 68,180,36
ta 0,236,49152
mvaa 68,196,108
ta 2,236,49664
mvaa 64,176,108
dl 231,44,54,3072
mvaa 64,192,36
s_add_u32 m0,s62,16384
s_add_u32 s54,s54,s57
ta 228,237,49152
mvaa 32,68,36
ta 230,237,49664
mvaa 32,64,108
ta 212,237,32768
mvaa 36,76,108
ta 214,237,33280
mvaa 36,72,36
dl 228,40,53,0
mvaa 28,72,32
L_vs14:
s_cbranch_execz L_vr0s
ta 224,234,36864
mvaa 28,76,104
ta 226,234,37376
mvaa 24,64,104
ta 208,234,53248
mvaa 24,68,32
ta 210,234,53760
mvaa 16,68,28
dl 229,40,53,1024
mvaa 16,64,100
s_add_u32 s53,s53,s58
ta 236,235,53248
mvaa 20,76,100
ta 238,235,53760
mvaa 20,72,28
ta 220,235,36864
mvaa 12,72,24
ta 222,235,37376
mvaa 12,76,96
dl 230,44,54,2048
mvaa 8,64,96
ta 232,236,36864
mvaa 8,68,24
ta 234,236,37376
mvaa 76,24,188
ta 216,236,53248
mvaa 76,96,204
ta 218,236,53760
mvaa 96,96,184
dl 231,44,54,3072
mvaa 96,24,200
s_add_u32 s54,s54,s58
ta 164,237,57344
s_add_u32 s58,s56,8192
mvaa 100,28,200
ta 166,237,57856
mvaa 100,100,184
ta 148,237,40960
mvaa 80,100,204
ta 150,237,41472
mvaa 80,28,188
ta 160,238,45056
mvaa 84,32,188
ta 162,238,45568
mvaa 84,104,204
ta 144,238,61440
mvaa 104,104,184
ta 146,238,61952
mvaa 104,32,200
ta 180,237,53248
mvaa 108,36,200
ta 182,237,53760
mvaa 108,108,184
ta 196,237,36864
mvaa 88,108,204
ta 198,237,37376
mvaa 88,36,188
ta 176,234,40960
s_waitcnt lgkmcnt(10)
mvaa 0,172,4
ta 178,234,41472
mvaa 0,156,80
ta 192,234,57344
mvaa 4,168,80
ta 194,234,57856
mvaa 4,152,4
ra 24,232,0
mvaa 92,152,8
ra 28,232,2048
mvaa 92,168,84
ra 32,232,4096
mvaa 72,156,84
ta 200,235,57344
mvaa 72,172,8
ta 202,235,57856
mvaa 144,172,12
ta 184,235,40960
mvaa 144,156,88
ta 186,235,41472
mvaa 148,168,88
ta 204,236,40960
mvaa 148,152,12
ta 206,236,41472
mvaa 156,152,16
ra 36,232,6144
mvaa 156,168,92
ta 188,236,57344
mvaa 152,156,92
ta 190,236,57856
mvaa 152,172,16
ta 152,239,61440
mvaa 160,20,16
ta 154,239,61952
mvaa 160,0,92
ta 168,239,45056
mvaa 164,228,92
ta 170,239,45568
mvaa 164,212,16
mvaa 172,212,12
mvaa 172,228,88
mvaa 168,0,88
mvaa 168,20,12
mvaa 176,20,8
mvaa 176,0,84
mvaa 180,228,84
mvaa 180,212,8
mvaa 188,212,4
mvaa 188,228,80
mvaa 184,0,80
mvaa 184,20,4
mvaa 192,224,4
mvaa 192,208,80
mvaa 196,236,80
mvaa 196,220,4
mvaa 204,220,8
mvaa 204,236,84
mvaa 200,208,84
mvaa 200,224,8
mvaa 208,224,12
mvaa 208,208,88
mvaa 212,236,88
mvaa 212,220,12
mvaa 220,220,16
mvaa 220,236,92
mvaa 216,208,92
mvaa 216,224,16
maaa 128,232,16
maaa 128,216,92
s_waitcnt lgkmcnt(0)
maaa 112,180,92
s_waitcnt vmcnt(0)
s_barrier
s_add_u32 m0,s62,32768
maaa 112,196,16
dl 228,40,53,0
maaa 116,196,12
ta 40,234,0
maaa 116,180,88
ta 42,234,512
maaa 132,216,88
ta 44,235,0
maaa 132,232,12
ta 46,235,512
maaa 136,232,8
dl 229,40,53,1024
maaa 136,216,84
s_add_u32 s53,s53,s57
ta 48,236,0
maaa 120,180,84
ta 50,236,512
maaa 120,196,8
ta 52,237,0
maaa 124,196,4
ta 54,237,512
maaa 124,180,80
dl 230,44,54,2048
maaa 140,216,80
ta 56,234,4096
maaa 140,232,4
ta 58,234,4608
mvaa 40,176,4
ta 60,235,4096
mvaa 40,192,80
ta 62,235,4608
mvaa 44,200,80
dl 231,44,54,3072
mvaa 44,184,4
s_add_u32 m0,s62,49152
ta 64,236,4096
s_add_u32 s54,s54,s57
mvaa 52,184,8
s_add_u32 s57,s56,4096
ta 66,236,4608
mvaa 52,200,84
ta 68,237,4096
mvaa 48,192,84
ta 70,237,4608
mvaa 48,176,8
buffer_load_dword v248,v250,s[8:11],0 offen sc1
mvaa 56,176,12
ta 72,234,8192
mvaa 56,192,88
ta 74,234,8704
mvaa 60,200,88
ta 76,235,8192
mvaa 60,184,12
ta 78,235,8704
mvaa 68,184,16
dl 228,40,53,0
mvaa 68,200,92
ta 96,236,8192
mvaa 64,192,92
ta 98,236,8704
mvaa 64,176,16
)"
R"(ta 100,237,8192
mvaa 32,204,16
ta 102,237,8704
mvaa 32,188,92
dl 229,40,53,1024
mvaa 36,164,92
s_lshl_b32 s43,s19,15
ta 104,238,12288
s_add_u32 s42,s43,16384
mvaa 36,148,16
s_add_u32 s53,s87,83898368
ta 106,238,12800
s_lshl_b32 s87,s91,8
mvaa 28,148,12
s_lshl_b32 s91,s23,2
ta 108,239,12288
s_mov_b32 s40,858993459
mvaa 28,164,88
s_mov_b32 s41,858993459
ta 110,239,12800
v_cndmask_b32_e64 v228,0,v226,s[40:41]
mvaa 24,188,88
dl 230,44,54,2048
mvaa 24,204,12
ra 20,233,0
v_cndmask_b32_e64 v229,0,v226,s[40:41]
mvaa 16,204,8
ra 0,232,32768
v_cndmask_b32_e64 v230,0,v226,s[40:41]
mvaa 16,188,84
ta 212,235,20480
mvaa 20,164,84
ta 214,235,20992
mvaa 20,148,8
dl 231,44,54,3072
mvaa 12,148,4
s_add_u32 s54,s87,s88
ta 236,237,20480
s_lshl_b32 s87,s12,16
mvaa 12,164,80
s_add_u32 s87,s87,s54
ta 238,237,20992
s_andn2_b32 s87,s87,64
mvaa 8,188,80
s_lshl_b32 s88,s66,5
ta 244,235,24576
s_add_u32 s63,s87,s88
mvaa 8,204,4
s_lshl_b32 s88,s67,5
ta 246,235,25088
s_add_u32 s65,s87,s88
mvaa 76,4,160
s_and_b32 s87,s20,4
ta 216,236,20480
s_lshl_b32 s87,s87,8
mvaa 76,80,144
s_and_b32 s88,s20,3
ta 218,236,20992
s_add_u32 s85,s85,s87
mvaa 96,80,152
s_add_u32 s85,s85,s89
ta 192,234,20480
s_lshl_b32 s89,s88,20
mvaa 96,4,168
s_add_u32 s85,s85,s89
ta 194,234,20992
s_and_b32 s89,s86,7
mvaa 100,8,168
s_add_u32 s49,s49,s87
ta 164,235,16384
s_lshl_b32 s89,s89,7
mvaa 100,84,152
s_add_u32 s49,s49,s89
ta 166,235,16896
s_lshl_b32 s89,s88,18
mvaa 80,84,144
s_add_u32 s49,s49,s89
ta 188,237,16384
s_lshl_b32 s89,s12,6
mvaa 80,8,160
s_add_u32 s89,s89,83886080
ta 190,237,16896
s_add_u32 s91,s91,s89
mvaa 84,12,160
s_mov_b32 s46,16384
ra 80,233,4096
s_mov_b32 s86,268435456
mvaa 84,88,144
s_bfe_u32 s87,s98,327691
s_cselect_b32 s87,2048,0
ra 4,233,32768
v_cndmask_b32_e64 v231,0,v226,s[40:41]
mvaa 104,88,152
s_mov_b32 s40,0xf0000
ra 84,232,36864
s_mov_b32 s41,0xfff00ff
mvaa 104,12,168
s_sub_u32 s98,s98,s87
ra 8,233,2048
v_cndmask_b32_e64 v226,0,v252,s[40:41]
mvaa 108,16,168
s_mov_b32 s40,0x3f0003
ra 88,233,36864
s_mov_b32 s41,0x3fff03ff
mvaa 108,92,152
s_sub_u32 s47,32768,s43
ra 12,232,34816
v_cndmask_b32_e64 v227,0,v252,s[40:41]
mvaa 88,92,144
s_mov_b32 s40,0xfff0ffff
ta 168,236,16384
s_mov_b32 s41,0xf000ff00
mvaa 88,16,160
s_add_u32 s47,s47,32768
ta 170,236,16896
s_lshr_b32 s45,s15,1
s_waitcnt lgkmcnt(10)
mvaa 0,40,24
s_xor_b32 s62,s63,128
ta 144,234,16384
s_xor_b32 s64,s65,128
mvaa 4,44,24
s_ff1_i32_b32 s80,s45
ta 146,234,16896
s_sub_u32 s44,s56,2048
mvaa 92,44,28
ra 92,233,6144
mvaa 72,40,28
ra 16,233,34816
mvaa 144,40,32
mvaa 148,44,32
mvaa 156,44,36
mvaa 152,40,36
mvaa 160,48,36
ra 44,232,38912
v_mov_b32_e32 v232,0xc1a00000
mvaa 164,52,36
ra 40,233,38912
v_mov_b32_e32 v233,0xc1a00000
mvaa 172,52,32
mvaa 168,48,32
mvaa 176,48,28
mvaa 180,52,28
mvaa 188,52,24
mvaa 184,48,24
mvaa 192,56,24
ta 52,239,28672
mvaa 196,60,24
ta 54,239,29184
mvaa 204,60,28
mvaa 200,56,28
mvaa 208,56,32
mvaa 212,60,32
mvaa 220,60,36
mvaa 216,56,36
maaa 128,64,36
maaa 112,68,36
maaa 116,68,32
maaa 132,64,32
maaa 136,64,28
maaa 120,68,28
maaa 124,68,24
maaa 140,64,24
mvaa 40,72,24
mvaa 44,76,24
ta 64,238,28672
ta 66,238,29184
mvaa 52,76,28
s_waitcnt vmcnt(4)
v_and_b32_e32 v248,v242,v248
v_cmp_ne_u32_e64 s[100:101],v243,v248
mvaa 48,72,28
s_waitcnt vmcnt(0)
s_barrier
ta 148,234,32768
ta 150,234,33280
ta 152,234,49152
ta 154,234,49664
ta 156,235,49152
mvaa 56,72,32
s_cmp_lg_u64 s[100:101],0
s_cbranch_scc1 L_q_spin
L_q_ok:
ta 158,235,49664
ta 160,235,32768
ta 162,235,33280
ta 172,236,32768
ta 174,236,33280
ta 176,236,49152
ta 178,236,49664
mvaa 60,76,32
buffer_load_dword v242,v247,s[8:11],s91 offen offset:0 sc1
ta 180,237,49152
ta 182,237,49664
ta 184,237,32768
ta 186,237,33280
ta 196,234,36864
mvaa 68,76,36
s_mov_b32 m0,s95
ta 198,234,37376
ta 200,234,53248
ta 202,234,53760
ta 204,235,53248
ta 206,235,53760
ta 208,235,36864
ta 210,235,37376
mvaa 64,72,36
s_lshl_b32 s35,s90,2
ta 220,236,36864
ta 222,236,37376
ta 224,236,53248
mvaa 32,96,36
s_lshl_b32 s90,s22,2
ta 226,236,53760
ta 228,237,53248
mvaa 36,100,36
s_add_u32 s90,s90,s89
ta 230,237,53760
ta 232,237,36864
mvaa 28,100,32
ds 249,8,42,0
ta 234,237,37376
ta 240,235,40960
mvaa 24,96,32
s_add_u32 s35,s35,83886080
ta 242,235,41472
ta 48,239,45056
mvaa 16,96,28
ta 50,239,45568
ta 60,239,61440
mvaa 20,100,28
ta 62,239,61952
ta 56,238,61440
mvaa 12,100,24
ta 58,238,61952
ta 68,238,45056
mvaa 8,96,24
ds 249,8,42,1024
ta 70,238,45568
ta 76,237,24576
mvaa 76,24,104
ta 78,237,25088
ta 72,237,40960
mvaa 96,24,108
ta 74,237,41472
ta 100,237,57344
mvaa 100,28,108
ta 102,237,57856
ta 96,236,57344
mvaa 80,28,104
ta 98,236,57856
ta 24,236,40960
mvaa 84,32,104
ds 249,8,42,2048
ta 26,236,41472
mvaa 104,32,108
ta 28,236,24576
ta 30,236,25088
mvaa 108,36,108
v_mov_b32_e32 v248,1024
v_lshl_add_u32 v238,v244,3,s4
mvaa 88,36,104
ta 32,234,24576
ta 34,234,25088
s_waitcnt lgkmcnt(15)
mvaa 0,144,20
ta 108,234,40960
ta 110,234,41472
mvaa 0,148,0
ds 249,8,42,3072
ta 36,234,57344
ta 38,234,57856
ta 104,235,57344
ta 106,235,57856
mvaa 0,152,4
v_min_u32_e32 v238,L_att_end-L_att_pc-4,v238
v_mov_b32_e32 v234,0xc1a00000
mvaa 4,156,4
v_mov_b32_e32 v235,0xc1a00000
v_cndmask_b32_e64 v250,0,v252,s[40:41]
mvaa 4,160,0
s_mov_b32 s40,0xffc0fffc
s_mov_b32 s41,0xc000fc00
v_mul_f32_e32 v0,0x3e0293ee,v0
mvaa 4,164,20
v_mul_f32_e32 v1,0x3e0293ee,v1
v_mul_f32_e32 v2,0x3e0293ee,v2
mvaa 92,164,8
buffer_load_dword v240,v247,s[8:11],s90 offen offset:0 sc1
mvaa 92,160,12
v_mul_f32_e32 v3,0x3e0293ee,v3
cv 0,0,1
mvaa 92,156,16
v_mul_f32_e32 v4,0x3e0293ee,v4
cv 1,2,3
mvaa 72,152,16
v_mul_f32_e32 v5,0x3e0293ee,v5
s_waitcnt lgkmcnt(0)
s_barrier
mvaa 72,148,12
s_mov_b32 m0,s70
v_mul_f32_e32 v6,0x3e0293ee,v6
v_mul_f32_e32 v7,0x3e0293ee,v7
mvaa 72,144,8
buffer_load_dword v241,v247,s[8:11],s90 offen offset:256 sc1
mvaa 144,144,80
cv 2,4,5
cv 3,6,7
mvaa 144,148,84
v_mul_f32_e32 v92,0x3e0293ee,v92
v_mul_f32_e32 v93,0x3e0293ee,v93
mvaa 144,152,88
v_mul_f32_e32 v94,0x3e0293ee,v94
v_mul_f32_e32 v72,0x3e0293ee,v72
mvaa 148,156,88
v_mul_f32_e32 v95,0x3e0293ee,v95
v_mul_f32_e32 v73,0x3e0293ee,v73
mvaa 148,160,84
ds 249,8,43,0
mvaa 148,164,80
v_mul_f32_e32 v74,0x3e0293ee,v74
v_mul_f32_e32 v75,0x3e0293ee,v75
mvaa 156,164,92
cv 72,72,73
cv 73,74,75
mvaa 156,160,44
cv 74,92,93
cv 75,94,95
mvaa 156,156,40
v_mul_f32_e32 v144,0x3e0293ee,v144
v_mul_f32_e32 v145,0x3e0293ee,v145
mvaa 152,152,40
ds 249,8,43,1024
mvaa 152,148,44
v_mul_f32_e32 v146,0x3e0293ee,v146
v_mul_f32_e32 v147,0x3e0293ee,v147
mvaa 152,144,92
cv 144,144,145
v_mul_f32_e32 v148,0x3e0293ee,v148
mvaa 160,168,92
cv 145,146,147
v_mul_f32_e32 v149,0x3e0293ee,v149
mvaa 160,172,44
v_mul_f32_e32 v150,0x3e0293ee,v150
v_mul_f32_e32 v151,0x3e0293ee,v151
mvaa 160,176,40
L_vs15:
s_cbranch_execz L_vr0s
ds 249,8,43,2048
mvaa 164,180,40
cv 146,148,149
cv 147,150,151
mvaa 164,184,44
v_mul_f32_e32 v156,0x3e0293ee,v156
v_mul_f32_e32 v157,0x3e0293ee,v157
mvaa 164,188,92
v_mul_f32_e32 v152,0x3e0293ee,v152
v_mul_f32_e32 v158,0x3e0293ee,v158
mvaa 172,188,80
v_mul_f32_e32 v159,0x3e0293ee,v159
v_mul_f32_e32 v153,0x3e0293ee,v153
mvaa 172,184,84
ds 249,8,43,3072
s_mov_b32 m0,s72
mvaa 172,180,88
v_mul_f32_e32 v154,0x3e0293ee,v154
v_mul_f32_e32 v155,0x3e0293ee,v155
mvaa 168,176,88
cv 152,152,153
cv 153,154,155
mvaa 168,172,84
ds_write_b128 v251,v[0:3] offset:0
mvaa 168,168,80
cv 154,156,157
cv 155,158,159
mvaa 176,168,8
ds 255,8,42,0
mvaa 176,172,12
v_mul_f32_e32 v160,0x3e0293ee,v160
v_mul_f32_e32 v161,0x3e0293ee,v161
mvaa 176,176,16
v_mul_f32_e32 v162,0x3e0293ee,v162
v_mul_f32_e32 v163,0x3e0293ee,v163
mvaa 180,180,16
cv 160,160,161
v_mul_f32_e32 v164,0x3e0293ee,v164
mvaa 180,184,12
v_mul_f32_e32 v165,0x3e0293ee,v165
cv 161,162,163
mvaa 180,188,8
ds 255,8,42,1024
ds_write_b128 v251,v[72:75] offset:4096
mvaa 188,188,20
v_mul_f32_e32 v166,0x3e0293ee,v166
v_mul_f32_e32 v167,0x3e0293ee,v167
mvaa 188,184,0
cv 162,164,165
cv 163,166,167
mvaa 188,180,4
v_mul_f32_e32 v172,0x3e0293ee,v172
v_mul_f32_e32 v173,0x3e0293ee,v173
mvaa 184,176,4
v_mul_f32_e32 v174,0x3e0293ee,v174
v_mul_f32_e32 v175,0x3e0293ee,v175
mvaa 184,172,0
ds 255,8,42,2048
mvaa 184,168,20
v_mul_f32_e32 v168,0x3e0293ee,v168
v_mul_f32_e32 v169,0x3e0293ee,v169
mvaa 192,192,20
ds_write_b128 v251,v[144:147] offset:8192
mvaa 192,196,0
v_mul_f32_e32 v170,0x3e0293ee,v170
v_mul_f32_e32 v171,0x3e0293ee,v171
mvaa 192,200,4
cv 168,168,169
cv 169,170,171
mvaa 196,204,4
ds 255,8,42,3072
mvaa 196,208,0
cv 170,172,173
cv 171,174,175
mvaa 196,212,20
v_mul_f32_e32 v176,0x3e0293ee,v176
v_mul_f32_e32 v177,0x3e0293ee,v177
mvaa 204,212,8
v_mul_f32_e32 v178,0x3e0293ee,v178
v_mul_f32_e32 v179,0x3e0293ee,v179
mvaa 204,208,12
ds_write_b128 v251,v[152:155] offset:12288
mvaa 204,204,16
buffer_load_dword v243,v247,s[8:11],s91 offen offset:256 sc1
mvaa 200,200,16
cv 176,176,177
cv 177,178,179
mvaa 200,196,12
v_mul_f32_e32 v180,0x3e0293ee,v180
v_mul_f32_e32 v181,0x3e0293ee,v181
mvaa 200,192,8
v_mul_f32_e32 v182,0x3e0293ee,v182
v_mul_f32_e32 v183,0x3e0293ee,v183
mvaa 208,192,80
cv 178,180,181
cv 179,182,183
mvaa 208,196,84
v_mul_f32_e32 v188,0x3e0293ee,v188
v_mul_f32_e32 v189,0x3e0293ee,v189
mvaa 208,200,88
ds_write_b128 v251,v[160:163] offset:13312
mvaa 212,204,88
v_mul_f32_e32 v190,0x3e0293ee,v190
v_mul_f32_e32 v191,0x3e0293ee,v191
mvaa 212,208,84
v_mul_f32_e32 v184,0x3e0293ee,v184
v_mul_f32_e32 v185,0x3e0293ee,v185
mvaa 212,212,80
v_mul_f32_e32 v186,0x3e0293ee,v186
v_mul_f32_e32 v187,0x3e0293ee,v187
mvaa 220,212,92
cv 184,184,185
cv 185,186,187
mvaa 220,208,44
cv 186,188,189
cv 187,190,191
mvaa 220,204,40
v_mul_f32_e32 v192,0x3e0293ee,v192
v_mul_f32_e32 v193,0x3e0293ee,v193
mvaa 216,200,40
ds_write_b128 v251,v[168:171] offset:9216
mvaa 216,196,44
v_mul_f32_e32 v194,0x3e0293ee,v194
v_mul_f32_e32 v195,0x3e0293ee,v195
mvaa 216,192,92
cv 192,192,193
cv 193,194,195
maaa 128,216,92
v_mul_f32_e32 v196,0x3e0293ee,v196
v_mul_f32_e32 v197,0x3e0293ee,v197
maaa 128,220,44
v_mul_f32_e32 v198,0x3e0293ee,v198
v_mul_f32_e32 v199,0x3e0293ee,v199
maaa 128,224,40
cv 194,196,197
cv 195,198,199
maaa 112,228,40
v_mul_f32_e32 v204,0x3e0293ee,v204
v_mul_f32_e32 v205,0x3e0293ee,v205
maaa 112,232,44
ds_write_b128 v251,v[176:179] offset:5120
maaa 112,236,92
v_mul_f32_e32 v206,0x3e0293ee,v206
v_mul_f32_e32 v207,0x3e0293ee,v207
maaa 116,236,80
v_mul_f32_e32 v200,0x3e0293ee,v200
v_mul_f32_e32 v201,0x3e0293ee,v201
maaa 116,232,84
v_mul_f32_e32 v202,0x3e0293ee,v202
v_mul_f32_e32 v203,0x3e0293ee,v203
maaa 116,228,88
cv 200,200,201
cv 201,202,203
maaa 132,224,88
cv 202,204,205
cv 203,206,207
maaa 132,220,84
v_mul_f32_e32 v208,0x3e0293ee,v208
v_mul_f32_e32 v209,0x3e0293ee,v209
maaa 132,216,80
ds_write_b128 v251,v[184:187] offset:1024
maaa 136,216,8
v_mul_f32_e32 v210,0x3e0293ee,v210
v_mul_f32_e32 v211,0x3e0293ee,v211
maaa 136,220,12
cv 208,208,209
cv 209,210,211
maaa 136,224,16
v_mul_f32_e32 v212,0x3e0293ee,v212
v_mul_f32_e32 v213,0x3e0293ee,v213
maaa 120,228,16
s_waitcnt vmcnt(11)
s_barrier
maaa 120,232,12
s_cmp_eq_u32 s12,0
s_cselect_b64 exec,1,0
buffer_atomic_add v248,off,s[8:11],s32 sc1
s_mov_b64 exec,-1
maaa 120,236,8
s_lshr_b32 s32,s18,1
v_mul_f32_e32 v214,0x3e0293ee,v214
v_mul_f32_e32 v215,0x3e0293ee,v215
maaa 124,236,20
s_lshl_b32 s32,s32,7
ds_write_b128 v251,v[192:195] offset:2048
maaa 124,232,0
s_add_u32 s32,s32,83894272
cv 210,212,213
cv 211,214,215
maaa 124,228,4
v_mul_f32_e32 v220,0x3e0293ee,v220
v_mul_f32_e32 v221,0x3e0293ee,v221
maaa 140,224,4
v_mul_f32_e32 v216,0x3e0293ee,v216
v_mul_f32_e32 v217,0x3e0293ee,v217
maaa 140,220,0
v_mul_f32_e32 v222,0x3e0293ee,v222
v_mul_f32_e32 v223,0x3e0293ee,v223
maaa 140,216,20
v_mul_f32_e32 v218,0x3e0293ee,v218
v_mul_f32_e32 v219,0x3e0293ee,v219
mvaa 88,92,64
cv 216,216,217
cv 217,218,219
mvaa 88,44,68
ds_write_b128 v251,v[200:203] offset:6144
mvaa 88,40,56
cv 218,220,221
cv 219,222,223
mvaa 108,40,60
v_accvgpr_read_b32 v0,a128
v_accvgpr_read_b32 v1,a129
mvaa 108,44,48
v_accvgpr_read_b32 v2,a130
v_accvgpr_read_b32 v3,a131
cv 88,88,89
cv 89,90,91
mvaa 108,92,52
buffer_store_dwordx2 v[88:89],v253,s[8:11],s99 offen offset:1024 sc0 sc1
mvaa 36,76,92
v_mul_f32_e32 v0,0x3e0293ee,v0
v_mul_f32_e32 v1,0x3e0293ee,v1
mvaa 36,72,44
v_mul_f32_e32 v2,0x3e0293ee,v2
v_mul_f32_e32 v3,0x3e0293ee,v3
mvaa 36,100,40
ds_write_b128 v251,v[208:211] offset:10240
mvaa 32,96,40
cv 108,108,109
cv 109,110,111
mvaa 32,24,44
buffer_store_dwordx2 v[108:109],v253,s[8:11],s99 offen offset:1536 sc0 sc1
mvaa 32,28,92
cv 0,0,1
cv 1,2,3
mvaa 64,32,92
v_accvgpr_read_b32 v4,a112
v_accvgpr_read_b32 v5,a113
mvaa 64,108,44
v_accvgpr_read_b32 v6,a114
v_mul_f32_e32 v4,0x3e0293ee,v4
mvaa 64,36,40
cv 32,32,33
cv 33,34,35
cv 34,36,37
cv 35,38,39
mvaa 68,104,40
buffer_store_dwordx4 v[32:35],v244,s[8:11],s99 offen sc0 sc1
ds_write_b128 v251,v[216:219] offset:14336
mvaa 68,240,44
v_mul_f32_e32 v5,0x3e0293ee,v5
v_mul_f32_e32 v6,0x3e0293ee,v6
mvaa 68,244,92
v_accvgpr_read_b32 v7,a115
v_mul_f32_e32 v7,0x3e0293ee,v7
mvaa 60,244,80
cv 2,4,5
cv 3,6,7
mvaa 60,240,84
ra 192,224,0
v_accvgpr_read_b32 v92,a116
mvaa 60,104,88
ra 196,224,1024
v_accvgpr_read_b32 v93,a117
mvaa 56,36,88
ra 200,224,2048
v_mul_f32_e32 v92,0x3e0293ee,v92
mvaa 56,108,84
ds_write_b128 v251,v[0:3] offset:15360
mvaa 56,32,80
v_mul_f32_e32 v93,0x3e0293ee,v93
v_accvgpr_read_b32 v94,a118
mvaa 24,28,80
v_accvgpr_read_b32 v95,a119
v_accvgpr_read_b32 v72,a132
mvaa 24,24,84
ra 204,224,3072
v_accvgpr_read_b32 v73,a133
mvaa 24,96,88
v_accvgpr_read_b32 v74,a134
v_mul_f32_e32 v94,0x3e0293ee,v94
v_mul_f32_e32 v95,0x3e0293ee,v95
mvaa 28,100,88
v_mul_f32_e32 v72,0x3e0293ee,v72
v_mul_f32_e32 v73,0x3e0293ee,v73
v_mul_f32_e32 v74,0x3e0293ee,v74
v_accvgpr_read_b32 v75,a135
mvaa 28,72,84
v_mul_f32_e32 v75,0x3e0293ee,v75
cv 72,72,73
cv 73,74,75
cv 74,92,93
mvaa 28,76,80
ra 208,224,4096
cv 75,94,95
mvaa 20,76,8
ds_write_b128 v251,v[72:75] offset:11264
mvaa 20,72,12
v_accvgpr_read_b32 v144,a136
v_accvgpr_read_b32 v145,a137
mvaa 20,100,16
s_mov_b64 exec,3
global_load_dword v239,v238,s[96:97]
s_mov_b64 exec,-1
ra 212,224,5120
ra 248,224,14336
ra 252,224,15360
mvaa 16,96,16
v_accvgpr_read_b32 v146,a138
v_mul_f32_e32 v144,0x3e0293ee,v144
v_mul_f32_e32 v145,0x3e0293ee,v145
v_mul_f32_e32 v146,0x3e0293ee,v146
v_accvgpr_read_b32 v147,a139
v_mul_f32_e32 v147,0x3e0293ee,v147
cv 144,144,145
mvaa 16,24,12
ra 236,224,11264
v_accvgpr_read_b32 v148,a120
v_accvgpr_read_b32 v149,a121
v_accvgpr_read_b32 v150,a122
cv 145,146,147
mvaa 16,28,8
v_mul_f32_e32 v148,0x3e0293ee,v148
v_mul_f32_e32 v149,0x3e0293ee,v149
v_mul_f32_e32 v150,0x3e0293ee,v150
v_accvgpr_read_b32 v151,a123
v_mul_f32_e32 v151,0x3e0293ee,v151
cv 146,148,149
mvaa 48,32,8
ra 228,224,9216
cv 147,150,151
mvaa 48,108,12
ds_write_b128 v251,v[144:147] offset:7168
mvaa 48,36,16
v_accvgpr_read_b32 v156,a124
v_accvgpr_read_b32 v157,a125
mvaa 52,104,16
ra 232,224,10240
ra 216,224,6144
ra 220,224,7168
ra 224,224,8192
v_accvgpr_read_b32 v158,a126
v_accvgpr_read_b32 v159,a127
mvaa 52,240,12
v_accvgpr_read_b32 v152,a140
v_mul_f32_e32 v156,0x3e0293ee,v156
v_mul_f32_e32 v157,0x3e0293ee,v157
v_mul_f32_e32 v158,0x3e0293ee,v158
v_mul_f32_e32 v159,0x3e0293ee,v159
mvaa 52,244,8
v_mul_f32_e32 v152,0x3e0293ee,v152
v_accvgpr_read_b32 v153,a141
v_mul_f32_e32 v153,0x3e0293ee,v153
v_accvgpr_read_b32 v154,a142
v_accvgpr_read_b32 v155,a143
mvaa 44,244,20
v_mul_f32_e32 v154,0x3e0293ee,v154
v_mul_f32_e32 v155,0x3e0293ee,v155
cv 152,152,153
cv 153,154,155
cv 154,156,157
mvaa 44,240,0
ra 244,224,13312
cv 155,158,159
mvaa 44,104,4
ds_write_b128 v251,v[152:155] offset:3072
mvaa 40,36,4
cv 64,64,65
cv 56,56,57
mvaa 40,108,0
cv 57,58,59
cv 58,60,61
mvaa 40,32,20
ra 240,224,12288
cv 59,62,63
mvaa 8,28,20
cv 16,16,17
cv 17,18,19
mvaa 8,24,0
cv 48,48,49
cv 194,44,45
mvaa 8,96,4
cv 195,46,47
cv 192,40,41
mvaa 12,100,4
cv 193,42,43
cv 49,50,51
mvaa 12,72,0
s_cmp_eq_u32 s19,0
s_cselect_b32 s76,32768,0
s_cselect_b32 s77,49152,s86
s_cselect_b32 s78,65536,s46
s_cselect_b32 s79,81920,s86
mvaa 12,76,20
s_add_u32 s86,s95,98304
cv 8,8,9
v_add_u32_e32 v236,s86,v244
mvaa 76,20,64
s_add_u32 s46,s46,16384
v_xor_b32_e32 v237,4096,v236
s_waitcnt lgkmcnt(0)
s_barrier
s_mov_b32 m0,s68
mvaa 76,0,68
ds 249,8,76,0
ra 128,237,32768
ra 132,237,33792
mvaa 76,4,56
s_add_u32 s95,s47,32768
ra 136,237,34816
cv 9,10,11
mvaa 96,4,60
ra 140,237,35840
ra 144,237,49152
mvaa 96,0,48
cv 10,12,13
cv 11,14,15
mvaa 96,20,52
ra 148,237,50176
cv 50,52,53
mvaa 100,8,52
ds 249,8,76,1024
ra 152,237,51200
ra 156,237,52224
mvaa 100,12,48
ra 176,236,16384
cv 51,54,55
mvaa 100,16,60
ra 180,236,17408
cv 18,20,21
mvaa 80,16,56
ra 184,236,18432
cv 19,22,23
mvaa 80,12,68
ra 188,236,19456
cv 24,24,25
mvaa 80,8,64
ds 249,8,76,2048
ra 160,236,0
mvaa 84,80,64
ra 164,236,1024
cv 25,26,27
mvaa 84,84,68
ra 168,236,2048
ra 172,236,3072
mvaa 84,88,56
cv 26,28,29
cv 27,30,31
mvaa 104,88,60
cv 65,66,67
cv 66,68,69
mvaa 104,84,48
ds 249,8,76,3072
mvaa 104,80,52
s_mov_b32 m0,s69
cv 67,70,71
v_cndmask_b32_e64 v252,0,v252,s[40:41]
cvvv 164,8,192,112
cv 70,76,77
cv 71,78,79
cvvv 168,8,48,116
ds 249,8,77,0
cvvv 172,16,48,112
cv 94,96,97
v_mov_b32_e32 v76,v88
cvvv 176,16,56,116
cv 95,98,99
s_waitcnt vmcnt(9) lgkmcnt(0)
s_barrier
cvvv 180,24,56,112
ds 249,8,77,1024
cvvv 184,24,64,116
ra 0,224,32768
cv 96,100,101
cvvv 188,32,64,112
ra 4,224,33792
cv 97,102,103
cvaa 0,192,128,232
buffer_load_dword v17,v247,s[8:11],s35 offen offset:64 sc1
mvaa 0,200,132
ra 8,224,34816
cv 72,80,81
mvaa 0,208,136
ra 12,224,35840
cv 73,82,83
mvaa 0,216,140
ds 249,8,77,2048
cvaa 4,220,140,232
ra 16,224,36864
cv 74,84,85
mvaa 4,212,136
ra 20,224,37888
v_max3_f32 v172,v168,v169,v172
mvaa 4,204,132
ds 249,8,77,3072
mvaa 4,196,128
s_mov_b32 m0,s74
ra 24,224,38912
v_max_f32_e32 v172,v172,v173
cvaa 32,224,128,232
ra 28,224,39936
v_max3_f32 v174,v170,v171,v174
mvaa 32,232,132
buffer_load_dword v19,v247,s[8:11],s35 offen offset:192 sc1
mvaa 32,240,136
ra 32,224,40960
v_max_f32_e32 v174,v174,v175
mvaa 32,248,140
ra 36,224,41984
ra 40,224,43008
cvaa 36,252,140,232
buffer_load_dword v18,v247,s[8:11],s35 offen offset:128 sc1
ra 44,224,44032
mvaa 36,244,136
ra 48,224,45056
v_max_f32_e32 v148,v172,v174
mvaa 36,236,132
ra 56,224,47104
ra 60,224,48128
mvaa 36,228,128
buffer_load_dword v16,v247,s[8:11],s35 offen offset:0 sc1
ra 52,224,46080
cvaa 8,192,144,232
L_vs16:
s_cbranch_execz L_vr0s
v_mov_b32_e32 v149,v148
cv 75,86,87
mvaa 8,200,148
v_permlane16_swap_b32_e32 v148,v149
mvaa 8,208,152
ds 255,8,43,0
mvaa 8,216,156
v_max_f32_e32 v148,v148,v149
v_mov_b32_e32 v149,v148
cvaa 12,220,156,232
cv 98,104,105
v_max3_f32 v180,v176,v177,v180
mvaa 12,212,152
ds 255,8,43,1024
mvaa 12,204,148
v_permlane32_swap_b32_e32 v148,v149
mvaa 12,196,144
v_max_f32_e32 v148,v148,v149
s_waitcnt lgkmcnt(0)
s_barrier
cvaa 40,224,144,232
ds 255,8,43,2048
mvaa 40,232,148
ra 64,225,0
v_mul_f32_e32 v148,0xbe8293ee,v148
mvaa 40,240,152
ra 68,225,1024
v_fmamk_f32 v172,v172,0x3e8293ee,v148
mvaa 40,248,156
ds 255,8,43,3072
cvaa 44,252,156,232
s_mov_b32 m0,s70
ra 72,225,2048
v_fmamk_f32 v174,v174,0x3e8293ee,v148
mvaa 44,244,152
ra 76,225,3072
v_max3_f32 v182,v178,v179,v182
mvaa 44,236,148
ds 249,8,78,0
mvaa 44,228,144
ra 80,225,4096
v_max_f32_e32 v180,v180,v181
cvaa 24,192,176,128
ra 84,225,5120
v_max_f32_e32 v182,v182,v183
mvaa 24,200,180
ds 249,8,78,1024
mvaa 24,208,184
ra 88,225,6144
v_max_f32_e32 v152,v180,v182
mvaa 24,216,188
ra 92,225,7168
v_mov_b32_e32 v153,v152
cvaa 28,220,188,132
ds 249,8,78,2048
mvaa 28,212,184
ra 96,225,8192
v_max3_f32 v188,v184,v185,v188
mvaa 28,204,180
ra 100,225,9216
ra 104,225,10240
mvaa 28,196,176
ds 249,8,78,3072
ra 108,225,11264
cvaa 56,224,176,136
s_mov_b32 m0,s71
ra 112,225,12288
v_max3_f32 v190,v186,v187,v190
mvaa 56,232,180
ra 116,225,13312
ra 120,225,14336
mvaa 56,240,184
ds 249,8,79,0
ra 124,225,15360
mvaa 56,248,188
ex 172
cvaa 60,252,188,140
ex 174
mvaa 60,244,184
ds 249,8,79,1024
mvaa 60,236,180
v_permlane16_swap_b32_e32 v152,v153
mvaa 60,228,176
cv 172,172,172
cv 174,174,174
cvaa 80,0,160,128
ds 249,8,79,2048
mvaa 80,8,164
v_and_b32_e32 v168,v226,v172
v_max_f32_e32 v152,v152,v153
mvaa 80,16,168
v_and_b32_e32 v169,v227,v174
v_and_b32_e32 v170,v250,v172
v_and_b32_e32 v171,v252,v174
mvaa 80,24,172
ds 249,8,79,3072
v_mov_b32_e32 v153,v152
cvaa 84,28,172,132
v_max_f32_e32 v188,v188,v189
v_permlane32_swap_b32_e32 v152,v153
v_max_f32_e32 v152,v152,v153
mvaa 84,20,168
v_mul_f32_e32 v152,0xbe8293ee,v152
v_fmamk_f32 v180,v180,0x3e8293ee,v152
mvaa 84,12,164
v_fmamk_f32 v182,v182,0x3e8293ee,v152
v_max_f32_e32 v190,v190,v191
mvaa 84,4,160
ex 180
cvaa 112,32,160,136
ex 182
v_max_f32_e32 v156,v188,v190
mvaa 112,40,164
v_mov_b32_e32 v157,v156
cv 180,180,180
mvaa 112,48,168
v_permlane16_swap_b32_e32 v156,v157
mvaa 112,56,172
cv 182,182,182
v_and_b32_e32 v176,v226,v180
v_max_f32_e32 v156,v156,v157
s_waitcnt vmcnt(15) lgkmcnt(0)
s_barrier
s_mov_b32 m0,s72
cvaa 116,60,172,140
ds 255,8,76,0
v_and_b32_e32 v177,v227,v182
)"
R"(mvaa 116,52,168
v_and_b32_e32 v178,v250,v180
v_and_b32_e32 v179,v252,v182
v_mov_b32_e32 v157,v156
mvaa 116,44,164
cv 99,106,107
v_permlane32_swap_b32_e32 v156,v157
v_max_f32_e32 v156,v156,v157
mvaa 116,36,160
ds 255,8,76,1024
v_mul_f32_e32 v156,0xbe8293ee,v156
zvvv 148,228,168
v_fmamk_f32 v188,v188,0x3e8293ee,v156
ex 188
zvvv 200,70,168
v_fmamk_f32 v190,v190,0x3e8293ee,v156
ex 190
zvvv 204,94,168
ds 255,8,76,2048
zvvv 152,228,176
cv 188,188,188
cv 190,190,190
v_and_b32_e32 v184,v226,v188
zvvv 208,72,176
v_and_b32_e32 v185,v227,v190
v_and_b32_e32 v186,v250,v188
v_and_b32_e32 v187,v252,v190
zvvv 212,96,176
ds 255,8,76,3072
s_mov_b32 m0,s73
v_mov_b32_e32 v77,v89
zvvv 156,228,184
v_mov_b32_e32 v100,v108
v_mov_b32_e32 v101,v109
zvvv 216,74,184
ex 0
zvvv 220,98,184
ds 255,8,77,0
cvaa 64,0,128,232
ex 1
mvaa 64,8,132
ex 2
mvaa 64,16,136
ds 255,8,77,1024
mvaa 64,24,140
ds 255,8,77,2048
v_mov_b32_e32 v170,v70
v_mov_b32_e32 v171,v71
cvaa 68,28,140,232
ex 3
mvaa 68,20,136
v_mov_b32_e32 v160,v120
v_mov_b32_e32 v161,v121
mvaa 68,12,132
ds 255,8,77,3072
ex 4
mvaa 68,4,128
ex 5
cvaa 96,32,128,232
ex 6
mvaa 96,40,132
v_sub_f32_e32 v17,1.0,v17
v_rcp_f32_e32 v148,v148
mvaa 96,48,136
v_mul_f32_e32 v148,v148,v17
s_waitcnt vmcnt(21)
v_sub_f32_e32 v18,1.0,v18
v_sub_f32_e32 v19,1.0,v19
mvaa 96,56,140
v_mul_f32_e32 v200,v148,v200
v_mul_f32_e32 v201,v148,v201
v_mul_f32_e32 v202,v148,v202
cvaa 100,60,140,232
v_mul_f32_e32 v203,v148,v203
cv 200,200,201
mvaa 100,52,136
v_mul_f32_e32 v204,v148,v204
v_mul_f32_e32 v205,v148,v205
cv 201,202,203
mvaa 100,44,132
v_mul_f32_e32 v206,v148,v206
v_mul_f32_e32 v207,v148,v207
cv 202,204,205
mvaa 100,36,128
buffer_load_dwordx4 v[176:179],v244,s[8:11],s98 offen sc1
cv 203,206,207
cvaa 72,0,144,232
v_rcp_f32_e32 v152,v152
mvaa 72,8,148
v_mul_f32_e32 v152,v152,v18
v_mul_f32_e32 v208,v152,v208
v_mul_f32_e32 v209,v152,v209
mvaa 72,16,152
buffer_load_dwordx2 v[168:169],v253,s[8:11],s98 offen offset:1024 sc1
v_mul_f32_e32 v210,v152,v210
mvaa 72,24,156
v_mul_f32_e32 v211,v152,v211
cv 208,208,209
cv 209,210,211
cvaa 76,28,156,232
v_mul_f32_e32 v212,v152,v212
v_mul_f32_e32 v213,v152,v213
mvaa 76,20,152
buffer_load_dwordx2 v[172:173],v253,s[8:11],s98 offen offset:1536 sc1
v_mul_f32_e32 v214,v152,v214
mvaa 76,12,148
v_mul_f32_e32 v215,v152,v215
v_rcp_f32_e32 v156,v156
mvaa 76,4,144
cv 210,212,213
cv 211,214,215
v_mul_f32_e32 v156,v156,v19
cvaa 104,32,144,232
buffer_store_dwordx4 v[200:203],v244,s[8:11],s34 offen sc0 sc1
mvaa 104,40,148
v_mul_f32_e32 v216,v156,v216
v_mul_f32_e32 v217,v156,v217
v_mul_f32_e32 v218,v156,v218
mvaa 104,48,152
v_mul_f32_e32 v219,v156,v219
cv 216,216,217
cv 217,218,219
mvaa 104,56,156
buffer_store_dwordx4 v[208:211],v244,s[8:11],s50 offen sc0 sc1
v_mul_f32_e32 v220,v156,v220
cvaa 108,60,156,232
v_mul_f32_e32 v221,v156,v221
v_mul_f32_e32 v222,v156,v222
mvaa 108,52,152
v_mul_f32_e32 v223,v156,v223
cv 218,220,221
cv 219,222,223
mvaa 108,44,148
buffer_store_dwordx4 v[216:219],v244,s[8:11],s51 offen sc0 sc1
v_mov_b32_e32 v162,v122
mvaa 108,36,144
v_mov_b32_e32 v163,v123
ex 7
cvaa 88,0,176,232
ex 32
mvaa 88,8,180
ex 33
v_bfe_i32 v248,v244,4,1
mvaa 88,16,184
v_mov_b32_e32 v175,v95
ex 34
mvaa 88,24,188
s_cmp_ge_i32 s47,s46
s_cselect_b32 s76,s95,s46
s_cselect_b32 s77,s95,268435456
v_mov_b32_e32 v174,v94
ex 35
cvaa 92,28,188,232
s_add_u32 s77,s77,16384
ex 36
mvaa 92,20,184
ex 37
v_and_b32_e32 v248,2863311530,v248
mvaa 92,12,180
cv 0,0,1
ex 38
mvaa 92,4,176
cv 1,2,3
ex 39
cvaa 120,32,176,232
buffer_load_dword v247,v246,s[24:27],s49 offen
mvaa 120,40,180
s_add_u32 s49,s49,32768
cv 2,4,5
ex 8
mvaa 120,48,184
cv 3,6,7
ex 9
mvaa 120,56,188
buffer_load_dword v247,v245,s[36:39],s85 offen
cv 4,32,33
cvaa 124,60,188,232
s_add_u32 s85,s85,131072
ex 10
mvaa 124,52,184
cv 5,34,35
ex 11
mvaa 124,44,180
buffer_load_dword v247,v246,s[24:27],s49 offen
cv 6,36,37
mvaa 124,36,176
s_cmp_lg_u32 s19,0
s_cbranch_scc1 L_j0
s_nop 1
s_lshl_b32 s86,s12,12
s_add_u32 s86,s86,131072
v_add_u32_e32 v236,s86,v244
ra 160,236,0
ra 164,236,1024
ra 168,236,2048
ra 172,236,3072
ra 176,236,16384
ra 180,236,17408
ra 184,236,18432
ra 188,236,19456
v_add_u32_e32 v224,s94,v224
s_waitcnt lgkmcnt(0)
L_j0:
cv 7,38,39
ex 12
zaav 0,64,0
s_add_u32 s49,s49,32768
ex 13
maav 0,96,4
ex 14
v_or_b32_e32 v248,1145324612,v248
zaav 4,68,0
ex 15
v_mov_b64_e32 v[144:145],0
maav 4,100,4
cv 8,8,9
ex 40
zaav 8,72,0
ex 41
maav 8,104,4
cv 9,10,11
ex 42
zaav 12,76,0
cv 10,12,13
ex 43
maav 12,108,4
cv 11,14,15
ex 44
v_smfmac_f32_16x16x64_bf16 v[144:147],v[228:231],v[0:7],v248
ex 45
zaav 16,80,0
cv 12,40,41
ex 46
maav 16,112,4
s_waitcnt vmcnt(8)
v_max3_u32 v180,v176,v177,v178
cv 13,42,43
zaav 20,84,0
s_waitcnt vmcnt(7)
v_max3_u32 v180,v180,v179,v168
s_waitcnt vmcnt(6)
v_max3_u32 v180,v180,v169,v172
v_max_u32_e32 v180,v180,v173
v_cmp_eq_u32_e64 s[40:41],-1,v180
maav 20,116,4
s_cmp_lg_u64 s[40:41],0
s_cbranch_scc1 L_bx
L_bxr:
cv 14,44,45
ex 47
mvvv 160,176,192
zaav 24,88,0
cv 15,46,47
v_sub_f32_e32 v16,1.0,v16
maav 24,120,4
ex 24
zaav 28,92,0
ex 25
maav 28,124,4
v_max3_f32 v164,v160,v161,v164
v_max_f32_e32 v164,v164,v165
v_max3_f32 v166,v162,v163,v166
v_max_f32_e32 v166,v166,v167
v_max_f32_e32 v180,v164,v166
v_mov_b32_e32 v181,v180
zaav 32,64,8
v_mov_b64_e32 v[148:149],0
v_permlane16_swap_b32_e32 v180,v181
v_max_f32_e32 v180,v180,v181
v_mov_b32_e32 v181,v180
maav 32,96,12
ex 26
v_permlane32_swap_b32_e32 v180,v181
v_max_f32_e32 v180,v180,v181
v_mul_f32_e32 v180,0xbe8293ee,v180
v_fmamk_f32 v164,v164,0x3e8293ee,v180
zaav 36,68,8
ex 164
v_fmamk_f32 v166,v166,0x3e8293ee,v180
ex 166
cv 164,164,164
cv 166,166,166
maav 36,100,12
v_and_b32_e32 v160,v226,v164
v_and_b32_e32 v161,v227,v166
v_and_b32_e32 v162,v250,v164
v_and_b32_e32 v163,v252,v166
zaav 40,72,8
ex 27
zvvv 180,228,160
maav 40,104,12
ex 28
zvvv 184,168,160
zaav 44,76,8
ex 29
zvvv 188,172,160
maav 44,108,12
ex 30
v_rcp_f32_e32 v180,v180
v_smfmac_f32_16x16x64_bf16 v[148:151],v[228:231],v[8:15],v248
v_mul_f32_e32 v180,v180,v16
v_mul_f32_e32 v184,v180,v184
v_mul_f32_e32 v185,v180,v185
v_mul_f32_e32 v186,v180,v186
v_mul_f32_e32 v187,v180,v187
v_mul_f32_e32 v188,v180,v188
zaav 48,80,8
v_mul_f32_e32 v189,v180,v189
v_mul_f32_e32 v190,v180,v190
v_mul_f32_e32 v191,v180,v191
cv 184,184,185
cv 185,186,187
cv 186,188,189
cv 187,190,191
maav 48,112,12
buffer_store_dwordx4 v[184:187],v244,s[8:11],s33 offen sc0 sc1
ex 31
cv 24,24,25
cv 25,26,27
cv 26,28,29
zaav 52,84,8
cv 27,30,31
ex 56
ex 57
ex 58
maav 52,116,12
ex 59
ex 60
ex 61
zaav 56,88,8
ra 192,224,0
ra 196,224,1024
ex 62
maav 56,120,12
ra 200,224,2048
cv 28,56,57
ex 63
zaav 60,92,8
ra 204,224,3072
ra 208,224,4096
cv 29,58,59
cv 30,60,61
maav 60,124,12
ra 212,224,5120
cv 31,62,63
ex 64
zvav 192,64,24
ra 216,224,6144
ra 220,224,7168
ex 65
mvav 192,96,28
ra 224,224,8192
v_mov_b32_e32 v251,-1
ex 66
zvav 196,68,24
ra 228,224,9216
ra 232,224,10240
ex 67
mvav 196,100,28
buffer_load_dword v237,off,s[8:11],s32 sc1
ra 236,224,11264
v_mov_b64_e32 v[152:153],0
zvav 200,72,24
ra 240,224,12288
ra 244,224,13312
v_mov_b64_e32 v[156:157],0
cv 64,64,65
mvav 200,104,28
ra 248,224,14336
ra 252,224,15360
ex 68
zvav 204,76,24
ex 69
ex 70
mvav 204,108,28
cv 65,66,67
ex 71
cv 66,68,69
v_smfmac_f32_16x16x64_bf16 v[156:159],v[228:231],v[24:31],v248
ex 96
ex 97
zvav 208,80,24
ex 98
ex 99
mvav 208,112,28
cv 67,70,71
cv 68,96,97
ex 100
zvav 212,84,24
ex 101
ex 102
mvav 212,116,28
cv 69,98,99
ex 103
cv 70,100,101
zvav 216,88,24
ex 72
ex 73
mvav 216,120,28
ex 74
ex 75
zvav 220,92,24
ex 76
ex 77
mvav 220,124,28
v_mov_b32_e32 v250,-1
v_mov_b32_e32 v252,-1
v_mov_b32_e32 v253,-1
cv 71,102,103
s_mov_b64 exec,0
s_bitcmp1_b32 170,s20
s_cbranch_scc1 L_vbO
s_cmp_eq_u32 s12,0
s_cbranch_scc1 L_vs18
s_cmp_eq_u32 s12,1
s_cbranch_scc1 L_vs19
s_cmp_eq_u32 s12,2
s_cbranch_scc1 L_vs20
s_branch L_vs21
L_vbO:
s_cmp_eq_u32 s12,0
s_cbranch_scc1 L_vs25
s_cmp_eq_u32 s12,1
s_cbranch_scc1 L_vs26
s_cmp_eq_u32 s12,2
s_cbranch_scc1 L_vs27
s_branch L_vs28
L_vr1:
s_mov_b64 exec,-1
s_waitcnt lgkmcnt(0)
s_barrier
cvaa 0,192,128,232
mvaa 0,200,132
s_add_u32 s81,s81,1
ex 78
mvaa 0,208,136
s_add_u32 s44,s44,1024
ex 79
mvaa 0,216,140
cv 72,72,73
cv 73,74,75
cvaa 4,220,140,232
s_mov_b32 m0,s68
ex 104
mvaa 4,212,136
ds 249,8,76,0
mvaa 4,204,132
ex 105
mvaa 4,196,128
ex 106
cvaa 32,224,128,232
ra 64,225,32768
cv 74,76,77
mvaa 32,232,132
ex 107
mvaa 32,240,136
ra 68,225,33792
cv 75,78,79
mvaa 32,248,140
ex 108
cvaa 36,252,140,232
ds 249,8,76,1024
mvaa 36,244,136
ex 109
mvaa 36,236,132
ra 72,225,34816
cv 76,104,105
mvaa 36,228,128
ex 110
cvaa 8,192,144,232
ra 76,225,35840
cv 77,106,107
mvaa 8,200,148
ex 111
mvaa 8,208,152
ex 80
mvaa 8,216,156
ds 249,8,76,2048
cvaa 12,220,156,232
ra 80,225,36864
cv 78,108,109
mvaa 12,212,152
ex 81
mvaa 12,204,148
ra 84,225,37888
cv 79,110,111
mvaa 12,196,144
ex 82
cvaa 40,224,144,232
ex 83
mvaa 40,232,148
ra 88,225,38912
cv 80,80,81
mvaa 40,240,152
ds 249,8,76,3072
mvaa 40,248,156
ex 84
cvaa 44,252,156,232
ra 92,225,39936
cv 81,82,83
mvaa 44,244,152
ex 85
mvaa 44,236,148
ex 86
mvaa 44,228,144
ra 96,225,40960
cv 82,84,85
cvaa 16,192,160,232
s_mov_b32 m0,s69
ex 87
mvaa 16,200,164
ra 100,225,41984
cv 83,86,87
mvaa 16,208,168
L_vs17:
s_cbranch_execz L_vr1
ds 249,8,77,0
mvaa 16,216,172
ex 112
cvaa 20,220,172,232
ex 113
mvaa 20,212,168
ra 104,225,43008
cv 84,112,113
mvaa 20,204,164
ex 114
mvaa 20,196,160
ra 108,225,44032
cvaa 48,224,160,232
ex 115
mvaa 48,232,164
ds 249,8,77,1024
mvaa 48,240,168
ex 116
mvaa 48,248,172
ra 112,225,45056
cv 85,114,115
cvaa 52,252,172,232
ex 117
mvaa 52,244,168
ra 116,225,46080
cv 86,116,117
mvaa 52,236,164
ex 118
mvaa 52,228,160
ex 119
cvaa 24,192,176,232
ds 249,8,77,2048
mvaa 24,200,180
ra 120,225,47104
cv 87,118,119
mvaa 24,208,184
ex 88
mvaa 24,216,188
ra 124,225,48128
cvaa 28,220,188,232
mvaa 28,212,184
ex 89
mvaa 28,204,180
ex 90
mvaa 28,196,176
ds 249,8,77,3072
cvaa 56,224,176,232
ex 91
mvaa 56,232,180
ex 92
mvaa 56,240,184
ex 93
mvaa 56,248,188
ex 94
cvaa 60,252,188,232
mvaa 60,244,184
ex 95
mvaa 60,236,180
cv 88,88,89
cv 89,90,91
mvaa 60,228,176
s_waitcnt vmcnt(8) lgkmcnt(0)
s_barrier
maav 0,64,64
ex 120
maav 0,96,68
s_mov_b32 m0,s74
ex 121
maav 4,68,64
ds 255,8,78,0
maav 4,100,68
ex 122
maav 8,72,64
ex 123
maav 8,104,68
cv 90,92,93
cv 91,94,95
maav 12,76,64
ex 124
maav 12,108,68
ex 125
v_smfmac_f32_16x16x64_bf16 v[144:147],v[228:231],v[64:71],v248
cv 92,120,121
maav 16,80,64
ex 126
maav 16,112,68
buffer_load_dword v247,v245,s[36:39],s85 offen
maav 20,84,64
ex 127
maav 20,116,68
cv 93,122,123
cv 94,124,125
maav 24,88,64
cv 95,126,127
maav 24,120,68
ra 192,224,32768
maav 28,92,64
ds 255,8,78,1024
maav 28,124,68
s_lshl_b32 s86,s19,3
s_lshr_b32 s87,s18,2
s_add_u32 s86,s86,s87
maav 32,64,72
ra 196,224,33792
maav 32,96,76
s_add_u32 s85,s85,131072
maav 36,68,72
ra 200,224,34816
maav 36,100,76
s_lshl_b32 s86,s86,7
s_add_u32 s86,s86,83898368
v_mov_b32_e32 v238,1
maav 40,72,72
ds 255,8,78,2048
maav 40,104,76
s_cmp_eq_u32 s12,0
s_cselect_b64 exec,1,0
buffer_atomic_add v238,off,s[8:11],s86 sc1
s_mov_b64 exec,-1
maav 44,76,72
ra 204,224,35840
maav 44,108,76
v_cmp_ne_u32_e32 vcc,16912,v237
v_smfmac_f32_16x16x64_bf16 v[148:151],v[228:231],v[72:79],v248
ra 208,224,36864
maav 48,80,72
s_cbranch_vccnz L_spl
L_sprl:
maav 48,112,76
maav 52,84,72
ds 255,8,78,3072
maav 52,116,76
ra 212,224,37888
maav 56,88,72
ex 0
maav 56,120,76
ra 216,224,38912
maav 60,92,72
ex 1
maav 60,124,76
s_mov_b32 m0,s75
ex 2
zvav 160,64,80
ra 220,224,39936
cv 0,0,1
mvav 160,96,84
ds 255,8,79,0
zvav 164,68,80
ex 3
mvav 164,100,84
ra 224,224,40960
cv 1,2,3
zvav 168,72,80
ex 4
mvav 168,104,84
ra 228,224,41984
zvav 172,76,80
ds 255,8,79,1024
mvav 172,108,84
s_add_u32 s46,s46,16384
ex 5
v_smfmac_f32_16x16x64_bf16 v[152:155],v[228:231],v[80:87],v248
ra 232,224,43008
zvav 176,80,80
s_add_u32 s47,s47,32768
ex 6
mvav 176,112,84
s_add_u32 s95,s47,32768
ex 7
zvav 180,84,80
ra 236,224,44032
cv 2,4,5
mvav 180,116,84
ex 32
zvav 184,88,80
ds 255,8,79,2048
mvav 184,120,84
ex 33
zvav 188,92,80
ra 240,224,45056
cv 3,6,7
mvav 188,124,84
ex 34
mvav 192,64,88
ra 244,224,46080
cv 4,32,33
mvav 192,96,92
ex 35
mvav 196,68,88
ex 36
mvav 196,100,92
ds 255,8,79,3072
mvav 200,72,88
ra 248,224,47104
cv 5,34,35
mvav 200,104,92
s_cmp_ge_i32 s47,s46
ex 37
mvav 204,76,88
s_cselect_b32 s78,s95,s46
ex 38
mvav 204,108,92
s_cselect_b32 s79,s95,268435456
ex 39
v_smfmac_f32_16x16x64_bf16 v[156:159],v[228:231],v[88:95],v248
ra 252,224,48128
mvav 208,80,88
s_add_u32 s79,s79,16384
ex 8
mvav 208,112,92
ex 9
mvav 212,84,88
ex 10
mvav 212,116,92
ex 11
mvav 216,88,88
cv 6,36,37
cv 7,38,39
mvav 216,120,92
ex 12
mvav 220,92,88
ex 13
mvav 220,124,92
s_waitcnt vmcnt(16) lgkmcnt(0)
s_barrier
cvaa 64,192,128,232
s_bitcmp0_b32 s82,s81
s_cbranch_scc1 L_loop
s_bitcmp1_b32 s15,s81
s_cbranch_scc1 L_short
.p2align 6
L_loop:
mvaa 64,200,132
s_add_u32 s81,s81,1
ex 14
mvaa 64,208,136
s_add_u32 s44,s44,1024
ex 15
mvaa 64,216,140
cv 8,8,9
cv 9,10,11
cvaa 68,220,140,232
s_mov_b32 m0,s70
ex 40
mvaa 68,212,136
ds 249,8,78,0
mvaa 68,204,132
ex 41
mvaa 68,196,128
ex 42
cvaa 96,224,128,232
ra 64,225,0
cv 10,12,13
mvaa 96,232,132
ex 43
mvaa 96,240,136
ra 68,225,1024
cv 11,14,15
mvaa 96,248,140
ex 44
cvaa 100,252,140,232
ds 249,8,78,1024
mvaa 100,244,136
ex 45
mvaa 100,236,132
ra 72,225,2048
cv 12,40,41
mvaa 100,228,128
ex 46
cvaa 72,192,144,232
ra 76,225,3072
cv 13,42,43
mvaa 72,200,148
ex 47
mvaa 72,208,152
ex 16
mvaa 72,216,156
ds 249,8,78,2048
cvaa 76,220,156,232
ra 80,225,4096
cv 14,44,45
mvaa 76,212,152
ex 17
mvaa 76,204,148
ra 84,225,5120
cv 15,46,47
mvaa 76,196,144
ex 18
cvaa 104,224,144,232
ex 19
mvaa 104,232,148
ra 88,225,6144
cv 16,16,17
mvaa 104,240,152
ds 249,8,78,3072
mvaa 104,248,156
ex 20
cvaa 108,252,156,232
ra 92,225,7168
cv 17,18,19
mvaa 108,244,152
ex 21
mvaa 108,236,148
ex 22
mvaa 108,228,144
ra 96,225,8192
cv 18,20,21
cvaa 80,192,160,232
s_mov_b32 m0,s71
ex 23
mvaa 80,200,164
ra 100,225,9216
cv 19,22,23
mvaa 80,208,168
ds 249,8,79,0
mvaa 80,216,172
ex 48
cvaa 84,220,172,232
ex 49
mvaa 84,212,168
ra 104,225,10240
cv 20,48,49
mvaa 84,204,164
ex 50
mvaa 84,196,160
ra 108,225,11264
cvaa 112,224,160,232
ex 51
mvaa 112,232,164
ds 249,8,79,1024
mvaa 112,240,168
ex 52
mvaa 112,248,172
ra 112,225,12288
cv 21,50,51
cvaa 116,252,172,232
ex 53
mvaa 116,244,168
ra 116,225,13312
cv 22,52,53
mvaa 116,236,164
ex 54
mvaa 116,228,160
ex 55
cvaa 88,192,176,232
ds 249,8,79,2048
mvaa 88,200,180
ra 120,225,14336
cv 23,54,55
mvaa 88,208,184
ex 24
mvaa 88,216,188
ra 124,225,15360
cvaa 92,220,188,232
buffer_store_dwordx4 v[250:253],v244,s[8:11],s44 offen offset:1024 sc0 sc1
mvaa 92,212,184
ex 25
mvaa 92,204,180
ex 26
mvaa 92,196,176
ds 249,8,79,3072
cvaa 120,224,176,232
ex 27
mvaa 120,232,180
ex 28
mvaa 120,240,184
ex 29
mvaa 120,248,188
ex 30
cvaa 124,252,188,232
buffer_store_dwordx4 v[250:253],v244,s[8:11],s44 offen sc0 sc1
mvaa 124,244,184
ex 31
mvaa 124,236,180
cv 24,24,25
cv 25,26,27
mvaa 124,228,176
s_waitcnt vmcnt(19) lgkmcnt(0)
s_barrier
maav 0,64,0
ex 56
maav 0,96,4
s_mov_b32 m0,s72
ex 57
maav 4,68,0
ds 255,8,76,0
maav 4,100,4
ex 58
maav 8,72,0
ex 59
maav 8,104,4
cv 26,28,29
cv 27,30,31
maav 12,76,0
s_cmp_lg_u32 s80,s81
ex 60
maav 12,108,4
ex 61
v_smfmac_f32_16x16x64_bf16 v[144:147],v[228:231],v[0:7],v248
cv 28,56,57
maav 16,80,0
ex 62
maav 16,112,4
buffer_load_dword v247,v246,s[24:27],s49 offen
maav 20,84,0
ex 63
maav 20,116,4
cv 29,58,59
cv 30,60,61
maav 24,88,0
s_cbranch_scc1 L_evb_r
v_add_u32_e32 v224,s94,v224
s_lshl_b32 s86,s12,12
s_add_u32 s86,s86,131072
v_add_u32_e32 v236,s86,v244
ra 160,236,0
ra 164,236,1024
ra 168,236,2048
ra 172,236,3072
ra 176,236,16384
ra 180,236,17408
ra 184,236,18432
ra 188,236,19456
L_evb_r:
cv 31,62,63
maav 24,120,4
ra 192,224,0
maav 28,92,0
ds 255,8,76,1024
maav 28,124,4
maav 32,64,8
ra 196,224,1024
maav 32,96,12
s_add_u32 s49,s49,32768
maav 36,68,8
ra 200,224,2048
maav 36,100,12
maav 40,72,8
ds 255,8,76,2048
maav 40,104,12
maav 44,76,8
ra 204,224,3072
maav 44,108,12
v_smfmac_f32_16x16x64_bf16 v[148:151],v[228:231],v[8:15],v248
ra 208,224,4096
maav 48,80,8
maav 48,112,12
maav 52,84,8
ds 255,8,76,3072
maav 52,116,12
ra 212,224,5120
maav 56,88,8
ex 64
maav 56,120,12
ra 216,224,6144
maav 60,92,8
ex 65
maav 60,124,12
s_mov_b32 m0,s73
ex 66
mvav 160,64,16
ra 220,224,7168
cv 64,64,65
mvav 160,96,20
ds 255,8,77,0
mvav 164,68,16
ex 67
mvav 164,100,20
ra 224,224,8192
cv 65,66,67
mvav 168,72,16
ex 68
mvav 168,104,20
ra 228,224,9216
mvav 172,76,16
ds 255,8,77,1024
mvav 172,108,20
s_add_u32 s46,s46,16384
ex 69
v_smfmac_f32_16x16x64_bf16 v[152:155],v[228:231],v[16:23],v248
ra 232,224,10240
mvav 176,80,16
s_add_u32 s47,s47,32768
ex 70
)"
R"(mvav 176,112,20
s_add_u32 s95,s47,32768
ex 71
mvav 180,84,16
ra 236,224,11264
cv 66,68,69
mvav 180,116,20
ex 96
mvav 184,88,16
ds 255,8,77,2048
mvav 184,120,20
ex 97
mvav 188,92,16
ra 240,224,12288
cv 67,70,71
mvav 188,124,20
ex 98
mvav 192,64,24
ra 244,224,13312
cv 68,96,97
mvav 192,96,28
ex 99
mvav 196,68,24
ex 100
mvav 196,100,28
ds 255,8,77,3072
mvav 200,72,24
ra 248,224,14336
cv 69,98,99
mvav 200,104,28
s_cmp_ge_i32 s47,s46
ex 101
mvav 204,76,24
s_cselect_b32 s76,s95,s46
ex 102
mvav 204,108,28
s_cselect_b32 s77,s95,268435456
ex 103
v_smfmac_f32_16x16x64_bf16 v[156:159],v[228:231],v[24:31],v248
ra 252,224,15360
mvav 208,80,24
s_add_u32 s77,s77,16384
ex 72
mvav 208,112,28
ex 73
mvav 212,84,24
ex 74
mvav 212,116,28
ex 75
mvav 216,88,24
cv 70,100,101
cv 71,102,103
mvav 216,120,28
ex 76
mvav 220,92,24
ex 77
mvav 220,124,28
s_waitcnt vmcnt(19) lgkmcnt(0)
s_barrier
cvaa 0,192,128,232
mvaa 0,200,132
s_add_u32 s81,s81,1
ex 78
mvaa 0,208,136
s_add_u32 s44,s44,1024
ex 79
mvaa 0,216,140
cv 72,72,73
cv 73,74,75
cvaa 4,220,140,232
s_mov_b32 m0,s68
ex 104
mvaa 4,212,136
ds 249,8,76,0
mvaa 4,204,132
ex 105
mvaa 4,196,128
ex 106
cvaa 32,224,128,232
ra 64,225,32768
cv 74,76,77
mvaa 32,232,132
ex 107
mvaa 32,240,136
ra 68,225,33792
cv 75,78,79
mvaa 32,248,140
ex 108
cvaa 36,252,140,232
ds 249,8,76,1024
mvaa 36,244,136
ex 109
mvaa 36,236,132
ra 72,225,34816
cv 76,104,105
mvaa 36,228,128
ex 110
cvaa 8,192,144,232
ra 76,225,35840
cv 77,106,107
mvaa 8,200,148
ex 111
mvaa 8,208,152
ex 80
mvaa 8,216,156
ds 249,8,76,2048
cvaa 12,220,156,232
ra 80,225,36864
cv 78,108,109
mvaa 12,212,152
ex 81
mvaa 12,204,148
ra 84,225,37888
cv 79,110,111
mvaa 12,196,144
ex 82
cvaa 40,224,144,232
ex 83
mvaa 40,232,148
ra 88,225,38912
cv 80,80,81
mvaa 40,240,152
ds 249,8,76,3072
mvaa 40,248,156
ex 84
cvaa 44,252,156,232
ra 92,225,39936
cv 81,82,83
mvaa 44,244,152
ex 85
mvaa 44,236,148
ex 86
mvaa 44,228,144
ra 96,225,40960
cv 82,84,85
cvaa 16,192,160,232
s_mov_b32 m0,s69
ex 87
mvaa 16,200,164
ra 100,225,41984
cv 83,86,87
mvaa 16,208,168
ds 249,8,77,0
mvaa 16,216,172
ex 112
cvaa 20,220,172,232
ex 113
mvaa 20,212,168
ra 104,225,43008
cv 84,112,113
mvaa 20,204,164
ex 114
mvaa 20,196,160
ra 108,225,44032
cvaa 48,224,160,232
ex 115
mvaa 48,232,164
ds 249,8,77,1024
mvaa 48,240,168
ex 116
mvaa 48,248,172
ra 112,225,45056
cv 85,114,115
cvaa 52,252,172,232
ex 117
mvaa 52,244,168
ra 116,225,46080
cv 86,116,117
mvaa 52,236,164
ex 118
mvaa 52,228,160
ex 119
cvaa 24,192,176,232
ds 249,8,77,2048
mvaa 24,200,180
ra 120,225,47104
cv 87,118,119
mvaa 24,208,184
ex 88
mvaa 24,216,188
ra 124,225,48128
cvaa 28,220,188,232
mvaa 28,212,184
ex 89
mvaa 28,204,180
ex 90
mvaa 28,196,176
ds 249,8,77,3072
cvaa 56,224,176,232
ex 91
mvaa 56,232,180
ex 92
mvaa 56,240,184
ex 93
mvaa 56,248,188
ex 94
cvaa 60,252,188,232
mvaa 60,244,184
ex 95
mvaa 60,236,180
cv 88,88,89
cv 89,90,91
mvaa 60,228,176
s_waitcnt vmcnt(18) lgkmcnt(0)
s_barrier
maav 0,64,64
ex 120
maav 0,96,68
s_mov_b32 m0,s74
ex 121
maav 4,68,64
ds 255,8,78,0
maav 4,100,68
ex 122
maav 8,72,64
ex 123
maav 8,104,68
cv 90,92,93
cv 91,94,95
maav 12,76,64
ex 124
maav 12,108,68
ex 125
v_smfmac_f32_16x16x64_bf16 v[144:147],v[228:231],v[64:71],v248
cv 92,120,121
maav 16,80,64
ex 126
maav 16,112,68
buffer_load_dword v247,v245,s[36:39],s85 offen
maav 20,84,64
ex 127
maav 20,116,68
cv 93,122,123
cv 94,124,125
maav 24,88,64
cv 95,126,127
maav 24,120,68
ra 192,224,32768
maav 28,92,64
ds 255,8,78,1024
maav 28,124,68
maav 32,64,72
ra 196,224,33792
maav 32,96,76
s_add_u32 s85,s85,131072
maav 36,68,72
ra 200,224,34816
maav 36,100,76
maav 40,72,72
ds 255,8,78,2048
maav 40,104,76
maav 44,76,72
ra 204,224,35840
maav 44,108,76
v_smfmac_f32_16x16x64_bf16 v[148:151],v[228:231],v[72:79],v248
ra 208,224,36864
maav 48,80,72
maav 48,112,76
maav 52,84,72
ds 255,8,78,3072
maav 52,116,76
ra 212,224,37888
maav 56,88,72
ex 0
maav 56,120,76
ra 216,224,38912
maav 60,92,72
ex 1
maav 60,124,76
s_mov_b32 m0,s75
ex 2
mvav 160,64,80
ra 220,224,39936
cv 0,0,1
mvav 160,96,84
ds 255,8,79,0
mvav 164,68,80
ex 3
mvav 164,100,84
ra 224,224,40960
cv 1,2,3
mvav 168,72,80
ex 4
mvav 168,104,84
ra 228,224,41984
mvav 172,76,80
ds 255,8,79,1024
mvav 172,108,84
s_add_u32 s46,s46,16384
ex 5
v_smfmac_f32_16x16x64_bf16 v[152:155],v[228:231],v[80:87],v248
ra 232,224,43008
mvav 176,80,80
s_add_u32 s47,s47,32768
ex 6
mvav 176,112,84
s_add_u32 s95,s47,32768
ex 7
mvav 180,84,80
ra 236,224,44032
cv 2,4,5
mvav 180,116,84
ex 32
mvav 184,88,80
ds 255,8,79,2048
mvav 184,120,84
ex 33
mvav 188,92,80
ra 240,224,45056
cv 3,6,7
mvav 188,124,84
ex 34
mvav 192,64,88
ra 244,224,46080
cv 4,32,33
mvav 192,96,92
ex 35
mvav 196,68,88
ex 36
mvav 196,100,92
ds 255,8,79,3072
mvav 200,72,88
ra 248,224,47104
cv 5,34,35
mvav 200,104,92
s_cmp_ge_i32 s47,s46
ex 37
mvav 204,76,88
s_cselect_b32 s78,s95,s46
ex 38
mvav 204,108,92
s_cselect_b32 s79,s95,268435456
ex 39
v_smfmac_f32_16x16x64_bf16 v[156:159],v[228:231],v[88:95],v248
ra 252,224,48128
mvav 208,80,88
s_add_u32 s79,s79,16384
ex 8
mvav 208,112,92
ex 9
mvav 212,84,88
ex 10
mvav 212,116,92
ex 11
mvav 216,88,88
cv 6,36,37
cv 7,38,39
mvav 216,120,92
ex 12
mvav 220,92,88
ex 13
mvav 220,124,92
s_waitcnt vmcnt(17) lgkmcnt(0)
s_barrier
cvaa 64,192,128,232
s_bitcmp0_b32 s82,s81
s_cbranch_scc1 L_loop
s_bitcmp0_b32 s15,s81
s_cbranch_scc1 L_eva_r
L_short:
v_add_f32_e32 v226,v152,v153
v_add_f32_e32 v227,v156,v157
v_subrev_u32_e32 v236,0x23800000,v226
v_subrev_u32_e32 v237,0x23800000,v227
v_max_u32_e32 v236,v236,v237
v_cmp_lt_u32_e64 s[40:41],s48,v236
v_rcp_f32_e32 v226,v226
v_rcp_f32_e32 v227,v227
s_add_u32 s86,s13,4096
v_mov_b32_e32 v236,0
v_mov_b32_e32 v237,0
v_mov_b32_e32 v238,0
v_mov_b32_e32 v239,0
v_mov_b64_e32 v[152:153],0
v_mov_b64_e32 v[156:157],0
v_mul_f32_e32 v226,v242,v226
v_mul_f32_e32 v227,v243,v227
v_add_u32_e32 v225,s94,v225
v_pk_mul_f32 v[160:161],v[160:161],v[226:227] op_sel:[0,0] op_sel_hi:[1,0]
v_pk_mul_f32 v[162:163],v[162:163],v[226:227] op_sel:[0,0] op_sel_hi:[1,0]
v_pk_mul_f32 v[164:165],v[164:165],v[226:227] op_sel:[0,0] op_sel_hi:[1,0]
v_pk_mul_f32 v[166:167],v[166:167],v[226:227] op_sel:[0,0] op_sel_hi:[1,0]
cv 160,160,161
cv 161,162,163
cv 162,164,165
cv 163,166,167
buffer_store_dwordx4 v[160:163],v244,s[8:11],s13 offen offset:0 sc0 sc1
v_pk_mul_f32 v[168:169],v[168:169],v[226:227] op_sel:[0,0] op_sel_hi:[1,0]
v_pk_mul_f32 v[170:171],v[170:171],v[226:227] op_sel:[0,0] op_sel_hi:[1,0]
v_pk_mul_f32 v[172:173],v[172:173],v[226:227] op_sel:[0,0] op_sel_hi:[1,0]
v_pk_mul_f32 v[174:175],v[174:175],v[226:227] op_sel:[0,0] op_sel_hi:[1,0]
cv 168,168,169
cv 169,170,171
cv 170,172,173
cv 171,174,175
buffer_store_dwordx4 v[168:171],v244,s[8:11],s13 offen offset:2048 sc0 sc1
v_pk_mul_f32 v[176:177],v[176:177],v[226:227] op_sel:[0,0] op_sel_hi:[1,0]
v_pk_mul_f32 v[178:179],v[178:179],v[226:227] op_sel:[0,0] op_sel_hi:[1,0]
v_pk_mul_f32 v[180:181],v[180:181],v[226:227] op_sel:[0,0] op_sel_hi:[1,0]
v_pk_mul_f32 v[182:183],v[182:183],v[226:227] op_sel:[0,0] op_sel_hi:[1,0]
cv 176,176,177
cv 177,178,179
cv 178,180,181
v_mfma_f32_32x32x16_bf16 v[160:175],v[236:239],v[236:239],0
cv 179,182,183
buffer_store_dwordx4 v[176:179],v244,s[8:11],s86 offen offset:0 sc0 sc1
v_pk_mul_f32 v[184:185],v[184:185],v[226:227] op_sel:[0,0] op_sel_hi:[1,0]
v_pk_mul_f32 v[186:187],v[186:187],v[226:227] op_sel:[0,0] op_sel_hi:[1,0]
v_pk_mul_f32 v[188:189],v[188:189],v[226:227] op_sel:[0,0] op_sel_hi:[1,0]
v_pk_mul_f32 v[190:191],v[190:191],v[226:227] op_sel:[0,0] op_sel_hi:[1,0]
cv 184,184,185
cv 185,186,187
cv 186,188,189
cv 187,190,191
buffer_store_dwordx4 v[184:187],v244,s[8:11],s86 offen offset:2048 sc0 sc1
v_pk_mul_f32 v[192:193],v[192:193],v[226:227] op_sel:[0,1] op_sel_hi:[1,1]
v_pk_mul_f32 v[194:195],v[194:195],v[226:227] op_sel:[0,1] op_sel_hi:[1,1]
v_pk_mul_f32 v[196:197],v[196:197],v[226:227] op_sel:[0,1] op_sel_hi:[1,1]
v_pk_mul_f32 v[198:199],v[198:199],v[226:227] op_sel:[0,1] op_sel_hi:[1,1]
cv 192,192,193
cv 193,194,195
cv 194,196,197
v_mfma_f32_32x32x16_bf16 v[176:191],v[236:239],v[236:239],0
cv 195,198,199
buffer_store_dwordx4 v[192:195],v244,s[8:11],s13 offen offset:1024 sc0 sc1
v_pk_mul_f32 v[200:201],v[200:201],v[226:227] op_sel:[0,1] op_sel_hi:[1,1]
v_pk_mul_f32 v[202:203],v[202:203],v[226:227] op_sel:[0,1] op_sel_hi:[1,1]
v_pk_mul_f32 v[204:205],v[204:205],v[226:227] op_sel:[0,1] op_sel_hi:[1,1]
v_pk_mul_f32 v[206:207],v[206:207],v[226:227] op_sel:[0,1] op_sel_hi:[1,1]
cv 200,200,201
cv 201,202,203
cv 202,204,205
cv 203,206,207
buffer_store_dwordx4 v[200:203],v244,s[8:11],s13 offen offset:3072 sc0 sc1
v_pk_mul_f32 v[208:209],v[208:209],v[226:227] op_sel:[0,1] op_sel_hi:[1,1]
v_pk_mul_f32 v[210:211],v[210:211],v[226:227] op_sel:[0,1] op_sel_hi:[1,1]
v_pk_mul_f32 v[212:213],v[212:213],v[226:227] op_sel:[0,1] op_sel_hi:[1,1]
v_pk_mul_f32 v[214:215],v[214:215],v[226:227] op_sel:[0,1] op_sel_hi:[1,1]
cv 208,208,209
cv 209,210,211
cv 210,212,213
v_mfma_f32_32x32x16_bf16 v[192:207],v[236:239],v[236:239],0
cv 211,214,215
buffer_store_dwordx4 v[208:211],v244,s[8:11],s86 offen offset:1024 sc0 sc1
v_pk_mul_f32 v[216:217],v[216:217],v[226:227] op_sel:[0,1] op_sel_hi:[1,1]
v_pk_mul_f32 v[218:219],v[218:219],v[226:227] op_sel:[0,1] op_sel_hi:[1,1]
v_pk_mul_f32 v[220:221],v[220:221],v[226:227] op_sel:[0,1] op_sel_hi:[1,1]
v_pk_mul_f32 v[222:223],v[222:223],v[226:227] op_sel:[0,1] op_sel_hi:[1,1]
cv 216,216,217
cv 217,218,219
cv 218,220,221
cv 219,222,223
buffer_store_dwordx4 v[216:219],v244,s[8:11],s86 offen offset:3072 sc0 sc1
s_nop 1
v_mfma_f32_32x32x16_bf16 v[208:223],v[236:239],v[236:239],0
L_eva_r:
s_cmp_lg_u32 s81,14
s_cbranch_scc1 L_loop
s_add_u32 s81,s81,1
s_bitcmp1_b32 170,s20
s_cbranch_scc1 L_odd
L_last:
s_lshl_b32 s86,s21,2
s_sub_u32 s87,s13,s67
s_add_u32 s86,s86,s12
mvaa 64,200,132
s_lshl_b32 s86,s86,8
v_add_u32_e32 v252,s87,v244
s_add_u32 s86,s86,L_last-L_att_pc+1024
v_lshl_add_u32 v226,v244,3,s86
s_lshl_b32 s86,s12,1
s_bfe_u32 s87,s21,65539
mvaa 64,208,136
s_add_u32 s86,s86,s87
v_min_u32_e32 v226,L_att_end-L_att_pc-4,v226
s_lshl_b32 s86,s86,7
s_add_u32 s86,s86,L_pend2-L_att_pc
v_writelane_b32 v226,s86,2
mvaa 64,216,140
s_mov_b64 exec,7
global_load_dword v253,v226,s[96:97]
s_mov_b64 exec,-1
cvaa 68,220,140,232
s_add_u32 m0,s52,32768
v_and_b32_e32 v224,16,v244
v_lshlrev_b32_e32 v224,5,v224
mvaa 68,212,136
s_xor_b32 s51,s62,0
v_lshrrev_b32_e32 v239,4,v244
v_lshrrev_b32_e32 v236,3,v239
mvaa 68,204,132
s_lshl_b32 s87,s12,13
v_and_b32_e32 v237,112,v244
v_and_b32_e32 v238,48,v239
mvaa 68,196,128
s_xor_b32 s87,s87,8192
ra 64,225,0
v_lshlrev_b32_e32 v238,1,v238
cvaa 96,224,128,232
buffer_load_dword v253,v224,s[8:11],s53 offen sc1
mvaa 96,232,132
s_mov_b32 s84,6
ra 68,225,1024
v_xor_b32_e32 v237,v237,v238
mvaa 96,240,136
s_add_u32 s81,s81,1
ra 72,225,2048
v_lshl_add_u32 v245,v236,11,v237
mvaa 96,248,140
s_mov_b32 s84,4
v_add_u32_e32 v246,15360,v245
v_add_u32_e32 v249,30720,v245
cvaa 100,252,140,232
s_andn2_b32 s69,64,s54
ra 76,225,3072
s_waitcnt vmcnt(9)
v_add_u32_e32 v247,46080,v245
mvaa 100,244,136
dl 245,24,51,0
mvaa 100,236,132
s_add_u32 s88,s14,4096
ra 80,225,4096
cv 8,8,9
mvaa 100,228,128
s_lshl_b32 s35,s19,1
ra 84,225,5120
cv 9,10,11
cvaa 72,192,144,232
s_lshr_b32 s33,40569,s35
ra 88,225,6144
cv 10,12,13
mvaa 72,200,148
s_and_b32 s33,s33,3
ra 92,225,7168
v_add_u32_e32 v250,4096,v252
mvaa 72,208,152
dl 246,24,51,1024
mvaa 72,216,156
s_lshl_b32 s33,s33,13
ra 96,225,8192
v_mov_b32_e32 v251,4
cvaa 76,220,156,232
s_lshr_b32 s34,59294,s35
ra 100,225,9216
s_and_b32 s34,s34,3
mvaa 76,212,152
s_lshl_b32 s34,s34,13
ra 104,225,10240
s_xor_b32 s35,s33,s34
mvaa 76,204,148
s_and_b32 s98,s19,3
ra 108,225,11264
s_lshl_b32 s98,s98,13
mvaa 76,196,144
dl 249,24,51,2048
cvaa 104,224,144,232
s_mov_b32 s74,0x300003
ra 112,225,12288
s_mov_b32 s75,0x30000300
mvaa 104,232,148
s_lshl_b32 s71,s12,15
ra 116,225,13312
s_add_u32 s71,s71,s54
mvaa 104,240,152
s_mov_b64 s[44:45],s[30:31]
ra 120,225,14336
s_mov_b32 s46,4194304
mvaa 104,248,156
s_mov_b32 s47,131072
ex 14
cvaa 108,252,156,232
dl 247,24,51,3072
mvaa 108,244,152
s_add_u32 m0,s52,49152
s_xor_b32 s51,s63,0
ra 124,225,15360
mvaa 108,236,148
s_lshl_b32 s70,s23,11
ex 15
mvaa 108,228,144
s_add_u32 s70,s70,s71
ex 40
cvaa 80,192,160,232
ex 41
mvaa 80,200,164
dl 245,24,51,0
mvaa 80,208,168
ex 42
mvaa 80,216,172
ex 43
cvaa 84,220,172,232
ex 44
mvaa 84,212,168
ex 45
mvaa 84,204,164
dl 246,24,51,1024
mvaa 84,196,160
ex 46
cvaa 112,224,160,232
ex 47
mvaa 112,232,164
ex 16
mvaa 112,240,168
ex 17
mvaa 112,248,172
dl 249,24,51,2048
cvaa 116,252,172,232
ex 18
mvaa 116,244,168
ex 19
mvaa 116,236,164
ex 20
mvaa 116,228,160
ex 21
cvaa 88,192,176,232
dl 247,24,51,3072
s_waitcnt vmcnt(8)
v_cmp_ne_u32_e32 vcc,4,v253
mvaa 88,200,180
s_cbranch_vccnz L_spbe
L_sprbe:
ex 22
mvaa 88,208,184
s_xor_b32 s50,s66,262144
ex 23
mvaa 88,216,188
ex 48
cvaa 92,220,188,232
ex 49
mvaa 92,212,184
la 128,252,50,0
mvaa 92,204,180
s_waitcnt lgkmcnt(0)
s_barrier
mvaa 92,196,176
s_mov_b32 m0,s72
ex 50
cvaa 120,224,176,232
s_lshl_b32 s72,s22,11
ex 51
mvaa 120,232,180
s_add_u32 s72,s72,s71
ex 52
mvaa 120,240,184
ds 255,8,76,0
mvaa 120,248,188
s_add_u32 s71,s70,131072
ex 53
cvaa 124,252,188,232
ex 54
mvaa 124,244,184
ex 55
mvaa 124,236,180
ex 24
mvaa 124,228,176
ds 255,8,76,1024
maav 0,64,0
s_cmp_lg_u32 s19,7
s_cbranch_scc1 L_j7e
s_nop 1
s_lshl_b32 s86,s12,12
s_add_u32 s86,s86,131072
v_add_u32_e32 v236,s86,v244
ra 160,236,0
ra 164,236,1024
ra 168,236,2048
ra 172,236,3072
ra 176,236,16384
ra 180,236,17408
ra 184,236,18432
ra 188,236,19456
v_add_u32_e32 v224,s94,v224
s_waitcnt lgkmcnt(0)
L_j7e:
ex 25
maav 0,96,4
ex 26
maav 4,68,0
ex 27
maav 4,100,4
cv 11,14,15
cv 12,40,41
maav 8,72,0
ds 255,8,76,2048
maav 8,104,4
cv 13,42,43
cv 14,44,45
maav 12,76,0
cv 15,46,47
cv 16,16,17
maav 12,108,4
ex 28
v_smfmac_f32_16x16x64_bf16 v[144:147],v[228:231],v[0:7],v248
cv 17,18,19
maav 16,80,0
ds 255,8,76,3072
maav 16,112,4
s_mov_b32 m0,s73
ex 29
maav 20,84,0
s_add_u32 s73,s72,131072
ex 30
maav 20,116,4
ex 31
maav 24,88,0
ex 56
maav 24,120,4
ds 255,8,77,0
maav 28,92,0
ex 57
maav 28,124,4
ex 58
maav 32,64,8
L_vs18:
s_cbranch_execz L_vs22
ex 59
maav 32,96,12
ex 60
maav 36,68,8
ds 255,8,77,1024
maav 36,100,12
cv 18,20,21
cv 19,22,23
maav 40,72,8
cv 20,48,49
cv 21,50,51
maav 40,104,12
cv 22,52,53
cv 23,54,55
maav 44,76,8
ex 61
maav 44,108,12
ds 255,8,77,2048
v_smfmac_f32_16x16x64_bf16 v[148:151],v[228:231],v[8:15],v248
cv 24,24,25
maav 48,80,8
ex 62
maav 48,112,12
ex 63
maav 52,84,8
ra 192,244,0
cv 25,26,27
maav 52,116,12
ds 255,8,77,3072
maav 56,88,8
cv 26,28,29
cv 27,30,31
maav 56,120,12
cv 28,56,57
cv 29,58,59
maav 60,92,8
cv 30,60,61
cv 31,62,63
maav 60,124,12
ex 64
mvav 160,64,16
la 132,252,50,2048
mvav 160,96,20
ex 65
mvav 164,68,16
ex 66
mvav 164,100,20
ex 67
mvav 168,72,16
ex 68
mvav 168,104,20
la 136,250,50,0
mvav 172,76,16
ex 69
mvav 172,108,20
ex 70
v_smfmac_f32_16x16x64_bf16 v[152:155],v[228:231],v[16:23],v248
ra 200,244,2048
mvav 176,80,16
ra 208,244,4096
cv 64,64,65
mvav 176,112,20
la 140,250,50,2048
mvav 180,84,16
ra 216,244,6144
cv 65,66,67
mvav 180,116,20
ex 71
mvav 184,88,16
ex 96
mvav 184,120,20
s_barrier
ex 97
mvav 188,92,16
la 156,250,50,3072
mvav 188,124,20
s_add_u32 m0,s52,131072
ra 196,244,1024
s_xor_b32 s51,s62,1048576
mvav 192,64,24
ra 204,244,3072
cv 66,68,69
mvav 192,96,28
ra 212,244,5120
cv 67,70,71
mvav 196,68,24
ra 220,244,7168
cv 68,96,97
mvav 196,100,28
la 152,250,50,1024
mvav 200,72,24
ex 98
mvav 200,104,28
ex 99
mvav 204,76,24
ra 224,244,8192
cv 69,98,99
mvav 204,108,28
ra 232,244,10240
v_add_u32_e32 v6,s87,v244
v_smfmac_f32_16x16x64_bf16 v[156:159],v[228:231],v[24:31],v248
ra 240,244,12288
mvav 208,80,24
la 148,252,50,3072
mvav 208,112,28
s_xor_b32 s87,s52,4096
ra 248,244,14336
v_add_u32_e32 v7,98304,v6
mvav 212,84,24
s_add_u32 s87,s87,150528
ra 252,244,15360
v_add_u32_e32 v5,65536,v244
mvav 212,116,28
ra 244,244,13312
v_add_u32_e32 v32,s87,v244
mvav 216,88,24
ra 228,244,9216
v_and_b32_e32 v224,960,v244
mvav 216,120,28
la 144,252,50,1024
mvav 220,92,24
s_add_u32 s50,s50,s55
ra 236,244,11264
mvav 220,124,28
ra 64,225,32768
s_waitcnt lgkmcnt(8)
cvaa 24,192,176,232
dl 245,24,51,0
mvaa 24,200,180
ra 96,225,40960
mvaa 24,208,184
ra 68,225,33792
mvaa 24,216,188
dl 246,24,51,1024
cvaa 16,192,160,128
ra 100,225,41984
mvaa 16,200,164
ra 72,225,34816
mvaa 16,208,168
dl 249,24,51,2048
mvaa 16,216,172
ra 104,225,43008
cvaa 28,196,176,232
ra 76,225,35840
mvaa 28,204,180
dl 247,24,51,3072
mvaa 28,212,184
s_add_u32 m0,s52,147456
s_xor_b32 s51,s63,1048576
ra 108,225,44032
mvaa 28,220,188
ra 80,225,36864
cvaa 20,196,160,132
dl 245,24,51,0
mvaa 20,204,164
ra 112,225,45056
mvaa 20,212,168
ra 192,244,16384
mvaa 20,220,172
dl 246,24,51,1024
cvaa 56,224,176,232
ra 200,244,18432
s_waitcnt lgkmcnt(7)
mvaa 56,232,180
ra 208,244,20480
mvaa 56,240,184
dl 249,24,51,2048
mvaa 56,248,188
ra 216,244,22528
cvaa 48,224,160,136
ra 196,244,17408
mvaa 48,232,164
ra 204,244,19456
mvaa 48,240,168
ra 212,244,21504
mvaa 48,248,172
ra 220,244,23552
cvaa 60,228,176,232
ra 224,244,24576
mvaa 60,236,180
ra 232,244,26624
mvaa 60,244,184
ra 240,244,28672
mvaa 60,252,188
ra 248,244,30720
cvaa 52,228,160,140
ra 84,225,37888
mvaa 52,236,164
ra 116,225,46080
mvaa 52,244,168
la 160,252,50,0
ra 228,244,25600
mvaa 52,252,172
ra 236,244,27648
s_waitcnt lgkmcnt(10)
cvaa 8,192,176,128
ra 244,244,29696
mvaa 8,200,180
la 164,252,50,2048
ra 252,244,31744
mvaa 8,208,184
ra 88,225,38912
mvaa 8,216,188
ra 120,225,47104
cvaa 12,196,176,132
la 168,250,50,0
mvaa 12,204,180
ra 92,225,39936
ra 124,225,48128
s_waitcnt lgkmcnt(9)
mvaa 12,212,184
ex 100
mvaa 12,220,188
la 172,250,50,2048
cvaa 40,224,176,136
ex 101
mvaa 40,232,180
ex 102
mvaa 40,240,184
ex 103
mvaa 40,248,188
cv 70,100,101
cv 71,102,103
s_waitcnt lgkmcnt(4)
cvaa 44,228,176,140
ex 72
mvaa 44,236,180
s_waitcnt vmcnt(18) lgkmcnt(0)
s_barrier
ex 73
mvaa 44,244,184
la 176,252,50,1024
mvaa 44,252,188
ex 74
maav 0,64,64
ex 75
maav 0,96,68
la 188,250,50,3072
maav 4,68,64
ra 192,5,0
cv 72,72,73
maav 4,100,68
ex 76
maav 8,72,64
la 184,250,50,1024
maav 8,104,68
ds_write_b128 v6,a[0:3] offset:0
maav 12,76,64
ex 77
maav 12,108,68
la 180,252,50,3072
cv 73,74,75
ex 78
v_smfmac_f32_16x16x64_bf16 v[144:147],v[228:231],v[64:71],v248
ds_write_b128 v6,a[4:7] offset:1024
maav 16,80,64
cv 74,76,77
ex 79
ex 104
maav 16,112,68
ds_write_b128 v6,a[8:11] offset:2048
cv 75,78,79
maav 20,84,64
ex 105
ex 106
maav 20,116,68
ds_write_b128 v6,a[12:15] offset:3072
cv 76,104,105
maav 24,88,64
ex 107
ex 108
cv 77,106,107
maav 24,120,68
ex 109
ex 110
maav 28,92,64
ds_write_b128 v6,a[16:19] offset:4096
cv 78,108,109
maav 28,124,68
ex 111
ex 80
cv 79,110,111
maav 32,64,72
ex 81
ex 82
)"
R"(maav 32,96,76
ds_write_b128 v6,a[20:23] offset:5120
maav 36,68,72
ex 83
ex 84
cv 80,80,81
maav 36,100,76
ex 85
ex 86
maav 40,72,72
ds_write_b128 v6,a[24:27] offset:6144
cv 81,82,83
maav 40,104,76
ex 87
ex 112
cv 82,84,85
maav 44,76,72
ex 113
ex 114
maav 44,108,76
ds_write_b128 v6,a[28:31] offset:7168
cv 83,86,87
v_smfmac_f32_16x16x64_bf16 v[148:151],v[228:231],v[72:79],v248
ex 115
ex 116
maav 48,80,72
ex 117
ex 118
maav 48,112,76
ds_write_b128 v7,a[32:35] offset:0
maav 52,84,72
ex 119
cv 84,112,113
ex 88
maav 52,116,76
ra 196,5,1024
ex 89
maav 56,88,72
ds_write_b128 v7,a[36:39] offset:1024
cv 85,114,115
maav 56,120,76
ex 90
cv 86,116,117
ex 91
maav 60,92,72
ra 200,5,2048
ra 204,5,3072
ex 92
maav 60,124,76
ds_write_b128 v7,a[40:43] offset:2048
cv 87,118,119
mvav 160,64,80
ex 93
cv 88,88,89
ex 94
mvav 160,96,84
ra 208,5,4096
ra 212,5,5120
ex 95
mvav 164,68,80
ra 216,5,6144
ex 120
ex 121
mvav 164,100,84
ds_write_b128 v7,a[44:47] offset:3072
v_add_f32_e32 v130,v144,v145
mvav 168,72,80
ex 122
v_add_f32_e32 v131,v148,v149
mvav 168,104,84
ds_write_b128 v7,a[48:51] offset:4096
mvav 172,76,80
cv 89,90,91
ex 123
ex 124
mvav 172,108,84
ds_write_b128 v32,v[128:131]
v_smfmac_f32_16x16x64_bf16 v[152:155],v[228:231],v[80:87],v248
cv 90,92,93
ex 125
mvav 176,80,80
ds_write_b128 v7,a[52:55] offset:5120
cv 91,94,95
mvav 176,112,84
ra 220,5,7168
ra 224,5,8192
ex 126
ex 127
mvav 180,84,80
ra 232,5,10240
ra 240,5,12288
cv 92,120,121
ex 24
mvav 180,116,84
ds_write_b128 v7,a[56:59] offset:6144
cv 93,122,123
mvav 184,88,80
ex 25
ex 26
mvav 184,120,84
ex 16
ex 27
mvav 188,92,80
ds_write_b128 v7,a[60:63] offset:7168
cv 94,124,125
mvav 188,124,84
ra 248,5,14336
ra 228,5,9216
ra 236,5,11264
ex 17
cv 95,126,127
mvav 192,64,88
ra 244,5,13312
ra 252,5,15360
ex 18
ex 19
mvav 192,96,92
ex 28
cv 16,16,17
ex 29
mvav 196,68,88
ex 30
ex 31
v_xor_b32_e32 v6,8192,v6
v_xor_b32_e32 v7,8192,v7
mvav 196,100,92
s_waitcnt lgkmcnt(5)
s_xor_b32 s87,s87,0
mvav 200,72,88
s_xor_b32 s50,s66,294912
s_barrier
ra 64,5,16384
ra 96,5,24576
rv 64,6,0
rv 68,6,1024
rv 72,6,2048
mvav 200,104,92
la 4,252,50,0
rv 76,6,3072
rv 80,6,4096
rv 84,6,5120
rv 96,7,0
mvav 204,76,88
rv 100,7,1024
ex 20
ex 21
cv 17,18,19
mvav 204,108,92
rv 104,7,2048
rv 108,7,3072
rv 112,7,4096
rv 116,7,5120
ex 22
v_smfmac_f32_16x16x64_bf16 v[156:159],v[228:231],v[88:95],v248
rv 120,7,6144
rv 124,7,7168
ex 23
mvav 208,80,88
la 12,252,50,2048
ex 48
ex 49
mvav 208,112,92
ra 68,5,17408
ra 100,5,25600
ra 72,5,18432
cv 18,20,21
mvav 212,84,88
ra 104,5,26624
ra 76,5,19456
cv 19,22,23
ex 50
mvav 212,116,92
la 20,250,50,0
ra 108,5,27648
cv 20,48,49
ex 51
mvav 216,88,88
ra 80,5,20480
ra 112,5,28672
ra 84,5,21504
cv 21,50,51
mvav 216,120,92
ra 116,5,29696
ex 52
ex 53
mvav 220,92,88
la 28,250,50,2048
ex 54
ex 55
mvav 220,124,92
ra 88,5,22528
ra 120,5,30720
cv 22,52,53
cv 23,54,55
s_bitcmp1_b32 s82,s81
s_cbranch_scc1 L_pend2
L_ret_eb4:
mvav 160,192,16
la 60,250,50,3072
ra 92,5,23552
rv 88,6,6144
ra 124,5,31744
rv 92,6,7168
mvav 160,224,20
ex 56
ex 57
v_smfmac_f32_16x16x64_bf16 v[152:155],v[228:231],v[16:23],v248
v_xor_b32_e32 v32,4096,v32
mvav 164,196,16
la 52,250,50,1024
ds_read_b128 v[236:239],v32
ex 58
s_waitcnt lgkmcnt(7)
mvav 164,228,20
ex 59
ex 60
ex 61
mvav 168,200,16
cv 24,24,25
ex 62
mvav 168,232,20
la 44,252,50,3072
cv 25,26,27
ex 63
mvav 172,204,16
ex 8
cv 26,28,29
ex 9
mvav 172,236,20
s_waitcnt lgkmcnt(0)
s_barrier
ex 10
cv 27,30,31
mvav 176,208,16
la 36,252,50,1024
ex 11
cv 28,56,57
mvav 176,240,20
s_add_u32 m0,s52,147456
ex 12
ex 13
s_xor_b32 s51,s63,1048576
cv 29,58,59
mvav 180,212,16
s_add_u32 s50,s50,s55
ex 14
ex 15
cv 30,60,61
mvav 180,244,20
dl 247,24,51,3072
ex 40
cv 31,62,63
mvav 184,216,16
s_add_u32 m0,s52,0
ex 41
ex 42
cv 8,8,9
mvav 184,248,20
s_xor_b32 s51,s64,0
ex 43
ex 44
cv 9,10,11
mvav 188,220,16
la 0,252,50,0
ex 45
cv 10,12,13
mvav 188,252,20
ex 46
ex 47
cv 11,14,15
cv 12,40,41
v_smfmac_f32_16x16x64_bf16 v[156:159],v[228:231],v[24:31],v248
cv 13,42,43
cv 14,44,45
cv 15,46,47
v_bfe_u32 v225,v244,1,5
s_nop 0
v_smfmac_f32_16x16x64_bf16 v[156:159],v[228:231],v[8:15],v248
v_lshl_add_u32 v224,v224,1,v225
v_bfe_u32 v225,v244,7,2
v_lshl_add_u32 v224,v225,5,v224
mvav 192,192,24
la 8,252,50,2048
v_xor_b32_e32 v224,s69,v224
v_add_f32_e32 v152,v152,v153
v_add_f32_e32 v137,v152,v238
v_subrev_u32_e32 v236,0x23800000,v137
mvav 192,224,28
v_rcp_f32_e32 v137,v137
mvav 192,64,8
tv 128,224,32768
v_mul_f32_e32 v232,v240,v137
v_mul_f32_e32 v64,v232,v64
v_mul_f32_e32 v65,v232,v65
v_mul_f32_e32 v66,v232,v66
mvav 192,96,12
la 16,250,50,0
v_mul_f32_e32 v67,v232,v67
v_mul_f32_e32 v68,v232,v68
v_mul_f32_e32 v69,v232,v69
v_mul_f32_e32 v70,v232,v70
mvav 196,196,24
v_fmac_f32_e32 v64,v232,v160
v_fmac_f32_e32 v65,v232,v161
v_mul_f32_e32 v71,v232,v71
mvav 196,228,28
L_vs19:
s_cbranch_execz L_vs23
v_fmac_f32_e32 v66,v232,v162
v_fmac_f32_e32 v67,v232,v163
v_fmac_f32_e32 v68,v232,v164
v_fmac_f32_e32 v69,v232,v165
v_fmac_f32_e32 v70,v232,v166
mvav 196,68,8
la 24,250,50,2048
v_fmac_f32_e32 v71,v232,v167
cv 160,64,65
cv 161,66,67
cv 162,68,69
mvav 196,100,12
tv 130,224,34816
tv 132,224,36864
cv 163,70,71
mvav 200,200,24
tv 134,224,38912
tv 144,224,40960
v_mul_f32_e32 v72,v232,v72
v_mul_f32_e32 v73,v232,v73
v_mul_f32_e32 v74,v232,v74
mvav 200,232,28
buffer_store_dwordx4 v[160:163],v244,s[8:11],s14 offen offset:0 sc0 sc1
v_mul_f32_e32 v75,v232,v75
v_mul_f32_e32 v76,v232,v76
v_mul_f32_e32 v77,v232,v77
v_mul_f32_e32 v78,v232,v78
mvav 200,72,8
v_fmac_f32_e32 v72,v232,v168
v_fmac_f32_e32 v73,v232,v169
v_mul_f32_e32 v79,v232,v79
mvav 200,104,12
v_fmac_f32_e32 v74,v232,v170
v_fmac_f32_e32 v75,v232,v171
v_fmac_f32_e32 v76,v232,v172
v_fmac_f32_e32 v77,v232,v173
v_fmac_f32_e32 v78,v232,v174
mvav 204,204,24
la 56,250,50,3072
v_fmac_f32_e32 v79,v232,v175
cv 168,72,73
cv 169,74,75
cv 170,76,77
mvav 204,236,28
tv 146,224,43008
tv 148,224,45056
cv 171,78,79
mvav 204,76,8
tv 150,224,47104
v_mul_f32_e32 v80,v232,v80
v_mul_f32_e32 v81,v232,v81
v_mul_f32_e32 v82,v232,v82
mvav 204,108,12
buffer_store_dwordx4 v[168:171],v244,s[8:11],s14 offen offset:2048 sc0 sc1
v_mul_f32_e32 v83,v232,v83
v_mul_f32_e32 v84,v232,v84
v_mul_f32_e32 v85,v232,v85
v_mul_f32_e32 v86,v232,v86
mvav 208,208,24
v_mul_f32_e32 v87,v232,v87
v_fmac_f32_e32 v80,v232,v176
v_fmac_f32_e32 v81,v232,v177
v_fmac_f32_e32 v82,v232,v178
mvav 208,240,28
v_fmac_f32_e32 v83,v232,v179
v_fmac_f32_e32 v84,v232,v180
v_fmac_f32_e32 v85,v232,v181
cv 176,80,81
cv 177,82,83
mvav 208,80,8
la 48,250,50,1024
v_fmac_f32_e32 v86,v232,v182
v_fmac_f32_e32 v87,v232,v183
cv 178,84,85
cv 179,86,87
mvav 208,112,12
v_mul_f32_e32 v88,v232,v88
v_mul_f32_e32 v89,v232,v89
v_mul_f32_e32 v90,v232,v90
mvav 212,212,24
v_mul_f32_e32 v91,v232,v91
v_mul_f32_e32 v92,v232,v92
v_mul_f32_e32 v93,v232,v93
v_mul_f32_e32 v94,v232,v94
v_mul_f32_e32 v95,v232,v95
mvav 212,244,28
buffer_store_dwordx4 v[176:179],v244,s[8:11],s88 offen offset:0 sc0 sc1
v_fmac_f32_e32 v88,v232,v184
v_fmac_f32_e32 v89,v232,v185
v_fmac_f32_e32 v90,v232,v186
v_fmac_f32_e32 v91,v232,v187
mvav 212,84,8
cv 184,88,89
v_fmac_f32_e32 v92,v232,v188
cv 185,90,91
mvav 212,116,12
v_fmac_f32_e32 v93,v232,v189
v_fmac_f32_e32 v94,v232,v190
v_fmac_f32_e32 v95,v232,v191
cv 186,92,93
cv 187,94,95
mvav 216,216,24
la 40,252,50,3072
v_add_f32_e32 v156,v156,v157
v_add_f32_e32 v141,v156,v239
v_subrev_u32_e32 v237,0x23800000,v141
mvav 216,248,28
v_rcp_f32_e32 v141,v141
mvav 216,88,8
v_mul_f32_e32 v233,v241,v141
v_mul_f32_e32 v96,v233,v96
v_mul_f32_e32 v97,v233,v97
v_mul_f32_e32 v98,v233,v98
v_mul_f32_e32 v99,v233,v99
v_mul_f32_e32 v100,v233,v100
mvav 216,120,12
buffer_store_dwordx4 v[184:187],v244,s[8:11],s88 offen offset:2048 sc0 sc1
v_fmac_f32_e32 v96,v233,v192
v_fmac_f32_e32 v97,v233,v193
v_mul_f32_e32 v101,v233,v101
v_mul_f32_e32 v102,v233,v102
mvav 220,220,24
v_mul_f32_e32 v103,v233,v103
v_fmac_f32_e32 v98,v233,v194
v_fmac_f32_e32 v99,v233,v195
mvav 220,252,28
v_fmac_f32_e32 v100,v233,v196
v_fmac_f32_e32 v101,v233,v197
cv 192,96,97
cv 193,98,99
mvav 220,92,8
la 32,252,50,1024
v_fmac_f32_e32 v102,v233,v198
v_fmac_f32_e32 v103,v233,v199
cv 194,100,101
cv 195,102,103
mvav 220,124,12
v_xor_b32_e32 v225,32,v224
v_xor_b32_e32 v226,64,v224
s_waitcnt lgkmcnt(0)
zvva 0,128,128
tv 138,225,34816
tv 142,225,38912
tv 152,225,40960
s_waitcnt vmcnt(36)
mvva 0,132,132
tv 136,225,32768
tv 154,225,43008
mvva 0,144,136
tv 140,225,36864
tv 158,225,47104
mvva 0,148,140
tv 156,225,45056
v_mul_f32_e32 v104,v233,v104
v_mul_f32_e32 v105,v233,v105
v_mul_f32_e32 v106,v233,v106
zvva 4,148,156
buffer_store_dwordx4 v[192:195],v244,s[8:11],s14 offen offset:1024 sc0 sc1
v_mul_f32_e32 v107,v233,v107
v_mul_f32_e32 v108,v233,v108
v_mul_f32_e32 v109,v233,v109
v_mul_f32_e32 v110,v233,v110
mvva 4,144,152
v_fmac_f32_e32 v104,v233,v200
v_fmac_f32_e32 v105,v233,v201
v_fmac_f32_e32 v106,v233,v202
v_mul_f32_e32 v111,v233,v111
mvva 4,132,148
v_fmac_f32_e32 v107,v233,v203
v_fmac_f32_e32 v108,v233,v204
v_fmac_f32_e32 v109,v233,v205
cv 200,104,105
cv 201,106,107
mvva 4,128,144
v_fmac_f32_e32 v110,v233,v206
v_fmac_f32_e32 v111,v233,v207
cv 202,108,109
cv 203,110,111
s_waitcnt vmcnt(17)
zvva 8,128,160
v_cmp_lt_u32_e64 s[42:43],s48,v236
v_mul_f32_e32 v112,v233,v112
s_or_b64 s[40:41],s[40:41],s[42:43]
v_cmp_lt_u32_e64 s[42:43],s48,v237
mvva 8,132,164
s_or_b64 s[40:41],s[40:41],s[42:43]
v_mul_f32_e32 v113,v233,v113
v_mul_f32_e32 v114,v233,v114
v_mul_f32_e32 v115,v233,v115
mvva 8,144,168
dl 245,36,51,0
v_mul_f32_e32 v116,v233,v116
v_mul_f32_e32 v117,v233,v117
v_mul_f32_e32 v118,v233,v118
v_mul_f32_e32 v119,v233,v119
mvva 8,148,172
v_mul_f32_e32 v120,v233,v120
v_fmac_f32_e32 v112,v233,v208
v_fmac_f32_e32 v113,v233,v209
v_fmac_f32_e32 v114,v233,v210
v_fmac_f32_e32 v115,v233,v211
zvva 12,148,188
v_fmac_f32_e32 v116,v233,v212
v_fmac_f32_e32 v117,v233,v213
cv 208,112,113
cv 209,114,115
mvva 12,144,184
v_fmac_f32_e32 v118,v233,v214
v_fmac_f32_e32 v119,v233,v215
cv 210,116,117
cv 211,118,119
mvva 12,132,180
v_mul_f32_e32 v121,v233,v121
v_mul_f32_e32 v122,v233,v122
v_mul_f32_e32 v123,v233,v123
mvva 12,128,176
v_mul_f32_e32 v124,v233,v124
v_mul_f32_e32 v125,v233,v125
v_mul_f32_e32 v126,v233,v126
v_mul_f32_e32 v127,v233,v127
s_waitcnt lgkmcnt(0)
zvva 28,136,176
buffer_store_dwordx4 v[200:203],v244,s[8:11],s14 offen offset:3072 sc0 sc1
v_fmac_f32_e32 v120,v233,v216
v_fmac_f32_e32 v121,v233,v217
v_fmac_f32_e32 v122,v233,v218
v_fmac_f32_e32 v123,v233,v219
mvva 28,140,180
tv 144,226,40960
tv 128,226,32768
tv 130,226,34816
cv 216,120,121
cv 217,122,123
mvva 28,152,184
tv 132,226,36864
tv 134,226,38912
v_fmac_f32_e32 v124,v233,v220
v_fmac_f32_e32 v125,v233,v221
v_fmac_f32_e32 v126,v233,v222
mvva 28,156,188
tv 146,226,43008
v_fmac_f32_e32 v127,v233,v223
cv 218,124,125
cv 219,126,127
zvva 24,156,172
tv 148,226,45056
tv 150,226,47104
mvva 24,152,168
v_xor_b32_e32 v227,96,v224
v_add_u32_e32 v232,131072,v224
mvva 24,140,164
buffer_store_dwordx4 v[208:211],v244,s[8:11],s88 offen offset:1024 sc0 sc1
mvva 24,136,160
v_add_u32_e32 v233,131072,v225
v_add_u32_e32 v234,131072,v226
zvva 20,136,144
v_add_u32_e32 v235,131072,v227
v_mov_b32_e32 v242,83914752
mvva 20,140,148
v_add_u32_e32 v228,65536,v224
v_add_u32_e32 v229,65536,v225
mvva 20,152,152
v_add_u32_e32 v230,65536,v226
v_add_u32_e32 v231,65536,v227
mvva 20,156,156
v_and_b32_e32 v243,240,v244
v_lshlrev_b32_e32 v243,7,v243
zvva 16,156,140
buffer_store_dwordx4 v[216:219],v244,s[8:11],s88 offen offset:3072 sc0 sc1
s_cmp_lg_u64 s[40:41],0
s_cbranch_scc1 L_redo
L_redo_r4:
mvva 16,152,136
tv 156,227,45056
v_mov_b32_e32 v236,16256
mvva 16,140,132
tv 158,227,47104
v_and_b32_e32 v248,256,v244
mvva 16,136,128
tv 152,227,40960
v_lshrrev_b32_e32 v248,3,v248
s_waitcnt lgkmcnt(3)
zvva 32,128,128
tv 140,227,36864
v_add_u32_e32 v243,v243,v248
mvva 32,132,132
tv 136,227,32768
tv 138,227,34816
mvva 32,144,136
dl 246,36,51,1024
tv 142,227,38912
mvva 32,148,140
tv 154,227,43008
v_and_b32_e32 v248,512,v244
zvva 36,148,156
v_lshrrev_b32_e32 v248,5,v248
v_add_u32_e32 v243,v243,v248
mvva 36,144,152
v_and_b32_e32 v248,16,v244
v_lshlrev_b32_e32 v248,v248,v236
mvva 36,132,148
v_cndmask_b32_e64 v238,0,v248,s[74:75]
s_mov_b32 s74,0xc0000c
mvva 36,128,144
s_mov_b32 s75,0xc0000c00
v_mov_b32_e32 v240,0
v_cndmask_b32_e64 v239,0,v248,s[74:75]
zvva 40,128,160
dl 249,36,51,2048
mvva 40,132,164
v_mov_b32_e32 v241,0
v_mov_b32_e32 v236,0
mvva 40,144,168
v_mov_b32_e32 v237,0
mvva 40,148,172
zvva 44,148,188
mvva 44,144,184
mvva 44,132,180
dl 247,36,51,3072
s_add_u32 m0,s52,16384
s_xor_b32 s51,s65,0
mvva 44,128,176
tv 144,224,57344
s_waitcnt lgkmcnt(1)
zvva 60,136,176
tv 132,224,53248
tv 134,224,55296
mvva 60,140,180
tv 128,224,49152
tv 130,224,51200
mvva 60,152,184
mvva 60,156,188
tv 146,224,59392
zvva 56,156,172
dl 245,36,51,0
tv 148,224,61440
tv 150,224,63488
mvva 56,152,168
mvva 56,140,164
mvva 56,136,160
zvva 52,136,144
mvva 52,140,148
mvva 52,152,152
dl 246,36,51,1024
mvva 52,156,156
zvva 48,156,140
mvva 48,152,136
mvva 48,140,132
tv 156,225,61440
mvva 48,136,128
tv 152,225,57344
s_waitcnt lgkmcnt(2)
zvva 64,128,128
dl 249,36,51,2048
tv 140,225,53248
mvva 64,132,132
tv 136,225,49152
tv 138,225,51200
mvva 64,144,136
tv 142,225,55296
mvva 64,148,140
tv 154,225,59392
zvva 68,148,156
tv 158,225,63488
mvva 68,144,152
mvva 68,132,148
dl 247,36,51,3072
s_lshl_b32 s51,s33,5
s_add_u32 m0,s52,98304
s_xor_b32 s51,s64,s51
mvva 68,128,144
zvva 72,128,160
mvva 72,132,164
s_waitcnt vmcnt(18)
s_mov_b32 s90,0
mvva 72,144,168
s_xor_b32 s50,s67,0
mvva 72,148,172
zvva 76,148,188
dl 245,36,51,0
mvva 76,144,184
mvva 76,132,180
tv 148,226,61440
mvva 76,128,176
tv 144,226,57344
s_waitcnt lgkmcnt(2)
zvva 92,136,176
tv 132,226,53248
mvva 92,140,180
tv 128,226,49152
tv 130,226,51200
mvva 92,152,184
dl 246,36,51,1024
tv 134,226,55296
mvva 92,156,188
tv 146,226,59392
zvva 88,156,172
tv 150,226,63488
mvva 88,152,168
mvva 88,140,164
mvva 88,136,160
zvva 84,136,144
dl 249,36,51,2048
mvva 84,140,148
mvva 84,152,152
mvva 84,156,156
zvva 80,156,140
mvva 80,152,136
mvva 80,140,132
dl 247,36,51,3072
mvva 80,136,128
s_lshl_b32 s51,s33,5
tv 152,227,57344
s_add_u32 m0,s52,114688
s_waitcnt lgkmcnt(1)
zvva 96,128,128
s_xor_b32 s51,s65,s51
tv 140,227,53248
tv 142,227,55296
mvva 96,132,132
tv 136,227,49152
tv 138,227,51200
mvva 96,144,136
mvva 96,148,140
tv 154,227,59392
zvva 100,148,156
lv 164,252,50,0
tv 156,227,61440
tv 158,227,63488
mvva 100,144,152
mvva 100,132,148
mvva 100,128,144
zvva 104,128,160
mvva 104,132,164
mvva 104,144,168
lv 172,252,50,2048
mvva 104,148,172
zvva 108,148,188
mvva 108,144,184
mvva 108,132,180
mvva 108,128,176
s_barrier
s_waitcnt lgkmcnt(0)
zvva 124,136,176
lv 180,250,50,0
tv 132,232,4096
tv 144,232,8192
tv 146,232,10240
mvva 124,140,180
tv 128,232,0
tv 130,232,2048
mvva 124,152,184
tv 134,232,6144
mvva 124,156,188
tv 148,232,12288
tv 150,232,14336
zvva 120,156,172
mvva 120,152,168
mvva 120,140,164
lv 188,250,50,2048
mvva 120,136,160
zvva 116,136,144
mvva 116,140,148
mvva 116,152,152
mvva 116,156,156
zvva 112,156,140
lv 220,250,50,3072
mvva 112,152,136
mvva 112,140,132
tv 156,233,12288
mvva 112,136,128
tv 152,233,8192
s_waitcnt lgkmcnt(2)
mvva 0,128,4
tv 140,233,4096
mvva 0,132,12
tv 136,233,0
tv 138,233,2048
mvva 0,144,20
lv 212,250,50,1024
tv 142,233,6144
mvva 0,148,28
tv 154,233,10240
mvva 4,148,60
tv 158,233,14336
mvva 4,144,52
mvva 4,132,44
mvva 4,128,36
mvva 8,128,0
lv 204,252,50,3072
mvva 8,132,8
mvva 8,144,16
mvva 8,148,24
mvva 12,148,56
s_waitcnt vmcnt(27)
mvva 12,144,48
s_waitcnt vmcnt(25)
mvva 12,132,40
lv 196,252,50,1024
s_waitcnt vmcnt(24)
mvva 12,128,32
tv 144,234,8192
s_waitcnt lgkmcnt(1)
mvva 28,136,32
tv 132,234,4096
tv 134,234,6144
mvva 28,140,40
tv 128,234,0
tv 130,234,2048
mvva 28,152,48
mvva 28,156,56
tv 146,234,10240
mvva 24,156,24
dl 245,36,51,0
s_waitcnt vmcnt(20)
s_barrier
tv 148,234,12288
tv 150,234,14336
mvva 24,152,16
mvva 24,140,8
mvva 24,136,0
mvva 20,136,36
mvva 20,140,44
mvva 20,152,52
s_cmp_eq_u32 s12,0
s_cselect_b64 exec,1,0
buffer_store_dword v251,off,s[8:11],s61 sc0 sc1
s_mov_b64 exec,-1
mvva 20,156,60
mvva 16,156,28
mvva 16,152,20
mvva 16,140,12
tv 156,235,12288
mvva 16,136,4
tv 152,235,8192
s_waitcnt lgkmcnt(2)
mvva 32,128,4
dl 246,36,51,1024
tv 140,235,4096
mvva 32,132,12
tv 136,235,0
tv 138,235,2048
mvva 32,144,20
tv 142,235,6144
mvva 32,148,28
tv 154,235,10240
mvva 36,148,60
tv 158,235,14336
mvva 36,144,52
mvva 36,132,44
dl 249,36,51,2048
mvva 36,128,36
mvva 40,128,0
mvva 40,132,8
mvva 40,144,16
mvva 40,148,24
mvva 44,148,56
dl 247,36,51,3072
s_lshl_b32 s51,s34,5
s_add_u32 m0,s52,65536
s_xor_b32 s51,s64,s51
mvva 44,144,48
tv 148,232,28672
mvva 44,132,40
s_xor_b32 s53,s60,s33
tv 150,232,30720
mvva 44,128,32
tv 144,232,24576
s_waitcnt lgkmcnt(3)
mvva 60,136,32
tv 132,232,20480
mvva 60,140,40
tv 128,232,16384
tv 130,232,18432
mvva 60,152,48
dl 245,36,51,0
tv 134,232,22528
mvva 60,156,56
tv 146,232,26624
mvva 56,156,24
mvva 56,152,16
mvva 56,140,8
mvva 56,136,0
mvva 52,136,36
buffer_load_dword v253,v242,s[8:11],s53 offen sc1
mvva 52,140,44
mvva 52,152,52
mvva 52,156,60
mvva 48,156,28
mvva 48,152,20
mvva 48,140,12
dl 246,36,51,1024
mvva 48,136,4
tv 152,233,24576
s_waitcnt lgkmcnt(1)
mvva 64,128,4
tv 140,233,20480
tv 142,233,22528
mvva 64,132,12
tv 136,233,16384
tv 138,233,18432
mvva 64,144,20
L_vs20:
s_cbranch_execz L_vs24
mvva 64,148,28
tv 154,233,26624
mvva 68,148,60
dl 249,36,51,2048
tv 156,233,28672
tv 158,233,30720
mvva 68,144,52
mvva 68,132,44
mvva 68,128,36
mvva 72,128,0
mvva 72,132,8
mvva 72,144,16
dl 247,36,51,3072
s_lshl_b32 s51,s34,5
s_add_u32 m0,s52,81920
s_xor_b32 s51,s65,s51
mvva 72,148,24
mvva 76,148,56
mvva 76,144,48
mvva 76,132,40
tv 148,234,28672
mvva 76,128,32
tv 144,234,24576
s_waitcnt lgkmcnt(2)
mvva 92,136,32
dl 245,36,51,0
tv 132,234,20480
mvva 92,140,40
tv 128,234,16384
tv 130,234,18432
mvva 92,152,48
tv 134,234,22528
mvva 92,156,56
tv 146,234,26624
mvva 88,156,24
tv 150,234,30720
mvva 88,152,16
mvva 88,140,8
dl 246,36,51,1024
mvva 88,136,0
mvva 84,136,36
mvva 84,140,44
mvva 84,152,52
mvva 84,156,60
mvva 80,156,28
dl 249,36,51,2048
mvva 80,152,20
mvva 80,140,12
tv 156,235,28672
mvva 80,136,4
tv 152,235,24576
s_waitcnt lgkmcnt(2)
mvva 96,128,4
tv 140,235,20480
mvva 96,132,12
tv 136,235,16384
tv 138,235,18432
mvva 96,144,20
dl 247,36,51,3072
s_lshl_b32 s51,s35,5
s_add_u32 m0,s52,32768
s_xor_b32 s51,s64,s51
tv 142,235,22528
mvva 96,148,28
tv 154,235,26624
mvva 100,148,60
tv 158,235,30720
mvva 100,144,52
mvva 100,132,44
mvva 100,128,36
mvva 104,128,0
dl 245,36,51,0
mvva 104,132,8
mvva 104,144,16
mvva 104,148,24
mvva 108,148,56
mvva 108,144,48
mvva 108,132,40
dl 246,36,51,1024
mvva 108,128,32
s_waitcnt vmcnt(27)
s_barrier
s_waitcnt lgkmcnt(0)
mvva 124,136,32
tv 132,224,4096
tv 134,224,6144
tv 144,224,8192
mvva 124,140,40
)"
R"(tv 128,224,0
tv 130,224,2048
mvva 124,152,48
mvva 124,156,56
tv 146,224,10240
mvva 120,156,24
dl 249,36,51,2048
tv 148,224,12288
tv 150,224,14336
mvva 120,152,16
mvva 120,140,8
mvva 120,136,0
mvva 116,136,36
mvva 116,140,44
mvva 116,152,52
dl 247,36,51,3072
s_lshl_b32 s51,s35,5
s_add_u32 m0,s52,49152
s_xor_b32 s51,s65,s51
mvva 116,156,60
mvva 112,156,28
mvva 112,152,20
s_waitcnt vmcnt(11)
v_cmp_ne_u32_e32 vcc,4,v253
mvva 112,140,12
dl 245,36,51,0
s_cbranch_vccnz L_spa3e
L_spra3e:
mvva 112,136,4
s_xor_b32 s50,s67,s33
tv 152,225,8192
s_waitcnt lgkmcnt(1)
mvvv 0,128,164
tv 140,225,4096
tv 142,225,6144
mvvv 0,132,172
tv 136,225,0
tv 138,225,2048
mvvv 0,144,180
mvvv 0,148,188
dl 246,36,51,1024
tv 154,225,10240
mvvv 4,148,220
tv 156,225,12288
tv 158,225,14336
mvvv 4,144,212
mvvv 4,132,204
mvvv 4,128,196
mvvv 8,128,160
la 196,252,50,0
s_xor_b32 s53,s60,s34
mvvv 8,132,168
mvvv 8,144,176
mvvv 8,148,184
mvvv 12,148,216
mvvv 12,144,208
la 204,252,50,2048
mvvv 12,132,200
tv 148,226,12288
mvvv 12,128,192
tv 144,226,8192
s_waitcnt lgkmcnt(2)
mvvv 28,136,192
tv 132,226,4096
mvvv 28,140,200
tv 128,226,0
tv 130,226,2048
mvvv 28,152,208
buffer_load_dword v253,v242,s[8:11],s53 offen sc1
tv 134,226,6144
mvvv 28,156,216
tv 146,226,10240
mvvv 24,156,184
tv 150,226,14336
mvvv 24,152,176
mvvv 24,140,168
mvvv 24,136,160
la 212,250,50,0
mvvv 20,136,196
mvvv 20,140,204
mvvv 20,152,212
mvvv 20,156,220
la 220,250,50,2048
mvvv 16,156,188
mvvv 16,152,180
mvvv 16,140,172
tv 156,227,12288
mvvv 16,136,164
la 252,250,50,3072
tv 152,227,8192
s_waitcnt lgkmcnt(2)
mvvv 32,128,164
tv 140,227,4096
mvvv 32,132,172
tv 136,227,0
tv 138,227,2048
mvvv 32,144,180
tv 142,227,6144
mvvv 32,148,188
la 244,250,50,1024
tv 154,227,10240
mvvv 36,148,220
tv 158,227,14336
mvvv 36,144,212
mvvv 36,132,204
mvvv 36,128,196
la 236,252,50,3072
mvvv 40,128,160
mvvv 40,132,168
mvvv 40,144,176
mvvv 40,148,184
mvvv 44,148,216
la 228,252,50,1024
s_add_u32 s50,s50,s55
mvvv 44,144,208
mvvv 44,132,200
tv 148,224,28672
tv 150,224,30720
mvvv 44,128,192
s_waitcnt lgkmcnt(2)
mvvv 60,136,192
la 192,252,50,0
tv 132,224,20480
mvvv 60,140,200
tv 128,224,16384
tv 130,224,18432
mvvv 60,152,208
tv 134,224,22528
mvvv 60,156,216
tv 144,224,24576
tv 146,224,26624
mvvv 56,156,184
la 200,252,50,2048
mvvv 56,152,176
mvvv 56,140,168
mvvv 56,136,160
mvvv 52,136,196
la 208,250,50,0
mvvv 52,140,204
mvvv 52,152,212
mvvv 52,156,220
mvvv 48,156,188
la 216,250,50,2048
mvvv 48,152,180
mvvv 48,140,172
tv 156,225,28672
mvvv 48,136,164
tv 152,225,24576
s_waitcnt lgkmcnt(2)
mvvv 64,128,164
tv 140,225,20480
mvvv 64,132,172
la 248,250,50,3072
tv 136,225,16384
tv 138,225,18432
mvvv 64,144,180
tv 142,225,22528
mvvv 64,148,188
tv 154,225,26624
mvvv 68,148,220
tv 158,225,30720
mvvv 68,144,212
la 240,250,50,1024
mvvv 68,132,204
mvvv 68,128,196
mvvv 72,128,160
mvvv 72,132,168
la 232,252,50,3072
mvvv 72,144,176
mvvv 72,148,184
mvvv 76,148,216
mvvv 76,144,208
mvvv 76,132,200
la 224,252,50,1024
mvvv 76,128,192
tv 144,226,24576
s_waitcnt lgkmcnt(1)
mvvv 92,136,192
tv 132,226,20480
tv 134,226,22528
mvvv 92,140,200
tv 128,226,16384
tv 130,226,18432
mvvv 92,152,208
dl 249,36,51,2048
mvvv 92,156,216
tv 146,226,26624
mvvv 88,156,184
tv 148,226,28672
tv 150,226,30720
mvvv 88,152,176
mvvv 88,140,168
dl 247,36,51,3072
s_lshl_b32 s51,s98,5
s_xor_b32 s51,s64,s51
s_add_u32 m0,s52,131072
s_xor_b32 s51,s51,4194304
mvvv 88,136,160
s_waitcnt vmcnt(16)
v_cmp_ne_u32_e32 vcc,4,v253
mvvv 84,136,196
s_cbranch_vccnz L_spa4e
L_spra4e:
mvvv 84,140,204
s_xor_b32 s50,s67,s34
mvvv 84,152,212
dl 245,36,51,0
mvvv 84,156,220
mvvv 80,156,188
mvvv 80,152,180
mvvv 80,140,172
la 68,252,50,0
mvvv 80,136,164
tv 152,227,24576
s_waitcnt lgkmcnt(1)
mvvv 96,128,164
tv 140,227,20480
tv 142,227,22528
mvvv 96,132,172
tv 136,227,16384
tv 138,227,18432
mvvv 96,144,180
s_xor_b32 s53,s60,s35
mvvv 96,148,188
la 76,252,50,2048
tv 154,227,26624
mvvv 100,148,220
tv 156,227,28672
tv 158,227,30720
mvvv 100,144,212
mvvv 100,132,204
mvvv 100,128,196
la 84,250,50,0
mvvv 104,128,160
mvvv 104,132,168
mvvv 104,144,176
mvvv 104,148,184
buffer_load_dword v253,v242,s[8:11],s53 offen sc1
mvvv 108,148,216
mvvv 108,144,208
mvvv 108,132,200
mvvv 108,128,192
s_barrier
s_waitcnt lgkmcnt(0)
mvvv 124,136,192
la 92,250,50,2048
tv 132,228,36864
tv 144,228,40960
tv 146,228,43008
mvvv 124,140,200
tv 128,228,32768
tv 130,228,34816
mvvv 124,152,208
tv 134,228,38912
mvvv 124,156,216
tv 148,228,45056
tv 150,228,47104
mvvv 120,156,184
la 124,250,50,3072
mvvv 120,152,176
mvvv 120,140,168
mvvv 120,136,160
mvvv 116,136,196
la 116,250,50,1024
mvvv 116,140,204
mvvv 116,152,212
mvvv 116,156,220
mvvv 112,156,188
la 108,252,50,3072
mvvv 112,152,180
mvvv 112,140,172
tv 156,229,45056
tv 158,229,47104
mvvv 112,136,164
s_waitcnt lgkmcnt(2)
mvva 0,128,196
la 100,252,50,1024
s_add_u32 s50,s50,s55
tv 140,229,36864
mvva 0,132,204
tv 136,229,32768
tv 138,229,34816
s_waitcnt vmcnt(21)
mvva 0,144,212
tv 142,229,38912
mvva 0,148,220
tv 152,229,40960
tv 154,229,43008
mvva 4,148,252
mvva 4,144,244
la 64,252,50,0
mvva 4,132,236
s_waitcnt vmcnt(21)
mvva 4,128,228
s_waitcnt vmcnt(20)
mvva 8,128,192
s_waitcnt vmcnt(19)
mvva 8,132,200
la 72,252,50,2048
s_waitcnt vmcnt(19)
mvva 8,144,208
s_waitcnt vmcnt(18)
mvva 8,148,216
s_waitcnt vmcnt(17)
mvva 12,148,248
s_waitcnt vmcnt(16)
mvva 12,144,240
la 80,250,50,0
s_waitcnt vmcnt(16)
mvva 12,132,232
tv 148,230,45056
s_waitcnt vmcnt(15)
mvva 12,128,224
tv 144,230,40960
s_waitcnt lgkmcnt(2)
mvva 28,136,224
tv 132,230,36864
mvva 28,140,232
la 88,250,50,2048
tv 128,230,32768
tv 130,230,34816
mvva 28,152,240
tv 134,230,38912
mvva 28,156,248
tv 146,230,43008
mvva 24,156,216
tv 150,230,47104
mvva 24,152,208
la 120,250,50,3072
mvva 24,140,200
mvva 24,136,192
mvva 20,136,228
mvva 20,140,236
mvva 20,152,244
la 112,250,50,1024
mvva 20,156,252
mvva 16,156,220
mvva 16,152,212
mvva 16,140,204
la 104,252,50,3072
mvva 16,136,196
tv 152,231,40960
s_waitcnt lgkmcnt(1)
mvva 32,128,196
tv 140,231,36864
tv 142,231,38912
mvva 32,132,204
tv 136,231,32768
tv 138,231,34816
mvva 32,144,212
la 96,252,50,1024
mvva 32,148,220
tv 154,231,43008
mvva 36,148,252
tv 156,231,45056
tv 158,231,47104
mvva 36,144,244
mvva 36,132,236
mvva 36,128,228
dl 246,36,51,1024
mvva 40,128,192
mvva 40,132,200
mvva 40,144,208
mvva 40,148,216
dl 249,36,51,2048
mvva 44,148,248
mvva 44,144,240
mvva 44,132,232
tv 148,228,61440
mvva 44,128,224
dl 247,36,51,3072
s_lshl_b32 s51,s98,5
s_xor_b32 s51,s65,s51
s_add_u32 m0,s52,147456
s_xor_b32 s51,s51,4194304
s_waitcnt lgkmcnt(1)
mvva 60,136,224
tv 144,228,57344
tv 132,228,53248
mvva 60,140,232
tv 128,228,49152
tv 130,228,51200
s_waitcnt vmcnt(16)
v_cmp_ne_u32_e32 vcc,4,v253
mvva 60,152,240
s_cbranch_vccnz L_spa5e
L_spra5e:
tv 134,228,55296
s_xor_b32 s50,s67,s35
mvva 60,156,248
dl 245,36,51,0
tv 146,228,59392
mvva 56,156,216
tv 150,228,63488
mvva 56,152,208
mvva 56,140,200
mvva 56,136,192
la 132,252,50,0
mvva 52,136,228
mvva 52,140,236
mvva 52,152,244
mvva 52,156,252
mvva 48,156,220
la 140,252,50,2048
s_xor_b32 s53,s60,s98
s_xor_b32 s53,s53,512
mvva 48,152,212
tv 156,229,61440
mvva 48,140,204
tv 158,229,63488
mvva 48,136,196
tv 152,229,57344
s_waitcnt lgkmcnt(3)
mvva 64,128,196
la 148,250,50,0
tv 140,229,53248
mvva 64,132,204
tv 136,229,49152
tv 138,229,51200
mvva 64,144,212
tv 142,229,55296
mvva 64,148,220
tv 154,229,59392
mvva 68,148,252
buffer_load_dword v253,v242,s[8:11],s53 offen sc1
mvva 68,144,244
mvva 68,132,236
mvva 68,128,228
mvva 72,128,192
mvva 72,132,200
la 156,250,50,2048
mvva 72,144,208
mvva 72,148,216
mvva 76,148,248
mvva 76,144,240
la 188,250,50,3072
mvva 76,132,232
tv 148,230,61440
mvva 76,128,224
tv 144,230,57344
s_waitcnt lgkmcnt(2)
mvva 92,136,224
tv 132,230,53248
mvva 92,140,232
la 180,250,50,1024
tv 128,230,49152
tv 130,230,51200
mvva 92,152,240
tv 134,230,55296
mvva 92,156,248
tv 146,230,59392
mvva 88,156,216
tv 150,230,63488
mvva 88,152,208
L_vs21:
s_cbranch_execz L_vs17
la 172,252,50,3072
mvva 88,140,200
mvva 88,136,192
mvva 84,136,228
mvva 84,140,236
la 164,252,50,1024
s_add_u32 s50,s50,s55
mvva 84,152,244
mvva 84,156,252
mvva 80,156,220
mvva 80,152,212
mvva 80,140,204
la 128,252,50,0
mvva 80,136,196
tv 152,231,57344
s_waitcnt lgkmcnt(1)
mvva 96,128,196
tv 140,231,53248
tv 142,231,55296
mvva 96,132,204
tv 136,231,49152
tv 138,231,51200
mvva 96,144,212
la 136,252,50,2048
mvva 96,148,220
tv 154,231,59392
mvva 100,148,252
tv 156,231,61440
tv 158,231,63488
mvva 100,144,244
mvva 100,132,236
la 144,250,50,0
mvva 100,128,228
mvva 104,128,192
mvva 104,132,200
mvva 104,144,208
la 152,250,50,2048
mvva 104,148,216
mvva 108,148,248
mvva 108,144,240
mvva 108,132,232
la 184,250,50,3072
mvva 108,128,224
s_barrier
s_waitcnt lgkmcnt(0)
mvva 124,136,224
tv 132,228,4096
tv 134,228,6144
tv 144,228,8192
mvva 124,140,232
tv 128,228,0
tv 130,228,2048
mvva 124,152,240
mvva 124,156,248
la 176,250,50,1024
tv 146,228,10240
mvva 120,156,216
tv 148,228,12288
tv 150,228,14336
mvva 120,152,208
mvva 120,140,200
mvva 120,136,192
la 168,252,50,3072
mvva 116,136,228
mvva 116,140,236
mvva 116,152,244
mvva 116,156,252
la 160,252,50,1024
mvva 112,156,220
mvva 112,152,212
mvva 112,140,204
tv 156,229,12288
tv 158,229,14336
mvva 112,136,196
s_waitcnt lgkmcnt(2)
mvva 0,128,68
dl 246,36,51,1024
tv 140,229,4096
mvva 0,132,76
tv 136,229,0
tv 138,229,2048
mvva 0,144,84
tv 142,229,6144
s_waitcnt vmcnt(22)
mvva 0,148,92
tv 152,229,8192
tv 154,229,10240
mvva 4,148,124
dl 249,36,51,2048
mvva 4,144,116
mvva 4,132,108
mvva 4,128,100
mvva 8,128,64
dl 247,36,51,3072
s_lshl_b32 s51,s98,5
s_xor_b32 s51,s64,s51
s_add_u32 m0,s52,0
s_xor_b32 s51,s51,4456448
mvva 8,132,72
mvva 8,144,80
mvva 8,148,88
s_waitcnt vmcnt(16)
v_cmp_ne_u32_e32 vcc,4,v253
mvva 12,148,120
dl 245,36,51,0
s_cbranch_vccnz L_spa6e
L_spra6e:
mvva 12,144,112
s_xor_b32 s50,s67,s98
mvva 12,132,104
s_xor_b32 s50,s50,131072
tv 148,230,12288
tv 150,230,14336
mvva 12,128,96
s_waitcnt lgkmcnt(2)
mvva 28,136,96
dl 246,36,51,1024
tv 132,230,4096
mvva 28,140,104
tv 128,230,0
tv 130,230,2048
mvva 28,152,112
tv 134,230,6144
mvva 28,156,120
tv 144,230,8192
tv 146,230,10240
mvva 24,156,88
mvva 24,152,80
dl 249,36,51,2048
mvva 24,140,72
s_xor_b32 s53,s60,s98
mvva 24,136,64
s_xor_b32 s53,s53,8704
mvva 20,136,100
mvva 20,140,108
dl 247,36,51,3072
mvva 20,152,116
s_lshl_b32 s51,s98,5
s_add_u32 m0,s52,16384
s_xor_b32 s51,s65,s51
mvva 20,156,124
s_xor_b32 s51,s51,4456448
mvva 16,156,92
mvva 16,152,84
buffer_load_dword v253,v242,s[8:11],s53 offen sc1
mvva 16,140,76
tv 156,231,12288
mvva 16,136,68
tv 152,231,8192
s_waitcnt lgkmcnt(2)
mvva 32,128,68
tv 140,231,4096
mvva 32,132,76
la 4,252,50,0
tv 136,231,0
tv 138,231,2048
mvva 32,144,84
tv 142,231,6144
mvva 32,148,92
tv 154,231,10240
mvva 36,148,124
tv 158,231,14336
mvva 36,144,116
la 12,252,50,2048
mvva 36,132,108
mvva 36,128,100
mvva 40,128,64
mvva 40,132,72
mvva 40,144,80
la 20,250,50,0
mvva 40,148,88
mvva 44,148,120
mvva 44,144,112
mvva 44,132,104
la 28,250,50,2048
mvva 44,128,96
tv 144,228,24576
s_waitcnt lgkmcnt(1)
mvva 60,136,96
tv 132,228,20480
tv 134,228,22528
mvva 60,140,104
tv 128,228,16384
tv 130,228,18432
mvva 60,152,112
la 60,250,50,3072
mvva 60,156,120
tv 146,228,26624
mvva 56,156,88
tv 148,228,28672
tv 150,228,30720
mvva 56,152,80
mvva 56,140,72
mvva 56,136,64
la 52,250,50,1024
mvva 52,136,100
mvva 52,140,108
mvva 52,152,116
mvva 52,156,124
la 44,252,50,3072
mvva 48,156,92
mvva 48,152,84
mvva 48,140,76
tv 156,229,28672
mvva 48,136,68
la 36,252,50,1024
s_add_u32 s50,s50,s55
tv 152,229,24576
s_waitcnt lgkmcnt(2)
mvva 64,128,68
tv 140,229,20480
mvva 64,132,76
tv 136,229,16384
tv 138,229,18432
mvva 64,144,84
tv 142,229,22528
mvva 64,148,92
la 0,252,50,0
tv 154,229,26624
mvva 68,148,124
tv 158,229,30720
mvva 68,144,116
mvva 68,132,108
mvva 68,128,100
la 8,252,50,2048
mvva 72,128,64
mvva 72,132,72
mvva 72,144,80
mvva 72,148,88
mvva 76,148,120
la 16,250,50,0
mvva 76,144,112
mvva 76,132,104
tv 148,230,28672
tv 150,230,30720
mvva 76,128,96
s_waitcnt lgkmcnt(2)
mvva 92,136,96
la 24,250,50,2048
tv 132,230,20480
mvva 92,140,104
tv 128,230,16384
tv 130,230,18432
mvva 92,152,112
tv 134,230,22528
mvva 92,156,120
tv 144,230,24576
tv 146,230,26624
mvva 88,156,88
la 56,250,50,3072
mvva 88,152,80
mvva 88,140,72
mvva 88,136,64
mvva 84,136,100
mvva 84,140,108
la 48,250,50,1024
mvva 84,152,116
mvva 84,156,124
mvva 80,156,92
mvva 80,152,84
la 40,252,50,3072
mvva 80,140,76
tv 156,231,28672
mvva 80,136,68
tv 152,231,24576
s_waitcnt lgkmcnt(2)
mvva 96,128,68
tv 140,231,20480
mvva 96,132,76
la 32,252,50,1024
tv 136,231,16384
tv 138,231,18432
mvva 96,144,84
tv 142,231,22528
mvva 96,148,92
tv 154,231,26624
mvva 100,148,124
tv 158,231,30720
mvva 100,144,116
dl 245,36,51,0
s_waitcnt vmcnt(17)
v_cmp_ne_u32_e32 vcc,4,v253
mvva 100,132,108
s_cbranch_vccnz L_spa7e
L_spra7e:
mvva 100,128,100
s_xor_b32 s50,s67,s98
mvva 104,128,64
s_xor_b32 s50,s50,139264
mvva 104,132,72
dl 246,36,51,1024
mvva 104,144,80
mvva 104,148,88
mvva 108,148,120
mvva 108,144,112
mvva 108,132,104
lv 164,252,50,0
mvva 108,128,96
s_barrier
s_waitcnt lgkmcnt(0)
mvva 124,136,96
s_xor_b32 s53,s60,s98
tv 132,224,36864
tv 134,224,38912
tv 144,224,40960
s_xor_b32 s53,s53,16896
mvva 124,140,104
tv 128,224,32768
tv 130,224,34816
mvva 124,152,112
lv 172,252,50,2048
mvva 124,156,120
tv 146,224,43008
mvva 120,156,88
tv 148,224,45056
tv 150,224,47104
mvva 120,152,80
mvva 120,140,72
buffer_load_dword v253,v242,s[8:11],s53 offen sc1
mvva 120,136,64
mvva 116,136,100
mvva 116,140,108
mvva 116,152,116
lv 180,250,50,0
mvva 116,156,124
mvva 112,156,92
mvva 112,152,84
mvva 112,140,76
lv 188,250,50,2048
mvva 112,136,68
tv 152,225,40960
s_waitcnt lgkmcnt(1)
mvva 0,128,132
tv 140,225,36864
tv 142,225,38912
mvva 0,132,140
tv 136,225,32768
tv 138,225,34816
mvva 0,144,148
mvva 0,148,156
lv 220,250,50,3072
tv 154,225,43008
mvva 4,148,188
tv 156,225,45056
tv 158,225,47104
mvva 4,144,180
mvva 4,132,172
mvva 4,128,164
lv 212,250,50,1024
mvva 8,128,128
mvva 8,132,136
mvva 8,144,144
mvva 8,148,152
lv 204,252,50,3072
mvva 12,148,184
mvva 12,144,176
mvva 12,132,168
tv 148,226,45056
tv 150,226,47104
mvva 12,128,160
s_waitcnt lgkmcnt(2)
mvva 28,136,160
lv 196,252,50,1024
s_add_u32 s50,s50,s55
tv 132,226,36864
mvva 28,140,168
tv 128,226,32768
tv 130,226,34816
mvva 28,152,176
tv 134,226,38912
mvva 28,156,184
tv 144,226,40960
tv 146,226,43008
mvva 24,156,152
lv 160,252,50,0
mvva 24,152,144
mvva 24,140,136
mvva 24,136,128
mvva 20,136,164
lv 168,252,50,2048
mvva 20,140,172
mvva 20,152,180
mvva 20,156,188
mvva 16,156,156
lv 176,250,50,0
mvva 16,152,148
mvva 16,140,140
tv 156,227,45056
tv 158,227,47104
mvva 16,136,132
s_waitcnt lgkmcnt(2)
mvva 32,128,132
lv 184,250,50,2048
tv 140,227,36864
mvva 32,132,140
tv 136,227,32768
tv 138,227,34816
mvva 32,144,148
tv 142,227,38912
mvva 32,148,156
tv 152,227,40960
tv 154,227,43008
mvva 36,148,188
mvva 36,144,180
lv 216,250,50,3072
mvva 36,132,172
mvva 36,128,164
mvva 40,128,128
mvva 40,132,136
lv 208,250,50,1024
mvva 40,144,144
mvva 40,148,152
mvva 44,148,184
mvva 44,144,176
lv 200,252,50,3072
mvva 44,132,168
tv 148,224,61440
mvva 44,128,160
tv 144,224,57344
s_waitcnt lgkmcnt(2)
mvva 60,136,160
tv 132,224,53248
mvva 60,140,168
tv 128,224,49152
tv 130,224,51200
mvva 60,152,176
lv 192,252,50,1024
tv 134,224,55296
mvva 60,156,184
tv 146,224,59392
mvva 56,156,152
tv 150,224,63488
mvva 56,152,144
mvva 56,140,136
dl 249,36,51,2048
mvva 56,136,128
mvva 52,136,164
mvva 52,140,172
mvva 52,152,180
dl 247,36,51,3072
s_lshl_b32 s51,s98,5
s_xor_b32 s51,s64,s51
s_add_u32 m0,s52,98304
s_xor_b32 s51,s51,4718592
mvva 52,156,188
mvva 48,156,156
mvva 48,152,148
mvva 48,140,140
dl 245,36,51,0
mvva 48,136,132
tv 152,225,57344
s_waitcnt vmcnt(17)
v_cmp_ne_u32_e32 vcc,4,v253
s_waitcnt lgkmcnt(1)
mvva 64,128,132
s_cbranch_vccnz L_spa8e
L_spra8e:
tv 140,225,53248
tv 142,225,55296
mvva 64,132,140
s_xor_b32 s50,s67,s98
tv 136,225,49152
tv 138,225,51200
mvva 64,144,148
dl 246,36,51,1024
mvva 64,148,156
s_xor_b32 s50,s50,147456
tv 154,225,59392
mvva 68,148,188
tv 156,225,61440
tv 158,225,63488
mvva 68,144,180
mvva 68,132,172
mvva 68,128,164
dl 249,36,51,2048
mvva 72,128,128
mvva 72,132,136
mvva 72,144,144
s_xor_b32 s53,s60,s98
mvva 72,148,152
dl 247,36,51,3072
s_xor_b32 s53,s53,25088
mvva 76,148,184
s_lshl_b32 s51,s98,5
s_add_u32 m0,s52,114688
s_xor_b32 s51,s65,s51
mvva 76,144,176
s_xor_b32 s51,s51,4718592
mvva 76,132,168
tv 148,226,61440
mvva 76,128,160
buffer_load_dword v253,v242,s[8:11],s53 offen sc1
tv 144,226,57344
s_waitcnt lgkmcnt(2)
mvva 92,136,160
tv 132,226,53248
mvva 92,140,168
tv 128,226,49152
tv 130,226,51200
mvva 92,152,176
tv 134,226,55296
mvva 92,156,184
la 196,252,50,0
tv 146,226,59392
mvva 88,156,152
tv 150,226,63488
mvva 88,152,144
mvva 88,140,136
mvva 88,136,128
la 204,252,50,2048
mvva 84,136,164
mvva 84,140,172
mvva 84,152,180
mvva 84,156,188
mvva 80,156,156
la 212,250,50,0
mvva 80,152,148
mvva 80,140,140
tv 156,227,61440
tv 158,227,63488
mvva 80,136,132
s_waitcnt lgkmcnt(2)
mvva 96,128,132
la 220,250,50,2048
tv 140,227,53248
mvva 96,132,140
tv 136,227,49152
tv 138,227,51200
mvva 96,144,148
tv 142,227,55296
mvva 96,148,156
tv 152,227,57344
tv 154,227,59392
mvva 100,148,188
la 252,250,50,3072
mvva 100,144,180
mvva 100,132,172
L_vs22:
s_cbranch_execz L_vr1
mvva 100,128,164
mvva 104,128,128
mvva 104,132,136
la 244,250,50,1024
mvva 104,144,144
)"
R"(mvva 104,148,152
mvva 108,148,184
mvva 108,144,176
la 236,252,50,3072
mvva 108,132,168
mvva 108,128,160
s_barrier
s_waitcnt lgkmcnt(0)
mvva 124,136,160
tv 132,232,4096
tv 144,232,8192
mvva 124,140,168
la 228,252,50,1024
s_add_u32 s50,s50,s55
tv 128,232,0
tv 130,232,2048
mvva 124,152,176
tv 134,232,6144
mvva 124,156,184
tv 146,232,10240
mvva 120,156,152
tv 148,232,12288
tv 150,232,14336
mvva 120,152,144
la 192,252,50,0
mvva 120,140,136
mvva 120,136,128
mvva 116,136,164
mvva 116,140,172
la 200,252,50,2048
mvva 116,152,180
mvva 116,156,188
mvva 112,156,156
mvva 112,152,148
mvva 112,140,140
la 208,250,50,0
mvva 112,136,132
tv 152,233,8192
s_waitcnt lgkmcnt(1)
mvva 0,128,4
tv 140,233,4096
tv 142,233,6144
mvva 0,132,12
tv 136,233,0
tv 138,233,2048
mvva 0,144,20
la 216,250,50,2048
mvva 0,148,28
tv 154,233,10240
mvva 4,148,60
tv 156,233,12288
tv 158,233,14336
mvva 4,144,52
mvva 4,132,44
la 248,250,50,3072
mvva 4,128,36
mvva 8,128,0
mvva 8,132,8
mvva 8,144,16
la 240,250,50,1024
mvva 8,148,24
mvva 12,148,56
mvva 12,144,48
mvva 12,132,40
la 232,252,50,3072
mvva 12,128,32
tv 144,234,8192
s_waitcnt lgkmcnt(1)
mvva 28,136,32
tv 132,234,4096
tv 134,234,6144
mvva 28,140,40
tv 128,234,0
tv 130,234,2048
mvva 28,152,48
mvva 28,156,56
la 224,252,50,1024
tv 146,234,10240
mvva 24,156,24
tv 148,234,12288
tv 150,234,14336
mvva 24,152,16
mvva 24,140,8
mvva 24,136,0
dl 245,36,51,0
mvva 20,136,36
mvva 20,140,44
s_waitcnt vmcnt(17)
v_cmp_ne_u32_e32 vcc,4,v253
mvva 20,152,52
s_cbranch_vccnz L_spa9e
L_spra9e:
mvva 20,156,60
dl 246,36,51,1024
mvva 16,156,28
s_xor_b32 s50,s67,s98
mvva 16,152,20
s_xor_b32 s50,s50,155648
mvva 16,140,12
tv 156,235,12288
tv 158,235,14336
mvva 16,136,4
s_waitcnt lgkmcnt(2)
mvva 32,128,4
dl 249,36,51,2048
tv 140,235,4096
mvva 32,132,12
tv 136,235,0
tv 138,235,2048
mvva 32,144,20
tv 142,235,6144
mvva 32,148,28
tv 152,235,8192
tv 154,235,10240
mvva 36,148,60
dl 247,36,51,3072
s_lshl_b32 s51,s98,5
s_xor_b32 s51,s64,s51
s_add_u32 m0,s52,65536
s_xor_b32 s51,s51,4980736
mvva 36,144,52
s_xor_b32 s53,s60,s98
mvva 36,132,44
s_xor_b32 s53,s53,25216
mvva 36,128,36
mvva 40,128,0
dl 245,36,51,0
mvva 40,132,8
mvva 40,144,16
mvva 40,148,24
mvva 44,148,56
buffer_load_dword v253,v242,s[8:11],s53 offen sc1
mvva 44,144,48
mvva 44,132,40
tv 148,232,28672
tv 150,232,30720
mvva 44,128,32
s_waitcnt lgkmcnt(2)
mvva 60,136,32
dl 246,36,51,1024
tv 132,232,20480
mvva 60,140,40
tv 128,232,16384
tv 130,232,18432
mvva 60,152,48
tv 134,232,22528
mvva 60,156,56
tv 144,232,24576
tv 146,232,26624
mvva 56,156,24
mvva 56,152,16
dl 249,36,51,2048
mvva 56,140,8
mvva 56,136,0
mvva 52,136,36
mvva 52,140,44
dl 247,36,51,3072
s_lshl_b32 s51,s98,5
s_xor_b32 s51,s64,s51
s_add_u32 m0,s52,81920
s_xor_b32 s51,s51,6029312
mvva 52,152,52
mvva 52,156,60
mvva 48,156,28
mvva 48,152,20
dl 245,36,51,0
mvva 48,140,12
tv 156,233,28672
mvva 48,136,4
tv 152,233,24576
s_waitcnt lgkmcnt(2)
mvva 64,128,4
tv 140,233,20480
mvva 64,132,12
tv 136,233,16384
tv 138,233,18432
mvva 64,144,20
dl 246,36,51,1024
tv 142,233,22528
mvva 64,148,28
tv 154,233,26624
mvva 68,148,60
tv 158,233,30720
mvva 68,144,52
mvva 68,132,44
dl 249,36,51,2048
mvva 68,128,36
mvva 72,128,0
mvva 72,132,8
mvva 72,144,16
dl 247,36,51,3072
mvva 72,148,24
s_lshl_b32 s51,s98,5
s_add_u32 m0,s52,32768
s_xor_b32 s51,s65,s51
mvva 76,148,56
s_xor_b32 s51,s51,4980736
mvva 76,144,48
mvva 76,132,40
la 68,252,50,0
mvva 76,128,32
tv 144,224,8192
s_waitcnt lgkmcnt(1)
mvva 92,136,32
tv 132,224,4096
tv 134,224,6144
mvva 92,140,40
tv 128,224,0
tv 130,224,2048
mvva 92,152,48
la 76,252,50,2048
mvva 92,156,56
tv 146,224,10240
mvva 88,156,24
tv 148,224,12288
tv 150,224,14336
mvva 88,152,16
mvva 88,140,8
mvva 88,136,0
la 84,250,50,0
mvva 84,136,36
mvva 84,140,44
mvva 84,152,52
mvva 84,156,60
la 92,250,50,2048
mvva 80,156,28
mvva 80,152,20
mvva 80,140,12
tv 156,225,12288
mvva 80,136,4
la 124,250,50,3072
tv 152,225,8192
s_waitcnt lgkmcnt(2)
mvvv 0,128,164
tv 140,225,4096
mvvv 0,132,172
tv 136,225,0
tv 138,225,2048
mvvv 0,144,180
tv 142,225,6144
mvvv 0,148,188
la 116,250,50,1024
tv 154,225,10240
mvvv 4,148,220
tv 158,225,14336
mvvv 4,144,212
mvvv 4,132,204
mvvv 4,128,196
la 108,252,50,3072
mvvv 8,128,160
mvvv 8,132,168
mvvv 8,144,176
mvvv 8,148,184
mvvv 12,148,216
la 100,252,50,1024
s_add_u32 s50,s50,s55
mvvv 12,144,208
mvvv 12,132,200
tv 148,226,12288
tv 150,226,14336
mvvv 12,128,192
s_barrier
tv 144,226,8192
s_waitcnt lgkmcnt(3)
mvvv 28,136,192
la 64,252,50,0
mvvv 28,140,200
tv 128,226,0
tv 130,226,2048
mvvv 28,152,208
tv 132,226,4096
tv 134,226,6144
mvvv 28,156,216
tv 146,226,10240
s_waitcnt vmcnt(16)
v_cmp_ne_u32_e32 vcc,4,v253
mvvv 24,156,184
la 72,252,50,2048
s_cbranch_vccnz L_spa10e
L_spra10e:
mvvv 24,152,176
v_and_b32_e32 v242,48,v244
v_lshlrev_b32_e32 v242,3,v242
mvvv 24,140,168
mvvv 24,136,160
mvvv 20,136,196
mvvv 20,140,204
la 80,250,50,0
mvvv 20,152,212
mvvv 20,156,220
mvvv 16,156,188
s_andn2_b32 s53,s61,384
mvvv 16,152,180
la 88,250,50,2048
mvvv 16,140,172
tv 156,227,12288
mvvv 16,136,164
tv 152,227,8192
s_waitcnt lgkmcnt(2)
mvvv 32,128,164
tv 140,227,4096
mvvv 32,132,172
buffer_load_dword v253,v242,s[8:11],s53 offen sc1
tv 136,227,0
tv 138,227,2048
mvvv 32,144,180
tv 142,227,6144
mvvv 32,148,188
tv 154,227,10240
mvvv 36,148,220
tv 158,227,14336
mvvv 36,144,212
la 120,250,50,3072
mvvv 36,132,204
mvvv 36,128,196
mvvv 40,128,160
mvvv 40,132,168
la 112,250,50,1024
mvvv 40,144,176
mvvv 40,148,184
mvvv 44,148,216
mvvv 44,144,208
mvvv 44,132,200
la 104,252,50,3072
mvvv 44,128,192
tv 144,224,24576
s_waitcnt lgkmcnt(1)
mvvv 60,136,192
tv 132,224,20480
tv 134,224,22528
mvvv 60,140,200
tv 128,224,16384
tv 130,224,18432
mvvv 60,152,208
la 96,252,50,1024
s_xor_b32 s50,s67,s98
s_xor_b32 s50,s50,188416
mvvv 60,156,216
tv 146,224,26624
mvvv 56,156,184
tv 148,224,28672
tv 150,224,30720
mvvv 56,152,176
mvvv 56,140,168
la 132,252,50,0
mvvv 56,136,160
mvvv 52,136,196
mvvv 52,140,204
mvvv 52,152,212
la 140,252,50,2048
mvvv 52,156,220
mvvv 48,156,188
mvvv 48,152,180
mvvv 48,140,172
la 148,250,50,0
mvvv 48,136,164
tv 152,225,24576
s_waitcnt lgkmcnt(1)
mvvv 64,128,164
tv 140,225,20480
tv 142,225,22528
mvvv 64,132,172
tv 136,225,16384
tv 138,225,18432
mvvv 64,144,180
mvvv 64,148,188
la 156,250,50,2048
tv 154,225,26624
mvvv 68,148,220
tv 156,225,28672
tv 158,225,30720
mvvv 68,144,212
mvvv 68,132,204
mvvv 68,128,196
la 188,250,50,3072
mvvv 72,128,160
mvvv 72,132,168
mvvv 72,144,176
mvvv 72,148,184
la 180,250,50,1024
mvvv 76,148,216
mvvv 76,144,208
mvvv 76,132,200
tv 148,228,45056
tv 150,228,47104
mvvv 76,128,192
s_waitcnt lgkmcnt(2)
mvvv 92,136,192
la 172,252,50,3072
tv 132,228,36864
mvvv 92,140,200
tv 128,228,32768
tv 130,228,34816
mvvv 92,152,208
tv 134,228,38912
mvvv 92,156,216
tv 144,228,40960
tv 146,228,43008
mvvv 88,156,184
la 164,252,50,1024
s_add_u32 s50,s50,s55
mvvv 88,152,176
mvvv 88,140,168
mvvv 88,136,160
mvvv 84,136,196
la 128,252,50,0
mvvv 84,140,204
mvvv 84,152,212
mvvv 84,156,220
mvvv 80,156,188
la 136,252,50,2048
mvvv 80,152,180
mvvv 80,140,172
tv 156,229,45056
tv 158,229,47104
mvvv 80,136,164
s_waitcnt lgkmcnt(2)
mvva 0,128,196
la 144,250,50,0
tv 140,229,36864
mvva 0,132,204
tv 136,229,32768
tv 138,229,34816
mvva 0,144,212
tv 142,229,38912
mvva 0,148,220
tv 152,229,40960
tv 154,229,43008
mvva 4,148,252
mvva 4,144,244
la 152,250,50,2048
mvva 4,132,236
mvva 4,128,228
mvva 8,128,192
mvva 8,132,200
la 184,250,50,3072
mvva 8,144,208
mvva 8,148,216
s_waitcnt vmcnt(17)
v_cmp_ne_u32_e32 vcc,4,v253
mvva 12,148,248
s_cbranch_vccnz L_sppe
L_sprpe:
mvva 12,144,240
la 176,250,50,1024
mvva 12,132,232
tv 148,230,45056
mvva 12,128,224
tv 144,230,40960
s_waitcnt lgkmcnt(2)
mvva 28,136,224
tv 132,230,36864
mvva 28,140,232
tv 128,230,32768
tv 130,230,34816
mvva 28,152,240
la 168,252,50,3072
tv 134,230,38912
mvva 28,156,248
tv 146,230,43008
mvva 24,156,216
tv 150,230,47104
mvva 24,152,208
mvva 24,140,200
la 160,252,50,1024
mvva 24,136,192
mvva 20,136,228
mvva 20,140,236
mvva 20,152,244
dl 245,36,51,0
mvva 20,156,252
mvva 16,156,220
mvva 16,152,212
mvva 16,140,204
dl 246,36,51,1024
mvva 16,136,196
tv 152,231,40960
s_waitcnt lgkmcnt(1)
mvva 32,128,196
tv 140,231,36864
tv 142,231,38912
mvva 32,132,204
tv 136,231,32768
tv 138,231,34816
mvva 32,144,212
dl 249,36,51,2048
mvva 32,148,220
tv 154,231,43008
mvva 36,148,252
tv 156,231,45056
tv 158,231,47104
mvva 36,144,244
mvva 36,132,236
mvva 36,128,228
dl 247,36,51,3072
s_lshl_b32 s51,s98,5
s_xor_b32 s51,s65,s51
s_add_u32 m0,s52,49152
s_xor_b32 s51,s51,6029312
mvva 40,128,192
mvva 40,132,200
mvva 40,144,208
mvva 40,148,216
dl 245,36,51,0
mvva 44,148,248
mvva 44,144,240
mvva 44,132,232
mvva 44,128,224
dl 246,36,51,1024
s_waitcnt lgkmcnt(0)
mvva 60,136,224
s_barrier
mvva 60,140,232
tv 144,228,57344
tv 146,228,59392
tv 128,228,49152
tv 130,228,51200
mvva 60,152,240
tv 132,228,53248
tv 134,228,55296
mvva 60,156,248
dl 249,36,51,2048
mvva 56,156,216
tv 148,228,61440
tv 150,228,63488
mvva 56,152,208
mvva 56,140,200
mvva 56,136,192
dl 247,36,51,3072
mvva 52,136,228
mvva 52,140,236
mvva 52,152,244
L_vs23:
s_cbranch_execz L_vr1
mvva 52,156,252
mvva 48,156,220
mvva 48,152,212
mvva 48,140,204
tv 156,229,61440
mvva 48,136,196
tv 152,229,57344
s_waitcnt lgkmcnt(2)
mvva 64,128,196
tv 140,229,53248
mvva 64,132,204
tv 136,229,49152
tv 138,229,51200
mvva 64,144,212
tv 142,229,55296
mvva 64,148,220
tv 154,229,59392
mvva 68,148,252
tv 158,229,63488
mvva 68,144,244
mvva 68,132,236
mvva 68,128,228
mvva 72,128,192
mvva 72,132,200
mvva 72,144,208
mvva 72,148,216
mvva 76,148,248
mvva 76,144,240
mvva 76,132,232
tv 148,228,12288
mvva 76,128,224
tv 144,228,8192
s_waitcnt lgkmcnt(2)
mvva 92,136,224
tv 132,228,4096
mvva 92,140,232
tv 128,228,0
tv 130,228,2048
mvva 92,152,240
tv 134,228,6144
mvva 92,156,248
tv 146,228,10240
mvva 88,156,216
tv 150,228,14336
mvva 88,152,208
mvva 88,140,200
mvva 88,136,192
mvva 84,136,228
mvva 84,140,236
mvva 84,152,244
mvva 84,156,252
mvva 80,156,220
mvva 80,152,212
mvva 80,140,204
tv 156,229,12288
mvva 80,136,196
tv 152,229,8192
s_waitcnt lgkmcnt(2)
mvva 0,128,68
tv 140,229,4096
mvva 0,132,76
tv 136,229,0
tv 138,229,2048
mvva 0,144,84
tv 142,229,6144
mvva 0,148,92
tv 154,229,10240
mvva 4,148,124
tv 158,229,14336
mvva 4,144,116
mvva 4,132,108
mvva 4,128,100
mvva 8,128,64
mvva 8,132,72
mvva 8,144,80
mvva 8,148,88
s_waitcnt vmcnt(10)
mvva 12,148,120
mvva 12,144,112
mvva 12,132,104
tv 148,228,28672
mvva 12,128,96
tv 144,228,24576
s_waitcnt lgkmcnt(2)
mvva 28,136,96
tv 132,228,20480
mvva 28,140,104
tv 128,228,16384
tv 130,228,18432
mvva 28,152,112
tv 134,228,22528
mvva 28,156,120
tv 146,228,26624
mvva 24,156,88
tv 150,228,30720
mvva 24,152,80
mvva 24,140,72
mvva 24,136,64
mvva 20,136,100
mvva 20,140,108
mvva 20,152,116
mvva 20,156,124
mvva 16,156,92
mvva 16,152,84
mvva 16,140,76
tv 156,229,28672
mvva 16,136,68
tv 152,229,24576
s_waitcnt lgkmcnt(2)
mvva 0,128,132
tv 140,229,20480
mvva 0,132,140
tv 136,229,16384
tv 138,229,18432
mvva 0,144,148
tv 142,229,22528
mvva 0,148,156
tv 154,229,26624
mvva 4,148,188
tv 158,229,30720
mvva 4,144,180
mvva 4,132,172
mvva 4,128,164
mvva 8,128,128
mvva 8,132,136
cv 0,0,1
cv 1,2,3
mvva 8,144,144
mvva 8,148,152
mvva 12,148,184
mvva 12,144,176
cv 4,4,5
cv 5,6,7
s_waitcnt vmcnt(8)
mvva 12,132,168
tv 148,230,12288
mvva 12,128,160
tv 144,230,8192
cv 8,8,9
s_waitcnt lgkmcnt(2)
mvva 28,136,160
tv 132,230,4096
cv 9,10,11
mvva 28,140,168
tv 128,230,0
tv 130,230,2048
mvva 28,152,176
tv 134,230,6144
cv 12,12,13
mvva 28,156,184
tv 146,230,10240
cv 13,14,15
mvva 24,156,152
tv 150,230,14336
mvva 24,152,144
mvva 24,140,136
mvva 24,136,128
mvva 20,136,164
cv 14,28,29
cv 15,30,31
mvva 20,140,172
buffer_store_dwordx4 v[12:15],v244,s[8:11],s56 offen offset:3072 sc0 sc1
mvva 20,152,180
mvva 20,156,188
mvva 16,156,156
cv 10,24,25
cv 11,26,27
mvva 16,152,148
buffer_store_dwordx4 v[8:11],v244,s[8:11],s56 offen offset:2048 sc0 sc1
mvva 16,140,140
tv 156,231,12288
mvva 16,136,132
cv 6,20,21
cv 7,22,23
s_waitcnt lgkmcnt(1)
mvva 32,128,68
tv 140,231,4096
mvva 32,132,76
tv 136,231,0
tv 138,231,2048
mvva 32,144,84
buffer_store_dwordx4 v[4:7],v244,s[8:11],s56 offen offset:1024 sc0 sc1
tv 142,231,6144
mvva 32,148,92
tv 152,231,8192
tv 154,231,10240
mvva 36,148,124
tv 158,231,14336
cv 2,16,17
cv 3,18,19
mvva 36,144,116
mvva 36,132,108
buffer_store_dwordx4 v[0:3],v244,s[8:11],s56 offen offset:0 sc0 sc1
mvva 36,128,100
mvva 40,128,64
mvva 40,132,72
mvva 40,144,80
mvva 40,148,88
mvva 44,148,120
mvva 44,144,112
mvva 44,132,104
tv 148,230,28672
mvva 44,128,96
s_waitcnt vmcnt(4)
s_barrier
tv 144,230,24576
s_waitcnt lgkmcnt(2)
mvva 60,136,96
tv 132,230,20480
mvva 60,140,104
tv 128,230,16384
tv 130,230,18432
mvva 60,152,112
tv 134,230,22528
mvva 60,156,120
tv 146,230,26624
mvva 56,156,88
tv 150,230,30720
mvva 56,152,80
mvva 56,140,72
mvva 56,136,64
mvva 52,136,100
mvva 52,140,108
mvva 52,152,116
mvva 52,156,124
mvva 48,156,92
mvva 48,152,84
mvva 48,140,76
tv 156,231,28672
mvva 48,136,68
tv 152,231,24576
s_waitcnt lgkmcnt(2)
mvva 32,128,132
tv 140,231,20480
mvva 32,132,140
tv 136,231,16384
tv 138,231,18432
mvva 32,144,148
tv 142,231,22528
mvva 32,148,156
tv 154,231,26624
mvva 36,148,188
tv 158,231,30720
mvva 36,144,180
mvva 36,132,172
mvva 36,128,164
mvva 40,128,128
mvva 40,132,136
cv 32,32,33
cv 33,34,35
mvva 40,144,144
mvva 40,148,152
mvva 44,148,184
mvva 44,144,176
cv 36,36,37
cv 37,38,39
mvva 44,132,168
tv 148,224,45056
mvva 44,128,160
tv 144,224,40960
cv 40,40,41
s_waitcnt lgkmcnt(2)
mvva 60,136,160
tv 132,224,36864
cv 41,42,43
mvva 60,140,168
tv 128,224,32768
tv 130,224,34816
mvva 60,152,176
tv 134,224,38912
cv 44,44,45
mvva 60,156,184
tv 146,224,43008
cv 45,46,47
mvva 56,156,152
tv 150,224,47104
mvva 56,152,144
mvva 56,140,136
mvva 56,136,128
mvva 52,136,164
cv 46,60,61
cv 47,62,63
mvva 52,140,172
buffer_store_dwordx4 v[44:47],v244,s[8:11],s57 offen offset:3072 sc0 sc1
mvva 52,152,180
mvva 52,156,188
mvva 48,156,156
cv 42,56,57
cv 43,58,59
mvva 48,152,148
buffer_store_dwordx4 v[40:43],v244,s[8:11],s57 offen offset:2048 sc0 sc1
mvva 48,140,140
tv 156,225,45056
mvva 48,136,132
cv 38,52,53
cv 39,54,55
s_waitcnt lgkmcnt(1)
mvva 64,128,68
tv 140,225,36864
mvva 64,132,76
buffer_store_dwordx4 v[36:39],v244,s[8:11],s57 offen offset:1024 sc0 sc1
tv 136,225,32768
tv 138,225,34816
mvva 64,144,84
tv 142,225,38912
cv 34,48,49
mvva 64,148,92
tv 152,225,40960
tv 154,225,43008
mvva 68,148,124
tv 158,225,47104
cv 35,50,51
mvva 68,144,116
buffer_store_dwordx4 v[32:35],v244,s[8:11],s57 offen offset:0 sc0 sc1
mvva 68,132,108
mvva 68,128,100
mvva 72,128,64
mvva 72,132,72
mvva 72,144,80
mvva 72,148,88
mvva 76,148,120
mvva 76,144,112
mvva 76,132,104
tv 148,224,61440
mvva 76,128,96
tv 144,224,57344
s_waitcnt lgkmcnt(2)
mvva 92,136,96
tv 132,224,53248
mvva 92,140,104
tv 128,224,49152
tv 130,224,51200
mvva 92,152,112
tv 134,224,55296
mvva 92,156,120
tv 146,224,59392
mvva 88,156,88
tv 150,224,63488
mvva 88,152,80
mvva 88,140,72
mvva 88,136,64
mvva 84,136,100
mvva 84,140,108
mvva 84,152,116
mvva 84,156,124
mvva 80,156,92
mvva 80,152,84
mvva 80,140,76
tv 156,225,61440
mvva 80,136,68
tv 152,225,57344
s_waitcnt lgkmcnt(2)
mvva 64,128,132
tv 140,225,53248
mvva 64,132,140
tv 136,225,49152
tv 138,225,51200
mvva 64,144,148
tv 142,225,55296
mvva 64,148,156
tv 154,225,59392
mvva 68,148,188
tv 158,225,63488
mvva 68,144,180
mvva 68,132,172
mvva 68,128,164
s_xor_b32 s82,s59,12
mvva 72,128,128
s_mul_i32 s82,s82,12288
cv 64,64,65
s_add_u32 s82,s82,0
mvva 72,132,136
lv 16,244,82,0
mvva 72,144,144
cv 65,66,67
cv 68,68,69
mvva 72,148,152
cv 69,70,71
mvva 76,148,184
mvva 76,144,176
mvva 76,132,168
lv 20,244,82,1024
mvva 76,128,160
tv 144,234,24576
s_waitcnt lgkmcnt(1)
mvva 92,136,160
tv 132,234,20480
tv 134,234,22528
mvva 92,140,168
tv 128,234,16384
tv 130,234,18432
mvva 92,152,176
cv 72,72,73
cv 73,74,75
mvva 92,156,184
lv 24,244,82,2048
tv 146,234,26624
mvva 88,156,152
tv 148,234,28672
tv 150,234,30720
mvva 88,152,144
cv 76,76,77
cv 77,78,79
mvva 88,140,136
cv 78,92,93
cv 79,94,95
mvva 88,136,128
mvva 84,136,164
lv 28,244,82,3072
mvva 84,140,172
mvva 84,152,180
mvva 84,156,188
mvva 80,156,156
buffer_store_dwordx4 v[76:79],v244,s[8:11],s58 offen offset:3072 sc0 sc1
mvva 80,152,148
cv 74,88,89
cv 75,90,91
mvva 80,140,140
tv 156,235,28672
mvva 80,136,132
cv 70,84,85
cv 71,86,87
s_waitcnt lgkmcnt(1)
mvva 96,128,4
buffer_store_dwordx4 v[72:75],v244,s[8:11],s58 offen offset:2048 sc0 sc1
tv 140,235,20480
mvva 96,132,12
tv 136,235,16384
tv 138,235,18432
mvva 96,144,20
tv 142,235,22528
cv 66,80,81
mvva 96,148,28
tv 152,235,24576
tv 154,235,26624
mvva 100,148,60
tv 158,235,30720
cv 67,82,83
mvva 100,144,52
buffer_store_dwordx4 v[68:71],v244,s[8:11],s58 offen offset:1024 sc0 sc1
mvva 100,132,44
mvva 100,128,36
mvva 104,128,0
mvva 104,132,8
buffer_store_dwordx4 v[64:67],v244,s[8:11],s58 offen offset:0 sc0 sc1
mvva 104,144,16
mvva 104,148,24
mvva 108,148,56
mvva 108,144,48
mvva 108,132,40
tv 148,226,28672
mvva 108,128,32
tv 144,226,24576
s_waitcnt lgkmcnt(2)
mvva 124,136,32
tv 132,226,20480
mvva 124,140,40
tv 128,226,16384
tv 130,226,18432
mvva 124,152,48
tv 134,226,22528
mvva 124,156,56
tv 146,226,26624
mvva 120,156,24
tv 150,226,30720
mvva 120,152,16
)"
R"(mvva 120,140,8
mvva 120,136,0
mvva 116,136,36
mvva 116,140,44
s_waitcnt vmcnt(7)
v_max_u32_e32 v248,v16,v17
mvva 116,152,52
v_max3_u32 v248,v248,v18,v19
s_waitcnt vmcnt(6)
v_max3_u32 v248,v248,v20,v21
v_max3_u32 v248,v248,v22,v23
mvva 116,156,60
s_waitcnt vmcnt(5)
v_max3_u32 v248,v248,v24,v25
v_max3_u32 v248,v248,v26,v27
s_waitcnt vmcnt(4)
v_max3_u32 v248,v248,v28,v29
mvva 112,156,28
v_max3_u32 v248,v248,v30,v31
v_cmp_eq_u32_e32 vcc,-1,v248
mvva 112,152,20
s_cbranch_vccnz L_spx0e
L_sprx0e:
mvva 112,140,12
tv 156,227,28672
tv 158,227,30720
v_lshlrev_b32_e32 v251,16,v18
v_and_b32_e32 v255,4294901760,v18
mvva 112,136,4
tv 152,227,24576
tv 154,227,26624
s_waitcnt lgkmcnt(4)
mvvv 96,128,164
v_lshlrev_b32_e32 v253,16,v19
v_and_b32_e32 v242,4294901760,v19
mvvv 96,132,172
tv 136,227,16384
tv 138,227,18432
mvvv 96,144,180
tv 140,227,20480
tv 142,227,22528
v_add_f32_e32 v112,v112,v251
mvvv 96,148,188
v_add_f32_e32 v113,v113,v255
v_add_f32_e32 v114,v114,v253
mvvv 100,148,220
v_add_f32_e32 v115,v115,v242
v_lshlrev_b32_e32 v251,16,v22
mvvv 100,144,212
v_and_b32_e32 v255,4294901760,v22
v_lshlrev_b32_e32 v253,16,v23
mvvv 100,132,204
v_and_b32_e32 v242,4294901760,v23
v_add_f32_e32 v116,v116,v251
v_add_f32_e32 v117,v117,v255
mvvv 100,128,196
v_add_f32_e32 v118,v118,v253
v_add_f32_e32 v119,v119,v242
mvvv 104,128,160
v_lshlrev_b32_e32 v251,16,v26
v_and_b32_e32 v255,4294901760,v26
mvvv 104,132,168
v_lshlrev_b32_e32 v253,16,v27
v_and_b32_e32 v242,4294901760,v27
mvvv 104,144,176
v_add_f32_e32 v120,v120,v251
v_add_f32_e32 v121,v121,v255
v_add_f32_e32 v122,v122,v253
mvvv 104,148,184
v_add_f32_e32 v123,v123,v242
v_lshlrev_b32_e32 v251,16,v30
mvvv 108,148,216
L_vs24:
s_cbranch_execz L_vr1
v_and_b32_e32 v255,4294901760,v30
v_lshlrev_b32_e32 v253,16,v31
mvvv 108,144,208
v_and_b32_e32 v242,4294901760,v31
v_add_f32_e32 v124,v124,v251
mvvv 108,132,200
v_add_f32_e32 v125,v125,v255
v_add_f32_e32 v126,v126,v253
v_add_f32_e32 v127,v127,v242
mvvv 108,128,192
tv 144,230,57344
v_lshlrev_b32_e32 v251,16,v16
s_waitcnt lgkmcnt(1)
mvvv 124,136,192
tv 132,230,53248
v_and_b32_e32 v255,4294901760,v16
mvvv 124,140,200
tv 128,230,49152
tv 130,230,51200
mvvv 124,152,208
tv 134,230,55296
v_lshlrev_b32_e32 v253,16,v17
mvvv 124,156,216
tv 146,230,59392
v_and_b32_e32 v242,4294901760,v17
mvvv 120,156,184
tv 148,230,61440
tv 150,230,63488
mvvv 120,152,176
v_add_f32_e32 v96,v96,v251
v_add_f32_e32 v97,v97,v255
mvvv 120,140,168
v_add_f32_e32 v98,v98,v253
v_add_f32_e32 v99,v99,v242
mvvv 120,136,160
v_lshlrev_b32_e32 v251,16,v20
v_and_b32_e32 v255,4294901760,v20
mvvv 116,136,196
v_lshlrev_b32_e32 v253,16,v21
v_and_b32_e32 v242,4294901760,v21
mvvv 116,140,204
v_add_f32_e32 v100,v100,v251
v_add_f32_e32 v101,v101,v255
mvvv 116,152,212
v_add_f32_e32 v102,v102,v253
v_add_f32_e32 v103,v103,v242
mvvv 116,156,220
v_lshlrev_b32_e32 v251,16,v24
v_and_b32_e32 v255,4294901760,v24
mvvv 112,156,188
v_lshlrev_b32_e32 v253,16,v25
v_and_b32_e32 v242,4294901760,v25
mvvv 112,152,180
v_add_f32_e32 v104,v104,v251
v_add_f32_e32 v105,v105,v255
mvvv 112,140,172
tv 156,231,61440
v_add_f32_e32 v106,v106,v253
mvvv 112,136,164
tv 152,231,57344
v_add_f32_e32 v107,v107,v242
s_waitcnt lgkmcnt(2)
mvva 96,128,196
tv 140,231,53248
v_lshlrev_b32_e32 v251,16,v28
mvva 96,132,204
tv 136,231,49152
tv 138,231,51200
mvva 96,144,212
tv 142,231,55296
v_and_b32_e32 v255,4294901760,v28
mvva 96,148,220
tv 154,231,59392
v_lshlrev_b32_e32 v253,16,v29
mvva 100,148,252
tv 158,231,63488
v_and_b32_e32 v242,4294901760,v29
mvva 100,144,244
v_add_f32_e32 v108,v108,v251
v_add_f32_e32 v109,v109,v255
mvva 100,132,236
v_add_f32_e32 v110,v110,v253
v_add_f32_e32 v111,v111,v242
mvva 100,128,228
mvva 104,128,192
mvva 104,132,200
mvva 104,144,208
mvva 104,148,216
mvva 108,148,248
mvva 108,144,240
mvva 108,132,232
tv 148,226,45056
mvva 108,128,224
tv 144,226,40960
s_waitcnt lgkmcnt(2)
mvva 124,136,224
s_xor_b32 s83,s59,8
tv 132,226,36864
s_mul_i32 s83,s83,12288
mvva 124,140,232
s_add_u32 s83,s83,4096
tv 128,226,32768
tv 130,226,34816
mvva 124,152,240
lv 48,244,83,0
tv 134,226,38912
mvva 124,156,248
tv 146,226,43008
mvva 120,156,216
lv 52,244,83,1024
tv 150,226,47104
mvva 120,152,208
mvva 120,140,200
lv 56,244,83,2048
mvva 120,136,192
mvva 116,136,228
lv 60,244,83,3072
mvva 116,140,236
mvva 116,152,244
s_xor_b32 s84,s59,4
mvva 116,156,252
s_mul_i32 s84,s84,12288
mvva 112,156,220
s_add_u32 s84,s84,8192
mvva 112,152,212
lv 80,244,84,0
mvva 112,140,204
tv 156,227,45056
mvva 112,136,196
lv 84,244,84,1024
tv 152,227,40960
s_waitcnt lgkmcnt(2)
mvva 96,128,68
tv 140,227,36864
mvva 96,132,76
lv 88,244,84,2048
tv 136,227,32768
tv 138,227,34816
mvva 96,144,84
tv 142,227,38912
mvva 96,148,92
lv 92,244,84,3072
tv 154,227,43008
mvva 100,148,124
tv 158,227,47104
mvva 100,144,116
mvva 100,132,108
mvva 100,128,100
mvva 104,128,64
mvva 104,132,72
mvva 104,144,80
mvva 104,148,88
mvva 108,148,120
mvva 108,144,112
mvva 108,132,104
tv 148,226,61440
mvva 108,128,96
tv 144,226,57344
s_waitcnt lgkmcnt(2)
mvva 124,136,96
tv 132,226,53248
mvva 124,140,104
tv 128,226,49152
tv 130,226,51200
mvva 124,152,112
tv 134,226,55296
mvva 124,156,120
tv 146,226,59392
mvva 120,156,88
tv 150,226,63488
mvva 120,152,80
mvva 120,140,72
mvva 120,136,64
mvva 116,136,100
mvva 116,140,108
mvva 116,152,116
mvva 116,156,124
mvva 112,156,92
mvva 112,152,84
mvva 112,140,76
tv 156,227,61440
mvva 112,136,68
tv 152,227,57344
s_waitcnt lgkmcnt(2)
mvva 96,128,132
tv 140,227,53248
mvva 96,132,140
tv 136,227,49152
tv 138,227,51200
mvva 96,144,148
tv 142,227,55296
mvva 96,148,156
tv 154,227,59392
mvva 100,148,188
tv 158,227,63488
mvva 100,144,180
mvva 100,132,172
mvva 100,128,164
mvva 104,128,128
mvva 104,132,136
mvva 104,144,144
mvva 104,148,152
s_waitcnt vmcnt(7)
v_max_u32_e32 v248,v48,v49
mvva 108,148,184
v_max3_u32 v248,v248,v50,v51
s_waitcnt vmcnt(6)
v_max3_u32 v248,v248,v52,v53
mvva 108,144,176
v_max3_u32 v248,v248,v54,v55
s_waitcnt vmcnt(5)
v_max3_u32 v248,v248,v56,v57
mvva 108,132,168
v_max3_u32 v248,v248,v58,v59
s_waitcnt vmcnt(4)
v_max3_u32 v248,v248,v60,v61
mvva 108,128,160
v_max3_u32 v248,v248,v62,v63
v_cmp_eq_u32_e32 vcc,-1,v248
s_waitcnt lgkmcnt(0)
mvva 124,136,160
s_cbranch_vccnz L_spx1e
L_sprx1e:
mvva 124,140,168
mvva 124,152,176
mvva 124,156,184
s_waitcnt vmcnt(3)
v_max_u32_e32 v248,v80,v81
mvva 120,156,152
v_max3_u32 v248,v248,v82,v83
s_waitcnt vmcnt(2)
v_max3_u32 v248,v248,v84,v85
mvva 120,152,144
v_max3_u32 v248,v248,v86,v87
s_waitcnt vmcnt(1)
v_max3_u32 v248,v248,v88,v89
mvva 120,140,136
v_max3_u32 v248,v248,v90,v91
s_waitcnt vmcnt(0)
v_max3_u32 v248,v248,v92,v93
mvva 120,136,128
v_max3_u32 v248,v248,v94,v95
v_cmp_eq_u32_e32 vcc,-1,v248
mvva 116,136,164
s_cbranch_vccnz L_spx2e
L_sprx2e:
mvva 116,140,172
mvva 116,152,180
mvva 116,156,188
mvva 112,156,156
mvva 112,152,148
mvva 112,140,140
mvva 112,136,132
mvvv 108,238,60
mvvv 108,238,92
mvvv 124,236,60
mvvv 124,236,92
mvvv 104,238,56
mvvv 104,238,88
mvvv 120,236,56
mvvv 120,236,88
mvvv 100,238,52
mvvv 100,238,84
cv 108,108,109
cv 109,110,111
cv 110,124,125
cv 111,126,127
cv 104,104,105
cv 105,106,107
mvvv 116,236,52
v_permlane16_swap_b32_e32 v108,v110
v_permlane16_swap_b32_e32 v109,v111
cv 106,120,121
cv 107,122,123
mvvv 116,236,84
buffer_store_dwordx4 v[108:111],v243,s[44:47],s73 offen sc0 sc1
v_permlane16_swap_b32_e32 v104,v106
v_permlane16_swap_b32_e32 v105,v107
mvvv 96,238,48
cv 100,100,101
cv 101,102,103
mvvv 96,238,80
buffer_store_dwordx4 v[104:107],v243,s[44:47],s72 offen sc0 sc1
cv 102,116,117
cv 103,118,119
mvvv 112,236,48
v_permlane16_swap_b32_e32 v100,v102
v_permlane16_swap_b32_e32 v101,v103
mvvv 112,236,80
buffer_store_dwordx4 v[100:103],v243,s[44:47],s71 offen sc0 sc1
cv 96,96,97
cv 97,98,99
s_nop 4
cv 98,112,113
cv 99,114,115
s_nop 0
v_permlane16_swap_b32_e32 v96,v98
v_permlane16_swap_b32_e32 v97,v99
buffer_store_dwordx4 v[96:99],v243,s[44:47],s70 offen sc0 sc1
s_endpgm
L_odd:
L_vs25:
s_cbranch_execz L_vs29
s_lshl_b32 s86,s21,2
s_sub_u32 s87,s13,s67
s_add_u32 s86,s86,s12
mvaa 64,200,132
s_lshl_b32 s86,s86,8
v_add_u32_e32 v252,s87,v244
s_add_u32 s86,s86,L_odd-L_att_pc+1024
v_lshl_add_u32 v226,v244,3,s86
s_lshl_b32 s86,s12,1
s_bfe_u32 s87,s21,65539
mvaa 64,208,136
s_add_u32 s86,s86,s87
v_min_u32_e32 v226,L_att_end-L_att_pc-4,v226
s_lshl_b32 s86,s86,7
s_add_u32 s86,s86,L_pend2-L_att_pc
v_writelane_b32 v226,s86,2
mvaa 64,216,140
s_mov_b64 exec,7
global_load_dword v253,v226,s[96:97]
s_mov_b64 exec,-1
cvaa 68,220,140,232
s_add_u32 m0,s52,32768
v_and_b32_e32 v224,16,v244
v_lshlrev_b32_e32 v224,5,v224
mvaa 68,212,136
s_xor_b32 s51,s62,0
v_lshrrev_b32_e32 v239,4,v244
v_lshrrev_b32_e32 v236,3,v239
mvaa 68,204,132
s_lshl_b32 s87,s12,13
v_and_b32_e32 v237,112,v244
v_and_b32_e32 v238,48,v239
mvaa 68,196,128
s_xor_b32 s87,s87,8192
ra 64,225,0
v_lshlrev_b32_e32 v238,1,v238
cvaa 96,224,128,232
buffer_load_dword v253,v224,s[8:11],s53 offen sc1
mvaa 96,232,132
s_mov_b32 s84,7
ra 68,225,1024
v_xor_b32_e32 v237,v237,v238
mvaa 96,240,136
s_add_u32 s81,s81,1
ra 72,225,2048
v_lshl_add_u32 v245,v236,11,v237
mvaa 96,248,140
s_mov_b32 s84,5
v_add_u32_e32 v246,15360,v245
v_add_u32_e32 v249,30720,v245
cvaa 100,252,140,232
s_andn2_b32 s69,64,s54
ra 76,225,3072
s_waitcnt vmcnt(9)
v_add_u32_e32 v247,46080,v245
mvaa 100,244,136
dl 245,24,51,0
mvaa 100,236,132
s_add_u32 s88,s14,4096
ra 80,225,4096
cv 8,8,9
mvaa 100,228,128
s_lshl_b32 s35,s19,1
ra 84,225,5120
cv 9,10,11
cvaa 72,192,144,232
s_lshr_b32 s33,40569,s35
ra 88,225,6144
cv 10,12,13
mvaa 72,200,148
s_and_b32 s33,s33,3
ra 92,225,7168
v_add_u32_e32 v250,4096,v252
mvaa 72,208,152
dl 246,24,51,1024
mvaa 72,216,156
s_lshl_b32 s33,s33,13
ra 96,225,8192
v_mov_b32_e32 v251,4
cvaa 76,220,156,232
s_lshr_b32 s34,59294,s35
ra 100,225,9216
s_and_b32 s34,s34,3
mvaa 76,212,152
s_lshl_b32 s34,s34,13
ra 104,225,10240
s_xor_b32 s35,s33,s34
mvaa 76,204,148
s_and_b32 s98,s19,3
ra 108,225,11264
s_lshl_b32 s98,s98,13
mvaa 76,196,144
dl 249,24,51,2048
cvaa 104,224,144,232
s_mov_b32 s74,0x300003
ra 112,225,12288
s_mov_b32 s75,0x30000300
mvaa 104,232,148
s_lshl_b32 s71,s12,15
ra 116,225,13312
s_add_u32 s71,s71,s54
mvaa 104,240,152
s_mov_b64 s[44:45],s[30:31]
ra 120,225,14336
s_mov_b32 s46,4194304
mvaa 104,248,156
s_mov_b32 s47,131072
ex 14
cvaa 108,252,156,232
dl 247,24,51,3072
mvaa 108,244,152
s_add_u32 m0,s52,49152
s_xor_b32 s51,s63,0
ra 124,225,15360
mvaa 108,236,148
s_lshl_b32 s70,s23,11
ex 15
mvaa 108,228,144
s_add_u32 s70,s70,s71
ex 40
cvaa 80,192,160,232
ex 41
mvaa 80,200,164
dl 245,24,51,0
mvaa 80,208,168
ex 42
mvaa 80,216,172
ex 43
cvaa 84,220,172,232
ex 44
mvaa 84,212,168
ex 45
mvaa 84,204,164
dl 246,24,51,1024
mvaa 84,196,160
ex 46
cvaa 112,224,160,232
ex 47
mvaa 112,232,164
ex 16
mvaa 112,240,168
ex 17
mvaa 112,248,172
dl 249,24,51,2048
cvaa 116,252,172,232
ex 18
mvaa 116,244,168
ex 19
mvaa 116,236,164
ex 20
mvaa 116,228,160
ex 21
cvaa 88,192,176,232
dl 247,24,51,3072
s_waitcnt vmcnt(8)
v_cmp_ne_u32_e32 vcc,4,v253
mvaa 88,200,180
s_cbranch_vccnz L_spbo
L_sprbo:
ex 22
mvaa 88,208,184
s_xor_b32 s50,s66,262144
ex 23
mvaa 88,216,188
ex 48
cvaa 92,220,188,232
ex 49
mvaa 92,212,184
la 128,252,50,0
mvaa 92,204,180
s_waitcnt lgkmcnt(0)
s_barrier
mvaa 92,196,176
s_mov_b32 m0,s72
ex 50
cvaa 120,224,176,232
s_lshl_b32 s72,s22,11
ex 51
mvaa 120,232,180
s_add_u32 s72,s72,s71
ex 52
mvaa 120,240,184
ds 255,8,76,0
mvaa 120,248,188
s_add_u32 s71,s70,131072
ex 53
cvaa 124,252,188,232
ex 54
mvaa 124,244,184
ex 55
mvaa 124,236,180
ex 24
mvaa 124,228,176
ds 255,8,76,1024
maav 0,64,0
s_cmp_lg_u32 s19,7
s_cbranch_scc1 L_j7o
s_nop 1
s_lshl_b32 s86,s12,12
s_add_u32 s86,s86,131072
v_add_u32_e32 v236,s86,v244
ra 160,236,0
ra 164,236,1024
ra 168,236,2048
ra 172,236,3072
ra 176,236,16384
ra 180,236,17408
ra 184,236,18432
ra 188,236,19456
v_add_u32_e32 v224,s94,v224
s_waitcnt lgkmcnt(0)
L_j7o:
ex 25
maav 0,96,4
ex 26
maav 4,68,0
ex 27
maav 4,100,4
cv 11,14,15
cv 12,40,41
maav 8,72,0
ds 255,8,76,2048
maav 8,104,4
cv 13,42,43
cv 14,44,45
maav 12,76,0
cv 15,46,47
cv 16,16,17
maav 12,108,4
ex 28
v_smfmac_f32_16x16x64_bf16 v[144:147],v[228:231],v[0:7],v248
cv 17,18,19
maav 16,80,0
ds 255,8,76,3072
maav 16,112,4
s_mov_b32 m0,s73
ex 29
maav 20,84,0
s_add_u32 s73,s72,131072
ex 30
maav 20,116,4
ex 31
maav 24,88,0
ex 56
maav 24,120,4
ds 255,8,77,0
maav 28,92,0
ex 57
maav 28,124,4
ex 58
maav 32,64,8
ex 59
maav 32,96,12
ex 60
maav 36,68,8
ds 255,8,77,1024
maav 36,100,12
cv 18,20,21
cv 19,22,23
maav 40,72,8
cv 20,48,49
cv 21,50,51
maav 40,104,12
cv 22,52,53
cv 23,54,55
maav 44,76,8
ex 61
maav 44,108,12
ds 255,8,77,2048
v_smfmac_f32_16x16x64_bf16 v[148:151],v[228:231],v[8:15],v248
cv 24,24,25
maav 48,80,8
ex 62
maav 48,112,12
ex 63
maav 52,84,8
ra 192,244,0
cv 25,26,27
maav 52,116,12
ds 255,8,77,3072
maav 56,88,8
cv 26,28,29
cv 27,30,31
maav 56,120,12
cv 28,56,57
cv 29,58,59
maav 60,92,8
cv 30,60,61
cv 31,62,63
maav 60,124,12
ex 64
mvav 160,64,16
la 132,252,50,2048
mvav 160,96,20
L_vs26:
s_cbranch_execz L_vs30
ex 65
mvav 164,68,16
ex 66
mvav 164,100,20
ex 67
mvav 168,72,16
ex 68
mvav 168,104,20
la 136,250,50,0
mvav 172,76,16
ex 69
mvav 172,108,20
ex 70
v_smfmac_f32_16x16x64_bf16 v[152:155],v[228:231],v[16:23],v248
ra 200,244,2048
mvav 176,80,16
ra 208,244,4096
cv 64,64,65
mvav 176,112,20
la 140,250,50,2048
mvav 180,84,16
ra 216,244,6144
cv 65,66,67
mvav 180,116,20
ex 71
mvav 184,88,16
ex 96
mvav 184,120,20
s_barrier
ex 97
mvav 188,92,16
la 156,250,50,3072
mvav 188,124,20
s_add_u32 m0,s52,131072
ra 196,244,1024
s_xor_b32 s51,s62,1048576
mvav 192,64,24
ra 204,244,3072
cv 66,68,69
mvav 192,96,28
ra 212,244,5120
cv 67,70,71
mvav 196,68,24
ra 220,244,7168
cv 68,96,97
mvav 196,100,28
la 152,250,50,1024
mvav 200,72,24
ex 98
mvav 200,104,28
ex 99
mvav 204,76,24
ra 224,244,8192
cv 69,98,99
mvav 204,108,28
ra 232,244,10240
v_add_u32_e32 v6,s87,v244
v_smfmac_f32_16x16x64_bf16 v[156:159],v[228:231],v[24:31],v248
ra 240,244,12288
mvav 208,80,24
la 148,252,50,3072
mvav 208,112,28
s_xor_b32 s87,s52,4096
ra 248,244,14336
v_add_u32_e32 v7,98304,v6
mvav 212,84,24
s_add_u32 s87,s87,150528
ra 252,244,15360
v_add_u32_e32 v5,65536,v244
mvav 212,116,28
ra 244,244,13312
v_add_u32_e32 v32,s87,v244
mvav 216,88,24
ra 228,244,9216
v_and_b32_e32 v224,960,v244
mvav 216,120,28
la 144,252,50,1024
mvav 220,92,24
s_add_u32 s50,s50,s55
ra 236,244,11264
mvav 220,124,28
ra 64,225,32768
s_waitcnt lgkmcnt(8)
cvaa 24,192,176,232
dl 245,24,51,0
mvaa 24,200,180
ra 96,225,40960
mvaa 24,208,184
ra 68,225,33792
mvaa 24,216,188
dl 246,24,51,1024
cvaa 16,192,160,128
ra 100,225,41984
mvaa 16,200,164
ra 72,225,34816
mvaa 16,208,168
dl 249,24,51,2048
mvaa 16,216,172
ra 104,225,43008
cvaa 28,196,176,232
ra 76,225,35840
mvaa 28,204,180
dl 247,24,51,3072
mvaa 28,212,184
s_add_u32 m0,s52,147456
s_xor_b32 s51,s63,1048576
ra 108,225,44032
mvaa 28,220,188
ra 80,225,36864
cvaa 20,196,160,132
dl 245,24,51,0
mvaa 20,204,164
ra 112,225,45056
mvaa 20,212,168
ra 192,244,16384
mvaa 20,220,172
dl 246,24,51,1024
cvaa 56,224,176,232
ra 200,244,18432
s_waitcnt lgkmcnt(7)
mvaa 56,232,180
ra 208,244,20480
mvaa 56,240,184
dl 249,24,51,2048
mvaa 56,248,188
ra 216,244,22528
cvaa 48,224,160,136
ra 196,244,17408
mvaa 48,232,164
ra 204,244,19456
mvaa 48,240,168
ra 212,244,21504
mvaa 48,248,172
ra 220,244,23552
cvaa 60,228,176,232
ra 224,244,24576
mvaa 60,236,180
ra 232,244,26624
mvaa 60,244,184
ra 240,244,28672
mvaa 60,252,188
ra 248,244,30720
cvaa 52,228,160,140
ra 84,225,37888
mvaa 52,236,164
ra 116,225,46080
mvaa 52,244,168
la 160,252,50,0
ra 228,244,25600
mvaa 52,252,172
ra 236,244,27648
s_waitcnt lgkmcnt(10)
cvaa 8,192,176,128
ra 244,244,29696
mvaa 8,200,180
la 164,252,50,2048
ra 252,244,31744
mvaa 8,208,184
ra 88,225,38912
mvaa 8,216,188
ra 120,225,47104
cvaa 12,196,176,132
la 168,250,50,0
mvaa 12,204,180
ra 92,225,39936
ra 124,225,48128
s_waitcnt lgkmcnt(9)
mvaa 12,212,184
ex 100
mvaa 12,220,188
la 172,250,50,2048
cvaa 40,224,176,136
ex 101
mvaa 40,232,180
ex 102
mvaa 40,240,184
ex 103
mvaa 40,248,188
cv 70,100,101
cv 71,102,103
s_waitcnt lgkmcnt(4)
cvaa 44,228,176,140
ex 72
mvaa 44,236,180
s_waitcnt vmcnt(18) lgkmcnt(0)
s_barrier
ex 73
mvaa 44,244,184
la 176,252,50,1024
mvaa 44,252,188
ex 74
maav 0,64,64
ex 75
maav 0,96,68
la 188,250,50,3072
maav 4,68,64
ra 192,5,0
cv 72,72,73
maav 4,100,68
ex 76
maav 8,72,64
la 184,250,50,1024
maav 8,104,68
ds_write_b128 v6,a[0:3] offset:0
maav 12,76,64
ex 77
maav 12,108,68
la 180,252,50,3072
cv 73,74,75
ex 78
v_smfmac_f32_16x16x64_bf16 v[144:147],v[228:231],v[64:71],v248
ds_write_b128 v6,a[4:7] offset:1024
maav 16,80,64
cv 74,76,77
ex 79
ex 104
maav 16,112,68
ds_write_b128 v6,a[8:11] offset:2048
cv 75,78,79
maav 20,84,64
ex 105
ex 106
maav 20,116,68
ds_write_b128 v6,a[12:15] offset:3072
cv 76,104,105
maav 24,88,64
ex 107
ex 108
cv 77,106,107
maav 24,120,68
ex 109
ex 110
maav 28,92,64
ds_write_b128 v6,a[16:19] offset:4096
cv 78,108,109
maav 28,124,68
ex 111
ex 80
cv 79,110,111
maav 32,64,72
ex 81
ex 82
maav 32,96,76
ds_write_b128 v6,a[20:23] offset:5120
maav 36,68,72
ex 83
ex 84
cv 80,80,81
maav 36,100,76
ex 85
ex 86
maav 40,72,72
ds_write_b128 v6,a[24:27] offset:6144
cv 81,82,83
maav 40,104,76
ex 87
ex 112
cv 82,84,85
maav 44,76,72
ex 113
ex 114
maav 44,108,76
ds_write_b128 v6,a[28:31] offset:7168
cv 83,86,87
v_smfmac_f32_16x16x64_bf16 v[148:151],v[228:231],v[72:79],v248
ex 115
ex 116
maav 48,80,72
ex 117
ex 118
maav 48,112,76
ds_write_b128 v7,a[32:35] offset:0
maav 52,84,72
ex 119
cv 84,112,113
ex 88
maav 52,116,76
ra 196,5,1024
ex 89
maav 56,88,72
ds_write_b128 v7,a[36:39] offset:1024
cv 85,114,115
maav 56,120,76
ex 90
cv 86,116,117
ex 91
maav 60,92,72
ra 200,5,2048
ra 204,5,3072
ex 92
maav 60,124,76
ds_write_b128 v7,a[40:43] offset:2048
cv 87,118,119
mvav 160,64,80
ex 93
cv 88,88,89
ex 94
mvav 160,96,84
ra 208,5,4096
ra 212,5,5120
ex 95
mvav 164,68,80
ra 216,5,6144
ex 120
ex 121
mvav 164,100,84
ds_write_b128 v7,a[44:47] offset:3072
v_add_f32_e32 v130,v144,v145
mvav 168,72,80
ex 122
v_add_f32_e32 v131,v148,v149
mvav 168,104,84
ds_write_b128 v7,a[48:51] offset:4096
mvav 172,76,80
cv 89,90,91
ex 123
ex 124
mvav 172,108,84
ds_write_b128 v32,v[128:131]
v_smfmac_f32_16x16x64_bf16 v[152:155],v[228:231],v[80:87],v248
cv 90,92,93
ex 125
mvav 176,80,80
ds_write_b128 v7,a[52:55] offset:5120
cv 91,94,95
mvav 176,112,84
ra 220,5,7168
)"
R"(ra 224,5,8192
ex 126
ex 127
mvav 180,84,80
ra 232,5,10240
ra 240,5,12288
cv 92,120,121
ex 24
mvav 180,116,84
ds_write_b128 v7,a[56:59] offset:6144
cv 93,122,123
mvav 184,88,80
ex 25
ex 26
mvav 184,120,84
ex 16
ex 27
mvav 188,92,80
ds_write_b128 v7,a[60:63] offset:7168
cv 94,124,125
mvav 188,124,84
ra 248,5,14336
ra 228,5,9216
ra 236,5,11264
ex 17
cv 95,126,127
mvav 192,64,88
ra 244,5,13312
ra 252,5,15360
ex 18
ex 19
mvav 192,96,92
ex 28
cv 16,16,17
ex 29
mvav 196,68,88
ex 30
ex 31
v_xor_b32_e32 v6,8192,v6
v_xor_b32_e32 v7,8192,v7
mvav 196,100,92
s_waitcnt lgkmcnt(5)
s_xor_b32 s87,s87,0
mvav 200,72,88
s_xor_b32 s50,s66,294912
s_barrier
ra 64,5,16384
ra 96,5,24576
rv 64,6,0
rv 68,6,1024
rv 72,6,2048
mvav 200,104,92
la 4,252,50,0
rv 76,6,3072
rv 80,6,4096
rv 84,6,5120
rv 96,7,0
mvav 204,76,88
rv 100,7,1024
ex 20
ex 21
cv 17,18,19
mvav 204,108,92
rv 104,7,2048
rv 108,7,3072
rv 112,7,4096
rv 116,7,5120
ex 22
v_smfmac_f32_16x16x64_bf16 v[156:159],v[228:231],v[88:95],v248
rv 120,7,6144
rv 124,7,7168
ex 23
mvav 208,80,88
la 12,252,50,2048
ex 48
ex 49
mvav 208,112,92
ra 68,5,17408
ra 100,5,25600
ra 72,5,18432
cv 18,20,21
mvav 212,84,88
ra 104,5,26624
ra 76,5,19456
cv 19,22,23
ex 50
mvav 212,116,92
la 20,250,50,0
ra 108,5,27648
cv 20,48,49
ex 51
mvav 216,88,88
ra 80,5,20480
ra 112,5,28672
ra 84,5,21504
cv 21,50,51
mvav 216,120,92
ra 116,5,29696
ex 52
ex 53
mvav 220,92,88
la 28,250,50,2048
ex 54
ex 55
mvav 220,124,92
ra 88,5,22528
ra 120,5,30720
cv 22,52,53
cv 23,54,55
s_bitcmp1_b32 s82,s81
s_cbranch_scc1 L_pend2
L_ret_eb5:
mvav 160,192,16
la 60,250,50,3072
ra 92,5,23552
rv 88,6,6144
ra 124,5,31744
rv 92,6,7168
mvav 160,224,20
ex 56
ex 57
v_smfmac_f32_16x16x64_bf16 v[152:155],v[228:231],v[16:23],v248
v_xor_b32_e32 v32,4096,v32
mvav 164,196,16
la 52,250,50,1024
ds_read_b128 v[236:239],v32
ex 58
s_waitcnt lgkmcnt(7)
mvav 164,228,20
ex 59
ex 60
ex 61
mvav 168,200,16
cv 24,24,25
ex 62
mvav 168,232,20
la 44,252,50,3072
cv 25,26,27
ex 63
mvav 172,204,16
ex 8
cv 26,28,29
ex 9
mvav 172,236,20
s_waitcnt lgkmcnt(0)
s_barrier
ex 10
cv 27,30,31
mvav 176,208,16
la 36,252,50,1024
ex 11
cv 28,56,57
mvav 176,240,20
s_add_u32 m0,s52,147456
ex 12
ex 13
s_xor_b32 s51,s63,1048576
cv 29,58,59
mvav 180,212,16
s_add_u32 s50,s50,s55
ex 14
ex 15
cv 30,60,61
mvav 180,244,20
dl 247,24,51,3072
ex 40
cv 31,62,63
mvav 184,216,16
s_add_u32 m0,s52,0
ex 41
ex 42
cv 8,8,9
mvav 184,248,20
s_xor_b32 s51,s64,0
ex 43
ex 44
cv 9,10,11
mvav 188,220,16
la 0,252,50,0
ex 45
cv 10,12,13
mvav 188,252,20
ex 46
ex 47
cv 11,14,15
cv 12,40,41
v_smfmac_f32_16x16x64_bf16 v[156:159],v[228:231],v[24:31],v248
cv 13,42,43
cv 14,44,45
cv 15,46,47
v_bfe_u32 v225,v244,1,5
s_nop 0
v_smfmac_f32_16x16x64_bf16 v[156:159],v[228:231],v[8:15],v248
v_lshl_add_u32 v224,v224,1,v225
v_bfe_u32 v225,v244,7,2
v_lshl_add_u32 v224,v225,5,v224
mvav 192,192,24
la 8,252,50,2048
v_xor_b32_e32 v224,s69,v224
v_add_f32_e32 v152,v152,v153
v_add_f32_e32 v137,v152,v238
v_subrev_u32_e32 v236,0x23800000,v137
mvav 192,224,28
v_rcp_f32_e32 v137,v137
mvav 192,64,8
tv 128,224,32768
v_mul_f32_e32 v232,v240,v137
v_mul_f32_e32 v64,v232,v64
v_mul_f32_e32 v65,v232,v65
v_mul_f32_e32 v66,v232,v66
mvav 192,96,12
la 16,250,50,0
v_mul_f32_e32 v67,v232,v67
v_mul_f32_e32 v68,v232,v68
v_mul_f32_e32 v69,v232,v69
v_mul_f32_e32 v70,v232,v70
mvav 196,196,24
v_fmac_f32_e32 v64,v232,v160
v_fmac_f32_e32 v65,v232,v161
v_mul_f32_e32 v71,v232,v71
mvav 196,228,28
v_fmac_f32_e32 v66,v232,v162
v_fmac_f32_e32 v67,v232,v163
v_fmac_f32_e32 v68,v232,v164
v_fmac_f32_e32 v69,v232,v165
v_fmac_f32_e32 v70,v232,v166
mvav 196,68,8
la 24,250,50,2048
v_fmac_f32_e32 v71,v232,v167
cv 160,64,65
cv 161,66,67
cv 162,68,69
mvav 196,100,12
tv 130,224,34816
tv 132,224,36864
cv 163,70,71
mvav 200,200,24
tv 134,224,38912
tv 144,224,40960
v_mul_f32_e32 v72,v232,v72
v_mul_f32_e32 v73,v232,v73
v_mul_f32_e32 v74,v232,v74
mvav 200,232,28
buffer_store_dwordx4 v[160:163],v244,s[8:11],s14 offen offset:0 sc0 sc1
v_mul_f32_e32 v75,v232,v75
v_mul_f32_e32 v76,v232,v76
v_mul_f32_e32 v77,v232,v77
v_mul_f32_e32 v78,v232,v78
mvav 200,72,8
v_fmac_f32_e32 v72,v232,v168
v_fmac_f32_e32 v73,v232,v169
v_mul_f32_e32 v79,v232,v79
mvav 200,104,12
v_fmac_f32_e32 v74,v232,v170
v_fmac_f32_e32 v75,v232,v171
v_fmac_f32_e32 v76,v232,v172
v_fmac_f32_e32 v77,v232,v173
v_fmac_f32_e32 v78,v232,v174
mvav 204,204,24
la 56,250,50,3072
v_fmac_f32_e32 v79,v232,v175
cv 168,72,73
cv 169,74,75
cv 170,76,77
mvav 204,236,28
tv 146,224,43008
tv 148,224,45056
cv 171,78,79
mvav 204,76,8
tv 150,224,47104
v_mul_f32_e32 v80,v232,v80
v_mul_f32_e32 v81,v232,v81
v_mul_f32_e32 v82,v232,v82
mvav 204,108,12
L_vs27:
s_cbranch_execz L_vs31
buffer_store_dwordx4 v[168:171],v244,s[8:11],s14 offen offset:2048 sc0 sc1
v_mul_f32_e32 v83,v232,v83
v_mul_f32_e32 v84,v232,v84
v_mul_f32_e32 v85,v232,v85
v_mul_f32_e32 v86,v232,v86
mvav 208,208,24
v_mul_f32_e32 v87,v232,v87
v_fmac_f32_e32 v80,v232,v176
v_fmac_f32_e32 v81,v232,v177
v_fmac_f32_e32 v82,v232,v178
mvav 208,240,28
v_fmac_f32_e32 v83,v232,v179
v_fmac_f32_e32 v84,v232,v180
v_fmac_f32_e32 v85,v232,v181
cv 176,80,81
cv 177,82,83
mvav 208,80,8
la 48,250,50,1024
v_fmac_f32_e32 v86,v232,v182
v_fmac_f32_e32 v87,v232,v183
cv 178,84,85
cv 179,86,87
mvav 208,112,12
v_mul_f32_e32 v88,v232,v88
v_mul_f32_e32 v89,v232,v89
v_mul_f32_e32 v90,v232,v90
mvav 212,212,24
v_mul_f32_e32 v91,v232,v91
v_mul_f32_e32 v92,v232,v92
v_mul_f32_e32 v93,v232,v93
v_mul_f32_e32 v94,v232,v94
v_mul_f32_e32 v95,v232,v95
mvav 212,244,28
buffer_store_dwordx4 v[176:179],v244,s[8:11],s88 offen offset:0 sc0 sc1
v_fmac_f32_e32 v88,v232,v184
v_fmac_f32_e32 v89,v232,v185
v_fmac_f32_e32 v90,v232,v186
v_fmac_f32_e32 v91,v232,v187
mvav 212,84,8
cv 184,88,89
v_fmac_f32_e32 v92,v232,v188
cv 185,90,91
mvav 212,116,12
v_fmac_f32_e32 v93,v232,v189
v_fmac_f32_e32 v94,v232,v190
v_fmac_f32_e32 v95,v232,v191
cv 186,92,93
cv 187,94,95
mvav 216,216,24
la 40,252,50,3072
v_add_f32_e32 v156,v156,v157
v_add_f32_e32 v141,v156,v239
v_subrev_u32_e32 v237,0x23800000,v141
mvav 216,248,28
v_rcp_f32_e32 v141,v141
mvav 216,88,8
v_mul_f32_e32 v233,v241,v141
v_mul_f32_e32 v96,v233,v96
v_mul_f32_e32 v97,v233,v97
v_mul_f32_e32 v98,v233,v98
v_mul_f32_e32 v99,v233,v99
v_mul_f32_e32 v100,v233,v100
mvav 216,120,12
buffer_store_dwordx4 v[184:187],v244,s[8:11],s88 offen offset:2048 sc0 sc1
v_fmac_f32_e32 v96,v233,v192
v_fmac_f32_e32 v97,v233,v193
v_mul_f32_e32 v101,v233,v101
v_mul_f32_e32 v102,v233,v102
mvav 220,220,24
v_mul_f32_e32 v103,v233,v103
v_fmac_f32_e32 v98,v233,v194
v_fmac_f32_e32 v99,v233,v195
mvav 220,252,28
v_fmac_f32_e32 v100,v233,v196
v_fmac_f32_e32 v101,v233,v197
cv 192,96,97
cv 193,98,99
mvav 220,92,8
la 32,252,50,1024
v_fmac_f32_e32 v102,v233,v198
v_fmac_f32_e32 v103,v233,v199
cv 194,100,101
cv 195,102,103
mvav 220,124,12
v_xor_b32_e32 v225,32,v224
v_xor_b32_e32 v226,64,v224
s_waitcnt lgkmcnt(0)
zvva 0,128,128
tv 138,225,34816
tv 142,225,38912
tv 152,225,40960
s_waitcnt vmcnt(36)
mvva 0,132,132
tv 136,225,32768
tv 154,225,43008
mvva 0,144,136
tv 140,225,36864
tv 158,225,47104
mvva 0,148,140
tv 156,225,45056
v_mul_f32_e32 v104,v233,v104
v_mul_f32_e32 v105,v233,v105
v_mul_f32_e32 v106,v233,v106
zvva 4,148,156
buffer_store_dwordx4 v[192:195],v244,s[8:11],s14 offen offset:1024 sc0 sc1
v_mul_f32_e32 v107,v233,v107
v_mul_f32_e32 v108,v233,v108
v_mul_f32_e32 v109,v233,v109
v_mul_f32_e32 v110,v233,v110
mvva 4,144,152
v_fmac_f32_e32 v104,v233,v200
v_fmac_f32_e32 v105,v233,v201
v_fmac_f32_e32 v106,v233,v202
v_mul_f32_e32 v111,v233,v111
mvva 4,132,148
v_fmac_f32_e32 v107,v233,v203
v_fmac_f32_e32 v108,v233,v204
v_fmac_f32_e32 v109,v233,v205
cv 200,104,105
cv 201,106,107
mvva 4,128,144
v_fmac_f32_e32 v110,v233,v206
v_fmac_f32_e32 v111,v233,v207
cv 202,108,109
cv 203,110,111
s_waitcnt vmcnt(17)
zvva 8,128,160
v_cmp_lt_u32_e64 s[42:43],s48,v236
v_mul_f32_e32 v112,v233,v112
s_or_b64 s[40:41],s[40:41],s[42:43]
v_cmp_lt_u32_e64 s[42:43],s48,v237
mvva 8,132,164
s_or_b64 s[40:41],s[40:41],s[42:43]
v_mul_f32_e32 v113,v233,v113
v_mul_f32_e32 v114,v233,v114
v_mul_f32_e32 v115,v233,v115
mvva 8,144,168
dl 245,36,51,0
v_mul_f32_e32 v116,v233,v116
v_mul_f32_e32 v117,v233,v117
v_mul_f32_e32 v118,v233,v118
v_mul_f32_e32 v119,v233,v119
mvva 8,148,172
v_mul_f32_e32 v120,v233,v120
v_fmac_f32_e32 v112,v233,v208
v_fmac_f32_e32 v113,v233,v209
v_fmac_f32_e32 v114,v233,v210
v_fmac_f32_e32 v115,v233,v211
zvva 12,148,188
v_fmac_f32_e32 v116,v233,v212
v_fmac_f32_e32 v117,v233,v213
cv 208,112,113
cv 209,114,115
mvva 12,144,184
v_fmac_f32_e32 v118,v233,v214
v_fmac_f32_e32 v119,v233,v215
cv 210,116,117
cv 211,118,119
mvva 12,132,180
v_mul_f32_e32 v121,v233,v121
v_mul_f32_e32 v122,v233,v122
v_mul_f32_e32 v123,v233,v123
mvva 12,128,176
v_mul_f32_e32 v124,v233,v124
v_mul_f32_e32 v125,v233,v125
v_mul_f32_e32 v126,v233,v126
v_mul_f32_e32 v127,v233,v127
s_waitcnt lgkmcnt(0)
zvva 28,136,176
buffer_store_dwordx4 v[200:203],v244,s[8:11],s14 offen offset:3072 sc0 sc1
v_fmac_f32_e32 v120,v233,v216
v_fmac_f32_e32 v121,v233,v217
v_fmac_f32_e32 v122,v233,v218
v_fmac_f32_e32 v123,v233,v219
mvva 28,140,180
tv 144,226,40960
tv 128,226,32768
tv 130,226,34816
cv 216,120,121
cv 217,122,123
mvva 28,152,184
tv 132,226,36864
tv 134,226,38912
v_fmac_f32_e32 v124,v233,v220
v_fmac_f32_e32 v125,v233,v221
v_fmac_f32_e32 v126,v233,v222
mvva 28,156,188
tv 146,226,43008
v_fmac_f32_e32 v127,v233,v223
cv 218,124,125
cv 219,126,127
zvva 24,156,172
tv 148,226,45056
tv 150,226,47104
mvva 24,152,168
v_xor_b32_e32 v227,96,v224
v_add_u32_e32 v232,131072,v224
mvva 24,140,164
buffer_store_dwordx4 v[208:211],v244,s[8:11],s88 offen offset:1024 sc0 sc1
mvva 24,136,160
v_add_u32_e32 v233,131072,v225
v_add_u32_e32 v234,131072,v226
zvva 20,136,144
v_add_u32_e32 v235,131072,v227
v_mov_b32_e32 v242,83914752
mvva 20,140,148
v_add_u32_e32 v228,65536,v224
v_add_u32_e32 v229,65536,v225
mvva 20,152,152
v_add_u32_e32 v230,65536,v226
v_add_u32_e32 v231,65536,v227
mvva 20,156,156
v_and_b32_e32 v243,240,v244
v_lshlrev_b32_e32 v243,7,v243
zvva 16,156,140
buffer_store_dwordx4 v[216:219],v244,s[8:11],s88 offen offset:3072 sc0 sc1
s_cmp_lg_u64 s[40:41],0
s_cbranch_scc1 L_redo
L_redo_r5:
mvva 16,152,136
tv 156,227,45056
v_mov_b32_e32 v236,16256
mvva 16,140,132
tv 158,227,47104
v_and_b32_e32 v248,256,v244
mvva 16,136,128
tv 152,227,40960
v_lshrrev_b32_e32 v248,3,v248
s_waitcnt lgkmcnt(3)
zvva 32,128,128
tv 140,227,36864
v_add_u32_e32 v243,v243,v248
mvva 32,132,132
tv 136,227,32768
tv 138,227,34816
mvva 32,144,136
dl 246,36,51,1024
tv 142,227,38912
mvva 32,148,140
tv 154,227,43008
v_and_b32_e32 v248,512,v244
zvva 36,148,156
v_lshrrev_b32_e32 v248,5,v248
v_add_u32_e32 v243,v243,v248
mvva 36,144,152
v_and_b32_e32 v248,16,v244
v_lshlrev_b32_e32 v248,v248,v236
mvva 36,132,148
v_cndmask_b32_e64 v238,0,v248,s[74:75]
s_mov_b32 s74,0xc0000c
mvva 36,128,144
s_mov_b32 s75,0xc0000c00
v_mov_b32_e32 v240,0
v_cndmask_b32_e64 v239,0,v248,s[74:75]
zvva 40,128,160
dl 249,36,51,2048
mvva 40,132,164
v_mov_b32_e32 v241,0
v_mov_b32_e32 v236,0
mvva 40,144,168
v_mov_b32_e32 v237,0
mvva 40,148,172
zvva 44,148,188
mvva 44,144,184
mvva 44,132,180
dl 247,36,51,3072
s_add_u32 m0,s52,16384
s_xor_b32 s51,s65,0
mvva 44,128,176
tv 144,224,57344
s_waitcnt lgkmcnt(1)
zvva 60,136,176
tv 132,224,53248
tv 134,224,55296
mvva 60,140,180
tv 128,224,49152
tv 130,224,51200
mvva 60,152,184
mvva 60,156,188
tv 146,224,59392
zvva 56,156,172
dl 245,36,51,0
tv 148,224,61440
tv 150,224,63488
mvva 56,152,168
mvva 56,140,164
mvva 56,136,160
zvva 52,136,144
mvva 52,140,148
mvva 52,152,152
dl 246,36,51,1024
mvva 52,156,156
zvva 48,156,140
mvva 48,152,136
mvva 48,140,132
tv 156,225,61440
mvva 48,136,128
tv 152,225,57344
s_waitcnt lgkmcnt(2)
zvva 64,128,128
dl 249,36,51,2048
tv 140,225,53248
mvva 64,132,132
tv 136,225,49152
tv 138,225,51200
mvva 64,144,136
tv 142,225,55296
mvva 64,148,140
tv 154,225,59392
zvva 68,148,156
tv 158,225,63488
mvva 68,144,152
mvva 68,132,148
dl 247,36,51,3072
s_lshl_b32 s51,s33,5
s_add_u32 m0,s52,98304
s_xor_b32 s51,s64,s51
mvva 68,128,144
zvva 72,128,160
mvva 72,132,164
s_waitcnt vmcnt(18)
s_mov_b32 s90,0
mvva 72,144,168
s_xor_b32 s50,s67,0
mvva 72,148,172
zvva 76,148,188
dl 245,36,51,0
mvva 76,144,184
mvva 76,132,180
tv 148,226,61440
mvva 76,128,176
tv 144,226,57344
s_waitcnt lgkmcnt(2)
zvva 92,136,176
tv 132,226,53248
mvva 92,140,180
tv 128,226,49152
tv 130,226,51200
mvva 92,152,184
dl 246,36,51,1024
tv 134,226,55296
mvva 92,156,188
tv 146,226,59392
zvva 88,156,172
tv 150,226,63488
mvva 88,152,168
mvva 88,140,164
mvva 88,136,160
zvva 84,136,144
dl 249,36,51,2048
mvva 84,140,148
mvva 84,152,152
mvva 84,156,156
zvva 80,156,140
mvva 80,152,136
mvva 80,140,132
dl 247,36,51,3072
mvva 80,136,128
s_lshl_b32 s51,s33,5
tv 152,227,57344
s_add_u32 m0,s52,114688
s_waitcnt lgkmcnt(1)
zvva 96,128,128
s_xor_b32 s51,s65,s51
tv 140,227,53248
tv 142,227,55296
mvva 96,132,132
tv 136,227,49152
tv 138,227,51200
mvva 96,144,136
mvva 96,148,140
tv 154,227,59392
zvva 100,148,156
lv 164,252,50,0
tv 156,227,61440
tv 158,227,63488
mvva 100,144,152
mvva 100,132,148
mvva 100,128,144
zvva 104,128,160
mvva 104,132,164
mvva 104,144,168
lv 172,252,50,2048
mvva 104,148,172
zvva 108,148,188
mvva 108,144,184
mvva 108,132,180
mvva 108,128,176
s_barrier
s_waitcnt lgkmcnt(0)
zvva 124,136,176
lv 180,250,50,0
tv 132,232,4096
tv 144,232,8192
tv 146,232,10240
mvva 124,140,180
tv 128,232,0
tv 130,232,2048
mvva 124,152,184
tv 134,232,6144
mvva 124,156,188
tv 148,232,12288
tv 150,232,14336
zvva 120,156,172
mvva 120,152,168
mvva 120,140,164
lv 188,250,50,2048
mvva 120,136,160
zvva 116,136,144
mvva 116,140,148
mvva 116,152,152
mvva 116,156,156
zvva 112,156,140
lv 220,250,50,3072
mvva 112,152,136
mvva 112,140,132
tv 156,233,12288
mvva 112,136,128
tv 152,233,8192
s_waitcnt lgkmcnt(2)
mvva 0,128,4
tv 140,233,4096
mvva 0,132,12
tv 136,233,0
tv 138,233,2048
mvva 0,144,20
lv 212,250,50,1024
tv 142,233,6144
mvva 0,148,28
tv 154,233,10240
mvva 4,148,60
tv 158,233,14336
mvva 4,144,52
mvva 4,132,44
mvva 4,128,36
mvva 8,128,0
lv 204,252,50,3072
mvva 8,132,8
mvva 8,144,16
mvva 8,148,24
mvva 12,148,56
s_waitcnt vmcnt(27)
mvva 12,144,48
s_waitcnt vmcnt(25)
mvva 12,132,40
lv 196,252,50,1024
s_waitcnt vmcnt(24)
mvva 12,128,32
tv 144,234,8192
s_waitcnt lgkmcnt(1)
mvva 28,136,32
tv 132,234,4096
tv 134,234,6144
mvva 28,140,40
tv 128,234,0
tv 130,234,2048
mvva 28,152,48
mvva 28,156,56
tv 146,234,10240
mvva 24,156,24
dl 245,36,51,0
s_waitcnt vmcnt(20)
s_barrier
tv 148,234,12288
tv 150,234,14336
mvva 24,152,16
mvva 24,140,8
mvva 24,136,0
mvva 20,136,36
mvva 20,140,44
mvva 20,152,52
s_cmp_eq_u32 s12,0
s_cselect_b64 exec,1,0
buffer_store_dword v251,off,s[8:11],s61 sc0 sc1
s_mov_b64 exec,-1
mvva 20,156,60
mvva 16,156,28
mvva 16,152,20
mvva 16,140,12
tv 156,235,12288
mvva 16,136,4
tv 152,235,8192
s_waitcnt lgkmcnt(2)
mvva 32,128,4
dl 246,36,51,1024
tv 140,235,4096
mvva 32,132,12
tv 136,235,0
tv 138,235,2048
mvva 32,144,20
tv 142,235,6144
mvva 32,148,28
tv 154,235,10240
mvva 36,148,60
tv 158,235,14336
mvva 36,144,52
mvva 36,132,44
dl 249,36,51,2048
mvva 36,128,36
mvva 40,128,0
mvva 40,132,8
mvva 40,144,16
mvva 40,148,24
mvva 44,148,56
dl 247,36,51,3072
s_lshl_b32 s51,s34,5
s_add_u32 m0,s52,65536
s_xor_b32 s51,s64,s51
mvva 44,144,48
tv 148,232,28672
mvva 44,132,40
s_xor_b32 s53,s60,s33
tv 150,232,30720
mvva 44,128,32
tv 144,232,24576
s_waitcnt lgkmcnt(3)
mvva 60,136,32
tv 132,232,20480
mvva 60,140,40
tv 128,232,16384
tv 130,232,18432
mvva 60,152,48
dl 245,36,51,0
tv 134,232,22528
mvva 60,156,56
tv 146,232,26624
mvva 56,156,24
mvva 56,152,16
mvva 56,140,8
mvva 56,136,0
mvva 52,136,36
buffer_load_dword v253,v242,s[8:11],s53 offen sc1
mvva 52,140,44
mvva 52,152,52
mvva 52,156,60
mvva 48,156,28
mvva 48,152,20
mvva 48,140,12
dl 246,36,51,1024
mvva 48,136,4
tv 152,233,24576
s_waitcnt lgkmcnt(1)
mvva 64,128,4
tv 140,233,20480
tv 142,233,22528
mvva 64,132,12
tv 136,233,16384
tv 138,233,18432
mvva 64,144,20
mvva 64,148,28
tv 154,233,26624
mvva 68,148,60
dl 249,36,51,2048
tv 156,233,28672
tv 158,233,30720
mvva 68,144,52
mvva 68,132,44
mvva 68,128,36
mvva 72,128,0
mvva 72,132,8
mvva 72,144,16
dl 247,36,51,3072
s_lshl_b32 s51,s34,5
s_add_u32 m0,s52,81920
s_xor_b32 s51,s65,s51
mvva 72,148,24
mvva 76,148,56
mvva 76,144,48
mvva 76,132,40
tv 148,234,28672
mvva 76,128,32
tv 144,234,24576
s_waitcnt lgkmcnt(2)
mvva 92,136,32
dl 245,36,51,0
tv 132,234,20480
mvva 92,140,40
tv 128,234,16384
tv 130,234,18432
mvva 92,152,48
tv 134,234,22528
mvva 92,156,56
tv 146,234,26624
mvva 88,156,24
tv 150,234,30720
mvva 88,152,16
mvva 88,140,8
dl 246,36,51,1024
mvva 88,136,0
L_vs28:
s_cbranch_execz L_vs17
mvva 84,136,36
mvva 84,140,44
mvva 84,152,52
mvva 84,156,60
mvva 80,156,28
dl 249,36,51,2048
mvva 80,152,20
mvva 80,140,12
tv 156,235,28672
mvva 80,136,4
tv 152,235,24576
s_waitcnt lgkmcnt(2)
mvva 96,128,4
tv 140,235,20480
mvva 96,132,12
tv 136,235,16384
tv 138,235,18432
mvva 96,144,20
dl 247,36,51,3072
s_lshl_b32 s51,s35,5
s_add_u32 m0,s52,32768
s_xor_b32 s51,s64,s51
tv 142,235,22528
mvva 96,148,28
tv 154,235,26624
mvva 100,148,60
tv 158,235,30720
mvva 100,144,52
mvva 100,132,44
mvva 100,128,36
mvva 104,128,0
dl 245,36,51,0
mvva 104,132,8
mvva 104,144,16
mvva 104,148,24
mvva 108,148,56
mvva 108,144,48
mvva 108,132,40
dl 246,36,51,1024
mvva 108,128,32
s_waitcnt vmcnt(27)
s_barrier
s_waitcnt lgkmcnt(0)
mvva 124,136,32
tv 132,224,4096
tv 134,224,6144
tv 144,224,8192
mvva 124,140,40
tv 128,224,0
tv 130,224,2048
mvva 124,152,48
mvva 124,156,56
tv 146,224,10240
mvva 120,156,24
dl 249,36,51,2048
tv 148,224,12288
tv 150,224,14336
mvva 120,152,16
mvva 120,140,8
mvva 120,136,0
mvva 116,136,36
mvva 116,140,44
mvva 116,152,52
dl 247,36,51,3072
s_lshl_b32 s51,s35,5
s_add_u32 m0,s52,49152
s_xor_b32 s51,s65,s51
mvva 116,156,60
mvva 112,156,28
mvva 112,152,20
s_waitcnt vmcnt(11)
v_cmp_ne_u32_e32 vcc,4,v253
mvva 112,140,12
dl 245,36,51,0
s_cbranch_vccnz L_spa3o
L_spra3o:
mvva 112,136,4
s_xor_b32 s50,s67,s33
tv 152,225,8192
s_waitcnt lgkmcnt(1)
mvvv 0,128,164
tv 140,225,4096
tv 142,225,6144
mvvv 0,132,172
tv 136,225,0
tv 138,225,2048
mvvv 0,144,180
dl 246,36,51,1024
mvvv 0,148,188
tv 154,225,10240
mvvv 4,148,220
tv 156,225,12288
tv 158,225,14336
mvvv 4,144,212
mvvv 4,132,204
la 196,252,50,0
mvvv 4,128,196
mvvv 8,128,160
s_xor_b32 s53,s60,s34
mvvv 8,132,168
mvvv 8,144,176
la 204,252,50,2048
mvvv 8,148,184
mvvv 12,148,216
mvvv 12,144,208
mvvv 12,132,200
buffer_load_dword v253,v242,s[8:11],s53 offen sc1
mvvv 12,128,192
tv 144,226,8192
s_waitcnt lgkmcnt(1)
mvvv 28,136,192
tv 132,226,4096
tv 134,226,6144
mvvv 28,140,200
tv 128,226,0
tv 130,226,2048
mvvv 28,152,208
la 212,250,50,0
mvvv 28,156,216
tv 146,226,10240
mvvv 24,156,184
tv 148,226,12288
tv 150,226,14336
mvvv 24,152,176
mvvv 24,140,168
mvvv 24,136,160
la 220,250,50,2048
mvvv 20,136,196
mvvv 20,140,204
mvvv 20,152,212
mvvv 20,156,220
la 252,250,50,3072
mvvv 16,156,188
)"
R"(mvvv 16,152,180
mvvv 16,140,172
tv 156,227,12288
mvvv 16,136,164
la 244,250,50,1024
tv 152,227,8192
s_waitcnt lgkmcnt(2)
mvvv 32,128,164
tv 140,227,4096
mvvv 32,132,172
tv 136,227,0
tv 138,227,2048
mvvv 32,144,180
tv 142,227,6144
mvvv 32,148,188
la 236,252,50,3072
tv 154,227,10240
mvvv 36,148,220
tv 158,227,14336
mvvv 36,144,212
mvvv 36,132,204
mvvv 36,128,196
la 228,252,50,1024
s_add_u32 s50,s50,s55
mvvv 40,128,160
mvvv 40,132,168
mvvv 40,144,176
mvvv 40,148,184
mvvv 44,148,216
la 192,252,50,0
mvvv 44,144,208
mvvv 44,132,200
tv 148,224,28672
tv 150,224,30720
mvvv 44,128,192
s_waitcnt lgkmcnt(2)
mvvv 60,136,192
la 200,252,50,2048
tv 132,224,20480
mvvv 60,140,200
tv 128,224,16384
tv 130,224,18432
mvvv 60,152,208
tv 134,224,22528
mvvv 60,156,216
tv 144,224,24576
tv 146,224,26624
mvvv 56,156,184
la 208,250,50,0
mvvv 56,152,176
mvvv 56,140,168
mvvv 56,136,160
mvvv 52,136,196
la 216,250,50,2048
mvvv 52,140,204
mvvv 52,152,212
mvvv 52,156,220
mvvv 48,156,188
la 248,250,50,3072
mvvv 48,152,180
mvvv 48,140,172
tv 156,225,28672
mvvv 48,136,164
tv 152,225,24576
s_waitcnt lgkmcnt(2)
mvvv 64,128,164
tv 140,225,20480
mvvv 64,132,172
la 240,250,50,1024
tv 136,225,16384
tv 138,225,18432
mvvv 64,144,180
tv 142,225,22528
mvvv 64,148,188
tv 154,225,26624
mvvv 68,148,220
tv 158,225,30720
mvvv 68,144,212
la 232,252,50,3072
mvvv 68,132,204
mvvv 68,128,196
mvvv 72,128,160
mvvv 72,132,168
la 224,252,50,1024
mvvv 72,144,176
mvvv 72,148,184
mvvv 76,148,216
mvvv 76,144,208
mvvv 76,132,200
dl 249,36,51,2048
mvvv 76,128,192
tv 144,226,24576
s_waitcnt lgkmcnt(1)
mvvv 92,136,192
tv 132,226,20480
tv 134,226,22528
mvvv 92,140,200
tv 128,226,16384
tv 130,226,18432
mvvv 92,152,208
dl 247,36,51,3072
s_lshl_b32 s51,s98,5
s_xor_b32 s51,s64,s51
s_add_u32 m0,s52,131072
s_xor_b32 s51,s51,4194304
mvvv 92,156,216
tv 146,226,26624
mvvv 88,156,184
tv 148,226,28672
tv 150,226,30720
mvvv 88,152,176
mvvv 88,140,168
dl 245,36,51,0
mvvv 88,136,160
s_waitcnt vmcnt(17)
v_cmp_ne_u32_e32 vcc,4,v253
mvvv 84,136,196
s_cbranch_vccnz L_spa4o
L_spra4o:
mvvv 84,140,204
s_xor_b32 s50,s67,s34
mvvv 84,152,212
dl 246,36,51,1024
mvvv 84,156,220
mvvv 80,156,188
mvvv 80,152,180
mvvv 80,140,172
la 68,252,50,0
mvvv 80,136,164
tv 152,227,24576
s_waitcnt lgkmcnt(1)
mvvv 96,128,164
tv 140,227,20480
tv 142,227,22528
mvvv 96,132,172
tv 136,227,16384
tv 138,227,18432
mvvv 96,144,180
s_xor_b32 s53,s60,s35
mvvv 96,148,188
la 76,252,50,2048
tv 154,227,26624
mvvv 100,148,220
tv 156,227,28672
tv 158,227,30720
mvvv 100,144,212
mvvv 100,132,204
mvvv 100,128,196
la 84,250,50,0
mvvv 104,128,160
mvvv 104,132,168
mvvv 104,144,176
mvvv 104,148,184
buffer_load_dword v253,v242,s[8:11],s53 offen sc1
mvvv 108,148,216
mvvv 108,144,208
mvvv 108,132,200
mvvv 108,128,192
s_barrier
s_waitcnt lgkmcnt(0)
mvvv 124,136,192
la 92,250,50,2048
tv 132,228,36864
tv 144,228,40960
tv 146,228,43008
mvvv 124,140,200
tv 128,228,32768
tv 130,228,34816
mvvv 124,152,208
tv 134,228,38912
mvvv 124,156,216
tv 148,228,45056
tv 150,228,47104
mvvv 120,156,184
la 124,250,50,3072
mvvv 120,152,176
mvvv 120,140,168
mvvv 120,136,160
mvvv 116,136,196
la 116,250,50,1024
mvvv 116,140,204
mvvv 116,152,212
mvvv 116,156,220
mvvv 112,156,188
la 108,252,50,3072
mvvv 112,152,180
mvvv 112,140,172
tv 156,229,45056
tv 158,229,47104
mvvv 112,136,164
s_waitcnt lgkmcnt(2)
mvva 0,128,196
la 100,252,50,1024
s_add_u32 s50,s50,s55
tv 140,229,36864
mvva 0,132,204
tv 136,229,32768
tv 138,229,34816
s_waitcnt vmcnt(21)
mvva 0,144,212
tv 142,229,38912
mvva 0,148,220
tv 152,229,40960
tv 154,229,43008
mvva 4,148,252
mvva 4,144,244
la 64,252,50,0
mvva 4,132,236
mvva 4,128,228
s_waitcnt vmcnt(21)
mvva 8,128,192
s_waitcnt vmcnt(20)
mvva 8,132,200
la 72,252,50,2048
s_waitcnt vmcnt(20)
mvva 8,144,208
s_waitcnt vmcnt(19)
mvva 8,148,216
s_waitcnt vmcnt(18)
mvva 12,148,248
s_waitcnt vmcnt(17)
mvva 12,144,240
la 80,250,50,0
s_waitcnt vmcnt(17)
mvva 12,132,232
tv 148,230,45056
s_waitcnt vmcnt(16)
mvva 12,128,224
tv 144,230,40960
s_waitcnt lgkmcnt(2)
mvva 28,136,224
tv 132,230,36864
mvva 28,140,232
la 88,250,50,2048
tv 128,230,32768
tv 130,230,34816
mvva 28,152,240
tv 134,230,38912
mvva 28,156,248
tv 146,230,43008
mvva 24,156,216
tv 150,230,47104
mvva 24,152,208
la 120,250,50,3072
mvva 24,140,200
mvva 24,136,192
mvva 20,136,228
mvva 20,140,236
mvva 20,152,244
la 112,250,50,1024
mvva 20,156,252
mvva 16,156,220
mvva 16,152,212
mvva 16,140,204
la 104,252,50,3072
mvva 16,136,196
tv 152,231,40960
s_waitcnt lgkmcnt(1)
mvva 32,128,196
tv 140,231,36864
tv 142,231,38912
mvva 32,132,204
tv 136,231,32768
tv 138,231,34816
mvva 32,144,212
la 96,252,50,1024
mvva 32,148,220
tv 154,231,43008
mvva 36,148,252
tv 156,231,45056
tv 158,231,47104
mvva 36,144,244
mvva 36,132,236
mvva 36,128,228
dl 249,36,51,2048
mvva 40,128,192
mvva 40,132,200
mvva 40,144,208
mvva 40,148,216
dl 247,36,51,3072
s_lshl_b32 s51,s98,5
s_xor_b32 s51,s65,s51
s_add_u32 m0,s52,147456
s_xor_b32 s51,s51,4194304
mvva 44,148,248
mvva 44,144,240
mvva 44,132,232
tv 148,228,61440
mvva 44,128,224
dl 245,36,51,0
tv 144,228,57344
s_waitcnt lgkmcnt(2)
mvva 60,136,224
tv 132,228,53248
mvva 60,140,232
tv 128,228,49152
tv 130,228,51200
s_waitcnt vmcnt(16)
v_cmp_ne_u32_e32 vcc,4,v253
mvva 60,152,240
s_cbranch_vccnz L_spa5o
L_spra5o:
tv 134,228,55296
s_xor_b32 s50,s67,s35
mvva 60,156,248
dl 246,36,51,1024
tv 146,228,59392
mvva 56,156,216
tv 150,228,63488
mvva 56,152,208
mvva 56,140,200
mvva 56,136,192
la 132,252,50,0
mvva 52,136,228
mvva 52,140,236
mvva 52,152,244
mvva 52,156,252
mvva 48,156,220
la 140,252,50,2048
s_xor_b32 s53,s60,s98
s_xor_b32 s53,s53,512
mvva 48,152,212
tv 156,229,61440
mvva 48,140,204
tv 158,229,63488
mvva 48,136,196
tv 152,229,57344
s_waitcnt lgkmcnt(3)
mvva 64,128,196
la 148,250,50,0
tv 140,229,53248
mvva 64,132,204
tv 136,229,49152
tv 138,229,51200
mvva 64,144,212
tv 142,229,55296
mvva 64,148,220
tv 154,229,59392
mvva 68,148,252
buffer_load_dword v253,v242,s[8:11],s53 offen sc1
mvva 68,144,244
mvva 68,132,236
mvva 68,128,228
mvva 72,128,192
mvva 72,132,200
la 156,250,50,2048
mvva 72,144,208
mvva 72,148,216
mvva 76,148,248
mvva 76,144,240
la 188,250,50,3072
mvva 76,132,232
tv 148,230,61440
mvva 76,128,224
tv 144,230,57344
s_waitcnt lgkmcnt(2)
mvva 92,136,224
tv 132,230,53248
mvva 92,140,232
la 180,250,50,1024
tv 128,230,49152
tv 130,230,51200
mvva 92,152,240
tv 134,230,55296
mvva 92,156,248
tv 146,230,59392
mvva 88,156,216
tv 150,230,63488
mvva 88,152,208
la 172,252,50,3072
mvva 88,140,200
mvva 88,136,192
mvva 84,136,228
mvva 84,140,236
la 164,252,50,1024
s_add_u32 s50,s50,s55
mvva 84,152,244
mvva 84,156,252
mvva 80,156,220
mvva 80,152,212
mvva 80,140,204
la 128,252,50,0
mvva 80,136,196
tv 152,231,57344
s_waitcnt lgkmcnt(1)
mvva 96,128,196
tv 140,231,53248
tv 142,231,55296
mvva 96,132,204
tv 136,231,49152
tv 138,231,51200
mvva 96,144,212
la 136,252,50,2048
mvva 96,148,220
tv 154,231,59392
mvva 100,148,252
tv 156,231,61440
tv 158,231,63488
mvva 100,144,244
mvva 100,132,236
la 144,250,50,0
mvva 100,128,228
mvva 104,128,192
mvva 104,132,200
mvva 104,144,208
la 152,250,50,2048
mvva 104,148,216
mvva 108,148,248
L_vs29:
s_cbranch_execz L_vr1
mvva 108,144,240
mvva 108,132,232
la 184,250,50,3072
mvva 108,128,224
s_barrier
s_waitcnt lgkmcnt(0)
mvva 124,136,224
tv 132,228,4096
tv 134,228,6144
tv 144,228,8192
mvva 124,140,232
tv 128,228,0
tv 130,228,2048
mvva 124,152,240
mvva 124,156,248
la 176,250,50,1024
tv 146,228,10240
mvva 120,156,216
tv 148,228,12288
tv 150,228,14336
mvva 120,152,208
mvva 120,140,200
mvva 120,136,192
la 168,252,50,3072
mvva 116,136,228
mvva 116,140,236
mvva 116,152,244
mvva 116,156,252
la 160,252,50,1024
mvva 112,156,220
mvva 112,152,212
mvva 112,140,204
tv 156,229,12288
tv 158,229,14336
mvva 112,136,196
s_waitcnt lgkmcnt(2)
mvva 0,128,68
dl 249,36,51,2048
tv 140,229,4096
mvva 0,132,76
tv 136,229,0
tv 138,229,2048
mvva 0,144,84
tv 142,229,6144
s_waitcnt vmcnt(22)
mvva 0,148,92
tv 152,229,8192
tv 154,229,10240
mvva 4,148,124
dl 247,36,51,3072
s_lshl_b32 s51,s98,5
s_xor_b32 s51,s64,s51
s_add_u32 m0,s52,0
s_xor_b32 s51,s51,4456448
mvva 4,144,116
mvva 4,132,108
mvva 4,128,100
mvva 8,128,64
dl 245,36,51,0
mvva 8,132,72
mvva 8,144,80
mvva 8,148,88
s_waitcnt vmcnt(16)
v_cmp_ne_u32_e32 vcc,4,v253
mvva 12,148,120
dl 246,36,51,1024
s_cbranch_vccnz L_spa6o
L_spra6o:
mvva 12,144,112
s_xor_b32 s50,s67,s98
mvva 12,132,104
s_xor_b32 s50,s50,131072
tv 148,230,12288
tv 150,230,14336
mvva 12,128,96
s_waitcnt lgkmcnt(2)
mvva 28,136,96
dl 249,36,51,2048
tv 132,230,4096
mvva 28,140,104
tv 128,230,0
tv 130,230,2048
mvva 28,152,112
tv 134,230,6144
mvva 28,156,120
tv 144,230,8192
tv 146,230,10240
mvva 24,156,88
mvva 24,152,80
la 4,252,50,0
mvva 24,140,72
s_xor_b32 s53,s60,s98
mvva 24,136,64
s_xor_b32 s53,s53,8704
mvva 20,136,100
mvva 20,140,108
la 12,252,50,2048
mvva 20,152,116
mvva 20,156,124
mvva 16,156,92
mvva 16,152,84
buffer_load_dword v253,v242,s[8:11],s53 offen sc1
mvva 16,140,76
tv 156,231,12288
mvva 16,136,68
tv 152,231,8192
s_waitcnt lgkmcnt(2)
mvva 32,128,68
tv 140,231,4096
mvva 32,132,76
la 20,250,50,0
tv 136,231,0
tv 138,231,2048
mvva 32,144,84
tv 142,231,6144
mvva 32,148,92
tv 154,231,10240
mvva 36,148,124
tv 158,231,14336
mvva 36,144,116
la 28,250,50,2048
mvva 36,132,108
mvva 36,128,100
mvva 40,128,64
mvva 40,132,72
mvva 40,144,80
la 60,250,50,3072
mvva 40,148,88
mvva 44,148,120
mvva 44,144,112
mvva 44,132,104
la 52,250,50,1024
mvva 44,128,96
tv 144,228,24576
s_waitcnt lgkmcnt(1)
mvva 60,136,96
tv 132,228,20480
tv 134,228,22528
mvva 60,140,104
tv 128,228,16384
tv 130,228,18432
mvva 60,152,112
la 44,252,50,3072
mvva 60,156,120
tv 146,228,26624
mvva 56,156,88
tv 148,228,28672
tv 150,228,30720
mvva 56,152,80
mvva 56,140,72
mvva 56,136,64
la 36,252,50,1024
s_add_u32 s50,s50,s55
mvva 52,136,100
mvva 52,140,108
mvva 52,152,116
mvva 52,156,124
la 0,252,50,0
mvva 48,156,92
mvva 48,152,84
mvva 48,140,76
tv 156,229,28672
mvva 48,136,68
la 8,252,50,2048
tv 152,229,24576
s_waitcnt lgkmcnt(2)
mvva 64,128,68
tv 140,229,20480
mvva 64,132,76
tv 136,229,16384
tv 138,229,18432
mvva 64,144,84
tv 142,229,22528
mvva 64,148,92
la 16,250,50,0
tv 154,229,26624
mvva 68,148,124
tv 158,229,30720
mvva 68,144,116
mvva 68,132,108
mvva 68,128,100
la 24,250,50,2048
mvva 72,128,64
mvva 72,132,72
mvva 72,144,80
mvva 72,148,88
mvva 76,148,120
la 56,250,50,3072
mvva 76,144,112
mvva 76,132,104
tv 148,224,45056
tv 150,224,47104
mvva 76,128,96
s_waitcnt lgkmcnt(2)
mvva 92,136,96
la 48,250,50,1024
tv 132,224,36864
mvva 92,140,104
tv 128,224,32768
tv 130,224,34816
mvva 92,152,112
tv 134,224,38912
mvva 92,156,120
tv 144,224,40960
tv 146,224,43008
mvva 88,156,88
la 40,252,50,3072
mvva 88,152,80
mvva 88,140,72
mvva 88,136,64
mvva 84,136,100
mvva 84,140,108
la 32,252,50,1024
mvva 84,152,116
mvva 84,156,124
mvva 80,156,92
mvva 80,152,84
dl 247,36,51,3072
s_lshl_b32 s51,s98,5
s_xor_b32 s51,s64,s51
s_add_u32 m0,s52,16384
s_xor_b32 s51,s51,4718592
mvva 80,140,76
tv 152,225,40960
mvva 80,136,68
tv 154,225,43008
s_waitcnt lgkmcnt(2)
mvva 0,128,132
tv 140,225,36864
mvva 0,132,140
dl 245,36,51,0
tv 136,225,32768
tv 138,225,34816
mvva 0,144,148
tv 142,225,38912
s_waitcnt vmcnt(24)
mvva 0,148,156
mvva 4,148,188
tv 156,225,45056
tv 158,225,47104
mvva 4,144,180
dl 246,36,51,1024
s_waitcnt vmcnt(17)
v_cmp_ne_u32_e32 vcc,4,v253
mvva 4,132,172
s_cbranch_vccnz L_spa7o
L_spra7o:
mvva 4,128,164
s_xor_b32 s50,s67,s98
mvva 8,128,128
s_xor_b32 s50,s50,139264
mvva 8,132,136
dl 249,36,51,2048
mvva 8,144,144
mvva 8,148,152
mvva 12,148,184
mvva 12,144,176
mvva 12,132,168
dl 247,36,51,3072
mvva 12,128,160
s_barrier
tv 144,226,40960
s_lshl_b32 s51,s98,5
s_waitcnt lgkmcnt(1)
mvva 28,136,160
s_xor_b32 s53,s60,s98
tv 132,226,36864
tv 134,226,38912
s_xor_b32 s53,s53,16896
mvva 28,140,168
s_xor_b32 s51,s65,s51
tv 128,226,32768
tv 130,226,34816
mvva 28,152,176
lv 164,252,50,0
mvva 28,156,184
s_add_u32 m0,s52,98304
tv 146,226,43008
s_xor_b32 s51,s51,4456448
mvva 24,156,152
tv 148,226,45056
tv 150,226,47104
mvva 24,152,144
mvva 24,140,136
buffer_load_dword v253,v242,s[8:11],s53 offen sc1
mvva 24,136,128
mvva 20,136,164
mvva 20,140,172
mvva 20,152,180
lv 172,252,50,2048
mvva 20,156,188
mvva 16,156,156
mvva 16,152,148
mvva 16,140,140
lv 180,250,50,0
mvva 16,136,132
tv 152,227,40960
s_waitcnt lgkmcnt(1)
mvva 32,128,132
tv 140,227,36864
tv 142,227,38912
mvva 32,132,140
tv 136,227,32768
tv 138,227,34816
mvva 32,144,148
mvva 32,148,156
lv 188,250,50,2048
tv 154,227,43008
mvva 36,148,188
tv 156,227,45056
tv 158,227,47104
mvva 36,144,180
mvva 36,132,172
mvva 36,128,164
lv 220,250,50,3072
mvva 40,128,128
mvva 40,132,136
mvva 40,144,144
mvva 40,148,152
lv 212,250,50,1024
mvva 44,148,184
mvva 44,144,176
mvva 44,132,168
tv 148,224,61440
tv 150,224,63488
mvva 44,128,160
s_waitcnt lgkmcnt(2)
mvva 60,136,160
lv 204,252,50,3072
tv 132,224,53248
mvva 60,140,168
tv 128,224,49152
tv 130,224,51200
mvva 60,152,176
tv 134,224,55296
mvva 60,156,184
tv 144,224,57344
tv 146,224,59392
mvva 56,156,152
lv 196,252,50,1024
s_add_u32 s50,s50,s55
mvva 56,152,144
mvva 56,140,136
mvva 56,136,128
mvva 52,136,164
lv 160,252,50,0
mvva 52,140,172
mvva 52,152,180
mvva 52,156,188
mvva 48,156,156
lv 168,252,50,2048
mvva 48,152,148
mvva 48,140,140
tv 156,225,61440
tv 158,225,63488
mvva 48,136,132
s_waitcnt lgkmcnt(2)
mvva 64,128,132
lv 176,250,50,0
tv 140,225,53248
mvva 64,132,140
tv 136,225,49152
tv 138,225,51200
mvva 64,144,148
tv 142,225,55296
mvva 64,148,156
tv 152,225,57344
tv 154,225,59392
mvva 68,148,188
mvva 68,144,180
lv 184,250,50,2048
mvva 68,132,172
mvva 68,128,164
mvva 72,128,128
mvva 72,132,136
lv 216,250,50,3072
mvva 72,144,144
mvva 72,148,152
mvva 76,148,184
mvva 76,144,176
lv 208,250,50,1024
mvva 76,132,168
tv 148,232,12288
mvva 76,128,160
tv 144,232,8192
s_waitcnt lgkmcnt(2)
mvva 92,136,160
tv 132,232,4096
mvva 92,140,168
tv 128,232,0
tv 130,232,2048
mvva 92,152,176
lv 200,252,50,3072
tv 134,232,6144
mvva 92,156,184
tv 146,232,10240
mvva 88,156,152
tv 150,232,14336
mvva 88,152,144
mvva 88,140,136
lv 192,252,50,1024
mvva 88,136,128
mvva 84,136,164
mvva 84,140,172
mvva 84,152,180
dl 245,36,51,0
mvva 84,156,188
mvva 80,156,156
mvva 80,152,148
mvva 80,140,140
dl 246,36,51,1024
mvva 80,136,132
tv 152,233,8192
s_waitcnt vmcnt(17)
v_cmp_ne_u32_e32 vcc,4,v253
s_waitcnt lgkmcnt(1)
mvva 0,128,4
s_cbranch_vccnz L_spa8o
L_spra8o:
tv 140,233,4096
tv 142,233,6144
mvva 0,132,12
s_xor_b32 s50,s67,s98
tv 136,233,0
s_xor_b32 s50,s50,147456
tv 138,233,2048
mvva 0,144,20
dl 249,36,51,2048
mvva 0,148,28
tv 154,233,10240
v_and_b32_e32 v242,48,v244
mvva 4,148,60
tv 156,233,12288
tv 158,233,14336
mvva 4,144,52
v_lshlrev_b32_e32 v242,3,v242
mvva 4,132,44
mvva 4,128,36
la 196,252,50,0
mvva 8,128,0
s_andn2_b32 s53,s61,384
mvva 8,132,8
mvva 8,144,16
mvva 8,148,24
la 204,252,50,2048
mvva 12,148,56
mvva 12,144,48
mvva 12,132,40
tv 148,234,12288
mvva 12,128,32
buffer_load_dword v253,v242,s[8:11],s53 offen sc1
tv 144,234,8192
s_waitcnt lgkmcnt(2)
mvva 28,136,32
tv 132,234,4096
mvva 28,140,40
tv 128,234,0
tv 130,234,2048
mvva 28,152,48
tv 134,234,6144
mvva 28,156,56
la 212,250,50,0
tv 146,234,10240
mvva 24,156,24
tv 150,234,14336
mvva 24,152,16
mvva 24,140,8
mvva 24,136,0
la 220,250,50,2048
mvva 20,136,36
mvva 20,140,44
mvva 20,152,52
mvva 20,156,60
mvva 16,156,28
la 252,250,50,3072
mvva 16,152,20
mvva 16,140,12
tv 156,235,12288
tv 158,235,14336
mvva 16,136,4
s_waitcnt lgkmcnt(2)
mvva 32,128,4
la 244,250,50,1024
tv 140,235,4096
mvva 32,132,12
tv 136,235,0
tv 138,235,2048
mvva 32,144,20
tv 142,235,6144
mvva 32,148,28
tv 152,235,8192
tv 154,235,10240
mvva 36,148,60
la 236,252,50,3072
mvva 36,144,52
mvva 36,132,44
mvva 36,128,36
mvva 40,128,0
mvva 40,132,8
la 228,252,50,1024
s_add_u32 s50,s50,s55
mvva 40,144,16
mvva 40,148,24
mvva 44,148,56
mvva 44,144,48
la 192,252,50,0
mvva 44,132,40
mvva 44,128,32
s_barrier
s_waitcnt lgkmcnt(0)
mvva 60,136,32
tv 132,232,20480
tv 144,232,24576
mvva 60,140,40
la 200,252,50,2048
tv 128,232,16384
tv 130,232,18432
mvva 60,152,48
tv 134,232,22528
mvva 60,156,56
tv 146,232,26624
mvva 56,156,24
tv 148,232,28672
tv 150,232,30720
mvva 56,152,16
la 208,250,50,0
mvva 56,140,8
mvva 56,136,0
mvva 52,136,36
mvva 52,140,44
la 216,250,50,2048
mvva 52,152,52
mvva 52,156,60
mvva 48,156,28
mvva 48,152,20
mvva 48,140,12
la 248,250,50,3072
mvva 48,136,4
tv 152,233,24576
s_waitcnt lgkmcnt(1)
mvva 64,128,4
L_vs30:
s_cbranch_execz L_vr1
tv 140,233,20480
tv 142,233,22528
mvva 64,132,12
tv 136,233,16384
tv 138,233,18432
mvva 64,144,20
la 240,250,50,1024
mvva 64,148,28
tv 154,233,26624
mvva 68,148,60
tv 156,233,28672
tv 158,233,30720
mvva 68,144,52
mvva 68,132,44
la 232,252,50,3072
mvva 68,128,36
mvva 72,128,0
mvva 72,132,8
mvva 72,144,16
la 224,252,50,1024
mvva 72,148,24
mvva 76,148,56
mvva 76,144,48
mvva 76,132,40
dl 247,36,51,3072
s_lshl_b32 s51,s98,5
s_xor_b32 s51,s65,s51
s_add_u32 m0,s52,114688
s_xor_b32 s51,s51,4718592
mvva 76,128,32
tv 144,224,8192
s_waitcnt lgkmcnt(1)
mvva 92,136,32
tv 132,224,4096
tv 134,224,6144
mvva 92,140,40
tv 128,224,0
tv 130,224,2048
mvva 92,152,48
mvva 92,156,56
dl 245,36,51,0
tv 146,224,10240
mvva 88,156,24
tv 148,224,12288
tv 150,224,14336
mvva 88,152,16
mvva 88,140,8
)"
R"(mvva 88,136,0
dl 246,36,51,1024
s_waitcnt vmcnt(17)
v_cmp_ne_u32_e32 vcc,4,v253
mvva 84,136,36
s_cbranch_vccnz L_sppo
L_sprpo:
mvva 84,140,44
mvva 84,152,52
mvva 84,156,60
dl 249,36,51,2048
mvva 80,156,28
mvva 80,152,20
mvva 80,140,12
tv 156,225,12288
mvva 80,136,4
tv 152,225,8192
s_waitcnt lgkmcnt(2)
mvvv 0,128,164
dl 247,36,51,3072
tv 140,225,4096
mvvv 0,132,172
tv 136,225,0
tv 138,225,2048
mvvv 0,144,180
tv 142,225,6144
mvvv 0,148,188
tv 154,225,10240
mvvv 4,148,220
tv 158,225,14336
mvvv 4,144,212
mvvv 4,132,204
mvvv 4,128,196
mvvv 8,128,160
mvvv 8,132,168
mvvv 8,144,176
mvvv 8,148,184
mvvv 12,148,216
mvvv 12,144,208
mvvv 12,132,200
tv 148,224,28672
mvvv 12,128,192
tv 144,224,24576
s_waitcnt lgkmcnt(2)
mvvv 28,136,192
tv 132,224,20480
mvvv 28,140,200
tv 128,224,16384
tv 130,224,18432
mvvv 28,152,208
tv 134,224,22528
mvvv 28,156,216
tv 146,224,26624
mvvv 24,156,184
tv 150,224,30720
mvvv 24,152,176
mvvv 24,140,168
mvvv 24,136,160
mvvv 20,136,196
mvvv 20,140,204
mvvv 20,152,212
mvvv 20,156,220
mvvv 16,156,188
mvvv 16,152,180
mvvv 16,140,172
tv 156,225,28672
mvvv 16,136,164
tv 152,225,24576
s_waitcnt lgkmcnt(2)
mvva 0,128,196
tv 140,225,20480
mvva 0,132,204
tv 136,225,16384
tv 138,225,18432
s_waitcnt vmcnt(14)
mvva 0,144,212
tv 142,225,22528
mvva 0,148,220
tv 154,225,26624
mvva 4,148,252
tv 158,225,30720
mvva 4,144,244
mvva 4,132,236
s_waitcnt vmcnt(13)
mvva 4,128,228
s_waitcnt vmcnt(12)
mvva 8,128,192
cv 0,0,1
cv 1,2,3
s_waitcnt vmcnt(11)
mvva 8,132,200
s_waitcnt vmcnt(10)
mvva 8,144,208
cv 4,4,5
cv 5,6,7
s_waitcnt vmcnt(9)
mvva 8,148,216
s_waitcnt vmcnt(8)
mvva 12,148,248
s_waitcnt vmcnt(7)
mvva 12,144,240
s_waitcnt vmcnt(6)
mvva 12,132,232
tv 148,226,12288
s_waitcnt vmcnt(5)
mvva 12,128,224
tv 144,226,8192
cv 8,8,9
s_waitcnt lgkmcnt(2)
mvva 28,136,224
tv 132,226,4096
cv 9,10,11
mvva 28,140,232
tv 128,226,0
tv 130,226,2048
mvva 28,152,240
tv 134,226,6144
cv 12,12,13
mvva 28,156,248
tv 146,226,10240
cv 13,14,15
mvva 24,156,216
tv 150,226,14336
mvva 24,152,208
mvva 24,140,200
mvva 24,136,192
mvva 20,136,228
cv 14,28,29
cv 15,30,31
mvva 20,140,236
buffer_store_dwordx4 v[12:15],v244,s[8:11],s56 offen offset:3072 sc0 sc1
mvva 20,152,244
mvva 20,156,252
mvva 16,156,220
cv 10,24,25
cv 11,26,27
mvva 16,152,212
buffer_store_dwordx4 v[8:11],v244,s[8:11],s56 offen offset:2048 sc0 sc1
mvva 16,140,204
tv 156,227,12288
mvva 16,136,196
cv 6,20,21
cv 7,22,23
s_waitcnt lgkmcnt(1)
mvvv 32,128,164
tv 140,227,4096
mvvv 32,132,172
buffer_store_dwordx4 v[4:7],v244,s[8:11],s56 offen offset:1024 sc0 sc1
tv 136,227,0
tv 138,227,2048
mvvv 32,144,180
tv 142,227,6144
cv 2,16,17
mvvv 32,148,188
tv 152,227,8192
tv 154,227,10240
mvvv 36,148,220
tv 158,227,14336
cv 3,18,19
mvvv 36,144,212
buffer_store_dwordx4 v[0:3],v244,s[8:11],s56 offen offset:0 sc0 sc1
mvvv 36,132,204
mvvv 36,128,196
mvvv 40,128,160
mvvv 40,132,168
mvvv 40,144,176
mvvv 40,148,184
mvvv 44,148,216
mvvv 44,144,208
mvvv 44,132,200
tv 148,226,28672
mvvv 44,128,192
s_waitcnt vmcnt(4)
s_barrier
tv 144,226,24576
s_waitcnt lgkmcnt(2)
mvvv 60,136,192
tv 132,226,20480
mvvv 60,140,200
tv 128,226,16384
tv 130,226,18432
mvvv 60,152,208
tv 134,226,22528
mvvv 60,156,216
tv 146,226,26624
mvvv 56,156,184
tv 150,226,30720
mvvv 56,152,176
mvvv 56,140,168
mvvv 56,136,160
mvvv 52,136,196
mvvv 52,140,204
mvvv 52,152,212
mvvv 52,156,220
mvvv 48,156,188
mvvv 48,152,180
mvvv 48,140,172
tv 156,227,28672
mvvv 48,136,164
tv 152,227,24576
s_waitcnt lgkmcnt(2)
mvva 32,128,196
tv 140,227,20480
mvva 32,132,204
tv 136,227,16384
tv 138,227,18432
mvva 32,144,212
tv 142,227,22528
mvva 32,148,220
tv 154,227,26624
mvva 36,148,252
tv 158,227,30720
mvva 36,144,244
mvva 36,132,236
mvva 36,128,228
mvva 40,128,192
mvva 40,132,200
cv 32,32,33
cv 33,34,35
mvva 40,144,208
mvva 40,148,216
mvva 44,148,248
mvva 44,144,240
cv 36,36,37
cv 37,38,39
mvva 44,132,232
tv 148,228,45056
mvva 44,128,224
tv 144,228,40960
cv 40,40,41
s_waitcnt lgkmcnt(2)
mvva 60,136,224
tv 132,228,36864
cv 41,42,43
mvva 60,140,232
tv 128,228,32768
tv 130,228,34816
mvva 60,152,240
tv 134,228,38912
cv 44,44,45
mvva 60,156,248
tv 146,228,43008
cv 45,46,47
mvva 56,156,216
tv 150,228,47104
mvva 56,152,208
mvva 56,140,200
mvva 56,136,192
mvva 52,136,228
cv 46,60,61
cv 47,62,63
mvva 52,140,236
buffer_store_dwordx4 v[44:47],v244,s[8:11],s57 offen offset:3072 sc0 sc1
mvva 52,152,244
mvva 52,156,252
mvva 48,156,220
cv 42,56,57
cv 43,58,59
mvva 48,152,212
buffer_store_dwordx4 v[40:43],v244,s[8:11],s57 offen offset:2048 sc0 sc1
mvva 48,140,204
tv 156,229,45056
mvva 48,136,196
cv 38,52,53
cv 39,54,55
s_waitcnt lgkmcnt(1)
mvvv 64,128,164
tv 140,229,36864
mvvv 64,132,172
buffer_store_dwordx4 v[36:39],v244,s[8:11],s57 offen offset:1024 sc0 sc1
tv 136,229,32768
tv 138,229,34816
mvvv 64,144,180
tv 142,229,38912
cv 34,48,49
mvvv 64,148,188
tv 152,229,40960
tv 154,229,43008
mvvv 68,148,220
tv 158,229,47104
cv 35,50,51
mvvv 68,144,212
buffer_store_dwordx4 v[32:35],v244,s[8:11],s57 offen offset:0 sc0 sc1
mvvv 68,132,204
mvvv 68,128,196
mvvv 72,128,160
mvvv 72,132,168
mvvv 72,144,176
mvvv 72,148,184
mvvv 76,148,216
mvvv 76,144,208
mvvv 76,132,200
tv 148,228,61440
mvvv 76,128,192
tv 144,228,57344
s_waitcnt lgkmcnt(2)
mvvv 92,136,192
tv 132,228,53248
mvvv 92,140,200
tv 128,228,49152
tv 130,228,51200
mvvv 92,152,208
tv 134,228,55296
mvvv 92,156,216
tv 146,228,59392
mvvv 88,156,184
tv 150,228,63488
mvvv 88,152,176
mvvv 88,140,168
mvvv 88,136,160
mvvv 84,136,196
mvvv 84,140,204
mvvv 84,152,212
mvvv 84,156,220
mvvv 80,156,188
mvvv 80,152,180
mvvv 80,140,172
tv 156,229,61440
mvvv 80,136,164
tv 152,229,57344
s_waitcnt lgkmcnt(2)
mvva 64,128,196
tv 140,229,53248
mvva 64,132,204
tv 136,229,49152
tv 138,229,51200
mvva 64,144,212
tv 142,229,55296
mvva 64,148,220
tv 154,229,59392
mvva 68,148,252
tv 158,229,63488
mvva 68,144,244
mvva 68,132,236
mvva 68,128,228
s_xor_b32 s82,s59,12
mvva 72,128,192
s_mul_i32 s82,s82,12288
cv 64,64,65
s_add_u32 s82,s82,0
mvva 72,132,200
lv 16,244,82,0
mvva 72,144,208
cv 65,66,67
cv 68,68,69
mvva 72,148,216
cv 69,70,71
mvva 76,148,248
mvva 76,144,240
lv 20,244,82,1024
mvva 76,132,232
tv 148,230,28672
mvva 76,128,224
tv 144,230,24576
cv 72,72,73
s_waitcnt lgkmcnt(2)
mvva 92,136,224
tv 132,230,20480
cv 73,74,75
mvva 92,140,232
tv 128,230,16384
tv 130,230,18432
mvva 92,152,240
lv 24,244,82,2048
tv 134,230,22528
mvva 92,156,248
tv 146,230,26624
cv 76,76,77
mvva 88,156,216
tv 150,230,30720
cv 77,78,79
mvva 88,152,208
mvva 88,140,200
lv 28,244,82,3072
mvva 88,136,192
cv 78,92,93
cv 79,94,95
mvva 84,136,228
mvva 84,140,236
mvva 84,152,244
buffer_store_dwordx4 v[76:79],v244,s[8:11],s58 offen offset:3072 sc0 sc1
mvva 84,156,252
mvva 80,156,220
cv 74,88,89
cv 75,90,91
mvva 80,152,212
mvva 80,140,204
buffer_store_dwordx4 v[72:75],v244,s[8:11],s58 offen offset:2048 sc0 sc1
tv 156,231,28672
mvva 80,136,196
cv 70,84,85
cv 71,86,87
s_waitcnt lgkmcnt(1)
mvva 96,128,68
tv 140,231,20480
mvva 96,132,76
tv 136,231,16384
tv 138,231,18432
mvva 96,144,84
buffer_store_dwordx4 v[68:71],v244,s[8:11],s58 offen offset:1024 sc0 sc1
tv 142,231,22528
mvva 96,148,92
tv 152,231,24576
tv 154,231,26624
mvva 100,148,124
tv 158,231,30720
cv 66,80,81
cv 67,82,83
mvva 100,144,116
mvva 100,132,108
mvva 100,128,100
buffer_store_dwordx4 v[64:67],v244,s[8:11],s58 offen offset:0 sc0 sc1
mvva 104,128,64
mvva 104,132,72
mvva 104,144,80
mvva 104,148,88
mvva 108,148,120
mvva 108,144,112
mvva 108,132,104
tv 148,226,61440
mvva 108,128,96
tv 144,226,57344
s_waitcnt lgkmcnt(2)
mvva 124,136,96
tv 132,226,53248
mvva 124,140,104
tv 128,226,49152
tv 130,226,51200
mvva 124,152,112
tv 134,226,55296
mvva 124,156,120
tv 146,226,59392
mvva 120,156,88
tv 150,226,63488
mvva 120,152,80
mvva 120,140,72
mvva 120,136,64
mvva 116,136,100
mvva 116,140,108
s_waitcnt vmcnt(7)
v_max_u32_e32 v248,v16,v17
mvva 116,152,116
v_max3_u32 v248,v248,v18,v19
s_waitcnt vmcnt(6)
v_max3_u32 v248,v248,v20,v21
v_max3_u32 v248,v248,v22,v23
mvva 116,156,124
s_waitcnt vmcnt(5)
v_max3_u32 v248,v248,v24,v25
v_max3_u32 v248,v248,v26,v27
s_waitcnt vmcnt(4)
v_max3_u32 v248,v248,v28,v29
mvva 112,156,92
v_max3_u32 v248,v248,v30,v31
v_cmp_eq_u32_e32 vcc,-1,v248
mvva 112,152,84
s_cbranch_vccnz L_spx0o
L_sprx0o:
mvva 112,140,76
tv 156,227,61440
tv 158,227,63488
v_lshlrev_b32_e32 v251,16,v18
v_and_b32_e32 v255,4294901760,v18
mvva 112,136,68
tv 152,227,57344
tv 154,227,59392
s_waitcnt lgkmcnt(4)
mvva 96,128,132
v_lshlrev_b32_e32 v253,16,v19
v_and_b32_e32 v242,4294901760,v19
mvva 96,132,140
tv 136,227,49152
tv 138,227,51200
mvva 96,144,148
tv 140,227,53248
tv 142,227,55296
v_add_f32_e32 v112,v112,v251
mvva 96,148,156
v_add_f32_e32 v113,v113,v255
v_add_f32_e32 v114,v114,v253
mvva 100,148,188
v_add_f32_e32 v115,v115,v242
v_lshlrev_b32_e32 v251,16,v22
mvva 100,144,180
v_and_b32_e32 v255,4294901760,v22
v_lshlrev_b32_e32 v253,16,v23
mvva 100,132,172
v_and_b32_e32 v242,4294901760,v23
v_add_f32_e32 v116,v116,v251
v_add_f32_e32 v117,v117,v255
mvva 100,128,164
L_vs31:
s_cbranch_execz L_vr1
v_add_f32_e32 v118,v118,v253
v_add_f32_e32 v119,v119,v242
mvva 104,128,128
v_lshlrev_b32_e32 v251,16,v26
v_and_b32_e32 v255,4294901760,v26
mvva 104,132,136
v_lshlrev_b32_e32 v253,16,v27
v_and_b32_e32 v242,4294901760,v27
mvva 104,144,144
v_add_f32_e32 v120,v120,v251
v_add_f32_e32 v121,v121,v255
v_add_f32_e32 v122,v122,v253
mvva 104,148,152
v_add_f32_e32 v123,v123,v242
v_lshlrev_b32_e32 v251,16,v30
mvva 108,148,184
v_and_b32_e32 v255,4294901760,v30
v_lshlrev_b32_e32 v253,16,v31
mvva 108,144,176
v_and_b32_e32 v242,4294901760,v31
v_add_f32_e32 v124,v124,v251
mvva 108,132,168
v_add_f32_e32 v125,v125,v255
v_add_f32_e32 v126,v126,v253
v_add_f32_e32 v127,v127,v242
mvva 108,128,160
tv 144,234,24576
v_lshlrev_b32_e32 v251,16,v16
s_waitcnt lgkmcnt(1)
mvva 124,136,160
tv 132,234,20480
v_and_b32_e32 v255,4294901760,v16
mvva 124,140,168
tv 128,234,16384
tv 130,234,18432
mvva 124,152,176
tv 134,234,22528
v_lshlrev_b32_e32 v253,16,v17
mvva 124,156,184
tv 146,234,26624
v_and_b32_e32 v242,4294901760,v17
mvva 120,156,152
tv 148,234,28672
tv 150,234,30720
mvva 120,152,144
v_add_f32_e32 v96,v96,v251
v_add_f32_e32 v97,v97,v255
mvva 120,140,136
v_add_f32_e32 v98,v98,v253
v_add_f32_e32 v99,v99,v242
mvva 120,136,128
v_lshlrev_b32_e32 v251,16,v20
v_and_b32_e32 v255,4294901760,v20
mvva 116,136,164
v_lshlrev_b32_e32 v253,16,v21
v_and_b32_e32 v242,4294901760,v21
mvva 116,140,172
v_add_f32_e32 v100,v100,v251
v_add_f32_e32 v101,v101,v255
mvva 116,152,180
v_add_f32_e32 v102,v102,v253
v_add_f32_e32 v103,v103,v242
mvva 116,156,188
v_lshlrev_b32_e32 v251,16,v24
v_and_b32_e32 v255,4294901760,v24
mvva 112,156,156
v_lshlrev_b32_e32 v253,16,v25
v_and_b32_e32 v242,4294901760,v25
mvva 112,152,148
v_add_f32_e32 v104,v104,v251
v_add_f32_e32 v105,v105,v255
mvva 112,140,140
tv 156,235,28672
v_add_f32_e32 v106,v106,v253
mvva 112,136,132
tv 152,235,24576
v_add_f32_e32 v107,v107,v242
s_waitcnt lgkmcnt(2)
mvva 96,128,4
tv 140,235,20480
v_lshlrev_b32_e32 v251,16,v28
mvva 96,132,12
tv 136,235,16384
tv 138,235,18432
mvva 96,144,20
tv 142,235,22528
v_and_b32_e32 v255,4294901760,v28
mvva 96,148,28
tv 154,235,26624
v_lshlrev_b32_e32 v253,16,v29
mvva 100,148,60
tv 158,235,30720
v_and_b32_e32 v242,4294901760,v29
mvva 100,144,52
v_add_f32_e32 v108,v108,v251
v_add_f32_e32 v109,v109,v255
mvva 100,132,44
v_add_f32_e32 v110,v110,v253
v_add_f32_e32 v111,v111,v242
mvva 100,128,36
mvva 104,128,0
mvva 104,132,8
mvva 104,144,16
mvva 104,148,24
mvva 108,148,56
mvva 108,144,48
mvva 108,132,40
tv 148,230,45056
mvva 108,128,32
tv 144,230,40960
s_waitcnt lgkmcnt(2)
mvva 124,136,32
s_xor_b32 s83,s59,8
tv 132,230,36864
s_mul_i32 s83,s83,12288
mvva 124,140,40
s_add_u32 s83,s83,4096
tv 128,230,32768
tv 130,230,34816
mvva 124,152,48
lv 48,244,83,0
tv 134,230,38912
mvva 124,156,56
tv 146,230,43008
mvva 120,156,24
lv 52,244,83,1024
tv 150,230,47104
mvva 120,152,16
mvva 120,140,8
lv 56,244,83,2048
mvva 120,136,0
mvva 116,136,36
lv 60,244,83,3072
mvva 116,140,44
mvva 116,152,52
s_xor_b32 s84,s59,4
mvva 116,156,60
s_mul_i32 s84,s84,12288
mvva 112,156,28
s_add_u32 s84,s84,8192
mvva 112,152,20
lv 80,244,84,0
mvva 112,140,12
tv 156,231,45056
mvva 112,136,4
lv 84,244,84,1024
tv 152,231,40960
s_waitcnt lgkmcnt(2)
mvvv 96,128,164
tv 140,231,36864
mvvv 96,132,172
lv 88,244,84,2048
tv 136,231,32768
tv 138,231,34816
mvvv 96,144,180
tv 142,231,38912
mvvv 96,148,188
lv 92,244,84,3072
tv 154,231,43008
mvvv 100,148,220
tv 158,231,47104
mvvv 100,144,212
mvvv 100,132,204
mvvv 100,128,196
mvvv 104,128,160
mvvv 104,132,168
mvvv 104,144,176
mvvv 104,148,184
mvvv 108,148,216
mvvv 108,144,208
mvvv 108,132,200
tv 148,230,61440
mvvv 108,128,192
tv 144,230,57344
s_waitcnt lgkmcnt(2)
mvvv 124,136,192
tv 132,230,53248
mvvv 124,140,200
tv 128,230,49152
tv 130,230,51200
mvvv 124,152,208
tv 134,230,55296
mvvv 124,156,216
tv 146,230,59392
mvvv 120,156,184
tv 150,230,63488
mvvv 120,152,176
mvvv 120,140,168
mvvv 120,136,160
mvvv 116,136,196
mvvv 116,140,204
mvvv 116,152,212
mvvv 116,156,220
mvvv 112,156,188
mvvv 112,152,180
mvvv 112,140,172
tv 156,231,61440
mvvv 112,136,164
tv 152,231,57344
s_waitcnt lgkmcnt(2)
mvva 96,128,196
tv 140,231,53248
mvva 96,132,204
tv 136,231,49152
tv 138,231,51200
mvva 96,144,212
tv 142,231,55296
mvva 96,148,220
tv 154,231,59392
mvva 100,148,252
tv 158,231,63488
mvva 100,144,244
mvva 100,132,236
mvva 100,128,228
mvva 104,128,192
mvva 104,132,200
mvva 104,144,208
mvva 104,148,216
s_waitcnt vmcnt(7)
v_max_u32_e32 v248,v48,v49
mvva 108,148,248
v_max3_u32 v248,v248,v50,v51
s_waitcnt vmcnt(6)
v_max3_u32 v248,v248,v52,v53
mvva 108,144,240
v_max3_u32 v248,v248,v54,v55
s_waitcnt vmcnt(5)
v_max3_u32 v248,v248,v56,v57
mvva 108,132,232
v_max3_u32 v248,v248,v58,v59
s_waitcnt vmcnt(4)
v_max3_u32 v248,v248,v60,v61
mvva 108,128,224
v_max3_u32 v248,v248,v62,v63
v_cmp_eq_u32_e32 vcc,-1,v248
s_waitcnt lgkmcnt(0)
mvva 124,136,224
s_cbranch_vccnz L_spx1o
L_sprx1o:
mvva 124,140,232
mvva 124,152,240
mvva 124,156,248
s_waitcnt vmcnt(3)
v_max_u32_e32 v248,v80,v81
mvva 120,156,216
v_max3_u32 v248,v248,v82,v83
s_waitcnt vmcnt(2)
v_max3_u32 v248,v248,v84,v85
mvva 120,152,208
v_max3_u32 v248,v248,v86,v87
s_waitcnt vmcnt(1)
v_max3_u32 v248,v248,v88,v89
mvva 120,140,200
v_max3_u32 v248,v248,v90,v91
s_waitcnt vmcnt(0)
v_max3_u32 v248,v248,v92,v93
mvva 120,136,192
v_max3_u32 v248,v248,v94,v95
v_cmp_eq_u32_e32 vcc,-1,v248
mvva 116,136,228
s_cbranch_vccnz L_spx2o
L_sprx2o:
mvva 116,140,236
mvva 116,152,244
mvva 116,156,252
mvva 112,156,220
mvva 112,152,212
mvva 112,140,204
mvva 112,136,196
mvvv 108,238,60
mvvv 108,238,92
mvvv 124,236,60
mvvv 124,236,92
mvvv 104,238,56
mvvv 104,238,88
mvvv 120,236,56
mvvv 120,236,88
mvvv 100,238,52
mvvv 100,238,84
cv 108,108,109
cv 109,110,111
cv 110,124,125
cv 111,126,127
cv 104,104,105
cv 105,106,107
mvvv 116,236,52
v_permlane16_swap_b32_e32 v108,v110
v_permlane16_swap_b32_e32 v109,v111
cv 106,120,121
cv 107,122,123
mvvv 116,236,84
buffer_store_dwordx4 v[108:111],v243,s[44:47],s73 offen sc0 sc1
v_permlane16_swap_b32_e32 v104,v106
v_permlane16_swap_b32_e32 v105,v107
mvvv 96,238,48
cv 100,100,101
cv 101,102,103
mvvv 96,238,80
buffer_store_dwordx4 v[104:107],v243,s[44:47],s72 offen sc0 sc1
cv 102,116,117
cv 103,118,119
mvvv 112,236,48
v_permlane16_swap_b32_e32 v100,v102
v_permlane16_swap_b32_e32 v101,v103
mvvv 112,236,80
buffer_store_dwordx4 v[100:103],v243,s[44:47],s71 offen sc0 sc1
cv 96,96,97
cv 97,98,99
s_nop 4
cv 98,112,113
cv 99,114,115
s_nop 0
v_permlane16_swap_b32_e32 v96,v98
v_permlane16_swap_b32_e32 v97,v99
buffer_store_dwordx4 v[96:99],v243,s[44:47],s70 offen sc0 sc1
L_oddx:
s_endpgm
L_k_ec1:
buffer_store_dwordx4 a[252:255],v244,s[8:11],s35 offen offset:1024 sc0 sc1
s_branch L_k_ecr1
L_q_ack:
s_mov_b32 s86,65536
L_q_ack2:
s_sleep 8
buffer_load_dword v246,off,s[8:11],s35 offset:1024 sc1
s_waitcnt vmcnt(0)
v_readfirstlane_b32 s87,v246
s_nop 0
s_cmp_eq_u32 s87,s32
s_cbranch_scc1 L_q_acked
s_sub_u32 s86,s86,1
s_cbranch_scc0 L_q_ack2
s_branch L_q_acked
L_q_spin:
s_mov_b32 s6,65536
L_q_spin1:
s_sleep 8
buffer_load_dword v248,v250,s[8:11],0 offen sc1
s_or_b32 s7,%1,s12
s_cbranch_scc1 L_q_spin2
buffer_load_dwordx4 a[252:255],v244,s[8:11],s35 offen sc1
s_waitcnt vmcnt(0)
buffer_store_dwordx4 a[252:255],v244,s[8:11],s35 offen offset:1024 sc0 sc1
L_q_spin2:
s_waitcnt vmcnt(0)
v_and_b32_e32 v248,v242,v248
v_cmp_ne_u32_e64 s[100:101],v243,v248
s_nop 1
s_cmp_eq_u64 s[100:101],0
s_cbranch_scc1 L_q_ok
s_sub_u32 s6,s6,1
s_cbranch_scc0 L_q_spin1
s_branch L_q_ok
L_bx:
s_mov_b32 s87,65536
L_bx1:
s_sleep 2
buffer_load_dwordx4 v[176:179],v244,s[8:11],s98 offen sc1
buffer_load_dwordx2 v[168:169],v253,s[8:11],s98 offen offset:1024 sc1
buffer_load_dwordx2 v[172:173],v253,s[8:11],s98 offen offset:1536 sc1
s_waitcnt vmcnt(0)
v_max3_u32 v180,v176,v177,v178
v_max3_u32 v180,v180,v179,v168
v_max3_u32 v180,v180,v169,v172
v_max_u32_e32 v180,v180,v173
v_cmp_eq_u32_e64 s[40:41],-1,v180
s_nop 1
s_cmp_eq_u64 s[40:41],0
s_cbranch_scc1 L_bxr
s_sub_u32 s87,s87,1
s_cbranch_scc0 L_bx1
s_branch L_bxr
L_pend2:
s_nop 7
v_add_f32_e32 v226,v152,v153
v_add_f32_e32 v227,v156,v157
v_subrev_u32_e32 v236,0x23800000,v226
v_subrev_u32_e32 v237,0x23800000,v227
v_max_u32_e32 v236,v236,v237
v_cmp_lt_u32_e64 s[40:41],s48,v236
v_rcp_f32_e32 v226,v226
v_rcp_f32_e32 v227,v227
s_add_u32 s86,s13,4096
v_mov_b32_e32 v236,0
v_mov_b32_e32 v237,0
v_mov_b32_e32 v238,0
v_mov_b32_e32 v239,0
v_mov_b64_e32 v[152:153],0
v_mov_b64_e32 v[156:157],0
v_mul_f32_e32 v226,v242,v226
v_mul_f32_e32 v227,v243,v227
v_add_u32_e32 v225,s94,v225
v_pk_mul_f32 v[160:161],v[160:161],v[226:227] op_sel:[0,0] op_sel_hi:[1,0]
v_pk_mul_f32 v[162:163],v[162:163],v[226:227] op_sel:[0,0] op_sel_hi:[1,0]
v_pk_mul_f32 v[164:165],v[164:165],v[226:227] op_sel:[0,0] op_sel_hi:[1,0]
v_pk_mul_f32 v[166:167],v[166:167],v[226:227] op_sel:[0,0] op_sel_hi:[1,0]
cv 160,160,161
cv 161,162,163
cv 162,164,165
cv 163,166,167
buffer_store_dwordx4 v[160:163],v244,s[8:11],s13 offen offset:0 sc0 sc1
v_pk_mul_f32 v[168:169],v[168:169],v[226:227] op_sel:[0,0] op_sel_hi:[1,0]
v_pk_mul_f32 v[170:171],v[170:171],v[226:227] op_sel:[0,0] op_sel_hi:[1,0]
v_pk_mul_f32 v[172:173],v[172:173],v[226:227] op_sel:[0,0] op_sel_hi:[1,0]
v_pk_mul_f32 v[174:175],v[174:175],v[226:227] op_sel:[0,0] op_sel_hi:[1,0]
cv 168,168,169
cv 169,170,171
cv 170,172,173
cv 171,174,175
buffer_store_dwordx4 v[168:171],v244,s[8:11],s13 offen offset:2048 sc0 sc1
v_pk_mul_f32 v[176:177],v[176:177],v[226:227] op_sel:[0,0] op_sel_hi:[1,0]
v_pk_mul_f32 v[178:179],v[178:179],v[226:227] op_sel:[0,0] op_sel_hi:[1,0]
v_pk_mul_f32 v[180:181],v[180:181],v[226:227] op_sel:[0,0] op_sel_hi:[1,0]
v_pk_mul_f32 v[182:183],v[182:183],v[226:227] op_sel:[0,0] op_sel_hi:[1,0]
cv 176,176,177
cv 177,178,179
cv 178,180,181
v_mfma_f32_32x32x16_bf16 v[160:175],v[236:239],v[236:239],0
cv 179,182,183
buffer_store_dwordx4 v[176:179],v244,s[8:11],s86 offen offset:0 sc0 sc1
v_pk_mul_f32 v[184:185],v[184:185],v[226:227] op_sel:[0,0] op_sel_hi:[1,0]
v_pk_mul_f32 v[186:187],v[186:187],v[226:227] op_sel:[0,0] op_sel_hi:[1,0]
v_pk_mul_f32 v[188:189],v[188:189],v[226:227] op_sel:[0,0] op_sel_hi:[1,0]
v_pk_mul_f32 v[190:191],v[190:191],v[226:227] op_sel:[0,0] op_sel_hi:[1,0]
cv 184,184,185
cv 185,186,187
cv 186,188,189
cv 187,190,191
buffer_store_dwordx4 v[184:187],v244,s[8:11],s86 offen offset:2048 sc0 sc1
v_pk_mul_f32 v[192:193],v[192:193],v[226:227] op_sel:[0,1] op_sel_hi:[1,1]
v_pk_mul_f32 v[194:195],v[194:195],v[226:227] op_sel:[0,1] op_sel_hi:[1,1]
v_pk_mul_f32 v[196:197],v[196:197],v[226:227] op_sel:[0,1] op_sel_hi:[1,1]
v_pk_mul_f32 v[198:199],v[198:199],v[226:227] op_sel:[0,1] op_sel_hi:[1,1]
cv 192,192,193
cv 193,194,195
cv 194,196,197
v_mfma_f32_32x32x16_bf16 v[176:191],v[236:239],v[236:239],0
cv 195,198,199
buffer_store_dwordx4 v[192:195],v244,s[8:11],s13 offen offset:1024 sc0 sc1
v_pk_mul_f32 v[200:201],v[200:201],v[226:227] op_sel:[0,1] op_sel_hi:[1,1]
v_pk_mul_f32 v[202:203],v[202:203],v[226:227] op_sel:[0,1] op_sel_hi:[1,1]
v_pk_mul_f32 v[204:205],v[204:205],v[226:227] op_sel:[0,1] op_sel_hi:[1,1]
v_pk_mul_f32 v[206:207],v[206:207],v[226:227] op_sel:[0,1] op_sel_hi:[1,1]
cv 200,200,201
cv 201,202,203
cv 202,204,205
cv 203,206,207
buffer_store_dwordx4 v[200:203],v244,s[8:11],s13 offen offset:3072 sc0 sc1
v_pk_mul_f32 v[208:209],v[208:209],v[226:227] op_sel:[0,1] op_sel_hi:[1,1]
v_pk_mul_f32 v[210:211],v[210:211],v[226:227] op_sel:[0,1] op_sel_hi:[1,1]
v_pk_mul_f32 v[212:213],v[212:213],v[226:227] op_sel:[0,1] op_sel_hi:[1,1]
v_pk_mul_f32 v[214:215],v[214:215],v[226:227] op_sel:[0,1] op_sel_hi:[1,1]
cv 208,208,209
cv 209,210,211
cv 210,212,213
v_mfma_f32_32x32x16_bf16 v[192:207],v[236:239],v[236:239],0
cv 211,214,215
buffer_store_dwordx4 v[208:211],v244,s[8:11],s86 offen offset:1024 sc0 sc1
v_pk_mul_f32 v[216:217],v[216:217],v[226:227] op_sel:[0,1] op_sel_hi:[1,1]
v_pk_mul_f32 v[218:219],v[218:219],v[226:227] op_sel:[0,1] op_sel_hi:[1,1]
v_pk_mul_f32 v[220:221],v[220:221],v[226:227] op_sel:[0,1] op_sel_hi:[1,1]
v_pk_mul_f32 v[222:223],v[222:223],v[226:227] op_sel:[0,1] op_sel_hi:[1,1]
cv 216,216,217
cv 217,218,219
cv 218,220,221
cv 219,222,223
buffer_store_dwordx4 v[216:219],v244,s[8:11],s86 offen offset:3072 sc0 sc1
s_nop 1
v_mfma_f32_32x32x16_bf16 v[208:223],v[236:239],v[236:239],0
s_nop 7
s_nop 1
s_cmp_eq_u32 s84,4
s_cbranch_scc1 L_ret_eb4
s_cmp_lg_u32 s80,s81
s_branch L_ret_eb5
L_redo:
s_waitcnt vmcnt(0) lgkmcnt(0)
s_nop 15
s_nop 3
s_load_dwordx2 s[76:77],%0,0
s_load_dwordx2 s[80:81],%0,16
s_mov_b32 s78,4194304
s_mov_b32 s79,131072
s_mov_b32 s82,8388608
s_mov_b32 s83,131072
s_mov_b32 s86,0
s_waitcnt lgkmcnt(0)
L_rd_t:
s_nop 15
s_nop 3
s_cmp_lt_u32 s86,2
s_cselect_b32 s88,s23,s22
s_cselect_b32 s95,s13,s14
s_and_b32 s89,s86,1
s_lshl_b32 s90,s89,10
s_add_u32 s95,s95,s90
s_lshl_b32 s89,s89,6
s_add_u32 s88,s88,s89
s_lshl_b32 s89,s12,4
s_add_u32 s88,s88,s89
v_bfe_u32 v213,v244,4,4
v_lshrrev_b32_e32 v214,8,v244
v_add_u32_e32 v210,s88,v213
v_lshlrev_b32_e32 v208,11,v210
v_lshl_add_u32 v208,v214,4,v208
v_lshlrev_b32_e32 v209,16,v214
v_lshl_add_u32 v209,v213,1,v209
)"
R"(s_lshl_b32 s89,s18,8
v_add_u32_e32 v209,s89,v209
v_lshlrev_b32_e32 v214,2,v214
v_sub_u32_e32 v210,v210,v214
v_accvgpr_write_b32 a192,0
v_accvgpr_write_b32 a193,0
v_accvgpr_write_b32 a194,0
v_accvgpr_write_b32 a195,0
v_accvgpr_write_b32 a196,0
v_accvgpr_write_b32 a197,0
v_accvgpr_write_b32 a198,0
v_accvgpr_write_b32 a199,0
v_accvgpr_write_b32 a200,0
v_accvgpr_write_b32 a201,0
v_accvgpr_write_b32 a202,0
v_accvgpr_write_b32 a203,0
v_accvgpr_write_b32 a204,0
v_accvgpr_write_b32 a205,0
v_accvgpr_write_b32 a206,0
v_accvgpr_write_b32 a207,0
v_accvgpr_write_b32 a208,0
v_accvgpr_write_b32 a209,0
v_accvgpr_write_b32 a210,0
v_accvgpr_write_b32 a211,0
v_accvgpr_write_b32 a212,0
v_accvgpr_write_b32 a213,0
v_accvgpr_write_b32 a214,0
v_accvgpr_write_b32 a215,0
v_accvgpr_write_b32 a216,0
v_accvgpr_write_b32 a217,0
v_accvgpr_write_b32 a218,0
v_accvgpr_write_b32 a219,0
v_accvgpr_write_b32 a220,0
v_accvgpr_write_b32 a221,0
v_accvgpr_write_b32 a222,0
v_accvgpr_write_b32 a223,0
s_mov_b32 s87,0
L_rd_q:
s_nop 15
s_nop 3
s_lshl_b32 s89,s87,6
s_lshl_b32 s90,s87,18
buffer_load_dwordx4 v[200:203],v208,s[76:79],s89 offen
buffer_load_ushort v160,v209,s[80:83],s90 offen offset:0
buffer_load_ushort v164,v209,s[80:83],s90 offen offset:32
buffer_load_ushort v168,v209,s[80:83],s90 offen offset:64
buffer_load_ushort v172,v209,s[80:83],s90 offen offset:96
buffer_load_ushort v176,v209,s[80:83],s90 offen offset:128
buffer_load_ushort v180,v209,s[80:83],s90 offen offset:160
buffer_load_ushort v184,v209,s[80:83],s90 offen offset:192
buffer_load_ushort v188,v209,s[80:83],s90 offen offset:224
s_add_u32 s90,s90,8192
buffer_load_ushort v192,v209,s[80:83],s90 offen offset:0
buffer_load_ushort v193,v209,s[80:83],s90 offen offset:32
buffer_load_ushort v194,v209,s[80:83],s90 offen offset:64
buffer_load_ushort v195,v209,s[80:83],s90 offen offset:96
buffer_load_ushort v196,v209,s[80:83],s90 offen offset:128
buffer_load_ushort v197,v209,s[80:83],s90 offen offset:160
buffer_load_ushort v198,v209,s[80:83],s90 offen offset:192
buffer_load_ushort v199,v209,s[80:83],s90 offen offset:224
s_add_u32 s90,s90,8192
s_waitcnt vmcnt(0)
v_lshl_or_b32 v160,v192,16,v160
v_lshl_or_b32 v164,v193,16,v164
v_lshl_or_b32 v168,v194,16,v168
v_lshl_or_b32 v172,v195,16,v172
v_lshl_or_b32 v176,v196,16,v176
v_lshl_or_b32 v180,v197,16,v180
v_lshl_or_b32 v184,v198,16,v184
v_lshl_or_b32 v188,v199,16,v188
buffer_load_ushort v161,v209,s[80:83],s90 offen offset:0
buffer_load_ushort v165,v209,s[80:83],s90 offen offset:32
buffer_load_ushort v169,v209,s[80:83],s90 offen offset:64
buffer_load_ushort v173,v209,s[80:83],s90 offen offset:96
buffer_load_ushort v177,v209,s[80:83],s90 offen offset:128
buffer_load_ushort v181,v209,s[80:83],s90 offen offset:160
buffer_load_ushort v185,v209,s[80:83],s90 offen offset:192
buffer_load_ushort v189,v209,s[80:83],s90 offen offset:224
s_add_u32 s90,s90,8192
buffer_load_ushort v192,v209,s[80:83],s90 offen offset:0
buffer_load_ushort v193,v209,s[80:83],s90 offen offset:32
buffer_load_ushort v194,v209,s[80:83],s90 offen offset:64
buffer_load_ushort v195,v209,s[80:83],s90 offen offset:96
buffer_load_ushort v196,v209,s[80:83],s90 offen offset:128
buffer_load_ushort v197,v209,s[80:83],s90 offen offset:160
buffer_load_ushort v198,v209,s[80:83],s90 offen offset:192
buffer_load_ushort v199,v209,s[80:83],s90 offen offset:224
s_add_u32 s90,s90,8192
s_waitcnt vmcnt(0)
v_lshl_or_b32 v161,v192,16,v161
v_lshl_or_b32 v165,v193,16,v165
v_lshl_or_b32 v169,v194,16,v169
v_lshl_or_b32 v173,v195,16,v173
v_lshl_or_b32 v177,v196,16,v177
v_lshl_or_b32 v181,v197,16,v181
v_lshl_or_b32 v185,v198,16,v185
v_lshl_or_b32 v189,v199,16,v189
buffer_load_ushort v162,v209,s[80:83],s90 offen offset:0
buffer_load_ushort v166,v209,s[80:83],s90 offen offset:32
buffer_load_ushort v170,v209,s[80:83],s90 offen offset:64
buffer_load_ushort v174,v209,s[80:83],s90 offen offset:96
buffer_load_ushort v178,v209,s[80:83],s90 offen offset:128
buffer_load_ushort v182,v209,s[80:83],s90 offen offset:160
buffer_load_ushort v186,v209,s[80:83],s90 offen offset:192
buffer_load_ushort v190,v209,s[80:83],s90 offen offset:224
s_add_u32 s90,s90,8192
buffer_load_ushort v192,v209,s[80:83],s90 offen offset:0
buffer_load_ushort v193,v209,s[80:83],s90 offen offset:32
buffer_load_ushort v194,v209,s[80:83],s90 offen offset:64
buffer_load_ushort v195,v209,s[80:83],s90 offen offset:96
buffer_load_ushort v196,v209,s[80:83],s90 offen offset:128
buffer_load_ushort v197,v209,s[80:83],s90 offen offset:160
buffer_load_ushort v198,v209,s[80:83],s90 offen offset:192
buffer_load_ushort v199,v209,s[80:83],s90 offen offset:224
s_add_u32 s90,s90,8192
s_waitcnt vmcnt(0)
v_lshl_or_b32 v162,v192,16,v162
v_lshl_or_b32 v166,v193,16,v166
v_lshl_or_b32 v170,v194,16,v170
v_lshl_or_b32 v174,v195,16,v174
v_lshl_or_b32 v178,v196,16,v178
v_lshl_or_b32 v182,v197,16,v182
v_lshl_or_b32 v186,v198,16,v186
v_lshl_or_b32 v190,v199,16,v190
buffer_load_ushort v163,v209,s[80:83],s90 offen offset:0
buffer_load_ushort v167,v209,s[80:83],s90 offen offset:32
buffer_load_ushort v171,v209,s[80:83],s90 offen offset:64
buffer_load_ushort v175,v209,s[80:83],s90 offen offset:96
buffer_load_ushort v179,v209,s[80:83],s90 offen offset:128
buffer_load_ushort v183,v209,s[80:83],s90 offen offset:160
buffer_load_ushort v187,v209,s[80:83],s90 offen offset:192
buffer_load_ushort v191,v209,s[80:83],s90 offen offset:224
s_add_u32 s90,s90,8192
buffer_load_ushort v192,v209,s[80:83],s90 offen offset:0
buffer_load_ushort v193,v209,s[80:83],s90 offen offset:32
buffer_load_ushort v194,v209,s[80:83],s90 offen offset:64
buffer_load_ushort v195,v209,s[80:83],s90 offen offset:96
buffer_load_ushort v196,v209,s[80:83],s90 offen offset:128
buffer_load_ushort v197,v209,s[80:83],s90 offen offset:160
buffer_load_ushort v198,v209,s[80:83],s90 offen offset:192
buffer_load_ushort v199,v209,s[80:83],s90 offen offset:224
s_add_u32 s90,s90,8192
s_waitcnt vmcnt(0)
v_lshl_or_b32 v163,v192,16,v163
v_lshl_or_b32 v167,v193,16,v167
v_lshl_or_b32 v171,v194,16,v171
v_lshl_or_b32 v175,v195,16,v175
v_lshl_or_b32 v179,v196,16,v179
v_lshl_or_b32 v183,v197,16,v183
v_lshl_or_b32 v187,v198,16,v187
v_lshl_or_b32 v191,v199,16,v191
mavv 192,160,200
mavv 196,164,200
mavv 200,168,200
mavv 204,172,200
mavv 208,176,200
mavv 212,180,200
mavv 216,184,200
mavv 220,188,200
s_add_u32 s87,s87,1
s_cmp_lt_u32 s87,32
s_cbranch_scc1 L_rd_q
s_nop 15
s_nop 3
v_accvgpr_read_b32 v192,a192
v_accvgpr_read_b32 v193,a193
v_accvgpr_read_b32 v194,a194
v_accvgpr_read_b32 v195,a195
v_accvgpr_read_b32 v196,a196
v_accvgpr_read_b32 v197,a197
v_accvgpr_read_b32 v198,a198
v_accvgpr_read_b32 v199,a199
v_mul_f32_e32 v192,0x3e0293ee,v192
v_mul_f32_e32 v193,0x3e0293ee,v193
v_mul_f32_e32 v194,0x3e0293ee,v194
v_mul_f32_e32 v195,0x3e0293ee,v195
v_mul_f32_e32 v196,0x3e0293ee,v196
v_mul_f32_e32 v197,0x3e0293ee,v197
v_mul_f32_e32 v198,0x3e0293ee,v198
v_mul_f32_e32 v199,0x3e0293ee,v199
cv 192,192,193
cv 193,194,195
cv 194,196,197
cv 195,198,199
v_accvgpr_write_b32 a224,v192
v_accvgpr_write_b32 a225,v193
v_accvgpr_write_b32 a226,v194
v_accvgpr_write_b32 a227,v195
v_accvgpr_read_b32 v192,a200
v_accvgpr_read_b32 v193,a201
v_accvgpr_read_b32 v194,a202
v_accvgpr_read_b32 v195,a203
v_accvgpr_read_b32 v196,a204
v_accvgpr_read_b32 v197,a205
v_accvgpr_read_b32 v198,a206
v_accvgpr_read_b32 v199,a207
v_mul_f32_e32 v192,0x3e0293ee,v192
v_mul_f32_e32 v193,0x3e0293ee,v193
v_mul_f32_e32 v194,0x3e0293ee,v194
v_mul_f32_e32 v195,0x3e0293ee,v195
v_mul_f32_e32 v196,0x3e0293ee,v196
v_mul_f32_e32 v197,0x3e0293ee,v197
v_mul_f32_e32 v198,0x3e0293ee,v198
v_mul_f32_e32 v199,0x3e0293ee,v199
cv 192,192,193
cv 193,194,195
cv 194,196,197
cv 195,198,199
v_accvgpr_write_b32 a228,v192
v_accvgpr_write_b32 a229,v193
v_accvgpr_write_b32 a230,v194
v_accvgpr_write_b32 a231,v195
v_accvgpr_read_b32 v192,a208
v_accvgpr_read_b32 v193,a209
v_accvgpr_read_b32 v194,a210
v_accvgpr_read_b32 v195,a211
v_accvgpr_read_b32 v196,a212
v_accvgpr_read_b32 v197,a213
v_accvgpr_read_b32 v198,a214
v_accvgpr_read_b32 v199,a215
v_mul_f32_e32 v192,0x3e0293ee,v192
v_mul_f32_e32 v193,0x3e0293ee,v193
v_mul_f32_e32 v194,0x3e0293ee,v194
v_mul_f32_e32 v195,0x3e0293ee,v195
v_mul_f32_e32 v196,0x3e0293ee,v196
v_mul_f32_e32 v197,0x3e0293ee,v197
v_mul_f32_e32 v198,0x3e0293ee,v198
v_mul_f32_e32 v199,0x3e0293ee,v199
cv 192,192,193
cv 193,194,195
cv 194,196,197
cv 195,198,199
v_accvgpr_write_b32 a232,v192
v_accvgpr_write_b32 a233,v193
v_accvgpr_write_b32 a234,v194
v_accvgpr_write_b32 a235,v195
v_accvgpr_read_b32 v192,a216
v_accvgpr_read_b32 v193,a217
v_accvgpr_read_b32 v194,a218
v_accvgpr_read_b32 v195,a219
v_accvgpr_read_b32 v196,a220
v_accvgpr_read_b32 v197,a221
v_accvgpr_read_b32 v198,a222
v_accvgpr_read_b32 v199,a223
v_mul_f32_e32 v192,0x3e0293ee,v192
v_mul_f32_e32 v193,0x3e0293ee,v193
v_mul_f32_e32 v194,0x3e0293ee,v194
v_mul_f32_e32 v195,0x3e0293ee,v195
v_mul_f32_e32 v196,0x3e0293ee,v196
v_mul_f32_e32 v197,0x3e0293ee,v197
v_mul_f32_e32 v198,0x3e0293ee,v198
v_mul_f32_e32 v199,0x3e0293ee,v199
cv 192,192,193
cv 193,194,195
cv 194,196,197
cv 195,198,199
v_accvgpr_write_b32 a236,v192
v_accvgpr_write_b32 a237,v193
v_accvgpr_write_b32 a238,v194
v_accvgpr_write_b32 a239,v195
v_mov_b32_e32 v160,0
v_mov_b32_e32 v161,0
v_mov_b32_e32 v162,0
v_mov_b32_e32 v163,0
v_mov_b32_e32 v164,0
v_mov_b32_e32 v165,0
v_mov_b32_e32 v166,0
v_mov_b32_e32 v167,0
v_mov_b32_e32 v168,0
v_mov_b32_e32 v169,0
v_mov_b32_e32 v170,0
v_mov_b32_e32 v171,0
v_mov_b32_e32 v172,0
v_mov_b32_e32 v173,0
v_mov_b32_e32 v174,0
v_mov_b32_e32 v175,0
v_mov_b32_e32 v176,0
v_mov_b32_e32 v177,0
v_mov_b32_e32 v178,0
v_mov_b32_e32 v179,0
v_mov_b32_e32 v180,0
v_mov_b32_e32 v181,0
v_mov_b32_e32 v182,0
v_mov_b32_e32 v183,0
v_mov_b32_e32 v184,0
v_mov_b32_e32 v185,0
v_mov_b32_e32 v186,0
v_mov_b32_e32 v187,0
v_mov_b32_e32 v188,0
v_mov_b32_e32 v189,0
v_mov_b32_e32 v190,0
v_mov_b32_e32 v191,0
v_mov_b32_e32 v216,0xf149f2ca
v_mov_b32_e32 v211,v216
v_mov_b32_e32 v212,0
s_lshr_b32 s91,s88,5
s_mov_b32 s87,0
L_rd_a:
s_nop 15
s_nop 3
s_mul_i32 s89,s18,531456
s_lshl_b32 s90,s87,13
s_add_u32 s89,s89,s90
s_add_u32 s90,s89,16777216
la 64,244,90,0
la 68,244,90,1024
la 72,244,90,2048
la 76,244,90,3072
s_add_u32 s90,s89,16781312
la 80,244,90,0
la 84,244,90,1024
la 88,244,90,2048
la 92,244,90,3072
s_add_u32 s90,s89,33783808
la 96,244,90,0
la 100,244,90,1024
la 104,244,90,2048
la 108,244,90,3072
s_add_u32 s90,s89,33787904
la 112,244,90,0
la 116,244,90,1024
la 120,244,90,2048
la 124,244,90,3072
s_waitcnt vmcnt(0)
zvaa 192,64,224
mvaa 192,72,228
mvaa 192,80,232
mvaa 192,88,236
zvaa 196,68,224
mvaa 196,76,228
mvaa 196,84,232
mvaa 196,92,236
v_cmp_gt_i32_e32 vcc,0,v210
s_nop 2
v_cndmask_b32_e32 v192,v192,v216,vcc
v_cmp_gt_i32_e32 vcc,1,v210
s_nop 1
v_cndmask_b32_e32 v193,v193,v216,vcc
v_cmp_gt_i32_e32 vcc,2,v210
s_nop 1
v_cndmask_b32_e32 v194,v194,v216,vcc
v_cmp_gt_i32_e32 vcc,3,v210
s_nop 1
v_cndmask_b32_e32 v195,v195,v216,vcc
v_cmp_gt_i32_e32 vcc,16,v210
s_nop 1
v_cndmask_b32_e32 v196,v196,v216,vcc
v_cmp_gt_i32_e32 vcc,17,v210
s_nop 1
v_cndmask_b32_e32 v197,v197,v216,vcc
v_cmp_gt_i32_e32 vcc,18,v210
s_nop 1
v_cndmask_b32_e32 v198,v198,v216,vcc
v_cmp_gt_i32_e32 vcc,19,v210
s_nop 1
v_cndmask_b32_e32 v199,v199,v216,vcc
v_max3_f32 v213,v192,v193,v194
v_max3_f32 v214,v195,v196,v197
v_max3_f32 v213,v213,v198,v199
v_max_f32_e32 v213,v213,v214
v_mov_b32_e32 v214,v213
s_nop 1
v_permlane16_swap_b32_e32 v213,v214
v_max_f32_e32 v213,v213,v214
v_mov_b32_e32 v214,v213
s_nop 1
v_permlane32_swap_b32_e32 v213,v214
v_max_f32_e32 v213,v213,v214
v_max_f32_e32 v213,v213,v211
v_sub_f32_e32 v215,v211,v213
ex 215
v_mov_b32_e32 v211,v213
v_sub_f32_e32 v192,v192,v211
v_sub_f32_e32 v193,v193,v211
v_sub_f32_e32 v194,v194,v211
v_sub_f32_e32 v195,v195,v211
v_sub_f32_e32 v196,v196,v211
v_sub_f32_e32 v197,v197,v211
v_sub_f32_e32 v198,v198,v211
v_sub_f32_e32 v199,v199,v211
ex 192
ex 193
ex 194
ex 195
ex 196
ex 197
ex 198
ex 199
v_mul_f32_e32 v212,v215,v212
v_add_f32_e32 v212,v212,v192
v_add_f32_e32 v212,v212,v193
v_add_f32_e32 v212,v212,v194
v_add_f32_e32 v212,v212,v195
v_add_f32_e32 v212,v212,v196
v_add_f32_e32 v212,v212,v197
v_add_f32_e32 v212,v212,v198
v_add_f32_e32 v212,v212,v199
cv 192,192,193
cv 193,194,195
cv 194,196,197
cv 195,198,199
v_mul_f32_e32 v160,v215,v160
v_mul_f32_e32 v161,v215,v161
v_mul_f32_e32 v162,v215,v162
v_mul_f32_e32 v163,v215,v163
v_mul_f32_e32 v164,v215,v164
v_mul_f32_e32 v165,v215,v165
v_mul_f32_e32 v166,v215,v166
v_mul_f32_e32 v167,v215,v167
v_mul_f32_e32 v168,v215,v168
v_mul_f32_e32 v169,v215,v169
v_mul_f32_e32 v170,v215,v170
v_mul_f32_e32 v171,v215,v171
v_mul_f32_e32 v172,v215,v172
v_mul_f32_e32 v173,v215,v173
v_mul_f32_e32 v174,v215,v174
v_mul_f32_e32 v175,v215,v175
v_mul_f32_e32 v176,v215,v176
v_mul_f32_e32 v177,v215,v177
v_mul_f32_e32 v178,v215,v178
v_mul_f32_e32 v179,v215,v179
v_mul_f32_e32 v180,v215,v180
v_mul_f32_e32 v181,v215,v181
v_mul_f32_e32 v182,v215,v182
v_mul_f32_e32 v183,v215,v183
v_mul_f32_e32 v184,v215,v184
v_mul_f32_e32 v185,v215,v185
v_mul_f32_e32 v186,v215,v186
v_mul_f32_e32 v187,v215,v187
v_mul_f32_e32 v188,v215,v188
v_mul_f32_e32 v189,v215,v189
v_mul_f32_e32 v190,v215,v190
v_mul_f32_e32 v191,v215,v191
mvav 160,96,192
mvav 164,100,192
mvav 168,104,192
mvav 172,108,192
mvav 176,112,192
mvav 180,116,192
mvav 184,120,192
mvav 188,124,192
v_subrev_u32_e32 v210,32,v210
s_add_u32 s87,s87,1
s_cmp_le_u32 s87,s91
s_cbranch_scc1 L_rd_a
s_nop 15
s_nop 3
s_lshl_b32 s89,s88,2
s_add_u32 s89,s89,83886080
v_bfe_u32 v213,v244,2,6
buffer_load_dword v215,v213,s[8:11],s89 offen sc1
v_mov_b32_e32 v214,v212
s_nop 1
v_permlane16_swap_b32_e32 v212,v214
v_add_f32_e32 v212,v212,v214
v_mov_b32_e32 v214,v212
s_nop 1
v_permlane32_swap_b32_e32 v212,v214
v_add_f32_e32 v212,v212,v214
v_rcp_f32_e32 v212,v212
s_waitcnt vmcnt(0)
v_mul_f32_e32 v215,v215,v212
v_mul_f32_e32 v160,v215,v160
v_mul_f32_e32 v161,v215,v161
v_mul_f32_e32 v162,v215,v162
v_mul_f32_e32 v163,v215,v163
v_mul_f32_e32 v164,v215,v164
v_mul_f32_e32 v165,v215,v165
v_mul_f32_e32 v166,v215,v166
v_mul_f32_e32 v167,v215,v167
v_mul_f32_e32 v168,v215,v168
v_mul_f32_e32 v169,v215,v169
v_mul_f32_e32 v170,v215,v170
v_mul_f32_e32 v171,v215,v171
v_mul_f32_e32 v172,v215,v172
v_mul_f32_e32 v173,v215,v173
v_mul_f32_e32 v174,v215,v174
v_mul_f32_e32 v175,v215,v175
v_mul_f32_e32 v176,v215,v176
v_mul_f32_e32 v177,v215,v177
v_mul_f32_e32 v178,v215,v178
v_mul_f32_e32 v179,v215,v179
v_mul_f32_e32 v180,v215,v180
v_mul_f32_e32 v181,v215,v181
v_mul_f32_e32 v182,v215,v182
v_mul_f32_e32 v183,v215,v183
v_mul_f32_e32 v184,v215,v184
v_mul_f32_e32 v185,v215,v185
v_mul_f32_e32 v186,v215,v186
v_mul_f32_e32 v187,v215,v187
v_mul_f32_e32 v188,v215,v188
v_mul_f32_e32 v189,v215,v189
v_mul_f32_e32 v190,v215,v190
v_mul_f32_e32 v191,v215,v191
s_add_u32 s89,s95,4096
cv 160,160,161
cv 161,162,163
cv 162,164,165
cv 163,166,167
buffer_store_dwordx4 v[160:163],v244,s[8:11],s95 offen offset:0 sc0 sc1
cv 168,168,169
cv 169,170,171
cv 170,172,173
cv 171,174,175
buffer_store_dwordx4 v[168:171],v244,s[8:11],s95 offen offset:2048 sc0 sc1
cv 176,176,177
cv 177,178,179
cv 178,180,181
cv 179,182,183
buffer_store_dwordx4 v[176:179],v244,s[8:11],s89 offen offset:0 sc0 sc1
cv 184,184,185
cv 185,186,187
cv 186,188,189
cv 187,190,191
buffer_store_dwordx4 v[184:187],v244,s[8:11],s89 offen offset:2048 sc0 sc1
s_waitcnt vmcnt(0)
v_mov_b32_e32 v192,v160
v_accvgpr_read_b32 v160,a240
v_accvgpr_write_b32 a240,v192
v_mov_b32_e32 v193,v161
v_accvgpr_read_b32 v161,a241
v_accvgpr_write_b32 a241,v193
v_mov_b32_e32 v194,v162
v_accvgpr_read_b32 v162,a242
v_accvgpr_write_b32 a242,v194
v_mov_b32_e32 v195,v163
v_accvgpr_read_b32 v163,a243
v_accvgpr_write_b32 a243,v195
v_mov_b32_e32 v200,v168
v_accvgpr_read_b32 v168,a244
v_accvgpr_write_b32 a244,v200
v_mov_b32_e32 v201,v169
v_accvgpr_read_b32 v169,a245
v_accvgpr_write_b32 a245,v201
v_mov_b32_e32 v202,v170
v_accvgpr_read_b32 v170,a246
v_accvgpr_write_b32 a246,v202
v_mov_b32_e32 v203,v171
v_accvgpr_read_b32 v171,a247
v_accvgpr_write_b32 a247,v203
v_mov_b32_e32 v208,v176
v_accvgpr_read_b32 v176,a248
v_accvgpr_write_b32 a248,v208
v_mov_b32_e32 v209,v177
v_accvgpr_read_b32 v177,a249
v_accvgpr_write_b32 a249,v209
v_mov_b32_e32 v210,v178
v_accvgpr_read_b32 v178,a250
v_accvgpr_write_b32 a250,v210
v_mov_b32_e32 v211,v179
v_accvgpr_read_b32 v179,a251
v_accvgpr_write_b32 a251,v211
v_mov_b32_e32 v216,v184
v_accvgpr_read_b32 v184,a252
v_accvgpr_write_b32 a252,v216
v_mov_b32_e32 v217,v185
v_accvgpr_read_b32 v185,a253
v_accvgpr_write_b32 a253,v217
v_mov_b32_e32 v218,v186
v_accvgpr_read_b32 v186,a254
v_accvgpr_write_b32 a254,v218
v_mov_b32_e32 v219,v187
v_accvgpr_read_b32 v187,a255
v_accvgpr_write_b32 a255,v219
s_add_u32 s86,s86,1
s_cmp_lt_u32 s86,4
s_cbranch_scc1 L_rd_t
s_nop 15
s_nop 3
s_nop 1
s_cmp_eq_u32 s84,4
s_cbranch_scc1 L_redo_r4
s_cmp_lg_u32 s80,s81
s_branch L_redo_r5
L_k_ev:
L_v_ev:
buffer_load_dwordx4 a[252:255],v244,s[8:11],s35 offen sc1
s_branch L_k_evr
L_spl:
s_mov_b32 s91,65536
L_spll:
s_sleep 2
buffer_load_dword v237,off,s[8:11],s32 sc1
s_waitcnt vmcnt(0)
v_cmp_ne_u32_e32 vcc,16912,v237
s_nop 1
s_cbranch_vccz L_sprl
s_sub_u32 s91,s91,1
s_cbranch_scc0 L_spll
s_branch L_sprl
L_spbe:
s_mov_b32 s91,65536
L_splbe:
s_sleep 2
buffer_load_dword v253,v224,s[8:11],s53 offen sc1
s_waitcnt vmcnt(0)
v_cmp_ne_u32_e32 vcc,4,v253
s_nop 1
s_cbranch_vccz L_sprbe
s_sub_u32 s91,s91,1
s_cbranch_scc0 L_splbe
s_branch L_sprbe
L_spa3e:
s_mov_b32 s91,65536
L_spla3e:
s_sleep 2
buffer_load_dword v253,v242,s[8:11],s53 offen sc1
s_waitcnt vmcnt(0)
v_cmp_ne_u32_e32 vcc,4,v253
s_nop 1
s_cbranch_vccz L_spra3e
s_sub_u32 s91,s91,1
s_cbranch_scc0 L_spla3e
s_branch L_spra3e
L_spa4e:
s_mov_b32 s91,65536
L_spla4e:
s_sleep 2
buffer_load_dword v253,v242,s[8:11],s53 offen sc1
s_waitcnt vmcnt(0)
v_cmp_ne_u32_e32 vcc,4,v253
s_nop 1
s_cbranch_vccz L_spra4e
s_sub_u32 s91,s91,1
s_cbranch_scc0 L_spla4e
s_branch L_spra4e
L_spa5e:
s_mov_b32 s91,65536
L_spla5e:
s_sleep 2
buffer_load_dword v253,v242,s[8:11],s53 offen sc1
s_waitcnt vmcnt(0)
v_cmp_ne_u32_e32 vcc,4,v253
s_nop 1
s_cbranch_vccz L_spra5e
s_sub_u32 s91,s91,1
s_cbranch_scc0 L_spla5e
s_branch L_spra5e
L_spa6e:
s_mov_b32 s91,65536
L_spla6e:
s_sleep 2
buffer_load_dword v253,v242,s[8:11],s53 offen sc1
s_waitcnt vmcnt(0)
v_cmp_ne_u32_e32 vcc,4,v253
s_nop 1
s_cbranch_vccz L_spra6e
s_sub_u32 s91,s91,1
s_cbranch_scc0 L_spla6e
s_branch L_spra6e
L_spa7e:
s_mov_b32 s91,65536
L_spla7e:
s_sleep 2
buffer_load_dword v253,v242,s[8:11],s53 offen sc1
s_waitcnt vmcnt(0)
v_cmp_ne_u32_e32 vcc,4,v253
s_nop 1
s_cbranch_vccz L_spra7e
s_sub_u32 s91,s91,1
s_cbranch_scc0 L_spla7e
s_branch L_spra7e
L_spa8e:
s_mov_b32 s91,65536
L_spla8e:
s_sleep 2
buffer_load_dword v253,v242,s[8:11],s53 offen sc1
s_waitcnt vmcnt(0)
v_cmp_ne_u32_e32 vcc,4,v253
s_nop 1
s_cbranch_vccz L_spra8e
s_sub_u32 s91,s91,1
s_cbranch_scc0 L_spla8e
s_branch L_spra8e
L_spa9e:
s_mov_b32 s91,65536
L_spla9e:
s_sleep 2
buffer_load_dword v253,v242,s[8:11],s53 offen sc1
s_waitcnt vmcnt(0)
v_cmp_ne_u32_e32 vcc,4,v253
s_nop 1
s_cbranch_vccz L_spra9e
s_sub_u32 s91,s91,1
s_cbranch_scc0 L_spla9e
s_branch L_spra9e
L_spa10e:
s_mov_b32 s91,65536
L_spla10e:
s_sleep 2
buffer_load_dword v253,v242,s[8:11],s53 offen sc1
s_waitcnt vmcnt(0)
v_cmp_ne_u32_e32 vcc,4,v253
s_nop 1
s_cbranch_vccz L_spra10e
s_sub_u32 s91,s91,1
s_cbranch_scc0 L_spla10e
s_branch L_spra10e
L_sppe:
s_mov_b32 s91,65536
L_splpe:
s_sleep 2
buffer_load_dword v253,v242,s[8:11],s53 offen sc1
s_waitcnt vmcnt(0)
v_cmp_ne_u32_e32 vcc,4,v253
s_nop 1
s_cbranch_vccz L_sprpe
s_sub_u32 s91,s91,1
s_cbranch_scc0 L_splpe
s_branch L_sprpe
L_spx0e:
s_mov_b32 s91,65536
L_splx0e:
lv 16,244,82,0
lv 20,244,82,1024
lv 24,244,82,2048
lv 28,244,82,3072
s_waitcnt vmcnt(0)
v_max_u32_e32 v248,v16,v17
v_max3_u32 v248,v248,v18,v19
v_max3_u32 v248,v248,v20,v21
v_max3_u32 v248,v248,v22,v23
v_max3_u32 v248,v248,v24,v25
v_max3_u32 v248,v248,v26,v27
v_max3_u32 v248,v248,v28,v29
v_max3_u32 v248,v248,v30,v31
v_cmp_eq_u32_e32 vcc,-1,v248
s_nop 1
s_cbranch_vccz L_sprx0e
s_sub_u32 s91,s91,1
s_cbranch_scc0 L_splx0e
s_branch L_sprx0e
L_spx1e:
s_mov_b32 s91,65536
L_splx1e:
lv 48,244,83,0
lv 52,244,83,1024
lv 56,244,83,2048
lv 60,244,83,3072
s_waitcnt vmcnt(0)
v_max_u32_e32 v248,v48,v49
v_max3_u32 v248,v248,v50,v51
v_max3_u32 v248,v248,v52,v53
v_max3_u32 v248,v248,v54,v55
v_max3_u32 v248,v248,v56,v57
v_max3_u32 v248,v248,v58,v59
v_max3_u32 v248,v248,v60,v61
v_max3_u32 v248,v248,v62,v63
v_cmp_eq_u32_e32 vcc,-1,v248
s_nop 1
s_cbranch_vccz L_sprx1e
s_sub_u32 s91,s91,1
s_cbranch_scc0 L_splx1e
s_branch L_sprx1e
L_spx2e:
s_mov_b32 s91,65536
L_splx2e:
lv 80,244,84,0
lv 84,244,84,1024
lv 88,244,84,2048
lv 92,244,84,3072
s_waitcnt vmcnt(0)
v_max_u32_e32 v248,v80,v81
v_max3_u32 v248,v248,v82,v83
v_max3_u32 v248,v248,v84,v85
v_max3_u32 v248,v248,v86,v87
v_max3_u32 v248,v248,v88,v89
v_max3_u32 v248,v248,v90,v91
v_max3_u32 v248,v248,v92,v93
v_max3_u32 v248,v248,v94,v95
v_cmp_eq_u32_e32 vcc,-1,v248
s_nop 1
s_cbranch_vccz L_sprx2e
s_sub_u32 s91,s91,1
s_cbranch_scc0 L_splx2e
s_branch L_sprx2e
L_spbo:
s_mov_b32 s91,65536
L_splbo:
s_sleep 2
buffer_load_dword v253,v224,s[8:11],s53 offen sc1
s_waitcnt vmcnt(0)
v_cmp_ne_u32_e32 vcc,4,v253
s_nop 1
s_cbranch_vccz L_sprbo
s_sub_u32 s91,s91,1
s_cbranch_scc0 L_splbo
s_branch L_sprbo
L_spa3o:
s_mov_b32 s91,65536
L_spla3o:
s_sleep 2
buffer_load_dword v253,v242,s[8:11],s53 offen sc1
s_waitcnt vmcnt(0)
v_cmp_ne_u32_e32 vcc,4,v253
s_nop 1
s_cbranch_vccz L_spra3o
s_sub_u32 s91,s91,1
s_cbranch_scc0 L_spla3o
s_branch L_spra3o
L_spa4o:
s_mov_b32 s91,65536
L_spla4o:
s_sleep 2
buffer_load_dword v253,v242,s[8:11],s53 offen sc1
s_waitcnt vmcnt(0)
v_cmp_ne_u32_e32 vcc,4,v253
s_nop 1
s_cbranch_vccz L_spra4o
s_sub_u32 s91,s91,1
s_cbranch_scc0 L_spla4o
s_branch L_spra4o
L_spa5o:
s_mov_b32 s91,65536
L_spla5o:
s_sleep 2
buffer_load_dword v253,v242,s[8:11],s53 offen sc1
s_waitcnt vmcnt(0)
v_cmp_ne_u32_e32 vcc,4,v253
s_nop 1
s_cbranch_vccz L_spra5o
s_sub_u32 s91,s91,1
s_cbranch_scc0 L_spla5o
s_branch L_spra5o
L_spa6o:
s_mov_b32 s91,65536
L_spla6o:
s_sleep 2
buffer_load_dword v253,v242,s[8:11],s53 offen sc1
s_waitcnt vmcnt(0)
v_cmp_ne_u32_e32 vcc,4,v253
s_nop 1
s_cbranch_vccz L_spra6o
s_sub_u32 s91,s91,1
s_cbranch_scc0 L_spla6o
s_branch L_spra6o
L_spa7o:
s_mov_b32 s91,65536
L_spla7o:
s_sleep 2
buffer_load_dword v253,v242,s[8:11],s53 offen sc1
s_waitcnt vmcnt(0)
v_cmp_ne_u32_e32 vcc,4,v253
s_nop 1
s_cbranch_vccz L_spra7o
s_sub_u32 s91,s91,1
s_cbranch_scc0 L_spla7o
s_branch L_spra7o
L_spa8o:
s_mov_b32 s91,65536
L_spla8o:
s_sleep 2
buffer_load_dword v253,v242,s[8:11],s53 offen sc1
s_waitcnt vmcnt(0)
v_cmp_ne_u32_e32 vcc,4,v253
s_nop 1
s_cbranch_vccz L_spra8o
s_sub_u32 s91,s91,1
s_cbranch_scc0 L_spla8o
s_branch L_spra8o
L_sppo:
s_mov_b32 s91,65536
L_splpo:
s_sleep 2
buffer_load_dword v253,v242,s[8:11],s53 offen sc1
s_waitcnt vmcnt(0)
v_cmp_ne_u32_e32 vcc,4,v253
s_nop 1
s_cbranch_vccz L_sprpo
s_sub_u32 s91,s91,1
s_cbranch_scc0 L_splpo
s_branch L_sprpo
L_spx0o:
s_mov_b32 s91,65536
L_splx0o:
lv 16,244,82,0
lv 20,244,82,1024
lv 24,244,82,2048
lv 28,244,82,3072
s_waitcnt vmcnt(0)
v_max_u32_e32 v248,v16,v17
v_max3_u32 v248,v248,v18,v19
v_max3_u32 v248,v248,v20,v21
v_max3_u32 v248,v248,v22,v23
v_max3_u32 v248,v248,v24,v25
v_max3_u32 v248,v248,v26,v27
v_max3_u32 v248,v248,v28,v29
v_max3_u32 v248,v248,v30,v31
v_cmp_eq_u32_e32 vcc,-1,v248
s_nop 1
s_cbranch_vccz L_sprx0o
s_sub_u32 s91,s91,1
s_cbranch_scc0 L_splx0o
s_branch L_sprx0o
L_spx1o:
s_mov_b32 s91,65536
L_splx1o:
lv 48,244,83,0
lv 52,244,83,1024
lv 56,244,83,2048
lv 60,244,83,3072
s_waitcnt vmcnt(0)
v_max_u32_e32 v248,v48,v49
v_max3_u32 v248,v248,v50,v51
v_max3_u32 v248,v248,v52,v53
v_max3_u32 v248,v248,v54,v55
v_max3_u32 v248,v248,v56,v57
v_max3_u32 v248,v248,v58,v59
v_max3_u32 v248,v248,v60,v61
v_max3_u32 v248,v248,v62,v63
v_cmp_eq_u32_e32 vcc,-1,v248
s_nop 1
s_cbranch_vccz L_sprx1o
s_sub_u32 s91,s91,1
s_cbranch_scc0 L_splx1o
s_branch L_sprx1o
L_spx2o:
s_mov_b32 s91,65536
L_splx2o:
lv 80,244,84,0
lv 84,244,84,1024
lv 88,244,84,2048
lv 92,244,84,3072
s_waitcnt vmcnt(0)
v_max_u32_e32 v248,v80,v81
v_max3_u32 v248,v248,v82,v83
v_max3_u32 v248,v248,v84,v85
v_max3_u32 v248,v248,v86,v87
v_max3_u32 v248,v248,v88,v89
v_max3_u32 v248,v248,v90,v91
v_max3_u32 v248,v248,v92,v93
v_max3_u32 v248,v248,v94,v95
v_cmp_eq_u32_e32 vcc,-1,v248
s_nop 1
s_cbranch_vccz L_sprx2o
s_sub_u32 s91,s91,1
s_cbranch_scc0 L_splx2o
s_branch L_sprx2o
L_att_end:
s_endpgm
.p2align 12
.purgem cv
.purgem cvaa
.purgem cvvv
.purgem dl
.purgem ds
.purgem ex
.purgem la
.purgem lv
.purgem maaa
.purgem maav
.purgem mavv
.purgem mvaa
.purgem mvav
.purgem mvva
.purgem mvvv
.purgem ra
.purgem rv
.purgem ta
.purgem tv
.purgem zaaa
.purgem zaav
.purgem zvaa
.purgem zvav
.purgem zvva
.purgem zvvv
)"
        :: "s"(__builtin_amdgcn_kernarg_segment_ptr()), "s"(blockIdx.x), "v"(tid)
        : "v0", "v1", "v2", "v3", "v4", "v5", "v6", "v7", "v8", "v9", "v10", "v11", "v12", "v13", "v14", "v15", "v16", "v17", "v18", "v19", "v20", "v21", "v22", "v23", "v24", "v25", "v26", "v27", "v28", "v29", "v30", "v31", "v32", "v33", "v34", "v35", "v36", "v37", "v38", "v39", "v40", "v41", "v42", "v43", "v44", "v45", "v46", "v47", "v48", "v49", "v50", "v51", "v52", "v53", "v54", "v55", "v56", "v57", "v58", "v59", "v60", "v61", "v62", "v63", "v64", "v65", "v66", "v67", "v68", "v69", "v70", "v71", "v72", "v73", "v74", "v75", "v76", "v77", "v78", "v79", "v80", "v81", "v82", "v83", "v84", "v85", "v86", "v87", "v88", "v89", "v90", "v91", "v92", "v93", "v94", "v95", "v96", "v97", "v98", "v99", "v100", "v101", "v102", "v103", "v104", "v105", "v106", "v107", "v108", "v109", "v110", "v111", "v112", "v113", "v114", "v115", "v116", "v117", "v118", "v119", "v120", "v121", "v122", "v123", "v124", "v125", "v126", "v127", "v128", "v129", "v130", "v131", "v132", "v133", "v134", "v135", "v136", "v137", "v138", "v139", "v140", "v141", "v142", "v143", "v144", "v145", "v146", "v147", "v148", "v149", "v150", "v151", "v152", "v153", "v154", "v155", "v156", "v157", "v158", "v159", "v160", "v161", "v162", "v163", "v164", "v165", "v166", "v167", "v168", "v169", "v170", "v171", "v172", "v173", "v174", "v175", "v176", "v177", "v178", "v179", "v180", "v181", "v182", "v183", "v184", "v185", "v186", "v187", "v188", "v189", "v190", "v191", "v192", "v193", "v194", "v195", "v196", "v197", "v198", "v199", "v200", "v201", "v202", "v203", "v204", "v205", "v206", "v207", "v208", "v209", "v210", "v211", "v212", "v213", "v214", "v215", "v216", "v217", "v218", "v219", "v220", "v221", "v222", "v223", "v224", "v225", "v226", "v227", "v228", "v229", "v230", "v231", "v232", "v233", "v234", "v235", "v236", "v237", "v238", "v239", "v240", "v241", "v242", "v243", "v244", "v245", "v246", "v247", "v248", "v249", "v250", "v251", "v252", "v253", "v254", "a0", "a1", "a2", "a3", "a4", "a5", "a6", "a7", "a8", "a9", "a10", "a11", "a12", "a13", "a14", "a15", "a16", "a17", "a18", "a19", "a20", "a21", "a22", "a23", "a24", "a25", "a26", "a27", "a28", "a29", "a30", "a31", "a32", "a33", "a34", "a35", "a36", "a37", "a38", "a39", "a40", "a41", "a42", "a43", "a44", "a45", "a46", "a47", "a48", "a49", "a50", "a51", "a52", "a53", "a54", "a55", "a56", "a57", "a58", "a59", "a60", "a61", "a62", "a63", "a64", "a65", "a66", "a67", "a68", "a69", "a70", "a71", "a72", "a73", "a74", "a75", "a76", "a77", "a78", "a79", "a80", "a81", "a82", "a83", "a84", "a85", "a86", "a87", "a88", "a89", "a90", "a91", "a92", "a93", "a94", "a95", "a96", "a97", "a98", "a99", "a100", "a101", "a102", "a103", "a104", "a105", "a106", "a107", "a108", "a109", "a110", "a111", "a112", "a113", "a114", "a115", "a116", "a117", "a118", "a119", "a120", "a121", "a122", "a123", "a124", "a125", "a126", "a127", "a128", "a129", "a130", "a131", "a132", "a133", "a134", "a135", "a136", "a137", "a138", "a139", "a140", "a141", "a142", "a143", "a144", "a145", "a146", "a147", "a148", "a149", "a150", "a151", "a152", "a153", "a154", "a155", "a156", "a157", "a158", "a159", "a160", "a161", "a162", "a163", "a164", "a165", "a166", "a167", "a168", "a169", "a170", "a171", "a172", "a173", "a174", "a175", "a176", "a177", "a178", "a179", "a180", "a181", "a182", "a183", "a184", "a185", "a186", "a187", "a188", "a189", "a190", "a191", "a192", "a193", "a194", "a195", "a196", "a197", "a198", "a199", "a200", "a201", "a202", "a203", "a204", "a205", "a206", "a207", "a208", "a209", "a210", "a211", "a212", "a213", "a214", "a215", "a216", "a217", "a218", "a219", "a220", "a221", "a222", "a223", "a224", "a225", "a226", "a227", "a228", "a229", "a230", "a231", "a232", "a233", "a234", "a235", "a236", "a237", "a238", "a239", "a240", "a241", "a242", "a243", "a244", "a245", "a246", "a247", "a248", "a249", "a250", "a251", "a252", "a253", "a254", "a255", "s4", "s5", "s6", "s7", "s8", "s9", "s10", "s11", "s12", "s13", "s14", "s15", "s16", "s17", "s18", "s19", "s20", "s21", "s22", "s23", "s24", "s25", "s26", "s27", "s28", "s29", "s30", "s31", "s32", "s33", "s34", "s35", "s36", "s37", "s38", "s39", "s40", "s41", "s42", "s43", "s44", "s45", "s46", "s47", "s48", "s49", "s50", "s51", "s52", "s53", "s54", "s55", "s56", "s57", "s58", "s59", "s60", "s61", "s62", "s63", "s64", "s65", "s66", "s67", "s68", "s69", "s70", "s71", "s72", "s73", "s74", "s75", "s76", "s77", "s78", "s79", "s80", "s81", "s82", "s83", "s84", "s85", "s86", "s87", "s88", "s89", "s90", "s91", "s92", "s93", "s94", "s95", "s96", "s97", "s98", "s99", "s100", "s101", "vcc", "scc", "m0", "memory");
}

extern "C" void kernel_launch(void* const* d_in, const int* in_sizes, int n_in, void* d_out, int out_size, void* d_ws, size_t ws_size, hipStream_t stream) {
    (void)in_sizes; (void)n_in; (void)out_size; (void)ws_size;
    char* ws = (char*)d_ws + 0ull;
    Args a;
    a.p[0] = d_in[0]; a.p[1] = d_in[1]; a.p[2] = d_in[2]; a.p[3] = d_in[3]; a.p[4] = d_in[4]; a.p[5] = d_in[5]; a.p[6] = d_in[7]; a.p[7] = d_in[8]; a.p[8] = d_in[9]; a.p[9] = d_out; a.p[10] = ws;
    mega<<<256, 256, 0, stream>>>(a);
}
```
